```python
import torch

def _softmax(x, axis=-1):
    return torch.softmax(x, dim=axis)

def _gelu(x):
    return torch.nn.functional.gelu(x, approximate='tanh')

def _silu(x):
    return torch.nn.functional.silu(x)

def _sigmoid(x):
    return torch.sigmoid(x)

def _layernorm(x, axis=-1, eps=1e-05):
    m = x.mean(dim=axis, keepdim=True)
    v = x.var(dim=axis, keepdim=True, unbiased=False)
    return (x - m) / torch.sqrt(v + eps)

def _scatter_add(tgt, idx, src, axis=0):
    return tgt.clone().index_add_(axis, idx, src)

def _segment_sum(src, seg, n):
    out = torch.zeros((n,) + src.shape[1:], dtype=src.dtype)
    return out.index_add_(0, seg, src)

def _pad(x, pw):
    flat = tuple((v for pair in reversed(pw) for v in pair))
    return torch.nn.functional.pad(x, flat)
kernel_name = 'interleave_layers__memory_aug_recurrent_mem_tr__glu_gated_geglu__prompt'

def setup_inputs():

    _w = lambda a: (lambda t: t.to(torch.bfloat16) if t.is_floating_point() else t)(torch.as_tensor(a))

    def _A_setup():
        B, S, Dm = (1, 2048, 1024)
        H, Dh = (32, 128)
        M, seg = (8, 32)
        seg = min(seg, S)
        S = S // seg * seg
        return {'x': _w((lambda *s: torch.randn(*s, dtype=torch.bfloat16) * 0.05)(B, S, Dm)), 'mem0': _w((lambda *s: torch.randn(*s, dtype=torch.bfloat16) * 0.2)(B, M, Dm)), 'Wq': _w((lambda *s: torch.randn(*s, dtype=torch.bfloat16) * 1.6)(Dm, H, Dh) * Dm ** (-0.5)), 'Wk': _w((lambda *s: torch.randn(*s, dtype=torch.bfloat16) * 1.6)(Dm, H, Dh) * Dm ** (-0.5)), 'Wv': _w((lambda *s: torch.randn(*s, dtype=torch.bfloat16) * 0.2)(Dm, H, Dh) * Dm ** (-0.5)), 'Wo': _w((lambda *s: torch.randn(*s, dtype=torch.bfloat16) * 0.2)(H, Dh, Dm) * (H * Dh) ** (-0.5)), 'seg_len': seg}
    _A_in = _A_setup()

    def _B_setup():
        B, S, Dm, Df = (1, 2048, 1024, 3584)
        return {'x': _w((lambda *s: torch.randn(*s, dtype=torch.bfloat16) * 0.05)(B, S, Dm)), 'w_gate': _w((lambda *s: torch.randn(*s, dtype=torch.bfloat16) * 0.4)(Dm, Df) * Dm ** (-0.5)), 'w_up': _w((lambda *s: torch.randn(*s, dtype=torch.bfloat16) * 0.4)(Dm, Df) * Dm ** (-0.5)), 'w_down': _w((lambda *s: torch.randn(*s, dtype=torch.bfloat16) * 0.4)(Df, Dm) * Df ** (-0.5))}
    _B_in = _B_setup()
    return {'A_x': _A_in['x'], 'A_mem0': _A_in['mem0'], 'A_Wq': _A_in['Wq'], 'A_Wk': _A_in['Wk'], 'A_Wv': _A_in['Wv'], 'A_Wo': _A_in['Wo'], 'A_seg_len': _A_in['seg_len'], 'B_x': _B_in['x'], 'B_w_gate': _B_in['w_gate'], 'B_w_up': _B_in['w_up'], 'B_w_down': _B_in['w_down']}

def reference(A_x, A_mem0, A_Wq, A_Wk, A_Wv, A_Wo, A_seg_len, B_x, B_w_gate, B_w_up, B_w_down):

    def _A_ref(x, mem0, Wq, Wk, Wv, Wo, seg_len):
        B, S, Dm = x.shape
        H, Dh = (Wq.shape[1], Wq.shape[2])
        M = mem0.shape[1]
        scale = Dh ** (-0.5)
        n_seg = S // seg_len
        L = M + seg_len
        idx = torch.arange(L)
        mask = (idx[:, None] < M) | (idx[:, None] >= idx[None, :])
        mem = mem0
        outs = []
        for s in range(n_seg):
            xs = x[:, s * seg_len:(s + 1) * seg_len, :].clone()
            h = torch.concatenate([mem, xs], axis=1)
            q = torch.einsum('btd,dhe->bhte', h, Wq)
            k = torch.einsum('btd,dhe->bhte', h, Wk)
            v = torch.einsum('btd,dhe->bhte', h, Wv)
            scores = torch.einsum('bhqe,bhke->bhqk', q, k) * scale
            scores = torch.where(mask[None, None], scores, -1000000000.0)
            p = _softmax(scores, axis=-1)
            o = torch.einsum('bhqk,bhke->bhqe', p, v)
            o = torch.einsum('bhte,hed->btd', o, Wo)
            mem = o[:, :M, :].clone()
            outs.append(o[:, M:, :])
        return torch.concatenate(outs, axis=1)

    def _B_ref(x, w_gate, w_up, w_down):
        g = torch.einsum('bsd,df->bsf', x, w_gate)
        u = torch.einsum('bsd,df->bsf', x, w_up)
        h = _gelu(g) * u
        return torch.einsum('bsf,fd->bsd', h, w_down)
    x = A_x
    x = x + _A_ref(_layernorm(x, axis=-1), A_mem0, A_Wq, A_Wk, A_Wv, A_Wo, A_seg_len)
    x = x + _B_ref(_layernorm(x, axis=-1), B_w_gate, B_w_up, B_w_down)
    x = x + _A_ref(_layernorm(x, axis=-1), A_mem0, A_Wq, A_Wk, A_Wv, A_Wo, A_seg_len)
    x = x + _B_ref(_layernorm(x, axis=-1), B_w_gate, B_w_up, B_w_down)
    return x
```

```cpp
#include <hip/hip_runtime.h>
#include <stdint.h>

typedef unsigned short u16;
typedef unsigned long long u64;
typedef __attribute__((ext_vector_type(8))) __bf16 bf8;
typedef __attribute__((ext_vector_type(16))) __bf16 bf16v;
typedef __attribute__((ext_vector_type(4))) float f32x4;
typedef __attribute__((ext_vector_type(16))) float f32x16;
typedef __attribute__((ext_vector_type(2))) float f32x2;
typedef __attribute__((ext_vector_type(8))) unsigned v8u;
typedef __attribute__((ext_vector_type(4))) unsigned v4u;
typedef __attribute__((ext_vector_type(3))) unsigned v3u;
typedef __attribute__((ext_vector_type(2))) unsigned v2u;
typedef __amdgpu_buffer_rsrc_t rsrc_t;
typedef __attribute__((address_space(3))) char lds_char;
#define DEVI __device__ __forceinline__
#define LDSP(T, p) ((__attribute__((address_space(3))) T*)(p))

typedef __attribute__((ext_vector_type(2))) __bf16 bf16x2_t;
DEVI unsigned pack2(float lo, float hi) { f32x2 v = {lo, hi}; bf16x2_t b = __builtin_convertvector(v, bf16x2_t); return __builtin_bit_cast(unsigned, b); }
DEVI float bflo(unsigned u) { return __uint_as_float(u << 16); }
DEVI float bfhi(unsigned u) { return __uint_as_float(u & 0xffff0000u); }
DEVI float rbf(float x) { return bflo(pack2(x, x)); }
constexpr int NV = 112;
#define MR_(R, A_STR, V_STR, OUTS, INS, W) do { if constexpr ((R) < 256) asm volatile(A_STR : OUTS : INS, [lo] "n"(R), [hi] "n"((R) + (W) - 1)); \
                                                else asm volatile(V_STR : OUTS : INS, [lo] "n"((R) - 256 + NV), [hi] "n"((R) - 256 + NV + (W) - 1)); } while (0)
#define CM_ ,
template <int R> DEVI void mwr(unsigned x) { if constexpr (R < 256) asm volatile("v_accvgpr_write_b32 a[%1], %0" :: "v"(x), "n"(R)); else asm volatile("v_mov_b32 v[%1], %0" :: "v"(x), "n"(R - 256 + NV)); }
template <int R> DEVI void mwr4(v4u x) { mwr<R>(x[0]); mwr<R + 1>(x[1]); mwr<R + 2>(x[2]); mwr<R + 3>(x[3]); }
#define M16 "v_mfma_f32_16x16x32_bf16 "
#define M32 "v_mfma_f32_32x32x16_bf16 "
#define S16 "v_smfmac_f32_16x16x64_bf16 "
template <int R, bool Z> DEVI void mfA(f32x4& d, v4u b) {
    if constexpr (Z) MR_(R, M16 "%[d], a[%[lo]:%[hi]], %[b], 0", M16 "%[d], v[%[lo]:%[hi]], %[b], 0", [d] "=&v"(d), [b] "v"(b), 4);
    else MR_(R, M16 "%[d], a[%[lo]:%[hi]], %[b], %[d]", M16 "%[d], v[%[lo]:%[hi]], %[b], %[d]", [d] "+v"(d), [b] "v"(b), 4);
}
template <int R, bool Z> DEVI void mfB(f32x4& d, v4u a) {
    if constexpr (Z) MR_(R, M16 "%[d], %[a], a[%[lo]:%[hi]], 0", M16 "%[d], %[a], v[%[lo]:%[hi]], 0", [d] "=&v"(d), [a] "v"(a), 4);
    else MR_(R, M16 "%[d], %[a], a[%[lo]:%[hi]], %[d]", M16 "%[d], %[a], v[%[lo]:%[hi]], %[d]", [d] "+v"(d), [a] "v"(a), 4);
}
template <int R, bool Z> DEVI void mf32A(f32x16& d, v4u b) {
    if constexpr (Z) MR_(R, M32 "%[d], a[%[lo]:%[hi]], %[b], 0", M32 "%[d], v[%[lo]:%[hi]], %[b], 0", [d] "=&v"(d), [b] "v"(b), 4);
    else MR_(R, M32 "%[d], a[%[lo]:%[hi]], %[b], %[d]", M32 "%[d], v[%[lo]:%[hi]], %[b], %[d]", [d] "+v"(d), [b] "v"(b), 4);
}
template <bool Z> DEVI void mfv(f32x4& d, v4u a, v4u b) {
    if constexpr (Z) asm volatile(M16 "%0, %1, %2, 0" : "=&v"(d) : "v"(a), "v"(b)); else asm volatile(M16 "%0, %1, %2, %0" : "+v"(d) : "v"(a), "v"(b));
}
template <int R> DEVI void smf(f32x4& d, v4u a, int idx) { MR_(R, S16 "%[d], %[a], a[%[lo]:%[hi]], %[i]", S16 "%[d], %[a], v[%[lo]:%[hi]], %[i]", [d] "+v"(d), [a] "v"(a) CM_ [i] "v"(idx), 8); }
DEVI void smfv(f32x4& d, v4u a, v8u b, int idx) { asm volatile(S16 "%0, %1, %2, %3" : "+v"(d) : "v"(a), "v"(b), "v"(idx)); }
DEVI void zero4(f32x4& d) { d = f32x4{0.f, 0.f, 0.f, 0.f}; asm volatile("" : "+v"(d)); }
template <int N> DEVI void nop() { asm volatile("s_nop %0" :: "n"(N)); }
DEVI void fence(f32x4& d) { asm volatile("" : "+v"(d)); }
template <class... T> DEVI void fence(f32x4& d, T&... r) { fence(d); fence(r...); }
DEVI float selm(float a, float b, u64 m) { float r; asm("v_cndmask_b32 %0, %1, %2, %3" : "=v"(r) : "v"(a), "v"(b), "s"(m)); return r; }
DEVI int opqs(int x) { asm volatile("" : "+s"(x)); return x; }
DEVI v4u lo4(v8u v) { return __builtin_shufflevector(v, v, 0, 1, 2, 3); }
DEVI v4u hi4(v8u v) { return __builtin_shufflevector(v, v, 4, 5, 6, 7); }
DEVI v8u cat8(v4u a, v4u b) { return __builtin_shufflevector(a, b, 0, 1, 2, 3, 4, 5, 6, 7); }
constexpr int AUX_SC1 = 16;
DEVI v4u ld16(rsrc_t r, unsigned vo, unsigned so) { return __builtin_amdgcn_raw_buffer_load_b128(r, vo, so, AUX_SC1); }
DEVI v2u ld8(rsrc_t r, unsigned vo, unsigned so) { return __builtin_amdgcn_raw_buffer_load_b64(r, vo, so, AUX_SC1); }
DEVI unsigned ld4(rsrc_t r, unsigned vo, unsigned so) { return __builtin_amdgcn_raw_buffer_load_b32(r, vo, so, AUX_SC1); }
DEVI void st16(v4u v, rsrc_t r, unsigned vo) { __builtin_amdgcn_raw_buffer_store_b128(v, r, vo, 0, AUX_SC1); }
DEVI void st16s(v4u v, rsrc_t r, unsigned vo, unsigned so) { __builtin_amdgcn_raw_buffer_store_b128(v, r, vo, so, AUX_SC1); }
DEVI void st8(v2u v, rsrc_t r, unsigned vo, unsigned so) { __builtin_amdgcn_raw_buffer_store_b64(v, r, vo, so, AUX_SC1); }
DEVI void st4(unsigned v, rsrc_t r, unsigned vo, unsigned so) { __builtin_amdgcn_raw_buffer_store_b32(v, r, vo, so, AUX_SC1); }
#define CB asm volatile("" ::: "memory")
#define WL0 asm volatile("s_waitcnt lgkmcnt(0)" ::: "memory")
#define WV0 asm volatile("s_waitcnt vmcnt(0)" ::: "memory")
#define BAR do { __builtin_amdgcn_s_barrier(); CB; } while (0)
#define SB __builtin_amdgcn_sched_barrier(0)
DEVI float fadd(float a, float b) { float r; asm("v_add_f32 %0, %1, %2" : "=v"(r) : "v"(a), "v"(b)); return r; }
DEVI float vfma(float a, float b, float c) { float r; asm("v_fma_f32 %0, %1, %2, %3" : "=v"(r) : "v"(a), "v"(b), "v"(c)); return r; }
DEVI unsigned vxor(unsigned a, unsigned b) { unsigned r; asm volatile("v_xor_b32 %0, %1, %2" : "=v"(r) : "i"(b), "v"(a)); return r; }
DEVI int lane_id() { int r; asm volatile("v_mbcnt_lo_u32_b32 %0, -1, 0\n\tv_mbcnt_hi_u32_b32 %0, -1, %0" : "=v"(r)); __builtin_assume(r >= 0 && r < 64); return r; }
DEVI int opq(int x) { asm volatile("" : "+v"(x)); return x; }
DEVI float vmaxf(float a, float b) { float r; asm("v_max_f32 %0, %1, %2" : "=v"(r) : "v"(a), "v"(b)); return r; }
DEVI void swap32(float& a, float& b) { auto r = __builtin_amdgcn_permlane32_swap(__float_as_uint(a), __float_as_uint(b), false, false); a = __uint_as_float(r[0]); b = __uint_as_float(r[1]); }
DEVI void swap32u(unsigned& a, unsigned& b) { auto r = __builtin_amdgcn_permlane32_swap(a, b, false, false); a = r[0]; b = r[1]; }
DEVI void swap16u(unsigned& a, unsigned& b) { auto r = __builtin_amdgcn_permlane16_swap(a, b, false, false); a = r[0]; b = r[1]; }
DEVI float hadd(float a, float b) { swap32(a, b); return fadd(a, b); }
DEVI float red32(float x) { float y = x; swap32(x, y); return x + y; }
DEVI float red16(float x) { unsigned a = __float_as_uint(x), b = a; swap16u(a, b); return __uint_as_float(a) + __uint_as_float(b); }
DEVI float mx32(float x) { float y = x; swap32(x, y); return vmaxf(x, y); }
DEVI float mx16(float x) { unsigned a = __float_as_uint(x), b = a; swap16u(a, b); return vmaxf(__uint_as_float(a), __uint_as_float(b)); }
template <int N> struct IC { static constexpr int v = N; };
template <int I, int N, class F> DEVI void sfor(F&& f) { if constexpr (I < N) { f(IC<I>{}); sfor<I + 1, N>(f); } }
#ifndef NSEG
#define NSEG 64
#endif
#ifndef LAGD
#define LAGD 10
#endif
constexpr float SC_L2E = 0.08838834764831845f * 1.4426950408889634f;
constexpr unsigned SENT = 0xFFFFFFFFu;
#ifndef NUS_
#define NUS_ 8
#endif
#ifndef NRG_
#define NRG_ 8
#endif
constexpr int NREP = 8, NUS = NUS_, NRG = NRG_, NKF = 5, NGU = 38;
constexpr unsigned XB = 4u << 20;
constexpr unsigned O_XIN = 0;
constexpr unsigned O_XN  = O_XIN + 2 * XB;
constexpr unsigned O_XM  = O_XN + 2 * XB;
constexpr unsigned O_H   = O_XM + 2 * XB;
constexpr unsigned O_D   = O_H + 2u * 64 * 224 * 1024;
constexpr unsigned O_N = O_D + 2u * 64 * NKF * 65536;
constexpr unsigned DV(unsigned o) { return ((o & ~8191u) << 1) | (o & 8191u); }
DEVI unsigned na(unsigned nb, unsigned o) { return (DV(o) + nb) ^ ((o >> 7) & 8192u); }
constexpr unsigned N_PM  = 0;
constexpr unsigned N_PMM = N_PM + NRG * 16384;
constexpr unsigned N_PA  = (1u << 20) - 16384;
constexpr unsigned N_MEM = 1u << 20;
constexpr unsigned N_WM  = 2u << 20;
constexpr unsigned N_PTX = 6u << 20;
constexpr unsigned N_U   = 10u << 20;
static_assert(NRG == 8 && NUS == 8 && NREP == 8 && O_N % (4u << 20) == 0, "");
constexpr unsigned O_FLG_END = O_N + DV(N_MEM + NREP * 16384);
constexpr unsigned N_FX  = N_PA - 8192;
constexpr unsigned N_PG  = N_FX - 8192;
constexpr unsigned N_PD  = N_PG + 40 * 128;
constexpr unsigned O_PRB = O_N + (512u << 10), O_CAL = O_N + (768u << 10);
constexpr unsigned O_SX  = O_N + DV(N_PG) - 2u * 64 * 32 * 256;
constexpr unsigned O_FLG = O_SX;
static_assert(O_PRB >= O_N + DV(N_PMM + NRG * 8192) && O_FLG >= O_CAL + 8192 && (O_FLG_END - O_FLG) % 65536 == 0, "");
constexpr unsigned OOB = 0x80000000u;
constexpr unsigned WS_NEED = O_N + DV(N_U + NUS * (2u << 20));
constexpr unsigned O_PROF = 240u << 20;
static_assert(WS_NEED <= (200u << 20), "workspace too large");

#ifdef DRY
#define WSZ(x) 0u
#else
#define WSZ(x) (x)
#endif
struct Args { const u16 *x, *mem0, *Wq, *Wk, *Wv, *Wo, *Wg, *Wu, *Wd; char* ws; u16* out; };
#ifndef SPIN_LOG
#define SPIN_LOG 18
#endif
constexpr unsigned SPIN_CAP = 1u << SPIN_LOG;
DEVI void spin_bail(unsigned& spins) {
    asm volatile("s_add_u32 %0, %0, 1\n\ts_cmp_lt_u32 %0, %1\n\ts_cbranch_scc1 1f\n\ts_endpgm\n1:" : "+s"(spins) : "n"(SPIN_CAP) : "scc");
}
template <int K> DEVI void wrl(unsigned& v, u64& t) { asm volatile("" : "+s"(t)); asm volatile("v_writelane_b32 %0, %1, %2" : "+v"(v) : "s"((unsigned)t), "n"(K)); }
#ifdef TS
#ifndef TSCLK
#define TSCLK __builtin_amdgcn_s_memrealtime
#endif
#define TSV(u, i, v) __builtin_amdgcn_raw_buffer_store_b32((unsigned)(v), mkrs(a.ws, 256u << 20), lane_id() == 0 ? O_PROF + ((blockIdx.x * 4 + w) * 128 + (u)) * 64 + (i) * 4 : OOB, 0, 0)
#define TSW(u, i) TSV(u, i, TSCLK())
#define TSC(u, i) TSV(u, i, __builtin_amdgcn_s_memtime())
#define TSD u64 tsr[16] = {}
#define TSR(k) tsr[k] = rclk()
#define TSK(k) asm volatile("s_memtime %0" : "=s"(tsr[k]))
#define TSF(u, m) do { unsigned tv_ = 0; WL0; sfor<0, 16>([&](auto K) { if constexpr (((m) >> K.v) & 1) wrl<K.v>(tv_, tsr[K.v]); }); \
    const int tl_ = lane_id(); __builtin_amdgcn_raw_buffer_store_b32(tv_, mkrs(a.ws, 256u << 20), (tl_ < 16 && (((m) >> tl_) & 1)) ? O_PROF + ((blockIdx.x * 4 + w) * 128 + (u)) * 64 + tl_ * 4 : OOB, 0, 0); } while (0)
#if TS == 4
#undef TSV
#undef TSR
#undef TSK
#define TSV(u, i, v)
#define TSR(k)
#define TSK(k)
#define FSK(G, O, D) if constexpr ((G) >= (O) && ((G) - (O)) % (D) == 0 && ((G) - (O)) / (D) < FN) asm volatile("s_memtime %0" : "=s"(tsr[((G) - (O)) / (D)]))
#define FSF(u) TSF(u, (1 << FN) - 1)
#define FSD TSD
#endif
#else
#define TSW(u, i)
#define TSC(u, i)
#define TSV(u, i, v)
#define TSD
#define TSR(k)
#define TSK(k)
#define TSF(u, m)
#endif
#if TS != 4
#define FSK(G, O, D)
#define FSF(u)
#define FSD
#endif
DEVI unsigned rslot(int s) { return (unsigned)(s & (NRG - 1)); }
DEVI u64 pend(v4u d) { return __builtin_amdgcn_uicmp(max(d[0], d[2]), SENT, 32); }
DEVI u64 pend2(v2u d) { return __builtin_amdgcn_uicmp(d[0], SENT, 32); }
DEVI u64 pend1(unsigned d) { return __builtin_amdgcn_uicmp(d, SENT, 32); }
DEVI unsigned nosent(unsigned u) { return min(u, 0xFFFFFFFEu); }
#ifndef WPOL
#define WPOL ""
#endif
template <int OFF, bool SC1> DEVI void dma1(rsrc_t rs, unsigned vo, unsigned so) {
    if constexpr (SC1) asm volatile("buffer_load_dwordx4 %0, %1, %2 offen offset:%3 sc1 lds" :: "v"(vo), "s"(rs), "s"(so), "n"(OFF) : "memory");
    else asm volatile("buffer_load_dwordx4 %0, %1, %2 offen offset:%3 " WPOL " lds" :: "v"(vo), "s"(rs), "s"(so), "n"(OFF) : "memory");
}
DEVI void setm0(unsigned v) { asm volatile("s_mov_b32 m0, %0\n\ts_nop 0" :: "s"(v) : "memory"); }
DEVI void dma4(unsigned lds, rsrc_t rs, unsigned vo, unsigned so) { setm0(lds); dma1<0, true>(rs, vo, so); dma1<1024, true>(rs, vo, so); dma1<2048, true>(rs, vo, so); dma1<3072, true>(rs, vo, so); }
DEVI void dmaw(unsigned lds, rsrc_t rs, unsigned vo, unsigned so) { setm0(lds); asm volatile("buffer_load_dword %0, %1, %2 offen sc1 lds" :: "v"(vo), "s"(rs), "s"(so) : "memory"); }
template <int N> DEVI void wvm() { asm volatile("s_waitcnt vmcnt(%0)" :: "n"(N) : "memory"); }
template <int N> DEVI void wlg() { asm volatile("s_waitcnt lgkmcnt(%0)" :: "n"(N) : "memory"); }
DEVI void dma4p(u64 c0, u64 c1, u64 c2, u64 c3, rsrc_t rs, unsigned vo, unsigned so) {
    asm volatile(
        "s_cmp_eq_u64 %[c0], 0\n\ts_cbranch_scc1 1f\n\t"
        "buffer_load_dwordx4 %[vo], %[rs], %[so] offen sc1 lds\n"
        "1:\n\ts_cmp_eq_u64 %[c1], 0\n\ts_cbranch_scc1 2f\n\t"
        "buffer_load_dwordx4 %[vo], %[rs], %[so] offen offset:1024 sc1 lds\n"
        "2:\n\ts_cmp_eq_u64 %[c2], 0\n\ts_cbranch_scc1 3f\n\t"
        "buffer_load_dwordx4 %[vo], %[rs], %[so] offen offset:2048 sc1 lds\n"
        "3:\n\ts_cmp_eq_u64 %[c3], 0\n\ts_cbranch_scc1 4f\n\t"
        "buffer_load_dwordx4 %[vo], %[rs], %[so] offen offset:3072 sc1 lds\n"
        "4:"
        :: [c0] "s"(c0), [c1] "s"(c1), [c2] "s"(c2), [c3] "s"(c3), [vo] "v"(vo), [rs] "s"(rs), [so] "s"(so)
        : "memory", "scc");
}
#ifndef PSL
#define PSL 8
#endif
DEVI void wait_prog(rsrc_t rs, unsigned vo, unsigned so, int need) {
    unsigned spins = 0;
#pragma nounroll
    for (;;) {
        CB;
        const int v = (int)ld4(rs, vo, so);
        if (__builtin_amdgcn_sicmp(v, need, 40) == 0) return;
        spin_bail(spins);
        __builtin_amdgcn_s_sleep(PSL);
    }
}

typedef __attribute__((ext_vector_type(16))) int v16i;
typedef __attribute__((ext_vector_type(16))) unsigned v16u;
#ifdef TS
constexpr unsigned RSZ = 256u << 20;
#else
constexpr unsigned RSZ = WS_NEED;
#endif
DEVI u64 rclk() { u64 t; asm volatile("s_memrealtime %0" : "=s"(t)); return t; }
DEVI unsigned tsv(u64& t) { asm volatile("s_waitcnt lgkmcnt(0)" : "+s"(t)); return (unsigned)t; }
DEVI unsigned stamp(u64& t, int tag, int lead) { asm volatile("" : "+s"(t)); return (unsigned)tag << 24 | (((unsigned)t + lead) & 0xFFFFFFu); }
#ifndef SXTH
#define SXTH 20
#endif
DEVI int left(int t) { return (int)((unsigned)(t - (int)__builtin_amdgcn_s_memrealtime()) << 8); }
DEVI void hold(int t0, int d) { for (int n = 0; n < 4096 && (int)__builtin_amdgcn_s_memrealtime() - t0 < d; ++n) __builtin_amdgcn_s_sleep(4); }
DEVI int smin(int a, int b) { int r; asm("s_min_i32 %0, %1, %2" : "=s"(r) : "s"(a), "s"(b) : "scc"); return r; }
DEVI int smax(int a, int b) { int r; asm("s_max_i32 %0, %1, %2" : "=s"(r) : "s"(a), "s"(b) : "scc"); return r; }
DEVI void smin8x2(const v16i& f, int& m0, int& m1) {
    asm("s_min_i32 %0, %2, %3\n\ts_min_i32 %1, %10, %11\n\ts_min_i32 %0, %0, %4\n\ts_min_i32 %1, %1, %12\n\ts_min_i32 %0, %0, %5\n\ts_min_i32 %1, %1, %13\n\ts_min_i32 %0, %0, %6\n\ts_min_i32 %1, %1, %14\n\t"
        "s_min_i32 %0, %0, %7\n\ts_min_i32 %1, %1, %15\n\ts_min_i32 %0, %0, %8\n\ts_min_i32 %1, %1, %16\n\ts_min_i32 %0, %0, %9\n\ts_min_i32 %1, %1, %17"
        : "=&s"(m0), "=&s"(m1) : "s"(f[0]), "s"(f[1]), "s"(f[2]), "s"(f[3]), "s"(f[4]), "s"(f[5]), "s"(f[6]), "s"(f[7]),
          "s"(f[8]), "s"(f[9]), "s"(f[10]), "s"(f[11]), "s"(f[12]), "s"(f[13]), "s"(f[14]), "s"(f[15]) : "scc");
}
DEVI void smax8x2(const v16i& f, int& m0, int& m1) {
    asm("s_max_i32 %0, %2, %3\n\ts_max_i32 %1, %10, %11\n\ts_max_i32 %0, %0, %4\n\ts_max_i32 %1, %1, %12\n\ts_max_i32 %0, %0, %5\n\ts_max_i32 %1, %1, %13\n\ts_max_i32 %0, %0, %6\n\ts_max_i32 %1, %1, %14\n\t"
        "s_max_i32 %0, %0, %7\n\ts_max_i32 %1, %1, %15\n\ts_max_i32 %0, %0, %8\n\ts_max_i32 %1, %1, %16\n\ts_max_i32 %0, %0, %9\n\ts_max_i32 %1, %1, %17"
        : "=&s"(m0), "=&s"(m1) : "s"(f[0]), "s"(f[1]), "s"(f[2]), "s"(f[3]), "s"(f[4]), "s"(f[5]), "s"(f[6]), "s"(f[7]),
          "s"(f[8]), "s"(f[9]), "s"(f[10]), "s"(f[11]), "s"(f[12]), "s"(f[13]), "s"(f[14]), "s"(f[15]) : "scc");
}
#ifndef NVK
#define NVK 8
#endif
#ifndef DLY_V
#define DLY_V 500
#endif
#ifndef PSV
#define PSV 12
#endif
#ifndef DLY_P
#define DLY_P 150
#endif
DEVI unsigned prb(int k, int b) { return O_PRB + k * 16384 + b * 8192 + (k & 1) * 4096; }
DEVI void vote(rsrc_t rs, int w, int v, int far, int t0s) {
    const int lane = lane_id();
    constexpr unsigned N = (O_FLG_END - O_FLG) / 65536;
#pragma nounroll
    for (unsigned q = 0; q < N; ++q) st16(v4u{SENT, SENT, SENT, SENT}, rs, O_FLG + ((v * 4 + w) * N + q) * 1024 + lane * 16);
    const v2u e = ld8(rs, O_CAL + (lane & 31) * 256 + 128, 0);
    static_assert(NVK % 4 == 0 && NVK < 12, "");
    int best[2] = {1 << 30, 1 << 30};
    hold(t0s, DLY_P);
#pragma nounroll
    for (int k = 0; k <= NVK / 4; ++k)
#pragma unroll
        for (int b = 0; b < 2; ++b) {
            u64 t0, t1; int d;
            asm volatile("s_memtime %0\n\ts_waitcnt lgkmcnt(0)\n\ts_buffer_load_dword %2, %3, %4 glc\n\ts_waitcnt lgkmcnt(0)\n\ts_memtime %1\n\ts_waitcnt lgkmcnt(0)"
                         : "=&s"(t0), "=&s"(t1), "=&s"(d) : "s"(rs), "s"(prb(k ? 4 * k - w : 0, b) + v * 128) : "memory");
            if (k) best[b] = smin(best[b], (int)(t1 - t0));
        }
    *LDSP(int, (lane & 1) * 16 + w * 4) = best[lane & 1];
    WV0; WL0; BAR;
#pragma unroll
    for (int b = 0; b < 2; ++b) { const v4u t = *LDSP(const v4u, b * 16); best[b] = min(min(t[0], t[1]), min(t[2], t[3])); }
    st8(v2u{((e[0] + 2) & ~1u) | ((best[1] < best[0]) ^ far), e[1] + 1}, rs, lane < 32 && w == 0 ? O_CAL + lane * 256 + v * 8 : OOB, 0);
}
DEVI void vote_end(rsrc_t rs) {
    const unsigned o = O_CAL + (lane_id() & 31) * 256 + 128;
    const v2u e = ld8(rs, o, 0);
    st8(v2u{e[0] + 2, e[1] + 1}, rs, o, 0);
}
DEVI unsigned side(rsrc_t rs, int l) {
    unsigned spins = 0;
#pragma nounroll
    for (;;) {
        v16u f[2]; v2u e;
        const unsigned o = O_CAL + (blockIdx.x & 31) * 256;
        asm volatile("s_buffer_load_dwordx16 %0, %3, %4 glc\n\ts_buffer_load_dwordx16 %1, %3, %5 glc\n\ts_buffer_load_dwordx2 %2, %3, %6 glc\n\ts_waitcnt lgkmcnt(0)"
                     : "=&s"(f[0]), "=&s"(f[1]), "=&s"(e) : "s"(rs), "s"(o), "s"(o + 64), "s"(o + 128) : "memory");
        unsigned bad = 0, sum = 0;
#pragma unroll
        for (int i = 0; i < 16; ++i) { const unsigned x = f[i >> 3][2 * i & 15]; bad |= ((x ^ (e[0] + 2)) >> 1) | (f[i >> 3][(2 * i & 15) + 1] ^ (e[1] + 1)); sum += x & 1; }
#ifdef CSIDE
        sum = CSIDE * 16;
#endif
        if (bad == 0) return opqs(O_N + ((l ^ (sum > 8)) & 1) * 8192);
        spin_bail(spins);
        __builtin_amdgcn_s_sleep(PSV);
    }
}
#ifndef PSH
#define PSH 2
#endif
#ifndef HRED
#define HRED 1
#endif
template <class F> DEVI void wait_hint(rsrc_t rs, unsigned so, unsigned vo, int tag, F&& mid) {
    int mx;
#pragma nounroll
    for (int n = 0;; ++n) {
        v16i f;
        asm volatile("s_buffer_load_dwordx16 %0, %1, %2 glc\n\ts_waitcnt lgkmcnt(0)" : "=s"(f) : "s"(rs), "s"(so) : "memory");
#if HRED
        int m0, m1;
        smin8x2(f, m0, m1);
        if (smin(m0, m1) >= tag << 24) { smax8x2(f, m0, m1); mx = smax(m0, m1); break; }
#else
        int mn = f[0]; mx = f[0];
#pragma unroll
        for (int i = 1; i < 16; ++i) { mn = smin(mn, f[i]); mx = smax(mx, f[i]); }
        if (mn >= tag << 24) break;
#endif
        if (n == 40) { wait_prog(rs, vo, 0, tag << 24); n = 0; }
        __builtin_amdgcn_s_sleep(PSH);
    }
    mid(mx);
#pragma nounroll
    for (int n = 0; n < 64; ++n) if (left(mx) <= 0) break;
}
template <class WM> constexpr int widx(int j, int t) {
    int n = 0;
    for (int q = 0; q < j * WM::NP + t; ++q) n += WM::kind(q / WM::NP, q % WM::NP) == WM::kind(j, t);
    return n;
}
template <class WM> constexpr int lds_tile(int t) { for (int j = 0; j < WM::NT; ++j) if (WM::kind(j, t) == 1) return j; return -1; }
DEVI v4u ldsq(unsigned p, int off) { return *LDSP(const v4u, LDSP(char, p) + off); }
DEVI void stsq(unsigned p, int off, v4u v) { *LDSP(v4u, LDSP(char, p) + off) = v; }
DEVI v8u ldpair(unsigned wl, int slot) { return cat8(ldsq(wl, slot * 2048), ldsq(wl, slot * 2048 + 1024)); }
DEVI rsrc_t mkrs(const void* p, unsigned bytes) { return __builtin_amdgcn_make_buffer_rsrc((void*)p, 0, (int)bytes, 0x00020000); }
constexpr int LN_NOW = 8, NP0 = (96 - LN_NOW) * 4;
template <int SL = 0> DEVI void pull(const void* p, int n, int q, int nq) {
    const rsrc_t r = mkrs(p, n * 1024);
    const unsigned vo = lane_id() * 16;
#pragma nounroll
    for (; q < n; q += nq) { asm volatile("buffer_load_dwordx4 a[0:3], %0, %1, %2 offen" :: "v"(vo), "s"(r), "s"(q * 1024) : "memory"); if (SL) __builtin_amdgcn_s_sleep(SL); }
}
#ifndef DLY_F
#define DLY_F 800
#endif
#ifndef PFS
#define PFS 5
#endif
DEVI void pull_all(const Args& a, int w, int q, int t0) {
    q = q * 4 + w;
    pull(a.Wv, 8192, q, NP0); pull(a.Wo, 8192, q, NP0); pull(a.Wk, 8192, q, NP0);
    if (q >= 256) return;
    hold(t0, DLY_F);
    pull<PFS>(q & 1 ? a.Wg : a.Wu, 7168, q >> 1, 128); pull<PFS>(a.Wd, 7168, q, 256);
}
template <class WM, int j, int ks, int O0, int O1> DEVI void put_frag(unsigned ta, unsigned wl, v8u* Wc) {
    constexpr int t = ks >> 1, f = ks & 1, k = WM::kind(j, t), ix = widx<WM>(j, t);
    if constexpr (k == 0) {
        constexpr int R = WM::RB + 8 * ix + 4 * f;
        if constexpr (R < 256) asm volatile("ds_read_b64_tr_b16 a[%1:%2], %0 offset:%5\n\tds_read_b64_tr_b16 a[%3:%4], %0 offset:%6" :: "v"(ta), "n"(R), "n"(R + 1), "n"(R + 2), "n"(R + 3), "n"(O0), "n"(O1) : "memory");
        else asm volatile("ds_read_b64_tr_b16 v[%1:%2], %0 offset:%5\n\tds_read_b64_tr_b16 v[%3:%4], %0 offset:%6" :: "v"(ta), "n"(R - 256 + NV), "n"(R - 255 + NV), "n"(R - 254 + NV), "n"(R - 253 + NV), "n"(O0), "n"(O1) : "memory");
    } else {
        v2u p, q;
        asm volatile("ds_read_b64_tr_b16 %0, %2 offset:%3\n\tds_read_b64_tr_b16 %1, %2 offset:%4\n\ts_waitcnt lgkmcnt(0)" : "=&v"(p), "=&v"(q) : "v"(ta), "n"(O0), "n"(O1) : "memory");
        const v4u F = v4u{p[0], p[1], q[0], q[1]};
        if constexpr (k == 1) stsq(wl, (WM::LB + ix) * 2048 + f * 1024, F);
        else if constexpr (f == 0) Wc[WM::CBI + ix] = cat8(F, F);
        else Wc[WM::CBI + ix] = cat8(lo4(Wc[WM::CBI + ix]), F);
    }
}
template <int NB, int DEPTH, int NI, bool SHARED = false, int SL = DEPTH, class ISSUE, class PUT>
DEVI void load_blocks(ISSUE&& issue, PUT&& put) {
    sfor<0, DEPTH - 1>([&](auto B) { issue(B, IC<B.v % SL>{}); });
    sfor<0, NB>([&](auto B) {
        constexpr int b = B.v, left = NB - 1 - b < DEPTH - 2 ? NB - 1 - b : DEPTH - 2;
        wvm<NI * left>();
        wlg<0>();
        if constexpr (SHARED) BAR;
        if constexpr (b + DEPTH - 1 < NB) issue(IC<b + DEPTH - 1>{}, IC<(b + DEPTH - 1) % SL>{});
        put(B, IC<b % SL>{});
    });
    wlg<0>();
    if constexpr (SHARED) BAR;
}
DEVI void dma_blk(unsigned lds, rsrc_t rw, unsigned vo, unsigned so, unsigned rowb) { setm0(lds); dma1<0, false>(rw, vo, so); setm0(lds + 1024); dma1<0, false>(rw, vo, so + 16 * rowb); }
template <class WM, int NKS, int K0, int K1, int RTS, int LATE1, class FILL>
DEVI void gemm_pass(unsigned xa, unsigned wl, const v8u* Wc, v4u (&x)[2][3], f32x4 (&acc)[WM::NT][2], FILL&& fill) {
    auto rd = [&](auto KS, auto Q) {
        constexpr int ks = KS.v, q = Q.v, jl = lds_tile<WM>(ks >> 1), il = widx<WM>(jl < 0 ? 0 : jl, ks >> 1);
        if constexpr (q == 0 && jl >= 0) x[ks & 1][2] = ldsq(wl, (WM::LB + il) * 2048 + (ks & 1) * 1024);
        if constexpr (q == 1) x[ks & 1][1] = ldsq(xa, RTS + ks * 1024);
        if constexpr (q == 2) x[ks & 1][0] = ldsq(xa, ks * 1024);
    };
    sfor<K0 * 2 * WM::NT, K1 * 2 * WM::NT>([&](auto N) {
        constexpr int ks = N.v / (2 * WM::NT), n = N.v % (2 * WM::NT), j = n >> 1, rt = n & 1, t = ks >> 1, f = ks & 1, k = WM::kind(j, t), ix = widx<WM>(j, t);
        fill(IC<ks>{}, IC<n>{});
        SB;
        const v4u xv = x[ks & 1][rt];
        if constexpr (k == 0) mfA<WM::RB + 8 * ix + 4 * f, ks == 0>(acc[j][rt], xv);
        else if constexpr (k == 1) mfv<ks == 0>(acc[j][rt], x[ks & 1][2], xv);
        else mfv<ks == 0>(acc[j][rt], f ? hi4(Wc[WM::CBI + ix]) : lo4(Wc[WM::CBI + ix]), xv);
        if constexpr (ks + 1 < NKS) rd(IC<ks + 1>{}, IC<n - (ks == 1 ? LATE1 : 0)>{});
        SB;
    });
}
template <class WM> constexpr int next_lds(int n) { for (; n < 16 * WM::NT; ++n) if (WM::kind(2 * (n >> 5) + (n & 1), (n >> 1) & 15) == 1) return n; return -1; }
template <class WM> constexpr int lds_cnt(int n) { int c = 0; for (int q = next_lds<WM>(0); q >= 0 && q < n; q = next_lds<WM>(q + 1)) ++c; return c; }
template <class WM> constexpr int lds_nth(int k) { int q = next_lds<WM>(0); for (; k > 0 && q >= 0; --k) q = next_lds<WM>(q + 1); return q; }
template <class WM, int K, int NB> DEVI void pair_next(v8u (&pb)[NB], unsigned wl) {
    constexpr int n = lds_nth<WM>(K);
    if constexpr (n >= 0) { constexpr int ix = widx<WM>(2 * (n >> 5) + (n & 1), (n >> 1) & 15); pb[K % NB] = ldpair(wl, WM::LB + ix); }
}
template <class WM, int MO, int MR, int NB, class FILL>
DEVI void spass(unsigned ma, unsigned wl, const v8u* Wc, f32x4* e, v4u (&m)[MR], v8u (&pb)[NB], int sidx, FILL&& fill) {
    sfor<0, 16 * WM::NT>([&](auto N) {
        constexpr int n = N.v, t = (n >> 1) & 15, j = 2 * (n >> 5) + (n & 1), k = WM::kind(j, t), ix = widx<WM>(j, t), tn = (t + MR - 1) & 15, r = (n >> 1) % MR;
        fill(N);
        SB;
        if constexpr (k == 0) smf<WM::RB + 8 * ix>(e[j], m[r], sidx);
        else if constexpr (k == 1) { constexpr int q = lds_cnt<WM>(n); smfv(e[j], m[r], pb[q % NB], sidx); pair_next<WM, q + NB>(pb, wl); }
        else smfv(e[j], m[r], Wc[WM::CBI + ix], sidx);
        if constexpr ((n & 1) == 0 && n + 2 * MR - 2 < 16 * WM::NT) m[(r + MR - 1) % MR] = ldsq(ma, MO + (tn >> 3) * 32768 + (tn & 7) * 1024);
        SB;
    });
}
typedef __attribute__((ext_vector_type(4))) short s16x4;
DEVI v2u ldtr(unsigned p, int off) { return __builtin_bit_cast(v2u, __builtin_amdgcn_ds_read_tr16_b64_v4i16(LDSP(s16x4, LDSP(char, p) + off))); }
DEVI v4u ldtr2(unsigned p, int off) { const v2u a = ldtr(p, off), b = ldtr(p, off + 256); return v4u{a[0], a[1], b[0], b[1]}; }
DEVI void sts1(unsigned p, int off, unsigned v) { *LDSP(unsigned, LDSP(char, p) + off) = v; }
DEVI unsigned lds1(unsigned p, int off) { return *LDSP(const unsigned, LDSP(char, p) + off); }
DEVI void use(unsigned v) { asm volatile("" :: "v"(v) : "memory"); }
DEVI int mk_sidx(int lane) { return opq(0x4444 + (lane & 2) * 0x5555); }
DEVI int crow(int r, int hb) { return 4 * (r >> 1) + 2 * hb + (r & 1); }
#define A_LDS 0x28a0114002004410ull
#define A_VAR 0x0000000400020001ull
#define A_LOOP \
"s_brev_b32 s99, 1\ns_brev_b32 s98, 1\ns_brev_b32 s97, 1\ns_brev_b32 s96, 1\n" \
"s_branch 4f\n.p2align 6\n1:\ns_waitcnt lgkmcnt(0)\n" \
"v_mfma_f32_16x16x32_bf16 v[36:39], v[48:51], v[84:87], v[36:39]\nds_read_b128 v[32:35], v25 offset:3072\nds_read_b128 v[56:59], v24 offset:21504\nv_mfma_f32_16x16x32_bf16 v[72:75], v[48:51], v[80:83], v[72:75]\n" \
"ds_read_b128 v[60:63], v24 offset:54272\nv_bfe_u32 v48, v24, 6, 3\nv_mfma_f32_16x16x32_bf16 v[52:55], a[160:163], v[84:87], v[52:55]\ns_mov_b32 m0, s50\n" \
"s_add_u32 s99, s30, 2\nv_lshlrev_b32 v48, 2, v48\nv_mfma_f32_16x16x32_bf16 v[76:79], a[160:163], v[80:83], v[76:79]\ns_min_u32 s99, s99, 63\n" \
"s_lshl_b32 s99, s99, 5\ns_add_u32 s99, s99, s48\nv_mfma_f32_16x16x32_bf16 v[64:67], v[120:123], v[84:87], v[64:67]\ns_and_b32 s95, s30, 7\n" \
"s_lshl_b32 s95, s95, 18\ns_add_u32 s95, s95, s33\nv_mfma_f32_16x16x32_bf16 v[40:43], v[120:123], v[80:83], v[40:43]\ns_add_u32 s31, s31, 0x10000\n" \
"v_mfma_f32_16x16x32_bf16 v[44:47], v[224:227], v[84:87], v[44:47]\ns_waitcnt lgkmcnt(1)\nv_mfma_f32_16x16x32_bf16 v[68:71], v[224:227], v[80:83], v[68:71]\ns_waitcnt lgkmcnt(0)\n" \
"ds_read_b128 v[80:83], v24 offset:22528\nv_mfma_f32_16x16x32_bf16 v[36:39], v[32:35], v[56:59], v[36:39]\nds_read_b128 v[84:87], v24 offset:55296\nv_mfma_f32_16x16x32_bf16 v[72:75], v[32:35], v[60:63], v[72:75]\n" \
"v_mfma_f32_16x16x32_bf16 v[52:55], a[164:167], v[56:59], v[52:55]\nds_read_b128 v[32:35], v25 offset:18432\nv_mfma_f32_16x16x32_bf16 v[76:79], a[164:167], v[60:63], v[76:79]\nv_mfma_f32_16x16x32_bf16 v[64:67], v[124:127], v[56:59], v[64:67]\n" \
"v_mfma_f32_16x16x32_bf16 v[40:43], v[124:127], v[60:63], v[40:43]\nv_mfma_f32_16x16x32_bf16 v[44:47], v[228:231], v[56:59], v[44:47]\ns_waitcnt lgkmcnt(1)\nv_mfma_f32_16x16x32_bf16 v[68:71], v[228:231], v[60:63], v[68:71]\n" \
"ds_read_b128 v[56:59], v24 offset:23552\nv_mfma_f32_16x16x32_bf16 v[36:39], a[64:67], v[80:83], v[36:39]\nds_read_b128 v[60:63], v24 offset:56320\nv_mfma_f32_16x16x32_bf16 v[72:75], a[64:67], v[84:87], v[72:75]\n" \
"v_mfma_f32_16x16x32_bf16 v[52:55], a[168:171], v[80:83], v[52:55]\nds_read_b128 v[88:91], v25 offset:19456\nv_mfma_f32_16x16x32_bf16 v[76:79], a[168:171], v[84:87], v[76:79]\nv_mfma_f32_16x16x32_bf16 v[64:67], v[128:131], v[80:83], v[64:67]\n" \
"s_waitcnt lgkmcnt(3)\nv_mfma_f32_16x16x32_bf16 v[40:43], v[128:131], v[84:87], v[40:43]\ns_waitcnt vmcnt(13) lgkmcnt(1)\ns_barrier\n" \
"v_mfma_f32_16x16x32_bf16 v[44:47], v[32:35], v[80:83], v[44:47]\nbuffer_load_dword v48, s[8:11], s99 offen offset:0 sc1 lds\nv_mfma_f32_16x16x32_bf16 v[68:71], v[32:35], v[84:87], v[68:71]\nds_read_b128 v[48:51], v24 offset:24576\n" \
"s_add_u32 m0, s32, 0x4000\nv_mfma_f32_16x16x32_bf16 v[36:39], a[68:71], v[56:59], v[36:39]\nds_read_b128 v[32:35], v24 offset:57344\nv_mfma_f32_16x16x32_bf16 v[72:75], a[68:71], v[60:63], v[72:75]\n" \
"ds_read_b128 v[80:83], v25 offset:12288\nv_mfma_f32_16x16x32_bf16 v[52:55], a[172:175], v[56:59], v[52:55]\nv_mfma_f32_16x16x32_bf16 v[76:79], a[172:175], v[60:63], v[76:79]\nv_mfma_f32_16x16x32_bf16 v[64:67], v[132:135], v[56:59], v[64:67]\n" \
"s_waitcnt lgkmcnt(3)\nv_mfma_f32_16x16x32_bf16 v[40:43], v[132:135], v[60:63], v[40:43]\nv_mfma_f32_16x16x32_bf16 v[44:47], v[88:91], v[56:59], v[44:47]\ns_waitcnt lgkmcnt(1)\n" \
"v_mfma_f32_16x16x32_bf16 v[68:71], v[88:91], v[60:63], v[68:71]\nds_read_b128 v[56:59], v24 offset:25600\nv_mfma_f32_16x16x32_bf16 v[36:39], a[72:75], v[48:51], v[36:39]\nds_read_b128 v[60:63], v24 offset:58368\n" \
"v_mfma_f32_16x16x32_bf16 v[72:75], a[72:75], v[32:35], v[72:75]\nds_read_b128 v[84:87], v25 offset:13312\nv_mfma_f32_16x16x32_bf16 v[52:55], a[176:179], v[48:51], v[52:55]\ns_waitcnt lgkmcnt(3)\n" \
"v_mfma_f32_16x16x32_bf16 v[76:79], a[176:179], v[32:35], v[76:79]\nv_mfma_f32_16x16x32_bf16 v[64:67], v[80:83], v[48:51], v[64:67]\nv_mfma_f32_16x16x32_bf16 v[40:43], v[80:83], v[32:35], v[40:43]\nv_mfma_f32_16x16x32_bf16 v[44:47], v[232:235], v[48:51], v[44:47]\n" \
"s_waitcnt lgkmcnt(1)\nv_mfma_f32_16x16x32_bf16 v[68:71], v[232:235], v[32:35], v[68:71]\nbuffer_load_dwordx4 v24, s[8:11], s95 offen offset:0 sc1 lds\nv_mfma_f32_16x16x32_bf16 v[36:39], a[76:79], v[56:59], v[36:39]\n" \
"ds_read_b128 v[32:35], v24 offset:26624\nbuffer_load_dwordx4 v24, s[8:11], s95 offen offset:1024 sc1 lds\nv_mfma_f32_16x16x32_bf16 v[72:75], a[76:79], v[60:63], v[72:75]\nds_read_b128 v[48:51], v24 offset:59392\n" \
"buffer_load_dwordx4 v24, s[8:11], s95 offen offset:2048 sc1 lds\nv_mfma_f32_16x16x32_bf16 v[52:55], a[180:183], v[56:59], v[52:55]\ns_waitcnt lgkmcnt(2)\nbuffer_load_dwordx4 v24, s[8:11], s95 offen offset:3072 sc1 lds\n" \
"v_mfma_f32_16x16x32_bf16 v[76:79], a[180:183], v[60:63], v[76:79]\nds_read_b128 v[80:83], v25 offset:20480\ns_add_u32 s94, s30, 0\nv_mfma_f32_16x16x32_bf16 v[64:67], v[84:87], v[56:59], v[64:67]\n" \
"s_and_b32 s94, s94, 63\ns_lshl_b32 s93, s94, 17\ns_add_u32 s93, s93, s35\nv_mfma_f32_16x16x32_bf16 v[40:43], v[84:87], v[60:63], v[40:43]\n" \
"s_and_b32 s99, s94, 16\ns_lshl_b32 s99, s99, 9\ns_xor_b32 s93, s93, s99\nv_mfma_f32_16x16x32_bf16 v[44:47], v[236:239], v[56:59], v[44:47]\n" \
"s_and_b32 s94, s94, 7\ns_lshl_b32 s94, s94, 14\ns_lshl1_add_u32 s99, s94, s34\nv_mfma_f32_16x16x32_bf16 v[68:71], v[236:239], v[60:63], v[68:71]\n" \
"s_waitcnt lgkmcnt(1)\nds_read_b128 v[56:59], v24 offset:27648\nv_mfma_f32_16x16x32_bf16 v[36:39], a[80:83], v[32:35], v[36:39]\nds_read_b128 v[60:63], v24 offset:60416\n" \
"s_or_b32 s98, s93, s36\nv_mfma_f32_16x16x32_bf16 v[72:75], a[80:83], v[48:51], v[72:75]\ns_or_b32 s97, s93, s37\ns_and_b32 s93, s93, s39\n" \
"s_andn2_b32 s94, s94, s39\nv_mfma_f32_16x16x32_bf16 v[52:55], a[184:187], v[32:35], v[52:55]\nds_read_b128 v[84:87], v25 offset:21504\ns_add_u32 s96, s93, s38\n" \
"v_mfma_f32_16x16x32_bf16 v[76:79], a[184:187], v[48:51], v[76:79]\ns_add_u32 s96, s96, s94\ns_add_u32 s30, s30, 1\ns_lshl_b32 s94, s30, 24\n" \
"v_mfma_f32_16x16x32_bf16 v[64:67], v[136:139], v[32:35], v[64:67]\ns_waitcnt lgkmcnt(3)\nds_read_b128 v[88:91], v25 offset:4096\nv_mfma_f32_16x16x32_bf16 v[40:43], v[136:139], v[48:51], v[40:43]\n" \
"s_cmp_le_u32 s30, 64\ns_cselect_b32 s51, s14, 0\nv_mfma_f32_16x16x32_bf16 v[44:47], v[80:83], v[32:35], v[44:47]\ns_waitcnt lgkmcnt(2)\n" \
"v_mfma_f32_16x16x32_bf16 v[68:71], v[80:83], v[48:51], v[68:71]\nds_read_b128 v[32:35], v24 offset:28672\nv_mfma_f32_16x16x32_bf16 v[36:39], a[84:87], v[56:59], v[36:39]\nds_read_b128 v[48:51], v24 offset:61440\n" \
"v_mfma_f32_16x16x32_bf16 v[72:75], a[84:87], v[60:63], v[72:75]\nv_mfma_f32_16x16x32_bf16 v[52:55], a[188:191], v[56:59], v[52:55]\nv_mfma_f32_16x16x32_bf16 v[76:79], a[188:191], v[60:63], v[76:79]\nv_mfma_f32_16x16x32_bf16 v[64:67], v[140:143], v[56:59], v[64:67]\n" \
"s_waitcnt lgkmcnt(2)\nds_read_b128 v[80:83], v25 offset:5120\nv_mfma_f32_16x16x32_bf16 v[40:43], v[140:143], v[60:63], v[40:43]\nv_mfma_f32_16x16x32_bf16 v[44:47], v[84:87], v[56:59], v[44:47]\n" \
"s_waitcnt lgkmcnt(1)\nv_mfma_f32_16x16x32_bf16 v[68:71], v[84:87], v[60:63], v[68:71]\nds_read_b128 v[56:59], v24 offset:29696\nv_mfma_f32_16x16x32_bf16 v[36:39], v[88:91], v[32:35], v[36:39]\n" \
"ds_read_b128 v[60:63], v24 offset:62464\nv_mfma_f32_16x16x32_bf16 v[72:75], v[88:91], v[48:51], v[72:75]\nv_mfma_f32_16x16x32_bf16 v[52:55], a[192:195], v[32:35], v[52:55]\nv_mfma_f32_16x16x32_bf16 v[76:79], a[192:195], v[48:51], v[76:79]\n" \
"v_mfma_f32_16x16x32_bf16 v[64:67], v[144:147], v[32:35], v[64:67]\nv_mfma_f32_16x16x32_bf16 v[40:43], v[144:147], v[48:51], v[40:43]\nv_mfma_f32_16x16x32_bf16 v[44:47], v[240:243], v[32:35], v[44:47]\ns_waitcnt lgkmcnt(0)\n" \
"v_mfma_f32_16x16x32_bf16 v[68:71], v[240:243], v[48:51], v[68:71]\nds_read_b128 v[84:87], v24 offset:30720\nv_add_u32 v32, s32, v24\nv_mfma_f32_16x16x32_bf16 v[36:39], v[80:83], v[56:59], v[36:39]\n" \
"ds_read_b128 v[48:51], v24 offset:63488\nv_mfma_f32_16x16x32_bf16 v[72:75], v[80:83], v[60:63], v[72:75]\nv_mfma_f32_16x16x32_bf16 v[52:55], a[196:199], v[56:59], v[52:55]\nv_mfma_f32_16x16x32_bf16 v[76:79], a[196:199], v[60:63], v[76:79]\n" \
"v_mfma_f32_16x16x32_bf16 v[64:67], v[148:151], v[56:59], v[64:67]\nv_mfma_f32_16x16x32_bf16 v[40:43], v[148:151], v[60:63], v[40:43]\nv_mfma_f32_16x16x32_bf16 v[44:47], v[244:247], v[56:59], v[44:47]\ns_waitcnt vmcnt(0) lgkmcnt(0)\n" \
"ds_read_b128 v[80:83], v32 offset:19456\nds_read_b128 v[88:91], v32 offset:16384\nds_read_b128 v[56:59], v32 offset:17408\nv_mfma_f32_16x16x32_bf16 v[68:71], v[244:247], v[60:63], v[68:71]\n" \
"ds_read_b128 v[92:95], v32 offset:18432\nv_mfma_f32_16x16x32_bf16 v[36:39], a[88:91], v[84:87], v[36:39]\nds_read_b128 v[60:63], v24 offset:31744\nv_mfma_f32_16x16x32_bf16 v[72:75], a[88:91], v[48:51], v[72:75]\n" \
"ds_read_b128 v[96:99], v24 offset:64512\nv_mfma_f32_16x16x32_bf16 v[52:55], a[200:203], v[84:87], v[52:55]\ns_waitcnt lgkmcnt(3)\nv_max3_u32 v33, v88, v90, v56\n" \
"v_mfma_f32_16x16x32_bf16 v[76:79], a[200:203], v[48:51], v[76:79]\ns_waitcnt lgkmcnt(2)\nv_max3_u32 v33, v33, v58, v92\nv_max3_u32 v33, v33, v94, v80\n" \
"v_mfma_f32_16x16x32_bf16 v[64:67], v[152:155], v[84:87], v[64:67]\nv_max_u32 v33, v33, v82\nv_cmp_eq_u32 vcc, -1, v33\nv_mfma_f32_16x16x32_bf16 v[40:43], v[152:155], v[48:51], v[40:43]\n" \
"ds_read_b128 v[100:103], v25 offset:0\nv_mov_b64 v[108:109], 0\nv_mfma_f32_16x16x32_bf16 v[44:47], v[248:251], v[84:87], v[44:47]\nds_read_b128 v[104:107], v25 offset:1024\n" \
"v_mov_b64 v[110:111], 0\nv_mfma_f32_16x16x32_bf16 v[68:71], v[248:251], v[48:51], v[68:71]\ns_waitcnt lgkmcnt(2)\ns_cbranch_vccnz 2f\n" \
"3:\nv_mfma_f32_16x16x32_bf16 v[36:39], a[92:95], v[60:63], v[36:39]\ns_barrier\ns_memrealtime s[56:57]\n" \
"v_mfma_f32_16x16x32_bf16 v[52:55], a[204:207], v[60:63], v[52:55]\nds_read_b128 v[32:35], v24 offset:20480\nv_mov_b64 v[48:49], 0\nv_mfma_f32_16x16x32_bf16 v[64:67], v[156:159], v[60:63], v[64:67]\n" \
"ds_read_b128 v[56:59], v24 offset:21504\nv_mov_b64 v[50:51], 0\nv_mfma_f32_16x16x32_bf16 v[44:47], v[252:255], v[60:63], v[44:47]\nds_read_b128 v[80:83], v24 offset:16384\n" \
"v_add_u32 v60, s44, v24\nv_mfma_f32_16x16x32_bf16 v[72:75], a[92:95], v[96:99], v[72:75]\nds_read_b128 v[84:87], v24 offset:22528\nv_cvt_pk_bf16_f32 v36, v36, v52\n" \
"v_mfma_f32_16x16x32_bf16 v[76:79], a[204:207], v[96:99], v[76:79]\nds_read_b128 v[88:91], v24 offset:23552\nv_cvt_pk_bf16_f32 v37, v37, v53\nv_mfma_f32_16x16x32_bf16 v[40:43], v[156:159], v[96:99], v[40:43]\n" \
"s_waitcnt lgkmcnt(4)\nds_write_b64 v60, v[36:37] offset:0\nv_mfma_f32_16x16x32_bf16 v[68:71], v[252:255], v[96:99], v[68:71]\nds_read_b128 v[92:95], v24 offset:17408\n" \
"v_cvt_pk_bf16_f32 v38, v38, v54\nv_smfmac_f32_16x16x64_bf16 v[108:111], v[32:35], v[100:107], v26\ns_waitcnt lgkmcnt(5)\nv_cvt_pk_bf16_f32 v39, v39, v55\n" \
"v_smfmac_f32_16x16x64_bf16 v[48:51], v[32:35], a[120:127], v26\nds_read_b128 v[52:55], v24 offset:18432\nv_cvt_pk_bf16_f32 v64, v64, v44\nv_smfmac_f32_16x16x64_bf16 v[108:111], v[56:59], a[24:31], v26\n" \
"s_waitcnt lgkmcnt(4)\nds_write_b64 v60, v[38:39] offset:8\nv_smfmac_f32_16x16x64_bf16 v[48:51], v[56:59], a[128:135], v26\nds_read_b128 v[96:99], v24 offset:19456\n" \
"v_cvt_pk_bf16_f32 v65, v65, v45\nv_smfmac_f32_16x16x64_bf16 v[108:111], v[80:83], v[0:7], v26\nds_write_b64 v60, v[64:65] offset:32768\nv_smfmac_f32_16x16x64_bf16 v[48:51], v[80:83], a[96:103], v26\n" \
"ds_read_b128 v[36:39], v24 offset:49152\nv_cvt_pk_bf16_f32 v66, v66, v46\nv_smfmac_f32_16x16x64_bf16 v[108:111], v[84:87], a[32:39], v26\nds_read_b128 v[80:83], v25 offset:6144\n" \
"v_cvt_pk_bf16_f32 v67, v67, v47\nv_smfmac_f32_16x16x64_bf16 v[48:51], v[84:87], a[136:143], v26\ns_waitcnt lgkmcnt(5)\nds_read_b128 v[44:47], v24 offset:50176\n" \
"v_smfmac_f32_16x16x64_bf16 v[108:111], v[88:91], a[40:47], v26\nds_read_b128 v[84:87], v25 offset:7168\ns_add_u32 s95, s56, 0x76\nv_smfmac_f32_16x16x64_bf16 v[48:51], v[88:91], a[144:151], v26\n" \
"ds_read_b128 v[62:65], v24 offset:51200\ns_and_b32 s95, s95, 0xffffff\nv_smfmac_f32_16x16x64_bf16 v[108:111], v[92:95], a[0:7], v26\nds_read_b128 v[100:103], v25 offset:2048\n" \
"s_or_b32 s95, s95, s94\nv_smfmac_f32_16x16x64_bf16 v[48:51], v[92:95], v[8:15], v26\nds_read_b128 v[104:107], v25 offset:3072\nv_mov_b32 v61, s95\n" \
"v_smfmac_f32_16x16x64_bf16 v[108:111], v[52:55], a[8:15], v26\ns_waitcnt lgkmcnt(5)\nds_write_b64 v60, v[66:67] offset:32776\nv_smfmac_f32_16x16x64_bf16 v[48:51], v[52:55], a[104:111], v26\n" \
"ds_read_b128 v[88:91], v24 offset:56320\nv_cvt_pk_bf16_f32 v72, v72, v76\nv_smfmac_f32_16x16x64_bf16 v[108:111], v[96:99], a[16:23], v26\nbuffer_store_dword v61, v29, s[8:11], s49 offen offset:0 sc1\n" \
"v_smfmac_f32_16x16x64_bf16 v[48:51], v[96:99], a[112:119], v26\nds_read_b128 v[52:55], v24 offset:52224\nv_cvt_pk_bf16_f32 v73, v73, v77\nv_smfmac_f32_16x16x64_bf16 v[108:111], v[36:39], a[48:55], v26\n" \
"s_waitcnt lgkmcnt(4)\nds_write_b64 v60, v[72:73] offset:4096\nv_smfmac_f32_16x16x64_bf16 v[48:51], v[36:39], a[152:159], v26\nds_read_b128 v[96:99], v24 offset:53248\n" \
"v_cvt_pk_bf16_f32 v74, v74, v78\nv_smfmac_f32_16x16x64_bf16 v[108:111], v[44:47], a[56:63], v26\nds_read_b128 v[36:39], v24 offset:54272\nv_cvt_pk_bf16_f32 v75, v75, v79\n" \
"v_smfmac_f32_16x16x64_bf16 v[48:51], v[44:47], v[80:87], v26\ns_waitcnt lgkmcnt(4)\nds_write_b64 v60, v[74:75] offset:4104\nv_smfmac_f32_16x16x64_bf16 v[108:111], v[62:65], v[100:107], v26\n" \
"ds_read_b128 v[44:47], v24 offset:55296\nv_cvt_pk_bf16_f32 v40, v40, v68\nv_smfmac_f32_16x16x64_bf16 v[48:51], v[62:65], a[160:167], v26\nds_read_b128 v[72:75], v25 offset:4096\n" \
"v_cvt_pk_bf16_f32 v41, v41, v69\nv_smfmac_f32_16x16x64_bf16 v[108:111], v[88:91], a[88:95], v26\nds_read_b128 v[76:79], v25 offset:5120\nv_cvt_pk_bf16_f32 v42, v42, v70\n" \
"v_smfmac_f32_16x16x64_bf16 v[48:51], v[88:91], a[200:207], v26\ns_waitcnt lgkmcnt(5)\nds_read_b128 v[62:65], v25 offset:14336\nv_smfmac_f32_16x16x64_bf16 v[108:111], v[52:55], a[64:71], v26\n" \
"ds_read_b128 v[66:69], v25 offset:15360\nv_cvt_pk_bf16_f32 v43, v43, v71\nv_smfmac_f32_16x16x64_bf16 v[48:51], v[52:55], a[168:175], v26\nds_write_b64 v60, v[40:41] offset:36864\n" \
"v_smfmac_f32_16x16x64_bf16 v[108:111], v[96:99], a[72:79], v26\nds_read_b128 v[80:83], v24 offset:51200\nv_mov_b64 v[40:41], 0\nv_smfmac_f32_16x16x64_bf16 v[48:51], v[96:99], a[176:183], v26\n" \
"s_waitcnt lgkmcnt(5)\nds_write_b64 v60, v[42:43] offset:36872\nv_smfmac_f32_16x16x64_bf16 v[108:111], v[36:39], a[80:87], v26\nds_read_b128 v[84:87], v24 offset:18432\n" \
"v_mov_b64 v[42:43], 0\nv_smfmac_f32_16x16x64_bf16 v[48:51], v[36:39], a[184:191], v26\ns_waitcnt lgkmcnt(5)\nv_mov_b64 v[36:37], 0\n" \
"v_smfmac_f32_16x16x64_bf16 v[108:111], v[44:47], v[72:79], v26\nds_read_b32 v60, v28 offset:512\nv_mov_b64 v[38:39], 0\nv_smfmac_f32_16x16x64_bf16 v[48:51], v[44:47], a[192:199], v26\n" \
"ds_read_b128 v[70:73], v24 offset:54272\nv_add_u32 v61, s45, v24\nv_smfmac_f32_16x16x64_bf16 v[40:43], v[56:59], a[240:247], v26\ns_waitcnt lgkmcnt(4)\n" \
"ds_read_b128 v[96:99], v25 offset:18432\nv_smfmac_f32_16x16x64_bf16 v[36:39], v[56:59], v[62:69], v26\nds_read_b128 v[100:103], v25 offset:19456\nv_add_f32 v56, v108, v110\n" \
"v_smfmac_f32_16x16x64_bf16 v[40:43], v[32:35], a[232:239], v26\nds_read_b128 v[62:65], v25 offset:20480\nv_add_f32 v57, v48, v50\nv_smfmac_f32_16x16x64_bf16 v[36:39], v[32:35], v[192:199], v26\n" \
"ds_read_b128 v[66:69], v25 offset:21504\nv_add_f32 v58, v109, v111\nv_smfmac_f32_16x16x64_bf16 v[40:43], v[80:83], v[120:127], v26\nds_read_b128 v[32:35], v24 offset:23552\n" \
"v_add_f32 v59, v49, v51\nv_smfmac_f32_16x16x64_bf16 v[36:39], v[80:83], v[224:231], v26\nds_read_b128 v[74:77], v25 offset:16384\nv_cvt_pk_bf16_f32 v48, v56, v57\n" \
"v_smfmac_f32_16x16x64_bf16 v[40:43], v[88:91], v[152:159], v26\nds_read_b128 v[78:81], v25 offset:17408\nv_cvt_pk_bf16_f32 v49, v58, v59\nv_smfmac_f32_16x16x64_bf16 v[36:39], v[88:91], v[248:255], v26\n" \
"ds_read_b128 v[56:59], v24 offset:49152\nv_cndmask_b32 v61, v61, v28, s[28:29]\nv_smfmac_f32_16x16x64_bf16 v[40:43], v[44:47], v[144:151], v26\ns_waitcnt lgkmcnt(5)\n" \
"ds_read_b128 v[104:107], v25 offset:10240\nv_smfmac_f32_16x16x64_bf16 v[36:39], v[44:47], v[240:247], v26\nds_read_b128 v[108:111], v25 offset:11264\nv_cmp_eq_u32 vcc, -1, v60\n" \
"v_smfmac_f32_16x16x64_bf16 v[40:43], v[84:87], v[16:23], v26\nds_read_b128 v[44:47], v24 offset:50176\nv_smfmac_f32_16x16x64_bf16 v[36:39], v[84:87], v[176:183], v26\nds_write2_b32 v27, v48, v49 offset0:0 offset1:4\n" \
"v_smfmac_f32_16x16x64_bf16 v[40:43], v[52:55], v[128:135], v26\nds_read_b128 v[82:85], v24 offset:53248\nv_smfmac_f32_16x16x64_bf16 v[36:39], v[52:55], v[96:103], v26\ns_waitcnt lgkmcnt(5)\n" \
"ds_read_b128 v[48:51], v25 offset:12288\nv_smfmac_f32_16x16x64_bf16 v[40:43], v[70:73], v[136:143], v26\nds_read_b128 v[52:55], v25 offset:13312\nv_smfmac_f32_16x16x64_bf16 v[36:39], v[70:73], v[62:69], v26\n" \
"ds_read_b128 v[86:89], v24 offset:19456\nv_mov_b32 v63, s43\nv_smfmac_f32_16x16x64_bf16 v[40:43], v[32:35], a[248:255], v26\ns_waitcnt lgkmcnt(4)\n" \
"s_barrier\nv_smfmac_f32_16x16x64_bf16 v[36:39], v[32:35], v[74:81], v26\nds_read_b128 v[64:67], v24 offset:22528\nv_cndmask_b32 v32, v63, 0, s[16:17]\n" \
"v_smfmac_f32_16x16x64_bf16 v[40:43], v[56:59], v[104:111], v26\nds_read_b128 v[68:71], v25 offset:8192\nv_cndmask_b32 v33, v63, 0, s[18:19]\nv_smfmac_f32_16x16x64_bf16 v[36:39], v[56:59], v[208:215], v26\n" \
"ds_read_b128 v[72:75], v25 offset:9216\nv_cndmask_b32 v34, v63, 0, s[20:21]\nv_smfmac_f32_16x16x64_bf16 v[40:43], v[44:47], v[112:119], v26\nds_read_b128 v[56:59], v24 offset:16384\n" \
"v_cndmask_b32 v35, v63, 0, s[22:23]\nv_smfmac_f32_16x16x64_bf16 v[36:39], v[44:47], v[216:223], v26\nds_read_b128 v[76:79], v61 offset:0\nv_mov_b32 v62, 0xff61b1e6\n" \
"v_smfmac_f32_16x16x64_bf16 v[40:43], v[92:95], a[216:223], v26\nds_read_b128 v[44:47], v24 offset:57344\nv_cndmask_b32 v80, v62, v63, s[16:17]\nv_smfmac_f32_16x16x64_bf16 v[36:39], v[92:95], v[168:175], v26\n" \
"s_waitcnt lgkmcnt(5)\nds_read_b128 v[90:93], v24 offset:61440\nv_smfmac_f32_16x16x64_bf16 v[40:43], v[82:85], v[48:55], v26\nds_read_b128 v[94:97], v61 offset:1024\n" \
"v_cndmask_b32 v81, v62, v63, s[18:19]\nv_smfmac_f32_16x16x64_bf16 v[36:39], v[82:85], v[232:239], v26\nds_read_b128 v[48:51], v24 offset:58368\nv_cndmask_b32 v82, v62, v63, s[20:21]\n" \
"v_smfmac_f32_16x16x64_bf16 v[40:43], v[86:89], a[224:231], v26\nds_read_b128 v[52:55], v24 offset:62464\nv_cndmask_b32 v83, v62, v63, s[22:23]\nv_smfmac_f32_16x16x64_bf16 v[36:39], v[86:89], v[184:191], v26\n" \
"s_waitcnt lgkmcnt(5)\nds_read_b128 v[84:87], v61 offset:2048\nv_smfmac_f32_16x16x64_bf16 v[40:43], v[64:67], v[68:75], v26\nds_read_b128 v[98:101], v24 offset:59392\n" \
"ds_read_b128 v[68:71], v24 offset:63488\nv_smfmac_f32_16x16x64_bf16 v[36:39], v[64:67], v[200:207], v26\nds_read_b128 v[72:75], v61 offset:3072\nds_read_b128 v[60:63], v24 offset:60416\n" \
"v_smfmac_f32_16x16x64_bf16 v[40:43], v[56:59], a[208:215], v26\ns_waitcnt lgkmcnt(7)\nds_read_b128 v[64:67], v24 offset:64512\nv_smfmac_f32_16x16x64_bf16 v[36:39], v[56:59], v[160:167], v26\n" \
"ds_read_b128 v[102:105], v24 offset:0\nv_mfma_f32_16x16x32_bf16 v[32:35], v[44:47], v[76:79], v[32:35]\ns_waitcnt lgkmcnt(7)\nv_add_f32 v44, v40, v42\n" \
"v_add_f32 v46, v41, v43\nv_mfma_f32_16x16x32_bf16 v[80:83], v[90:93], v[76:79], v[80:83]\nv_add_f32 v45, v36, v38\nv_add_f32 v47, v37, v39\n" \
"v_cvt_pk_bf16_f32 v36, v44, v45\nv_cvt_pk_bf16_f32 v37, v46, v47\nv_mfma_f32_16x16x32_bf16 v[32:35], v[48:51], v[94:97], v[32:35]\ns_waitcnt lgkmcnt(4)\n" \
"ds_write2_b32 v27, v36, v37 offset0:128 offset1:132\nv_mfma_f32_16x16x32_bf16 v[80:83], v[52:55], v[94:97], v[80:83]\nds_read_b128 v[48:51], v24 offset:32768\nv_mfma_f32_16x16x32_bf16 v[32:35], v[98:101], v[84:87], v[32:35]\n" \
"s_waitcnt lgkmcnt(3)\ns_cmp_eq_u64 vcc, 0\ns_cselect_b32 s14, s10, 0\nv_mfma_f32_16x16x32_bf16 v[80:83], v[68:71], v[84:87], v[80:83]\n" \
"s_cmp_eq_u32 s51, 0\ns_cbranch_scc1 4f\n5:\nv_mfma_f32_16x16x32_bf16 v[32:35], v[60:63], v[72:75], v[32:35]\n" \
"s_waitcnt lgkmcnt(1)\ns_barrier\nds_read_b128 v[40:43], v28 offset:512\nds_read_b128 v[56:59], v28 offset:1536\n" \
"v_mfma_f32_16x16x32_bf16 v[80:83], v[64:67], v[72:75], v[80:83]\nds_read_b128 v[44:47], v28 offset:2560\nds_read_b128 v[60:63], v28 offset:3584\nv_mfma_f32_16x16x32_bf16 v[36:39], v[0:3], v[102:105], 0\n" \
"ds_read_b128 v[88:91], v24 offset:1024\nv_max3_f32 v68, v32, v33, v34\nv_max3_f32 v68, v68, v35, s47\nv_mfma_f32_16x16x32_bf16 v[52:55], a[96:99], v[102:105], 0\n" \
"ds_read_b128 v[98:101], v24 offset:33792\nv_max3_f32 v92, v80, v81, v82\nv_max3_f32 v69, v68, v92, v83\nv_max3_f32 v68, v68, v92, v83\n" \
"v_mul_f32 v92, s46, v68\nv_fma_f32 v32, v32, s46, -v92\nv_fma_f32 v34, v34, s46, -v92\nv_mfma_f32_16x16x32_bf16 v[64:67], a[208:211], v[102:105], 0\n" \
"s_waitcnt lgkmcnt(4)\nv_permlane32_swap_b32 v68, v69\nv_fma_f32 v33, v33, s46, -v92\nv_fma_f32 v35, v35, s46, -v92\n" \
"v_fma_f32 v80, v80, s46, -v92\nv_fma_f32 v82, v82, s46, -v92\nv_fma_f32 v81, v81, s46, -v92\nv_mfma_f32_16x16x32_bf16 v[106:109], v[40:43], v[76:79], 0\n" \
"v_exp_f32 v32, v32\nv_exp_f32 v34, v34\nv_exp_f32 v33, v33\nv_fma_f32 v83, v83, s46, -v92\n" \
"v_mfma_f32_16x16x32_bf16 v[106:109], v[56:59], v[94:97], v[106:109]\ns_waitcnt lgkmcnt(0)\nv_exp_f32 v35, v35\nv_exp_f32 v80, v80\n" \
"v_exp_f32 v82, v82\nv_add_f32 v56, v32, v34\nv_mfma_f32_16x16x32_bf16 v[106:109], v[44:47], v[84:87], v[106:109]\nv_exp_f32 v81, v81\n" \
"v_exp_f32 v83, v83\nv_add_f32 v57, v33, v35\nv_add_f32 v58, v80, v82\nv_add_f32 v56, v56, v57\n" \
"v_mfma_f32_16x16x32_bf16 v[106:109], v[60:63], v[72:75], v[106:109]\nv_add_f32 v59, v81, v83\nv_add_f32 v56, v56, v58\nv_add_f32 v56, v56, v59\n" \
"v_mfma_f32_16x16x32_bf16 v[44:47], v[160:163], v[102:105], 0\nv_cndmask_b32 v60, v32, v34, s[24:25]\nv_cndmask_b32 v61, v33, v35, s[24:25]\nv_mfma_f32_16x16x32_bf16 v[72:75], v[0:3], v[48:51], 0\n" \
"v_max3_f32 v40, v106, v107, v108\nv_max3_f32 v68, v68, v69, v109\nv_max_f32 v41, v40, v68\nv_max_f32 v40, v40, v68\n" \
"v_cndmask_b32 v84, v106, v108, s[26:27]\nv_cndmask_b32 v85, v107, v109, s[26:27]\nv_mfma_f32_16x16x32_bf16 v[76:79], a[96:99], v[48:51], 0\nv_permlane16_swap_b32 v40, v41\n" \
"v_max_f32 v40, v40, v41\nv_fma_f32 v92, v40, -s46, v92\nv_mul_f32 v40, s46, v40\nv_fma_f32 v84, v84, s46, -v40\n" \
"v_fma_f32 v85, v85, s46, -v40\nv_exp_f32 v92, v92\nv_exp_f32 v84, v84\nv_exp_f32 v85, v85\n" \
"v_mfma_f32_16x16x32_bf16 v[40:43], a[208:211], v[48:51], 0\nv_fma_f32 v56, v56, v92, v84\nv_add_f32 v57, v56, v85\nv_add_f32 v56, v85, v56\n" \
"v_mfma_f32_16x16x32_bf16 v[68:71], v[160:163], v[48:51], 0\nv_cndmask_b32 v62, v80, v82, s[24:25]\nv_permlane16_swap_b32 v56, v57\nv_add_f32 v58, v56, v57\n" \
"v_add_f32 v56, v57, v56\nv_cndmask_b32 v63, v81, v83, s[24:25]\nv_mfma_f32_16x16x32_bf16 v[36:39], v[4:7], v[88:91], v[36:39]\nds_read_b128 v[48:51], v24 offset:2048\n" \
"v_permlane32_swap_b32 v56, v58\nv_add_f32 v56, v56, v58\nv_rcp_f32 v56, v56\nv_mfma_f32_16x16x32_bf16 v[72:75], v[4:7], v[98:101], v[72:75]\n" \
"ds_read_b128 v[94:97], v24 offset:34816\nv_mul_f32 v92, v92, v56\nv_mul_f32 v60, v60, v92\nv_mul_f32 v61, v61, v92\n" \
"v_mul_f32 v84, v84, v56\nv_mul_f32 v85, v85, v56\nv_mfma_f32_16x16x32_bf16 v[52:55], a[100:103], v[88:91], v[52:55]\nv_mul_f32 v62, v62, v92\n" \
"v_mul_f32 v63, v63, v92\nv_cvt_pk_bf16_f32 v56, v60, v61\nv_cvt_pk_bf16_f32 v58, v84, v85\nv_cvt_pk_bf16_f32 v57, v62, v63\n" \
"v_min_u32 v56, -2, v56\nv_min_u32 v58, -2, v58\nv_mfma_f32_16x16x32_bf16 v[76:79], a[100:103], v[98:101], v[76:79]\nbuffer_store_dwordx2 v[56:57], v29, s[8:11], s99 offen offset:0 sc1\n" \
"buffer_store_dword v58, v30, s[8:11], s96 offen offset:1536 sc1\nv_mfma_f32_16x16x32_bf16 v[64:67], a[212:215], v[88:91], v[64:67]\ns_add_u32 m0, s32, 0x4000\nv_mul_f32 v32, v32, v92\n" \
"v_mul_f32 v33, v33, v92\nv_mfma_f32_16x16x32_bf16 v[40:43], a[212:215], v[98:101], v[40:43]\ns_add_u32 s99, s31, 0x4000\nv_mul_f32 v34, v34, v92\n" \
"v_mul_f32 v35, v35, v92\nv_mfma_f32_16x16x32_bf16 v[44:47], v[164:167], v[88:91], v[44:47]\ns_waitcnt lgkmcnt(0)\nv_cvt_pk_bf16_f32 v56, v32, v33\n" \
"v_cvt_pk_bf16_f32 v57, v34, v35\nv_mfma_f32_16x16x32_bf16 v[68:71], v[164:167], v[98:101], v[68:71]\nds_read_b128 v[32:35], v24 offset:3072\nv_mul_f32 v80, v80, v92\n" \
"v_mfma_f32_16x16x32_bf16 v[36:39], a[0:3], v[48:51], v[36:39]\nds_read_b128 v[60:63], v24 offset:35840\nv_mul_f32 v81, v81, v92\nv_mfma_f32_16x16x32_bf16 v[72:75], a[0:3], v[94:97], v[72:75]\n" \
"buffer_store_dwordx2 v[56:57], v29, s[8:11], s97 offen offset:1024 sc1\nv_mul_f32 v82, v82, v92\nv_mfma_f32_16x16x32_bf16 v[52:55], v[8:11], v[48:51], v[52:55]\nv_mul_f32 v83, v83, v92\n" \
"v_cvt_pk_bf16_f32 v58, v80, v81\nv_mfma_f32_16x16x32_bf16 v[76:79], v[8:11], v[94:97], v[76:79]\nv_cvt_pk_bf16_f32 v59, v82, v83\nv_mfma_f32_16x16x32_bf16 v[64:67], a[216:219], v[48:51], v[64:67]\n" \
"buffer_store_dwordx4 v[56:59], v24, s[8:11], s98 offen offset:0 sc1\nbuffer_load_dwordx4 v24, s[8:11], s99 offen offset:0 sc1 lds\nv_mfma_f32_16x16x32_bf16 v[40:43], a[216:219], v[94:97], v[40:43]\nbuffer_load_dwordx4 v24, s[8:11], s99 offen offset:1024 sc1 lds\n" \
"v_mfma_f32_16x16x32_bf16 v[44:47], v[168:171], v[48:51], v[44:47]\ns_waitcnt lgkmcnt(0)\nbuffer_load_dwordx4 v24, s[8:11], s99 offen offset:2048 sc1 lds\nv_mfma_f32_16x16x32_bf16 v[68:71], v[168:171], v[94:97], v[68:71]\n" \
"ds_read_b128 v[48:51], v24 offset:4096\nv_mfma_f32_16x16x32_bf16 v[36:39], a[4:7], v[32:35], v[36:39]\nds_read_b128 v[56:59], v24 offset:36864\nv_mfma_f32_16x16x32_bf16 v[72:75], a[4:7], v[60:63], v[72:75]\n" \
"buffer_load_dwordx4 v24, s[8:11], s99 offen offset:3072 sc1 lds\nv_mfma_f32_16x16x32_bf16 v[52:55], v[12:15], v[32:35], v[52:55]\nv_mfma_f32_16x16x32_bf16 v[76:79], v[12:15], v[60:63], v[76:79]\nv_mfma_f32_16x16x32_bf16 v[64:67], a[220:223], v[32:35], v[64:67]\n" \
"v_mfma_f32_16x16x32_bf16 v[40:43], a[220:223], v[60:63], v[40:43]\nv_mfma_f32_16x16x32_bf16 v[44:47], v[172:175], v[32:35], v[44:47]\ns_waitcnt lgkmcnt(0)\nv_mfma_f32_16x16x32_bf16 v[68:71], v[172:175], v[60:63], v[68:71]\n" \
"ds_read_b128 v[32:35], v24 offset:5120\nv_mfma_f32_16x16x32_bf16 v[36:39], a[8:11], v[48:51], v[36:39]\nds_read_b128 v[80:83], v24 offset:37888\nv_mfma_f32_16x16x32_bf16 v[72:75], a[8:11], v[56:59], v[72:75]\n" \
"v_mfma_f32_16x16x32_bf16 v[52:55], a[104:107], v[48:51], v[52:55]\nv_mfma_f32_16x16x32_bf16 v[76:79], a[104:107], v[56:59], v[76:79]\nv_mfma_f32_16x16x32_bf16 v[64:67], v[16:19], v[48:51], v[64:67]\nv_mfma_f32_16x16x32_bf16 v[40:43], v[16:19], v[56:59], v[40:43]\n" \
"v_mfma_f32_16x16x32_bf16 v[44:47], v[176:179], v[48:51], v[44:47]\ns_waitcnt lgkmcnt(0)\nv_mfma_f32_16x16x32_bf16 v[68:71], v[176:179], v[56:59], v[68:71]\nds_read_b128 v[48:51], v24 offset:6144\n" \
"v_mfma_f32_16x16x32_bf16 v[36:39], a[12:15], v[32:35], v[36:39]\nds_read_b128 v[56:59], v24 offset:38912\nv_mfma_f32_16x16x32_bf16 v[72:75], a[12:15], v[80:83], v[72:75]\nv_mfma_f32_16x16x32_bf16 v[52:55], a[108:111], v[32:35], v[52:55]\n" \
"v_mfma_f32_16x16x32_bf16 v[76:79], a[108:111], v[80:83], v[76:79]\nv_mfma_f32_16x16x32_bf16 v[64:67], v[20:23], v[32:35], v[64:67]\nv_mfma_f32_16x16x32_bf16 v[40:43], v[20:23], v[80:83], v[40:43]\nv_mfma_f32_16x16x32_bf16 v[44:47], v[180:183], v[32:35], v[44:47]\n" \
"s_waitcnt lgkmcnt(0)\nv_mfma_f32_16x16x32_bf16 v[68:71], v[180:183], v[80:83], v[68:71]\nds_read_b128 v[60:63], v24 offset:7168\nv_mfma_f32_16x16x32_bf16 v[36:39], a[16:19], v[48:51], v[36:39]\n" \
"ds_read_b128 v[32:35], v24 offset:39936\nv_mfma_f32_16x16x32_bf16 v[72:75], a[16:19], v[56:59], v[72:75]\nv_mfma_f32_16x16x32_bf16 v[52:55], a[112:115], v[48:51], v[52:55]\nv_mfma_f32_16x16x32_bf16 v[76:79], a[112:115], v[56:59], v[76:79]\n" \
"s_add_u32 m0, s32, 0x6000\ns_add_u32 s99, s31, 0x6000\nv_mfma_f32_16x16x32_bf16 v[64:67], a[224:227], v[48:51], v[64:67]\nds_read_b128 v[80:83], v25 offset:0\n" \
"v_mfma_f32_16x16x32_bf16 v[40:43], a[224:227], v[56:59], v[40:43]\nv_mfma_f32_16x16x32_bf16 v[44:47], v[184:187], v[48:51], v[44:47]\ns_waitcnt lgkmcnt(1)\nv_mfma_f32_16x16x32_bf16 v[68:71], v[184:187], v[56:59], v[68:71]\n" \
"ds_read_b128 v[48:51], v24 offset:8192\ns_add_u32 s98, s30, 1\nv_mfma_f32_16x16x32_bf16 v[36:39], a[20:23], v[60:63], v[36:39]\nds_read_b128 v[56:59], v24 offset:40960\n" \
"s_and_b32 s98, s98, 63\nv_mfma_f32_16x16x32_bf16 v[72:75], a[20:23], v[32:35], v[72:75]\nbuffer_load_dwordx4 v24, s[8:11], s99 offen offset:0 sc1 lds\nv_mfma_f32_16x16x32_bf16 v[52:55], a[116:119], v[60:63], v[52:55]\n" \
"s_lshl_b32 s97, s98, 17\ns_add_u32 s97, s97, s35\ns_and_b32 s96, s98, 16\nv_mfma_f32_16x16x32_bf16 v[76:79], a[116:119], v[32:35], v[76:79]\n" \
"buffer_load_dwordx4 v24, s[8:11], s99 offen offset:1024 sc1 lds\nv_mfma_f32_16x16x32_bf16 v[64:67], a[228:231], v[60:63], v[64:67]\nds_read_b128 v[84:87], v25 offset:1024\ns_lshl_b32 s96, s96, 9\n" \
"v_mfma_f32_16x16x32_bf16 v[40:43], a[228:231], v[32:35], v[40:43]\ns_xor_b32 s97, s97, s96\ns_and_b32 s98, s98, 7\ns_lshl_b32 s96, s98, 20\n" \
"v_mfma_f32_16x16x32_bf16 v[44:47], v[188:191], v[60:63], v[44:47]\ns_and_b32 s95, s98, 2\ns_lshl_b32 s98, s98, s41\ns_and_b32 s97, s97, s39\n" \
"v_mfma_f32_16x16x32_bf16 v[68:71], v[188:191], v[32:35], v[68:71]\ns_waitcnt lgkmcnt(1)\nds_read_b128 v[32:35], v24 offset:9216\nv_mfma_f32_16x16x32_bf16 v[36:39], v[80:83], v[48:51], v[36:39]\n" \
"ds_read_b128 v[60:63], v24 offset:41984\ns_andn2_b32 s98, s98, s39\nv_mfma_f32_16x16x32_bf16 v[72:75], v[80:83], v[56:59], v[72:75]\ns_add_u32 s97, s97, s40\n" \
"s_add_u32 s97, s97, s98\ns_add_u32 s96, s96, s55\nv_mfma_f32_16x16x32_bf16 v[52:55], a[120:123], v[48:51], v[52:55]\nbuffer_load_dwordx4 v24, s[8:11], s99 offen offset:2048 sc1 lds\n" \
"v_mfma_f32_16x16x32_bf16 v[76:79], a[120:123], v[56:59], v[76:79]\nbuffer_load_dwordx4 v24, s[8:11], s99 offen offset:3072 sc1 lds\nv_mfma_f32_16x16x32_bf16 v[64:67], a[232:235], v[48:51], v[64:67]\ns_lshl_b32 s95, s95, 12\n" \
"s_xor_b32 s96, s96, s95\nv_mfma_f32_16x16x32_bf16 v[40:43], a[232:235], v[56:59], v[40:43]\nv_mfma_f32_16x16x32_bf16 v[44:47], v[192:195], v[48:51], v[44:47]\ns_waitcnt lgkmcnt(0)\n" \
"v_mfma_f32_16x16x32_bf16 v[68:71], v[192:195], v[56:59], v[68:71]\nds_read_b128 v[48:51], v24 offset:10240\nv_and_b32 v56, s42, v24\nv_mfma_f32_16x16x32_bf16 v[36:39], v[84:87], v[32:35], v[36:39]\n" \
"ds_read_b128 v[80:83], v24 offset:43008\nv_mov_b64 v[88:89], -1\nv_mfma_f32_16x16x32_bf16 v[72:75], v[84:87], v[60:63], v[72:75]\nv_mov_b64 v[90:91], -1\n" \
"v_lshl_add_u32 v56, v56, 21, v24\nv_mfma_f32_16x16x32_bf16 v[52:55], a[124:127], v[32:35], v[52:55]\nds_read_b128 v[84:87], v25 offset:14336\nv_mfma_f32_16x16x32_bf16 v[76:79], a[124:127], v[60:63], v[76:79]\n" \
"buffer_store_dwordx4 v[88:91], v56, s[8:11], s97 offen offset:0 sc1\nv_mfma_f32_16x16x32_bf16 v[64:67], a[236:239], v[32:35], v[64:67]\nv_mfma_f32_16x16x32_bf16 v[40:43], a[236:239], v[60:63], v[40:43]\nv_mfma_f32_16x16x32_bf16 v[44:47], v[196:199], v[32:35], v[44:47]\n" \
"s_waitcnt lgkmcnt(1)\nv_mfma_f32_16x16x32_bf16 v[68:71], v[196:199], v[60:63], v[68:71]\nds_read_b128 v[32:35], v24 offset:11264\nv_mfma_f32_16x16x32_bf16 v[36:39], a[24:27], v[48:51], v[36:39]\n" \
"ds_read_b128 v[56:59], v24 offset:44032\nv_mfma_f32_16x16x32_bf16 v[72:75], a[24:27], v[80:83], v[72:75]\nv_mfma_f32_16x16x32_bf16 v[52:55], a[128:131], v[48:51], v[52:55]\nds_read_b128 v[60:63], v25 offset:15360\n" \
"v_mfma_f32_16x16x32_bf16 v[76:79], a[128:131], v[80:83], v[76:79]\nbuffer_store_dwordx4 v[88:91], v24, s[8:11], s96 offen offset:0 sc1\nv_mfma_f32_16x16x32_bf16 v[64:67], a[240:243], v[48:51], v[64:67]\ns_waitcnt lgkmcnt(3)\n" \
"v_mfma_f32_16x16x32_bf16 v[40:43], a[240:243], v[80:83], v[40:43]\nv_mfma_f32_16x16x32_bf16 v[44:47], v[84:87], v[48:51], v[44:47]\ns_waitcnt lgkmcnt(1)\nv_mfma_f32_16x16x32_bf16 v[68:71], v[84:87], v[80:83], v[68:71]\n" \
"ds_read_b128 v[48:51], v24 offset:12288\nv_mfma_f32_16x16x32_bf16 v[36:39], a[28:31], v[32:35], v[36:39]\nds_read_b128 v[80:83], v24 offset:45056\nv_mfma_f32_16x16x32_bf16 v[72:75], a[28:31], v[56:59], v[72:75]\n" \
"ds_read_b128 v[84:87], v25 offset:8192\nv_mfma_f32_16x16x32_bf16 v[52:55], a[132:135], v[32:35], v[52:55]\nv_mfma_f32_16x16x32_bf16 v[76:79], a[132:135], v[56:59], v[76:79]\nbuffer_store_dwordx4 v[88:91], v24, s[8:11], s96 offen offset:1024 sc1\n" \
"v_mfma_f32_16x16x32_bf16 v[64:67], a[244:247], v[32:35], v[64:67]\ns_waitcnt lgkmcnt(3)\nv_mfma_f32_16x16x32_bf16 v[40:43], a[244:247], v[56:59], v[40:43]\nv_mfma_f32_16x16x32_bf16 v[44:47], v[60:63], v[32:35], v[44:47]\n" \
"s_waitcnt lgkmcnt(1)\nv_mfma_f32_16x16x32_bf16 v[68:71], v[60:63], v[56:59], v[68:71]\nds_read_b128 v[32:35], v24 offset:13312\nv_mfma_f32_16x16x32_bf16 v[36:39], a[32:35], v[48:51], v[36:39]\n" \
"ds_read_b128 v[56:59], v24 offset:46080\nv_mfma_f32_16x16x32_bf16 v[72:75], a[32:35], v[80:83], v[72:75]\nds_read_b128 v[60:63], v25 offset:9216\nv_mfma_f32_16x16x32_bf16 v[52:55], a[136:139], v[48:51], v[52:55]\n" \
"s_waitcnt lgkmcnt(3)\nv_mfma_f32_16x16x32_bf16 v[76:79], a[136:139], v[80:83], v[76:79]\nbuffer_store_dwordx4 v[88:91], v24, s[8:11], s96 offen offset:2048 sc1\nv_mfma_f32_16x16x32_bf16 v[64:67], v[84:87], v[48:51], v[64:67]\n" \
"v_mfma_f32_16x16x32_bf16 v[40:43], v[84:87], v[80:83], v[40:43]\nv_mfma_f32_16x16x32_bf16 v[44:47], v[200:203], v[48:51], v[44:47]\ns_waitcnt lgkmcnt(1)\nv_mfma_f32_16x16x32_bf16 v[68:71], v[200:203], v[80:83], v[68:71]\n" \
"ds_read_b128 v[48:51], v24 offset:14336\nv_mfma_f32_16x16x32_bf16 v[36:39], a[36:39], v[32:35], v[36:39]\nds_read_b128 v[80:83], v24 offset:47104\nv_mfma_f32_16x16x32_bf16 v[72:75], a[36:39], v[56:59], v[72:75]\n" \
"v_mfma_f32_16x16x32_bf16 v[52:55], a[140:143], v[32:35], v[52:55]\ns_waitcnt lgkmcnt(2)\nds_read_b128 v[84:87], v25 offset:16384\nv_mfma_f32_16x16x32_bf16 v[76:79], a[140:143], v[56:59], v[76:79]\n" \
"buffer_store_dwordx4 v[88:91], v24, s[8:11], s96 offen offset:3072 sc1\nv_mfma_f32_16x16x32_bf16 v[64:67], v[60:63], v[32:35], v[64:67]\nv_mfma_f32_16x16x32_bf16 v[40:43], v[60:63], v[56:59], v[40:43]\nv_mfma_f32_16x16x32_bf16 v[44:47], v[204:207], v[32:35], v[44:47]\n" \
"s_waitcnt lgkmcnt(1)\nv_mfma_f32_16x16x32_bf16 v[68:71], v[204:207], v[56:59], v[68:71]\nds_read_b128 v[32:35], v24 offset:15360\nv_mfma_f32_16x16x32_bf16 v[36:39], a[40:43], v[48:51], v[36:39]\n" \
"ds_read_b128 v[56:59], v24 offset:48128\nv_mfma_f32_16x16x32_bf16 v[72:75], a[40:43], v[80:83], v[72:75]\nv_mfma_f32_16x16x32_bf16 v[52:55], a[144:147], v[48:51], v[52:55]\nds_read_b128 v[60:63], v25 offset:17408\n" \
"v_mfma_f32_16x16x32_bf16 v[76:79], a[144:147], v[80:83], v[76:79]\nv_mfma_f32_16x16x32_bf16 v[64:67], a[248:251], v[48:51], v[64:67]\ns_waitcnt lgkmcnt(3)\nv_mfma_f32_16x16x32_bf16 v[40:43], a[248:251], v[80:83], v[40:43]\n" \
"s_waitcnt vmcnt(9) lgkmcnt(1)\ns_barrier\nv_mfma_f32_16x16x32_bf16 v[44:47], v[84:87], v[48:51], v[44:47]\ns_add_u32 m0, s32, 0\n" \
"s_add_u32 s99, s31, 0x10000\nv_mfma_f32_16x16x32_bf16 v[68:71], v[84:87], v[80:83], v[68:71]\nds_read_b128 v[48:51], v24 offset:16384\nv_mfma_f32_16x16x32_bf16 v[36:39], a[44:47], v[32:35], v[36:39]\n" \
"ds_read_b128 v[80:83], v24 offset:49152\nv_mfma_f32_16x16x32_bf16 v[72:75], a[44:47], v[56:59], v[72:75]\nds_read_b128 v[84:87], v25 offset:10240\nv_mfma_f32_16x16x32_bf16 v[52:55], a[148:151], v[32:35], v[52:55]\n" \
"buffer_load_dwordx4 v24, s[12:15], s99 offen offset:0 sc1 lds\nv_mfma_f32_16x16x32_bf16 v[76:79], a[148:151], v[56:59], v[76:79]\nv_mfma_f32_16x16x32_bf16 v[64:67], a[252:255], v[32:35], v[64:67]\ns_waitcnt lgkmcnt(3)\n" \
"v_mfma_f32_16x16x32_bf16 v[40:43], a[252:255], v[56:59], v[40:43]\nbuffer_load_dwordx4 v24, s[12:15], s99 offen offset:1024 sc1 lds\nv_mfma_f32_16x16x32_bf16 v[44:47], v[60:63], v[32:35], v[44:47]\ns_waitcnt lgkmcnt(1)\n" \
"v_mfma_f32_16x16x32_bf16 v[68:71], v[60:63], v[56:59], v[68:71]\nds_read_b128 v[32:35], v24 offset:17408\nv_mfma_f32_16x16x32_bf16 v[36:39], a[48:51], v[48:51], v[36:39]\nds_read_b128 v[56:59], v24 offset:50176\n" \
"v_mfma_f32_16x16x32_bf16 v[72:75], a[48:51], v[80:83], v[72:75]\nds_read_b128 v[60:63], v25 offset:11264\nv_mfma_f32_16x16x32_bf16 v[52:55], a[152:155], v[48:51], v[52:55]\ns_waitcnt lgkmcnt(3)\n" \
"buffer_load_dwordx4 v24, s[12:15], s99 offen offset:2048 sc1 lds\nv_mfma_f32_16x16x32_bf16 v[76:79], a[152:155], v[80:83], v[76:79]\nv_mfma_f32_16x16x32_bf16 v[64:67], v[84:87], v[48:51], v[64:67]\nv_mfma_f32_16x16x32_bf16 v[40:43], v[84:87], v[80:83], v[40:43]\n" \
"buffer_load_dwordx4 v24, s[12:15], s99 offen offset:3072 sc1 lds\nv_mfma_f32_16x16x32_bf16 v[44:47], v[208:211], v[48:51], v[44:47]\ns_waitcnt lgkmcnt(1)\nds_read_b128 v[48:51], v25 offset:6144\n" \
"v_mfma_f32_16x16x32_bf16 v[68:71], v[208:211], v[80:83], v[68:71]\nds_read_b128 v[84:87], v24 offset:18432\nv_mfma_f32_16x16x32_bf16 v[36:39], a[52:55], v[32:35], v[36:39]\nds_read_b128 v[80:83], v24 offset:51200\n" \
"s_add_u32 m0, s32, 0x2000\nv_mfma_f32_16x16x32_bf16 v[72:75], a[52:55], v[56:59], v[72:75]\ns_add_u32 s99, s31, 0x12000\nv_mfma_f32_16x16x32_bf16 v[52:55], a[156:159], v[32:35], v[52:55]\n" \
"s_waitcnt lgkmcnt(3)\nv_mfma_f32_16x16x32_bf16 v[76:79], a[156:159], v[56:59], v[76:79]\nv_mfma_f32_16x16x32_bf16 v[64:67], v[60:63], v[32:35], v[64:67]\nbuffer_load_dwordx4 v24, s[12:15], s99 offen offset:0 sc1 lds\n" \
"v_mfma_f32_16x16x32_bf16 v[40:43], v[60:63], v[56:59], v[40:43]\nv_mfma_f32_16x16x32_bf16 v[44:47], v[212:215], v[32:35], v[44:47]\ns_waitcnt lgkmcnt(0)\nds_read_b128 v[32:35], v25 offset:7168\n" \
"v_mfma_f32_16x16x32_bf16 v[68:71], v[212:215], v[56:59], v[68:71]\nds_read_b128 v[60:63], v24 offset:19456\nv_mfma_f32_16x16x32_bf16 v[36:39], a[56:59], v[84:87], v[36:39]\nds_read_b128 v[56:59], v24 offset:52224\n" \
"v_mfma_f32_16x16x32_bf16 v[72:75], a[56:59], v[80:83], v[72:75]\nbuffer_load_dwordx4 v24, s[12:15], s99 offen offset:1024 sc1 lds\nv_mfma_f32_16x16x32_bf16 v[52:55], v[48:51], v[84:87], v[52:55]\nv_mfma_f32_16x16x32_bf16 v[76:79], v[48:51], v[80:83], v[76:79]\n" \
"v_mfma_f32_16x16x32_bf16 v[64:67], v[112:115], v[84:87], v[64:67]\nds_read_b128 v[48:51], v25 offset:2048\nv_mfma_f32_16x16x32_bf16 v[40:43], v[112:115], v[80:83], v[40:43]\nbuffer_load_dwordx4 v24, s[12:15], s99 offen offset:2048 sc1 lds\n" \
"v_mfma_f32_16x16x32_bf16 v[44:47], v[216:219], v[84:87], v[44:47]\ns_waitcnt lgkmcnt(1)\nv_mfma_f32_16x16x32_bf16 v[68:71], v[216:219], v[80:83], v[68:71]\nds_read_b128 v[84:87], v24 offset:20480\n" \
"v_mfma_f32_16x16x32_bf16 v[36:39], a[60:63], v[60:63], v[36:39]\nds_read_b128 v[80:83], v24 offset:53248\nv_mfma_f32_16x16x32_bf16 v[72:75], a[60:63], v[56:59], v[72:75]\nbuffer_load_dwordx4 v24, s[12:15], s99 offen offset:3072 sc1 lds\n" \
"v_mfma_f32_16x16x32_bf16 v[52:55], v[32:35], v[60:63], v[52:55]\nv_mfma_f32_16x16x32_bf16 v[76:79], v[32:35], v[56:59], v[76:79]\nv_mfma_f32_16x16x32_bf16 v[64:67], v[116:119], v[60:63], v[64:67]\nv_mfma_f32_16x16x32_bf16 v[40:43], v[116:119], v[56:59], v[40:43]\n" \
"v_mfma_f32_16x16x32_bf16 v[44:47], v[220:223], v[60:63], v[44:47]\nv_mfma_f32_16x16x32_bf16 v[68:71], v[220:223], v[56:59], v[68:71]\ns_branch 1b\n2:\n" \
"s_add_u32 s52, s52, 1\ns_cmp_lt_u32 s52, 0x40000\ns_cbranch_scc0 9f\nv_max_u32 v33, v88, v90\n" \
"v_cmp_eq_u32 vcc, -1, v33\ns_cbranch_vccz 6f\nbuffer_load_dwordx4 v24, s[8:11], s95 offen offset:0 sc1 lds\n6:\n" \
"v_max_u32 v33, v56, v58\nv_cmp_eq_u32 vcc, -1, v33\ns_cbranch_vccz 6f\nbuffer_load_dwordx4 v24, s[8:11], s95 offen offset:1024 sc1 lds\n" \
"6:\nv_max_u32 v33, v92, v94\nv_cmp_eq_u32 vcc, -1, v33\ns_cbranch_vccz 6f\n" \
"buffer_load_dwordx4 v24, s[8:11], s95 offen offset:2048 sc1 lds\n6:\nv_max_u32 v33, v80, v82\nv_cmp_eq_u32 vcc, -1, v33\n" \
"s_cbranch_vccz 6f\nbuffer_load_dwordx4 v24, s[8:11], s95 offen offset:3072 sc1 lds\n6:\ns_waitcnt vmcnt(0)\n" \
"ds_read_b128 v[88:91], v32 offset:16384\nds_read_b128 v[56:59], v32 offset:17408\nds_read_b128 v[92:95], v32 offset:18432\nds_read_b128 v[80:83], v32 offset:19456\n" \
"s_waitcnt lgkmcnt(0)\nv_max3_u32 v33, v88, v90, v56\nv_max3_u32 v33, v33, v58, v92\nv_max3_u32 v33, v33, v94, v80\n" \
"v_max_u32 v33, v33, v82\nv_cmp_eq_u32 vcc, -1, v33\ns_cbranch_vccnz 2b\ns_branch 3b\n" \
"4:\ns_cmp_gt_u32 s30, 64\ns_cbranch_scc1 9f\ns_cmp_eq_u32 s30, 64\n" \
"s_cbranch_scc1 5b\nv_bfe_u32 v36, v24, 6, 3\nv_lshlrev_b32 v36, 2, v36\ns_lshl_b32 s95, s30, 5\n" \
"s_add_u32 s95, s95, s48\n7:\nbuffer_load_dword v37, v36, s[8:11], s95 offen sc1\ns_waitcnt vmcnt(0)\n" \
"v_cmp_eq_u32 vcc, -1, v37\ns_cbranch_vccz 8f\ns_add_u32 s52, s52, 1\ns_cmp_lt_u32 s52, 0x40000\n" \
"s_cbranch_scc0 9f\ns_sleep 8\ns_branch 7b\n8:\n" \
"s_mov_b32 m0, s53\ns_add_u32 s95, s30, 1\ns_min_u32 s95, s95, 63\ns_lshl_b32 s95, s95, 5\n" \
"s_add_u32 s95, s95, s48\nbuffer_load_dword v36, s[8:11], s95 offen offset:0 sc1 lds\ns_add_u32 m0, s32, 0\ns_add_u32 s95, s31, 0\n" \
"buffer_load_dwordx4 v24, s[8:11], s95 offen offset:0 sc1 lds\nbuffer_load_dwordx4 v24, s[8:11], s95 offen offset:1024 sc1 lds\nbuffer_load_dwordx4 v24, s[8:11], s95 offen offset:2048 sc1 lds\nbuffer_load_dwordx4 v24, s[8:11], s95 offen offset:3072 sc1 lds\n" \
"s_add_u32 m0, s32, 0x2000\ns_add_u32 s95, s31, 0x2000\nbuffer_load_dwordx4 v24, s[8:11], s95 offen offset:0 sc1 lds\nbuffer_load_dwordx4 v24, s[8:11], s95 offen offset:1024 sc1 lds\n" \
"buffer_load_dwordx4 v24, s[8:11], s95 offen offset:2048 sc1 lds\nbuffer_load_dwordx4 v24, s[8:11], s95 offen offset:3072 sc1 lds\nv_lshrrev_b32 v36, 2, v24\nv_add_u32 v36, 0x11000, v36\n" \
"s_waitcnt vmcnt(0) lgkmcnt(0)\ns_barrier\nds_read_b32 v37, v36 offset:0\nds_read_b128 v[102:105], v24 offset:0\n" \
"ds_read_b128 v[48:51], v24 offset:32768\ns_waitcnt lgkmcnt(0)\nv_cmp_eq_u32 vcc, -1, v37\ns_cmp_eq_u64 vcc, 0\n" \
"s_cselect_b32 s14, s10, 0\ns_branch 5b\n9:\ns_waitcnt vmcnt(0)\n" \
"s_endpgm\n"
#define B_LDS 0x44840c0202a00110ull
#define B_VAR 0x0000000000010001ull
#define B_LV 8192
#define B_LOOP \
"v_mov_b32 v110, v20\ns_branch 4f\n.p2align 6\n1:\n" \
"s_waitcnt lgkmcnt(4)\nv_mfma_f32_16x16x32_bf16 v[32:35], a[8:11], v[20:23], v[32:35]\ns_add_u32 m0, s22, 0\ns_add_u32 s43, s20, 7\n" \
"s_and_b32 s43, s43, 7\nv_mfma_f32_16x16x32_bf16 v[52:55], a[8:11], v[60:63], v[52:55]\ns_lshl_b32 s43, s43, 18\ns_add_u32 s43, s43, s23\n" \
"v_mfma_f32_16x16x32_bf16 v[56:59], a[112:115], v[20:23], v[56:59]\nds_read_b128 v[28:31], v17 offset:0\nv_mfma_f32_16x16x32_bf16 v[44:47], a[112:115], v[60:63], v[44:47]\nv_mfma_f32_16x16x32_bf16 v[32:35], a[12:15], v[40:43], v[32:35]\n" \
"s_waitcnt vmcnt(8) lgkmcnt(1)\ns_barrier\nv_mfma_f32_16x16x32_bf16 v[52:55], a[12:15], v[64:67], v[52:55]\nds_read_b128 v[20:23], v16 offset:8192\n" \
"v_mfma_f32_16x16x32_bf16 v[56:59], a[116:119], v[40:43], v[56:59]\nds_read_b128 v[48:51], v16 offset:40960\nv_mfma_f32_16x16x32_bf16 v[44:47], a[116:119], v[64:67], v[44:47]\nds_read_b128 v[40:43], v16 offset:9216\n" \
"v_mfma_f32_16x16x32_bf16 v[32:35], a[16:19], v[24:27], v[32:35]\nds_read_b128 v[60:63], v17 offset:1024\nv_mfma_f32_16x16x32_bf16 v[52:55], a[16:19], v[36:39], v[52:55]\nbuffer_load_dwordx4 v16, s[8:11], s43 offen offset:0 sc1 lds\n" \
"v_mfma_f32_16x16x32_bf16 v[56:59], a[120:123], v[24:27], v[56:59]\nds_read_b128 v[64:67], v16 offset:41984\nv_mfma_f32_16x16x32_bf16 v[44:47], a[120:123], v[36:39], v[44:47]\nds_read_b128 v[24:27], v16 offset:10240\n" \
"v_mfma_f32_16x16x32_bf16 v[32:35], a[20:23], v[68:71], v[32:35]\nbuffer_load_dwordx4 v16, s[8:11], s43 offen offset:1024 sc1 lds\nv_mfma_f32_16x16x32_bf16 v[52:55], a[20:23], v[72:75], v[52:55]\nds_read_b128 v[36:39], v16 offset:43008\n" \
"v_mfma_f32_16x16x32_bf16 v[56:59], a[124:127], v[68:71], v[56:59]\ns_waitcnt lgkmcnt(3)\nds_read_b128 v[68:71], v17 offset:4096\nv_mfma_f32_16x16x32_bf16 v[44:47], a[124:127], v[72:75], v[44:47]\n" \
"buffer_load_dwordx4 v16, s[8:11], s43 offen offset:2048 sc1 lds\nv_mfma_f32_16x16x32_bf16 v[32:35], v[28:31], v[20:23], v[32:35]\nds_read_b128 v[72:75], v16 offset:11264\nv_mfma_f32_16x16x32_bf16 v[52:55], v[28:31], v[48:51], v[52:55]\n" \
"ds_read_b128 v[76:79], v16 offset:44032\nv_mfma_f32_16x16x32_bf16 v[56:59], a[128:131], v[20:23], v[56:59]\nbuffer_load_dwordx4 v16, s[8:11], s43 offen offset:3072 sc1 lds\nv_mfma_f32_16x16x32_bf16 v[44:47], a[128:131], v[48:51], v[44:47]\n" \
"s_waitcnt lgkmcnt(3)\nds_read_b128 v[20:23], v17 offset:5120\nv_mfma_f32_16x16x32_bf16 v[32:35], v[60:63], v[40:43], v[32:35]\nds_read_b128 v[28:31], v16 offset:12288\n" \
"v_mfma_f32_16x16x32_bf16 v[52:55], v[60:63], v[64:67], v[52:55]\ns_add_u32 m0, s22, 0x6000\nds_read_b128 v[48:51], v16 offset:45056\nv_mfma_f32_16x16x32_bf16 v[56:59], a[132:135], v[40:43], v[56:59]\n" \
"s_add_u32 s42, s21, -0xa000\nv_mfma_f32_16x16x32_bf16 v[44:47], a[132:135], v[64:67], v[44:47]\nds_read_b128 v[40:43], v16 offset:13312\nv_mfma_f32_16x16x32_bf16 v[32:35], a[24:27], v[24:27], v[32:35]\n" \
"s_waitcnt lgkmcnt(4)\nds_read_b128 v[60:63], v16 offset:46080\nv_mfma_f32_16x16x32_bf16 v[52:55], a[24:27], v[36:39], v[52:55]\nbuffer_load_dwordx4 v16, s[12:15], s42 offen offset:0 sc1 lds\n" \
"v_mfma_f32_16x16x32_bf16 v[56:59], v[68:71], v[24:27], v[56:59]\nv_mfma_f32_16x16x32_bf16 v[44:47], v[68:71], v[36:39], v[44:47]\nds_read_b128 v[24:27], v16 offset:14336\nv_lshlrev_b32 v111, 5, v16\n" \
"v_mfma_f32_16x16x32_bf16 v[32:35], a[28:31], v[72:75], v[32:35]\nds_read_b128 v[36:39], v16 offset:47104\nv_and_b32 v110, 0x6000, v111\nv_mfma_f32_16x16x32_bf16 v[52:55], a[28:31], v[76:79], v[52:55]\n" \
"s_waitcnt lgkmcnt(3)\nds_read_b128 v[64:67], v17 offset:6144\nv_mfma_f32_16x16x32_bf16 v[56:59], v[20:23], v[72:75], v[56:59]\nbuffer_load_dwordx4 v16, s[12:15], s42 offen offset:1024 sc1 lds\n" \
"v_mfma_f32_16x16x32_bf16 v[44:47], v[20:23], v[76:79], v[44:47]\nds_read_b128 v[68:71], v16 offset:15360\nv_add_u32 v111, v111, v110\nv_mfma_f32_16x16x32_bf16 v[32:35], a[32:35], v[28:31], v[32:35]\n" \
"ds_read_b128 v[20:23], v16 offset:48128\nv_and_b32 v110, 0x4000, v110\nv_mfma_f32_16x16x32_bf16 v[52:55], a[32:35], v[48:51], v[52:55]\nds_read_b128 v[72:75], v17 offset:7168\n" \
"v_lshl_add_u32 v111, v110, 16, v111\nv_mfma_f32_16x16x32_bf16 v[56:59], a[136:139], v[28:31], v[56:59]\nds_read_b128 v[76:79], v17 offset:2048\nv_mfma_f32_16x16x32_bf16 v[44:47], a[136:139], v[48:51], v[44:47]\n" \
"s_waitcnt lgkmcnt(5)\nbuffer_load_dwordx4 v16, s[12:15], s42 offen offset:2048 sc1 lds\nv_mfma_f32_16x16x32_bf16 v[32:35], a[36:39], v[40:43], v[32:35]\nds_read_b128 v[28:31], v16 offset:16384\n" \
"v_mfma_f32_16x16x32_bf16 v[52:55], a[36:39], v[60:63], v[52:55]\nds_read_b128 v[48:51], v16 offset:49152\nv_mfma_f32_16x16x32_bf16 v[56:59], a[140:143], v[40:43], v[56:59]\nds_read_b128 v[80:83], v17 offset:3072\n" \
"v_mfma_f32_16x16x32_bf16 v[44:47], a[140:143], v[60:63], v[44:47]\nds_read_b128 v[40:43], v16 offset:17408\nv_mfma_f32_16x16x32_bf16 v[32:35], a[40:43], v[24:27], v[32:35]\ns_waitcnt lgkmcnt(4)\n" \
"buffer_load_dwordx4 v16, s[12:15], s42 offen offset:3072 sc1 lds\nv_mfma_f32_16x16x32_bf16 v[52:55], a[40:43], v[36:39], v[52:55]\nds_read_b128 v[60:63], v16 offset:50176\nv_add_u32 v110, s22, v16\n" \
"v_mfma_f32_16x16x32_bf16 v[56:59], v[64:67], v[24:27], v[56:59]\ns_add_u32 m0, s22, 0\nv_mfma_f32_16x16x32_bf16 v[44:47], v[64:67], v[36:39], v[44:47]\nds_read_b128 v[24:27], v16 offset:18432\n" \
"v_mfma_f32_16x16x32_bf16 v[32:35], a[44:47], v[68:71], v[32:35]\nds_read_b128 v[36:39], v16 offset:51200\nv_mfma_f32_16x16x32_bf16 v[52:55], a[44:47], v[20:23], v[52:55]\nds_read_b128 v[64:67], v17 offset:8192\n" \
"v_mfma_f32_16x16x32_bf16 v[56:59], v[72:75], v[68:71], v[56:59]\ns_waitcnt lgkmcnt(4)\nds_read_b128 v[68:71], v17 offset:8192\nv_mfma_f32_16x16x32_bf16 v[44:47], v[72:75], v[20:23], v[44:47]\n" \
"ds_read_b128 v[84:87], v16 offset:19456\nv_mfma_f32_16x16x32_bf16 v[32:35], v[76:79], v[28:31], v[32:35]\ns_waitcnt vmcnt(4)\nds_read_b128 v[20:23], v110 offset:3072\n" \
"ds_read_b128 v[88:91], v110 offset:0\nds_read_b128 v[92:95], v110 offset:1024\nv_mfma_f32_16x16x32_bf16 v[52:55], v[76:79], v[48:51], v[52:55]\nds_read_b128 v[96:99], v110 offset:2048\n" \
"v_mfma_f32_16x16x32_bf16 v[56:59], a[144:147], v[28:31], v[56:59]\nds_read_b128 v[76:79], v16 offset:52224\nv_mfma_f32_16x16x32_bf16 v[44:47], a[144:147], v[48:51], v[44:47]\ns_waitcnt lgkmcnt(7)\n" \
"ds_read_b128 v[72:75], v17 offset:9216\nv_mfma_f32_16x16x32_bf16 v[32:35], v[80:83], v[40:43], v[32:35]\ns_waitcnt lgkmcnt(3)\nv_max3_u32 v109, v88, v90, v92\n" \
"v_mfma_f32_16x16x32_bf16 v[52:55], v[80:83], v[60:63], v[52:55]\ns_waitcnt lgkmcnt(2)\nv_max3_u32 v109, v109, v94, v96\nv_max3_u32 v109, v109, v98, v20\n" \
"v_mfma_f32_16x16x32_bf16 v[56:59], a[148:151], v[40:43], v[56:59]\nv_max_u32 v109, v109, v22\nv_cmp_eq_u32 vcc, -1, v109\nv_mfma_f32_16x16x32_bf16 v[44:47], a[148:151], v[60:63], v[44:47]\n" \
"ds_read_b128 v[100:103], v17 offset:4096\nv_mov_b64 v[28:29], 0\nv_mfma_f32_16x16x32_bf16 v[32:35], a[48:51], v[24:27], v[32:35]\nds_read_b128 v[104:107], v17 offset:5120\n" \
"v_mov_b64 v[30:31], 0\nv_mfma_f32_16x16x32_bf16 v[52:55], a[48:51], v[36:39], v[52:55]\ns_cbranch_vccnz 2f\n3:\n" \
"v_mfma_f32_16x16x32_bf16 v[56:59], v[64:67], v[24:27], v[56:59]\ns_barrier\nds_read_b128 v[20:23], v16 offset:2048\nv_mfma_f32_16x16x32_bf16 v[44:47], v[68:71], v[36:39], v[44:47]\n" \
"s_memrealtime s[40:41]\nds_read_b128 v[24:27], v16 offset:5120\nv_mfma_f32_16x16x32_bf16 v[32:35], a[52:55], v[84:87], v[32:35]\ns_waitcnt lgkmcnt(4)\n" \
"ds_read_b128 v[36:39], v16 offset:1024\nv_mfma_f32_16x16x32_bf16 v[52:55], a[52:55], v[76:79], v[52:55]\nds_read_b128 v[40:43], v16 offset:33792\nv_mov_b64 v[48:49], 0\n" \
"v_mfma_f32_16x16x32_bf16 v[56:59], v[72:75], v[84:87], v[56:59]\ns_lshl_b32 s43, s20, 24\nv_mov_b64 v[50:51], 0\nv_mfma_f32_16x16x32_bf16 v[44:47], v[72:75], v[76:79], v[44:47]\n" \
"s_waitcnt lgkmcnt(3)\nds_read_b128 v[60:63], v16 offset:3072\nv_smfmac_f32_16x16x64_bf16 v[28:31], v[20:23], a[8:15], v18\ns_waitcnt lgkmcnt(3)\n" \
"v_smfmac_f32_16x16x64_bf16 v[48:51], v[20:23], a[112:119], v18\nds_read_b128 v[64:67], v16 offset:34816\nv_smfmac_f32_16x16x64_bf16 v[28:31], v[24:27], a[24:31], v18\ns_waitcnt lgkmcnt(3)\n" \
"ds_read_b128 v[20:23], v17 offset:6144\nv_smfmac_f32_16x16x64_bf16 v[48:51], v[24:27], v[100:107], v18\nds_read_b128 v[76:79], v16 offset:36864\nv_smfmac_f32_16x16x64_bf16 v[28:31], v[36:39], a[0:7], v18\n" \
"s_waitcnt lgkmcnt(4)\nds_read_b128 v[24:27], v17 offset:7168\nv_smfmac_f32_16x16x64_bf16 v[48:51], v[36:39], a[104:111], v18\nds_read_b128 v[80:83], v16 offset:6144\n" \
"s_add_u32 s42, s40, 0x64\nv_smfmac_f32_16x16x64_bf16 v[28:31], v[40:43], a[48:55], v18\ns_and_b32 s42, s42, 0xffffff\ns_or_b32 s42, s42, s43\n" \
"v_mov_b32 v110, s42\nv_smfmac_f32_16x16x64_bf16 v[48:51], v[40:43], v[68:75], v18\ns_waitcnt lgkmcnt(4)\nds_read_b128 v[36:39], v16 offset:7168\n" \
"v_smfmac_f32_16x16x64_bf16 v[28:31], v[60:63], a[16:23], v18\nbuffer_store_dword v110, v111, s[8:11], s27 offen offset:0 sc1\nv_smfmac_f32_16x16x64_bf16 v[48:51], v[60:63], a[120:127], v18\nds_read_b128 v[40:43], v16 offset:35840\n" \
"v_smfmac_f32_16x16x64_bf16 v[28:31], v[64:67], a[56:63], v18\ns_waitcnt lgkmcnt(4)\nv_smfmac_f32_16x16x64_bf16 v[48:51], v[64:67], a[152:159], v18\nds_read_b128 v[60:63], v16 offset:0\n" \
"s_add_u32 s16, s20, 1\nv_smfmac_f32_16x16x64_bf16 v[28:31], v[76:79], a[72:79], v18\nds_read_b128 v[64:67], v17 offset:0\nv_mov_b64 v[72:73], 0\n" \
"v_smfmac_f32_16x16x64_bf16 v[48:51], v[76:79], a[168:175], v18\ns_waitcnt lgkmcnt(4)\nds_read_b128 v[76:79], v16 offset:39936\nv_smfmac_f32_16x16x64_bf16 v[28:31], v[80:83], a[32:39], v18\n" \
"ds_read_b128 v[68:71], v17 offset:1024\nv_mov_b64 v[74:75], 0\nv_smfmac_f32_16x16x64_bf16 v[48:51], v[80:83], a[136:143], v18\ns_waitcnt lgkmcnt(4)\n" \
"ds_read_b128 v[80:83], v16 offset:37888\nv_smfmac_f32_16x16x64_bf16 v[28:31], v[36:39], a[40:47], v18\nds_read_b128 v[84:87], v17 offset:2048\ns_and_b32 s16, s16, 7\n" \
"v_smfmac_f32_16x16x64_bf16 v[48:51], v[36:39], v[20:27], v18\nds_read_b128 v[92:95], v16 offset:4096\ns_lshl_b32 s16, s16, 22\nv_smfmac_f32_16x16x64_bf16 v[28:31], v[40:43], a[64:71], v18\n" \
"ds_read_b128 v[88:91], v17 offset:3072\ns_add_u32 s16, s16, s25\nv_smfmac_f32_16x16x64_bf16 v[48:51], v[40:43], a[160:167], v18\ns_waitcnt lgkmcnt(6)\n" \
"ds_read_b128 v[20:23], v16 offset:38912\nv_smfmac_f32_16x16x64_bf16 v[28:31], v[60:63], v[0:7], v18\nds_read_b128 v[24:27], v16 offset:20480\ns_add_u32 s17, s16, 0x1000\n" \
"v_smfmac_f32_16x16x64_bf16 v[48:51], v[60:63], v[8:15], v18\ns_waitcnt lgkmcnt(6)\nds_read_b128 v[36:39], v16 offset:32768\nv_smfmac_f32_16x16x64_bf16 v[28:31], v[76:79], a[96:103], v18\n" \
"ds_read_b128 v[40:43], v16 offset:53248\ns_add_u32 s18, s16, 0x4000\nv_smfmac_f32_16x16x64_bf16 v[48:51], v[76:79], a[192:199], v18\ns_waitcnt lgkmcnt(5)\n" \
"ds_read_b128 v[60:63], v16 offset:21504\nv_smfmac_f32_16x16x64_bf16 v[28:31], v[80:83], a[80:87], v18\nds_read_b128 v[76:79], v16 offset:54272\nv_lshrrev_b32 v111, 16, v18\n" \
"v_smfmac_f32_16x16x64_bf16 v[48:51], v[80:83], a[176:183], v18\nds_write_b128 v16, v[72:75] offset:45056\nv_smfmac_f32_16x16x64_bf16 v[28:31], v[92:95], v[64:71], v18\ns_add_u32 s19, s16, 0x5000\n" \
"s_and_b32 s43, s20, 2\ns_lshl_b32 s43, s43, 12\nv_smfmac_f32_16x16x64_bf16 v[48:51], v[92:95], a[128:135], v18\ns_waitcnt lgkmcnt(4)\n" \
"ds_read_b128 v[64:67], v16 offset:22528\nv_smfmac_f32_16x16x64_bf16 v[28:31], v[20:23], a[88:95], v18\nds_read_b128 v[68:71], v16 offset:55296\ns_and_b32 s42, s20, 7\n" \
"v_smfmac_f32_16x16x64_bf16 v[48:51], v[20:23], a[184:191], v18\nds_read_b128 v[92:95], v17 offset:10240\ns_lshl_b32 s42, s42, 20\nv_smfmac_f32_16x16x64_bf16 v[28:31], v[36:39], v[84:91], v18\n" \
"ds_read_b128 v[96:99], v17 offset:11264\ns_add_u32 s42, s42, s24\nv_smfmac_f32_16x16x64_bf16 v[48:51], v[36:39], a[144:151], v18\ns_waitcnt lgkmcnt(4)\n" \
"v_mfma_f32_16x16x32_bf16 v[32:35], a[56:59], v[24:27], v[32:35]\nv_add_f32 v28, v28, v30\nv_add_f32 v29, v29, v31\nv_cvt_pk_bf16_f32 v28, v28, v29\n" \
"v_mfma_f32_16x16x32_bf16 v[52:55], a[56:59], v[40:43], v[52:55]\nv_add_f32 v48, v48, v50\nv_add_f32 v49, v49, v51\nv_cvt_pk_bf16_f32 v48, v48, v49\n" \
"v_mfma_f32_16x16x32_bf16 v[56:59], a[152:155], v[24:27], v[56:59]\nds_write2_b32 v111, v28, v48 offset0:0 offset1:4\nv_mfma_f32_16x16x32_bf16 v[44:47], a[152:155], v[40:43], v[44:47]\nds_read_b128 v[20:23], v16 offset:23552\n" \
"v_mov_b64 v[24:25], 0\nv_mfma_f32_16x16x32_bf16 v[32:35], a[60:63], v[60:63], v[32:35]\ns_add_u32 s99, s20, 2\nv_mov_b64 v[26:27], 0\n" \
"v_bfe_u32 v111, v16, 4, 3\nv_mfma_f32_16x16x32_bf16 v[52:55], a[60:63], v[76:79], v[52:55]\ns_waitcnt vmcnt(1) lgkmcnt(1)\ns_barrier\n" \
"ds_read_b64_tr_b16 v[28:29], v19 offset:0\nds_read_b64_tr_b16 v[30:31], v19 offset:256\nv_mfma_f32_16x16x32_bf16 v[56:59], a[156:159], v[60:63], v[56:59]\nds_read_b64_tr_b16 v[36:37], v19 offset:1024\n" \
"ds_read_b64_tr_b16 v[38:39], v19 offset:1280\nv_mfma_f32_16x16x32_bf16 v[44:47], a[156:159], v[76:79], v[44:47]\nds_read_b128 v[40:43], v16 offset:56320\nv_mov_b64 v[48:49], 0\n" \
"v_mfma_f32_16x16x32_bf16 v[32:35], a[64:67], v[64:67], v[32:35]\ns_min_u32 s99, s99, 63\nv_mov_b64 v[50:51], 0\nv_lshlrev_b32 v111, 2, v111\n" \
"v_mfma_f32_16x16x32_bf16 v[52:55], a[64:67], v[68:71], v[52:55]\ns_lshl_b32 s99, s99, 5\ns_add_u32 s99, s99, s26\nv_mov_b64 v[60:61], 0\n" \
"v_mfma_f32_16x16x32_bf16 v[56:59], a[160:163], v[64:67], v[56:59]\ns_waitcnt lgkmcnt(1)\nbuffer_load_dword v110, v111, s[8:11], s99 offen sc1\nv_smfmac_f32_16x16x64_bf16 v[72:75], v[28:31], a[200:207], v18\n" \
"ds_read_b128 v[64:67], v16 offset:45056\nv_mov_b64 v[62:63], 0\nv_smfmac_f32_16x16x64_bf16 v[72:75], v[36:39], v[92:99], v18\nds_read_b128 v[76:79], v17 offset:12288\n" \
"v_smfmac_f32_16x16x64_bf16 v[24:27], v[28:31], a[224:231], v18\nds_read_b128 v[80:83], v17 offset:13312\nv_mfma_f32_16x16x32_bf16 v[44:47], a[160:163], v[68:71], v[44:47]\nds_read_b128 v[84:87], v17 offset:14336\n" \
"v_add_f32 v72, v72, v74\nv_smfmac_f32_16x16x64_bf16 v[24:27], v[36:39], a[232:239], v18\nds_read_b128 v[68:71], v16 offset:45056\nv_add_f32 v73, v73, v75\n" \
"v_mfma_f32_16x16x32_bf16 v[32:35], a[68:71], v[20:23], v[32:35]\nds_read_b128 v[88:91], v17 offset:15360\nv_cvt_pk_bf16_f32 v92, v72, v73\nv_smfmac_f32_16x16x64_bf16 v[48:51], v[28:31], a[208:215], v18\n" \
"ds_read_b128 v[72:75], v16 offset:45056\nv_add_f32 v24, v24, v26\nv_smfmac_f32_16x16x64_bf16 v[48:51], v[36:39], a[216:223], v18\nds_read_b128 v[96:99], v16 offset:24576\n" \
"v_add_f32 v25, v25, v27\nv_smfmac_f32_16x16x64_bf16 v[60:63], v[28:31], a[240:247], v18\ns_waitcnt lgkmcnt(5)\nv_cvt_pk_bf16_f32 v93, v24, v25\n" \
"v_mfma_f32_16x16x32_bf16 v[52:55], a[68:71], v[40:43], v[52:55]\nv_permlane32_swap_b32 v92, v93\nv_smfmac_f32_16x16x64_bf16 v[60:63], v[36:39], a[248:255], v18\nds_read_b128 v[24:27], v16 offset:45056\n" \
"v_add_f32 v48, v48, v50\nv_smfmac_f32_16x16x64_bf16 v[64:67], v[28:31], v[112:119], v18\nds_read_b128 v[100:103], v17 offset:16384\nv_add_f32 v49, v49, v51\n" \
"v_mfma_f32_16x16x32_bf16 v[56:59], a[164:167], v[20:23], v[56:59]\ns_waitcnt lgkmcnt(3)\nv_add_f32 v60, v60, v62\nv_add_f32 v61, v61, v63\n" \
"v_smfmac_f32_16x16x64_bf16 v[64:67], v[36:39], v[120:127], v18\nds_read_b128 v[20:23], v16 offset:45056\nv_cvt_pk_bf16_f32 v94, v48, v49\nv_smfmac_f32_16x16x64_bf16 v[68:71], v[28:31], v[128:135], v18\n" \
"ds_read_b128 v[48:51], v16 offset:25600\nv_cvt_pk_bf16_f32 v95, v60, v61\nv_smfmac_f32_16x16x64_bf16 v[68:71], v[36:39], v[136:143], v18\nds_read_b128 v[60:63], v16 offset:45056\n" \
"v_permlane32_swap_b32 v94, v95\nv_mfma_f32_16x16x32_bf16 v[44:47], a[164:167], v[40:43], v[44:47]\nv_permlane16_swap_b32 v92, v94\nv_smfmac_f32_16x16x64_bf16 v[72:75], v[28:31], v[76:83], v18\n" \
"ds_read_b128 v[104:107], v17 offset:17408\nv_permlane16_swap_b32 v93, v95\nv_min_u32 v92, -2, v92\nv_smfmac_f32_16x16x64_bf16 v[72:75], v[36:39], v[84:91], v18\n" \
"s_waitcnt lgkmcnt(4)\nds_read_b128 v[40:43], v16 offset:45056\nv_mfma_f32_16x16x32_bf16 v[32:35], a[72:75], v[96:99], v[32:35]\nbuffer_store_dwordx4 v[92:95], v16, s[8:11], s29 offen offset:0 sc1\n" \
"ds_read_b128 v[76:79], v16 offset:26624\nv_add_f32 v64, v64, v66\nv_smfmac_f32_16x16x64_bf16 v[24:27], v[28:31], v[144:151], v18\nds_read_b128 v[80:83], v17 offset:18432\n" \
"v_add_f32 v65, v65, v67\nv_smfmac_f32_16x16x64_bf16 v[24:27], v[36:39], v[152:159], v18\nds_read_b128 v[88:91], v16 offset:45056\nv_add_f32 v68, v68, v70\n" \
"v_mfma_f32_16x16x32_bf16 v[56:59], a[168:171], v[96:99], v[56:59]\ns_waitcnt lgkmcnt(4)\nv_add_f32 v69, v69, v71\nv_add_f32 v72, v72, v74\n" \
"v_smfmac_f32_16x16x64_bf16 v[20:23], v[28:31], v[160:167], v18\nds_read_b128 v[84:87], v17 offset:19456\nv_add_f32 v73, v73, v75\nv_smfmac_f32_16x16x64_bf16 v[20:23], v[36:39], v[168:175], v18\n" \
"ds_read_b128 v[92:95], v16 offset:45056\nv_add_f32 v24, v24, v26\nv_mfma_f32_16x16x32_bf16 v[32:35], a[76:79], v[48:51], v[32:35]\nv_add_f32 v25, v25, v27\n" \
"v_cvt_pk_bf16_f32 v96, v64, v65\nv_cvt_pk_bf16_f32 v97, v68, v69\nv_cvt_pk_bf16_f32 v98, v72, v73\nv_cvt_pk_bf16_f32 v99, v24, v25\n" \
"v_smfmac_f32_16x16x64_bf16 v[60:63], v[28:31], v[184:191], v18\nds_read_b128 v[24:27], v16 offset:27648\nv_permlane32_swap_b32 v96, v97\nv_permlane32_swap_b32 v98, v99\n" \
"v_smfmac_f32_16x16x64_bf16 v[60:63], v[36:39], v[192:199], v18\nds_read_b128 v[64:67], v16 offset:45056\nv_permlane16_swap_b32 v96, v98\nv_mfma_f32_16x16x32_bf16 v[56:59], a[172:175], v[48:51], v[56:59]\n" \
"s_waitcnt lgkmcnt(3)\nv_permlane16_swap_b32 v97, v99\nv_min_u32 v96, -2, v96\nv_smfmac_f32_16x16x64_bf16 v[40:43], v[28:31], v[100:107], v18\n" \
"ds_read_b128 v[68:71], v17 offset:20480\nv_add_f32 v20, v20, v22\nv_smfmac_f32_16x16x64_bf16 v[40:43], v[36:39], v[176:183], v18\nds_read_b128 v[48:51], v16 offset:45056\n" \
"v_add_f32 v21, v21, v23\nv_mfma_f32_16x16x32_bf16 v[32:35], a[80:83], v[76:79], v[32:35]\nbuffer_store_dwordx4 v[96:99], v16, s[8:11], s29 offen offset:1024 sc1\nds_read_b128 v[72:75], v17 offset:21504\n" \
"v_add_f32 v60, v60, v62\nv_smfmac_f32_16x16x64_bf16 v[88:91], v[28:31], v[200:207], v18\nds_read_b128 v[96:99], v16 offset:28672\nv_add_f32 v61, v61, v63\n" \
"v_smfmac_f32_16x16x64_bf16 v[88:91], v[36:39], v[80:87], v18\ns_waitcnt lgkmcnt(4)\nds_read_b128 v[80:83], v16 offset:45056\nv_smfmac_f32_16x16x64_bf16 v[92:95], v[28:31], v[208:215], v18\n" \
"ds_read_b128 v[100:103], v17 offset:22528\nv_add_f32 v40, v40, v42\nv_mfma_f32_16x16x32_bf16 v[56:59], a[176:179], v[76:79], v[56:59]\nv_add_f32 v41, v41, v43\n" \
"v_add_f32 v88, v88, v90\nv_smfmac_f32_16x16x64_bf16 v[92:95], v[36:39], v[216:223], v18\nds_read_b128 v[104:107], v17 offset:23552\nv_add_f32 v89, v89, v91\n" \
"v_mfma_f32_16x16x32_bf16 v[32:35], a[84:87], v[24:27], v[32:35]\nv_cvt_pk_bf16_f32 v76, v20, v21\nv_cvt_pk_bf16_f32 v77, v60, v61\nv_cvt_pk_bf16_f32 v78, v40, v41\n" \
"v_cvt_pk_bf16_f32 v79, v88, v89\nv_smfmac_f32_16x16x64_bf16 v[64:67], v[28:31], v[232:239], v18\nds_read_b128 v[20:23], v16 offset:29696\nv_permlane32_swap_b32 v76, v77\n" \
"v_permlane32_swap_b32 v78, v79\nv_smfmac_f32_16x16x64_bf16 v[64:67], v[36:39], v[240:247], v18\nds_read_b128 v[40:43], v16 offset:30720\nv_permlane16_swap_b32 v76, v78\n" \
"v_mfma_f32_16x16x32_bf16 v[56:59], a[180:183], v[24:27], v[56:59]\ns_waitcnt lgkmcnt(3)\nv_permlane16_swap_b32 v77, v79\nv_min_u32 v76, -2, v76\n" \
"v_smfmac_f32_16x16x64_bf16 v[48:51], v[28:31], v[68:75], v18\nds_read_b128 v[24:27], v16 offset:31744\nv_add_f32 v92, v92, v94\nv_smfmac_f32_16x16x64_bf16 v[48:51], v[36:39], v[224:231], v18\n" \
"ds_read_b128 v[60:63], v16 offset:57344\nv_add_f32 v93, v93, v95\nv_mfma_f32_16x16x32_bf16 v[32:35], a[88:91], v[96:99], v[32:35]\ns_waitcnt lgkmcnt(3)\n" \
"buffer_store_dwordx4 v[76:79], v16, s[8:11], s29 offen offset:2048 sc1\nv_add_f32 v64, v64, v66\nv_add_f32 v65, v65, v67\nv_cvt_pk_bf16_f32 v68, v92, v93\n" \
"v_smfmac_f32_16x16x64_bf16 v[80:83], v[28:31], v[100:107], v18\nds_read_b128 v[72:75], v16 offset:58368\nv_add_f32 v48, v48, v50\nv_smfmac_f32_16x16x64_bf16 v[80:83], v[36:39], v[248:255], v18\n" \
"ds_read_b128 v[28:31], v16 offset:59392\nv_add_f32 v49, v49, v51\nv_mfma_f32_16x16x32_bf16 v[56:59], a[184:187], v[96:99], v[56:59]\nv_cvt_pk_bf16_f32 v69, v64, v65\n" \
"v_cvt_pk_bf16_f32 v70, v48, v49\nv_mfma_f32_16x16x32_bf16 v[32:35], a[92:95], v[20:23], v[32:35]\ns_waitcnt lgkmcnt(3)\nv_permlane32_swap_b32 v68, v69\n" \
"v_add_f32 v80, v80, v82\nv_add_f32 v81, v81, v83\nv_cvt_pk_bf16_f32 v71, v80, v81\nv_mfma_f32_16x16x32_bf16 v[56:59], a[188:191], v[20:23], v[56:59]\n" \
"ds_read_b128 v[36:39], v16 offset:60416\nv_permlane32_swap_b32 v70, v71\nv_mfma_f32_16x16x32_bf16 v[32:35], a[96:99], v[40:43], v[32:35]\nv_permlane16_swap_b32 v68, v70\n" \
"v_permlane16_swap_b32 v69, v71\nv_min_u32 v68, -2, v68\nv_mfma_f32_16x16x32_bf16 v[56:59], a[192:195], v[40:43], v[56:59]\nbuffer_store_dwordx4 v[68:71], v16, s[8:11], s29 offen offset:3072 sc1\n" \
"ds_read_b128 v[20:23], v16 offset:61440\nv_mfma_f32_16x16x32_bf16 v[32:35], a[100:103], v[24:27], v[32:35]\ns_xor_b32 s29, s42, s43\nds_read_b128 v[40:43], v16 offset:62464\n" \
"v_mfma_f32_16x16x32_bf16 v[56:59], a[196:199], v[24:27], v[56:59]\ns_waitcnt lgkmcnt(3)\nds_read_b128 v[48:51], v16 offset:63488\nv_mfma_f32_16x16x32_bf16 v[52:55], a[72:75], v[60:63], v[52:55]\n" \
"s_waitcnt vmcnt(4)\nv_cmp_eq_u32 s[42:43], -1, v110\nv_cvt_pk_bf16_f32 v32, v32, v33\nv_cvt_pk_bf16_f32 v33, v34, v35\n" \
"v_mfma_f32_16x16x32_bf16 v[44:47], a[168:171], v[60:63], v[44:47]\nv_cvt_pk_bf16_f32 v34, v56, v57\nv_cvt_pk_bf16_f32 v35, v58, v59\nv_mfma_f32_16x16x32_bf16 v[52:55], a[76:79], v[72:75], v[52:55]\n" \
"s_cmp_eq_u64 s[42:43], 0\ns_cselect_b32 s14, s10, 0\ns_cmp_lt_i32 s20, 62\ns_cselect_b32 s14, s14, 0\n" \
"v_mfma_f32_16x16x32_bf16 v[44:47], a[172:175], v[72:75], v[44:47]\ns_cmp_eq_u32 s14, 0\ns_cbranch_scc1 4f\n5:\n" \
"v_mfma_f32_16x16x32_bf16 v[52:55], a[80:83], v[28:31], v[52:55]\nds_read_b128 v[56:59], v16 offset:64512\nv_add_u32 v111, s28, v16\nv_mfma_f32_16x16x32_bf16 v[44:47], a[176:179], v[28:31], v[44:47]\n" \
"s_waitcnt lgkmcnt(1)\nds_write_b128 v111, v[32:35] offset:0\nv_mfma_f32_16x16x32_bf16 v[52:55], a[84:87], v[36:39], v[52:55]\ns_add_u32 m0, s22, 0\n" \
"ds_read_b128 v[60:63], v17 offset:10240\nv_mfma_f32_16x16x32_bf16 v[44:47], a[180:183], v[36:39], v[44:47]\nbuffer_load_dwordx4 v16, s[12:15], s21 offen offset:0 sc1 lds\nv_mfma_f32_16x16x32_bf16 v[52:55], a[88:91], v[20:23], v[52:55]\n" \
"ds_read_b128 v[64:67], v17 offset:11264\ns_add_u32 s20, s20, 1\nv_mfma_f32_16x16x32_bf16 v[44:47], a[184:187], v[20:23], v[44:47]\ns_waitcnt lgkmcnt(2)\n" \
"s_barrier\nv_mfma_f32_16x16x32_bf16 v[52:55], a[92:95], v[40:43], v[52:55]\nds_read_b128 v[20:23], v16 offset:8192\nv_mfma_f32_16x16x32_bf16 v[44:47], a[188:191], v[40:43], v[44:47]\n" \
"ds_read_b128 v[24:27], v16 offset:9216\nv_mfma_f32_16x16x32_bf16 v[52:55], a[96:99], v[48:51], v[52:55]\nds_read_b128 v[28:31], v16 offset:10240\nv_add_u32 v111, s28, v16\n" \
"v_mfma_f32_16x16x32_bf16 v[44:47], a[192:195], v[48:51], v[44:47]\nbuffer_load_dwordx4 v16, s[12:15], s21 offen offset:1024 sc1 lds\nds_read_b128 v[32:35], v16 offset:11264\nv_mfma_f32_16x16x32_bf16 v[52:55], a[100:103], v[56:59], v[52:55]\n" \
"s_waitcnt lgkmcnt(3)\nv_mfma_f32_16x16x32_bf16 v[44:47], a[196:199], v[56:59], v[44:47]\ns_waitcnt lgkmcnt(2)\nv_mfma_f32_16x16x32_bf16 v[36:39], v[20:23], a[200:203], 0\n" \
"s_waitcnt lgkmcnt(1)\nv_cvt_pk_bf16_f32 v52, v52, v53\nv_cvt_pk_bf16_f32 v53, v54, v55\nv_mfma_f32_16x16x32_bf16 v[36:39], v[24:27], a[204:207], v[36:39]\n" \
"v_cvt_pk_bf16_f32 v54, v44, v45\nv_cvt_pk_bf16_f32 v55, v46, v47\nv_mfma_f32_16x16x32_bf16 v[36:39], v[28:31], v[60:63], v[36:39]\ns_waitcnt lgkmcnt(0)\n" \
"buffer_load_dwordx4 v16, s[12:15], s21 offen offset:2048 sc1 lds\nv_mfma_f32_16x16x32_bf16 v[36:39], v[32:35], v[64:67], v[36:39]\nds_write_b128 v111, v[52:55] offset:32768\nv_mfma_f32_16x16x32_bf16 v[40:43], v[20:23], a[208:211], 0\n" \
"v_mfma_f32_16x16x32_bf16 v[40:43], v[24:27], a[212:215], v[40:43]\nv_cvt_pk_bf16_f32 v36, v36, v37\nv_cvt_pk_bf16_f32 v37, v38, v39\nv_mfma_f32_16x16x32_bf16 v[40:43], v[28:31], a[216:219], v[40:43]\n" \
"ds_read_b128 v[44:47], v17 offset:18432\nv_mfma_f32_16x16x32_bf16 v[40:43], v[32:35], a[220:223], v[40:43]\ns_waitcnt lgkmcnt(1)\ns_barrier\n" \
"v_mfma_f32_16x16x32_bf16 v[48:51], v[20:23], a[224:227], 0\nbuffer_load_dwordx4 v16, s[12:15], s21 offen offset:3072 sc1 lds\nv_mfma_f32_16x16x32_bf16 v[48:51], v[24:27], a[228:231], v[48:51]\ns_add_u32 m0, s22, 0x4000\n" \
"s_add_u32 s43, s21, 0x4000\nv_cvt_pk_bf16_f32 v40, v40, v41\nv_mfma_f32_16x16x32_bf16 v[48:51], v[28:31], a[232:235], v[48:51]\nds_read_b128 v[52:55], v17 offset:19456\n" \
"v_cvt_pk_bf16_f32 v41, v42, v43\nv_mfma_f32_16x16x32_bf16 v[48:51], v[32:35], a[236:239], v[48:51]\nbuffer_load_dwordx4 v16, s[12:15], s43 offen offset:0 sc1 lds\nv_mfma_f32_16x16x32_bf16 v[56:59], v[20:23], v[144:147], 0\n" \
"ds_read_b128 v[60:63], v16 offset:40960\nv_mfma_f32_16x16x32_bf16 v[56:59], v[24:27], v[148:151], v[56:59]\nds_read_b128 v[64:67], v16 offset:41984\nv_cvt_pk_bf16_f32 v48, v48, v49\n" \
"v_mfma_f32_16x16x32_bf16 v[56:59], v[28:31], v[152:155], v[56:59]\nds_read_b128 v[68:71], v16 offset:43008\nv_cvt_pk_bf16_f32 v49, v50, v51\nv_mfma_f32_16x16x32_bf16 v[56:59], v[32:35], v[156:159], v[56:59]\n" \
"ds_read_b128 v[72:75], v17 offset:10240\nv_mfma_f32_16x16x32_bf16 v[76:79], v[20:23], v[200:203], 0\ns_waitcnt lgkmcnt(4)\nbuffer_load_dwordx4 v16, s[12:15], s43 offen offset:1024 sc1 lds\n" \
"v_mfma_f32_16x16x32_bf16 v[76:79], v[24:27], v[204:207], v[76:79]\nds_read_b128 v[80:83], v16 offset:44032\nds_read_b128 v[84:87], v17 offset:11264\nv_mfma_f32_16x16x32_bf16 v[76:79], v[28:31], v[44:47], v[76:79]\n" \
"s_waitcnt lgkmcnt(5)\nv_cvt_pk_bf16_f32 v56, v56, v57\nv_cvt_pk_bf16_f32 v57, v58, v59\nv_mfma_f32_16x16x32_bf16 v[76:79], v[32:35], v[52:55], v[76:79]\n" \
"s_waitcnt lgkmcnt(4)\nv_mfma_f32_16x16x32_bf16 v[44:47], v[60:63], a[200:203], 0\ns_waitcnt lgkmcnt(2)\nv_mfma_f32_16x16x32_bf16 v[44:47], v[64:67], a[204:207], v[44:47]\n" \
"buffer_load_dwordx4 v16, s[12:15], s43 offen offset:2048 sc1 lds\nv_mfma_f32_16x16x32_bf16 v[44:47], v[68:71], v[72:75], v[44:47]\ns_waitcnt lgkmcnt(0)\nv_cvt_pk_bf16_f32 v76, v76, v77\n" \
"v_cvt_pk_bf16_f32 v77, v78, v79\nv_mfma_f32_16x16x32_bf16 v[44:47], v[80:83], v[84:87], v[44:47]\nv_mfma_f32_16x16x32_bf16 v[52:55], v[60:63], a[208:211], 0\nv_mfma_f32_16x16x32_bf16 v[52:55], v[64:67], a[212:215], v[52:55]\n" \
"buffer_load_dwordx4 v16, s[12:15], s43 offen offset:3072 sc1 lds\nv_mfma_f32_16x16x32_bf16 v[52:55], v[68:71], a[216:219], v[52:55]\nv_cvt_pk_bf16_f32 v38, v44, v45\nv_cvt_pk_bf16_f32 v39, v46, v47\n" \
"v_mfma_f32_16x16x32_bf16 v[52:55], v[80:83], a[220:223], v[52:55]\nds_read_b128 v[44:47], v17 offset:18432\nv_mfma_f32_16x16x32_bf16 v[72:75], v[60:63], a[224:227], 0\nds_read_b128 v[84:87], v17 offset:19456\n" \
"v_mfma_f32_16x16x32_bf16 v[72:75], v[64:67], a[228:231], v[72:75]\nbuffer_store_dwordx4 v[36:39], v16, s[8:11], s16 offen offset:0 sc1\nv_cvt_pk_bf16_f32 v42, v52, v53\nv_cvt_pk_bf16_f32 v43, v54, v55\n" \
"v_mfma_f32_16x16x32_bf16 v[72:75], v[68:71], a[232:235], v[72:75]\nv_mfma_f32_16x16x32_bf16 v[72:75], v[80:83], a[236:239], v[72:75]\nv_mfma_f32_16x16x32_bf16 v[36:39], v[60:63], v[144:147], 0\nv_mfma_f32_16x16x32_bf16 v[36:39], v[64:67], v[148:151], v[36:39]\n" \
"v_cvt_pk_bf16_f32 v50, v72, v73\nv_cvt_pk_bf16_f32 v51, v74, v75\nv_mfma_f32_16x16x32_bf16 v[36:39], v[68:71], v[152:155], v[36:39]\nv_mfma_f32_16x16x32_bf16 v[36:39], v[80:83], v[156:159], v[36:39]\n" \
"v_mfma_f32_16x16x32_bf16 v[52:55], v[60:63], v[200:203], 0\ns_waitcnt lgkmcnt(0)\nbuffer_store_dwordx4 v[40:43], v16, s[8:11], s16 offen offset:1024 sc1\nv_mfma_f32_16x16x32_bf16 v[52:55], v[64:67], v[204:207], v[52:55]\n" \
"v_cvt_pk_bf16_f32 v58, v36, v37\nv_cvt_pk_bf16_f32 v59, v38, v39\nv_mfma_f32_16x16x32_bf16 v[52:55], v[68:71], v[44:47], v[52:55]\nv_mfma_f32_16x16x32_bf16 v[52:55], v[80:83], v[84:87], v[52:55]\n" \
"v_mfma_f32_16x16x32_bf16 v[36:39], v[20:23], a[240:243], 0\nv_mfma_f32_16x16x32_bf16 v[40:43], v[60:63], a[240:243], 0\nv_cvt_pk_bf16_f32 v78, v52, v53\nv_cvt_pk_bf16_f32 v79, v54, v55\n" \
"v_mfma_f32_16x16x32_bf16 v[36:39], v[24:27], a[244:247], v[36:39]\nv_mfma_f32_16x16x32_bf16 v[40:43], v[64:67], a[244:247], v[40:43]\nv_mfma_f32_16x16x32_bf16 v[36:39], v[28:31], a[248:251], v[36:39]\nbuffer_store_dwordx4 v[48:51], v16, s[8:11], s16 offen offset:2048 sc1\n" \
"ds_read_b128 v[44:47], v17 offset:12288\nv_mfma_f32_16x16x32_bf16 v[40:43], v[68:71], a[248:251], v[40:43]\nv_mfma_f32_16x16x32_bf16 v[36:39], v[32:35], a[252:255], v[36:39]\nds_read_b128 v[48:51], v17 offset:13312\n" \
"v_mfma_f32_16x16x32_bf16 v[40:43], v[80:83], a[252:255], v[40:43]\nv_mfma_f32_16x16x32_bf16 v[52:55], v[20:23], v[128:131], 0\nds_read_b128 v[72:75], v17 offset:14336\nv_cvt_pk_bf16_f32 v36, v36, v37\n" \
"v_mfma_f32_16x16x32_bf16 v[84:87], v[60:63], v[128:131], 0\nv_cvt_pk_bf16_f32 v37, v38, v39\nv_cvt_pk_bf16_f32 v38, v40, v41\nv_mfma_f32_16x16x32_bf16 v[52:55], v[24:27], v[132:135], v[52:55]\n" \
"ds_read_b128 v[88:91], v17 offset:15360\nv_cvt_pk_bf16_f32 v39, v42, v43\nv_mfma_f32_16x16x32_bf16 v[84:87], v[64:67], v[132:135], v[84:87]\nbuffer_store_dwordx4 v[56:59], v16, s[8:11], s17 offen offset:3072 sc1\n" \
"v_mfma_f32_16x16x32_bf16 v[52:55], v[28:31], v[136:139], v[52:55]\nv_mfma_f32_16x16x32_bf16 v[84:87], v[68:71], v[136:139], v[84:87]\nv_mfma_f32_16x16x32_bf16 v[52:55], v[32:35], v[140:143], v[52:55]\ns_waitcnt vmcnt(8) lgkmcnt(1)\n" \
"s_barrier\nv_mfma_f32_16x16x32_bf16 v[84:87], v[80:83], v[140:143], v[84:87]\nv_mfma_f32_16x16x32_bf16 v[40:43], v[20:23], v[44:47], 0\nv_cvt_pk_bf16_f32 v52, v52, v53\n" \
"v_cvt_pk_bf16_f32 v53, v54, v55\nv_mfma_f32_16x16x32_bf16 v[56:59], v[60:63], v[44:47], 0\nv_cvt_pk_bf16_f32 v54, v84, v85\nv_cvt_pk_bf16_f32 v55, v86, v87\n" \
"v_mfma_f32_16x16x32_bf16 v[40:43], v[24:27], v[48:51], v[40:43]\ns_add_u32 m0, s22, 0x2000\ns_add_u32 s43, s21, 0x2000\ns_add_u32 s21, s21, 0x10000\n" \
"v_mfma_f32_16x16x32_bf16 v[56:59], v[64:67], v[48:51], v[56:59]\nbuffer_store_dwordx4 v[76:79], v16, s[8:11], s18 offen offset:3072 sc1\nv_mfma_f32_16x16x32_bf16 v[40:43], v[28:31], v[72:75], v[40:43]\ns_waitcnt lgkmcnt(0)\n" \
"ds_read_b128 v[44:47], v17 offset:16384\nv_mfma_f32_16x16x32_bf16 v[56:59], v[68:71], v[72:75], v[56:59]\nv_mfma_f32_16x16x32_bf16 v[40:43], v[32:35], v[88:91], v[40:43]\nbuffer_load_dwordx4 v16, s[12:15], s43 offen offset:0 sc1 lds\n" \
"v_mfma_f32_16x16x32_bf16 v[56:59], v[80:83], v[88:91], v[56:59]\nds_read_b128 v[48:51], v17 offset:17408\nv_mfma_f32_16x16x32_bf16 v[72:75], v[20:23], v[112:115], 0\nv_cvt_pk_bf16_f32 v40, v40, v41\n" \
"v_cvt_pk_bf16_f32 v41, v42, v43\nv_mfma_f32_16x16x32_bf16 v[76:79], v[60:63], v[112:115], 0\nv_cvt_pk_bf16_f32 v42, v56, v57\nv_cvt_pk_bf16_f32 v43, v58, v59\n" \
"v_mfma_f32_16x16x32_bf16 v[72:75], v[24:27], v[116:119], v[72:75]\nbuffer_store_dwordx4 v[36:39], v16, s[8:11], s16 offen offset:3072 sc1\nv_mfma_f32_16x16x32_bf16 v[76:79], v[64:67], v[116:119], v[76:79]\nv_mfma_f32_16x16x32_bf16 v[72:75], v[28:31], v[120:123], v[72:75]\n" \
"v_mfma_f32_16x16x32_bf16 v[76:79], v[68:71], v[120:123], v[76:79]\nbuffer_load_dwordx4 v16, s[12:15], s43 offen offset:1024 sc1 lds\nv_mfma_f32_16x16x32_bf16 v[72:75], v[32:35], v[124:127], v[72:75]\ns_waitcnt lgkmcnt(0)\n" \
"v_mfma_f32_16x16x32_bf16 v[76:79], v[80:83], v[124:127], v[76:79]\nv_mfma_f32_16x16x32_bf16 v[36:39], v[20:23], v[44:47], 0\nv_cvt_pk_bf16_f32 v72, v72, v73\nv_cvt_pk_bf16_f32 v73, v74, v75\n" \
"v_mfma_f32_16x16x32_bf16 v[56:59], v[60:63], v[44:47], 0\nbuffer_store_dwordx4 v[52:55], v16, s[8:11], s17 offen offset:2048 sc1\nv_cvt_pk_bf16_f32 v74, v76, v77\nv_cvt_pk_bf16_f32 v75, v78, v79\n" \
"v_mfma_f32_16x16x32_bf16 v[36:39], v[24:27], v[48:51], v[36:39]\nv_mfma_f32_16x16x32_bf16 v[56:59], v[64:67], v[48:51], v[56:59]\nv_mfma_f32_16x16x32_bf16 v[36:39], v[28:31], v[176:179], v[36:39]\nv_mfma_f32_16x16x32_bf16 v[56:59], v[68:71], v[176:179], v[56:59]\n" \
"buffer_load_dwordx4 v16, s[12:15], s43 offen offset:2048 sc1 lds\nv_mfma_f32_16x16x32_bf16 v[36:39], v[32:35], v[180:183], v[36:39]\nv_mfma_f32_16x16x32_bf16 v[56:59], v[80:83], v[180:183], v[56:59]\nv_mfma_f32_16x16x32_bf16 v[44:47], v[20:23], v[184:187], 0\n" \
"v_cvt_pk_bf16_f32 v36, v36, v37\nv_cvt_pk_bf16_f32 v37, v38, v39\nv_mfma_f32_16x16x32_bf16 v[48:51], v[60:63], v[184:187], 0\nbuffer_store_dwordx4 v[40:43], v16, s[8:11], s17 offen offset:1024 sc1\n" \
"v_cvt_pk_bf16_f32 v38, v56, v57\nv_cvt_pk_bf16_f32 v39, v58, v59\nv_mfma_f32_16x16x32_bf16 v[44:47], v[24:27], v[188:191], v[44:47]\nv_mfma_f32_16x16x32_bf16 v[48:51], v[64:67], v[188:191], v[48:51]\n" \
"v_mfma_f32_16x16x32_bf16 v[44:47], v[28:31], v[192:195], v[44:47]\nv_mfma_f32_16x16x32_bf16 v[48:51], v[68:71], v[192:195], v[48:51]\nbuffer_load_dwordx4 v16, s[12:15], s43 offen offset:3072 sc1 lds\nv_mfma_f32_16x16x32_bf16 v[44:47], v[32:35], v[196:199], v[44:47]\n" \
"v_mfma_f32_16x16x32_bf16 v[48:51], v[80:83], v[196:199], v[48:51]\nv_mfma_f32_16x16x32_bf16 v[40:43], v[20:23], v[160:163], 0\nbuffer_store_dwordx4 v[72:75], v16, s[8:11], s17 offen offset:0 sc1\nv_cvt_pk_bf16_f32 v44, v44, v45\n" \
"v_cvt_pk_bf16_f32 v45, v46, v47\nv_mfma_f32_16x16x32_bf16 v[52:55], v[60:63], v[160:163], 0\nv_cvt_pk_bf16_f32 v46, v48, v49\nv_cvt_pk_bf16_f32 v47, v50, v51\n" \
"v_mfma_f32_16x16x32_bf16 v[40:43], v[24:27], v[164:167], v[40:43]\nv_mfma_f32_16x16x32_bf16 v[52:55], v[64:67], v[164:167], v[52:55]\nv_mfma_f32_16x16x32_bf16 v[40:43], v[28:31], v[168:171], v[40:43]\nds_read_b128 v[48:51], v17 offset:20480\n" \
"v_mfma_f32_16x16x32_bf16 v[52:55], v[68:71], v[168:171], v[52:55]\nv_mfma_f32_16x16x32_bf16 v[40:43], v[32:35], v[172:175], v[40:43]\nds_read_b128 v[56:59], v17 offset:21504\nv_mfma_f32_16x16x32_bf16 v[52:55], v[80:83], v[172:175], v[52:55]\n" \
"v_mfma_f32_16x16x32_bf16 v[72:75], v[20:23], v[232:235], 0\nbuffer_store_dwordx4 v[36:39], v16, s[8:11], s18 offen offset:1024 sc1\nv_cvt_pk_bf16_f32 v40, v40, v41\nv_cvt_pk_bf16_f32 v41, v42, v43\n" \
"v_mfma_f32_16x16x32_bf16 v[36:39], v[60:63], v[232:235], 0\nv_cvt_pk_bf16_f32 v42, v52, v53\nv_cvt_pk_bf16_f32 v43, v54, v55\nv_mfma_f32_16x16x32_bf16 v[72:75], v[24:27], v[236:239], v[72:75]\n" \
"v_mfma_f32_16x16x32_bf16 v[36:39], v[64:67], v[236:239], v[36:39]\nv_mfma_f32_16x16x32_bf16 v[72:75], v[28:31], v[240:243], v[72:75]\nv_mfma_f32_16x16x32_bf16 v[36:39], v[68:71], v[240:243], v[36:39]\nv_mfma_f32_16x16x32_bf16 v[72:75], v[32:35], v[244:247], v[72:75]\n" \
"s_waitcnt lgkmcnt(0)\nv_mfma_f32_16x16x32_bf16 v[36:39], v[80:83], v[244:247], v[36:39]\nbuffer_store_dwordx4 v[44:47], v16, s[8:11], s18 offen offset:2048 sc1\nv_mfma_f32_16x16x32_bf16 v[52:55], v[20:23], v[48:51], 0\n" \
"v_cvt_pk_bf16_f32 v72, v72, v73\nv_cvt_pk_bf16_f32 v73, v74, v75\nv_mfma_f32_16x16x32_bf16 v[44:47], v[60:63], v[48:51], 0\nv_cvt_pk_bf16_f32 v74, v36, v37\n" \
"v_cvt_pk_bf16_f32 v75, v38, v39\nv_mfma_f32_16x16x32_bf16 v[52:55], v[24:27], v[56:59], v[52:55]\nv_mfma_f32_16x16x32_bf16 v[44:47], v[64:67], v[56:59], v[44:47]\nv_mfma_f32_16x16x32_bf16 v[52:55], v[28:31], v[224:227], v[52:55]\n" \
"ds_read_b128 v[36:39], v17 offset:22528\nv_mfma_f32_16x16x32_bf16 v[44:47], v[68:71], v[224:227], v[44:47]\nv_mfma_f32_16x16x32_bf16 v[52:55], v[32:35], v[228:231], v[52:55]\nbuffer_store_dwordx4 v[40:43], v16, s[8:11], s18 offen offset:0 sc1\n" \
"ds_read_b128 v[48:51], v17 offset:23552\nv_mfma_f32_16x16x32_bf16 v[44:47], v[80:83], v[228:231], v[44:47]\nv_mfma_f32_16x16x32_bf16 v[40:43], v[20:23], v[208:211], 0\nv_cvt_pk_bf16_f32 v52, v52, v53\n" \
"v_cvt_pk_bf16_f32 v53, v54, v55\nv_mfma_f32_16x16x32_bf16 v[56:59], v[60:63], v[208:211], 0\nv_cvt_pk_bf16_f32 v54, v44, v45\nv_cvt_pk_bf16_f32 v55, v46, v47\n" \
"v_mfma_f32_16x16x32_bf16 v[40:43], v[24:27], v[212:215], v[40:43]\nv_mfma_f32_16x16x32_bf16 v[56:59], v[64:67], v[212:215], v[56:59]\nds_read_b128 v[44:47], v16 offset:0\nv_mfma_f32_16x16x32_bf16 v[40:43], v[28:31], v[216:219], v[40:43]\n" \
"ds_read_b128 v[76:79], v16 offset:32768\nv_mfma_f32_16x16x32_bf16 v[56:59], v[68:71], v[216:219], v[56:59]\nv_mfma_f32_16x16x32_bf16 v[40:43], v[32:35], v[220:223], v[40:43]\ns_waitcnt lgkmcnt(2)\n" \
"buffer_store_dwordx4 v[72:75], v16, s[8:11], s19 offen offset:2048 sc1\nv_mfma_f32_16x16x32_bf16 v[56:59], v[80:83], v[220:223], v[56:59]\nds_read_b128 v[84:87], v16 offset:1024\nv_mfma_f32_16x16x32_bf16 v[72:75], v[20:23], v[36:39], 0\n" \
"ds_read_b128 v[88:91], v16 offset:33792\nv_cvt_pk_bf16_f32 v40, v40, v41\nv_mfma_f32_16x16x32_bf16 v[20:23], v[60:63], v[36:39], 0\nv_cvt_pk_bf16_f32 v41, v42, v43\n" \
"v_cvt_pk_bf16_f32 v42, v56, v57\nv_mfma_f32_16x16x32_bf16 v[72:75], v[24:27], v[48:51], v[72:75]\nv_cvt_pk_bf16_f32 v43, v58, v59\nv_mfma_f32_16x16x32_bf16 v[20:23], v[64:67], v[48:51], v[20:23]\n" \
"ds_read_b128 v[24:27], v16 offset:2048\nv_mfma_f32_16x16x32_bf16 v[72:75], v[28:31], v[248:251], v[72:75]\nds_read_b128 v[36:39], v16 offset:34816\nv_mfma_f32_16x16x32_bf16 v[20:23], v[68:71], v[248:251], v[20:23]\n" \
"buffer_store_dwordx4 v[52:55], v16, s[8:11], s19 offen offset:1024 sc1\nv_mfma_f32_16x16x32_bf16 v[72:75], v[32:35], v[252:255], v[72:75]\ns_waitcnt lgkmcnt(2)\nv_mfma_f32_16x16x32_bf16 v[20:23], v[80:83], v[252:255], v[20:23]\n" \
"ds_read_b128 v[28:31], v16 offset:3072\nv_mfma_f32_16x16x32_bf16 v[32:35], v[0:3], v[44:47], 0\nds_read_b128 v[48:51], v16 offset:35840\nv_cvt_pk_bf16_f32 v72, v72, v73\n" \
"v_mfma_f32_16x16x32_bf16 v[52:55], v[0:3], v[76:79], 0\nv_cvt_pk_bf16_f32 v73, v74, v75\nv_cvt_pk_bf16_f32 v74, v20, v21\nv_mfma_f32_16x16x32_bf16 v[56:59], v[8:11], v[44:47], 0\n" \
"v_cvt_pk_bf16_f32 v75, v22, v23\nv_mfma_f32_16x16x32_bf16 v[44:47], v[8:11], v[76:79], 0\nds_read_b128 v[20:23], v16 offset:4096\nv_mfma_f32_16x16x32_bf16 v[32:35], v[4:7], v[84:87], v[32:35]\n" \
"ds_read_b128 v[60:63], v16 offset:36864\nv_mfma_f32_16x16x32_bf16 v[52:55], v[4:7], v[88:91], v[52:55]\nbuffer_store_dwordx4 v[40:43], v16, s[8:11], s19 offen offset:0 sc1\nv_mfma_f32_16x16x32_bf16 v[56:59], v[12:15], v[84:87], v[56:59]\n" \
"s_waitcnt lgkmcnt(2)\nv_mfma_f32_16x16x32_bf16 v[44:47], v[12:15], v[88:91], v[44:47]\nds_read_b128 v[40:43], v16 offset:5120\nv_mfma_f32_16x16x32_bf16 v[32:35], a[0:3], v[24:27], v[32:35]\n" \
"ds_read_b128 v[64:67], v16 offset:37888\nv_mfma_f32_16x16x32_bf16 v[52:55], a[0:3], v[36:39], v[52:55]\nv_mfma_f32_16x16x32_bf16 v[56:59], a[104:107], v[24:27], v[56:59]\nv_mfma_f32_16x16x32_bf16 v[44:47], a[104:107], v[36:39], v[44:47]\n" \
"ds_read_b128 v[24:27], v16 offset:6144\nv_mfma_f32_16x16x32_bf16 v[32:35], a[4:7], v[28:31], v[32:35]\nbuffer_store_dwordx4 v[72:75], v16, s[8:11], s19 offen offset:3072 sc1\nds_read_b128 v[36:39], v16 offset:38912\n" \
"v_mfma_f32_16x16x32_bf16 v[52:55], a[4:7], v[48:51], v[52:55]\nds_read_b128 v[68:71], v16 offset:7168\nv_mfma_f32_16x16x32_bf16 v[56:59], a[108:111], v[28:31], v[56:59]\nds_read_b128 v[72:75], v16 offset:39936\n" \
"v_mfma_f32_16x16x32_bf16 v[44:47], a[108:111], v[48:51], v[44:47]\ns_branch 1b\n2:\ns_cmp_lt_i32 s20, 1\n" \
"s_cbranch_scc1 3b\ns_add_u32 s30, s30, 1\ns_cmp_lt_u32 s30, 0x40000\ns_cbranch_scc0 9f\n" \
"v_max_u32 v109, v88, v90\nv_cmp_eq_u32 vcc, -1, v109\ns_cbranch_vccz 6f\nbuffer_load_dwordx4 v16, s[8:11], s43 offen offset:0 sc1 lds\n" \
"6:\nv_max_u32 v109, v92, v94\nv_cmp_eq_u32 vcc, -1, v109\ns_cbranch_vccz 6f\n" \
"buffer_load_dwordx4 v16, s[8:11], s43 offen offset:1024 sc1 lds\n6:\nv_max_u32 v109, v96, v98\nv_cmp_eq_u32 vcc, -1, v109\n" \
"s_cbranch_vccz 6f\nbuffer_load_dwordx4 v16, s[8:11], s43 offen offset:2048 sc1 lds\n6:\nv_max_u32 v109, v20, v22\n" \
"v_cmp_eq_u32 vcc, -1, v109\ns_cbranch_vccz 6f\nbuffer_load_dwordx4 v16, s[8:11], s43 offen offset:3072 sc1 lds\n6:\n" \
"s_waitcnt vmcnt(0)\nds_read_b128 v[88:91], v110 offset:0\nds_read_b128 v[92:95], v110 offset:1024\nds_read_b128 v[96:99], v110 offset:2048\n" \
"ds_read_b128 v[20:23], v110 offset:3072\ns_waitcnt lgkmcnt(0)\nv_max3_u32 v109, v88, v90, v92\nv_max3_u32 v109, v109, v94, v96\n" \
"v_max3_u32 v109, v109, v98, v20\nv_max_u32 v109, v109, v22\nv_cmp_eq_u32 vcc, -1, v109\ns_cbranch_vccnz 2b\n" \
"s_branch 3b\n4:\ns_cmp_ge_i32 s20, 64\ns_cbranch_scc1 9f\n" \
"s_cmp_ge_i32 s20, 62\ns_cbranch_scc1 5b\nv_bfe_u32 v111, v16, 4, 3\nv_lshlrev_b32 v111, 2, v111\n" \
"s_lshl_b32 s43, s20, 5\ns_add_u32 s43, s43, 64\ns_add_u32 s43, s43, s26\n7:\n" \
"v_cmp_eq_u32 vcc, -1, v110\ns_cbranch_vccz 8f\ns_add_u32 s30, s30, 1\ns_cmp_lt_u32 s30, 0x40000\n" \
"s_cbranch_scc0 9f\ns_sleep 8\nbuffer_load_dword v110, v111, s[8:11], s43 offen sc1\ns_waitcnt vmcnt(0)\n" \
"s_branch 7b\n8:\ns_mov_b32 s14, s10\ns_branch 5b\n" \
"9:\ns_waitcnt vmcnt(0)\ns_endpgm\n"
#define D_LOOP \
"s_branch 4f\n.p2align 6\n1:\ns_waitcnt lgkmcnt(0)\n" \
"v_mfma_f32_16x16x32_bf16 v[44:47], a[8:11], v[8:11], v[44:47]\nds_read_b128 v[16:19], v4 offset:3072\ns_mov_b32 m0, s24\nv_mfma_f32_16x16x32_bf16 v[64:67], a[8:11], v[12:15], v[64:67]\n" \
"ds_read_b128 v[32:35], v4 offset:35840\nbuffer_load_dword v1, s[16:19], 0 offen offset:0 sc1 lds\nv_mfma_f32_16x16x32_bf16 v[48:51], a[104:107], v[8:11], v[48:51]\ns_add_u32 m0, s23, 0\n" \
"s_and_b32 s98, s20, 1\ns_lshl_b32 s98, s98, 3\nv_mfma_f32_16x16x32_bf16 v[20:23], a[104:107], v[12:15], v[20:23]\nv_mfma_f32_16x16x32_bf16 v[56:59], a[200:203], v[8:11], v[56:59]\n" \
"v_mfma_f32_16x16x32_bf16 v[24:27], a[200:203], v[12:15], v[24:27]\nv_mfma_f32_16x16x32_bf16 v[60:63], v[152:155], v[8:11], v[60:63]\ns_waitcnt lgkmcnt(1)\nv_mfma_f32_16x16x32_bf16 v[28:31], v[152:155], v[12:15], v[28:31]\n" \
"s_waitcnt lgkmcnt(0)\nds_read_b128 v[8:11], v4 offset:4096\nv_mfma_f32_16x16x32_bf16 v[44:47], a[12:15], v[16:19], v[44:47]\nds_read_b128 v[12:15], v4 offset:36864\n" \
"v_mfma_f32_16x16x32_bf16 v[64:67], a[12:15], v[32:35], v[64:67]\nv_mfma_f32_16x16x32_bf16 v[48:51], a[108:111], v[16:19], v[48:51]\nv_mfma_f32_16x16x32_bf16 v[20:23], a[108:111], v[32:35], v[20:23]\nv_mfma_f32_16x16x32_bf16 v[56:59], a[204:207], v[16:19], v[56:59]\n" \
"v_mfma_f32_16x16x32_bf16 v[24:27], a[204:207], v[32:35], v[24:27]\nv_mfma_f32_16x16x32_bf16 v[60:63], v[156:159], v[16:19], v[60:63]\ns_waitcnt lgkmcnt(0)\nv_mfma_f32_16x16x32_bf16 v[28:31], v[156:159], v[32:35], v[28:31]\n" \
"ds_read_b128 v[16:19], v4 offset:5120\nv_mfma_f32_16x16x32_bf16 v[44:47], a[16:19], v[8:11], v[44:47]\nds_read_b128 v[32:35], v4 offset:37888\nv_mfma_f32_16x16x32_bf16 v[64:67], a[16:19], v[12:15], v[64:67]\n" \
"v_mfma_f32_16x16x32_bf16 v[48:51], a[112:115], v[8:11], v[48:51]\nv_mfma_f32_16x16x32_bf16 v[20:23], a[112:115], v[12:15], v[20:23]\nv_mfma_f32_16x16x32_bf16 v[56:59], a[208:211], v[8:11], v[56:59]\nv_mfma_f32_16x16x32_bf16 v[24:27], a[208:211], v[12:15], v[24:27]\n" \
"v_mfma_f32_16x16x32_bf16 v[60:63], v[160:163], v[8:11], v[60:63]\ns_waitcnt lgkmcnt(0)\nv_mfma_f32_16x16x32_bf16 v[28:31], v[160:163], v[12:15], v[28:31]\nds_read_b128 v[8:11], v4 offset:6144\n" \
"v_mfma_f32_16x16x32_bf16 v[44:47], a[20:23], v[16:19], v[44:47]\nds_read_b128 v[12:15], v4 offset:38912\nv_mfma_f32_16x16x32_bf16 v[64:67], a[20:23], v[32:35], v[64:67]\nv_mfma_f32_16x16x32_bf16 v[48:51], a[116:119], v[16:19], v[48:51]\n" \
"v_mfma_f32_16x16x32_bf16 v[20:23], a[116:119], v[32:35], v[20:23]\nv_mfma_f32_16x16x32_bf16 v[56:59], a[212:215], v[16:19], v[56:59]\nv_mfma_f32_16x16x32_bf16 v[24:27], a[212:215], v[32:35], v[24:27]\ns_add_u32 s99, s20, 1\n" \
"s_lshr_b32 s97, s99, 1\nv_readlane_b32 s97, v3, s97\nv_mfma_f32_16x16x32_bf16 v[60:63], v[164:167], v[16:19], v[60:63]\ns_waitcnt lgkmcnt(0)\n" \
"s_and_b32 s99, s99, 1\ns_lshl_b32 s99, s99, 3\nv_mfma_f32_16x16x32_bf16 v[28:31], v[164:167], v[32:35], v[28:31]\nds_read_b128 v[16:19], v4 offset:7168\n" \
"s_lshr_b32 s97, s97, s99\nv_mfma_f32_16x16x32_bf16 v[44:47], a[24:27], v[8:11], v[44:47]\nds_read_b128 v[32:35], v4 offset:39936\ns_and_b32 s97, s97, 0xff\n" \
"v_mfma_f32_16x16x32_bf16 v[64:67], a[24:27], v[12:15], v[64:67]\ns_mul_i32 s97, s97, 0x38000\ns_add_u32 s97, s97, s21\nv_mfma_f32_16x16x32_bf16 v[48:51], a[120:123], v[8:11], v[48:51]\n" \
"v_mfma_f32_16x16x32_bf16 v[20:23], a[120:123], v[12:15], v[20:23]\nv_mfma_f32_16x16x32_bf16 v[56:59], a[216:219], v[8:11], v[56:59]\nv_mfma_f32_16x16x32_bf16 v[24:27], a[216:219], v[12:15], v[24:27]\nv_mfma_f32_16x16x32_bf16 v[60:63], v[168:171], v[8:11], v[60:63]\n" \
"s_waitcnt lgkmcnt(0)\nv_mfma_f32_16x16x32_bf16 v[28:31], v[168:171], v[12:15], v[28:31]\nds_read_b128 v[8:11], v4 offset:8192\nv_mfma_f32_16x16x32_bf16 v[44:47], a[28:31], v[16:19], v[44:47]\n" \
"ds_read_b128 v[12:15], v4 offset:40960\nv_mfma_f32_16x16x32_bf16 v[64:67], a[28:31], v[32:35], v[64:67]\nv_mfma_f32_16x16x32_bf16 v[48:51], a[124:127], v[16:19], v[48:51]\nv_mfma_f32_16x16x32_bf16 v[20:23], a[124:127], v[32:35], v[20:23]\n" \
"v_mfma_f32_16x16x32_bf16 v[56:59], a[220:223], v[16:19], v[56:59]\nv_mfma_f32_16x16x32_bf16 v[24:27], a[220:223], v[32:35], v[24:27]\ns_add_u32 s99, s20, 2\nv_mov_b32 v111, s20\n" \
"v_mfma_f32_16x16x32_bf16 v[60:63], v[172:175], v[16:19], v[60:63]\ns_waitcnt lgkmcnt(0)\nv_mfma_f32_16x16x32_bf16 v[28:31], v[172:175], v[32:35], v[28:31]\nds_read_b128 v[16:19], v4 offset:9216\n" \
"v_mfma_f32_16x16x32_bf16 v[44:47], a[32:35], v[8:11], v[44:47]\nds_read_b128 v[32:35], v4 offset:41984\nv_mfma_f32_16x16x32_bf16 v[64:67], a[32:35], v[12:15], v[64:67]\nv_mfma_f32_16x16x32_bf16 v[48:51], a[128:131], v[8:11], v[48:51]\n" \
"s_waitcnt vmcnt(0)\ns_barrier\nv_mfma_f32_16x16x32_bf16 v[20:23], a[128:131], v[12:15], v[20:23]\nbuffer_store_dword v111, v2, s[8:11], s25 offen offset:0 sc1\n" \
"v_mfma_f32_16x16x32_bf16 v[56:59], a[224:227], v[8:11], v[56:59]\nds_read_b32 v110, v5 offset:0\ns_mov_b32 s25, 0\nv_mfma_f32_16x16x32_bf16 v[24:27], a[224:227], v[12:15], v[24:27]\n" \
"v_mfma_f32_16x16x32_bf16 v[60:63], v[176:179], v[8:11], v[60:63]\ns_waitcnt lgkmcnt(1)\nv_mfma_f32_16x16x32_bf16 v[28:31], v[176:179], v[12:15], v[28:31]\nds_read_b128 v[8:11], v4 offset:10240\n" \
"v_mfma_f32_16x16x32_bf16 v[44:47], a[36:39], v[16:19], v[44:47]\nds_read_b128 v[12:15], v4 offset:43008\nv_mfma_f32_16x16x32_bf16 v[64:67], a[36:39], v[32:35], v[64:67]\ns_waitcnt lgkmcnt(2)\n" \
"v_cmp_gt_i32 s[94:95], s99, v110\nv_mfma_f32_16x16x32_bf16 v[48:51], a[132:135], v[16:19], v[48:51]\nv_mfma_f32_16x16x32_bf16 v[20:23], a[132:135], v[32:35], v[20:23]\ns_cmp_eq_u64 s[94:95], 0\n" \
"s_cselect_b32 s14, s10, 0\nv_mfma_f32_16x16x32_bf16 v[56:59], a[228:231], v[16:19], v[56:59]\nv_mfma_f32_16x16x32_bf16 v[24:27], a[228:231], v[32:35], v[24:27]\nbuffer_load_dwordx4 v0, s[12:15], s97 offen offset:0 sc1 lds\n" \
"v_mfma_f32_16x16x32_bf16 v[60:63], v[180:183], v[16:19], v[60:63]\ns_waitcnt lgkmcnt(0)\nv_mfma_f32_16x16x32_bf16 v[28:31], v[180:183], v[32:35], v[28:31]\nds_read_b128 v[16:19], v4 offset:11264\n" \
"v_mfma_f32_16x16x32_bf16 v[44:47], a[40:43], v[8:11], v[44:47]\nds_read_b128 v[32:35], v4 offset:44032\nv_mfma_f32_16x16x32_bf16 v[64:67], a[40:43], v[12:15], v[64:67]\nbuffer_load_dwordx4 v0, s[12:15], s97 offen offset:1024 sc1 lds\n" \
"v_mfma_f32_16x16x32_bf16 v[48:51], a[136:139], v[8:11], v[48:51]\nv_mfma_f32_16x16x32_bf16 v[20:23], a[136:139], v[12:15], v[20:23]\nv_mfma_f32_16x16x32_bf16 v[56:59], a[232:235], v[8:11], v[56:59]\nv_mfma_f32_16x16x32_bf16 v[24:27], a[232:235], v[12:15], v[24:27]\n" \
"buffer_load_dwordx4 v0, s[12:15], s97 offen offset:2048 sc1 lds\nv_mfma_f32_16x16x32_bf16 v[60:63], v[184:187], v[8:11], v[60:63]\ns_waitcnt lgkmcnt(0)\nv_mfma_f32_16x16x32_bf16 v[28:31], v[184:187], v[12:15], v[28:31]\n" \
"ds_read_b128 v[8:11], v4 offset:12288\nv_mfma_f32_16x16x32_bf16 v[44:47], a[44:47], v[16:19], v[44:47]\nds_read_b128 v[12:15], v4 offset:45056\nv_mfma_f32_16x16x32_bf16 v[64:67], a[44:47], v[32:35], v[64:67]\n" \
"buffer_load_dwordx4 v0, s[12:15], s97 offen offset:3072 sc1 lds\nv_mfma_f32_16x16x32_bf16 v[48:51], a[140:143], v[16:19], v[48:51]\ns_add_u32 m0, s23, 0x1000\ns_add_u32 s97, s97, 0x1000\n" \
"v_mfma_f32_16x16x32_bf16 v[20:23], a[140:143], v[32:35], v[20:23]\nv_mfma_f32_16x16x32_bf16 v[56:59], a[236:239], v[16:19], v[56:59]\nv_mfma_f32_16x16x32_bf16 v[24:27], a[236:239], v[32:35], v[24:27]\nbuffer_load_dwordx4 v0, s[12:15], s97 offen offset:0 sc1 lds\n" \
"v_mfma_f32_16x16x32_bf16 v[60:63], v[188:191], v[16:19], v[60:63]\ns_waitcnt lgkmcnt(0)\nv_mfma_f32_16x16x32_bf16 v[28:31], v[188:191], v[32:35], v[28:31]\nds_read_b128 v[16:19], v4 offset:13312\n" \
"v_mfma_f32_16x16x32_bf16 v[44:47], a[48:51], v[8:11], v[44:47]\nds_read_b128 v[32:35], v4 offset:46080\nv_mfma_f32_16x16x32_bf16 v[64:67], a[48:51], v[12:15], v[64:67]\nbuffer_load_dwordx4 v0, s[12:15], s97 offen offset:1024 sc1 lds\n" \
"v_mfma_f32_16x16x32_bf16 v[48:51], a[144:147], v[8:11], v[48:51]\nv_mfma_f32_16x16x32_bf16 v[20:23], a[144:147], v[12:15], v[20:23]\nv_mfma_f32_16x16x32_bf16 v[56:59], a[240:243], v[8:11], v[56:59]\nv_mfma_f32_16x16x32_bf16 v[24:27], a[240:243], v[12:15], v[24:27]\n" \
"buffer_load_dwordx4 v0, s[12:15], s97 offen offset:2048 sc1 lds\nv_mfma_f32_16x16x32_bf16 v[60:63], v[192:195], v[8:11], v[60:63]\ns_waitcnt lgkmcnt(0)\nv_mfma_f32_16x16x32_bf16 v[28:31], v[192:195], v[12:15], v[28:31]\n" \
"ds_read_b128 v[8:11], v4 offset:14336\nv_mfma_f32_16x16x32_bf16 v[44:47], a[52:55], v[16:19], v[44:47]\nds_read_b128 v[12:15], v4 offset:47104\nv_mfma_f32_16x16x32_bf16 v[64:67], a[52:55], v[32:35], v[64:67]\n" \
"buffer_load_dwordx4 v0, s[12:15], s97 offen offset:3072 sc1 lds\nv_mfma_f32_16x16x32_bf16 v[48:51], a[148:151], v[16:19], v[48:51]\ns_add_u32 m0, s23, 0x2000\ns_add_u32 s97, s97, 0x1000\n" \
"s_xor_b32 s23, s23, 0x10000\nv_mfma_f32_16x16x32_bf16 v[20:23], a[148:151], v[32:35], v[20:23]\nv_mfma_f32_16x16x32_bf16 v[56:59], a[244:247], v[16:19], v[56:59]\nv_mfma_f32_16x16x32_bf16 v[24:27], a[244:247], v[32:35], v[24:27]\n" \
"buffer_load_dwordx4 v0, s[12:15], s97 offen offset:0 sc1 lds\nv_mfma_f32_16x16x32_bf16 v[60:63], v[196:199], v[16:19], v[60:63]\ns_waitcnt lgkmcnt(0)\nv_mfma_f32_16x16x32_bf16 v[28:31], v[196:199], v[32:35], v[28:31]\n" \
"ds_read_b128 v[16:19], v4 offset:15360\nv_mfma_f32_16x16x32_bf16 v[44:47], a[56:59], v[8:11], v[44:47]\nds_read_b128 v[32:35], v4 offset:48128\nv_mfma_f32_16x16x32_bf16 v[64:67], a[56:59], v[12:15], v[64:67]\n" \
"buffer_load_dwordx4 v0, s[12:15], s97 offen offset:1024 sc1 lds\nv_mfma_f32_16x16x32_bf16 v[48:51], a[152:155], v[8:11], v[48:51]\nv_mfma_f32_16x16x32_bf16 v[20:23], a[152:155], v[12:15], v[20:23]\nv_mfma_f32_16x16x32_bf16 v[56:59], a[248:251], v[8:11], v[56:59]\n" \
"v_mfma_f32_16x16x32_bf16 v[24:27], a[248:251], v[12:15], v[24:27]\nbuffer_load_dwordx4 v0, s[12:15], s97 offen offset:2048 sc1 lds\nv_mfma_f32_16x16x32_bf16 v[60:63], v[200:203], v[8:11], v[60:63]\ns_waitcnt lgkmcnt(0)\n" \
"v_mfma_f32_16x16x32_bf16 v[28:31], v[200:203], v[12:15], v[28:31]\nds_read_b128 v[8:11], v4 offset:16384\nv_mfma_f32_16x16x32_bf16 v[44:47], a[60:63], v[16:19], v[44:47]\nds_read_b128 v[12:15], v4 offset:49152\n" \
"v_mfma_f32_16x16x32_bf16 v[64:67], a[60:63], v[32:35], v[64:67]\nbuffer_load_dwordx4 v0, s[12:15], s97 offen offset:3072 sc1 lds\nv_mfma_f32_16x16x32_bf16 v[48:51], a[156:159], v[16:19], v[48:51]\nv_mfma_f32_16x16x32_bf16 v[20:23], a[156:159], v[32:35], v[20:23]\n" \
"v_mfma_f32_16x16x32_bf16 v[56:59], a[252:255], v[16:19], v[56:59]\nv_mfma_f32_16x16x32_bf16 v[24:27], a[252:255], v[32:35], v[24:27]\nv_mfma_f32_16x16x32_bf16 v[60:63], v[204:207], v[16:19], v[60:63]\ns_waitcnt lgkmcnt(0)\n" \
"v_mfma_f32_16x16x32_bf16 v[28:31], v[204:207], v[32:35], v[28:31]\nds_read_b128 v[16:19], v4 offset:17408\nv_mfma_f32_16x16x32_bf16 v[44:47], a[64:67], v[8:11], v[44:47]\nds_read_b128 v[32:35], v4 offset:50176\n" \
"v_mfma_f32_16x16x32_bf16 v[64:67], a[64:67], v[12:15], v[64:67]\nv_mfma_f32_16x16x32_bf16 v[48:51], a[160:163], v[8:11], v[48:51]\nv_mfma_f32_16x16x32_bf16 v[20:23], a[160:163], v[12:15], v[20:23]\nv_mfma_f32_16x16x32_bf16 v[56:59], v[112:115], v[8:11], v[56:59]\n" \
"v_mfma_f32_16x16x32_bf16 v[24:27], v[112:115], v[12:15], v[24:27]\nv_mfma_f32_16x16x32_bf16 v[60:63], v[208:211], v[8:11], v[60:63]\ns_waitcnt lgkmcnt(0)\nv_mfma_f32_16x16x32_bf16 v[28:31], v[208:211], v[12:15], v[28:31]\n" \
"ds_read_b128 v[8:11], v4 offset:18432\nv_mfma_f32_16x16x32_bf16 v[44:47], a[68:71], v[16:19], v[44:47]\nds_read_b128 v[12:15], v4 offset:51200\nv_mfma_f32_16x16x32_bf16 v[64:67], a[68:71], v[32:35], v[64:67]\n" \
"v_mfma_f32_16x16x32_bf16 v[48:51], a[164:167], v[16:19], v[48:51]\nv_mfma_f32_16x16x32_bf16 v[20:23], a[164:167], v[32:35], v[20:23]\ns_lshr_b32 s99, s20, 1\ns_add_u32 s20, s20, 1\n" \
"v_readlane_b32 s99, v3, s99\nv_mfma_f32_16x16x32_bf16 v[56:59], v[116:119], v[16:19], v[56:59]\nv_mfma_f32_16x16x32_bf16 v[24:27], v[116:119], v[32:35], v[24:27]\ns_lshr_b32 s99, s99, s98\n" \
"s_and_b32 s99, s99, 0xff\ns_mul_i32 s99, s99, 0x50000\nv_mfma_f32_16x16x32_bf16 v[60:63], v[212:215], v[16:19], v[60:63]\ns_waitcnt lgkmcnt(0)\n" \
"s_add_u32 s99, s99, s22\ns_add_u32 s98, s99, 0x8000\nv_mfma_f32_16x16x32_bf16 v[28:31], v[212:215], v[32:35], v[28:31]\nds_read_b128 v[16:19], v4 offset:19456\n" \
"v_mfma_f32_16x16x32_bf16 v[44:47], a[72:75], v[8:11], v[44:47]\nds_read_b128 v[32:35], v4 offset:52224\nv_mfma_f32_16x16x32_bf16 v[64:67], a[72:75], v[12:15], v[64:67]\nv_mfma_f32_16x16x32_bf16 v[48:51], a[168:171], v[8:11], v[48:51]\n" \
"v_mfma_f32_16x16x32_bf16 v[20:23], a[168:171], v[12:15], v[20:23]\nv_mfma_f32_16x16x32_bf16 v[56:59], v[120:123], v[8:11], v[56:59]\nv_mfma_f32_16x16x32_bf16 v[24:27], v[120:123], v[12:15], v[24:27]\nv_mfma_f32_16x16x32_bf16 v[60:63], v[216:219], v[8:11], v[60:63]\n" \
"s_waitcnt lgkmcnt(0)\nv_mfma_f32_16x16x32_bf16 v[28:31], v[216:219], v[12:15], v[28:31]\nds_read_b128 v[8:11], v4 offset:20480\nv_mfma_f32_16x16x32_bf16 v[44:47], a[76:79], v[16:19], v[44:47]\n" \
"ds_read_b128 v[12:15], v4 offset:53248\nv_mfma_f32_16x16x32_bf16 v[64:67], a[76:79], v[32:35], v[64:67]\nv_mfma_f32_16x16x32_bf16 v[48:51], a[172:175], v[16:19], v[48:51]\nv_mfma_f32_16x16x32_bf16 v[20:23], a[172:175], v[32:35], v[20:23]\n" \
"v_mfma_f32_16x16x32_bf16 v[56:59], v[124:127], v[16:19], v[56:59]\nv_mfma_f32_16x16x32_bf16 v[24:27], v[124:127], v[32:35], v[24:27]\nv_mfma_f32_16x16x32_bf16 v[60:63], v[220:223], v[16:19], v[60:63]\ns_waitcnt lgkmcnt(0)\n" \
"v_mfma_f32_16x16x32_bf16 v[28:31], v[220:223], v[32:35], v[28:31]\nds_read_b128 v[36:39], v4 offset:21504\nv_mfma_f32_16x16x32_bf16 v[44:47], a[80:83], v[8:11], v[44:47]\nds_read_b128 v[32:35], v4 offset:54272\n" \
"v_mfma_f32_16x16x32_bf16 v[64:67], a[80:83], v[12:15], v[64:67]\nv_mfma_f32_16x16x32_bf16 v[48:51], a[176:179], v[8:11], v[48:51]\nv_mfma_f32_16x16x32_bf16 v[20:23], a[176:179], v[12:15], v[20:23]\nv_mfma_f32_16x16x32_bf16 v[56:59], v[128:131], v[8:11], v[56:59]\n" \
"v_mfma_f32_16x16x32_bf16 v[24:27], v[128:131], v[12:15], v[24:27]\nds_read_b128 v[40:43], v4 offset:22528\nv_mfma_f32_16x16x32_bf16 v[60:63], v[224:227], v[8:11], v[60:63]\ns_waitcnt lgkmcnt(1)\n" \
"v_mfma_f32_16x16x32_bf16 v[28:31], v[224:227], v[12:15], v[28:31]\nds_read_b128 v[16:19], v4 offset:55296\nv_xor_b32 v4, 0x10000, v4\nv_mfma_f32_16x16x32_bf16 v[44:47], a[84:87], v[36:39], v[44:47]\n" \
"v_mfma_f32_16x16x32_bf16 v[64:67], a[84:87], v[32:35], v[64:67]\nv_mfma_f32_16x16x32_bf16 v[48:51], a[180:183], v[36:39], v[48:51]\nv_mfma_f32_16x16x32_bf16 v[20:23], a[180:183], v[32:35], v[20:23]\ns_waitcnt vmcnt(0) lgkmcnt(0)\n" \
"s_barrier\nv_mfma_f32_16x16x32_bf16 v[56:59], v[132:135], v[36:39], v[56:59]\nv_mfma_f32_16x16x32_bf16 v[24:27], v[132:135], v[32:35], v[24:27]\nv_mfma_f32_16x16x32_bf16 v[60:63], v[228:231], v[36:39], v[60:63]\n" \
"v_mfma_f32_16x16x32_bf16 v[28:31], v[228:231], v[32:35], v[28:31]\nds_read_b128 v[8:11], v4 offset:0\nv_mfma_f32_16x16x32_bf16 v[44:47], a[88:91], v[40:43], v[44:47]\nds_read_b128 v[12:15], v4 offset:32768\n" \
"v_mfma_f32_16x16x32_bf16 v[48:51], a[184:187], v[40:43], v[48:51]\nv_mfma_f32_16x16x32_bf16 v[56:59], v[136:139], v[40:43], v[56:59]\nv_cvt_pk_bf16_f32 v36, v44, v45\nv_cvt_pk_bf16_f32 v37, v46, v47\n" \
"v_mfma_f32_16x16x32_bf16 v[60:63], v[232:235], v[40:43], v[60:63]\nv_cvt_pk_bf16_f32 v38, v48, v49\nv_cvt_pk_bf16_f32 v39, v50, v51\nv_mfma_f32_16x16x32_bf16 v[64:67], a[88:91], v[16:19], v[64:67]\n" \
"v_cvt_pk_bf16_f32 v32, v56, v57\nv_cvt_pk_bf16_f32 v33, v58, v59\nv_mfma_f32_16x16x32_bf16 v[20:23], a[184:187], v[16:19], v[20:23]\nbuffer_store_dwordx4 v[36:39], v6, s[8:11], s99 offen offset:0 sc1\n" \
"v_cvt_pk_bf16_f32 v34, v60, v61\nv_cvt_pk_bf16_f32 v35, v62, v63\nv_mfma_f32_16x16x32_bf16 v[24:27], v[136:139], v[16:19], v[24:27]\ns_waitcnt lgkmcnt(0)\n" \
"v_cvt_pk_bf16_f32 v36, v64, v65\nv_cvt_pk_bf16_f32 v37, v66, v67\nv_mfma_f32_16x16x32_bf16 v[28:31], v[232:235], v[16:19], v[28:31]\nds_read_b128 v[40:43], v4 offset:1024\n" \
"v_cvt_pk_bf16_f32 v38, v20, v21\nv_mfma_f32_16x16x32_bf16 v[44:47], a[0:3], v[8:11], 0\nds_read_b128 v[16:19], v4 offset:33792\nv_cvt_pk_bf16_f32 v39, v22, v23\n" \
"v_mfma_f32_16x16x32_bf16 v[48:51], a[96:99], v[8:11], 0\nbuffer_store_dwordx4 v[32:35], v6, s[8:11], s99 offen offset:512 sc1\nv_cvt_pk_bf16_f32 v52, v24, v25\nv_cvt_pk_bf16_f32 v53, v26, v27\n" \
"v_cvt_pk_bf16_f32 v54, v28, v29\nv_mfma_f32_16x16x32_bf16 v[56:59], a[192:195], v[8:11], 0\nv_cvt_pk_bf16_f32 v55, v30, v31\nv_mfma_f32_16x16x32_bf16 v[60:63], v[144:147], v[8:11], 0\n" \
"v_mfma_f32_16x16x32_bf16 v[64:67], a[0:3], v[12:15], 0\nv_mfma_f32_16x16x32_bf16 v[20:23], a[96:99], v[12:15], 0\nbuffer_store_dwordx4 v[36:39], v6, s[8:11], s98 offen offset:0 sc1\nv_mfma_f32_16x16x32_bf16 v[24:27], a[192:195], v[12:15], 0\n" \
"s_waitcnt lgkmcnt(0)\nv_mfma_f32_16x16x32_bf16 v[28:31], v[144:147], v[12:15], 0\nds_read_b128 v[8:11], v4 offset:2048\nv_mfma_f32_16x16x32_bf16 v[44:47], a[4:7], v[40:43], v[44:47]\n" \
"ds_read_b128 v[12:15], v4 offset:34816\nv_mfma_f32_16x16x32_bf16 v[64:67], a[4:7], v[16:19], v[64:67]\nbuffer_store_dwordx4 v[52:55], v6, s[8:11], s98 offen offset:512 sc1\nv_mfma_f32_16x16x32_bf16 v[48:51], a[100:103], v[40:43], v[48:51]\n" \
"v_mfma_f32_16x16x32_bf16 v[20:23], a[100:103], v[16:19], v[20:23]\nv_mfma_f32_16x16x32_bf16 v[56:59], a[196:199], v[40:43], v[56:59]\nv_mfma_f32_16x16x32_bf16 v[24:27], a[196:199], v[16:19], v[24:27]\nv_mfma_f32_16x16x32_bf16 v[60:63], v[148:151], v[40:43], v[60:63]\n" \
"s_cmp_eq_u32 s14, 0\nv_mfma_f32_16x16x32_bf16 v[28:31], v[148:151], v[16:19], v[28:31]\ns_cbranch_scc0 1b\n4:\n" \
"buffer_load_dword v68, v1, s[8:11], 0 offen sc1\nv_mov_b32 v69, s20\ns_add_u32 s98, s20, 1\ns_waitcnt vmcnt(0)\n" \
"v_cmp_gt_i32 vcc, s98, v68\ns_cbranch_vccz 8f\nbuffer_store_dword v69, v2, s[8:11], 0 offen sc1\ns_mov_b32 s25, 0x40000000\n" \
"s_cmp_ge_u32 s20, 0x80\ns_cbranch_scc1 9f\n7:\ns_add_u32 s26, s26, 1\n" \
"s_cmp_lt_u32 s26, 0x40000\ns_cbranch_scc0 9f\ns_sleep 8\nbuffer_load_dword v68, v1, s[8:11], 0 offen sc1\n" \
"s_waitcnt vmcnt(0)\nv_cmp_gt_i32 vcc, s98, v68\ns_cbranch_vccnz 7b\n8:\n" \
"s_lshr_b32 s98, s20, 1\nv_readlane_b32 s98, v3, s98\ns_and_b32 s99, s20, 1\ns_lshl_b32 s99, s99, 3\n" \
"s_lshr_b32 s98, s98, s99\ns_and_b32 s98, s98, 0xff\ns_mul_i32 s98, s98, 0x38000\ns_add_u32 s98, s98, s21\n" \
"s_xor_b32 s99, s23, 0x10000\ns_add_u32 m0, s99, 0\ns_add_u32 s98, s98, 0\nbuffer_load_dwordx4 v0, s[8:11], s98 offen offset:0 sc1 lds\n" \
"buffer_load_dwordx4 v0, s[8:11], s98 offen offset:1024 sc1 lds\nbuffer_load_dwordx4 v0, s[8:11], s98 offen offset:2048 sc1 lds\nbuffer_load_dwordx4 v0, s[8:11], s98 offen offset:3072 sc1 lds\ns_add_u32 m0, s99, 0x1000\n" \
"s_add_u32 s98, s98, 0x1000\nbuffer_load_dwordx4 v0, s[8:11], s98 offen offset:0 sc1 lds\nbuffer_load_dwordx4 v0, s[8:11], s98 offen offset:1024 sc1 lds\nbuffer_load_dwordx4 v0, s[8:11], s98 offen offset:2048 sc1 lds\n" \
"buffer_load_dwordx4 v0, s[8:11], s98 offen offset:3072 sc1 lds\ns_add_u32 m0, s99, 0x2000\ns_add_u32 s98, s98, 0x1000\nbuffer_load_dwordx4 v0, s[8:11], s98 offen offset:0 sc1 lds\n" \
"buffer_load_dwordx4 v0, s[8:11], s98 offen offset:1024 sc1 lds\nbuffer_load_dwordx4 v0, s[8:11], s98 offen offset:2048 sc1 lds\nbuffer_load_dwordx4 v0, s[8:11], s98 offen offset:3072 sc1 lds\nbuffer_store_dword v69, v2, s[8:11], s25 offen sc1\n" \
"s_mov_b32 s25, 0x40000000\ns_waitcnt vmcnt(1) lgkmcnt(0)\ns_barrier\nds_read_b128 v[68:71], v4 offset:0\n" \
"ds_read_b128 v[72:75], v4 offset:32768\nds_read_b128 v[76:79], v4 offset:1024\nds_read_b128 v[80:83], v4 offset:33792\nds_read_b128 v[8:11], v4 offset:2048\n" \
"ds_read_b128 v[12:15], v4 offset:34816\ns_waitcnt lgkmcnt(0)\nv_mfma_f32_16x16x32_bf16 v[44:47], a[0:3], v[68:71], 0\nv_mfma_f32_16x16x32_bf16 v[48:51], a[96:99], v[68:71], 0\n" \
"v_mfma_f32_16x16x32_bf16 v[56:59], a[192:195], v[68:71], 0\nv_mfma_f32_16x16x32_bf16 v[60:63], v[144:147], v[68:71], 0\nv_mfma_f32_16x16x32_bf16 v[64:67], a[0:3], v[72:75], 0\nv_mfma_f32_16x16x32_bf16 v[20:23], a[96:99], v[72:75], 0\n" \
"v_mfma_f32_16x16x32_bf16 v[24:27], a[192:195], v[72:75], 0\nv_mfma_f32_16x16x32_bf16 v[28:31], v[144:147], v[72:75], 0\nv_mfma_f32_16x16x32_bf16 v[44:47], a[4:7], v[76:79], v[44:47]\nv_mfma_f32_16x16x32_bf16 v[48:51], a[100:103], v[76:79], v[48:51]\n" \
"v_mfma_f32_16x16x32_bf16 v[56:59], a[196:199], v[76:79], v[56:59]\nv_mfma_f32_16x16x32_bf16 v[60:63], v[148:151], v[76:79], v[60:63]\nv_mfma_f32_16x16x32_bf16 v[64:67], a[4:7], v[80:83], v[64:67]\nv_mfma_f32_16x16x32_bf16 v[20:23], a[100:103], v[80:83], v[20:23]\n" \
"v_mfma_f32_16x16x32_bf16 v[24:27], a[196:199], v[80:83], v[24:27]\nv_mfma_f32_16x16x32_bf16 v[28:31], v[148:151], v[80:83], v[28:31]\ns_branch 1b\n9:\n" \
"s_waitcnt vmcnt(0)\ns_endpgm\n"
#define G_LOOP \
"s_branch 4f\n.p2align 6\n1:\ns_waitcnt lgkmcnt(2)\n" \
"v_mfma_f32_16x16x32_bf16 v[8:11], a[104:107], v[36:39], v[8:11]\ns_lshr_b32 s99, s20, 1\ns_and_b32 s98, s20, 1\nv_mov_b32 v110, s20\n" \
"v_mfma_f32_16x16x32_bf16 v[12:15], a[104:107], v[40:43], v[12:15]\ns_add_u32 s20, s20, 1\ns_add_u32 s97, s20, 1\ns_lshr_b32 s96, s97, 1\n" \
"v_mfma_f32_16x16x32_bf16 v[16:19], a[232:235], v[36:39], v[16:19]\ns_and_b32 s97, s97, 1\ns_lshl_b32 s97, s97, 3\nv_readlane_b32 s96, v6, s96\n" \
"v_mfma_f32_16x16x32_bf16 v[20:23], a[232:235], v[40:43], v[20:23]\nds_read_b128 v[52:55], v7 offset:28672\nv_xor_b32 v4, 0x400, v4\nv_mfma_f32_16x16x32_bf16 v[24:27], v[216:219], v[36:39], v[24:27]\n" \
"ds_read_b128 v[56:59], v7 offset:61440\ns_lshr_b32 s96, s96, s97\nv_mfma_f32_16x16x32_bf16 v[28:31], v[216:219], v[40:43], v[28:31]\ns_waitcnt lgkmcnt(2)\n" \
"s_and_b32 s96, s96, 0xff\ns_lshl_b32 s97, s96, 13\nv_mfma_f32_16x16x32_bf16 v[8:11], a[108:111], v[44:47], v[8:11]\ns_lshl_b32 s98, s98, 3\n" \
"v_mov_b32 v32, v111\nv_mov_b32 v33, v111\nv_mfma_f32_16x16x32_bf16 v[12:15], a[108:111], v[48:51], v[12:15]\ns_lshl_b32 s96, s96, 16\n" \
"v_mul_f32 v252, 0xc0135761, v34\nv_mul_f32 v34, v34, v34\nv_mfma_f32_16x16x32_bf16 v[16:19], a[236:239], v[44:47], v[16:19]\ns_add_u32 s96, s96, s21\n" \
"v_permlane16_swap_b32 v32, v33\nv_mfma_f32_16x16x32_bf16 v[20:23], a[236:239], v[48:51], v[20:23]\nds_read_b128 v[36:39], v7 offset:29696\nv_readlane_b32 s99, v6, s99\n" \
"v_mfma_f32_16x16x32_bf16 v[24:27], v[220:223], v[44:47], v[24:27]\nds_read_b128 v[40:43], v7 offset:62464\nv_mul_f32 v34, v34, v252\nv_mfma_f32_16x16x32_bf16 v[28:31], v[220:223], v[48:51], v[28:31]\n" \
"s_waitcnt lgkmcnt(2)\nds_read_b128 v[44:47], v7 offset:30720\nv_mfma_f32_16x16x32_bf16 v[8:11], a[112:115], v[52:55], v[8:11]\nds_read_b128 v[48:51], v7 offset:63488\n" \
"v_mul_f32 v254, v35, v35\nv_mfma_f32_16x16x32_bf16 v[12:15], a[112:115], v[56:59], v[12:15]\ns_lshr_b32 s99, s99, s98\ns_and_b32 s99, s99, 0xff\n" \
"v_mul_f32 v253, 0x3d372713, v34\nv_mfma_f32_16x16x32_bf16 v[16:19], a[240:243], v[52:55], v[16:19]\nds_read_b128 v[60:63], v7 offset:31744\ns_mul_i32 s99, s99, 0x38000\n" \
"v_mfma_f32_16x16x32_bf16 v[20:23], a[240:243], v[56:59], v[20:23]\ns_or_b32 s16, s99, s18\ns_or_b32 s17, s99, s19\nv_mfma_f32_16x16x32_bf16 v[24:27], v[224:227], v[52:55], v[24:27]\n" \
"ds_read_b128 v[64:67], v7 offset:64512\nv_xor_b32 v7, 0x10000, v7\nv_mfma_f32_16x16x32_bf16 v[28:31], v[224:227], v[56:59], v[28:31]\ns_waitcnt lgkmcnt(4)\n" \
"buffer_load_dwordx4 v[52:55], v1, s[8:11], s97 offen offset:0 sc1\nv_mfma_f32_16x16x32_bf16 v[8:11], a[116:119], v[36:39], v[8:11]\nbuffer_load_dwordx4 v[56:59], v1, s[8:11], s97 offen offset:1024 sc1\nv_mfma_f32_16x16x32_bf16 v[12:15], a[116:119], v[40:43], v[12:15]\n" \
"v_mfma_f32_16x16x32_bf16 v[16:19], a[244:247], v[36:39], v[16:19]\ns_waitcnt vmcnt(5) lgkmcnt(0)\ns_barrier\nv_mfma_f32_16x16x32_bf16 v[20:23], a[244:247], v[40:43], v[20:23]\n" \
"ds_read_b128 v[68:71], v4 offset:0\nv_mfma_f32_16x16x32_bf16 v[24:27], v[228:231], v[36:39], v[24:27]\nds_read_b128 v[72:75], v7 offset:0\nv_mfma_f32_16x16x32_bf16 v[28:31], v[228:231], v[40:43], v[28:31]\n" \
"ds_read_b128 v[36:39], v7 offset:32768\nv_mfma_f32_16x16x32_bf16 v[8:11], a[120:123], v[44:47], v[8:11]\nds_read_b128 v[40:43], v7 offset:1024\nv_mfma_f32_16x16x32_bf16 v[12:15], a[120:123], v[48:51], v[12:15]\n" \
"s_waitcnt lgkmcnt(3)\nv_add_f32 v68, v68, v69\nv_mfma_f32_16x16x32_bf16 v[8:11], a[124:127], v[60:63], v[8:11]\nds_read_b128 v[76:79], v7 offset:33792\n" \
"v_add_f32 v70, v70, v71\nv_mfma_f32_16x16x32_bf16 v[12:15], a[124:127], v[64:67], v[12:15]\nv_cmp_u_f32 s[98:99], v68, v70\nv_mfma_f32_16x16x32_bf16 v[16:19], a[248:251], v[44:47], v[16:19]\n" \
"s_waitcnt vmcnt(2)\nbuffer_store_dword v110, v3, s[8:11], s23 offen offset:0 sc1\nv_mfma_f32_16x16x32_bf16 v[20:23], a[248:251], v[48:51], v[20:23]\ns_mov_b32 s23, 0\n" \
"v_fma_f32 v68, v32, v240, v8\nv_fma_f32 v69, v32, v241, v9\nv_mfma_f32_16x16x32_bf16 v[16:19], a[252:255], v[60:63], v[16:19]\nv_fma_f32 v70, v32, v242, v10\n" \
"v_fma_f32 v71, v32, v243, v11\nv_mfma_f32_16x16x32_bf16 v[20:23], a[252:255], v[64:67], v[20:23]\ns_cmp_lt_u32 s20, 0x80\ns_cselect_b32 s98, s98, -1\n" \
"v_fma_f32 v80, v33, v240, v12\nv_mfma_f32_16x16x32_bf16 v[24:27], v[232:235], v[44:47], v[24:27]\nv_fma_f32 v81, v33, v241, v13\nv_fma_f32 v82, v33, v242, v14\n" \
"v_mfma_f32_16x16x32_bf16 v[28:31], v[232:235], v[48:51], v[28:31]\nv_fma_f32 v83, v33, v243, v15\nv_fma_f32 v44, v32, v244, v16\nv_mfma_f32_16x16x32_bf16 v[24:27], v[236:239], v[60:63], v[24:27]\n" \
"s_waitcnt lgkmcnt(0)\nv_fma_f32 v45, v32, v245, v17\nv_fma_f32 v46, v32, v246, v18\nv_mfma_f32_16x16x32_bf16 v[28:31], v[236:239], v[64:67], v[28:31]\n" \
"s_cmp_lg_u64 s[98:99], 0\ns_cbranch_scc1 4f\n5:\nv_mfma_f32_16x16x32_bf16 v[8:11], a[0:3], v[72:75], 0\n" \
"s_xor_b32 m0, s22, 0\nv_fma_f32 v47, v32, v247, v19\nv_fma_f32 v48, v33, v244, v20\nv_mfma_f32_16x16x32_bf16 v[12:15], a[0:3], v[36:39], 0\n" \
"v_fma_f32 v49, v33, v245, v21\nv_fma_f32 v50, v33, v246, v22\nv_mfma_f32_16x16x32_bf16 v[8:11], a[4:7], v[40:43], v[8:11]\nv_fma_f32 v51, v33, v247, v23\n" \
"v_fma_f32 v60, v32, v248, v24\nv_mfma_f32_16x16x32_bf16 v[12:15], a[4:7], v[76:79], v[12:15]\nds_read_b128 v[64:67], v7 offset:2048\nv_fma_f32 v61, v32, v249, v25\n" \
"v_mfma_f32_16x16x32_bf16 v[16:19], a[128:131], v[72:75], 0\nds_read_b128 v[84:87], v7 offset:34816\nv_fma_f32 v62, v32, v250, v26\nv_mfma_f32_16x16x32_bf16 v[20:23], a[128:131], v[36:39], 0\n" \
"v_fma_f32 v63, v32, v251, v27\nv_fma_f32 v88, v33, v248, v28\nv_mfma_f32_16x16x32_bf16 v[16:19], a[132:135], v[40:43], v[16:19]\nv_fma_f32 v89, v33, v249, v29\n" \
"v_fma_f32 v90, v33, v250, v30\nv_mfma_f32_16x16x32_bf16 v[20:23], a[132:135], v[76:79], v[20:23]\nv_fma_f32 v91, v33, v251, v31\nv_xor_b32 v5, 0x400, v5\n" \
"v_mfma_f32_16x16x32_bf16 v[24:27], v[112:115], v[72:75], 0\nv_permlane16_swap_b32 v68, v80\nv_mfma_f32_16x16x32_bf16 v[28:31], v[112:115], v[36:39], 0\nds_read_b128 v[72:75], v7 offset:3072\n" \
"v_mul_f32 v36, v68, v68\nv_mfma_f32_16x16x32_bf16 v[24:27], v[116:119], v[40:43], v[24:27]\nds_read_b128 v[92:95], v7 offset:35840\nv_fma_f32 v36, v36, v253, v252\n" \
"v_mfma_f32_16x16x32_bf16 v[28:31], v[116:119], v[76:79], v[28:31]\ns_waitcnt lgkmcnt(2)\nv_permlane16_swap_b32 v69, v81\nv_mfma_f32_16x16x32_bf16 v[8:11], a[8:11], v[64:67], v[8:11]\n" \
"v_permlane16_swap_b32 v70, v82\nv_mfma_f32_16x16x32_bf16 v[12:15], a[8:11], v[84:87], v[12:15]\nv_permlane16_swap_b32 v71, v83\nv_mfma_f32_16x16x32_bf16 v[16:19], a[136:139], v[64:67], v[16:19]\n" \
"v_mul_f32 v37, v69, v69\nv_mul_f32 v38, v70, v70\nv_mfma_f32_16x16x32_bf16 v[20:23], a[136:139], v[84:87], v[20:23]\nds_read_b128 v[40:43], v7 offset:4096\n" \
"v_mul_f32 v39, v71, v71\nv_mfma_f32_16x16x32_bf16 v[24:27], v[120:123], v[64:67], v[24:27]\nds_read_b128 v[76:79], v7 offset:36864\nv_fma_f32 v37, v37, v253, v252\n" \
"v_mfma_f32_16x16x32_bf16 v[28:31], v[120:123], v[84:87], v[28:31]\ns_waitcnt lgkmcnt(2)\nv_fma_f32 v38, v38, v253, v252\nv_fma_f32 v39, v39, v253, v252\n" \
"v_mfma_f32_16x16x32_bf16 v[8:11], a[12:15], v[72:75], v[8:11]\nv_mul_f32 v36, v36, v68\nv_mul_f32 v37, v37, v69\nv_mfma_f32_16x16x32_bf16 v[12:15], a[12:15], v[92:95], v[12:15]\n" \
"v_mul_f32 v38, v38, v70\nv_mul_f32 v39, v39, v71\nv_mfma_f32_16x16x32_bf16 v[16:19], a[140:143], v[72:75], v[16:19]\nv_exp_f32 v36, v36\n" \
"v_mfma_f32_16x16x32_bf16 v[20:23], a[140:143], v[92:95], v[20:23]\nds_read_b128 v[64:67], v7 offset:5120\nv_fma_f32 v36, v36, v254, v254\nv_mfma_f32_16x16x32_bf16 v[24:27], v[124:127], v[72:75], v[24:27]\n" \
"ds_read_b128 v[84:87], v7 offset:37888\nv_mul_f32 v80, v68, v80\nv_mfma_f32_16x16x32_bf16 v[28:31], v[124:127], v[92:95], v[28:31]\ns_waitcnt lgkmcnt(2)\n" \
"v_exp_f32 v37, v37\nv_mfma_f32_16x16x32_bf16 v[8:11], a[16:19], v[40:43], v[8:11]\nv_exp_f32 v38, v38\nv_mfma_f32_16x16x32_bf16 v[12:15], a[16:19], v[76:79], v[12:15]\n" \
"v_exp_f32 v39, v39\nv_mfma_f32_16x16x32_bf16 v[16:19], a[144:147], v[40:43], v[16:19]\nv_fma_f32 v37, v37, v254, v254\nv_fma_f32 v38, v38, v254, v254\n" \
"v_mfma_f32_16x16x32_bf16 v[20:23], a[144:147], v[76:79], v[20:23]\nds_read_b128 v[72:75], v7 offset:6144\nv_fma_f32 v39, v39, v254, v254\nv_mfma_f32_16x16x32_bf16 v[24:27], v[128:131], v[40:43], v[24:27]\n" \
"ds_read_b128 v[92:95], v7 offset:38912\nv_mul_f32 v81, v69, v81\nv_mfma_f32_16x16x32_bf16 v[28:31], v[128:131], v[76:79], v[28:31]\ns_waitcnt lgkmcnt(2)\n" \
"v_rcp_f32 v36, v36\nv_mfma_f32_16x16x32_bf16 v[8:11], a[20:23], v[64:67], v[8:11]\nv_rcp_f32 v37, v37\nv_mfma_f32_16x16x32_bf16 v[12:15], a[20:23], v[84:87], v[12:15]\n" \
"v_rcp_f32 v38, v38\nv_mfma_f32_16x16x32_bf16 v[16:19], a[148:151], v[64:67], v[16:19]\nv_rcp_f32 v39, v39\nv_mfma_f32_16x16x32_bf16 v[20:23], a[148:151], v[84:87], v[20:23]\n" \
"ds_read_b128 v[40:43], v7 offset:7168\nv_mul_f32 v82, v70, v82\nv_mfma_f32_16x16x32_bf16 v[24:27], v[132:135], v[64:67], v[24:27]\nds_read_b128 v[76:79], v7 offset:39936\n" \
"v_mul_f32 v83, v71, v83\nv_mfma_f32_16x16x32_bf16 v[28:31], v[132:135], v[84:87], v[28:31]\ns_waitcnt vmcnt(1) lgkmcnt(2)\nv_max3_u32 v35, v52, v54, v56\n" \
"v_max_u32 v35, v35, v58\nv_mfma_f32_16x16x32_bf16 v[8:11], a[24:27], v[72:75], v[8:11]\nv_cmp_eq_u32 s[98:99], -1, v35\nv_mul_f32 v80, v80, v36\n" \
"v_mfma_f32_16x16x32_bf16 v[12:15], a[24:27], v[92:95], v[12:15]\nv_mul_f32 v81, v81, v37\nv_mul_f32 v82, v82, v38\nv_mfma_f32_16x16x32_bf16 v[16:19], a[152:155], v[72:75], v[16:19]\n" \
"s_cmp_eq_u64 s[98:99], 0\ns_cselect_b32 s14, s10, 0\nv_mul_f32 v83, v83, v39\nv_mfma_f32_16x16x32_bf16 v[20:23], a[152:155], v[92:95], v[20:23]\n" \
"ds_read_b128 v[36:39], v7 offset:8192\nv_cvt_pk_bf16_f32 v80, v80, v81\nv_mfma_f32_16x16x32_bf16 v[24:27], v[136:139], v[72:75], v[24:27]\nds_read_b128 v[64:67], v7 offset:40960\n" \
"v_cvt_pk_bf16_f32 v81, v82, v83\nv_mfma_f32_16x16x32_bf16 v[28:31], v[136:139], v[92:95], v[28:31]\ns_waitcnt lgkmcnt(2)\nbuffer_load_dwordx4 v0, s[12:15], s96 offen offset:0 sc1 lds\n" \
"v_mfma_f32_16x16x32_bf16 v[8:11], a[28:31], v[40:43], v[8:11]\nv_permlane16_swap_b32 v44, v48\nv_mfma_f32_16x16x32_bf16 v[12:15], a[28:31], v[76:79], v[12:15]\nv_permlane16_swap_b32 v45, v49\n" \
"v_mfma_f32_16x16x32_bf16 v[16:19], a[156:159], v[40:43], v[16:19]\nv_permlane16_swap_b32 v46, v50\nv_mfma_f32_16x16x32_bf16 v[20:23], a[156:159], v[76:79], v[20:23]\nds_read_b128 v[68:71], v7 offset:9216\n" \
"v_mul_f32 v72, v44, v44\nv_mfma_f32_16x16x32_bf16 v[24:27], v[140:143], v[40:43], v[24:27]\ns_waitcnt lgkmcnt(1)\nbuffer_load_dwordx4 v0, s[12:15], s96 offen offset:1024 sc1 lds\n" \
"v_mfma_f32_16x16x32_bf16 v[28:31], v[140:143], v[76:79], v[28:31]\nds_read_b128 v[40:43], v7 offset:41984\nv_mul_f32 v73, v45, v45\nv_mfma_f32_16x16x32_bf16 v[8:11], a[32:35], v[36:39], v[8:11]\n" \
"v_permlane16_swap_b32 v47, v51\nv_mfma_f32_16x16x32_bf16 v[12:15], a[32:35], v[64:67], v[12:15]\nv_mul_f32 v74, v46, v46\nv_mul_f32 v75, v47, v47\n" \
"v_mfma_f32_16x16x32_bf16 v[16:19], a[160:163], v[36:39], v[16:19]\nv_fma_f32 v72, v72, v253, v252\nv_fma_f32 v73, v73, v253, v252\nv_mfma_f32_16x16x32_bf16 v[20:23], a[160:163], v[64:67], v[20:23]\n" \
"buffer_load_dwordx4 v0, s[12:15], s96 offen offset:2048 sc1 lds\nv_mfma_f32_16x16x32_bf16 v[24:27], v[144:147], v[36:39], v[24:27]\nds_read_b128 v[76:79], v7 offset:10240\nv_fma_f32 v74, v74, v253, v252\n" \
"v_mfma_f32_16x16x32_bf16 v[28:31], v[144:147], v[64:67], v[28:31]\ns_waitcnt lgkmcnt(1)\nds_read_b128 v[36:39], v7 offset:43008\nv_mfma_f32_16x16x32_bf16 v[8:11], a[36:39], v[68:71], v[8:11]\n" \
"v_fma_f32 v75, v75, v253, v252\nv_mul_f32 v72, v72, v44\nv_mfma_f32_16x16x32_bf16 v[12:15], a[36:39], v[40:43], v[12:15]\nv_mul_f32 v73, v73, v45\n" \
"v_mul_f32 v74, v74, v46\nv_mfma_f32_16x16x32_bf16 v[16:19], a[164:167], v[68:71], v[16:19]\nbuffer_load_dwordx4 v0, s[12:15], s96 offen offset:3072 sc1 lds\nv_mfma_f32_16x16x32_bf16 v[20:23], a[164:167], v[40:43], v[20:23]\n" \
"ds_read_b128 v[64:67], v7 offset:11264\ns_xor_b32 m0, s22, 0x1000\nv_mfma_f32_16x16x32_bf16 v[24:27], v[148:151], v[68:71], v[24:27]\nds_read_b128 v[84:87], v7 offset:44032\n" \
"s_xor_b32 s96, s96, 0x1000\nv_mfma_f32_16x16x32_bf16 v[28:31], v[148:151], v[40:43], v[28:31]\ns_waitcnt lgkmcnt(3)\nv_mul_f32 v75, v75, v47\n" \
"v_mul_f32 v48, v44, v48\nv_mfma_f32_16x16x32_bf16 v[8:11], a[40:43], v[76:79], v[8:11]\ns_waitcnt lgkmcnt(2)\nv_exp_f32 v72, v72\n" \
"v_mfma_f32_16x16x32_bf16 v[12:15], a[40:43], v[36:39], v[12:15]\nbuffer_load_dwordx4 v0, s[12:15], s96 offen offset:0 sc1 lds\nv_mfma_f32_16x16x32_bf16 v[16:19], a[168:171], v[76:79], v[16:19]\nv_exp_f32 v73, v73\n" \
"v_mfma_f32_16x16x32_bf16 v[20:23], a[168:171], v[36:39], v[20:23]\nds_read_b128 v[40:43], v7 offset:12288\nv_fma_f32 v72, v72, v254, v254\nv_mfma_f32_16x16x32_bf16 v[24:27], v[152:155], v[76:79], v[24:27]\n" \
"ds_read_b128 v[68:71], v7 offset:45056\nv_fma_f32 v73, v73, v254, v254\nv_mfma_f32_16x16x32_bf16 v[28:31], v[152:155], v[36:39], v[28:31]\ns_waitcnt lgkmcnt(2)\n" \
"v_exp_f32 v74, v74\nv_mfma_f32_16x16x32_bf16 v[8:11], a[44:47], v[64:67], v[8:11]\nbuffer_load_dwordx4 v0, s[12:15], s96 offen offset:1024 sc1 lds\nv_mfma_f32_16x16x32_bf16 v[12:15], a[44:47], v[84:87], v[12:15]\n" \
"v_exp_f32 v75, v75\nv_mfma_f32_16x16x32_bf16 v[16:19], a[172:175], v[64:67], v[16:19]\nv_fma_f32 v74, v74, v254, v254\nv_fma_f32 v75, v75, v254, v254\n" \
"v_mfma_f32_16x16x32_bf16 v[20:23], a[172:175], v[84:87], v[20:23]\nds_read_b128 v[36:39], v7 offset:13312\nv_mul_f32 v49, v45, v49\nv_mfma_f32_16x16x32_bf16 v[24:27], v[156:159], v[64:67], v[24:27]\n" \
"ds_read_b128 v[76:79], v7 offset:46080\nv_mul_f32 v50, v46, v50\nv_mfma_f32_16x16x32_bf16 v[28:31], v[156:159], v[84:87], v[28:31]\ns_waitcnt lgkmcnt(2)\n" \
"buffer_load_dwordx4 v0, s[12:15], s96 offen offset:2048 sc1 lds\nv_mfma_f32_16x16x32_bf16 v[8:11], a[48:51], v[40:43], v[8:11]\nv_rcp_f32 v72, v72\nv_mfma_f32_16x16x32_bf16 v[12:15], a[48:51], v[68:71], v[12:15]\n" \
"v_rcp_f32 v73, v73\nv_mfma_f32_16x16x32_bf16 v[16:19], a[176:179], v[40:43], v[16:19]\nv_rcp_f32 v74, v74\nv_mfma_f32_16x16x32_bf16 v[20:23], a[176:179], v[68:71], v[20:23]\n" \
"ds_read_b128 v[64:67], v7 offset:14336\nv_mul_f32 v51, v47, v51\nv_mfma_f32_16x16x32_bf16 v[24:27], v[160:163], v[40:43], v[24:27]\ns_waitcnt lgkmcnt(1)\n" \
"buffer_load_dwordx4 v0, s[12:15], s96 offen offset:3072 sc1 lds\nv_mfma_f32_16x16x32_bf16 v[28:31], v[160:163], v[68:71], v[28:31]\nds_read_b128 v[40:43], v7 offset:47104\ns_xor_b32 m0, s22, 0x8000\n" \
"v_mfma_f32_16x16x32_bf16 v[8:11], a[52:55], v[36:39], v[8:11]\ns_xor_b32 s96, s96, 0x9000\nv_rcp_f32 v75, v75\nv_mfma_f32_16x16x32_bf16 v[12:15], a[52:55], v[76:79], v[12:15]\n" \
"v_mul_f32 v48, v48, v72\nv_mul_f32 v49, v49, v73\nv_mfma_f32_16x16x32_bf16 v[16:19], a[180:183], v[36:39], v[16:19]\nv_mul_f32 v50, v50, v74\n" \
"v_mul_f32 v51, v51, v75\nv_mfma_f32_16x16x32_bf16 v[20:23], a[180:183], v[76:79], v[20:23]\nbuffer_load_dwordx4 v0, s[12:15], s96 offen offset:0 sc1 lds\nv_mfma_f32_16x16x32_bf16 v[24:27], v[164:167], v[36:39], v[24:27]\n" \
"ds_read_b128 v[44:47], v7 offset:15360\nv_cvt_pk_bf16_f32 v48, v48, v49\nv_mfma_f32_16x16x32_bf16 v[28:31], v[164:167], v[76:79], v[28:31]\ns_waitcnt lgkmcnt(1)\n" \
"ds_read_b128 v[36:39], v7 offset:48128\nv_mfma_f32_16x16x32_bf16 v[8:11], a[56:59], v[64:67], v[8:11]\nv_cvt_pk_bf16_f32 v49, v50, v51\nv_mfma_f32_16x16x32_bf16 v[12:15], a[56:59], v[40:43], v[12:15]\n" \
"v_permlane16_swap_b32 v60, v88\nv_mfma_f32_16x16x32_bf16 v[16:19], a[184:187], v[64:67], v[16:19]\nbuffer_load_dwordx4 v0, s[12:15], s96 offen offset:1024 sc1 lds\nv_mfma_f32_16x16x32_bf16 v[20:23], a[184:187], v[40:43], v[20:23]\n" \
"ds_read_b128 v[68:71], v7 offset:16384\nv_mul_f32 v72, v60, v60\nv_mfma_f32_16x16x32_bf16 v[24:27], v[168:171], v[64:67], v[24:27]\nds_read_b128 v[76:79], v7 offset:49152\n" \
"v_fma_f32 v72, v72, v253, v252\nv_mfma_f32_16x16x32_bf16 v[28:31], v[168:171], v[40:43], v[28:31]\ns_waitcnt lgkmcnt(3)\nv_permlane16_swap_b32 v61, v89\n" \
"v_mfma_f32_16x16x32_bf16 v[8:11], a[60:63], v[44:47], v[8:11]\ns_waitcnt lgkmcnt(2)\nv_permlane16_swap_b32 v62, v90\nv_mfma_f32_16x16x32_bf16 v[12:15], a[60:63], v[36:39], v[12:15]\n" \
"buffer_load_dwordx4 v0, s[12:15], s96 offen offset:2048 sc1 lds\nv_mfma_f32_16x16x32_bf16 v[16:19], a[188:191], v[44:47], v[16:19]\nv_permlane16_swap_b32 v63, v91\nv_mfma_f32_16x16x32_bf16 v[20:23], a[188:191], v[36:39], v[20:23]\n" \
"ds_read_b128 v[40:43], v7 offset:17408\nv_mul_f32 v73, v61, v61\nv_mfma_f32_16x16x32_bf16 v[24:27], v[172:175], v[44:47], v[24:27]\nds_read_b128 v[64:67], v7 offset:50176\n" \
"v_mul_f32 v74, v62, v62\nv_mfma_f32_16x16x32_bf16 v[28:31], v[172:175], v[36:39], v[28:31]\ns_waitcnt lgkmcnt(2)\nv_mul_f32 v75, v63, v63\n" \
"v_fma_f32 v73, v73, v253, v252\nv_mfma_f32_16x16x32_bf16 v[8:11], a[64:67], v[68:71], v[8:11]\nbuffer_load_dwordx4 v0, s[12:15], s96 offen offset:3072 sc1 lds\nv_mfma_f32_16x16x32_bf16 v[12:15], a[64:67], v[76:79], v[12:15]\n" \
"s_xor_b32 m0, s22, 0x9000\ns_xor_b32 s96, s96, 0x1000\nv_fma_f32 v74, v74, v253, v252\nv_mfma_f32_16x16x32_bf16 v[16:19], a[192:195], v[68:71], v[16:19]\n" \
"s_xor_b32 s22, s22, 0x10000\nv_fma_f32 v75, v75, v253, v252\nv_mul_f32 v72, v72, v60\nv_mfma_f32_16x16x32_bf16 v[20:23], a[192:195], v[76:79], v[20:23]\n" \
"ds_read_b128 v[36:39], v7 offset:18432\nv_mul_f32 v73, v73, v61\nv_mfma_f32_16x16x32_bf16 v[24:27], v[176:179], v[68:71], v[24:27]\nds_read_b128 v[44:47], v7 offset:51200\n" \
"v_mul_f32 v74, v74, v62\nv_mfma_f32_16x16x32_bf16 v[28:31], v[176:179], v[76:79], v[28:31]\ns_waitcnt lgkmcnt(2)\nbuffer_load_dwordx4 v0, s[12:15], s96 offen offset:0 sc1 lds\n" \
"v_mfma_f32_16x16x32_bf16 v[8:11], a[68:71], v[40:43], v[8:11]\nv_mul_f32 v75, v75, v63\nv_mul_f32 v88, v60, v88\nv_mfma_f32_16x16x32_bf16 v[12:15], a[68:71], v[64:67], v[12:15]\n" \
"v_exp_f32 v72, v72\nv_mfma_f32_16x16x32_bf16 v[16:19], a[196:199], v[40:43], v[16:19]\nv_exp_f32 v73, v73\nv_mfma_f32_16x16x32_bf16 v[20:23], a[196:199], v[64:67], v[20:23]\n" \
"ds_read_b128 v[68:71], v7 offset:19456\nv_fma_f32 v72, v72, v254, v254\nv_mfma_f32_16x16x32_bf16 v[24:27], v[180:183], v[40:43], v[24:27]\ns_waitcnt lgkmcnt(1)\n" \
"buffer_load_dwordx4 v0, s[12:15], s96 offen offset:1024 sc1 lds\nv_mfma_f32_16x16x32_bf16 v[28:31], v[180:183], v[64:67], v[28:31]\nds_read_b128 v[40:43], v7 offset:52224\nv_fma_f32 v73, v73, v254, v254\n" \
"v_mfma_f32_16x16x32_bf16 v[8:11], a[72:75], v[36:39], v[8:11]\nv_exp_f32 v74, v74\nv_mfma_f32_16x16x32_bf16 v[12:15], a[72:75], v[44:47], v[12:15]\nv_exp_f32 v75, v75\n" \
"v_mfma_f32_16x16x32_bf16 v[16:19], a[200:203], v[36:39], v[16:19]\nv_fma_f32 v74, v74, v254, v254\nv_fma_f32 v75, v75, v254, v254\nv_mfma_f32_16x16x32_bf16 v[20:23], a[200:203], v[44:47], v[20:23]\n" \
"buffer_load_dwordx4 v0, s[12:15], s96 offen offset:2048 sc1 lds\nv_mfma_f32_16x16x32_bf16 v[24:27], v[184:187], v[36:39], v[24:27]\nds_read_b128 v[64:67], v7 offset:20480\nv_mul_f32 v89, v61, v89\n" \
"v_mfma_f32_16x16x32_bf16 v[28:31], v[184:187], v[44:47], v[28:31]\ns_waitcnt lgkmcnt(1)\nds_read_b128 v[36:39], v7 offset:53248\nv_mfma_f32_16x16x32_bf16 v[8:11], a[76:79], v[68:71], v[8:11]\n" \
"v_rcp_f32 v72, v72\nv_mfma_f32_16x16x32_bf16 v[12:15], a[76:79], v[40:43], v[12:15]\nv_rcp_f32 v73, v73\nv_mfma_f32_16x16x32_bf16 v[16:19], a[204:207], v[68:71], v[16:19]\n" \
"buffer_load_dwordx4 v0, s[12:15], s96 offen offset:3072 sc1 lds\nv_mfma_f32_16x16x32_bf16 v[20:23], a[204:207], v[40:43], v[20:23]\nds_read_b128 v[44:47], v7 offset:21504\nv_mul_f32 v90, v62, v90\n" \
"v_mfma_f32_16x16x32_bf16 v[24:27], v[188:191], v[68:71], v[24:27]\ns_waitcnt lgkmcnt(2)\nbuffer_store_dwordx2 v[80:81], v2, s[8:11], s16 offen offset:0 sc1\nv_mul_f32 v91, v63, v91\n" \
"v_mfma_f32_16x16x32_bf16 v[28:31], v[188:191], v[40:43], v[28:31]\nds_read_b128 v[60:63], v7 offset:54272\nv_mul_f32 v88, v88, v72\nv_mfma_f32_16x16x32_bf16 v[8:11], a[80:83], v[64:67], v[8:11]\n" \
"s_waitcnt lgkmcnt(2)\nbuffer_store_dwordx2 v[48:49], v2, s[8:11], s17 offen offset:256 sc1\nv_mul_f32 v89, v89, v73\nv_mfma_f32_16x16x32_bf16 v[12:15], a[80:83], v[36:39], v[12:15]\n" \
"v_rcp_f32 v74, v74\nv_mfma_f32_16x16x32_bf16 v[16:19], a[208:211], v[64:67], v[16:19]\nv_rcp_f32 v75, v75\nv_mfma_f32_16x16x32_bf16 v[20:23], a[208:211], v[36:39], v[20:23]\n" \
"v_mul_f32 v90, v90, v74\nv_mul_f32 v91, v91, v75\nv_mfma_f32_16x16x32_bf16 v[24:27], v[192:195], v[64:67], v[24:27]\nds_read_b128 v[40:43], v7 offset:22528\n" \
"v_cvt_pk_bf16_f32 v88, v88, v89\nv_cvt_pk_bf16_f32 v89, v90, v91\nv_mfma_f32_16x16x32_bf16 v[28:31], v[192:195], v[36:39], v[28:31]\ns_waitcnt lgkmcnt(1)\n" \
"ds_read_b128 v[36:39], v7 offset:55296\nv_mfma_f32_16x16x32_bf16 v[8:11], a[84:87], v[44:47], v[8:11]\nbuffer_store_dwordx2 v[88:89], v2, s[8:11], s17 offen offset:512 sc1\nv_add_f32 v52, v52, v56\n" \
"v_mfma_f32_16x16x32_bf16 v[12:15], a[84:87], v[60:63], v[12:15]\nds_read_b128 v[48:51], v4 offset:0\nv_add_f32 v53, v53, v57\nv_mfma_f32_16x16x32_bf16 v[16:19], a[212:215], v[44:47], v[16:19]\n" \
"ds_read_b128 v[64:67], v4 offset:512\nv_add_f32 v54, v54, v58\nv_mfma_f32_16x16x32_bf16 v[20:23], a[212:215], v[60:63], v[20:23]\nds_read_b128 v[68:71], v7 offset:23552\n" \
"v_add_f32 v55, v55, v59\nv_mfma_f32_16x16x32_bf16 v[24:27], v[196:199], v[44:47], v[24:27]\ns_waitcnt lgkmcnt(4)\nds_read_b128 v[44:47], v7 offset:56320\n" \
"v_mfma_f32_16x16x32_bf16 v[28:31], v[196:199], v[60:63], v[28:31]\ns_waitcnt lgkmcnt(4)\nv_permlane16_swap_b32 v52, v54\nv_mfma_f32_16x16x32_bf16 v[8:11], a[88:91], v[40:43], v[8:11]\n" \
"v_permlane16_swap_b32 v53, v55\nv_mfma_f32_16x16x32_bf16 v[12:15], a[88:91], v[36:39], v[12:15]\nv_add_f32 v52, v52, v54\nv_add_f32 v53, v53, v55\n" \
"v_mfma_f32_16x16x32_bf16 v[16:19], a[216:219], v[40:43], v[16:19]\ns_waitcnt lgkmcnt(2)\nv_add_f32 v48, v48, v49\nv_add_f32 v50, v50, v51\n" \
"v_mfma_f32_16x16x32_bf16 v[20:23], a[216:219], v[36:39], v[20:23]\nds_read_b128 v[56:59], v7 offset:24576\nv_add_f32 v48, v48, v50\nv_mfma_f32_16x16x32_bf16 v[24:27], v[200:203], v[40:43], v[24:27]\n" \
"ds_read_b128 v[60:63], v7 offset:57344\nv_add_f32 v64, v64, v65\nv_mfma_f32_16x16x32_bf16 v[28:31], v[200:203], v[36:39], v[28:31]\ns_waitcnt lgkmcnt(2)\n" \
"v_add_f32 v66, v66, v67\nv_mul_f32 v48, 0xba800000, v48\nv_mfma_f32_16x16x32_bf16 v[8:11], a[92:95], v[68:71], v[8:11]\nv_add_f32 v64, v64, v66\n" \
"v_mul_f32 v65, v48, -v48\nv_mfma_f32_16x16x32_bf16 v[12:15], a[92:95], v[44:47], v[12:15]\nv_fmamk_f32 v35, v64, 0x3a800000, v65\nv_cvt_pk_bf16_f32 v35, v35, v35\n" \
"v_mfma_f32_16x16x32_bf16 v[16:19], a[220:223], v[68:71], v[16:19]\nv_lshlrev_b32 v35, 16, v35\nv_max_f32 v35, 0, v35\nv_mfma_f32_16x16x32_bf16 v[20:23], a[220:223], v[44:47], v[20:23]\n" \
"ds_read_b128 v[64:67], v7 offset:25600\nv_add_f32 v35, 0x3727c5ac, v35\nv_mfma_f32_16x16x32_bf16 v[24:27], v[204:207], v[68:71], v[24:27]\nds_read_b128 v[72:75], v7 offset:58368\n" \
"v_cvt_pk_bf16_f32 v111, v35, v48\nv_mfma_f32_16x16x32_bf16 v[28:31], v[204:207], v[44:47], v[28:31]\ns_waitcnt lgkmcnt(2)\nv_lshlrev_b32 v35, 16, v111\n" \
"v_and_b32 v111, 0xffff0000, v111\nv_mfma_f32_16x16x32_bf16 v[8:11], a[96:99], v[56:59], v[8:11]\nv_sqrt_f32 v35, v35\nv_mfma_f32_16x16x32_bf16 v[12:15], a[96:99], v[60:63], v[12:15]\n" \
"v_permlane32_swap_b32 v52, v53\nv_mfma_f32_16x16x32_bf16 v[16:19], a[224:227], v[56:59], v[16:19]\nv_cvt_pk_bf16_f32 v35, v35, v35\nv_add_f32 v52, v52, v53\n" \
"v_mfma_f32_16x16x32_bf16 v[20:23], a[224:227], v[60:63], v[20:23]\nds_read_b128 v[36:39], v7 offset:26624\nv_lshlrev_b32 v35, 16, v35\nv_mfma_f32_16x16x32_bf16 v[24:27], v[208:211], v[56:59], v[24:27]\n" \
"s_waitcnt lgkmcnt(1)\nds_read_b128 v[40:43], v7 offset:59392\nv_mfma_f32_16x16x32_bf16 v[28:31], v[208:211], v[60:63], v[28:31]\nds_write_b32 v5, v52 offset:0\n" \
"v_mfma_f32_16x16x32_bf16 v[8:11], a[100:103], v[64:67], v[8:11]\nv_rcp_f32 v34, v35\nv_mfma_f32_16x16x32_bf16 v[12:15], a[100:103], v[72:75], v[12:15]\nv_mfma_f32_16x16x32_bf16 v[16:19], a[228:231], v[64:67], v[16:19]\n" \
"v_mfma_f32_16x16x32_bf16 v[20:23], a[228:231], v[72:75], v[20:23]\nds_read_b128 v[44:47], v7 offset:27648\nv_mfma_f32_16x16x32_bf16 v[24:27], v[212:215], v[64:67], v[24:27]\nds_read_b128 v[48:51], v7 offset:60416\n" \
"v_mfma_f32_16x16x32_bf16 v[28:31], v[212:215], v[72:75], v[28:31]\ns_branch 1b\n4:\ns_barrier\n" \
"s_lshr_b32 s94, s20, 1\nv_readlane_b32 s94, v6, s94\ns_and_b32 s95, s20, 1\ns_lshl_b32 s95, s95, 3\n" \
"s_lshr_b32 s94, s94, s95\ns_and_b32 s94, s94, 0xff\ns_lshl_b32 s95, s94, 13\nbuffer_load_dwordx4 v[96:99], v1, s[8:11], s95 offen offset:0 sc1\n" \
"buffer_load_dwordx4 v[100:103], v1, s[8:11], s95 offen offset:1024 sc1\nv_fma_f32 v84, v32, v240, v8\nv_fma_f32 v85, v32, v241, v9\nv_fma_f32 v86, v32, v242, v10\n" \
"v_fma_f32 v87, v32, v243, v11\nv_fma_f32 v88, v33, v240, v12\nv_fma_f32 v89, v33, v241, v13\nv_fma_f32 v90, v33, v242, v14\n" \
"v_fma_f32 v91, v33, v243, v15\nv_permlane16_swap_b32 v84, v88\nv_permlane16_swap_b32 v85, v89\nv_permlane16_swap_b32 v86, v90\n" \
"v_permlane16_swap_b32 v87, v91\nv_mul_f32 v92, v84, v84\nv_mul_f32 v93, v85, v85\nv_mul_f32 v94, v86, v86\n" \
"v_mul_f32 v95, v87, v87\nv_fma_f32 v92, v92, v253, v252\nv_fma_f32 v93, v93, v253, v252\nv_fma_f32 v94, v94, v253, v252\n" \
"v_fma_f32 v95, v95, v253, v252\nv_mul_f32 v92, v92, v84\nv_mul_f32 v93, v93, v85\nv_mul_f32 v94, v94, v86\n" \
"v_mul_f32 v95, v95, v87\nv_mul_f32 v88, v84, v88\nv_mul_f32 v89, v85, v89\nv_mul_f32 v90, v86, v90\n" \
"v_mul_f32 v91, v87, v91\nv_exp_f32 v92, v92\nv_exp_f32 v93, v93\nv_exp_f32 v94, v94\n" \
"v_exp_f32 v95, v95\nv_fma_f32 v92, v92, v254, v254\nv_fma_f32 v93, v93, v254, v254\nv_fma_f32 v94, v94, v254, v254\n" \
"v_fma_f32 v95, v95, v254, v254\nv_rcp_f32 v92, v92\nv_rcp_f32 v93, v93\nv_rcp_f32 v94, v94\n" \
"v_rcp_f32 v95, v95\nv_mul_f32 v88, v88, v92\nv_mul_f32 v89, v89, v93\nv_mul_f32 v90, v90, v94\n" \
"v_mul_f32 v91, v91, v95\nv_cvt_pk_bf16_f32 v88, v88, v89\nv_cvt_pk_bf16_f32 v89, v90, v91\nbuffer_store_dwordx2 v[88:89], v2, s[8:11], s16 offen offset:0 sc1\n" \
"v_fma_f32 v84, v32, v244, v16\nv_fma_f32 v85, v32, v245, v17\nv_fma_f32 v86, v32, v246, v18\nv_fma_f32 v87, v32, v247, v19\n" \
"v_fma_f32 v88, v33, v244, v20\nv_fma_f32 v89, v33, v245, v21\nv_fma_f32 v90, v33, v246, v22\nv_fma_f32 v91, v33, v247, v23\n" \
"v_permlane16_swap_b32 v84, v88\nv_permlane16_swap_b32 v85, v89\nv_permlane16_swap_b32 v86, v90\nv_permlane16_swap_b32 v87, v91\n" \
"v_mul_f32 v92, v84, v84\nv_mul_f32 v93, v85, v85\nv_mul_f32 v94, v86, v86\nv_mul_f32 v95, v87, v87\n" \
"v_fma_f32 v92, v92, v253, v252\nv_fma_f32 v93, v93, v253, v252\nv_fma_f32 v94, v94, v253, v252\nv_fma_f32 v95, v95, v253, v252\n" \
"v_mul_f32 v92, v92, v84\nv_mul_f32 v93, v93, v85\nv_mul_f32 v94, v94, v86\nv_mul_f32 v95, v95, v87\n" \
"v_mul_f32 v88, v84, v88\nv_mul_f32 v89, v85, v89\nv_mul_f32 v90, v86, v90\nv_mul_f32 v91, v87, v91\n" \
"v_exp_f32 v92, v92\nv_exp_f32 v93, v93\nv_exp_f32 v94, v94\nv_exp_f32 v95, v95\n" \
"v_fma_f32 v92, v92, v254, v254\nv_fma_f32 v93, v93, v254, v254\nv_fma_f32 v94, v94, v254, v254\nv_fma_f32 v95, v95, v254, v254\n" \
"v_rcp_f32 v92, v92\nv_rcp_f32 v93, v93\nv_rcp_f32 v94, v94\nv_rcp_f32 v95, v95\n" \
"v_mul_f32 v88, v88, v92\nv_mul_f32 v89, v89, v93\nv_mul_f32 v90, v90, v94\nv_mul_f32 v91, v91, v95\n" \
"v_cvt_pk_bf16_f32 v88, v88, v89\nv_cvt_pk_bf16_f32 v89, v90, v91\nbuffer_store_dwordx2 v[88:89], v2, s[8:11], s17 offen offset:256 sc1\nv_fma_f32 v84, v32, v248, v24\n" \
"v_fma_f32 v85, v32, v249, v25\nv_fma_f32 v86, v32, v250, v26\nv_fma_f32 v87, v32, v251, v27\nv_fma_f32 v88, v33, v248, v28\n" \
"v_fma_f32 v89, v33, v249, v29\nv_fma_f32 v90, v33, v250, v30\nv_fma_f32 v91, v33, v251, v31\nv_permlane16_swap_b32 v84, v88\n" \
"v_permlane16_swap_b32 v85, v89\nv_permlane16_swap_b32 v86, v90\nv_permlane16_swap_b32 v87, v91\nv_mul_f32 v92, v84, v84\n" \
"v_mul_f32 v93, v85, v85\nv_mul_f32 v94, v86, v86\nv_mul_f32 v95, v87, v87\nv_fma_f32 v92, v92, v253, v252\n" \
"v_fma_f32 v93, v93, v253, v252\nv_fma_f32 v94, v94, v253, v252\nv_fma_f32 v95, v95, v253, v252\nv_mul_f32 v92, v92, v84\n" \
"v_mul_f32 v93, v93, v85\nv_mul_f32 v94, v94, v86\nv_mul_f32 v95, v95, v87\nv_mul_f32 v88, v84, v88\n" \
"v_mul_f32 v89, v85, v89\nv_mul_f32 v90, v86, v90\nv_mul_f32 v91, v87, v91\nv_exp_f32 v92, v92\n" \
"v_exp_f32 v93, v93\nv_exp_f32 v94, v94\nv_exp_f32 v95, v95\nv_fma_f32 v92, v92, v254, v254\n" \
"v_fma_f32 v93, v93, v254, v254\nv_fma_f32 v94, v94, v254, v254\nv_fma_f32 v95, v95, v254, v254\nv_rcp_f32 v92, v92\n" \
"v_rcp_f32 v93, v93\nv_rcp_f32 v94, v94\nv_rcp_f32 v95, v95\nv_mul_f32 v88, v88, v92\n" \
"v_mul_f32 v89, v89, v93\nv_mul_f32 v90, v90, v94\nv_mul_f32 v91, v91, v95\nv_cvt_pk_bf16_f32 v88, v88, v89\n" \
"v_cvt_pk_bf16_f32 v89, v90, v91\nbuffer_store_dwordx2 v[88:89], v2, s[8:11], s17 offen offset:512 sc1\ns_mov_b32 s16, 0x80000000\ns_mov_b32 s17, 0x80000000\n" \
"v_mov_b32 v84, s20\ns_cmp_ge_u32 s20, 0x80\ns_cbranch_scc1 6f\ns_waitcnt vmcnt(3)\n" \
"v_max3_u32 v85, v96, v98, v100\nv_max_u32 v85, v85, v102\nv_cmp_eq_u32 vcc, -1, v85\ns_cbranch_vccz 8f\n" \
"6:\ns_waitcnt vmcnt(0)\nbuffer_store_dword v84, v3, s[8:11], 0 offen offset:0 sc1\ns_mov_b32 s23, 0x40000000\n" \
"s_cbranch_scc1 9f\n7:\ns_add_u32 s24, s24, 1\ns_cmp_lt_u32 s24, 0x40000\n" \
"s_cbranch_scc0 9f\nbuffer_load_dwordx4 v[96:99], v1, s[8:11], s95 offen offset:0 sc1\nbuffer_load_dwordx4 v[100:103], v1, s[8:11], s95 offen offset:1024 sc1\ns_waitcnt vmcnt(0)\n" \
"v_max3_u32 v85, v96, v98, v100\nv_max_u32 v85, v85, v102\nv_cmp_eq_u32 vcc, -1, v85\ns_cbranch_vccz 8f\n" \
"s_sleep 8\ns_branch 7b\n8:\ns_lshl_b32 s94, s94, 16\n" \
"s_add_u32 s94, s94, s21\ns_xor_b32 s95, s22, 0x10000\ns_xor_b32 m0, s95, 0\ns_xor_b32 s94, s94, 0\n" \
"buffer_load_dwordx4 v0, s[8:11], s94 offen offset:0 sc1 lds\nbuffer_load_dwordx4 v0, s[8:11], s94 offen offset:1024 sc1 lds\nbuffer_load_dwordx4 v0, s[8:11], s94 offen offset:2048 sc1 lds\nbuffer_load_dwordx4 v0, s[8:11], s94 offen offset:3072 sc1 lds\n" \
"s_xor_b32 m0, s95, 0x1000\ns_xor_b32 s94, s94, 0x1000\nbuffer_load_dwordx4 v0, s[8:11], s94 offen offset:0 sc1 lds\nbuffer_load_dwordx4 v0, s[8:11], s94 offen offset:1024 sc1 lds\n" \
"buffer_load_dwordx4 v0, s[8:11], s94 offen offset:2048 sc1 lds\nbuffer_load_dwordx4 v0, s[8:11], s94 offen offset:3072 sc1 lds\ns_xor_b32 m0, s95, 0x8000\ns_xor_b32 s94, s94, 0x9000\n" \
"buffer_load_dwordx4 v0, s[8:11], s94 offen offset:0 sc1 lds\nbuffer_load_dwordx4 v0, s[8:11], s94 offen offset:1024 sc1 lds\nbuffer_load_dwordx4 v0, s[8:11], s94 offen offset:2048 sc1 lds\nbuffer_load_dwordx4 v0, s[8:11], s94 offen offset:3072 sc1 lds\n" \
"s_xor_b32 m0, s95, 0x9000\ns_xor_b32 s94, s94, 0x1000\nbuffer_load_dwordx4 v0, s[8:11], s94 offen offset:0 sc1 lds\nbuffer_load_dwordx4 v0, s[8:11], s94 offen offset:1024 sc1 lds\n" \
"buffer_load_dwordx4 v0, s[8:11], s94 offen offset:2048 sc1 lds\nbuffer_load_dwordx4 v0, s[8:11], s94 offen offset:3072 sc1 lds\nv_add_f32 v96, v96, v100\nv_add_f32 v97, v97, v101\n" \
"v_add_f32 v98, v98, v102\nv_add_f32 v99, v99, v103\ns_nop 1\nv_permlane16_swap_b32 v96, v98\n" \
"v_permlane16_swap_b32 v97, v99\nv_add_f32 v96, v96, v98\nv_add_f32 v97, v97, v99\ns_nop 1\n" \
"v_permlane32_swap_b32 v96, v97\nv_add_f32 v96, v96, v97\nds_write_b32 v5, v96\ns_add_u32 s97, s20, 1\n" \
"s_lshr_b32 s96, s97, 1\nv_readlane_b32 s96, v6, s96\ns_and_b32 s97, s97, 1\ns_lshl_b32 s97, s97, 3\n" \
"s_lshr_b32 s96, s96, s97\ns_and_b32 s96, s96, 0xff\ns_lshl_b32 s97, s96, 13\ns_lshl_b32 s96, s96, 16\n" \
"s_add_u32 s96, s96, s21\nbuffer_load_dwordx4 v[52:55], v1, s[8:11], s97 offen offset:0 sc1\nbuffer_load_dwordx4 v[56:59], v1, s[8:11], s97 offen offset:1024 sc1\ns_waitcnt vmcnt(18)\n" \
"buffer_store_dword v84, v3, s[8:11], s23 offen offset:0 sc1\ns_mov_b32 s23, 0x40000000\ns_waitcnt vmcnt(3) lgkmcnt(0)\ns_barrier\n" \
"ds_read_b128 v[72:75], v7 offset:0\nds_read_b128 v[36:39], v7 offset:32768\nds_read_b128 v[40:43], v7 offset:1024\nds_read_b128 v[76:79], v7 offset:33792\n" \
"s_waitcnt lgkmcnt(0)\ns_branch 5b\n9:\ns_waitcnt vmcnt(0)\n" \
"s_endpgm\n"
struct WMA { static constexpr int NT = 4, NP = 16, RB = 0, LB = 0, CBI = 0;
             static constexpr int kind(int j, int t) { return (A_LDS >> (16 * j + t) & 1) ? 1 : (A_VAR >> (16 * j + t) & 1) ? 2 : 0; } };
namespace la { constexpr int L_Q = 24576, L_K = 57344, L_QM = 65536, L_KM = 66048, L_SP = 69632, L_W = 73728; }
#ifndef LEAD_B
#define LEAD_B 132
#endif
DEVI float dror8(float x) { return __uint_as_float(__builtin_amdgcn_update_dpp(0u, __float_as_uint(x), 0x128, 0xF, 0xF, true)); }
DEVI float scm(float a, float m) { float r; asm("v_fma_f32 %0, %1, %2, -%3" : "=v"(r) : "v"(a), "s"(SC_L2E), "v"(m)); return r; }
DEVI float ex2(float x) { return __builtin_amdgcn_exp2f(x); }
DEVI float max3(float a, float b, float c) { float r; asm("v_max3_f32 %0, %1, %2, %3" : "=v"(r) : "v"(a), "v"(b), "v"(c)); return r; }
constexpr u64 tri(int i) { u64 m = 0; for (int l = 0; l < 64; ++l) if (4 * (l >> 4) + i <= (l & 15)) m |= 1ull << l; return m; }
DEVI unsigned vbfe(unsigned a, int o, int n) { unsigned r; asm volatile("v_bfe_u32 %0, %1, %2, %3" : "=v"(r) : "v"(a), "i"(o), "i"(n)); return r; }
DEVI unsigned vadds(unsigned a, unsigned b) { unsigned r; asm volatile("v_add_u32 %0, %1, %2" : "=v"(r) : "s"(b), "v"(a)); return r; }
DEVI float vexp(float a) { float r; asm volatile("v_exp_f32 %0, %1" : "=v"(r) : "v"(a)); return r; }
DEVI unsigned vors(unsigned a, unsigned b) { unsigned r; asm volatile("v_or_b32 %0, %1, %2" : "=v"(r) : "s"(b), "v"(a)); return r; }
DEVI float faddv(float a, float b) { float r; asm volatile("v_add_f32 %0, %1, %2" : "=v"(r) : "v"(a), "v"(b)); return r; }
DEVI unsigned packv(float a, float b) { unsigned r; asm volatile("v_cvt_pk_bf16_f32 %0, %1, %2" : "=v"(r) : "v"(a), "v"(b)); return r; }
DEVI void setm0q(unsigned v) { asm volatile("s_mov_b32 m0, %0" :: "s"(v) : "memory"); }
template <int OFF> DEVI void st16o(v4u v, rsrc_t r, unsigned vo, unsigned so) {
    asm volatile("buffer_store_dwordx4 %0, %1, %2, %3 offen offset:%4 sc1" :: "v"(v), "v"(vo), "s"(r), "s"(so), "n"(OFF) : "memory"); }
DEVI unsigned vlsa(unsigned a, int sh, unsigned c) { unsigned r; asm volatile("v_lshl_add_u32 %0, %1, %2, %3" : "=v"(r) : "v"(a), "i"(sh), "v"(c)); return r; }
DEVI unsigned vlss(unsigned a, int sh, unsigned c) { unsigned r; asm volatile("v_lshl_add_u32 %0, %1, %2, %3" : "=v"(r) : "v"(a), "i"(sh), "s"(c)); return r; }
template <int OFF> DEVI void sts4h(unsigned p, unsigned u0, unsigned u1) {
    asm volatile("ds_write_b16 %0, %1 offset:%3\n\tds_write_b16_d16_hi %0, %1 offset:%3+16\n\tds_write_b16 %0, %2 offset:%3+8\n\tds_write_b16_d16_hi %0, %2 offset:%3+24" :: "v"(p), "v"(u0), "v"(u1), "n"(OFF) : "memory");
}
DEVI void zero4v(f32x4& d) { f32x2 a, b; asm volatile("v_mov_b64 %0, 0\n\tv_mov_b64 %1, 0" : "=v"(a), "=v"(b)); d = __builtin_shufflevector(a, b, 0, 1, 2, 3); }
DEVI float sel0(float a, u64 m) { float r; asm("v_cndmask_b32 %0, %1, 0, %2" : "=v"(r) : "v"(a), "s"(m)); return r; }
DEVI float vmul(float a, float b) { float r; asm("v_mul_f32 %0, %1, %2" : "=v"(r) : "v"(a), "v"(b)); return r; }
DEVI void swap16(float& a, float& b) { unsigned x = __float_as_uint(a), y = __float_as_uint(b); swap16u(x, y); a = __uint_as_float(x); b = __uint_as_float(y); }
#ifndef PACE
#define PACE 8
#endif
DEVI void pace(int l) { asm volatile("s_cmp_eq_u32 %0, 0\n\ts_cbranch_scc1 1f\n\ts_sleep %1\n1:" :: "s"(l), "n"(PACE) : "scc"); }
DEVI void role_A(const Args& a, int w, int l, int h, int rep) {
    asm volatile("; MARK role_A"); __builtin_amdgcn_s_waitcnt(0);
    using namespace la;
    const int lane0 = lane_id();
    const rsrc_t rs = mkrs(a.ws, WS_NEED);
    const unsigned lane16 = lane0 * 16, wl = L_W + w * (11 * 2048) + lane16;
    v8u Wc[3];
    unsigned nb;
    {
        const int lane = lane0;
        const unsigned ta = (lane >> 4) * 2048 + w * 256 + (lane & 15) * 16;
        const rsrc_t r1 = mkrs(a.Wq, WSZ(8u << 20)), r2 = mkrs(a.Wk, WSZ(8u << 20));
        const unsigned v1 = (8 * w + ((lane >> 2) & 3)) * 8192 + h * 256 + (lane >> 4) * 64 + (lane & 3) * 16;
        const unsigned lw = opqs(w * 2048);
        load_blocks<32, 4, 4, true>([&](auto B, auto S) { pace(l); sfor<0, 2>([&](auto I) { dma_blk(lw + S.v * 16384 + I.v * 8192, B.v < 16 ? r1 : r2, v1, opqs((2 * (B.v & 15) + I.v) * 32 * 8192), 2048); }); },
                                    [&](auto B, auto S) { sfor<0, 4>([&](auto T) { constexpr int o = S.v * 16384 + (T.v >> 1) * 8192 + (T.v & 1) * 8;
                                                                                   put_frag<WMA, (B.v >> 4) * 2 + (T.v & 1), 2 * (B.v & 15) + (T.v >> 1), o, o + 1024>(ta, wl, Wc); });
                                                          if constexpr (B.v == 28) nb = side(rs, l); });
        TSW(127, 2);
    }
    const unsigned spm = na(nb, N_PM + h * 512), spmm = na(nb, N_PMM + h * 128), smem = na(nb, N_MEM + rep * 16384 + w * 4096);
    const int lane = lane_id(), c = lane & 15, g = lane >> 4;
    const unsigned own = (w >> 1) * 32768 + (w & 1) * 4096, wlo = (unsigned)(uintptr_t)a.ws, whi = (unsigned)((uintptr_t)a.ws >> 32), hi = -(w >> 1);
    const v8u pv{lane16, wl, (lane & 2) ? 0xEEEEu : 0x4444u, (unsigned)(L_QM + w * 1024 + (c >> 2) * 128 + g * 32 + (c & 3) * 4), (unsigned)(L_QM + g * 128 + (c & 7) * 16),
                 w == 2 ? ((g >> 1) * 16 + crow(c & 7, c >> 3)) * 16 + (g & 1) * 8 : w < 2 ? lane * 8 : lane < 32 ? DV(lane * 512) : 1u << 30,
                 w < 2 ? c * 16 + g * 4 : c < 8 ? crow(c, g & 1) * 8 + (g >> 1) * 4 : 1u << 30, 0u};
    const v16u s0{wlo, whi, RSZ, 0x20000u, wlo, whi, 0u, 0x20000u, (unsigned)tri(0) | hi, (unsigned)(tri(0) >> 32) | hi, (unsigned)tri(1) | hi, (unsigned)(tri(1) >> 32) | hi,
                  (unsigned)tri(2) | hi, (unsigned)(tri(2) >> 32) | hi, (unsigned)tri(3) | hi, (unsigned)(tri(3) >> 32) | hi};
    const v16u s1{0xFF00FF00u, 0xFF00FF00u, 0u, ~0u, hi, hi, 0u, O_XN + l * XB + own, own, smem, w == 2 ? spm : OOB, nb + DV(N_PTX + h * 2048), w == 1 ? 0u : OOB, w == 0 ? 0u : OOB,
                  w < 2 ? w * 256 : w == 2 ? spmm - 1536 : OOB, ~hi};
    const v16u s2{w < 2 ? w * 1024 : w == 2 ? spm : spmm, 17u - w, w < 2 ? 0u : w == 2 ? 0x200u : 0x380u, w ? 0u : __float_as_uint(-3e38f), (unsigned)(L_Q + w * 1024), (unsigned)(L_Q + (w & 1) * 4096),
                  __float_as_uint(SC_L2E), __float_as_uint(-1e30f), na(nb, N_FX), w == 3 ? na(nb, N_PA + ((h >> 3) * 32 + 8 + (h & 7)) * 4) : OOB, (unsigned)(L_KM + (w >> 1) * 1024 + (w & 1) * 256),
                  0u, 0u, (unsigned)(L_SP + w * 256), O_PROF + (blockIdx.x * 4 + w) * 8192, na(nb, N_WM + h * 16384 + w * 4096)};
    asm volatile(A_LOOP :: "{v[0:7]}"(Wc[0]), "{v[8:15]}"(Wc[1]), "{v[16:23]}"(Wc[2]), "{v[24:31]}"(pv), "{s[8:23]}"(s0), "{s[24:39]}"(s1), "{s[40:55]}"(s2),
                 "{s58}"(na(nb, N_U + 8 * w * 65536 + h * 2048)) : "memory", "s99", "vcc");
}
constexpr int bkind(int n) { return (B_LDS >> n & 1) ? 1 : (B_VAR >> n & 1) ? 2 : 0; }
struct WMV { static constexpr int NT = 2, NP = 16, RB = 0, LB = 0, CBI = 0; static constexpr int kind(int j, int t) { return bkind(16 * j + t); } };
struct WMO { static constexpr int NT = 16, NP = 2, LB = __builtin_popcount((unsigned)B_LDS), CBI = __builtin_popcount((unsigned)B_VAR), RB = 8 * (32 - LB - CBI);
             static constexpr int kind(int j, int t) { return bkind(32 + 2 * j + t); } };
#ifndef B_LV
#define B_LV 24576
#endif
namespace lb { constexpr int L_V = B_LV, L_VM = B_LV + 4096, L_W = 65536; }
DEVI void role_B(const Args& a, int w, int l, int h, int rep) {
    asm volatile("; MARK role_B"); __builtin_amdgcn_s_waitcnt(0);
    using namespace lb;
    const int lane0 = lane_id();
    const rsrc_t rs = mkrs(a.ws, WS_NEED);
    const unsigned lane16 = lane0 * 16, wl = L_W + w * (12 * 2048) + lane16;
    v8u Wc[2];
    unsigned nb, fx;
    {
        const int lane = lane0;
        const unsigned ta = (lane >> 4) * 2048 + w * 256 + (lane & 15) * 16, tb = (lane >> 5) * 8192 + w * 2048 + (lane & 16) * 64 + (lane & 15) * 16;
        const rsrc_t r1 = mkrs(a.Wv, WSZ(8u << 20)), r2 = mkrs(a.Wo, WSZ(8u << 20));
        const unsigned v1 = (8 * w + ((lane >> 2) & 3)) * 8192 + h * 256 + (lane >> 4) * 64 + (lane & 3) * 16, v2 = (h * 128 + ((lane >> 2) & 7)) * 2048 + w * 512 + (lane >> 5) * 64 + (lane & 3) * 16;
        const unsigned lw = opqs(w * 2048);
        load_blocks<32, 4, 4, true>([&](auto B, auto S) {
            constexpr int b = B.v & 15;
            pace(l);
            sfor<0, 2>([&](auto I) { if constexpr (B.v < 16) dma_blk(lw + S.v * 16384 + I.v * 8192, r1, v1, opqs((2 * b + I.v) * 32 * 8192), 2048);
                                     else dma_blk(lw + S.v * 16384 + I.v * 8192, r2, v2, opqs(((b & 3) * 32 + I.v * 16) * 2048 + (b >> 2) * 128), 1024); });
        }, [&](auto B, auto S) {
            constexpr int b = B.v & 15;
            sfor<0, 4>([&](auto T) { constexpr int o = S.v * 16384 + (T.v >> 1) * (B.v < 16 ? 8192 : 512) + (T.v & 1) * 8;
                                     if constexpr (B.v < 16) put_frag<WMV, T.v & 1, 2 * b + (T.v >> 1), o, o + 1024>(ta, wl, Wc); else put_frag<WMO, (b & 12) + T.v, b & 3, o, o + 256>(tb, wl, Wc); });
            if constexpr (B.v == 28) { nb = side(rs, l); fx = ld4(rs, (lane & 7) * 4, na(nb, N_FX)); }
        });
        TSW(127, 2);
    }
    const int lane = lane_id(), c = lane & 15, g = lane >> 4;
    const unsigned own = (w >> 1) * 32768 + (w & 1) * 4096, wlo = (unsigned)(uintptr_t)a.ws, whi = (unsigned)((uintptr_t)a.ws >> 32);
    const unsigned prow = L_VM + w * 512 + ((c >> 2) & 1) * 256 + g * 64 + (c >> 3) * 32 + (c & 1) * 8 + ((c >> 1) & 1) * 4;
    const v4u pv{lane16, wl, ((lane & 2) ? 0xEEEEu : 0x4444u) | prow << 16, (unsigned)(L_VM + (g >> 1) * 512 + (lane & 3) * 64 + (g & 1) * 32 + ((lane >> 2) & 3) * 8)};
    const v16u s0{wlo, whi, RSZ, 0x20000u, wlo, whi, 0u, 0x20000u, OOB, OOB, OOB, OOB, ~1u, O_XN + l * XB + own, own, na(nb, N_MEM + rep * 16384 + w * 4096)};
    const v8u s1{na(nb, N_WM + h * 16384 + w * 4096), na(nb, N_U + h * 65536 + w * 16384), na(nb, N_FX), w == 3 ? na(nb, N_PA + ((h >> 3) * 32 + (h & 7)) * 4) : OOB,
                 (unsigned)(L_V + w * 1024), OOB, 0u, O_PROF + (blockIdx.x * 4 + w) * 8192 + 64};
    asm volatile(B_LOOP :: "{v[0:7]}"(Wc[0]), "{v[8:15]}"(Wc[1]), "{v[16:19]}"(pv), "{v20}"(fx), "{s[8:23]}"(s0), "{s[24:31]}"(s1) : "memory", "s99", "vcc");
}
template <class NB> DEVI void put_tile(v4u (&u)[8], rsrc_t rs, int tile, int w, int lane, NB&& nbf) {
    const unsigned vo = tile * 32768 + w * 8192 + lane * 16, red = (lane & 15) * 16;
    float s = 0.f;
#pragma unroll
    for (int f = 0; f < 8; ++f) {
        st16(u[f], rs, O_XIN + vo + f * 1024);
#pragma unroll
        for (int i = 0; i < 4; ++i) s += bflo(u[f][i]) + bfhi(u[f][i]);
    }
    s = red32(red16(s));
    BAR;
    if (lane < 16) *LDSP(float, red + w * 4) = s;
    WL0; BAR;
    const f32x4 sa = __builtin_bit_cast(f32x4, ldsq(red, 0));
    const float mean = ((sa[0] + sa[1]) + (sa[2] + sa[3])) * (1.0f / 1024.0f);
    float q = 0.f;
#pragma unroll
    for (int f = 0; f < 8; ++f) {
        asm volatile("" : "+v"(u[f]));
#pragma unroll
        for (int i = 0; i < 4; ++i) { const float d0 = bflo(u[f][i]) - mean, d1 = bfhi(u[f][i]) - mean; q = fmaf(d0, d0, fmaf(d1, d1, q)); }
    }
    q = red32(red16(q));
    if (lane < 16) *LDSP(float, red + 256 + w * 4) = q;
    WL0; BAR;
    const f32x4 qa = __builtin_bit_cast(f32x4, ldsq(red, 256));
    const float mb = rbf(mean), inv = 1.0f / rbf(sqrtf(rbf(rbf(((qa[0] + qa[1]) + (qa[2] + qa[3])) * (1.0f / 1024.0f)) + 1e-5f)));
#pragma unroll
    for (int f = 0; f < 8; ++f) {
        v4u o;
        asm volatile("" : "+v"(u[f]));
#pragma unroll
        for (int i = 0; i < 4; ++i) { const unsigned d = pack2(bflo(u[f][i]) - mb, bfhi(u[f][i]) - mb); o[i] = pack2(bflo(d) * inv, bfhi(d) * inv); }
        st16(o, rs, O_XN + vo + f * 1024);
    }
    const unsigned nb = nbf();
    WV0;
    if (lane == 0) st4(0u, rs, na(nb, N_FX) + tile * 16 + w * 4, 0);
}
#ifndef SXL
#define SXL 1
#endif
#ifndef PTD
#define PTD 4
#endif
#ifndef HLD
#define HLD 170
#endif
#ifndef SXH
#define SXH 30
#endif
#ifndef PTL2
#define PTL2 0
#endif
#ifndef SXP
#define SXP 1
#endif
#ifndef SXH0
#define SXH0 SXH
#endif
#ifndef WF
#define WF 0
#endif
#ifndef UIN
#define UIN 8
#endif
namespace lr { constexpr int PT1 = 192, PT0 = 224, PTM = 240, VPM = NV, VMM = NV + 16, VW = NV + 28, VX = NV + 44, L_M = 1024, L_T = 8192; }
template <int R, int N, int OFF, bool CO = true> DEVI void lda(rsrc_t rs, unsigned vo, unsigned so) {
    if constexpr (!CO && N == 4) asm volatile("buffer_load_dwordx4 a[%3:%4], %0, %1, %2 offen offset:%5" :: "v"(vo), "s"(rs), "s"(so), "n"(R), "n"(R + 3), "n"(OFF) : "memory");
    else if constexpr (!CO) asm volatile("buffer_load_dwordx2 a[%3:%4], %0, %1, %2 offen offset:%5" :: "v"(vo), "s"(rs), "s"(so), "n"(R), "n"(R + 1), "n"(OFF) : "memory");
    else if constexpr (N == 4) asm volatile("buffer_load_dwordx4 a[%3:%4], %0, %1, %2 offen offset:%5 sc1" :: "v"(vo), "s"(rs), "s"(so), "n"(R), "n"(R + 3), "n"(OFF) : "memory");
    else asm volatile("buffer_load_dwordx2 a[%3:%4], %0, %1, %2 offen offset:%5 sc1" :: "v"(vo), "s"(rs), "s"(so), "n"(R), "n"(R + 1), "n"(OFF) : "memory");
}
template <int R> DEVI unsigned rdv() { unsigned t; asm volatile("v_mov_b32 %0, v[%1]" : "=v"(t) : "n"(R)); return t; }
template <int RA, int RB, bool Z, bool AV> DEVI void mfh(f32x4& d) {
    if constexpr (AV) { if constexpr (Z) asm volatile(M16 "%0, v[%1:%2], a[%3:%4], 0" : "=v"(d) : "n"(RA), "n"(RA + 3), "n"(RB), "n"(RB + 3));
                        else asm volatile(M16 "%0, v[%1:%2], a[%3:%4], %0" : "+v"(d) : "n"(RA), "n"(RA + 3), "n"(RB), "n"(RB + 3)); }
    else { if constexpr (Z) asm volatile(M16 "%0, a[%1:%2], a[%3:%4], 0" : "=v"(d) : "n"(RA), "n"(RA + 3), "n"(RB), "n"(RB + 3));
           else asm volatile(M16 "%0, a[%1:%2], a[%3:%4], %0" : "+v"(d) : "n"(RA), "n"(RA + 3), "n"(RB), "n"(RB + 3)); }
}
template <int RA, int RB, bool Z> DEVI void mfh16(f32x4& d) {
    if constexpr (Z) asm volatile("v_mfma_f32_16x16x16_bf16 %0, a[%1:%2], a[%3:%4], 0" : "=v"(d) : "n"(RA), "n"(RA + 1), "n"(RB), "n"(RB + 1));
    else asm volatile("v_mfma_f32_16x16x16_bf16 %0, a[%1:%2], a[%3:%4], %0" : "+v"(d) : "n"(RA), "n"(RA + 1), "n"(RB), "n"(RB + 1));
}
template <int RA, int RB, bool BV> DEVI void smfh(f32x4& d, int sidx) {
    if constexpr (BV) asm volatile(S16 "%0, v[%2:%3], v[%4:%5], %1" : "+v"(d) : "v"(sidx), "n"(RA), "n"(RA + 3), "n"(RB), "n"(RB + 7));
    else asm volatile(S16 "%0, v[%2:%3], a[%4:%5], %1" : "+v"(d) : "v"(sidx), "n"(RA), "n"(RA + 3), "n"(RB), "n"(RB + 7));
}
template <int R> DEVI u64 ltv(int need) { u64 m; asm volatile("v_cmp_lt_i32 %0, v[%2], %1" : "=s"(m) : "v"(need), "n"(R)); return m; }
template <int R> DEVI void ldv4(rsrc_t rs, unsigned vo, unsigned so) { asm volatile("buffer_load_dwordx4 v[%3:%4], %0, %1, %2 offen sc1" :: "v"(vo), "s"(rs), "s"(so), "n"(R), "n"(R + 3) : "memory"); }
template <int W0, int P0>
DEVI void poll_round(unsigned vw, unsigned lane16, unsigned vmm, rsrc_t rs, unsigned sw0, unsigned sw1, unsigned sp, unsigned sm) {
    asm volatile(
#if WF
        "buffer_load_dwordx4 v[%[w0]:%[w0]+3], %[vw], %[rs], %[sw0] offen sc1\n\t"
        "buffer_load_dwordx4 v[%[w0]+4:%[w0]+7], %[vw], %[rs], %[sw1] offen sc1\n\t"
        "buffer_load_dwordx4 v[%[w0]+8:%[w0]+11], %[vw], %[rs], %[sw0] offen offset:256 sc1\n\t"
        "buffer_load_dwordx4 v[%[w0]+12:%[w0]+15], %[vw], %[rs], %[sw1] offen offset:256 sc1\n\t"
#endif
        "buffer_load_dwordx4 v[%[p0]:%[p0]+3], %[l16], %[rs], %[sp] offen sc1\n\t"
        "buffer_load_dwordx4 v[%[p0]+4:%[p0]+7], %[l16], %[rs], %[sp] offen offset:1024 sc1\n\t"
        "buffer_load_dwordx4 v[%[p0]+8:%[p0]+11], %[l16], %[rs], %[sp] offen offset:2048 sc1\n\t"
        "buffer_load_dwordx4 v[%[p0]+12:%[p0]+15], %[l16], %[rs], %[sp] offen offset:3072 sc1\n\t"
        "buffer_load_dwordx2 v[%[p0]+16:%[p0]+17], %[vm], %[rs], %[sm] offen sc1\n\t"
        "buffer_load_dwordx2 v[%[p0]+18:%[p0]+19], %[vm], %[rs], %[sm] offen offset:128 sc1\n\t"
#if !WF
        "buffer_load_dwordx4 v[%[w0]:%[w0]+3], %[vw], %[rs], %[sw0] offen sc1\n\t"
        "buffer_load_dwordx4 v[%[w0]+4:%[w0]+7], %[vw], %[rs], %[sw1] offen sc1\n\t"
        "buffer_load_dwordx4 v[%[w0]+8:%[w0]+11], %[vw], %[rs], %[sw0] offen offset:256 sc1\n\t"
        "buffer_load_dwordx4 v[%[w0]+12:%[w0]+15], %[vw], %[rs], %[sw1] offen offset:256 sc1\n\t"
#endif
        :: [w0] "n"(W0), [p0] "n"(P0), [vw] "v"(vw), [l16] "v"(lane16), [vm] "v"(vmm), [rs] "s"(rs), [sw0] "s"(sw0), [sw1] "s"(sw1), [sp] "s"(sp), [sm] "s"(sm) : "memory");
}
template <int R0, int R1> DEVI void mx3(unsigned& m) { asm volatile("v_max3_u32 %0, %0, v[%1], v[%2]" : "+v"(m) : "n"(R0), "n"(R1)); }
DEVI void wr2x2(unsigned p, float r0, float r1, float r2, float r3) {
    asm volatile("ds_write2_b32 %0, %1, %3 offset0:0 offset1:2\n\tds_write2_b32 %0, %2, %4 offset0:8 offset1:10" :: "v"(p), "v"(r0), "v"(r1), "v"(r2), "v"(r3) : "memory");
}
#ifndef DLY_X
#define DLY_X 1400
#endif
DEVI void role_R(const Args& a, int w, int l, int T2, int t0) {
    asm volatile("; MARK role_R"); __builtin_amdgcn_s_waitcnt(0);
    using namespace lr;
    const int lane = lane_id(), c = lane & 15, g = lane >> 4;
    const rsrc_t rs = mkrs(a.ws, WS_NEED);
    const unsigned lane16 = lane * 16;
#pragma nounroll
    for (int k = 0; k < 4 * l; ++k) {
        const int tile = k * 32 + T2;
        if (tile >= LN_NOW) hold(t0, DLY_X);
        v4u u[8];
#pragma unroll
        for (int f = 0; f < 8; ++f) u[f] = *(const v4u*)(a.x + (size_t)(tile * 16 + c) * 1024 + (w * 8 + f) * 32 + g * 8);
        if (tile < 2 * NSEG) put_tile(u, rs, tile, w, lane, [&]() { hold(t0, DLY_V); return side(rs, 0); });
        TSW(127, 1 + k);
    }
    if (l == 0)
#pragma unroll
        for (int i = 0; i < 2; ++i) {
            const unsigned n2 = O_N + i * 8192;
#pragma unroll
            for (int k = 0; k < 4; ++k) st16(v4u{SENT, SENT, SENT, SENT}, rs, na(n2, N_WM + T2 * 16384 + w * 4096) + k * 1024 + lane16);
            st16(v4u{SENT, SENT, SENT, SENT}, rs, w < 2 ? na(n2, N_PTX + T2 * 2048) + w * 1024 + lane16 : lane >= (w == 2 ? 32 : 8) ? OOB : na(n2, w == 2 ? N_PM + T2 * 512 : N_PMM + T2 * 128) + lane16);
        }
    hold(t0, DLY_V);
    const unsigned nb = side(rs, l), vmem = na(nb, N_MEM + 2 * w * 16384 + T2 * 512) + lane * 8;
    auto put_mem = [&](v2u v, int s) { const unsigned so = rslot(s) * (NREP * 32768); st8(v, rs, vmem, so); st8(v, rs, vmem, so + 32768); };
    {
        const unsigned* m0 = (const unsigned*)(a.mem0 + (((lane >> 3) & 3) * 2 + ((lane >> 1) & 1)) * 1024 + T2 * 32 + (lane >> 5) * 16 + (lane & 1) * 8 + ((lane >> 2) & 1) * 2);
        const v2u mv{nosent(m0[0]), m0[2]};
        put_mem(v2u{SENT, SENT}, 1); put_mem(v2u{SENT, SENT}, 2);
        WV0; BAR;
        put_mem(mv, 0);
    }
    const int sidx = opq((lane & 2) ? 0xEEEE : 0x4444);
    const unsigned vmm = (8 * w + 2 * g) * 128 + c * 8, vw = (8 * w + g) * 32768 + DV(T2 * 512) + c * 16, vptm = g * 2048 + 1536 + c * 16;
    const unsigned sfl = na(nb, N_PA + (unsigned)(T2 * 4 + w) * 128), su0 = na(nb, N_U + 8 * w * 65536 + T2 * 2048), vfl = sfl + c * 4;
    const unsigned lred = L_M + w * 1024 + (c >> 3) * 512 + g * 128 + ((c >> 1) & 1) * 64 + ((c >> 2) & 1) * 16 + (c & 1) * 4;
    auto ld_u = [&](auto B, int s) {
        const unsigned so = su0 + rslot(s) * (4u << 20);
        sfor<0, 16>([&](auto I) { constexpr int j = I.v >> 1, e = I.v & 1; lda<64 * B.v + 16 * (j >> 1) + 8 * e + 4 * (j & 1), 4, e * 1024>(rs, lane16, so + j * 131072); });
    };
    wait_prog(rs, sfl + (lane & 7) * 4, 0, 0);
    ld_u(IC<0>{}, 0);
    wait_prog(rs, vfl, 0, 1 << 24);
    v2u sxv{0u, 0u};
    unsigned sxo = OOB;
    int tpub = t0;
    auto step = [&](auto B, int s) {
        constexpr int b = B.v;
        TSD;
        TSR(0); TSK(15);
        if (s == 8) { TSC(126, 0); TSW(126, 1); }
        if (s == 56) { TSC(126, 2); TSW(126, 3); }
        const unsigned spt = na(nb, N_PTX + (unsigned)(s * 32 + 8 * w) * 2048);
        auto ptx = [&](auto N) {
            constexpr int n = N.v & 31, j = n >> 1; constexpr bool co = N.v >= 32 || !PTL2;
            if constexpr (n == 20) ldv4<VX>(rs, lane16, O_XIN + l * XB + s * 65536 + (w & 1) * 32768 + T2 * 1024);
            else if constexpr (n >= 16) lda<PTM + 4 * (n - 16), 4, (n & 1) * 256, co>(rs, vptm, spt + (j - 8) * 16384);
            else if constexpr (n & 1) lda<PT0 + 2 * j, 2, 1024, co>(rs, lane * 8, spt + DV(j * 2048));
            else lda<PT1 + 4 * j, 4, 0, co>(rs, lane16, spt + DV(j * 2048));
        };
        {
            const unsigned sw = na(nb, N_WM + rslot(s) * 524288);
            if ((int)__builtin_amdgcn_s_memrealtime() - tpub < HLD) { wvm<16 - UIN>(); st8(sxv, rs, sxo, 0); sxo = OOB; hold(tpub, HLD); }
            wait_hint(rs, sfl, vfl, s + 1, [&](int mx) { if (SXH > -99 && left(mx) > (l ? SXH : SXH0) * 256) { wvm<16 - UIN>(); st8(sxv, rs, sxo, 0); sxo = OOB; } });
            TSR(5);
            unsigned spins = 0;
            f32x4 e0, e1;
            unsigned mx;
            auto look = [&]() { zero4(e0); zero4(e1); mx = opq(0); poll_round<VW, VPM>(vw, lane16, vmm, rs, sw, sw + 131072, na(nb, N_PM + w * 4096) + rslot(s) * 32768, na(nb, N_PMM) + rslot(s) * 16384); };
            auto prod = [&](auto NY) {
                sfor<0, 10>([&](auto N) {
                    constexpr int n = N.v, p = n >> 1, e = n & 1;
                    if constexpr (n < 8 && e == 0) wvm<(n ? NY.v : 0) + (WF ? 5 : 9) - p>();
                    if constexpr (n >= 8) wvm<NY.v + (WF ? 0 : 2 - 2 * e)>();
                    if constexpr (n == 1 && NY.v) { st8(sxv, rs, sxo, 0); sxo = OOB; }
                    if constexpr (p < 4) smfh<VPM + 4 * p, 64 * b + 16 * p + 8 * e, false>(e ? e1 : e0, sidx);
                    else smfh<VMM, VW + 8 * e, true>(e ? e1 : e0, sidx);
                    if constexpr (n < 8 && e == 0) mx3<VPM + 4 * p, VPM + 4 * p + 2>(mx);
                    if constexpr (n >= 8) { mx3<VMM + 2 * e, VMM + 2 * e + 1>(mx); mx3<VW + 8 * e, VW + 8 * e + 4>(mx); }
                    SB;
                });
            };
#pragma nounroll
            for (;;) {
                look(); prod(IC<SXP>{});
                nop<3>(); fence(e0, e1);
                if (pend1(mx) == 0) break;
                spin_bail(spins);
                nop<7>();
            }
            TSR(1);
#if defined(TS) && TS != 4
            tsr[8] = spins + 1;
#endif
            const float r0 = fadd(e0[0], e0[2]), r1 = fadd(e0[1], e0[3]), r2 = fadd(e1[0], e1[2]), r3 = fadd(e1[1], e1[3]);
            wr2x2(lred, r0, r1, r2, r3);
            sfor<0, PTD>(ptx);
            WL0; BAR;
            f32x4 q[4];
#pragma unroll
            for (int k = 0; k < 4; ++k) q[k] = __builtin_bit_cast(f32x4, ldsq(lane16, L_M + k * 1024));
            float o[4];
#pragma unroll
            for (int i = 0; i < 4; ++i) o[i] = fadd(fadd(q[0][i], q[1][i]), fadd(q[2][i], q[3][i]));
            const v2u mv{nosent(pack2(o[0], o[1])), pack2(o[2], o[3])};
            put_mem(mv, s + 1);
            if (!SXP) st8(sxv, rs, sxo, 0);
            tpub = (int)__builtin_amdgcn_s_memrealtime();
            TSR(2);
        }
        sfor<PTD, 21>(ptx);
        if (PTD < 21) BAR;
        f32x4 t[2][2], u[2];
        put_mem(v2u{SENT, SENT}, s + 3);
#pragma nounroll
        for (int n = 0;; ++n) {
            if (n) { sfor<32, 52>(ptx); WV0; }
            const unsigned su = su0 + rslot(s + 1) * (4u << 20);
            auto lu = [&](auto N) { constexpr int j = N.v >> 1, e = N.v & 1; lda<16 * (j >> 1) + 8 * e + 4 * (j & 1) + 64 * (1 - b), 4, e * 1024>(rs, lane16, su + j * 131072); };
            TSR(6);
            sfor<0, 16>([&](auto N) {
                constexpr int j = N.v >> 1, e = N.v & 1, R = 64 * b + 16 * (j >> 1) + 8 * e + 4 * (j & 1), nu = N.v * UIN / 16;
                if constexpr (e == 0) wvm<21 - 2 * j + (2 * j + 1 < PTD ? 3 - SXP : 0) + nu>();
                if constexpr ((N.v + 1) * UIN / 16 > nu) lu(IC<nu>{});
                mfh<R, PT1 + 4 * j, j == 0, false>(t[1][e]); mfh16<R, PT0 + 2 * j, j == 0>(u[e]);
            });
            wvm<2 + (PTD == 21 ? 3 - SXP : 0) + UIN>();
            sfor<0, 8>([&](auto N) { constexpr int q = N.v >> 2, rt = (N.v >> 1) & 1, e = N.v & 1; mfh<VW + 8 * e + 4 * q, PTM + 4 * (2 * q + rt), N.v < 2, true>(t[rt][e]); });
            nop<7>(); fence(t[0][0], t[0][1], t[1][0], t[1][1], u[0], u[1]);
#pragma unroll
            for (int e = 0; e < 2; ++e)
#pragma unroll
                for (int i = 0; i < 4; ++i) t[0][e][i] = fadd(t[0][e][i], u[e][i]);
            if (__builtin_amdgcn_fcmpf(t[0][0][0], t[1][0][0], 8) == 0 || n == 2) break;
            TSV(s, 7, 1);
            if (n == 1) wait_prog(rs, sfl + 32 + (lane & 7) * 4, 0, (s + 2) << 24);
        }
        TSR(3);
#pragma unroll
        for (int e = 0; e < 2; ++e) { stsq(lane16 + w * 4096, L_T + e * 1024, __builtin_bit_cast(v4u, t[0][e])); stsq(lane16 + w * 4096, L_T + 2048 + e * 1024, __builtin_bit_cast(v4u, t[1][e])); }
        WL0; BAR;
        if (w < 2) {
            float o[8], s1 = 0.f, s2 = 0.f;
#pragma unroll
            for (int e = 0; e < 2; ++e) {
                f32x4 q[4];
#pragma unroll
                for (int k = 0; k < 4; ++k) q[k] = __builtin_bit_cast(f32x4, ldsq(lane16 + w * 2048, L_T + k * 4096 + e * 1024));
#pragma unroll
                for (int i = 0; i < 4; ++i) o[4 * e + i] = fadd(fadd(q[0][i], q[1][i]), fadd(q[2][i], q[3][i]));
            }
            v4u x1;
            sfor<0, 4>([&](auto I) {
                constexpr int i = I.v;
                const unsigned x = rdv<VX + i>(), at = pack2(o[2 * i], o[2 * i + 1]);
                x1[i] = pack2(bflo(x) + bflo(at), bfhi(x) + bfhi(at));
                const float lo = bflo(x1[i]), hi = bfhi(x1[i]);
                s1 += lo + hi; s2 = fmaf(lo, lo, fmaf(hi, hi, s2));
            });
            st16(x1, rs, O_XM + l * XB + s * 65536 + w * 32768 + T2 * 1024 + lane16);
            sxv = v2u{nosent(__float_as_uint(red32(red16(s1)))), __float_as_uint(red32(red16(s2)))};
            sxo = lane < 16 ? O_SX + (unsigned)((l * 64 + s) * 32 + T2) * 256 + (SXL ? w * 128 + c * 8 : c * 16 + w * 8) : OOB;
        }
        TSR(4);
        {
            const unsigned su = su0 + rslot(s + 1) * (4u << 20);
            sfor<UIN, 16>([&](auto N) { constexpr int j = N.v >> 1, e = N.v & 1; lda<16 * (j >> 1) + 8 * e + 4 * (j & 1) + 64 * (1 - b), 4, e * 1024>(rs, lane16, su + j * 131072); });
        }
#if TS != 4
        TSF(s, 0x817F);
#endif
    };
#pragma nounroll
    for (int s = 0; s < NSEG; s += 2) { step(IC<0>{}, s); step(IC<1>{}, s + 1); }
    wvm<16 - UIN>(); st8(sxv, rs, sxo, 0);
    WV0;
}
#ifndef FSIDE
#define FSIDE 1
#endif
#ifndef PDX
#define PDX 0
#endif
constexpr int NU = 2 * NSEG;
#ifndef PSF
#define PSF 40
#endif
#ifndef PSR
#define PSR 12
#endif
#ifndef RPA
#define RPA 8
#endif
#ifndef GROT
#define GROT 1
#endif
#ifndef LAGK
#define LAGK 10
#endif
#ifndef LAGP
#define LAGP 0
#endif
DEVI unsigned unit_ls(unsigned u) {
    static_assert(NSEG == 64 && LAGD >= 6, "");
    static_assert(LAGP % 2 == 0 && LAGP < 2 * LAGK + 1, "");
    const unsigned d = u - LAGD + LAGP, p = d / (2 * LAGK + 1), r = d % (2 * LAGK + 1), n0 = LAGD - LAGP / 2 + p * (LAGK + 1) + (r >> 1);
    return u < LAGD || n0 >= 64 ? u : ((r & 1) || r == 2 * LAGK) ? n0 : 64 + u - n0;
}
DEVI unsigned unit_tab(int lane) { return opq(unit_ls(2 * lane) | unit_ls(2 * lane + 1) << 8); }
DEVI unsigned unit_get(unsigned tab, int u) { return ((unsigned)__builtin_amdgcn_readlane(tab, (u >> 1) & 63) >> ((u & 1) * 8)) & 255; }
struct WMG { static constexpr int NT = 3, NP = 16, RB = 0, LB = 0, CBI = 0; static constexpr int kind(int, int) { return 0; } };
struct WMD { static constexpr int NT = 4, NP = 12, RB = 0, LB = 0, CBI = 0; static constexpr int kind(int, int) { return 0; } };
namespace lg { constexpr int L_ST = 155648, L_OK = 159744; }
DEVI void role_GU(const Args& a, int w, int j) {
    asm volatile("; MARK role_GU"); __builtin_amdgcn_s_waitcnt(0);
    using namespace lg;
    const int lane = lane_id(), c = lane & 15, g = lane >> 4;
    const rsrc_t rs = mkrs(a.ws, WS_NEED);
    const unsigned lane16 = lane * 16, hc = 96 * j + 24 * w;
    {
        const unsigned ta = g * 4096 + (lane & 1) * 2048 + w * 256 + ((lane >> 2) & 3) * 64 + ((lane >> 1) & 1) * 8, tb = ta + 65536;
        const rsrc_t r1 = mkrs(a.Wg, WSZ(7168u * 1024)), r2 = mkrs(a.Wu, WSZ(7168u * 1024));
        const unsigned cb = 192 * j + g * 48 + (lane & 3) * 16, v1 = (lane & 3) == 3 || cb >= 7168 ? OOB : (8 * w + ((lane >> 2) & 3)) * 7168 + cb, lw = opqs(w * 4096), z = opqs(0);
        load_blocks<32, 8, 4, true>([&](auto B, auto S) { dma_blk(lw + S.v * 16384, r1, v1, z + B.v * 32 * 7168, 1792); dma_blk(lw + S.v * 16384 + 2048, r2, v1, z + B.v * 32 * 7168, 1792); },
                                    [&](auto B, auto S) { sfor<0, 3>([&](auto T) { constexpr int o = (S.v & 3) * 16384 + T.v * 16; put_frag<WMG, T.v, B.v, o, o + 1024>(S.v < 4 ? ta : tb, 0, nullptr); }); });
        TSW(120 - 65536, 0);
        v4u one{0x3F803F80u, 0x3F803F80u, 0x3F803F80u, 0x3F803F80u};
        asm volatile("s_nop 1" : "+v"(one));
        f32x4 cs[3];
        sfor<0, 96>([&](auto N) { constexpr int t = N.v % 3, ks = N.v / 3; mfA<8 * widx<WMG>(t, ks >> 1) + 4 * (ks & 1), ks == 0>(cs[t], one); });
        nop<7>(); fence(cs[0], cs[1], cs[2]);
        sfor<0, 3>([&](auto T) { mwr4<384 + 4 * T.v>(__builtin_bit_cast(v4u, cs[T.v])); });
        BAR;
    }
    const unsigned nf = side(rs, FSIDE);
    const unsigned wlo = (unsigned)(uintptr_t)a.ws, whi = (unsigned)((uintptr_t)a.ws >> 32);
    const unsigned xr = w * 8192 + (GROT ? (j & 1) * 4096 + (j & 2) * 16384 : 0);
    const v8u pv{lane16, O_SX + (8 * w + g) * 256 + c * 16, O_H + (g & 1) * 114688 + hc * 32 + c * 16 + (g >> 1) * 8, lane == 0 ? na(nf, N_PG) + j * 128 + w * 4 : OOB,
                 (unsigned)(L_ST + ((lane & 1) * 16 + ((lane & 31) >> 1)) * 16), L_ST + lane16 + w * 4, unit_tab(lane), lane16};
    const v16u s0{wlo, whi, RSZ, 0x20000u, wlo, whi, 0u, 0x20000u, OOB, OOB, hc < 3584 ? 0u : OOB, hc + 8 < 3584 ? 0u : OOB, 0u, O_XM + xr, 65536 + xr, 0u};
    const v2u s1{0u, O_PROF + (blockIdx.x * 4 + w) * 8192};
    asm volatile(G_LOOP :: "{v[0:7]}"(pv), "{s[8:23]}"(s0), "{s[24:25]}"(s1) : "memory", "s99", "vcc");
}
DEVI void role_DN(const Args& a, int w, int p, int cg) {
    asm volatile("; MARK role_DN"); __builtin_amdgcn_s_waitcnt(0);
    using namespace lg;
    const int lane = lane_id();
    const rsrc_t rs = mkrs(a.ws, WS_NEED);
    const unsigned lane16 = lane * 16;
    const int F0 = p < 3 ? 23 * p : (p == 3 ? 68 : 89), zb = p < 2 ? -1 : (p == 4 ? 0 : 22);
    {
        const unsigned ta = w * 18432 + (lane >> 4) * 512 + ((lane >> 2) & 3) * 64 + (lane & 3) * 16;
        const rsrc_t r1 = mkrs(a.Wd, WSZ(3584u * 2048));
        const unsigned v1 = (F0 * 32 + (lane >> 2)) * 2048 + cg * 512 + w * 128 + (lane & 3) * 16;
        load_blocks<46, 9, 2>([&](auto B, auto S) { dma_blk(w * 18432 + S.v * 2048, r1, (B.v >> 1) == zb ? OOB : v1, (B.v >> 1) * 65536 + (B.v & 1) * 64, 2048); },
                              [&](auto B, auto S) { constexpr int ks = B.v >> 1, t = 2 * (B.v & 1), o = S.v * 2048;
                                                    put_frag<WMD, t, ks, o, o + 256>(ta, 0, nullptr); put_frag<WMD, t + 1, ks, o + 8, o + 264>(ta, 0, nullptr); });
        TSW(120 - 65536, 0);
        BAR;
    }
    const int g0 = F0 / 3 * 4, gn = ((F0 + 22) / 3 + 1) * 4 - g0;
    const unsigned nf = side(rs, FSIDE);
    const unsigned wlo = (unsigned)(uintptr_t)a.ws, whi = (unsigned)((uintptr_t)a.ws >> 32);
    const v8u pv{lane16, na(nf, N_PG) + g0 * 32 + (lane < gn ? (lane >> 2) * 128 + (lane & 3) * 4 : 0), lane == 0 ? na(nf ^ PDX, N_PD) + (cg * NKF + p) * 128 + w * 4 : OOB,
                 unit_tab(lane), lane16, (unsigned)(L_OK + lane * 4), (unsigned)((lane & 8) * 2048 + (lane >> 4) * 128 + (lane & 7) * 16), 0u};
    const v16u s0{wlo, whi, RSZ, 0x20000u, wlo, whi, 0u, 0x20000u, wlo, whi, w == 0 ? RSZ : 0u, 0x20000u, 0u, O_H + (w >> 1) * 114688 + (F0 + (w & 1) * 12) * 1024,
                  O_D + p * 65536 + (cg * 8 + w * 2) * 512, (unsigned)(65536 + (w >> 1) * 32768 + (w & 1) * 12288)};
    const v4u s1{(unsigned)(L_OK + w * 256), 0u, 0u, O_PROF + (blockIdx.x * 4 + w) * 8192};
    asm volatile(D_LOOP :: "{v[0:7]}"(pv), "{s[8:23]}"(s0), "{s[24:27]}"(s1) : "memory", "s99", "vcc");
}
template <int R> DEVI void ldv1(rsrc_t rs, unsigned vo) { asm volatile("buffer_load_dword v[%2], %0, %1, 0 offen sc1" :: "v"(vo), "s"(rs), "n"(R) : "memory"); }
template <int R, int I> DEVI void unp2(f32x4& d) { float x, y; asm volatile("v_lshlrev_b32 %0, 16, v[%2]\n\tv_and_b32 %1, 0xffff0000, v[%2]" : "=v"(x), "=v"(y) : "n"(R)); d[I] = x; d[I + 1] = y; }
DEVI void role_RED(const Args& a, int w, int cu) {
    asm volatile("; MARK role_RED"); __builtin_amdgcn_s_waitcnt(0);
    const int lane0 = lane_id(), c = lane0 & 15, g = lane0 >> 4;
    const rsrc_t rs = mkrs(a.ws, WS_NEED);
    const unsigned nf = side(rs, FSIDE);
    v4u sel[2];
#pragma unroll
    for (int e = 0; e < 2; ++e)
#pragma unroll
        for (int i = 0; i < 4; ++i) sel[e][i] = (g == (c >> 2) && i == 2 * e + ((c >> 1) & 1)) ? ((c & 1) ? 0x3F800000u : 0x3F80u) : 0u;
    const unsigned vfl = na(nf ^ PDX, N_PD) + w * (NKF * 128) + (lane0 < 20 ? (lane0 >> 2) * 128 + (lane0 & 3) * 4 : 0);
    constexpr int VP = NV + 32;
    const unsigned lo = (c & 8) * 128 + g * 256 + (c & 7) * 16 + (cu & 1) * 128 + w * 8192, ld = (c & 8) * 64 + g * 128 + (c & 7) * 16 + (cu & 1) * 16384 + w * 4096;
    const unsigned lout = ((cu & 1) * 8 + (c & 7)) * 2048 + w * 512 + (c & 8) * 8 + g * 16, lw = g == (c >> 2) ? (c & 7) * 64 + w * 16 + (c & 8) : 1024 + lane0 * 8;
    const rsrc_t ro = mkrs(a.out, 4u << 20);
    v4u one{0x3F803F80u, 0x3F803F80u, 0x3F803F80u, 0x3F803F80u};
    asm volatile("" : "+v"(one));
    auto issue = [&](auto B, auto N0, auto N1, int k) {
        const unsigned ls = unit_ls(k >> 2), base = ((k >> 1) & 1) * 32768;
        sfor<N0.v, N1.v>([&](auto N) { constexpr int f = (N.v - 4) / 5, p = (N.v - 4) % 5;
            if constexpr (N.v < 4) ldv4<NV + 16 * B.v + 4 * N.v>(rs, lo, O_XM + ls * 65536 + base + N.v * 2048);
            else lda<80 * B.v + 20 * f + 4 * p, 4, f * 1024>(rs, ld, O_D + (ls * NKF + p) * 65536 + base); });
        if constexpr (N1.v == 24) ldv1<VP>(rs, vfl);
    };
    u64 late = 1, far = 1;
    bool pre = false;
    auto task = [&](auto B, int k) {
        constexpr int b = B.v;
        const int u = k >> 2, rt = (k >> 1) & 1, nx = ((k + 6) >> 2) + 1;
        const unsigned ls = unit_ls(u), tile = ls * 2 + rt;
        TSD; TSR(0);
        if (!pre) {
            if (late) {
                unsigned spins = 0;
#pragma nounroll
                for (;;) {
                    if (far) __builtin_amdgcn_s_sleep(PSF); else __builtin_amdgcn_s_sleep(PSR);
                    CB;
                    const int v = (int)ld4(rs, vfl, 0);
                    if (__builtin_amdgcn_sicmp(v, u + 1, 40) == 0) break;
                    far = __builtin_amdgcn_sicmp(v, u, 40);
                    spin_bail(spins);
                }
            }
            TSR(1);
            issue(B, IC<0>{}, IC<24>{}, k);
        }
        else { TSR(1); }
        const bool l0 = ls < 64;
        const rsrc_t rd = l0 ? rs : ro;
        const unsigned vd = opq((l0 ? O_XIN + XB + lo : lout - 64 * 65536) + tile * 32768), fs = l0 ? 2048 : 128;
        v4u x[4];
        f32x4 d[4][2], S, Q;
        auto unp = [&](auto F, auto I) { unp2<NV + 16 * b + 4 * F.v + I.v, 2 * (I.v & 1)>(d[F.v][I.v >> 1]); };
        auto fin = [&](auto F) {
            constexpr int f = F.v;
            fence(d[f][0], d[f][1]);
#pragma unroll
            for (int i = 0; i < 4; ++i) x[f][i] = pack2(d[f][i >> 1][2 * (i & 1)], d[f][i >> 1][2 * (i & 1) + 1]);
            asm volatile("" : "+v"(x[f]));
        };
        auto put = [&](auto F) { constexpr int f = F.v; st16s(x[f], rd, vd, f * fs); SB; mfv<f == 0>(S, one, x[f]); mfv<f == 0>(Q, x[f], x[f]); SB; };
        wvm<24>();
        sfor<0, 4>([&](auto I) { unp(IC<0>{}, I); });
        sfor<0, 4>([&](auto F) {
            constexpr int f = F.v;
            wvm<16 - 5 * f + (f ? f - 1 : 0)>();
            nop<1>();
            sfor<0, 10>([&](auto N) {
                mfB<80 * b + 20 * f + 4 * (N.v >> 1), false>(d[f][N.v & 1], sel[N.v & 1]);
                if constexpr (N.v < 4 && f < 3) unp(IC<(f + 1) & 3>{}, N);
                if constexpr (N.v == 5 && f > 0) fin(IC<(f + 3) & 3>{});
                SB;
            });
            if constexpr (f > 0) put(IC<(f + 3) & 3>{});
        });
        wvm<3>();
        TSR(2);
#ifdef TS
        tsr[4] = pre + 1;
#endif
        const bool p1 = ltv<VP>(nx) == 0;
        if (!p1) ldv1<VP + 1>(rs, vfl);
        nop<3>();
        fin(IC<3>{});
        nop<1>();
        put(IC<3>{});
        auto second = [&](auto NS) {
            wvm<NS.v>();
            pre = ltv<VP + 1>(nx) == 0;
            if (pre) { issue(IC<1 - b>{}, IC<0>{}, IC<24>{}, k + 6); if constexpr (NS.v == 5) wvm<25>(); }
            else {
                if constexpr (NS.v == 5) { ldv1<VP + 1>(rs, vfl); WV0; }
                late = ltv<VP + 1>(nx); far = ltv<VP + 1>(nx - 1);
            }
        };
        if (l0) {
            nop<7>();
            fence(S, Q);
            const float qd = selm(selm(Q[0], Q[1], 0xAAAAAAAAAAAAAAAAull), selm(Q[2], Q[3], 0xAAAAAAAAAAAAAAAAull), 0xCCCCCCCCCCCCCCCCull);
            *LDSP(f32x2, lw) = f32x2{S[0], qd};
            if (p1) issue(IC<1 - b>{}, IC<0>{}, IC<RPA>{}, k + 6);
            f32x2 xf[4][4];
#pragma unroll
            for (int f = 0; f < 4; ++f)
#pragma unroll
                for (int i = 0; i < 4; ++i) { xf[f][i] = f32x2{bflo(x[f][i]), bfhi(x[f][i])}; asm volatile("" : "+v"(xf[f][i])); }
            TSR(5);
            WL0; BAR;
            f32x4 t[4];
#pragma unroll
            for (int i = 0; i < 4; ++i) t[i] = __builtin_bit_cast(f32x4, ldsq((c & 7) * 64, i * 16));
            const f32x4 t4 = (t[0] + t[1]) + (t[2] + t[3]);
            const float mean = (t4[0] + t4[2]) * (1.0f / 1024.0f), var = fmaxf(rbf(fmaf(t4[1] + t4[3], 1.0f / 1024.0f, -mean * mean)), 0.f);
            const float inv = __builtin_amdgcn_rcpf(rbf(__builtin_amdgcn_sqrtf(rbf(var + 1e-5f)))), mi = -rbf(mean) * inv;
#pragma unroll
            for (int f = 0; f < 4; ++f) {
                v4u o;
#pragma unroll
                for (int i = 0; i < 4; ++i) xf[f][i] = __builtin_elementwise_fma(xf[f][i], f32x2{inv, inv}, f32x2{mi, mi});
#pragma unroll
                for (int i = 0; i < 4; ++i) o[i] = pack2(xf[f][i][0], xf[f][i][1]);
                st16(o, rs, vd + (O_XN - O_XIN) + f * 2048);
            }
            TSR(6);
            if (p1) { issue(IC<1 - b>{}, IC<RPA>{}, IC<24>{}, k + 6); TSR(7); wvm<25 - RPA>(); pre = true; } else second(IC<5>{});
            BAR;
            st4(0u, rs, lane0 < 2 && w == 0 ? na(nf ^ (FSIDE ? 0 : 8192), N_FX) + tile * 16 + (cu & 1) * 8 + lane0 * 4 : OOB, 0);
        }
        else if (p1) { issue(IC<1 - b>{}, IC<0>{}, IC<24>{}, k + 6); pre = true; }
        else second(IC<1>{});
        TSR(3); TSF(u, 0xFF);
    };
#pragma nounroll
    for (int k = cu;; k += 12) {
        task(IC<0>{}, k);
        if (k + 6 >= 4 * NU) break;
        task(IC<1>{}, k + 6);
        if (k + 12 >= 4 * NU) break;
    }
}
#ifndef OFFM
#define OFFM 0
#endif
#ifndef LSW
#define LSW 0
#endif
#ifndef PQ
#define PQ 1
#endif
#ifndef XFM
#define XFM 0
#endif
#ifndef DLY_A
#define DLY_A 150
#endif
#ifndef DLY_B
#define DLY_B 0
#endif
#ifndef DLY_1
#define DLY_1 1800
#endif
#ifndef DLY_G
#define DLY_G 1200
#endif
#ifndef DLY_D
#define DLY_D 1600
#endif
__global__ __launch_bounds__(256, 1) __attribute__((amdgpu_num_vgpr(NV))) void mega6(Args a) {
    asm volatile("" ::: "v255", "a255");
    const int w = __builtin_amdgcn_readfirstlane(threadIdx.x >> 6), b = blockIdx.x, xcd = b & 7, slot = b >> 3, l = (xcd >> 2) ^ LSW, idx = (slot & 7) * 4 + (xcd & 3), f = slot - 24;
    const int t0 = (int)__builtin_amdgcn_s_memrealtime(), xf = XFM ? 7 - xcd : xcd;
    TSW(127, 0);
    if (slot < 24 && l && (slot < 16 || idx >= LN_NOW)) pull_all(a, w, (slot >> 3) * 32 + idx - (slot < 16 ? 0 : LN_NOW), t0);
    if (PQ && f >= 0 && f < 4) pull(a.Wq + (1 << 20), 6144, (f * 8 + xcd) * 4 + w, 128);
    if (slot < 8) { hold(t0, l ? DLY_1 : DLY_A); role_A(a, w, l, idx, (xcd & 3) * 2); }
    else if (slot < 16) { hold(t0, l ? DLY_1 : DLY_B); role_B(a, w, l, idx, (xcd & 3) * 2 + 1); }
    else if (slot < 24) role_R(a, w, l, idx, t0);
    else if (f < (xf < 3 ? 6 : 4)) { hold(t0, DLY_G); if (!(OFFM & 1)) role_GU(a, w, xf < 3 ? 6 * xf + f : 6 + 4 * xf + f); }
    else if (xf < 3) {
        const int cu = xf + 3 * (f - 6), q = cu * 4 + w;
        use(ld4(mkrs(a.ws, WS_NEED), q < 2 * NVK + 2 ? prb(q >> 1, q & 1) + lane_id() * 32 : OOB, 0));
        hold(t0, DLY_D); if (!(OFFM & 4)) role_RED(a, w, cu);
        if (q == 0 && !(OFFM & 4)) vote_end(mkrs(a.ws, WS_NEED));
    }
    else {
        if (xf < 7) { vote(mkrs(a.ws, WS_NEED), w, (xf - 3) * 4 + f - 4, l, t0); }
        hold(t0, DLY_D); if (!(OFFM & 2)) role_DN(a, w, xf - 3, f - 4); }
}
extern "C" void kernel_launch(void* const* d_in, const int* in_sizes, int n_in, void* d_out, int out_size, void* d_ws, size_t ws_size, hipStream_t stream) {
    (void)in_sizes; (void)n_in; (void)out_size; (void)ws_size;
    (void)hipFuncSetAttribute((const void*)mega6, hipFuncAttributeMaxDynamicSharedMemorySize, 163840);
    d_ws = (void*)(((uintptr_t)d_ws + (2u << 20) - 1) & ~(uintptr_t)((2u << 20) - 1));
    Args a{(const u16*)d_in[0], (const u16*)d_in[1], (const u16*)d_in[2], (const u16*)d_in[3], (const u16*)d_in[4], (const u16*)d_in[5],
           (const u16*)d_in[8], (const u16*)d_in[9], (const u16*)d_in[10], (char*)d_ws, (u16*)d_out};
    mega6<<<256, 256, 163840, stream>>>(a);
}
```

```cpp
#include <hip/hip_runtime.h>
#include <stdint.h>

typedef unsigned short u16;
typedef unsigned long long u64;
typedef __attribute__((ext_vector_type(8))) __bf16 bf8;
typedef __attribute__((ext_vector_type(16))) __bf16 bf16v;
typedef __attribute__((ext_vector_type(4))) float f32x4;
typedef __attribute__((ext_vector_type(16))) float f32x16;
typedef __attribute__((ext_vector_type(2))) float f32x2;
typedef __attribute__((ext_vector_type(8))) unsigned v8u;
typedef __attribute__((ext_vector_type(4))) unsigned v4u;
typedef __attribute__((ext_vector_type(3))) unsigned v3u;
typedef __attribute__((ext_vector_type(2))) unsigned v2u;
typedef __amdgpu_buffer_rsrc_t rsrc_t;
typedef __attribute__((address_space(3))) char lds_char;
#define DEVI __device__ __forceinline__
#define LDSP(T, p) ((__attribute__((address_space(3))) T*)(p))

typedef __attribute__((ext_vector_type(2))) __bf16 bf16x2_t;
DEVI unsigned pack2(float lo, float hi) { f32x2 v = {lo, hi}; bf16x2_t b = __builtin_convertvector(v, bf16x2_t); return __builtin_bit_cast(unsigned, b); }
DEVI float bflo(unsigned u) { return __uint_as_float(u << 16); }
DEVI float bfhi(unsigned u) { return __uint_as_float(u & 0xffff0000u); }
DEVI float rbf(float x) { return bflo(pack2(x, x)); }
constexpr int NV = 112;
#define MR_(R, A_STR, V_STR, OUTS, INS, W) do { if constexpr ((R) < 256) asm volatile(A_STR : OUTS : INS, [lo] "n"(R), [hi] "n"((R) + (W) - 1)); \
                                                else asm volatile(V_STR : OUTS : INS, [lo] "n"((R) - 256 + NV), [hi] "n"((R) - 256 + NV + (W) - 1)); } while (0)
#define CM_ ,
template <int R> DEVI void mwr(unsigned x) { if constexpr (R < 256) asm volatile("v_accvgpr_write_b32 a[%1], %0" :: "v"(x), "n"(R)); else asm volatile("v_mov_b32 v[%1], %0" :: "v"(x), "n"(R - 256 + NV)); }
template <int R> DEVI void mwr4(v4u x) { mwr<R>(x[0]); mwr<R + 1>(x[1]); mwr<R + 2>(x[2]); mwr<R + 3>(x[3]); }
#define M16 "v_mfma_f32_16x16x32_bf16 "
#define M32 "v_mfma_f32_32x32x16_bf16 "
#define S16 "v_smfmac_f32_16x16x64_bf16 "
template <int R, bool Z> DEVI void mfA(f32x4& d, v4u b) {
    if constexpr (Z) MR_(R, M16 "%[d], a[%[lo]:%[hi]], %[b], 0", M16 "%[d], v[%[lo]:%[hi]], %[b], 0", [d] "=&v"(d), [b] "v"(b), 4);
    else MR_(R, M16 "%[d], a[%[lo]:%[hi]], %[b], %[d]", M16 "%[d], v[%[lo]:%[hi]], %[b], %[d]", [d] "+v"(d), [b] "v"(b), 4);
}
template <int R, bool Z> DEVI void mfB(f32x4& d, v4u a) {
    if constexpr (Z) MR_(R, M16 "%[d], %[a], a[%[lo]:%[hi]], 0", M16 "%[d], %[a], v[%[lo]:%[hi]], 0", [d] "=&v"(d), [a] "v"(a), 4);
    else MR_(R, M16 "%[d], %[a], a[%[lo]:%[hi]], %[d]", M16 "%[d], %[a], v[%[lo]:%[hi]], %[d]", [d] "+v"(d), [a] "v"(a), 4);
}
template <int R, bool Z> DEVI void mf32A(f32x16& d, v4u b) {
    if constexpr (Z) MR_(R, M32 "%[d], a[%[lo]:%[hi]], %[b], 0", M32 "%[d], v[%[lo]:%[hi]], %[b], 0", [d] "=&v"(d), [b] "v"(b), 4);
    else MR_(R, M32 "%[d], a[%[lo]:%[hi]], %[b], %[d]", M32 "%[d], v[%[lo]:%[hi]], %[b], %[d]", [d] "+v"(d), [b] "v"(b), 4);
}
template <bool Z> DEVI void mfv(f32x4& d, v4u a, v4u b) {
    if constexpr (Z) asm volatile(M16 "%0, %1, %2, 0" : "=&v"(d) : "v"(a), "v"(b)); else asm volatile(M16 "%0, %1, %2, %0" : "+v"(d) : "v"(a), "v"(b));
}
template <int R> DEVI void smf(f32x4& d, v4u a, int idx) { MR_(R, S16 "%[d], %[a], a[%[lo]:%[hi]], %[i]", S16 "%[d], %[a], v[%[lo]:%[hi]], %[i]", [d] "+v"(d), [a] "v"(a) CM_ [i] "v"(idx), 8); }
DEVI void smfv(f32x4& d, v4u a, v8u b, int idx) { asm volatile(S16 "%0, %1, %2, %3" : "+v"(d) : "v"(a), "v"(b), "v"(idx)); }
DEVI void zero4(f32x4& d) { d = f32x4{0.f, 0.f, 0.f, 0.f}; asm volatile("" : "+v"(d)); }
template <int N> DEVI void nop() { asm volatile("s_nop %0" :: "n"(N)); }
DEVI void fence(f32x4& d) { asm volatile("" : "+v"(d)); }
template <class... T> DEVI void fence(f32x4& d, T&... r) { fence(d); fence(r...); }
DEVI float selm(float a, float b, u64 m) { float r; asm("v_cndmask_b32 %0, %1, %2, %3" : "=v"(r) : "v"(a), "v"(b), "s"(m)); return r; }
DEVI int opqs(int x) { asm volatile("" : "+s"(x)); return x; }
DEVI v4u lo4(v8u v) { return __builtin_shufflevector(v, v, 0, 1, 2, 3); }
DEVI v4u hi4(v8u v) { return __builtin_shufflevector(v, v, 4, 5, 6, 7); }
DEVI v8u cat8(v4u a, v4u b) { return __builtin_shufflevector(a, b, 0, 1, 2, 3, 4, 5, 6, 7); }
constexpr int AUX_SC1 = 16;
DEVI v4u ld16(rsrc_t r, unsigned vo, unsigned so) { return __builtin_amdgcn_raw_buffer_load_b128(r, vo, so, AUX_SC1); }
DEVI v2u ld8(rsrc_t r, unsigned vo, unsigned so) { return __builtin_amdgcn_raw_buffer_load_b64(r, vo, so, AUX_SC1); }
DEVI unsigned ld4(rsrc_t r, unsigned vo, unsigned so) { return __builtin_amdgcn_raw_buffer_load_b32(r, vo, so, AUX_SC1); }
DEVI void st16(v4u v, rsrc_t r, unsigned vo) { __builtin_amdgcn_raw_buffer_store_b128(v, r, vo, 0, AUX_SC1); }
DEVI void st16s(v4u v, rsrc_t r, unsigned vo, unsigned so) { __builtin_amdgcn_raw_buffer_store_b128(v, r, vo, so, AUX_SC1); }
DEVI void st8(v2u v, rsrc_t r, unsigned vo, unsigned so) { __builtin_amdgcn_raw_buffer_store_b64(v, r, vo, so, AUX_SC1); }
DEVI void st4(unsigned v, rsrc_t r, unsigned vo, unsigned so) { __builtin_amdgcn_raw_buffer_store_b32(v, r, vo, so, AUX_SC1); }
#define CB asm volatile("" ::: "memory")
#define WL0 asm volatile("s_waitcnt lgkmcnt(0)" ::: "memory")
#define WV0 asm volatile("s_waitcnt vmcnt(0)" ::: "memory")
#define BAR do { __builtin_amdgcn_s_barrier(); CB; } while (0)
#define SB __builtin_amdgcn_sched_barrier(0)
DEVI float fadd(float a, float b) { float r; asm("v_add_f32 %0, %1, %2" : "=v"(r) : "v"(a), "v"(b)); return r; }
DEVI float vfma(float a, float b, float c) { float r; asm("v_fma_f32 %0, %1, %2, %3" : "=v"(r) : "v"(a), "v"(b), "v"(c)); return r; }
DEVI unsigned vxor(unsigned a, unsigned b) { unsigned r; asm volatile("v_xor_b32 %0, %1, %2" : "=v"(r) : "i"(b), "v"(a)); return r; }
DEVI int lane_id() { int r; asm volatile("v_mbcnt_lo_u32_b32 %0, -1, 0\n\tv_mbcnt_hi_u32_b32 %0, -1, %0" : "=v"(r)); __builtin_assume(r >= 0 && r < 64); return r; }
DEVI int opq(int x) { asm volatile("" : "+v"(x)); return x; }
DEVI float vmaxf(float a, float b) { float r; asm("v_max_f32 %0, %1, %2" : "=v"(r) : "v"(a), "v"(b)); return r; }
DEVI void swap32(float& a, float& b) { auto r = __builtin_amdgcn_permlane32_swap(__float_as_uint(a), __float_as_uint(b), false, false); a = __uint_as_float(r[0]); b = __uint_as_float(r[1]); }
DEVI void swap32u(unsigned& a, unsigned& b) { auto r = __builtin_amdgcn_permlane32_swap(a, b, false, false); a = r[0]; b = r[1]; }
DEVI void swap16u(unsigned& a, unsigned& b) { auto r = __builtin_amdgcn_permlane16_swap(a, b, false, false); a = r[0]; b = r[1]; }
DEVI float hadd(float a, float b) { swap32(a, b); return fadd(a, b); }
DEVI float red32(float x) { float y = x; swap32(x, y); return x + y; }
DEVI float red16(float x) { unsigned a = __float_as_uint(x), b = a; swap16u(a, b); return __uint_as_float(a) + __uint_as_float(b); }
DEVI float mx32(float x) { float y = x; swap32(x, y); return vmaxf(x, y); }
DEVI float mx16(float x) { unsigned a = __float_as_uint(x), b = a; swap16u(a, b); return vmaxf(__uint_as_float(a), __uint_as_float(b)); }
template <int N> struct IC { static constexpr int v = N; };
template <int I, int N, class F> DEVI void sfor(F&& f) { if constexpr (I < N) { f(IC<I>{}); sfor<I + 1, N>(f); } }
#ifndef NSEG
#define NSEG 64
#endif
#ifndef LAGD
#define LAGD 10
#endif
constexpr float SC_L2E = 0.08838834764831845f * 1.4426950408889634f;
constexpr unsigned SENT = 0xFFFFFFFFu;
#ifndef NUS_
#define NUS_ 8
#endif
#ifndef NRG_
#define NRG_ 8
#endif
constexpr int NREP = 8, NUS = NUS_, NRG = NRG_, NKF = 5, NGU = 38;
constexpr unsigned XB = 4u << 20;
constexpr unsigned O_XIN = 0;
constexpr unsigned O_XN  = O_XIN + 2 * XB;
constexpr unsigned O_XM  = O_XN + 2 * XB;
constexpr unsigned O_H   = O_XM + 2 * XB;
constexpr unsigned O_D   = O_H + 2u * 64 * 224 * 1024;
constexpr unsigned O_N = O_D + 2u * 64 * NKF * 65536;
constexpr unsigned DV(unsigned o) { return ((o & ~8191u) << 1) | (o & 8191u); }
DEVI unsigned na(unsigned nb, unsigned o) { return (DV(o) + nb) ^ ((o >> 7) & 8192u); }
constexpr unsigned N_PM  = 0;
constexpr unsigned N_PMM = N_PM + NRG * 16384;
constexpr unsigned N_PA  = (1u << 20) - 16384;
constexpr unsigned N_MEM = 1u << 20;
constexpr unsigned N_WM  = 2u << 20;
constexpr unsigned N_PTX = 6u << 20;
constexpr unsigned N_U   = 10u << 20;
static_assert(NRG == 8 && NUS == 8 && NREP == 8 && O_N % (4u << 20) == 0, "");
constexpr unsigned O_FLG_END = O_N + DV(N_MEM + NREP * 16384);
constexpr unsigned N_FX  = N_PA - 8192;
constexpr unsigned N_PG  = N_FX - 8192;
constexpr unsigned N_PD  = N_PG + 40 * 128;
constexpr unsigned O_PRB = O_N + (512u << 10), O_CAL = O_N + (768u << 10);
constexpr unsigned O_SX  = O_N + DV(N_PG) - 2u * 64 * 32 * 256;
constexpr unsigned O_FLG = O_SX;
static_assert(O_PRB >= O_N + DV(N_PMM + NRG * 8192) && O_FLG >= O_CAL + 8192 && (O_FLG_END - O_FLG) % 65536 == 0, "");
constexpr unsigned OOB = 0x80000000u;
constexpr unsigned WS_NEED = O_N + DV(N_U + NUS * (2u << 20));
constexpr unsigned O_PROF = 240u << 20;
static_assert(WS_NEED <= (200u << 20), "workspace too large");

#ifdef DRY
#define WSZ(x) 0u
#else
#define WSZ(x) (x)
#endif
struct Args { const u16 *x, *mem0, *Wq, *Wk, *Wv, *Wo, *Wg, *Wu, *Wd; char* ws; u16* out; };
#ifndef SPIN_LOG
#define SPIN_LOG 18
#endif
constexpr unsigned SPIN_CAP = 1u << SPIN_LOG;
DEVI void spin_bail(unsigned& spins) {
    asm volatile("s_add_u32 %0, %0, 1\n\ts_cmp_lt_u32 %0, %1\n\ts_cbranch_scc1 1f\n\ts_endpgm\n1:" : "+s"(spins) : "n"(SPIN_CAP) : "scc");
}
template <int K> DEVI void wrl(unsigned& v, u64& t) { asm volatile("" : "+s"(t)); asm volatile("v_writelane_b32 %0, %1, %2" : "+v"(v) : "s"((unsigned)t), "n"(K)); }
#ifdef TS
#ifndef TSCLK
#define TSCLK __builtin_amdgcn_s_memrealtime
#endif
#define TSV(u, i, v) __builtin_amdgcn_raw_buffer_store_b32((unsigned)(v), mkrs(a.ws, 256u << 20), lane_id() == 0 ? O_PROF + ((blockIdx.x * 4 + w) * 128 + (u)) * 64 + (i) * 4 : OOB, 0, 0)
#define TSW(u, i) TSV(u, i, TSCLK())
#define TSC(u, i) TSV(u, i, __builtin_amdgcn_s_memtime())
#define TSD u64 tsr[16] = {}
#define TSR(k) tsr[k] = rclk()
#define TSK(k) asm volatile("s_memtime %0" : "=s"(tsr[k]))
#define TSF(u, m) do { unsigned tv_ = 0; WL0; sfor<0, 16>([&](auto K) { if constexpr (((m) >> K.v) & 1) wrl<K.v>(tv_, tsr[K.v]); }); \
    const int tl_ = lane_id(); __builtin_amdgcn_raw_buffer_store_b32(tv_, mkrs(a.ws, 256u << 20), (tl_ < 16 && (((m) >> tl_) & 1)) ? O_PROF + ((blockIdx.x * 4 + w) * 128 + (u)) * 64 + tl_ * 4 : OOB, 0, 0); } while (0)
#if TS == 4
#undef TSV
#undef TSR
#undef TSK
#define TSV(u, i, v)
#define TSR(k)
#define TSK(k)
#define FSK(G, O, D) if constexpr ((G) >= (O) && ((G) - (O)) % (D) == 0 && ((G) - (O)) / (D) < FN) asm volatile("s_memtime %0" : "=s"(tsr[((G) - (O)) / (D)]))
#define FSF(u) TSF(u, (1 << FN) - 1)
#define FSD TSD
#endif
#else
#define TSW(u, i)
#define TSC(u, i)
#define TSV(u, i, v)
#define TSD
#define TSR(k)
#define TSK(k)
#define TSF(u, m)
#endif
#if TS != 4
#define FSK(G, O, D)
#define FSF(u)
#define FSD
#endif
DEVI unsigned rslot(int s) { return (unsigned)(s & (NRG - 1)); }
DEVI u64 pend(v4u d) { return __builtin_amdgcn_uicmp(max(d[0], d[2]), SENT, 32); }
DEVI u64 pend2(v2u d) { return __builtin_amdgcn_uicmp(d[0], SENT, 32); }
DEVI u64 pend1(unsigned d) { return __builtin_amdgcn_uicmp(d, SENT, 32); }
DEVI unsigned nosent(unsigned u) { return min(u, 0xFFFFFFFEu); }
#ifndef WPOL
#define WPOL ""
#endif
template <int OFF, bool SC1> DEVI void dma1(rsrc_t rs, unsigned vo, unsigned so) {
    if constexpr (SC1) asm volatile("buffer_load_dwordx4 %0, %1, %2 offen offset:%3 sc1 lds" :: "v"(vo), "s"(rs), "s"(so), "n"(OFF) : "memory");
    else asm volatile("buffer_load_dwordx4 %0, %1, %2 offen offset:%3 " WPOL " lds" :: "v"(vo), "s"(rs), "s"(so), "n"(OFF) : "memory");
}
DEVI void setm0(unsigned v) { asm volatile("s_mov_b32 m0, %0\n\ts_nop 0" :: "s"(v) : "memory"); }
DEVI void dma4(unsigned lds, rsrc_t rs, unsigned vo, unsigned so) { setm0(lds); dma1<0, true>(rs, vo, so); dma1<1024, true>(rs, vo, so); dma1<2048, true>(rs, vo, so); dma1<3072, true>(rs, vo, so); }
DEVI void dmaw(unsigned lds, rsrc_t rs, unsigned vo, unsigned so) { setm0(lds); asm volatile("buffer_load_dword %0, %1, %2 offen sc1 lds" :: "v"(vo), "s"(rs), "s"(so) : "memory"); }
template <int N> DEVI void wvm() { asm volatile("s_waitcnt vmcnt(%0)" :: "n"(N) : "memory"); }
template <int N> DEVI void wlg() { asm volatile("s_waitcnt lgkmcnt(%0)" :: "n"(N) : "memory"); }
DEVI void dma4p(u64 c0, u64 c1, u64 c2, u64 c3, rsrc_t rs, unsigned vo, unsigned so) {
    asm volatile(
        "s_cmp_eq_u64 %[c0], 0\n\ts_cbranch_scc1 1f\n\t"
        "buffer_load_dwordx4 %[vo], %[rs], %[so] offen sc1 lds\n"
        "1:\n\ts_cmp_eq_u64 %[c1], 0\n\ts_cbranch_scc1 2f\n\t"
        "buffer_load_dwordx4 %[vo], %[rs], %[so] offen offset:1024 sc1 lds\n"
        "2:\n\ts_cmp_eq_u64 %[c2], 0\n\ts_cbranch_scc1 3f\n\t"
        "buffer_load_dwordx4 %[vo], %[rs], %[so] offen offset:2048 sc1 lds\n"
        "3:\n\ts_cmp_eq_u64 %[c3], 0\n\ts_cbranch_scc1 4f\n\t"
        "buffer_load_dwordx4 %[vo], %[rs], %[so] offen offset:3072 sc1 lds\n"
        "4:"
        :: [c0] "s"(c0), [c1] "s"(c1), [c2] "s"(c2), [c3] "s"(c3), [vo] "v"(vo), [rs] "s"(rs), [so] "s"(so)
        : "memory", "scc");
}
#ifndef PSL
#define PSL 8
#endif
DEVI void wait_prog(rsrc_t rs, unsigned vo, unsigned so, int need) {
    unsigned spins = 0;
#pragma nounroll
    for (;;) {
        CB;
        const int v = (int)ld4(rs, vo, so);
        if (__builtin_amdgcn_sicmp(v, need, 40) == 0) return;
        spin_bail(spins);
        __builtin_amdgcn_s_sleep(PSL);
    }
}

typedef __attribute__((ext_vector_type(16))) int v16i;
typedef __attribute__((ext_vector_type(16))) unsigned v16u;
#ifdef TS
constexpr unsigned RSZ = 256u << 20;
#else
constexpr unsigned RSZ = WS_NEED;
#endif
DEVI u64 rclk() { u64 t; asm volatile("s_memrealtime %0" : "=s"(t)); return t; }
DEVI unsigned tsv(u64& t) { asm volatile("s_waitcnt lgkmcnt(0)" : "+s"(t)); return (unsigned)t; }
DEVI unsigned stamp(u64& t, int tag, int lead) { asm volatile("" : "+s"(t)); return (unsigned)tag << 24 | (((unsigned)t + lead) & 0xFFFFFFu); }
#ifndef SXTH
#define SXTH 20
#endif
DEVI int left(int t) { return (int)((unsigned)(t - (int)__builtin_amdgcn_s_memrealtime()) << 8); }
DEVI void hold(int t0, int d) { for (int n = 0; n < 4096 && (int)__builtin_amdgcn_s_memrealtime() - t0 < d; ++n) __builtin_amdgcn_s_sleep(4); }
DEVI int smin(int a, int b) { int r; asm("s_min_i32 %0, %1, %2" : "=s"(r) : "s"(a), "s"(b) : "scc"); return r; }
DEVI int smax(int a, int b) { int r; asm("s_max_i32 %0, %1, %2" : "=s"(r) : "s"(a), "s"(b) : "scc"); return r; }
DEVI void smin8x2(const v16i& f, int& m0, int& m1) {
    asm("s_min_i32 %0, %2, %3\n\ts_min_i32 %1, %10, %11\n\ts_min_i32 %0, %0, %4\n\ts_min_i32 %1, %1, %12\n\ts_min_i32 %0, %0, %5\n\ts_min_i32 %1, %1, %13\n\ts_min_i32 %0, %0, %6\n\ts_min_i32 %1, %1, %14\n\t"
        "s_min_i32 %0, %0, %7\n\ts_min_i32 %1, %1, %15\n\ts_min_i32 %0, %0, %8\n\ts_min_i32 %1, %1, %16\n\ts_min_i32 %0, %0, %9\n\ts_min_i32 %1, %1, %17"
        : "=&s"(m0), "=&s"(m1) : "s"(f[0]), "s"(f[1]), "s"(f[2]), "s"(f[3]), "s"(f[4]), "s"(f[5]), "s"(f[6]), "s"(f[7]),
          "s"(f[8]), "s"(f[9]), "s"(f[10]), "s"(f[11]), "s"(f[12]), "s"(f[13]), "s"(f[14]), "s"(f[15]) : "scc");
}
DEVI void smax8x2(const v16i& f, int& m0, int& m1) {
    asm("s_max_i32 %0, %2, %3\n\ts_max_i32 %1, %10, %11\n\ts_max_i32 %0, %0, %4\n\ts_max_i32 %1, %1, %12\n\ts_max_i32 %0, %0, %5\n\ts_max_i32 %1, %1, %13\n\ts_max_i32 %0, %0, %6\n\ts_max_i32 %1, %1, %14\n\t"
        "s_max_i32 %0, %0, %7\n\ts_max_i32 %1, %1, %15\n\ts_max_i32 %0, %0, %8\n\ts_max_i32 %1, %1, %16\n\ts_max_i32 %0, %0, %9\n\ts_max_i32 %1, %1, %17"
        : "=&s"(m0), "=&s"(m1) : "s"(f[0]), "s"(f[1]), "s"(f[2]), "s"(f[3]), "s"(f[4]), "s"(f[5]), "s"(f[6]), "s"(f[7]),
          "s"(f[8]), "s"(f[9]), "s"(f[10]), "s"(f[11]), "s"(f[12]), "s"(f[13]), "s"(f[14]), "s"(f[15]) : "scc");
}
#ifndef NVK
#define NVK 8
#endif
#ifndef DLY_V
#define DLY_V 500
#endif
#ifndef PSV
#define PSV 12
#endif
#ifndef DLY_P
#define DLY_P 150
#endif
DEVI unsigned prb(int k, int b) { return O_PRB + k * 16384 + b * 8192 + (k & 1) * 4096; }
DEVI void vote(rsrc_t rs, int w, int v, int far, int t0s) {
    const int lane = lane_id();
    constexpr unsigned N = (O_FLG_END - O_FLG) / 65536;
#pragma nounroll
    for (unsigned q = 0; q < N; ++q) st16(v4u{SENT, SENT, SENT, SENT}, rs, O_FLG + ((v * 4 + w) * N + q) * 1024 + lane * 16);
    const v2u e = ld8(rs, O_CAL + (lane & 31) * 256 + 128, 0);
    static_assert(NVK % 4 == 0 && NVK < 12, "");
    int best[2] = {1 << 30, 1 << 30};
    hold(t0s, DLY_P);
#pragma nounroll
    for (int k = 0; k <= NVK / 4; ++k)
#pragma unroll
        for (int b = 0; b < 2; ++b) {
            u64 t0, t1; int d;
            asm volatile("s_memtime %0\n\ts_waitcnt lgkmcnt(0)\n\ts_buffer_load_dword %2, %3, %4 glc\n\ts_waitcnt lgkmcnt(0)\n\ts_memtime %1\n\ts_waitcnt lgkmcnt(0)"
                         : "=&s"(t0), "=&s"(t1), "=&s"(d) : "s"(rs), "s"(prb(k ? 4 * k - w : 0, b) + v * 128) : "memory");
            if (k) best[b] = smin(best[b], (int)(t1 - t0));
        }
    *LDSP(int, (lane & 1) * 16 + w * 4) = best[lane & 1];
    WV0; WL0; BAR;
#pragma unroll
    for (int b = 0; b < 2; ++b) { const v4u t = *LDSP(const v4u, b * 16); best[b] = min(min(t[0], t[1]), min(t[2], t[3])); }
    st8(v2u{((e[0] + 2) & ~1u) | ((best[1] < best[0]) ^ far), e[1] + 1}, rs, lane < 32 && w == 0 ? O_CAL + lane * 256 + v * 8 : OOB, 0);
}
DEVI void vote_end(rsrc_t rs) {
    const unsigned o = O_CAL + (lane_id() & 31) * 256 + 128;
    const v2u e = ld8(rs, o, 0);
    st8(v2u{e[0] + 2, e[1] + 1}, rs, o, 0);
}
DEVI unsigned side(rsrc_t rs, int l) {
    unsigned spins = 0;
#pragma nounroll
    for (;;) {
        v16u f[2]; v2u e;
        const unsigned o = O_CAL + (blockIdx.x & 31) * 256;
        asm volatile("s_buffer_load_dwordx16 %0, %3, %4 glc\n\ts_buffer_load_dwordx16 %1, %3, %5 glc\n\ts_buffer_load_dwordx2 %2, %3, %6 glc\n\ts_waitcnt lgkmcnt(0)"
                     : "=&s"(f[0]), "=&s"(f[1]), "=&s"(e) : "s"(rs), "s"(o), "s"(o + 64), "s"(o + 128) : "memory");
        unsigned bad = 0, sum = 0;
#pragma unroll
        for (int i = 0; i < 16; ++i) { const unsigned x = f[i >> 3][2 * i & 15]; bad |= ((x ^ (e[0] + 2)) >> 1) | (f[i >> 3][(2 * i & 15) + 1] ^ (e[1] + 1)); sum += x & 1; }
#ifdef CSIDE
        sum = CSIDE * 16;
#endif
        if (bad == 0) return opqs(O_N + ((l ^ (sum > 8)) & 1) * 8192);
        spin_bail(spins);
        __builtin_amdgcn_s_sleep(PSV);
    }
}
#ifndef PSH
#define PSH 2
#endif
#ifndef HRED
#define HRED 1
#endif
template <class F> DEVI void wait_hint(rsrc_t rs, unsigned so, unsigned vo, int tag, F&& mid) {
    int mx;
#pragma nounroll
    for (int n = 0;; ++n) {
        v16i f;
        asm volatile("s_buffer_load_dwordx16 %0, %1, %2 glc\n\ts_waitcnt lgkmcnt(0)" : "=s"(f) : "s"(rs), "s"(so) : "memory");
#if HRED
        int m0, m1;
        smin8x2(f, m0, m1);
        if (smin(m0, m1) >= tag << 24) { smax8x2(f, m0, m1); mx = smax(m0, m1); break; }
#else
        int mn = f[0]; mx = f[0];
#pragma unroll
        for (int i = 1; i < 16; ++i) { mn = smin(mn, f[i]); mx = smax(mx, f[i]); }
        if (mn >= tag << 24) break;
#endif
        if (n == 40) { wait_prog(rs, vo, 0, tag << 24); n = 0; }
        __builtin_amdgcn_s_sleep(PSH);
    }
    mid(mx);
#pragma nounroll
    for (int n = 0; n < 64; ++n) if (left(mx) <= 0) break;
}
template <class WM> constexpr int widx(int j, int t) {
    int n = 0;
    for (int q = 0; q < j * WM::NP + t; ++q) n += WM::kind(q / WM::NP, q % WM::NP) == WM::kind(j, t);
    return n;
}
template <class WM> constexpr int lds_tile(int t) { for (int j = 0; j < WM::NT; ++j) if (WM::kind(j, t) == 1) return j; return -1; }
DEVI v4u ldsq(unsigned p, int off) { return *LDSP(const v4u, LDSP(char, p) + off); }
DEVI void stsq(unsigned p, int off, v4u v) { *LDSP(v4u, LDSP(char, p) + off) = v; }
DEVI v8u ldpair(unsigned wl, int slot) { return cat8(ldsq(wl, slot * 2048), ldsq(wl, slot * 2048 + 1024)); }
DEVI rsrc_t mkrs(const void* p, unsigned bytes) { return __builtin_amdgcn_make_buffer_rsrc((void*)p, 0, (int)bytes, 0x00020000); }
constexpr int LN_NOW = 8, NP0 = (96 - LN_NOW) * 4;
template <int SL = 0> DEVI void pull(const void* p, int n, int q, int nq) {
    const rsrc_t r = mkrs(p, n * 1024);
    const unsigned vo = lane_id() * 16;
#pragma nounroll
    for (; q < n; q += nq) { asm volatile("buffer_load_dwordx4 a[0:3], %0, %1, %2 offen" :: "v"(vo), "s"(r), "s"(q * 1024) : "memory"); if (SL) __builtin_amdgcn_s_sleep(SL); }
}
#ifndef DLY_F
#define DLY_F 800
#endif
#ifndef PFS
#define PFS 5
#endif
DEVI void pull_all(const Args& a, int w, int q, int t0) {
    q = q * 4 + w;
    pull(a.Wv, 8192, q, NP0); pull(a.Wo, 8192, q, NP0); pull(a.Wk, 8192, q, NP0);
    if (q >= 256) return;
    hold(t0, DLY_F);
    pull<PFS>(q & 1 ? a.Wg : a.Wu, 7168, q >> 1, 128); pull<PFS>(a.Wd, 7168, q, 256);
}
template <class WM, int j, int ks, int O0, int O1> DEVI void put_frag(unsigned ta, unsigned wl, v8u* Wc) {
    constexpr int t = ks >> 1, f = ks & 1, k = WM::kind(j, t), ix = widx<WM>(j, t);
    if constexpr (k == 0) {
        constexpr int R = WM::RB + 8 * ix + 4 * f;
        if constexpr (R < 256) asm volatile("ds_read_b64_tr_b16 a[%1:%2], %0 offset:%5\n\tds_read_b64_tr_b16 a[%3:%4], %0 offset:%6" :: "v"(ta), "n"(R), "n"(R + 1), "n"(R + 2), "n"(R + 3), "n"(O0), "n"(O1) : "memory");
        else asm volatile("ds_read_b64_tr_b16 v[%1:%2], %0 offset:%5\n\tds_read_b64_tr_b16 v[%3:%4], %0 offset:%6" :: "v"(ta), "n"(R - 256 + NV), "n"(R - 255 + NV), "n"(R - 254 + NV), "n"(R - 253 + NV), "n"(O0), "n"(O1) : "memory");
    } else {
        v2u p, q;
        asm volatile("ds_read_b64_tr_b16 %0, %2 offset:%3\n\tds_read_b64_tr_b16 %1, %2 offset:%4\n\ts_waitcnt lgkmcnt(0)" : "=&v"(p), "=&v"(q) : "v"(ta), "n"(O0), "n"(O1) : "memory");
        const v4u F = v4u{p[0], p[1], q[0], q[1]};
        if constexpr (k == 1) stsq(wl, (WM::LB + ix) * 2048 + f * 1024, F);
        else if constexpr (f == 0) Wc[WM::CBI + ix] = cat8(F, F);
        else Wc[WM::CBI + ix] = cat8(lo4(Wc[WM::CBI + ix]), F);
    }
}
template <int NB, int DEPTH, int NI, bool SHARED = false, int SL = DEPTH, class ISSUE, class PUT>
DEVI void load_blocks(ISSUE&& issue, PUT&& put) {
    sfor<0, DEPTH - 1>([&](auto B) { issue(B, IC<B.v % SL>{}); });
    sfor<0, NB>([&](auto B) {
        constexpr int b = B.v, left = NB - 1 - b < DEPTH - 2 ? NB - 1 - b : DEPTH - 2;
        wvm<NI * left>();
        wlg<0>();
        if constexpr (SHARED) BAR;
        if constexpr (b + DEPTH - 1 < NB) issue(IC<b + DEPTH - 1>{}, IC<(b + DEPTH - 1) % SL>{});
        put(B, IC<b % SL>{});
    });
    wlg<0>();
    if constexpr (SHARED) BAR;
}
DEVI void dma_blk(unsigned lds, rsrc_t rw, unsigned vo, unsigned so, unsigned rowb) { setm0(lds); dma1<0, false>(rw, vo, so); setm0(lds + 1024); dma1<0, false>(rw, vo, so + 16 * rowb); }
template <class WM, int NKS, int K0, int K1, int RTS, int LATE1, class FILL>
DEVI void gemm_pass(unsigned xa, unsigned wl, const v8u* Wc, v4u (&x)[2][3], f32x4 (&acc)[WM::NT][2], FILL&& fill) {
    auto rd = [&](auto KS, auto Q) {
        constexpr int ks = KS.v, q = Q.v, jl = lds_tile<WM>(ks >> 1), il = widx<WM>(jl < 0 ? 0 : jl, ks >> 1);
        if constexpr (q == 0 && jl >= 0) x[ks & 1][2] = ldsq(wl, (WM::LB + il) * 2048 + (ks & 1) * 1024);
        if constexpr (q == 1) x[ks & 1][1] = ldsq(xa, RTS + ks * 1024);
        if constexpr (q == 2) x[ks & 1][0] = ldsq(xa, ks * 1024);
    };
    sfor<K0 * 2 * WM::NT, K1 * 2 * WM::NT>([&](auto N) {
        constexpr int ks = N.v / (2 * WM::NT), n = N.v % (2 * WM::NT), j = n >> 1, rt = n & 1, t = ks >> 1, f = ks & 1, k = WM::kind(j, t), ix = widx<WM>(j, t);
        fill(IC<ks>{}, IC<n>{});
        SB;
        const v4u xv = x[ks & 1][rt];
        if constexpr (k == 0) mfA<WM::RB + 8 * ix + 4 * f, ks == 0>(acc[j][rt], xv);
        else if constexpr (k == 1) mfv<ks == 0>(acc[j][rt], x[ks & 1][2], xv);
        else mfv<ks == 0>(acc[j][rt], f ? hi4(Wc[WM::CBI + ix]) : lo4(Wc[WM::CBI + ix]), xv);
        if constexpr (ks + 1 < NKS) rd(IC<ks + 1>{}, IC<n - (ks == 1 ? LATE1 : 0)>{});
        SB;
    });
}
template <class WM> constexpr int next_lds(int n) { for (; n < 16 * WM::NT; ++n) if (WM::kind(2 * (n >> 5) + (n & 1), (n >> 1) & 15) == 1) return n; return -1; }
template <class WM> constexpr int lds_cnt(int n) { int c = 0; for (int q = next_lds<WM>(0); q >= 0 && q < n; q = next_lds<WM>(q + 1)) ++c; return c; }
template <class WM> constexpr int lds_nth(int k) { int q = next_lds<WM>(0); for (; k > 0 && q >= 0; --k) q = next_lds<WM>(q + 1); return q; }
template <class WM, int K, int NB> DEVI void pair_next(v8u (&pb)[NB], unsigned wl) {
    constexpr int n = lds_nth<WM>(K);
    if constexpr (n >= 0) { constexpr int ix = widx<WM>(2 * (n >> 5) + (n & 1), (n >> 1) & 15); pb[K % NB] = ldpair(wl, WM::LB + ix); }
}
template <class WM, int MO, int MR, int NB, class FILL>
DEVI void spass(unsigned ma, unsigned wl, const v8u* Wc, f32x4* e, v4u (&m)[MR], v8u (&pb)[NB], int sidx, FILL&& fill) {
    sfor<0, 16 * WM::NT>([&](auto N) {
        constexpr int n = N.v, t = (n >> 1) & 15, j = 2 * (n >> 5) + (n & 1), k = WM::kind(j, t), ix = widx<WM>(j, t), tn = (t + MR - 1) & 15, r = (n >> 1) % MR;
        fill(N);
        SB;
        if constexpr (k == 0) smf<WM::RB + 8 * ix>(e[j], m[r], sidx);
        else if constexpr (k == 1) { constexpr int q = lds_cnt<WM>(n); smfv(e[j], m[r], pb[q % NB], sidx); pair_next<WM, q + NB>(pb, wl); }
        else smfv(e[j], m[r], Wc[WM::CBI + ix], sidx);
        if constexpr ((n & 1) == 0 && n + 2 * MR - 2 < 16 * WM::NT) m[(r + MR - 1) % MR] = ldsq(ma, MO + (tn >> 3) * 32768 + (tn & 7) * 1024);
        SB;
    });
}
typedef __attribute__((ext_vector_type(4))) short s16x4;
DEVI v2u ldtr(unsigned p, int off) { return __builtin_bit_cast(v2u, __builtin_amdgcn_ds_read_tr16_b64_v4i16(LDSP(s16x4, LDSP(char, p) + off))); }
DEVI v4u ldtr2(unsigned p, int off) { const v2u a = ldtr(p, off), b = ldtr(p, off + 256); return v4u{a[0], a[1], b[0], b[1]}; }
DEVI void sts1(unsigned p, int off, unsigned v) { *LDSP(unsigned, LDSP(char, p) + off) = v; }
DEVI unsigned lds1(unsigned p, int off) { return *LDSP(const unsigned, LDSP(char, p) + off); }
DEVI void use(unsigned v) { asm volatile("" :: "v"(v) : "memory"); }
DEVI int mk_sidx(int lane) { return opq(0x4444 + (lane & 2) * 0x5555); }
DEVI int crow(int r, int hb) { return 4 * (r >> 1) + 2 * hb + (r & 1); }
#define A_LDS 0x28a0114002004410ull
#define A_VAR 0x0000000400020001ull
#define A_LOOP \
"s_brev_b32 s99, 1\ns_brev_b32 s98, 1\ns_brev_b32 s97, 1\ns_brev_b32 s96, 1\n" \
"s_branch 4f\n.p2align 6\n1:\ns_waitcnt lgkmcnt(0)\n" \
"v_mfma_f32_16x16x32_bf16 v[44:47], v[36:39], v[80:83], v[44:47]\nds_read_b128 v[32:35], v25 offset:3072\nds_read_b128 v[76:79], v24 offset:21504\nv_mfma_f32_16x16x32_bf16 v[48:51], v[36:39], v[40:43], v[48:51]\n" \
"ds_read_b128 v[84:87], v24 offset:54272\nv_bfe_u32 v31, v24, 6, 3\nv_mfma_f32_16x16x32_bf16 v[52:55], a[160:163], v[80:83], v[52:55]\ns_mov_b32 m0, s50\n" \
"s_add_u32 s99, s30, 2\nv_lshlrev_b32 v31, 2, v31\nv_mfma_f32_16x16x32_bf16 v[56:59], a[160:163], v[40:43], v[56:59]\ns_min_u32 s99, s99, 63\n" \
"s_lshl_b32 s99, s99, 5\ns_add_u32 s99, s99, s48\nv_mfma_f32_16x16x32_bf16 v[60:63], v[120:123], v[80:83], v[60:63]\ns_and_b32 s95, s30, 7\n" \
"s_lshl_b32 s95, s95, 18\ns_add_u32 s95, s95, s33\nv_mfma_f32_16x16x32_bf16 v[64:67], v[120:123], v[40:43], v[64:67]\ns_add_u32 s31, s31, 0x10000\n" \
"v_mfma_f32_16x16x32_bf16 v[68:71], v[224:227], v[80:83], v[68:71]\ns_waitcnt lgkmcnt(1)\nv_mfma_f32_16x16x32_bf16 v[72:75], v[224:227], v[40:43], v[72:75]\ns_waitcnt lgkmcnt(0)\n" \
"ds_read_b128 v[36:39], v24 offset:22528\nv_mfma_f32_16x16x32_bf16 v[44:47], v[32:35], v[76:79], v[44:47]\nds_read_b128 v[40:43], v24 offset:55296\nv_mfma_f32_16x16x32_bf16 v[48:51], v[32:35], v[84:87], v[48:51]\n" \
"v_mfma_f32_16x16x32_bf16 v[52:55], a[164:167], v[76:79], v[52:55]\nds_read_b128 v[32:35], v25 offset:18432\nv_mfma_f32_16x16x32_bf16 v[56:59], a[164:167], v[84:87], v[56:59]\nv_mfma_f32_16x16x32_bf16 v[60:63], v[124:127], v[76:79], v[60:63]\n" \
"v_mfma_f32_16x16x32_bf16 v[64:67], v[124:127], v[84:87], v[64:67]\nv_mfma_f32_16x16x32_bf16 v[68:71], v[228:231], v[76:79], v[68:71]\ns_waitcnt lgkmcnt(1)\nv_mfma_f32_16x16x32_bf16 v[72:75], v[228:231], v[84:87], v[72:75]\n" \
"ds_read_b128 v[76:79], v24 offset:23552\nv_mfma_f32_16x16x32_bf16 v[44:47], a[64:67], v[36:39], v[44:47]\nds_read_b128 v[80:83], v24 offset:56320\nv_mfma_f32_16x16x32_bf16 v[48:51], a[64:67], v[40:43], v[48:51]\n" \
"v_mfma_f32_16x16x32_bf16 v[52:55], a[168:171], v[36:39], v[52:55]\nds_read_b128 v[84:87], v25 offset:19456\nv_mfma_f32_16x16x32_bf16 v[56:59], a[168:171], v[40:43], v[56:59]\nv_mfma_f32_16x16x32_bf16 v[60:63], v[128:131], v[36:39], v[60:63]\n" \
"s_waitcnt lgkmcnt(3)\nv_mfma_f32_16x16x32_bf16 v[64:67], v[128:131], v[40:43], v[64:67]\ns_waitcnt vmcnt(13) lgkmcnt(1)\ns_barrier\n" \
"v_mfma_f32_16x16x32_bf16 v[68:71], v[32:35], v[36:39], v[68:71]\nbuffer_load_dword v31, s[8:11], s99 offen offset:0 sc1 lds\nv_mfma_f32_16x16x32_bf16 v[72:75], v[32:35], v[40:43], v[72:75]\nds_read_b128 v[36:39], v24 offset:24576\n" \
"s_add_u32 m0, s32, 0x4000\nv_mfma_f32_16x16x32_bf16 v[44:47], a[68:71], v[76:79], v[44:47]\nds_read_b128 v[32:35], v24 offset:57344\nv_mfma_f32_16x16x32_bf16 v[48:51], a[68:71], v[80:83], v[48:51]\n" \
"ds_read_b128 v[40:43], v25 offset:12288\nv_mfma_f32_16x16x32_bf16 v[52:55], a[172:175], v[76:79], v[52:55]\nv_mfma_f32_16x16x32_bf16 v[56:59], a[172:175], v[80:83], v[56:59]\nv_mfma_f32_16x16x32_bf16 v[60:63], v[132:135], v[76:79], v[60:63]\n" \
"s_waitcnt lgkmcnt(3)\nv_mfma_f32_16x16x32_bf16 v[64:67], v[132:135], v[80:83], v[64:67]\nv_mfma_f32_16x16x32_bf16 v[68:71], v[84:87], v[76:79], v[68:71]\ns_waitcnt lgkmcnt(1)\n" \
"v_mfma_f32_16x16x32_bf16 v[72:75], v[84:87], v[80:83], v[72:75]\nds_read_b128 v[76:79], v24 offset:25600\nv_mfma_f32_16x16x32_bf16 v[44:47], a[72:75], v[36:39], v[44:47]\nds_read_b128 v[80:83], v24 offset:58368\n" \
"v_mfma_f32_16x16x32_bf16 v[48:51], a[72:75], v[32:35], v[48:51]\nds_read_b128 v[84:87], v25 offset:13312\nv_mfma_f32_16x16x32_bf16 v[52:55], a[176:179], v[36:39], v[52:55]\ns_waitcnt lgkmcnt(3)\n" \
"v_mfma_f32_16x16x32_bf16 v[56:59], a[176:179], v[32:35], v[56:59]\nv_mfma_f32_16x16x32_bf16 v[60:63], v[40:43], v[36:39], v[60:63]\nv_mfma_f32_16x16x32_bf16 v[64:67], v[40:43], v[32:35], v[64:67]\nv_mfma_f32_16x16x32_bf16 v[68:71], v[232:235], v[36:39], v[68:71]\n" \
"s_waitcnt lgkmcnt(1)\nv_mfma_f32_16x16x32_bf16 v[72:75], v[232:235], v[32:35], v[72:75]\nbuffer_load_dwordx4 v24, s[8:11], s95 offen offset:0 sc1 lds\nv_mfma_f32_16x16x32_bf16 v[44:47], a[76:79], v[76:79], v[44:47]\n" \
"ds_read_b128 v[32:35], v24 offset:26624\nbuffer_load_dwordx4 v24, s[8:11], s95 offen offset:1024 sc1 lds\nv_mfma_f32_16x16x32_bf16 v[48:51], a[76:79], v[80:83], v[48:51]\nds_read_b128 v[36:39], v24 offset:59392\n" \
"buffer_load_dwordx4 v24, s[8:11], s95 offen offset:2048 sc1 lds\nv_mfma_f32_16x16x32_bf16 v[52:55], a[180:183], v[76:79], v[52:55]\ns_waitcnt lgkmcnt(2)\nbuffer_load_dwordx4 v24, s[8:11], s95 offen offset:3072 sc1 lds\n" \
"v_mfma_f32_16x16x32_bf16 v[56:59], a[180:183], v[80:83], v[56:59]\nds_read_b128 v[40:43], v25 offset:20480\ns_add_u32 s94, s30, 0\nv_mfma_f32_16x16x32_bf16 v[60:63], v[84:87], v[76:79], v[60:63]\n" \
"s_and_b32 s94, s94, 63\ns_lshl_b32 s93, s94, 17\ns_add_u32 s93, s93, s35\nv_mfma_f32_16x16x32_bf16 v[64:67], v[84:87], v[80:83], v[64:67]\n" \
"s_and_b32 s99, s94, 16\ns_lshl_b32 s99, s99, 9\ns_xor_b32 s93, s93, s99\nv_mfma_f32_16x16x32_bf16 v[68:71], v[236:239], v[76:79], v[68:71]\n" \
"s_and_b32 s94, s94, 7\ns_lshl_b32 s94, s94, 14\ns_lshl1_add_u32 s99, s94, s34\nv_mfma_f32_16x16x32_bf16 v[72:75], v[236:239], v[80:83], v[72:75]\n" \
"s_waitcnt lgkmcnt(1)\nds_read_b128 v[76:79], v24 offset:27648\nv_mfma_f32_16x16x32_bf16 v[44:47], a[80:83], v[32:35], v[44:47]\nds_read_b128 v[80:83], v24 offset:60416\n" \
"s_or_b32 s98, s93, s36\nv_mfma_f32_16x16x32_bf16 v[48:51], a[80:83], v[36:39], v[48:51]\ns_or_b32 s97, s93, s37\ns_and_b32 s93, s93, s39\n" \
"s_andn2_b32 s94, s94, s39\nv_mfma_f32_16x16x32_bf16 v[52:55], a[184:187], v[32:35], v[52:55]\nds_read_b128 v[84:87], v25 offset:21504\ns_add_u32 s96, s93, s38\n" \
"v_mfma_f32_16x16x32_bf16 v[56:59], a[184:187], v[36:39], v[56:59]\ns_add_u32 s96, s96, s94\ns_add_u32 s30, s30, 1\ns_lshl_b32 s94, s30, 24\n" \
"v_mfma_f32_16x16x32_bf16 v[60:63], v[136:139], v[32:35], v[60:63]\ns_waitcnt lgkmcnt(3)\nds_read_b128 v[88:91], v25 offset:4096\nv_mfma_f32_16x16x32_bf16 v[64:67], v[136:139], v[36:39], v[64:67]\n" \
"s_cmp_le_u32 s30, 64\ns_cselect_b32 s51, s14, 0\nv_mfma_f32_16x16x32_bf16 v[68:71], v[40:43], v[32:35], v[68:71]\ns_waitcnt lgkmcnt(2)\n" \
"v_mfma_f32_16x16x32_bf16 v[72:75], v[40:43], v[36:39], v[72:75]\nds_read_b128 v[32:35], v24 offset:28672\nv_mfma_f32_16x16x32_bf16 v[44:47], a[84:87], v[76:79], v[44:47]\nds_read_b128 v[36:39], v24 offset:61440\n" \
"v_mfma_f32_16x16x32_bf16 v[48:51], a[84:87], v[80:83], v[48:51]\nv_mfma_f32_16x16x32_bf16 v[52:55], a[188:191], v[76:79], v[52:55]\nv_mfma_f32_16x16x32_bf16 v[56:59], a[188:191], v[80:83], v[56:59]\nv_mfma_f32_16x16x32_bf16 v[60:63], v[140:143], v[76:79], v[60:63]\n" \
"s_waitcnt lgkmcnt(2)\nds_read_b128 v[40:43], v25 offset:5120\nv_mfma_f32_16x16x32_bf16 v[64:67], v[140:143], v[80:83], v[64:67]\nv_mfma_f32_16x16x32_bf16 v[68:71], v[84:87], v[76:79], v[68:71]\n" \
"s_waitcnt lgkmcnt(1)\nv_mfma_f32_16x16x32_bf16 v[72:75], v[84:87], v[80:83], v[72:75]\nds_read_b128 v[76:79], v24 offset:29696\nv_mfma_f32_16x16x32_bf16 v[44:47], v[88:91], v[32:35], v[44:47]\n" \
"ds_read_b128 v[80:83], v24 offset:62464\nv_mfma_f32_16x16x32_bf16 v[48:51], v[88:91], v[36:39], v[48:51]\nv_mfma_f32_16x16x32_bf16 v[52:55], a[192:195], v[32:35], v[52:55]\nv_mfma_f32_16x16x32_bf16 v[56:59], a[192:195], v[36:39], v[56:59]\n" \
"v_mfma_f32_16x16x32_bf16 v[60:63], v[144:147], v[32:35], v[60:63]\nv_mfma_f32_16x16x32_bf16 v[64:67], v[144:147], v[36:39], v[64:67]\nv_mfma_f32_16x16x32_bf16 v[68:71], v[240:243], v[32:35], v[68:71]\ns_waitcnt lgkmcnt(0)\n" \
"v_mfma_f32_16x16x32_bf16 v[72:75], v[240:243], v[36:39], v[72:75]\nds_read_b128 v[32:35], v24 offset:30720\nv_add_u32 v31, s32, v24\nv_mfma_f32_16x16x32_bf16 v[44:47], v[40:43], v[76:79], v[44:47]\n" \
"ds_read_b128 v[36:39], v24 offset:63488\nv_mfma_f32_16x16x32_bf16 v[48:51], v[40:43], v[80:83], v[48:51]\nv_mfma_f32_16x16x32_bf16 v[52:55], a[196:199], v[76:79], v[52:55]\nv_mfma_f32_16x16x32_bf16 v[56:59], a[196:199], v[80:83], v[56:59]\n" \
"v_mfma_f32_16x16x32_bf16 v[60:63], v[148:151], v[76:79], v[60:63]\ns_waitcnt vmcnt(0)\nds_read_b128 v[40:43], v31 offset:16384\nds_read_b128 v[84:87], v31 offset:17408\n" \
"v_mfma_f32_16x16x32_bf16 v[64:67], v[148:151], v[80:83], v[64:67]\nds_read_b128 v[88:91], v31 offset:18432\nds_read_b128 v[92:95], v31 offset:19456\nv_mfma_f32_16x16x32_bf16 v[68:71], v[244:247], v[76:79], v[68:71]\n" \
"s_waitcnt lgkmcnt(4)\nds_read_b128 v[76:79], v24 offset:31744\nv_mfma_f32_16x16x32_bf16 v[72:75], v[244:247], v[80:83], v[72:75]\nv_mfma_f32_16x16x32_bf16 v[44:47], a[88:91], v[32:35], v[44:47]\n" \
"ds_read_b128 v[80:83], v24 offset:64512\nv_mfma_f32_16x16x32_bf16 v[48:51], a[88:91], v[36:39], v[48:51]\ns_waitcnt lgkmcnt(4)\nv_max3_u32 v96, v40, v42, v84\n" \
"v_mfma_f32_16x16x32_bf16 v[52:55], a[200:203], v[32:35], v[52:55]\ns_waitcnt lgkmcnt(2)\nv_max3_u32 v96, v96, v86, v88\nv_max3_u32 v96, v96, v90, v92\n" \
"v_mfma_f32_16x16x32_bf16 v[56:59], a[200:203], v[36:39], v[56:59]\nv_max_u32 v96, v96, v94\nv_cmp_eq_u32 vcc, -1, v96\nv_mfma_f32_16x16x32_bf16 v[60:63], v[152:155], v[32:35], v[60:63]\n" \
"ds_read_b128 v[100:103], v25 offset:0\nv_mov_b64 v[108:109], 0\nv_mfma_f32_16x16x32_bf16 v[64:67], v[152:155], v[36:39], v[64:67]\nds_read_b128 v[104:107], v25 offset:1024\n" \
"v_mov_b64 v[110:111], 0\nv_mfma_f32_16x16x32_bf16 v[68:71], v[248:251], v[32:35], v[68:71]\ns_waitcnt lgkmcnt(2)\nv_mov_b64 v[32:33], 0\n" \
"v_mov_b64 v[34:35], 0\nv_mfma_f32_16x16x32_bf16 v[72:75], v[248:251], v[36:39], v[72:75]\ns_cbranch_vccnz 2f\n3:\n" \
"v_mfma_f32_16x16x32_bf16 v[44:47], a[92:95], v[76:79], v[44:47]\ns_barrier\ns_memrealtime s[56:57]\nv_mfma_f32_16x16x32_bf16 v[52:55], a[204:207], v[76:79], v[52:55]\n" \
"ds_read_b128 v[36:39], v24 offset:20480\nv_add_u32 v31, s44, v24\nv_mfma_f32_16x16x32_bf16 v[60:63], v[156:159], v[76:79], v[60:63]\nds_read_b128 v[40:43], v24 offset:21504\n" \
"v_mfma_f32_16x16x32_bf16 v[68:71], v[252:255], v[76:79], v[68:71]\nds_read_b128 v[84:87], v24 offset:16384\nv_cvt_pk_bf16_f32 v44, v44, v52\nv_mfma_f32_16x16x32_bf16 v[48:51], a[92:95], v[80:83], v[48:51]\n" \
"ds_read_b128 v[76:79], v24 offset:22528\nv_cvt_pk_bf16_f32 v45, v45, v53\nv_mfma_f32_16x16x32_bf16 v[56:59], a[204:207], v[80:83], v[56:59]\nds_read_b128 v[88:91], v24 offset:23552\n" \
"v_cvt_pk_bf16_f32 v46, v46, v54\nv_mfma_f32_16x16x32_bf16 v[64:67], v[156:159], v[80:83], v[64:67]\nds_read_b128 v[92:95], v24 offset:17408\nv_cvt_pk_bf16_f32 v47, v47, v55\n" \
"v_mfma_f32_16x16x32_bf16 v[72:75], v[252:255], v[80:83], v[72:75]\ns_waitcnt lgkmcnt(5)\nds_write_b64 v31, v[44:45] offset:0\nv_smfmac_f32_16x16x64_bf16 v[108:111], v[36:39], v[100:107], v26\n" \
"ds_read_b128 v[52:55], v24 offset:18432\nv_cvt_pk_bf16_f32 v60, v60, v68\nv_smfmac_f32_16x16x64_bf16 v[32:35], v[36:39], a[120:127], v26\ns_waitcnt lgkmcnt(5)\n" \
"ds_write_b64 v31, v[46:47] offset:8\nv_smfmac_f32_16x16x64_bf16 v[108:111], v[40:43], a[24:31], v26\nds_read_b128 v[80:83], v24 offset:19456\nv_cvt_pk_bf16_f32 v61, v61, v69\n" \
"v_smfmac_f32_16x16x64_bf16 v[32:35], v[40:43], a[128:135], v26\nds_write_b64 v31, v[60:61] offset:32768\nv_smfmac_f32_16x16x64_bf16 v[108:111], v[84:87], v[0:7], v26\nds_read_b128 v[44:47], v24 offset:49152\n" \
"v_cvt_pk_bf16_f32 v62, v62, v70\nv_smfmac_f32_16x16x64_bf16 v[32:35], v[84:87], a[96:103], v26\ns_waitcnt lgkmcnt(5)\ns_add_u32 s95, s56, 0x76\n" \
"v_cvt_pk_bf16_f32 v63, v63, v71\nv_smfmac_f32_16x16x64_bf16 v[108:111], v[76:79], a[32:39], v26\nds_read_b128 v[68:71], v24 offset:50176\ns_and_b32 s95, s95, 0xffffff\n" \
"v_smfmac_f32_16x16x64_bf16 v[32:35], v[76:79], a[136:143], v26\nds_read_b128 v[96:99], v25 offset:6144\ns_or_b32 s95, s95, s94\nv_smfmac_f32_16x16x64_bf16 v[108:111], v[88:91], a[40:47], v26\n" \
"ds_read_b128 v[100:103], v25 offset:7168\nv_mov_b32 v60, s95\nv_smfmac_f32_16x16x64_bf16 v[32:35], v[88:91], a[144:151], v26\nds_read_b128 v[76:79], v24 offset:51200\n" \
"v_cvt_pk_bf16_f32 v48, v48, v56\nv_smfmac_f32_16x16x64_bf16 v[108:111], v[92:95], a[0:7], v26\nds_read_b128 v[84:87], v25 offset:2048\nv_cvt_pk_bf16_f32 v49, v49, v57\n" \
"v_smfmac_f32_16x16x64_bf16 v[32:35], v[92:95], v[8:15], v26\ns_waitcnt lgkmcnt(5)\nds_read_b128 v[88:91], v25 offset:3072\nv_smfmac_f32_16x16x64_bf16 v[108:111], v[52:55], a[8:15], v26\n" \
"ds_read_b128 v[104:107], v24 offset:56320\nv_cvt_pk_bf16_f32 v50, v50, v58\nv_smfmac_f32_16x16x64_bf16 v[32:35], v[52:55], a[104:111], v26\nds_write_b64 v31, v[62:63] offset:32776\n" \
"v_smfmac_f32_16x16x64_bf16 v[108:111], v[80:83], a[16:23], v26\nbuffer_store_dword v60, v29, s[8:11], s49 offen offset:0 sc1\nv_smfmac_f32_16x16x64_bf16 v[32:35], v[80:83], a[112:119], v26\nds_read_b128 v[52:55], v24 offset:52224\n" \
"v_cvt_pk_bf16_f32 v51, v51, v59\nv_smfmac_f32_16x16x64_bf16 v[108:111], v[44:47], a[48:55], v26\nds_read_b128 v[56:59], v24 offset:53248\nv_cvt_pk_bf16_f32 v64, v64, v72\n" \
"v_smfmac_f32_16x16x64_bf16 v[32:35], v[44:47], a[152:159], v26\ns_waitcnt lgkmcnt(4)\nds_write_b64 v31, v[48:49] offset:4096\nv_smfmac_f32_16x16x64_bf16 v[108:111], v[68:71], a[56:63], v26\n" \
"ds_read_b128 v[44:47], v24 offset:54272\nv_cvt_pk_bf16_f32 v65, v65, v73\nv_smfmac_f32_16x16x64_bf16 v[32:35], v[68:71], v[96:103], v26\nds_read_b128 v[60:63], v24 offset:55296\n" \
"v_cvt_pk_bf16_f32 v66, v66, v74\nv_smfmac_f32_16x16x64_bf16 v[108:111], v[76:79], v[84:91], v26\nds_read_b128 v[68:71], v25 offset:4096\nv_cvt_pk_bf16_f32 v67, v67, v75\n" \
"v_smfmac_f32_16x16x64_bf16 v[32:35], v[76:79], a[160:167], v26\ns_waitcnt lgkmcnt(5)\nds_read_b128 v[72:75], v25 offset:5120\nv_smfmac_f32_16x16x64_bf16 v[108:111], v[104:107], a[88:95], v26\n" \
"ds_write_b64 v31, v[50:51] offset:4104\nv_smfmac_f32_16x16x64_bf16 v[32:35], v[104:107], a[200:207], v26\nds_read_b128 v[76:79], v25 offset:14336\nv_mov_b64 v[48:49], 0\n" \
"v_smfmac_f32_16x16x64_bf16 v[108:111], v[52:55], a[64:71], v26\nds_read_b128 v[80:83], v25 offset:15360\nv_mov_b64 v[50:51], 0\nv_smfmac_f32_16x16x64_bf16 v[32:35], v[52:55], a[168:175], v26\n" \
"s_waitcnt lgkmcnt(5)\nds_write_b64 v31, v[64:65] offset:36864\nv_smfmac_f32_16x16x64_bf16 v[108:111], v[56:59], a[72:79], v26\nds_read_b128 v[84:87], v24 offset:51200\n" \
"v_mov_b64 v[64:65], 0\nv_smfmac_f32_16x16x64_bf16 v[32:35], v[56:59], a[176:183], v26\nds_write_b64 v31, v[66:67] offset:36872\nv_smfmac_f32_16x16x64_bf16 v[108:111], v[44:47], a[80:87], v26\n" \
"ds_read_b128 v[56:59], v24 offset:18432\nv_mov_b64 v[66:67], 0\nv_smfmac_f32_16x16x64_bf16 v[32:35], v[44:47], a[184:191], v26\ns_waitcnt lgkmcnt(5)\n" \
"ds_read_b32 v31, v28 offset:512\nv_smfmac_f32_16x16x64_bf16 v[108:111], v[60:63], v[68:75], v26\nv_add_u32 v44, s45, v24\nv_smfmac_f32_16x16x64_bf16 v[32:35], v[60:63], a[192:199], v26\n" \
"ds_read_b128 v[68:71], v24 offset:54272\nv_cndmask_b32 v44, v44, v28, s[28:29]\nv_smfmac_f32_16x16x64_bf16 v[48:51], v[40:43], a[240:247], v26\ns_waitcnt lgkmcnt(4)\n" \
"ds_read_b128 v[96:99], v25 offset:18432\nv_smfmac_f32_16x16x64_bf16 v[64:67], v[40:43], v[76:83], v26\nds_read_b128 v[100:103], v25 offset:19456\nv_add_f32 v40, v108, v110\n" \
"v_smfmac_f32_16x16x64_bf16 v[48:51], v[36:39], a[232:239], v26\nds_read_b128 v[72:75], v25 offset:20480\nv_add_f32 v41, v32, v34\nv_smfmac_f32_16x16x64_bf16 v[64:67], v[36:39], v[192:199], v26\n" \
"ds_read_b128 v[76:79], v25 offset:21504\nv_add_f32 v42, v109, v111\nv_smfmac_f32_16x16x64_bf16 v[48:51], v[84:87], v[120:127], v26\nds_read_b128 v[36:39], v24 offset:23552\n" \
"v_add_f32 v43, v33, v35\nv_smfmac_f32_16x16x64_bf16 v[64:67], v[84:87], v[224:231], v26\nds_read_b128 v[80:83], v25 offset:16384\nv_cvt_pk_bf16_f32 v32, v40, v41\n" \
"v_smfmac_f32_16x16x64_bf16 v[48:51], v[104:107], v[152:159], v26\nds_read_b128 v[84:87], v25 offset:17408\nv_cvt_pk_bf16_f32 v33, v42, v43\nv_smfmac_f32_16x16x64_bf16 v[64:67], v[104:107], v[248:255], v26\n" \
"ds_write2_b32 v27, v32, v33 offset0:0 offset1:4\nv_smfmac_f32_16x16x64_bf16 v[48:51], v[60:63], v[144:151], v26\ns_waitcnt lgkmcnt(5)\nds_read_b128 v[32:35], v24 offset:49152\n" \
"v_smfmac_f32_16x16x64_bf16 v[64:67], v[60:63], v[240:247], v26\nds_read_b128 v[104:107], v25 offset:10240\nv_cmp_eq_u32 vcc, -1, v31\nv_smfmac_f32_16x16x64_bf16 v[48:51], v[56:59], v[16:23], v26\n" \
"ds_read_b128 v[108:111], v25 offset:11264\nv_smfmac_f32_16x16x64_bf16 v[64:67], v[56:59], v[176:183], v26\nds_read_b128 v[40:43], v24 offset:50176\nv_smfmac_f32_16x16x64_bf16 v[48:51], v[52:55], v[128:135], v26\n" \
"ds_read_b128 v[56:59], v24 offset:53248\nv_smfmac_f32_16x16x64_bf16 v[64:67], v[52:55], v[96:103], v26\ns_waitcnt lgkmcnt(5)\nds_read_b128 v[96:99], v25 offset:12288\n" \
"v_smfmac_f32_16x16x64_bf16 v[48:51], v[68:71], v[136:143], v26\nds_read_b128 v[100:103], v25 offset:13312\nv_smfmac_f32_16x16x64_bf16 v[64:67], v[68:71], v[72:79], v26\nds_read_b128 v[52:55], v24 offset:19456\n" \
"v_mov_b32 v47, s43\nv_smfmac_f32_16x16x64_bf16 v[48:51], v[36:39], a[248:255], v26\ns_barrier\nv_cndmask_b32 v76, v47, 0, s[16:17]\n" \
"v_smfmac_f32_16x16x64_bf16 v[64:67], v[36:39], v[80:87], v26\ns_waitcnt lgkmcnt(4)\nds_read_b128 v[36:39], v24 offset:22528\nv_smfmac_f32_16x16x64_bf16 v[48:51], v[32:35], v[104:111], v26\n" \
"ds_read_b128 v[68:71], v25 offset:8192\nv_cndmask_b32 v77, v47, 0, s[18:19]\nv_smfmac_f32_16x16x64_bf16 v[64:67], v[32:35], v[208:215], v26\nds_read_b128 v[72:75], v25 offset:9216\n" \
"v_cndmask_b32 v78, v47, 0, s[20:21]\nv_smfmac_f32_16x16x64_bf16 v[48:51], v[40:43], v[112:119], v26\nds_read_b128 v[32:35], v24 offset:16384\nv_cndmask_b32 v79, v47, 0, s[22:23]\n" \
"v_smfmac_f32_16x16x64_bf16 v[64:67], v[40:43], v[216:223], v26\nds_read_b128 v[60:63], v44 offset:0\nv_mov_b32 v46, 0xff61b1e6\nv_smfmac_f32_16x16x64_bf16 v[48:51], v[92:95], a[216:223], v26\n" \
"ds_read_b128 v[40:43], v24 offset:57344\nv_cndmask_b32 v80, v46, v47, s[16:17]\nv_smfmac_f32_16x16x64_bf16 v[64:67], v[92:95], v[168:175], v26\ns_waitcnt lgkmcnt(6)\n" \
"ds_read_b128 v[84:87], v24 offset:61440\nv_smfmac_f32_16x16x64_bf16 v[48:51], v[56:59], v[96:103], v26\nds_read_b128 v[88:91], v44 offset:1024\nv_cndmask_b32 v81, v46, v47, s[18:19]\n" \
"v_smfmac_f32_16x16x64_bf16 v[64:67], v[56:59], v[232:239], v26\nds_read_b128 v[92:95], v24 offset:58368\nv_cndmask_b32 v82, v46, v47, s[20:21]\nv_smfmac_f32_16x16x64_bf16 v[48:51], v[52:55], a[224:231], v26\n" \
"ds_read_b128 v[56:59], v24 offset:62464\nv_cndmask_b32 v83, v46, v47, s[22:23]\nv_smfmac_f32_16x16x64_bf16 v[64:67], v[52:55], v[184:191], v26\ns_waitcnt lgkmcnt(5)\n" \
"ds_read_b128 v[96:99], v44 offset:2048\nv_smfmac_f32_16x16x64_bf16 v[48:51], v[36:39], v[68:75], v26\nds_read_b128 v[52:55], v24 offset:59392\nv_smfmac_f32_16x16x64_bf16 v[64:67], v[36:39], v[200:207], v26\n" \
"ds_read_b128 v[68:71], v24 offset:63488\nv_smfmac_f32_16x16x64_bf16 v[48:51], v[32:35], a[208:215], v26\nds_read_b128 v[36:39], v44 offset:3072\nds_read_b128 v[44:47], v24 offset:60416\n" \
"v_smfmac_f32_16x16x64_bf16 v[64:67], v[32:35], v[160:167], v26\ns_waitcnt lgkmcnt(6)\nds_read_b128 v[32:35], v24 offset:64512\nv_mfma_f32_16x16x32_bf16 v[76:79], v[40:43], v[60:63], v[76:79]\n" \
"ds_read_b128 v[72:75], v24 offset:0\nv_add_f32 v40, v48, v50\nv_add_f32 v42, v49, v51\nv_mfma_f32_16x16x32_bf16 v[80:83], v[84:87], v[60:63], v[80:83]\n" \
"s_waitcnt lgkmcnt(6)\nv_add_f32 v41, v64, v66\nv_add_f32 v43, v65, v67\nv_cvt_pk_bf16_f32 v48, v40, v41\n" \
"v_cvt_pk_bf16_f32 v49, v42, v43\nv_mfma_f32_16x16x32_bf16 v[76:79], v[92:95], v[88:91], v[76:79]\ns_waitcnt lgkmcnt(4)\nds_write2_b32 v27, v48, v49 offset0:128 offset1:132\n" \
"v_mfma_f32_16x16x32_bf16 v[80:83], v[56:59], v[88:91], v[80:83]\nds_read_b128 v[40:43], v24 offset:32768\nv_mfma_f32_16x16x32_bf16 v[76:79], v[52:55], v[96:99], v[76:79]\ns_waitcnt lgkmcnt(4)\n" \
"s_cmp_eq_u64 vcc, 0\ns_cselect_b32 s14, s10, 0\nv_mfma_f32_16x16x32_bf16 v[80:83], v[68:71], v[96:99], v[80:83]\ns_cmp_eq_u32 s51, 0\n" \
"s_cbranch_scc1 4f\n5:\nv_mfma_f32_16x16x32_bf16 v[76:79], v[44:47], v[36:39], v[76:79]\ns_waitcnt lgkmcnt(1)\n" \
"s_barrier\nds_read_b128 v[48:51], v28 offset:512\nds_read_b128 v[56:59], v28 offset:1536\nv_mfma_f32_16x16x32_bf16 v[80:83], v[32:35], v[36:39], v[80:83]\n" \
"ds_read_b128 v[64:67], v28 offset:2560\nds_read_b128 v[32:35], v28 offset:3584\nv_mfma_f32_16x16x32_bf16 v[44:47], v[0:3], v[72:75], 0\nds_read_b128 v[84:87], v24 offset:1024\n" \
"v_max3_f32 v31, v76, v77, v78\nv_max3_f32 v31, v31, v79, s47\nv_mfma_f32_16x16x32_bf16 v[52:55], a[96:99], v[72:75], 0\ns_waitcnt lgkmcnt(4)\n" \
"ds_read_b128 v[92:95], v24 offset:33792\nv_max3_f32 v100, v80, v81, v82\nv_max3_f32 v101, v31, v100, v83\nv_max3_f32 v31, v31, v100, v83\n" \
"v_mul_f32 v100, s46, v31\nv_fma_f32 v76, v76, s46, -v100\nv_mfma_f32_16x16x32_bf16 v[104:107], v[48:51], v[60:63], 0\ns_waitcnt lgkmcnt(4)\n" \
"v_permlane32_swap_b32 v31, v101\nv_exp_f32 v76, v76\nv_fma_f32 v78, v78, s46, -v100\nv_mfma_f32_16x16x32_bf16 v[104:107], v[56:59], v[88:91], v[104:107]\n" \
"s_waitcnt lgkmcnt(2)\nv_exp_f32 v78, v78\nv_mfma_f32_16x16x32_bf16 v[104:107], v[64:67], v[96:99], v[104:107]\nv_fma_f32 v77, v77, s46, -v100\n" \
"v_fma_f32 v79, v79, s46, -v100\nv_mfma_f32_16x16x32_bf16 v[104:107], v[32:35], v[36:39], v[104:107]\nv_fma_f32 v80, v80, s46, -v100\nv_fma_f32 v82, v82, s46, -v100\n" \
"v_mfma_f32_16x16x32_bf16 v[60:63], a[208:211], v[72:75], 0\nv_exp_f32 v77, v77\nv_mfma_f32_16x16x32_bf16 v[68:71], v[160:163], v[72:75], 0\nv_max3_f32 v32, v104, v105, v106\n" \
"v_max3_f32 v31, v31, v101, v107\nv_max_f32 v33, v32, v31\nv_max_f32 v32, v32, v31\nv_exp_f32 v79, v79\n" \
"v_fma_f32 v81, v81, s46, -v100\nv_fma_f32 v83, v83, s46, -v100\nv_mfma_f32_16x16x32_bf16 v[48:51], v[0:3], v[40:43], 0\nv_permlane16_swap_b32 v32, v33\n" \
"v_max_f32 v32, v32, v33\nv_exp_f32 v80, v80\nv_exp_f32 v82, v82\nv_cndmask_b32 v34, v104, v106, s[26:27]\n" \
"v_fma_f32 v100, v32, -s46, v100\nv_mul_f32 v32, s46, v32\nv_exp_f32 v81, v81\nv_exp_f32 v83, v83\n" \
"v_cndmask_b32 v35, v105, v107, s[26:27]\nv_fma_f32 v34, v34, s46, -v32\nv_add_f32 v36, v76, v78\nv_add_f32 v37, v77, v79\n" \
"v_mfma_f32_16x16x32_bf16 v[56:59], a[96:99], v[40:43], 0\nv_fma_f32 v35, v35, s46, -v32\nv_add_f32 v38, v80, v82\nv_add_f32 v36, v36, v37\n" \
"v_cndmask_b32 v88, v76, v78, s[24:25]\nv_cndmask_b32 v89, v77, v79, s[24:25]\nv_cndmask_b32 v90, v80, v82, s[24:25]\nv_cndmask_b32 v91, v81, v83, s[24:25]\n" \
"v_exp_f32 v100, v100\nv_exp_f32 v34, v34\nv_add_f32 v39, v81, v83\nv_add_f32 v36, v36, v38\n" \
"v_mfma_f32_16x16x32_bf16 v[64:67], a[208:211], v[40:43], 0\ns_waitcnt lgkmcnt(0)\nv_exp_f32 v35, v35\nv_add_f32 v36, v36, v39\n" \
"v_fma_f32 v36, v36, v100, v34\nv_mfma_f32_16x16x32_bf16 v[72:75], v[160:163], v[40:43], 0\nv_add_f32 v37, v36, v35\nv_add_f32 v36, v35, v36\n" \
"v_mfma_f32_16x16x32_bf16 v[44:47], v[4:7], v[84:87], v[44:47]\nds_read_b128 v[40:43], v24 offset:2048\nv_permlane16_swap_b32 v36, v37\nv_mfma_f32_16x16x32_bf16 v[48:51], v[4:7], v[92:95], v[48:51]\n" \
"ds_read_b128 v[96:99], v24 offset:34816\nv_add_f32 v38, v36, v37\nv_add_f32 v36, v37, v36\nv_mfma_f32_16x16x32_bf16 v[52:55], a[100:103], v[84:87], v[52:55]\n" \
"v_mfma_f32_16x16x32_bf16 v[56:59], a[100:103], v[92:95], v[56:59]\nv_permlane32_swap_b32 v36, v38\nv_add_f32 v36, v36, v38\nv_mfma_f32_16x16x32_bf16 v[60:63], a[212:215], v[84:87], v[60:63]\n" \
"v_rcp_f32 v36, v36\nv_mfma_f32_16x16x32_bf16 v[64:67], a[212:215], v[92:95], v[64:67]\nv_mul_f32 v100, v100, v36\nv_mul_f32 v88, v88, v100\n" \
"v_mul_f32 v89, v89, v100\nv_mul_f32 v34, v34, v36\nv_mul_f32 v35, v35, v36\nv_mfma_f32_16x16x32_bf16 v[68:71], v[164:167], v[84:87], v[68:71]\n" \
"s_waitcnt lgkmcnt(0)\nv_mul_f32 v90, v90, v100\nv_mul_f32 v91, v91, v100\nv_cvt_pk_bf16_f32 v32, v88, v89\n" \
"v_cvt_pk_bf16_f32 v31, v34, v35\nv_cvt_pk_bf16_f32 v33, v90, v91\nv_min_u32 v32, -2, v32\nv_min_u32 v31, -2, v31\n" \
"v_mfma_f32_16x16x32_bf16 v[72:75], v[164:167], v[92:95], v[72:75]\nbuffer_store_dwordx2 v[32:33], v29, s[8:11], s99 offen offset:0 sc1\nbuffer_store_dword v31, v30, s[8:11], s96 offen offset:1536 sc1\nv_mfma_f32_16x16x32_bf16 v[44:47], a[0:3], v[40:43], v[44:47]\n" \
"ds_read_b128 v[84:87], v24 offset:3072\nv_mul_f32 v76, v76, v100\nv_mfma_f32_16x16x32_bf16 v[48:51], a[0:3], v[96:99], v[48:51]\nds_read_b128 v[32:35], v24 offset:35840\n" \
"v_mul_f32 v77, v77, v100\nv_mfma_f32_16x16x32_bf16 v[52:55], v[8:11], v[40:43], v[52:55]\nv_mul_f32 v78, v78, v100\nv_mul_f32 v79, v79, v100\n" \
"v_mfma_f32_16x16x32_bf16 v[56:59], v[8:11], v[96:99], v[56:59]\nv_cvt_pk_bf16_f32 v88, v76, v77\nv_cvt_pk_bf16_f32 v89, v78, v79\nv_mfma_f32_16x16x32_bf16 v[60:63], a[216:219], v[40:43], v[60:63]\n" \
"buffer_store_dwordx2 v[88:89], v29, s[8:11], s97 offen offset:1024 sc1\nv_mul_f32 v80, v80, v100\nv_mfma_f32_16x16x32_bf16 v[64:67], a[216:219], v[96:99], v[64:67]\ns_add_u32 m0, s32, 0x4000\n" \
"v_mul_f32 v81, v81, v100\nv_mul_f32 v82, v82, v100\nv_mfma_f32_16x16x32_bf16 v[68:71], v[168:171], v[40:43], v[68:71]\ns_add_u32 s99, s31, 0x4000\n" \
"v_mul_f32 v83, v83, v100\nv_cvt_pk_bf16_f32 v90, v80, v81\nv_mfma_f32_16x16x32_bf16 v[72:75], v[168:171], v[96:99], v[72:75]\ns_waitcnt lgkmcnt(0)\n" \
"ds_read_b128 v[36:39], v24 offset:4096\nv_mfma_f32_16x16x32_bf16 v[44:47], a[4:7], v[84:87], v[44:47]\nds_read_b128 v[40:43], v24 offset:36864\nv_cvt_pk_bf16_f32 v91, v82, v83\n" \
"v_mfma_f32_16x16x32_bf16 v[48:51], a[4:7], v[32:35], v[48:51]\nbuffer_store_dwordx4 v[88:91], v24, s[8:11], s98 offen offset:0 sc1\nbuffer_load_dwordx4 v24, s[8:11], s99 offen offset:0 sc1 lds\nv_mfma_f32_16x16x32_bf16 v[52:55], v[12:15], v[84:87], v[52:55]\n" \
"v_mfma_f32_16x16x32_bf16 v[56:59], v[12:15], v[32:35], v[56:59]\nv_mfma_f32_16x16x32_bf16 v[60:63], a[220:223], v[84:87], v[60:63]\nv_mfma_f32_16x16x32_bf16 v[64:67], a[220:223], v[32:35], v[64:67]\nbuffer_load_dwordx4 v24, s[8:11], s99 offen offset:1024 sc1 lds\n" \
"v_mfma_f32_16x16x32_bf16 v[68:71], v[172:175], v[84:87], v[68:71]\ns_waitcnt lgkmcnt(0)\nv_mfma_f32_16x16x32_bf16 v[72:75], v[172:175], v[32:35], v[72:75]\nds_read_b128 v[76:79], v24 offset:5120\n" \
"v_mfma_f32_16x16x32_bf16 v[44:47], a[8:11], v[36:39], v[44:47]\nds_read_b128 v[32:35], v24 offset:37888\nv_mfma_f32_16x16x32_bf16 v[48:51], a[8:11], v[40:43], v[48:51]\nbuffer_load_dwordx4 v24, s[8:11], s99 offen offset:2048 sc1 lds\n" \
"v_mfma_f32_16x16x32_bf16 v[52:55], a[104:107], v[36:39], v[52:55]\nv_mfma_f32_16x16x32_bf16 v[56:59], a[104:107], v[40:43], v[56:59]\nv_mfma_f32_16x16x32_bf16 v[60:63], v[16:19], v[36:39], v[60:63]\nv_mfma_f32_16x16x32_bf16 v[64:67], v[16:19], v[40:43], v[64:67]\n" \
"buffer_load_dwordx4 v24, s[8:11], s99 offen offset:3072 sc1 lds\nv_mfma_f32_16x16x32_bf16 v[68:71], v[176:179], v[36:39], v[68:71]\ns_waitcnt lgkmcnt(0)\nv_mfma_f32_16x16x32_bf16 v[72:75], v[176:179], v[40:43], v[72:75]\n" \
"ds_read_b128 v[36:39], v24 offset:6144\nv_mfma_f32_16x16x32_bf16 v[44:47], a[12:15], v[76:79], v[44:47]\nds_read_b128 v[40:43], v24 offset:38912\nv_mfma_f32_16x16x32_bf16 v[48:51], a[12:15], v[32:35], v[48:51]\n" \
"v_mfma_f32_16x16x32_bf16 v[52:55], a[108:111], v[76:79], v[52:55]\nv_mfma_f32_16x16x32_bf16 v[56:59], a[108:111], v[32:35], v[56:59]\nv_mfma_f32_16x16x32_bf16 v[60:63], v[20:23], v[76:79], v[60:63]\nv_mfma_f32_16x16x32_bf16 v[64:67], v[20:23], v[32:35], v[64:67]\n" \
"v_mfma_f32_16x16x32_bf16 v[68:71], v[180:183], v[76:79], v[68:71]\ns_waitcnt lgkmcnt(0)\nv_mfma_f32_16x16x32_bf16 v[72:75], v[180:183], v[32:35], v[72:75]\nds_read_b128 v[76:79], v24 offset:7168\n" \
"v_mfma_f32_16x16x32_bf16 v[44:47], a[16:19], v[36:39], v[44:47]\nds_read_b128 v[32:35], v24 offset:39936\nv_mfma_f32_16x16x32_bf16 v[48:51], a[16:19], v[40:43], v[48:51]\ns_add_u32 m0, s32, 0x6000\n" \
"s_add_u32 s99, s31, 0x6000\nv_mfma_f32_16x16x32_bf16 v[52:55], a[112:115], v[36:39], v[52:55]\nv_mfma_f32_16x16x32_bf16 v[56:59], a[112:115], v[40:43], v[56:59]\nv_mfma_f32_16x16x32_bf16 v[60:63], a[224:227], v[36:39], v[60:63]\n" \
"ds_read_b128 v[80:83], v25 offset:0\nv_mfma_f32_16x16x32_bf16 v[64:67], a[224:227], v[40:43], v[64:67]\nbuffer_load_dwordx4 v24, s[8:11], s99 offen offset:0 sc1 lds\nv_mfma_f32_16x16x32_bf16 v[68:71], v[184:187], v[36:39], v[68:71]\n" \
"s_add_u32 s98, s30, 1\ns_and_b32 s98, s98, 63\ns_lshl_b32 s97, s98, 17\nv_mfma_f32_16x16x32_bf16 v[72:75], v[184:187], v[40:43], v[72:75]\n" \
"s_waitcnt lgkmcnt(1)\nds_read_b128 v[36:39], v24 offset:8192\nv_mfma_f32_16x16x32_bf16 v[44:47], a[20:23], v[76:79], v[44:47]\nds_read_b128 v[40:43], v24 offset:40960\n" \
"s_add_u32 s97, s97, s35\nv_mfma_f32_16x16x32_bf16 v[48:51], a[20:23], v[32:35], v[48:51]\nbuffer_load_dwordx4 v24, s[8:11], s99 offen offset:1024 sc1 lds\nv_mfma_f32_16x16x32_bf16 v[52:55], a[116:119], v[76:79], v[52:55]\n" \
"s_and_b32 s96, s98, 16\ns_lshl_b32 s96, s96, 9\ns_xor_b32 s97, s97, s96\nv_mfma_f32_16x16x32_bf16 v[56:59], a[116:119], v[32:35], v[56:59]\n" \
"s_and_b32 s98, s98, 7\ns_lshl_b32 s96, s98, 20\ns_and_b32 s95, s98, 2\nv_mfma_f32_16x16x32_bf16 v[60:63], a[228:231], v[76:79], v[60:63]\n" \
"ds_read_b128 v[84:87], v25 offset:1024\ns_lshl_b32 s98, s98, s41\nv_mfma_f32_16x16x32_bf16 v[64:67], a[228:231], v[32:35], v[64:67]\ns_and_b32 s97, s97, s39\n" \
"s_andn2_b32 s98, s98, s39\ns_add_u32 s97, s97, s40\nv_mfma_f32_16x16x32_bf16 v[68:71], v[188:191], v[76:79], v[68:71]\ns_add_u32 s97, s97, s98\n" \
"buffer_load_dwordx4 v24, s[8:11], s99 offen offset:2048 sc1 lds\nv_mfma_f32_16x16x32_bf16 v[72:75], v[188:191], v[32:35], v[72:75]\ns_waitcnt lgkmcnt(1)\nds_read_b128 v[32:35], v24 offset:9216\n" \
"v_mfma_f32_16x16x32_bf16 v[44:47], v[80:83], v[36:39], v[44:47]\nds_read_b128 v[76:79], v24 offset:41984\ns_add_u32 s96, s96, s55\nv_mfma_f32_16x16x32_bf16 v[48:51], v[80:83], v[40:43], v[48:51]\n" \
"buffer_load_dwordx4 v24, s[8:11], s99 offen offset:3072 sc1 lds\nv_mfma_f32_16x16x32_bf16 v[52:55], a[120:123], v[36:39], v[52:55]\ns_lshl_b32 s95, s95, 12\ns_xor_b32 s96, s96, s95\n" \
"v_mfma_f32_16x16x32_bf16 v[56:59], a[120:123], v[40:43], v[56:59]\nv_mfma_f32_16x16x32_bf16 v[60:63], a[232:235], v[36:39], v[60:63]\nv_mfma_f32_16x16x32_bf16 v[64:67], a[232:235], v[40:43], v[64:67]\nv_and_b32 v31, s42, v24\n" \
"v_mov_b64 v[80:81], -1\nv_mfma_f32_16x16x32_bf16 v[68:71], v[192:195], v[36:39], v[68:71]\ns_waitcnt lgkmcnt(0)\nv_mov_b64 v[82:83], -1\n" \
"v_lshl_add_u32 v31, v31, 21, v24\nv_mfma_f32_16x16x32_bf16 v[72:75], v[192:195], v[40:43], v[72:75]\nds_read_b128 v[36:39], v24 offset:10240\nv_mfma_f32_16x16x32_bf16 v[44:47], v[84:87], v[32:35], v[44:47]\n" \
"ds_read_b128 v[40:43], v24 offset:43008\nv_mfma_f32_16x16x32_bf16 v[48:51], v[84:87], v[76:79], v[48:51]\nbuffer_store_dwordx4 v[80:83], v31, s[8:11], s97 offen offset:0 sc1\nv_mfma_f32_16x16x32_bf16 v[52:55], a[124:127], v[32:35], v[52:55]\n" \
"ds_read_b128 v[84:87], v25 offset:14336\nv_mfma_f32_16x16x32_bf16 v[56:59], a[124:127], v[76:79], v[56:59]\nv_mfma_f32_16x16x32_bf16 v[60:63], a[236:239], v[32:35], v[60:63]\nv_mfma_f32_16x16x32_bf16 v[64:67], a[236:239], v[76:79], v[64:67]\n" \
"v_mfma_f32_16x16x32_bf16 v[68:71], v[196:199], v[32:35], v[68:71]\ns_waitcnt lgkmcnt(1)\nv_mfma_f32_16x16x32_bf16 v[72:75], v[196:199], v[76:79], v[72:75]\nds_read_b128 v[32:35], v24 offset:11264\n" \
"v_mfma_f32_16x16x32_bf16 v[44:47], a[24:27], v[36:39], v[44:47]\nds_read_b128 v[76:79], v24 offset:44032\nv_mfma_f32_16x16x32_bf16 v[48:51], a[24:27], v[40:43], v[48:51]\nbuffer_store_dwordx4 v[80:83], v24, s[8:11], s96 offen offset:0 sc1\n" \
"v_mfma_f32_16x16x32_bf16 v[52:55], a[128:131], v[36:39], v[52:55]\nds_read_b128 v[88:91], v25 offset:15360\nv_mfma_f32_16x16x32_bf16 v[56:59], a[128:131], v[40:43], v[56:59]\nv_mfma_f32_16x16x32_bf16 v[60:63], a[240:243], v[36:39], v[60:63]\n" \
"s_waitcnt lgkmcnt(3)\nv_mfma_f32_16x16x32_bf16 v[64:67], a[240:243], v[40:43], v[64:67]\nv_mfma_f32_16x16x32_bf16 v[68:71], v[84:87], v[36:39], v[68:71]\ns_waitcnt lgkmcnt(1)\n" \
"v_mfma_f32_16x16x32_bf16 v[72:75], v[84:87], v[40:43], v[72:75]\nds_read_b128 v[36:39], v24 offset:12288\nv_mfma_f32_16x16x32_bf16 v[44:47], a[28:31], v[32:35], v[44:47]\nds_read_b128 v[40:43], v24 offset:45056\n" \
"v_mfma_f32_16x16x32_bf16 v[48:51], a[28:31], v[76:79], v[48:51]\nbuffer_store_dwordx4 v[80:83], v24, s[8:11], s96 offen offset:1024 sc1\nds_read_b128 v[84:87], v25 offset:8192\nv_mfma_f32_16x16x32_bf16 v[52:55], a[132:135], v[32:35], v[52:55]\n" \
"v_mfma_f32_16x16x32_bf16 v[56:59], a[132:135], v[76:79], v[56:59]\nv_mfma_f32_16x16x32_bf16 v[60:63], a[244:247], v[32:35], v[60:63]\ns_waitcnt lgkmcnt(3)\nv_mfma_f32_16x16x32_bf16 v[64:67], a[244:247], v[76:79], v[64:67]\n" \
"v_mfma_f32_16x16x32_bf16 v[68:71], v[88:91], v[32:35], v[68:71]\ns_waitcnt lgkmcnt(1)\nv_mfma_f32_16x16x32_bf16 v[72:75], v[88:91], v[76:79], v[72:75]\nds_read_b128 v[32:35], v24 offset:13312\n" \
"v_mfma_f32_16x16x32_bf16 v[44:47], a[32:35], v[36:39], v[44:47]\nds_read_b128 v[76:79], v24 offset:46080\nv_mfma_f32_16x16x32_bf16 v[48:51], a[32:35], v[40:43], v[48:51]\nbuffer_store_dwordx4 v[80:83], v24, s[8:11], s96 offen offset:2048 sc1\n" \
"ds_read_b128 v[88:91], v25 offset:9216\nv_mfma_f32_16x16x32_bf16 v[52:55], a[136:139], v[36:39], v[52:55]\ns_waitcnt lgkmcnt(3)\nv_mfma_f32_16x16x32_bf16 v[56:59], a[136:139], v[40:43], v[56:59]\n" \
"v_mfma_f32_16x16x32_bf16 v[60:63], v[84:87], v[36:39], v[60:63]\nv_mfma_f32_16x16x32_bf16 v[64:67], v[84:87], v[40:43], v[64:67]\nv_mfma_f32_16x16x32_bf16 v[68:71], v[200:203], v[36:39], v[68:71]\ns_waitcnt lgkmcnt(1)\n" \
"v_mfma_f32_16x16x32_bf16 v[72:75], v[200:203], v[40:43], v[72:75]\nds_read_b128 v[36:39], v24 offset:14336\nv_mfma_f32_16x16x32_bf16 v[44:47], a[36:39], v[32:35], v[44:47]\nds_read_b128 v[40:43], v24 offset:47104\n" \
"v_mfma_f32_16x16x32_bf16 v[48:51], a[36:39], v[76:79], v[48:51]\nbuffer_store_dwordx4 v[80:83], v24, s[8:11], s96 offen offset:3072 sc1\nv_mfma_f32_16x16x32_bf16 v[52:55], a[140:143], v[32:35], v[52:55]\ns_waitcnt lgkmcnt(2)\n" \
"ds_read_b128 v[80:83], v25 offset:16384\nv_mfma_f32_16x16x32_bf16 v[56:59], a[140:143], v[76:79], v[56:59]\nv_mfma_f32_16x16x32_bf16 v[60:63], v[88:91], v[32:35], v[60:63]\nv_mfma_f32_16x16x32_bf16 v[64:67], v[88:91], v[76:79], v[64:67]\n" \
"v_mfma_f32_16x16x32_bf16 v[68:71], v[204:207], v[32:35], v[68:71]\ns_waitcnt lgkmcnt(1)\nv_mfma_f32_16x16x32_bf16 v[72:75], v[204:207], v[76:79], v[72:75]\nds_read_b128 v[32:35], v24 offset:15360\n" \
"v_mfma_f32_16x16x32_bf16 v[44:47], a[40:43], v[36:39], v[44:47]\nds_read_b128 v[76:79], v24 offset:48128\nv_mfma_f32_16x16x32_bf16 v[48:51], a[40:43], v[40:43], v[48:51]\nv_mfma_f32_16x16x32_bf16 v[52:55], a[144:147], v[36:39], v[52:55]\n" \
"ds_read_b128 v[84:87], v25 offset:17408\nv_mfma_f32_16x16x32_bf16 v[56:59], a[144:147], v[40:43], v[56:59]\nv_mfma_f32_16x16x32_bf16 v[60:63], a[248:251], v[36:39], v[60:63]\ns_waitcnt lgkmcnt(3)\n" \
"v_mfma_f32_16x16x32_bf16 v[64:67], a[248:251], v[40:43], v[64:67]\ns_waitcnt vmcnt(9) lgkmcnt(1)\ns_barrier\nv_mfma_f32_16x16x32_bf16 v[68:71], v[80:83], v[36:39], v[68:71]\n" \
"s_add_u32 m0, s32, 0\ns_add_u32 s99, s31, 0x10000\nv_mfma_f32_16x16x32_bf16 v[72:75], v[80:83], v[40:43], v[72:75]\nds_read_b128 v[36:39], v24 offset:16384\n" \
"v_mfma_f32_16x16x32_bf16 v[44:47], a[44:47], v[32:35], v[44:47]\nds_read_b128 v[40:43], v24 offset:49152\nv_mfma_f32_16x16x32_bf16 v[48:51], a[44:47], v[76:79], v[48:51]\nds_read_b128 v[80:83], v25 offset:10240\n" \
"v_mfma_f32_16x16x32_bf16 v[52:55], a[148:151], v[32:35], v[52:55]\nbuffer_load_dwordx4 v24, s[12:15], s99 offen offset:0 sc1 lds\nv_mfma_f32_16x16x32_bf16 v[56:59], a[148:151], v[76:79], v[56:59]\nv_mfma_f32_16x16x32_bf16 v[60:63], a[252:255], v[32:35], v[60:63]\n" \
"s_waitcnt lgkmcnt(3)\nv_mfma_f32_16x16x32_bf16 v[64:67], a[252:255], v[76:79], v[64:67]\nbuffer_load_dwordx4 v24, s[12:15], s99 offen offset:1024 sc1 lds\nv_mfma_f32_16x16x32_bf16 v[68:71], v[84:87], v[32:35], v[68:71]\n" \
"s_waitcnt lgkmcnt(1)\nv_mfma_f32_16x16x32_bf16 v[72:75], v[84:87], v[76:79], v[72:75]\nds_read_b128 v[32:35], v24 offset:17408\nv_mfma_f32_16x16x32_bf16 v[44:47], a[48:51], v[36:39], v[44:47]\n" \
"ds_read_b128 v[76:79], v24 offset:50176\nv_mfma_f32_16x16x32_bf16 v[48:51], a[48:51], v[40:43], v[48:51]\nds_read_b128 v[84:87], v25 offset:11264\nv_mfma_f32_16x16x32_bf16 v[52:55], a[152:155], v[36:39], v[52:55]\n" \
"s_waitcnt lgkmcnt(3)\nbuffer_load_dwordx4 v24, s[12:15], s99 offen offset:2048 sc1 lds\nv_mfma_f32_16x16x32_bf16 v[56:59], a[152:155], v[40:43], v[56:59]\nv_mfma_f32_16x16x32_bf16 v[60:63], v[80:83], v[36:39], v[60:63]\n" \
"v_mfma_f32_16x16x32_bf16 v[64:67], v[80:83], v[40:43], v[64:67]\nbuffer_load_dwordx4 v24, s[12:15], s99 offen offset:3072 sc1 lds\nv_mfma_f32_16x16x32_bf16 v[68:71], v[208:211], v[36:39], v[68:71]\ns_waitcnt lgkmcnt(1)\n" \
"ds_read_b128 v[36:39], v25 offset:6144\nv_mfma_f32_16x16x32_bf16 v[72:75], v[208:211], v[40:43], v[72:75]\nds_read_b128 v[80:83], v24 offset:18432\nv_mfma_f32_16x16x32_bf16 v[44:47], a[52:55], v[32:35], v[44:47]\n" \
"ds_read_b128 v[40:43], v24 offset:51200\ns_add_u32 m0, s32, 0x2000\nv_mfma_f32_16x16x32_bf16 v[48:51], a[52:55], v[76:79], v[48:51]\ns_add_u32 s99, s31, 0x12000\n" \
"v_mfma_f32_16x16x32_bf16 v[52:55], a[156:159], v[32:35], v[52:55]\ns_waitcnt lgkmcnt(3)\nv_mfma_f32_16x16x32_bf16 v[56:59], a[156:159], v[76:79], v[56:59]\nv_mfma_f32_16x16x32_bf16 v[60:63], v[84:87], v[32:35], v[60:63]\n" \
"buffer_load_dwordx4 v24, s[12:15], s99 offen offset:0 sc1 lds\nv_mfma_f32_16x16x32_bf16 v[64:67], v[84:87], v[76:79], v[64:67]\nv_mfma_f32_16x16x32_bf16 v[68:71], v[212:215], v[32:35], v[68:71]\ns_waitcnt lgkmcnt(0)\n" \
"ds_read_b128 v[32:35], v25 offset:7168\nv_mfma_f32_16x16x32_bf16 v[72:75], v[212:215], v[76:79], v[72:75]\nds_read_b128 v[84:87], v24 offset:19456\nv_mfma_f32_16x16x32_bf16 v[44:47], a[56:59], v[80:83], v[44:47]\n" \
"ds_read_b128 v[76:79], v24 offset:52224\nv_mfma_f32_16x16x32_bf16 v[48:51], a[56:59], v[40:43], v[48:51]\nbuffer_load_dwordx4 v24, s[12:15], s99 offen offset:1024 sc1 lds\nv_mfma_f32_16x16x32_bf16 v[52:55], v[36:39], v[80:83], v[52:55]\n" \
"v_mfma_f32_16x16x32_bf16 v[56:59], v[36:39], v[40:43], v[56:59]\nv_mfma_f32_16x16x32_bf16 v[60:63], v[112:115], v[80:83], v[60:63]\nds_read_b128 v[36:39], v25 offset:2048\nv_mfma_f32_16x16x32_bf16 v[64:67], v[112:115], v[40:43], v[64:67]\n" \
"buffer_load_dwordx4 v24, s[12:15], s99 offen offset:2048 sc1 lds\nv_mfma_f32_16x16x32_bf16 v[68:71], v[216:219], v[80:83], v[68:71]\ns_waitcnt lgkmcnt(1)\nv_mfma_f32_16x16x32_bf16 v[72:75], v[216:219], v[40:43], v[72:75]\n" \
"ds_read_b128 v[80:83], v24 offset:20480\nv_mfma_f32_16x16x32_bf16 v[44:47], a[60:63], v[84:87], v[44:47]\nds_read_b128 v[40:43], v24 offset:53248\nv_mfma_f32_16x16x32_bf16 v[48:51], a[60:63], v[76:79], v[48:51]\n" \
"buffer_load_dwordx4 v24, s[12:15], s99 offen offset:3072 sc1 lds\nv_mfma_f32_16x16x32_bf16 v[52:55], v[32:35], v[84:87], v[52:55]\nv_mfma_f32_16x16x32_bf16 v[56:59], v[32:35], v[76:79], v[56:59]\nv_mfma_f32_16x16x32_bf16 v[60:63], v[116:119], v[84:87], v[60:63]\n" \
"v_mfma_f32_16x16x32_bf16 v[64:67], v[116:119], v[76:79], v[64:67]\nv_mfma_f32_16x16x32_bf16 v[68:71], v[220:223], v[84:87], v[68:71]\nv_mfma_f32_16x16x32_bf16 v[72:75], v[220:223], v[76:79], v[72:75]\ns_branch 1b\n" \
"2:\ns_add_u32 s52, s52, 1\ns_cmp_lt_u32 s52, 0x40000\ns_cbranch_scc0 9f\n" \
"v_max_u32 v96, v40, v42\nv_cmp_eq_u32 vcc, -1, v96\ns_cbranch_vccz 6f\nbuffer_load_dwordx4 v24, s[8:11], s95 offen offset:0 sc1 lds\n" \
"6:\nv_max_u32 v96, v84, v86\nv_cmp_eq_u32 vcc, -1, v96\ns_cbranch_vccz 6f\n" \
"buffer_load_dwordx4 v24, s[8:11], s95 offen offset:1024 sc1 lds\n6:\nv_max_u32 v96, v88, v90\nv_cmp_eq_u32 vcc, -1, v96\n" \
"s_cbranch_vccz 6f\nbuffer_load_dwordx4 v24, s[8:11], s95 offen offset:2048 sc1 lds\n6:\nv_max_u32 v96, v92, v94\n" \
"v_cmp_eq_u32 vcc, -1, v96\ns_cbranch_vccz 6f\nbuffer_load_dwordx4 v24, s[8:11], s95 offen offset:3072 sc1 lds\n6:\n" \
"s_waitcnt vmcnt(0)\nds_read_b128 v[40:43], v31 offset:16384\nds_read_b128 v[84:87], v31 offset:17408\nds_read_b128 v[88:91], v31 offset:18432\n" \
"ds_read_b128 v[92:95], v31 offset:19456\ns_waitcnt lgkmcnt(0)\nv_max3_u32 v96, v40, v42, v84\nv_max3_u32 v96, v96, v86, v88\n" \
"v_max3_u32 v96, v96, v90, v92\nv_max_u32 v96, v96, v94\nv_cmp_eq_u32 vcc, -1, v96\ns_cbranch_vccnz 2b\n" \
"s_branch 3b\n4:\ns_cmp_gt_u32 s30, 64\ns_cbranch_scc1 9f\n" \
"s_cmp_eq_u32 s30, 64\ns_cbranch_scc1 5b\nv_bfe_u32 v48, v24, 6, 3\nv_lshlrev_b32 v48, 2, v48\n" \
"s_lshl_b32 s95, s30, 5\ns_add_u32 s95, s95, s48\n7:\nbuffer_load_dword v49, v48, s[8:11], s95 offen sc1\n" \
"s_waitcnt vmcnt(0)\nv_cmp_eq_u32 vcc, -1, v49\ns_cbranch_vccz 8f\ns_add_u32 s52, s52, 1\n" \
"s_cmp_lt_u32 s52, 0x40000\ns_cbranch_scc0 9f\ns_sleep 8\ns_branch 7b\n" \
"8:\ns_mov_b32 m0, s53\ns_add_u32 s95, s30, 1\ns_min_u32 s95, s95, 63\n" \
"s_lshl_b32 s95, s95, 5\ns_add_u32 s95, s95, s48\nbuffer_load_dword v48, s[8:11], s95 offen offset:0 sc1 lds\ns_add_u32 m0, s32, 0\n" \
"s_add_u32 s95, s31, 0\nbuffer_load_dwordx4 v24, s[8:11], s95 offen offset:0 sc1 lds\nbuffer_load_dwordx4 v24, s[8:11], s95 offen offset:1024 sc1 lds\nbuffer_load_dwordx4 v24, s[8:11], s95 offen offset:2048 sc1 lds\n" \
"buffer_load_dwordx4 v24, s[8:11], s95 offen offset:3072 sc1 lds\ns_add_u32 m0, s32, 0x2000\ns_add_u32 s95, s31, 0x2000\nbuffer_load_dwordx4 v24, s[8:11], s95 offen offset:0 sc1 lds\n" \
"buffer_load_dwordx4 v24, s[8:11], s95 offen offset:1024 sc1 lds\nbuffer_load_dwordx4 v24, s[8:11], s95 offen offset:2048 sc1 lds\nbuffer_load_dwordx4 v24, s[8:11], s95 offen offset:3072 sc1 lds\nv_lshrrev_b32 v48, 2, v24\n" \
"v_add_u32 v48, 0x11000, v48\ns_waitcnt vmcnt(0) lgkmcnt(0)\ns_barrier\nds_read_b32 v49, v48 offset:0\n" \
"ds_read_b128 v[72:75], v24 offset:0\nds_read_b128 v[40:43], v24 offset:32768\ns_waitcnt lgkmcnt(0)\nv_cmp_eq_u32 vcc, -1, v49\n" \
"s_cmp_eq_u64 vcc, 0\ns_cselect_b32 s14, s10, 0\ns_branch 5b\n9:\n" \
"s_waitcnt vmcnt(0)\ns_endpgm\n"
#define B_LDS 0x44840c0202a00110ull
#define B_VAR 0x0000000000010001ull
#define B_LV 8192
#define B_LOOP \
"v_mov_b32 v111, v20\ns_branch 4f\n.p2align 6\n1:\n" \
"s_waitcnt lgkmcnt(4)\nv_mfma_f32_16x16x32_bf16 v[32:35], a[8:11], v[20:23], v[32:35]\ns_add_u32 m0, s22, 0\ns_add_u32 s43, s20, 7\n" \
"s_and_b32 s43, s43, 7\nv_mfma_f32_16x16x32_bf16 v[52:55], a[8:11], v[60:63], v[52:55]\ns_lshl_b32 s43, s43, 18\ns_add_u32 s43, s43, s23\n" \
"v_mfma_f32_16x16x32_bf16 v[56:59], a[112:115], v[20:23], v[56:59]\nds_read_b128 v[28:31], v17 offset:0\nv_mfma_f32_16x16x32_bf16 v[44:47], a[112:115], v[60:63], v[44:47]\nv_mfma_f32_16x16x32_bf16 v[32:35], a[12:15], v[40:43], v[32:35]\n" \
"s_waitcnt vmcnt(8) lgkmcnt(1)\ns_barrier\nv_mfma_f32_16x16x32_bf16 v[52:55], a[12:15], v[64:67], v[52:55]\nds_read_b128 v[20:23], v16 offset:8192\n" \
"v_mfma_f32_16x16x32_bf16 v[56:59], a[116:119], v[40:43], v[56:59]\nds_read_b128 v[48:51], v16 offset:40960\nv_mfma_f32_16x16x32_bf16 v[44:47], a[116:119], v[64:67], v[44:47]\nds_read_b128 v[40:43], v16 offset:9216\n" \
"v_mfma_f32_16x16x32_bf16 v[32:35], a[16:19], v[24:27], v[32:35]\nds_read_b128 v[60:63], v17 offset:1024\nv_mfma_f32_16x16x32_bf16 v[52:55], a[16:19], v[36:39], v[52:55]\nbuffer_load_dwordx4 v16, s[8:11], s43 offen offset:0 sc1 lds\n" \
"v_mfma_f32_16x16x32_bf16 v[56:59], a[120:123], v[24:27], v[56:59]\nds_read_b128 v[64:67], v16 offset:41984\nv_mfma_f32_16x16x32_bf16 v[44:47], a[120:123], v[36:39], v[44:47]\nds_read_b128 v[24:27], v16 offset:10240\n" \
"v_mfma_f32_16x16x32_bf16 v[32:35], a[20:23], v[68:71], v[32:35]\nbuffer_load_dwordx4 v16, s[8:11], s43 offen offset:1024 sc1 lds\nv_mfma_f32_16x16x32_bf16 v[52:55], a[20:23], v[72:75], v[52:55]\nds_read_b128 v[36:39], v16 offset:43008\n" \
"v_mfma_f32_16x16x32_bf16 v[56:59], a[124:127], v[68:71], v[56:59]\ns_waitcnt lgkmcnt(3)\nds_read_b128 v[68:71], v17 offset:4096\nv_mfma_f32_16x16x32_bf16 v[44:47], a[124:127], v[72:75], v[44:47]\n" \
"buffer_load_dwordx4 v16, s[8:11], s43 offen offset:2048 sc1 lds\nv_mfma_f32_16x16x32_bf16 v[32:35], v[28:31], v[20:23], v[32:35]\nds_read_b128 v[72:75], v16 offset:11264\nv_mfma_f32_16x16x32_bf16 v[52:55], v[28:31], v[48:51], v[52:55]\n" \
"ds_read_b128 v[76:79], v16 offset:44032\nv_mfma_f32_16x16x32_bf16 v[56:59], a[128:131], v[20:23], v[56:59]\nbuffer_load_dwordx4 v16, s[8:11], s43 offen offset:3072 sc1 lds\nv_mfma_f32_16x16x32_bf16 v[44:47], a[128:131], v[48:51], v[44:47]\n" \
"s_waitcnt lgkmcnt(3)\nds_read_b128 v[20:23], v17 offset:5120\nv_mfma_f32_16x16x32_bf16 v[32:35], v[60:63], v[40:43], v[32:35]\nds_read_b128 v[28:31], v16 offset:12288\n" \
"v_mfma_f32_16x16x32_bf16 v[52:55], v[60:63], v[64:67], v[52:55]\ns_add_u32 m0, s22, 0x6000\nds_read_b128 v[48:51], v16 offset:45056\nv_mfma_f32_16x16x32_bf16 v[56:59], a[132:135], v[40:43], v[56:59]\n" \
"s_add_u32 s42, s21, -0xa000\nv_mfma_f32_16x16x32_bf16 v[44:47], a[132:135], v[64:67], v[44:47]\nds_read_b128 v[40:43], v16 offset:13312\nv_mfma_f32_16x16x32_bf16 v[32:35], a[24:27], v[24:27], v[32:35]\n" \
"s_waitcnt lgkmcnt(4)\nds_read_b128 v[60:63], v16 offset:46080\nv_mfma_f32_16x16x32_bf16 v[52:55], a[24:27], v[36:39], v[52:55]\nbuffer_load_dwordx4 v16, s[12:15], s42 offen offset:0 sc1 lds\n" \
"v_mfma_f32_16x16x32_bf16 v[56:59], v[68:71], v[24:27], v[56:59]\nv_mfma_f32_16x16x32_bf16 v[44:47], v[68:71], v[36:39], v[44:47]\nds_read_b128 v[24:27], v16 offset:14336\nv_mfma_f32_16x16x32_bf16 v[32:35], a[28:31], v[72:75], v[32:35]\n" \
"s_waitcnt lgkmcnt(3)\nbuffer_load_dwordx4 v16, s[12:15], s42 offen offset:1024 sc1 lds\nv_mfma_f32_16x16x32_bf16 v[52:55], a[28:31], v[76:79], v[52:55]\nds_read_b128 v[36:39], v16 offset:47104\n" \
"v_mfma_f32_16x16x32_bf16 v[56:59], v[20:23], v[72:75], v[56:59]\nds_read_b128 v[64:67], v17 offset:6144\nv_mfma_f32_16x16x32_bf16 v[44:47], v[20:23], v[76:79], v[44:47]\nbuffer_load_dwordx4 v16, s[12:15], s42 offen offset:2048 sc1 lds\n" \
"v_mfma_f32_16x16x32_bf16 v[32:35], a[32:35], v[28:31], v[32:35]\nds_read_b128 v[20:23], v16 offset:15360\nv_lshlrev_b32 v111, 5, v16\nv_mfma_f32_16x16x32_bf16 v[52:55], a[32:35], v[48:51], v[52:55]\n" \
"ds_read_b128 v[68:71], v16 offset:48128\nv_and_b32 v110, 0x6000, v111\nv_mfma_f32_16x16x32_bf16 v[56:59], a[136:139], v[28:31], v[56:59]\ns_waitcnt lgkmcnt(4)\n" \
"buffer_load_dwordx4 v16, s[12:15], s42 offen offset:3072 sc1 lds\nv_mfma_f32_16x16x32_bf16 v[44:47], a[136:139], v[48:51], v[44:47]\nds_read_b128 v[28:31], v17 offset:7168\ns_add_u32 m0, s22, 0\n" \
"v_mfma_f32_16x16x32_bf16 v[32:35], a[36:39], v[40:43], v[32:35]\nds_read_b128 v[48:51], v16 offset:16384\nv_add_u32 v111, v111, v110\nv_mfma_f32_16x16x32_bf16 v[52:55], a[36:39], v[60:63], v[52:55]\n" \
"ds_read_b128 v[72:75], v17 offset:2048\nv_and_b32 v110, 0x4000, v110\nv_mfma_f32_16x16x32_bf16 v[56:59], a[140:143], v[40:43], v[56:59]\nds_read_b128 v[76:79], v16 offset:49152\n" \
"v_lshl_add_u32 v111, v110, 16, v111\nv_mfma_f32_16x16x32_bf16 v[44:47], a[140:143], v[60:63], v[44:47]\ns_waitcnt lgkmcnt(4)\nds_read_b128 v[40:43], v16 offset:17408\n" \
"v_mfma_f32_16x16x32_bf16 v[32:35], a[40:43], v[24:27], v[32:35]\nds_read_b128 v[60:63], v17 offset:3072\nv_mfma_f32_16x16x32_bf16 v[52:55], a[40:43], v[36:39], v[52:55]\nds_read_b128 v[80:83], v16 offset:50176\n" \
"v_mfma_f32_16x16x32_bf16 v[56:59], v[64:67], v[24:27], v[56:59]\nv_mfma_f32_16x16x32_bf16 v[44:47], v[64:67], v[36:39], v[44:47]\nds_read_b128 v[24:27], v16 offset:18432\nv_mfma_f32_16x16x32_bf16 v[32:35], a[44:47], v[20:23], v[32:35]\n" \
"s_waitcnt lgkmcnt(4)\nds_read_b128 v[36:39], v16 offset:51200\nv_mfma_f32_16x16x32_bf16 v[52:55], a[44:47], v[68:71], v[52:55]\nds_read_b128 v[64:67], v17 offset:8192\n" \
"v_add_u32 v110, s22, v16\nv_mfma_f32_16x16x32_bf16 v[56:59], v[28:31], v[20:23], v[56:59]\nv_mfma_f32_16x16x32_bf16 v[44:47], v[28:31], v[68:71], v[44:47]\nds_read_b128 v[20:23], v16 offset:19456\n" \
"v_mfma_f32_16x16x32_bf16 v[32:35], v[72:75], v[48:51], v[32:35]\nds_read_b128 v[28:31], v16 offset:52224\nv_mfma_f32_16x16x32_bf16 v[52:55], v[72:75], v[76:79], v[52:55]\nds_read_b128 v[68:71], v17 offset:9216\n" \
"v_mfma_f32_16x16x32_bf16 v[56:59], a[144:147], v[48:51], v[56:59]\ns_waitcnt lgkmcnt(5)\nds_read_b128 v[84:87], v17 offset:9216\nv_mfma_f32_16x16x32_bf16 v[44:47], a[144:147], v[76:79], v[44:47]\n" \
"ds_read_b128 v[48:51], v16 offset:20480\nv_mfma_f32_16x16x32_bf16 v[32:35], v[60:63], v[40:43], v[32:35]\ns_waitcnt vmcnt(4)\nds_read_b128 v[72:75], v110 offset:3072\n" \
"ds_read_b128 v[76:79], v110 offset:0\nds_read_b128 v[88:91], v110 offset:1024\nv_mfma_f32_16x16x32_bf16 v[52:55], v[60:63], v[80:83], v[52:55]\nds_read_b128 v[92:95], v110 offset:2048\n" \
"v_mfma_f32_16x16x32_bf16 v[56:59], a[148:151], v[40:43], v[56:59]\nds_read_b128 v[60:63], v16 offset:53248\nv_mfma_f32_16x16x32_bf16 v[44:47], a[148:151], v[80:83], v[44:47]\ns_waitcnt lgkmcnt(7)\n" \
"ds_read_b128 v[80:83], v17 offset:8192\nv_mfma_f32_16x16x32_bf16 v[32:35], a[48:51], v[24:27], v[32:35]\ns_waitcnt lgkmcnt(3)\nv_max3_u32 v109, v76, v78, v88\n" \
"v_mfma_f32_16x16x32_bf16 v[52:55], a[48:51], v[36:39], v[52:55]\ns_waitcnt lgkmcnt(2)\nv_max3_u32 v109, v109, v90, v92\nv_max3_u32 v109, v109, v94, v72\n" \
"v_mfma_f32_16x16x32_bf16 v[56:59], v[64:67], v[24:27], v[56:59]\nv_max_u32 v109, v109, v74\nv_cmp_eq_u32 vcc, -1, v109\nv_mfma_f32_16x16x32_bf16 v[44:47], v[64:67], v[36:39], v[44:47]\n" \
"ds_read_b128 v[96:99], v17 offset:4096\nv_mov_b64 v[24:25], 0\nv_mfma_f32_16x16x32_bf16 v[32:35], a[52:55], v[20:23], v[32:35]\nds_read_b128 v[100:103], v17 offset:5120\n" \
"v_mov_b64 v[26:27], 0\nv_mfma_f32_16x16x32_bf16 v[52:55], a[52:55], v[28:31], v[52:55]\ns_cbranch_vccnz 2f\n3:\n" \
"v_mfma_f32_16x16x32_bf16 v[56:59], v[68:71], v[20:23], v[56:59]\ns_barrier\nds_read_b128 v[20:23], v16 offset:2048\nv_mfma_f32_16x16x32_bf16 v[44:47], v[84:87], v[28:31], v[44:47]\n" \
"s_memrealtime s[40:41]\nds_read_b128 v[28:31], v16 offset:5120\nv_mfma_f32_16x16x32_bf16 v[32:35], a[56:59], v[48:51], v[32:35]\ns_waitcnt lgkmcnt(4)\n" \
"ds_read_b128 v[36:39], v16 offset:1024\nv_mfma_f32_16x16x32_bf16 v[52:55], a[56:59], v[60:63], v[52:55]\nds_read_b128 v[40:43], v16 offset:33792\nv_mov_b64 v[64:65], 0\n" \
"v_mfma_f32_16x16x32_bf16 v[56:59], a[152:155], v[48:51], v[56:59]\ns_lshl_b32 s43, s20, 24\nv_mov_b64 v[66:67], 0\nv_mfma_f32_16x16x32_bf16 v[44:47], a[152:155], v[60:63], v[44:47]\n" \
"s_waitcnt lgkmcnt(3)\nds_read_b128 v[48:51], v16 offset:3072\nv_smfmac_f32_16x16x64_bf16 v[24:27], v[20:23], a[8:15], v18\ns_waitcnt lgkmcnt(3)\n" \
"v_smfmac_f32_16x16x64_bf16 v[64:67], v[20:23], a[112:119], v18\nds_read_b128 v[60:63], v16 offset:34816\nv_smfmac_f32_16x16x64_bf16 v[24:27], v[28:31], a[24:31], v18\ns_waitcnt lgkmcnt(3)\n" \
"ds_read_b128 v[68:71], v17 offset:6144\nv_smfmac_f32_16x16x64_bf16 v[64:67], v[28:31], v[96:103], v18\nds_read_b128 v[20:23], v16 offset:36864\nv_smfmac_f32_16x16x64_bf16 v[24:27], v[36:39], a[0:7], v18\n" \
"s_waitcnt lgkmcnt(4)\nds_read_b128 v[72:75], v17 offset:7168\nv_smfmac_f32_16x16x64_bf16 v[64:67], v[36:39], a[104:111], v18\nds_read_b128 v[28:31], v16 offset:6144\n" \
"s_add_u32 s42, s40, 0x60\nv_smfmac_f32_16x16x64_bf16 v[24:27], v[40:43], a[48:55], v18\ns_and_b32 s42, s42, 0xffffff\ns_or_b32 s42, s42, s43\n" \
"v_mov_b32 v110, s42\nv_smfmac_f32_16x16x64_bf16 v[64:67], v[40:43], v[80:87], v18\ns_waitcnt lgkmcnt(4)\nds_read_b128 v[36:39], v16 offset:7168\n" \
"v_smfmac_f32_16x16x64_bf16 v[24:27], v[48:51], a[16:23], v18\nbuffer_store_dword v110, v111, s[8:11], s27 offen offset:0 sc1\nv_smfmac_f32_16x16x64_bf16 v[64:67], v[48:51], a[120:127], v18\nds_read_b128 v[40:43], v16 offset:35840\n" \
"v_smfmac_f32_16x16x64_bf16 v[24:27], v[60:63], a[56:63], v18\ns_waitcnt lgkmcnt(4)\nv_smfmac_f32_16x16x64_bf16 v[64:67], v[60:63], a[152:159], v18\nds_read_b128 v[48:51], v16 offset:0\n" \
"s_add_u32 s16, s20, 1\nv_smfmac_f32_16x16x64_bf16 v[24:27], v[20:23], a[72:79], v18\nds_read_b128 v[76:79], v17 offset:0\nv_mov_b64 v[60:61], 0\n" \
"v_smfmac_f32_16x16x64_bf16 v[64:67], v[20:23], a[168:175], v18\ns_waitcnt lgkmcnt(4)\nds_read_b128 v[20:23], v16 offset:39936\nv_smfmac_f32_16x16x64_bf16 v[24:27], v[28:31], a[32:39], v18\n" \
"ds_read_b128 v[80:83], v17 offset:1024\nv_mov_b64 v[62:63], 0\nv_smfmac_f32_16x16x64_bf16 v[64:67], v[28:31], a[136:143], v18\ns_waitcnt lgkmcnt(4)\n" \
"ds_read_b128 v[28:31], v16 offset:37888\nv_smfmac_f32_16x16x64_bf16 v[24:27], v[36:39], a[40:47], v18\nds_read_b128 v[84:87], v17 offset:2048\ns_and_b32 s16, s16, 7\n" \
"v_smfmac_f32_16x16x64_bf16 v[64:67], v[36:39], v[68:75], v18\nds_read_b128 v[92:95], v16 offset:4096\ns_lshl_b32 s16, s16, 22\nv_smfmac_f32_16x16x64_bf16 v[24:27], v[40:43], a[64:71], v18\n" \
"ds_read_b128 v[88:91], v17 offset:3072\ns_add_u32 s16, s16, s25\nv_smfmac_f32_16x16x64_bf16 v[64:67], v[40:43], a[160:167], v18\ns_waitcnt lgkmcnt(6)\n" \
"ds_read_b128 v[36:39], v16 offset:38912\nv_smfmac_f32_16x16x64_bf16 v[24:27], v[48:51], v[0:7], v18\nds_read_b128 v[40:43], v16 offset:21504\ns_add_u32 s17, s16, 0x1000\n" \
"v_smfmac_f32_16x16x64_bf16 v[64:67], v[48:51], v[8:15], v18\ns_waitcnt lgkmcnt(6)\nds_read_b128 v[48:51], v16 offset:32768\nv_smfmac_f32_16x16x64_bf16 v[24:27], v[20:23], a[96:103], v18\n" \
"ds_read_b128 v[68:71], v16 offset:54272\ns_add_u32 s18, s16, 0x4000\nv_smfmac_f32_16x16x64_bf16 v[64:67], v[20:23], a[192:199], v18\ns_waitcnt lgkmcnt(5)\n" \
"ds_read_b128 v[20:23], v16 offset:22528\nv_smfmac_f32_16x16x64_bf16 v[24:27], v[28:31], a[80:87], v18\nds_read_b128 v[72:75], v16 offset:55296\nv_lshrrev_b32 v111, 16, v18\n" \
"v_smfmac_f32_16x16x64_bf16 v[64:67], v[28:31], a[176:183], v18\nds_write_b128 v16, v[60:63] offset:45056\nv_smfmac_f32_16x16x64_bf16 v[24:27], v[92:95], v[76:83], v18\ns_add_u32 s19, s16, 0x5000\n" \
"s_and_b32 s43, s20, 2\ns_lshl_b32 s43, s43, 12\nv_smfmac_f32_16x16x64_bf16 v[64:67], v[92:95], a[128:135], v18\ns_waitcnt lgkmcnt(4)\n" \
"ds_read_b128 v[28:31], v16 offset:23552\nv_smfmac_f32_16x16x64_bf16 v[24:27], v[36:39], a[88:95], v18\nds_read_b128 v[76:79], v16 offset:56320\ns_and_b32 s42, s20, 7\n" \
"v_smfmac_f32_16x16x64_bf16 v[64:67], v[36:39], a[184:191], v18\nds_read_b128 v[92:95], v17 offset:10240\ns_add_u32 s99, s20, 2\nv_smfmac_f32_16x16x64_bf16 v[24:27], v[48:51], v[84:91], v18\n" \
"ds_read_b128 v[96:99], v17 offset:11264\ns_min_u32 s99, s99, 63\nv_smfmac_f32_16x16x64_bf16 v[64:67], v[48:51], a[144:151], v18\ns_lshl_b32 s99, s99, 5\n" \
"s_add_u32 s99, s99, s26\nv_bfe_u32 v110, v16, 4, 3\nv_mfma_f32_16x16x32_bf16 v[32:35], a[60:63], v[40:43], v[32:35]\ns_waitcnt lgkmcnt(4)\n" \
"v_add_f32 v24, v24, v26\nv_add_f32 v25, v25, v27\nv_cvt_pk_bf16_f32 v24, v24, v25\nv_mfma_f32_16x16x32_bf16 v[52:55], a[60:63], v[68:71], v[52:55]\n" \
"v_add_f32 v64, v64, v66\nv_add_f32 v65, v65, v67\nv_cvt_pk_bf16_f32 v64, v64, v65\nv_mfma_f32_16x16x32_bf16 v[56:59], a[156:159], v[40:43], v[56:59]\n" \
"ds_write2_b32 v111, v24, v64 offset0:0 offset1:4\nv_mfma_f32_16x16x32_bf16 v[44:47], a[156:159], v[68:71], v[44:47]\nds_read_b128 v[36:39], v17 offset:12288\nv_lshlrev_b32 v110, 2, v110\n" \
"v_mfma_f32_16x16x32_bf16 v[32:35], a[64:67], v[20:23], v[32:35]\ns_lshl_b32 s42, s42, 20\nv_mov_b64 v[24:25], 0\nv_mov_b64 v[26:27], 0\n" \
"v_mfma_f32_16x16x32_bf16 v[52:55], a[64:67], v[72:75], v[52:55]\ns_waitcnt vmcnt(1) lgkmcnt(1)\ns_barrier\nds_read_b64_tr_b16 v[48:49], v19 offset:0\n" \
"v_mfma_f32_16x16x32_bf16 v[56:59], a[160:163], v[20:23], v[56:59]\nds_read_b64_tr_b16 v[50:51], v19 offset:256\nds_read_b64_tr_b16 v[20:21], v19 offset:1024\nds_read_b64_tr_b16 v[22:23], v19 offset:1280\n" \
"v_mfma_f32_16x16x32_bf16 v[44:47], a[160:163], v[72:75], v[44:47]\nds_read_b128 v[64:67], v16 offset:24576\ns_add_u32 s42, s42, s24\nv_mfma_f32_16x16x32_bf16 v[32:35], a[68:71], v[28:31], v[32:35]\n" \
"buffer_load_dword v111, v110, s[8:11], s99 offen sc1\nv_mfma_f32_16x16x32_bf16 v[52:55], a[68:71], v[76:79], v[52:55]\ns_waitcnt lgkmcnt(3)\nv_mov_b64 v[68:69], 0\n" \
"v_mov_b64 v[70:71], 0\nv_mfma_f32_16x16x32_bf16 v[56:59], a[164:167], v[28:31], v[56:59]\nds_read_b128 v[72:75], v16 offset:25600\nv_mov_b64 v[28:29], 0\n" \
"v_smfmac_f32_16x16x64_bf16 v[60:63], v[48:51], a[200:207], v18\ns_waitcnt lgkmcnt(2)\nds_read_b128 v[40:43], v17 offset:13312\nv_smfmac_f32_16x16x64_bf16 v[60:63], v[20:23], v[92:99], v18\n" \
"ds_read_b128 v[80:83], v17 offset:14336\nv_mov_b64 v[30:31], 0\nv_smfmac_f32_16x16x64_bf16 v[24:27], v[48:51], a[224:231], v18\nds_read_b128 v[88:91], v16 offset:45056\n" \
"v_mfma_f32_16x16x32_bf16 v[44:47], a[164:167], v[76:79], v[44:47]\nds_read_b128 v[84:87], v17 offset:15360\nv_add_f32 v60, v60, v62\nv_smfmac_f32_16x16x64_bf16 v[24:27], v[20:23], a[232:239], v18\n" \
"s_waitcnt lgkmcnt(5)\nv_add_f32 v61, v61, v63\nv_mfma_f32_16x16x32_bf16 v[32:35], a[72:75], v[64:67], v[32:35]\nds_read_b128 v[92:95], v17 offset:16384\n" \
"v_cvt_pk_bf16_f32 v76, v60, v61\nv_smfmac_f32_16x16x64_bf16 v[68:71], v[48:51], a[208:215], v18\nds_read_b128 v[60:63], v16 offset:45056\nv_add_f32 v24, v24, v26\n" \
"v_smfmac_f32_16x16x64_bf16 v[68:71], v[20:23], a[216:223], v18\nds_read_b128 v[100:103], v16 offset:26624\nv_add_f32 v25, v25, v27\nv_smfmac_f32_16x16x64_bf16 v[28:31], v[48:51], a[240:247], v18\n" \
"ds_read_b128 v[104:107], v16 offset:45056\nv_cvt_pk_bf16_f32 v77, v24, v25\nv_mfma_f32_16x16x32_bf16 v[56:59], a[168:171], v[64:67], v[56:59]\ns_waitcnt lgkmcnt(4)\n" \
"v_permlane32_swap_b32 v76, v77\nv_smfmac_f32_16x16x64_bf16 v[28:31], v[20:23], a[248:255], v18\nds_read_b128 v[96:99], v17 offset:17408\nv_add_f32 v68, v68, v70\n" \
"v_smfmac_f32_16x16x64_bf16 v[88:91], v[48:51], v[112:119], v18\nds_read_b128 v[24:27], v16 offset:45056\nv_add_f32 v69, v69, v71\nv_mfma_f32_16x16x32_bf16 v[32:35], a[76:79], v[72:75], v[32:35]\n" \
"s_waitcnt lgkmcnt(4)\nv_add_f32 v28, v28, v30\nv_add_f32 v29, v29, v31\nv_smfmac_f32_16x16x64_bf16 v[88:91], v[20:23], v[120:127], v18\n" \
"ds_read_b128 v[64:67], v16 offset:27648\nv_cvt_pk_bf16_f32 v78, v68, v69\nv_smfmac_f32_16x16x64_bf16 v[60:63], v[48:51], v[128:135], v18\nds_read_b128 v[68:71], v16 offset:45056\n" \
"v_cvt_pk_bf16_f32 v79, v28, v29\nv_smfmac_f32_16x16x64_bf16 v[60:63], v[20:23], v[136:143], v18\ns_waitcnt lgkmcnt(4)\nv_permlane32_swap_b32 v78, v79\n" \
"v_mfma_f32_16x16x32_bf16 v[56:59], a[172:175], v[72:75], v[56:59]\nv_permlane16_swap_b32 v76, v78\nv_smfmac_f32_16x16x64_bf16 v[104:107], v[48:51], v[36:43], v18\nds_read_b128 v[28:31], v16 offset:45056\n" \
"v_permlane16_swap_b32 v77, v79\nv_min_u32 v76, -2, v76\nv_smfmac_f32_16x16x64_bf16 v[104:107], v[20:23], v[80:87], v18\nds_read_b128 v[36:39], v16 offset:28672\n" \
"v_add_f32 v88, v88, v90\nv_mfma_f32_16x16x32_bf16 v[32:35], a[80:83], v[100:103], v[32:35]\ns_waitcnt lgkmcnt(3)\nbuffer_store_dwordx4 v[76:79], v16, s[8:11], s29 offen offset:0 sc1\n" \
"v_add_f32 v89, v89, v91\nv_add_f32 v60, v60, v62\nv_add_f32 v61, v61, v63\nv_smfmac_f32_16x16x64_bf16 v[24:27], v[48:51], v[144:151], v18\n" \
"ds_read_b128 v[40:43], v16 offset:45056\nv_add_f32 v104, v104, v106\nv_smfmac_f32_16x16x64_bf16 v[24:27], v[20:23], v[152:159], v18\nds_read_b128 v[72:75], v17 offset:18432\n" \
"v_add_f32 v105, v105, v107\nv_mfma_f32_16x16x32_bf16 v[56:59], a[176:179], v[100:103], v[56:59]\ns_waitcnt lgkmcnt(3)\nv_cvt_pk_bf16_f32 v80, v88, v89\n" \
"v_cvt_pk_bf16_f32 v81, v60, v61\nv_smfmac_f32_16x16x64_bf16 v[68:71], v[48:51], v[160:167], v18\nds_read_b128 v[60:63], v16 offset:45056\nv_add_f32 v24, v24, v26\n" \
"v_smfmac_f32_16x16x64_bf16 v[68:71], v[20:23], v[168:175], v18\nds_read_b128 v[76:79], v17 offset:19456\nv_add_f32 v25, v25, v27\nv_mfma_f32_16x16x32_bf16 v[32:35], a[84:87], v[64:67], v[32:35]\n" \
"v_permlane32_swap_b32 v80, v81\nv_cvt_pk_bf16_f32 v82, v104, v105\nv_cvt_pk_bf16_f32 v83, v24, v25\nv_smfmac_f32_16x16x64_bf16 v[28:31], v[48:51], v[184:191], v18\n" \
"ds_read_b128 v[24:27], v16 offset:45056\nv_permlane32_swap_b32 v82, v83\nv_smfmac_f32_16x16x64_bf16 v[28:31], v[20:23], v[192:199], v18\nds_read_b128 v[84:87], v16 offset:29696\n" \
"v_permlane16_swap_b32 v80, v82\nv_mfma_f32_16x16x32_bf16 v[56:59], a[180:183], v[64:67], v[56:59]\ns_waitcnt lgkmcnt(3)\nv_permlane16_swap_b32 v81, v83\n" \
"v_min_u32 v80, -2, v80\nv_smfmac_f32_16x16x64_bf16 v[40:43], v[48:51], v[92:99], v18\nds_read_b128 v[64:67], v16 offset:45056\nv_add_f32 v68, v68, v70\n" \
"v_smfmac_f32_16x16x64_bf16 v[40:43], v[20:23], v[176:183], v18\nds_read_b128 v[88:91], v17 offset:20480\nv_add_f32 v69, v69, v71\nv_mfma_f32_16x16x32_bf16 v[32:35], a[88:91], v[36:39], v[32:35]\n" \
"s_waitcnt lgkmcnt(4)\nbuffer_store_dwordx4 v[80:83], v16, s[8:11], s29 offen offset:1024 sc1\nds_read_b128 v[92:95], v17 offset:21504\nv_add_f32 v28, v28, v30\n" \
"v_smfmac_f32_16x16x64_bf16 v[60:63], v[48:51], v[200:207], v18\nds_read_b128 v[80:83], v16 offset:45056\nv_add_f32 v29, v29, v31\nv_smfmac_f32_16x16x64_bf16 v[60:63], v[20:23], v[72:79], v18\n" \
"s_waitcnt lgkmcnt(4)\nds_read_b128 v[72:75], v16 offset:30720\nv_smfmac_f32_16x16x64_bf16 v[24:27], v[48:51], v[208:215], v18\nds_read_b128 v[96:99], v17 offset:22528\n" \
"v_add_f32 v40, v40, v42\nv_mfma_f32_16x16x32_bf16 v[56:59], a[184:187], v[36:39], v[56:59]\nv_add_f32 v41, v41, v43\nv_add_f32 v60, v60, v62\n" \
"v_smfmac_f32_16x16x64_bf16 v[24:27], v[20:23], v[216:223], v18\nds_read_b128 v[36:39], v16 offset:45056\nv_add_f32 v61, v61, v63\nv_mfma_f32_16x16x32_bf16 v[32:35], a[92:95], v[84:87], v[32:35]\n" \
"s_waitcnt lgkmcnt(4)\nv_cvt_pk_bf16_f32 v76, v68, v69\nv_cvt_pk_bf16_f32 v77, v28, v29\nv_cvt_pk_bf16_f32 v78, v40, v41\n" \
"v_cvt_pk_bf16_f32 v79, v60, v61\nv_smfmac_f32_16x16x64_bf16 v[64:67], v[48:51], v[232:239], v18\nds_read_b128 v[100:103], v17 offset:23552\nv_permlane32_swap_b32 v76, v77\n" \
"v_permlane32_swap_b32 v78, v79\nv_smfmac_f32_16x16x64_bf16 v[64:67], v[20:23], v[240:247], v18\nds_read_b128 v[28:31], v16 offset:31744\nv_permlane16_swap_b32 v76, v78\n" \
"v_mfma_f32_16x16x32_bf16 v[56:59], a[188:191], v[84:87], v[56:59]\ns_waitcnt lgkmcnt(3)\nv_permlane16_swap_b32 v77, v79\nv_min_u32 v76, -2, v76\n" \
"v_smfmac_f32_16x16x64_bf16 v[80:83], v[48:51], v[88:95], v18\nds_read_b128 v[40:43], v16 offset:57344\nv_add_f32 v24, v24, v26\nv_smfmac_f32_16x16x64_bf16 v[80:83], v[20:23], v[224:231], v18\n" \
"ds_read_b128 v[60:63], v16 offset:58368\nv_add_f32 v25, v25, v27\nv_mfma_f32_16x16x32_bf16 v[32:35], a[96:99], v[72:75], v[32:35]\ns_waitcnt lgkmcnt(3)\n" \
"buffer_store_dwordx4 v[76:79], v16, s[8:11], s29 offen offset:2048 sc1\nv_add_f32 v64, v64, v66\nv_add_f32 v65, v65, v67\nv_cvt_pk_bf16_f32 v68, v24, v25\n" \
"v_smfmac_f32_16x16x64_bf16 v[36:39], v[48:51], v[96:103], v18\nds_read_b128 v[24:27], v16 offset:59392\nv_add_f32 v80, v80, v82\nv_add_f32 v81, v81, v83\n" \
"v_cvt_pk_bf16_f32 v69, v64, v65\nv_cvt_pk_bf16_f32 v70, v80, v81\nv_smfmac_f32_16x16x64_bf16 v[36:39], v[20:23], v[248:255], v18\nds_read_b128 v[48:51], v16 offset:60416\n" \
"v_permlane32_swap_b32 v68, v69\nv_mfma_f32_16x16x32_bf16 v[56:59], a[192:195], v[72:75], v[56:59]\ns_waitcnt lgkmcnt(3)\nds_read_b128 v[20:23], v16 offset:61440\n" \
"v_mfma_f32_16x16x32_bf16 v[32:35], a[100:103], v[28:31], v[32:35]\nv_add_f32 v36, v36, v38\nv_add_f32 v37, v37, v39\nv_cvt_pk_bf16_f32 v71, v36, v37\n" \
"v_mfma_f32_16x16x32_bf16 v[56:59], a[196:199], v[28:31], v[56:59]\nds_read_b128 v[36:39], v16 offset:62464\nv_permlane32_swap_b32 v70, v71\nv_mfma_f32_16x16x32_bf16 v[52:55], a[72:75], v[40:43], v[52:55]\n" \
"s_waitcnt lgkmcnt(3)\nv_permlane16_swap_b32 v68, v70\nv_permlane16_swap_b32 v69, v71\nv_min_u32 v68, -2, v68\n" \
"v_mfma_f32_16x16x32_bf16 v[44:47], a[168:171], v[40:43], v[44:47]\nbuffer_store_dwordx4 v[68:71], v16, s[8:11], s29 offen offset:3072 sc1\nv_cvt_pk_bf16_f32 v32, v32, v33\nv_cvt_pk_bf16_f32 v33, v34, v35\n" \
"v_cvt_pk_bf16_f32 v34, v56, v57\nv_mfma_f32_16x16x32_bf16 v[52:55], a[76:79], v[60:63], v[52:55]\ns_xor_b32 s29, s42, s43\nv_cvt_pk_bf16_f32 v35, v58, v59\n" \
"v_mfma_f32_16x16x32_bf16 v[44:47], a[172:175], v[60:63], v[44:47]\nds_read_b128 v[40:43], v16 offset:63488\nv_mfma_f32_16x16x32_bf16 v[52:55], a[80:83], v[24:27], v[52:55]\ns_waitcnt vmcnt(4) lgkmcnt(1)\n" \
"v_cmp_eq_u32 s[42:43], -1, v111\nv_mfma_f32_16x16x32_bf16 v[44:47], a[176:179], v[24:27], v[44:47]\nds_read_b128 v[56:59], v16 offset:64512\nv_mfma_f32_16x16x32_bf16 v[52:55], a[84:87], v[48:51], v[52:55]\n" \
"s_cmp_eq_u64 s[42:43], 0\ns_cselect_b32 s14, s10, 0\ns_cmp_lt_i32 s20, 62\ns_cselect_b32 s14, s14, 0\n" \
"s_cmp_eq_u32 s14, 0\ns_cbranch_scc1 4f\n5:\nv_add_u32 v111, s28, v16\n" \
"v_mfma_f32_16x16x32_bf16 v[44:47], a[180:183], v[48:51], v[44:47]\nds_write_b128 v111, v[32:35] offset:0\ns_add_u32 m0, s22, 0\nv_mfma_f32_16x16x32_bf16 v[52:55], a[88:91], v[20:23], v[52:55]\n" \
"buffer_load_dwordx4 v16, s[12:15], s21 offen offset:0 sc1 lds\nv_mfma_f32_16x16x32_bf16 v[44:47], a[184:187], v[20:23], v[44:47]\nds_read_b128 v[48:51], v17 offset:10240\ns_add_u32 s20, s20, 1\n" \
"v_mfma_f32_16x16x32_bf16 v[52:55], a[92:95], v[36:39], v[52:55]\ns_waitcnt lgkmcnt(1)\ns_barrier\nds_read_b128 v[20:23], v16 offset:8192\n" \
"v_mfma_f32_16x16x32_bf16 v[44:47], a[188:191], v[36:39], v[44:47]\nds_read_b128 v[24:27], v16 offset:9216\nv_mfma_f32_16x16x32_bf16 v[52:55], a[96:99], v[40:43], v[52:55]\nds_read_b128 v[28:31], v16 offset:10240\n" \
"v_add_u32 v111, s28, v16\nv_mfma_f32_16x16x32_bf16 v[44:47], a[192:195], v[40:43], v[44:47]\nbuffer_load_dwordx4 v16, s[12:15], s21 offen offset:1024 sc1 lds\nds_read_b128 v[32:35], v16 offset:11264\n" \
"ds_read_b128 v[40:43], v17 offset:11264\nv_mfma_f32_16x16x32_bf16 v[52:55], a[100:103], v[56:59], v[52:55]\ns_waitcnt lgkmcnt(4)\nv_mfma_f32_16x16x32_bf16 v[44:47], a[196:199], v[56:59], v[44:47]\n" \
"s_waitcnt lgkmcnt(3)\nv_mfma_f32_16x16x32_bf16 v[36:39], v[20:23], a[200:203], 0\ns_waitcnt lgkmcnt(2)\nv_cvt_pk_bf16_f32 v52, v52, v53\n" \
"v_cvt_pk_bf16_f32 v53, v54, v55\nv_mfma_f32_16x16x32_bf16 v[36:39], v[24:27], a[204:207], v[36:39]\ns_waitcnt lgkmcnt(0)\nv_cvt_pk_bf16_f32 v54, v44, v45\n" \
"v_cvt_pk_bf16_f32 v55, v46, v47\nv_mfma_f32_16x16x32_bf16 v[36:39], v[28:31], v[48:51], v[36:39]\nbuffer_load_dwordx4 v16, s[12:15], s21 offen offset:2048 sc1 lds\nv_mfma_f32_16x16x32_bf16 v[36:39], v[32:35], v[40:43], v[36:39]\n" \
"ds_write_b128 v111, v[52:55] offset:32768\nv_mfma_f32_16x16x32_bf16 v[40:43], v[20:23], a[208:211], 0\nv_mfma_f32_16x16x32_bf16 v[40:43], v[24:27], a[212:215], v[40:43]\nv_cvt_pk_bf16_f32 v36, v36, v37\n" \
"v_cvt_pk_bf16_f32 v37, v38, v39\nv_mfma_f32_16x16x32_bf16 v[40:43], v[28:31], a[216:219], v[40:43]\ns_add_u32 s43, s21, 0x4000\nds_read_b128 v[44:47], v17 offset:18432\n" \
"v_mfma_f32_16x16x32_bf16 v[40:43], v[32:35], a[220:223], v[40:43]\ns_waitcnt lgkmcnt(1)\ns_barrier\nv_mfma_f32_16x16x32_bf16 v[48:51], v[20:23], a[224:227], 0\n" \
"buffer_load_dwordx4 v16, s[12:15], s21 offen offset:3072 sc1 lds\nv_mfma_f32_16x16x32_bf16 v[48:51], v[24:27], a[228:231], v[48:51]\ns_add_u32 m0, s22, 0x4000\nds_read_b128 v[52:55], v17 offset:19456\n" \
"v_mfma_f32_16x16x32_bf16 v[48:51], v[28:31], a[232:235], v[48:51]\nbuffer_load_dwordx4 v16, s[12:15], s43 offen offset:0 sc1 lds\nv_mfma_f32_16x16x32_bf16 v[48:51], v[32:35], a[236:239], v[48:51]\nds_read_b128 v[56:59], v16 offset:40960\n" \
"v_cvt_pk_bf16_f32 v40, v40, v41\nv_mfma_f32_16x16x32_bf16 v[60:63], v[20:23], v[144:147], 0\nds_read_b128 v[64:67], v16 offset:41984\nv_cvt_pk_bf16_f32 v41, v42, v43\n" \
"v_mfma_f32_16x16x32_bf16 v[60:63], v[24:27], v[148:151], v[60:63]\nds_read_b128 v[68:71], v16 offset:43008\nv_cvt_pk_bf16_f32 v48, v48, v49\nv_mfma_f32_16x16x32_bf16 v[60:63], v[28:31], v[152:155], v[60:63]\n" \
"ds_read_b128 v[72:75], v17 offset:10240\nv_cvt_pk_bf16_f32 v49, v50, v51\nv_mfma_f32_16x16x32_bf16 v[60:63], v[32:35], v[156:159], v[60:63]\nbuffer_load_dwordx4 v16, s[12:15], s43 offen offset:1024 sc1 lds\n" \
"v_mfma_f32_16x16x32_bf16 v[76:79], v[20:23], v[200:203], 0\ns_waitcnt lgkmcnt(4)\nds_read_b128 v[80:83], v16 offset:44032\nv_mfma_f32_16x16x32_bf16 v[76:79], v[24:27], v[204:207], v[76:79]\n" \
"ds_read_b128 v[84:87], v17 offset:11264\nv_cvt_pk_bf16_f32 v60, v60, v61\nv_mfma_f32_16x16x32_bf16 v[76:79], v[28:31], v[44:47], v[76:79]\ns_waitcnt lgkmcnt(4)\n" \
"v_cvt_pk_bf16_f32 v61, v62, v63\nv_mfma_f32_16x16x32_bf16 v[76:79], v[32:35], v[52:55], v[76:79]\nv_mfma_f32_16x16x32_bf16 v[44:47], v[56:59], a[200:203], 0\ns_waitcnt lgkmcnt(2)\n" \
"buffer_load_dwordx4 v16, s[12:15], s43 offen offset:2048 sc1 lds\nv_mfma_f32_16x16x32_bf16 v[44:47], v[64:67], a[204:207], v[44:47]\nv_cvt_pk_bf16_f32 v76, v76, v77\nv_cvt_pk_bf16_f32 v77, v78, v79\n" \
"v_mfma_f32_16x16x32_bf16 v[44:47], v[68:71], v[72:75], v[44:47]\ns_waitcnt lgkmcnt(0)\nv_mfma_f32_16x16x32_bf16 v[44:47], v[80:83], v[84:87], v[44:47]\nv_mfma_f32_16x16x32_bf16 v[52:55], v[56:59], a[208:211], 0\n" \
"buffer_load_dwordx4 v16, s[12:15], s43 offen offset:3072 sc1 lds\nv_mfma_f32_16x16x32_bf16 v[52:55], v[64:67], a[212:215], v[52:55]\nv_cvt_pk_bf16_f32 v38, v44, v45\nv_cvt_pk_bf16_f32 v39, v46, v47\n" \
"v_mfma_f32_16x16x32_bf16 v[52:55], v[68:71], a[216:219], v[52:55]\nds_read_b128 v[44:47], v17 offset:18432\nv_mfma_f32_16x16x32_bf16 v[52:55], v[80:83], a[220:223], v[52:55]\nds_read_b128 v[72:75], v17 offset:19456\n" \
"v_mfma_f32_16x16x32_bf16 v[84:87], v[56:59], a[224:227], 0\nv_mfma_f32_16x16x32_bf16 v[84:87], v[64:67], a[228:231], v[84:87]\nbuffer_store_dwordx4 v[36:39], v16, s[8:11], s16 offen offset:0 sc1\nv_cvt_pk_bf16_f32 v42, v52, v53\n" \
"v_cvt_pk_bf16_f32 v43, v54, v55\nv_mfma_f32_16x16x32_bf16 v[84:87], v[68:71], a[232:235], v[84:87]\nv_mfma_f32_16x16x32_bf16 v[84:87], v[80:83], a[236:239], v[84:87]\nv_mfma_f32_16x16x32_bf16 v[36:39], v[56:59], v[144:147], 0\n" \
"v_mfma_f32_16x16x32_bf16 v[36:39], v[64:67], v[148:151], v[36:39]\nv_cvt_pk_bf16_f32 v50, v84, v85\nv_cvt_pk_bf16_f32 v51, v86, v87\nv_mfma_f32_16x16x32_bf16 v[36:39], v[68:71], v[152:155], v[36:39]\n" \
"v_mfma_f32_16x16x32_bf16 v[36:39], v[80:83], v[156:159], v[36:39]\nv_mfma_f32_16x16x32_bf16 v[52:55], v[56:59], v[200:203], 0\ns_waitcnt lgkmcnt(0)\nbuffer_store_dwordx4 v[40:43], v16, s[8:11], s16 offen offset:1024 sc1\n" \
"v_mfma_f32_16x16x32_bf16 v[52:55], v[64:67], v[204:207], v[52:55]\nv_cvt_pk_bf16_f32 v62, v36, v37\nv_cvt_pk_bf16_f32 v63, v38, v39\nv_mfma_f32_16x16x32_bf16 v[52:55], v[68:71], v[44:47], v[52:55]\n" \
"v_mfma_f32_16x16x32_bf16 v[52:55], v[80:83], v[72:75], v[52:55]\nv_mfma_f32_16x16x32_bf16 v[36:39], v[20:23], a[240:243], 0\nv_mfma_f32_16x16x32_bf16 v[40:43], v[56:59], a[240:243], 0\nv_cvt_pk_bf16_f32 v78, v52, v53\n" \
"v_cvt_pk_bf16_f32 v79, v54, v55\nv_mfma_f32_16x16x32_bf16 v[36:39], v[24:27], a[244:247], v[36:39]\nv_mfma_f32_16x16x32_bf16 v[40:43], v[64:67], a[244:247], v[40:43]\nv_mfma_f32_16x16x32_bf16 v[36:39], v[28:31], a[248:251], v[36:39]\n" \
"buffer_store_dwordx4 v[48:51], v16, s[8:11], s16 offen offset:2048 sc1\nds_read_b128 v[44:47], v17 offset:12288\nv_mfma_f32_16x16x32_bf16 v[40:43], v[68:71], a[248:251], v[40:43]\nv_mfma_f32_16x16x32_bf16 v[36:39], v[32:35], a[252:255], v[36:39]\n" \
"ds_read_b128 v[48:51], v17 offset:13312\nv_mfma_f32_16x16x32_bf16 v[40:43], v[80:83], a[252:255], v[40:43]\nv_mfma_f32_16x16x32_bf16 v[52:55], v[20:23], v[128:131], 0\nds_read_b128 v[72:75], v17 offset:14336\n" \
"v_cvt_pk_bf16_f32 v36, v36, v37\nv_mfma_f32_16x16x32_bf16 v[84:87], v[56:59], v[128:131], 0\nv_cvt_pk_bf16_f32 v37, v38, v39\nv_cvt_pk_bf16_f32 v38, v40, v41\n" \
"v_mfma_f32_16x16x32_bf16 v[52:55], v[24:27], v[132:135], v[52:55]\ns_waitcnt vmcnt(7)\ns_barrier\nv_mfma_f32_16x16x32_bf16 v[84:87], v[64:67], v[132:135], v[84:87]\n" \
"buffer_store_dwordx4 v[60:63], v16, s[8:11], s17 offen offset:3072 sc1\nds_read_b128 v[88:91], v17 offset:15360\nv_cvt_pk_bf16_f32 v39, v42, v43\nv_mfma_f32_16x16x32_bf16 v[52:55], v[28:31], v[136:139], v[52:55]\n" \
"v_mfma_f32_16x16x32_bf16 v[84:87], v[68:71], v[136:139], v[84:87]\nv_mfma_f32_16x16x32_bf16 v[52:55], v[32:35], v[140:143], v[52:55]\ns_waitcnt lgkmcnt(1)\nv_mfma_f32_16x16x32_bf16 v[84:87], v[80:83], v[140:143], v[84:87]\n" \
"s_add_u32 m0, s22, 0x2000\ns_add_u32 s43, s21, 0x2000\ns_add_u32 s21, s21, 0x10000\nv_mfma_f32_16x16x32_bf16 v[40:43], v[20:23], v[44:47], 0\n" \
"v_cvt_pk_bf16_f32 v52, v52, v53\nv_cvt_pk_bf16_f32 v53, v54, v55\nv_mfma_f32_16x16x32_bf16 v[60:63], v[56:59], v[44:47], 0\nv_cvt_pk_bf16_f32 v54, v84, v85\n" \
"v_cvt_pk_bf16_f32 v55, v86, v87\nv_mfma_f32_16x16x32_bf16 v[40:43], v[24:27], v[48:51], v[40:43]\nv_mfma_f32_16x16x32_bf16 v[60:63], v[64:67], v[48:51], v[60:63]\nbuffer_store_dwordx4 v[76:79], v16, s[8:11], s18 offen offset:3072 sc1\n" \
"buffer_load_dwordx4 v16, s[12:15], s43 offen offset:0 sc1 lds\nv_mfma_f32_16x16x32_bf16 v[40:43], v[28:31], v[72:75], v[40:43]\ns_waitcnt lgkmcnt(0)\nds_read_b128 v[44:47], v17 offset:16384\n" \
"v_mfma_f32_16x16x32_bf16 v[60:63], v[68:71], v[72:75], v[60:63]\nv_mfma_f32_16x16x32_bf16 v[40:43], v[32:35], v[88:91], v[40:43]\nds_read_b128 v[48:51], v17 offset:17408\nv_mfma_f32_16x16x32_bf16 v[60:63], v[80:83], v[88:91], v[60:63]\n" \
"v_mfma_f32_16x16x32_bf16 v[72:75], v[20:23], v[112:115], 0\nv_cvt_pk_bf16_f32 v40, v40, v41\nv_cvt_pk_bf16_f32 v41, v42, v43\nv_mfma_f32_16x16x32_bf16 v[76:79], v[56:59], v[112:115], 0\n" \
"v_cvt_pk_bf16_f32 v42, v60, v61\nv_cvt_pk_bf16_f32 v43, v62, v63\nv_mfma_f32_16x16x32_bf16 v[72:75], v[24:27], v[116:119], v[72:75]\nbuffer_store_dwordx4 v[36:39], v16, s[8:11], s16 offen offset:3072 sc1\n" \
"v_mfma_f32_16x16x32_bf16 v[76:79], v[64:67], v[116:119], v[76:79]\nbuffer_load_dwordx4 v16, s[12:15], s43 offen offset:1024 sc1 lds\nv_mfma_f32_16x16x32_bf16 v[72:75], v[28:31], v[120:123], v[72:75]\nv_mfma_f32_16x16x32_bf16 v[76:79], v[68:71], v[120:123], v[76:79]\n" \
"v_mfma_f32_16x16x32_bf16 v[72:75], v[32:35], v[124:127], v[72:75]\ns_waitcnt lgkmcnt(0)\nv_mfma_f32_16x16x32_bf16 v[76:79], v[80:83], v[124:127], v[76:79]\nv_mfma_f32_16x16x32_bf16 v[36:39], v[20:23], v[44:47], 0\n" \
"v_cvt_pk_bf16_f32 v72, v72, v73\nv_cvt_pk_bf16_f32 v73, v74, v75\nv_mfma_f32_16x16x32_bf16 v[60:63], v[56:59], v[44:47], 0\nbuffer_store_dwordx4 v[52:55], v16, s[8:11], s17 offen offset:2048 sc1\n" \
"v_cvt_pk_bf16_f32 v74, v76, v77\nv_cvt_pk_bf16_f32 v75, v78, v79\nv_mfma_f32_16x16x32_bf16 v[36:39], v[24:27], v[48:51], v[36:39]\nv_mfma_f32_16x16x32_bf16 v[60:63], v[64:67], v[48:51], v[60:63]\n" \
"buffer_load_dwordx4 v16, s[12:15], s43 offen offset:2048 sc1 lds\nv_mfma_f32_16x16x32_bf16 v[36:39], v[28:31], v[176:179], v[36:39]\nv_mfma_f32_16x16x32_bf16 v[60:63], v[68:71], v[176:179], v[60:63]\nv_mfma_f32_16x16x32_bf16 v[36:39], v[32:35], v[180:183], v[36:39]\n" \
"v_mfma_f32_16x16x32_bf16 v[60:63], v[80:83], v[180:183], v[60:63]\nv_mfma_f32_16x16x32_bf16 v[44:47], v[20:23], v[184:187], 0\nv_cvt_pk_bf16_f32 v36, v36, v37\nv_cvt_pk_bf16_f32 v37, v38, v39\n" \
"v_mfma_f32_16x16x32_bf16 v[48:51], v[56:59], v[184:187], 0\nbuffer_store_dwordx4 v[40:43], v16, s[8:11], s17 offen offset:1024 sc1\nv_cvt_pk_bf16_f32 v38, v60, v61\nv_cvt_pk_bf16_f32 v39, v62, v63\n" \
"v_mfma_f32_16x16x32_bf16 v[44:47], v[24:27], v[188:191], v[44:47]\nv_mfma_f32_16x16x32_bf16 v[48:51], v[64:67], v[188:191], v[48:51]\nbuffer_load_dwordx4 v16, s[12:15], s43 offen offset:3072 sc1 lds\nv_mfma_f32_16x16x32_bf16 v[44:47], v[28:31], v[192:195], v[44:47]\n" \
"v_mfma_f32_16x16x32_bf16 v[48:51], v[68:71], v[192:195], v[48:51]\nv_mfma_f32_16x16x32_bf16 v[44:47], v[32:35], v[196:199], v[44:47]\nv_mfma_f32_16x16x32_bf16 v[48:51], v[80:83], v[196:199], v[48:51]\nv_mfma_f32_16x16x32_bf16 v[40:43], v[20:23], v[160:163], 0\n" \
"buffer_store_dwordx4 v[72:75], v16, s[8:11], s17 offen offset:0 sc1\nv_cvt_pk_bf16_f32 v44, v44, v45\nv_cvt_pk_bf16_f32 v45, v46, v47\nv_mfma_f32_16x16x32_bf16 v[52:55], v[56:59], v[160:163], 0\n" \
"v_cvt_pk_bf16_f32 v46, v48, v49\nv_cvt_pk_bf16_f32 v47, v50, v51\nv_mfma_f32_16x16x32_bf16 v[40:43], v[24:27], v[164:167], v[40:43]\nv_mfma_f32_16x16x32_bf16 v[52:55], v[64:67], v[164:167], v[52:55]\n" \
"v_mfma_f32_16x16x32_bf16 v[40:43], v[28:31], v[168:171], v[40:43]\nds_read_b128 v[48:51], v17 offset:20480\nv_mfma_f32_16x16x32_bf16 v[52:55], v[68:71], v[168:171], v[52:55]\nv_mfma_f32_16x16x32_bf16 v[40:43], v[32:35], v[172:175], v[40:43]\n" \
"ds_read_b128 v[60:63], v17 offset:21504\nv_mfma_f32_16x16x32_bf16 v[52:55], v[80:83], v[172:175], v[52:55]\nv_mfma_f32_16x16x32_bf16 v[72:75], v[20:23], v[232:235], 0\nbuffer_store_dwordx4 v[36:39], v16, s[8:11], s18 offen offset:1024 sc1\n" \
"v_cvt_pk_bf16_f32 v40, v40, v41\nv_cvt_pk_bf16_f32 v41, v42, v43\nv_mfma_f32_16x16x32_bf16 v[36:39], v[56:59], v[232:235], 0\nv_cvt_pk_bf16_f32 v42, v52, v53\n" \
"v_cvt_pk_bf16_f32 v43, v54, v55\nv_mfma_f32_16x16x32_bf16 v[72:75], v[24:27], v[236:239], v[72:75]\nv_mfma_f32_16x16x32_bf16 v[36:39], v[64:67], v[236:239], v[36:39]\nv_mfma_f32_16x16x32_bf16 v[72:75], v[28:31], v[240:243], v[72:75]\n" \
"v_mfma_f32_16x16x32_bf16 v[36:39], v[68:71], v[240:243], v[36:39]\nv_mfma_f32_16x16x32_bf16 v[72:75], v[32:35], v[244:247], v[72:75]\ns_waitcnt lgkmcnt(0)\nv_mfma_f32_16x16x32_bf16 v[36:39], v[80:83], v[244:247], v[36:39]\n" \
"buffer_store_dwordx4 v[44:47], v16, s[8:11], s18 offen offset:2048 sc1\nv_mfma_f32_16x16x32_bf16 v[52:55], v[20:23], v[48:51], 0\nv_cvt_pk_bf16_f32 v72, v72, v73\nv_cvt_pk_bf16_f32 v73, v74, v75\n" \
"v_mfma_f32_16x16x32_bf16 v[44:47], v[56:59], v[48:51], 0\nv_cvt_pk_bf16_f32 v74, v36, v37\nv_cvt_pk_bf16_f32 v75, v38, v39\nv_mfma_f32_16x16x32_bf16 v[52:55], v[24:27], v[60:63], v[52:55]\n" \
"v_mfma_f32_16x16x32_bf16 v[44:47], v[64:67], v[60:63], v[44:47]\nv_mfma_f32_16x16x32_bf16 v[52:55], v[28:31], v[224:227], v[52:55]\nds_read_b128 v[36:39], v17 offset:22528\nv_mfma_f32_16x16x32_bf16 v[44:47], v[68:71], v[224:227], v[44:47]\n" \
"v_mfma_f32_16x16x32_bf16 v[52:55], v[32:35], v[228:231], v[52:55]\nbuffer_store_dwordx4 v[40:43], v16, s[8:11], s18 offen offset:0 sc1\nds_read_b128 v[48:51], v17 offset:23552\nv_mfma_f32_16x16x32_bf16 v[44:47], v[80:83], v[228:231], v[44:47]\n" \
"v_mfma_f32_16x16x32_bf16 v[40:43], v[20:23], v[208:211], 0\nv_cvt_pk_bf16_f32 v52, v52, v53\nv_cvt_pk_bf16_f32 v53, v54, v55\nv_mfma_f32_16x16x32_bf16 v[60:63], v[56:59], v[208:211], 0\n" \
"v_cvt_pk_bf16_f32 v54, v44, v45\nv_cvt_pk_bf16_f32 v55, v46, v47\nv_mfma_f32_16x16x32_bf16 v[40:43], v[24:27], v[212:215], v[40:43]\nv_mfma_f32_16x16x32_bf16 v[60:63], v[64:67], v[212:215], v[60:63]\n" \
"ds_read_b128 v[44:47], v16 offset:0\nv_mfma_f32_16x16x32_bf16 v[40:43], v[28:31], v[216:219], v[40:43]\nds_read_b128 v[76:79], v16 offset:32768\nv_mfma_f32_16x16x32_bf16 v[60:63], v[68:71], v[216:219], v[60:63]\n" \
"v_mfma_f32_16x16x32_bf16 v[40:43], v[32:35], v[220:223], v[40:43]\ns_waitcnt lgkmcnt(2)\nbuffer_store_dwordx4 v[72:75], v16, s[8:11], s19 offen offset:2048 sc1\nv_mfma_f32_16x16x32_bf16 v[60:63], v[80:83], v[220:223], v[60:63]\n" \
"ds_read_b128 v[84:87], v16 offset:1024\nv_mfma_f32_16x16x32_bf16 v[72:75], v[20:23], v[36:39], 0\nds_read_b128 v[88:91], v16 offset:33792\nv_cvt_pk_bf16_f32 v40, v40, v41\n" \
"v_mfma_f32_16x16x32_bf16 v[20:23], v[56:59], v[36:39], 0\nv_cvt_pk_bf16_f32 v41, v42, v43\nv_cvt_pk_bf16_f32 v42, v60, v61\nv_mfma_f32_16x16x32_bf16 v[72:75], v[24:27], v[48:51], v[72:75]\n" \
"v_cvt_pk_bf16_f32 v43, v62, v63\nv_mfma_f32_16x16x32_bf16 v[20:23], v[64:67], v[48:51], v[20:23]\nds_read_b128 v[24:27], v16 offset:2048\nv_mfma_f32_16x16x32_bf16 v[72:75], v[28:31], v[248:251], v[72:75]\n" \
"ds_read_b128 v[36:39], v16 offset:34816\nv_mfma_f32_16x16x32_bf16 v[20:23], v[68:71], v[248:251], v[20:23]\nbuffer_store_dwordx4 v[52:55], v16, s[8:11], s19 offen offset:1024 sc1\nv_mfma_f32_16x16x32_bf16 v[72:75], v[32:35], v[252:255], v[72:75]\n" \
"s_waitcnt lgkmcnt(2)\nv_mfma_f32_16x16x32_bf16 v[20:23], v[80:83], v[252:255], v[20:23]\nds_read_b128 v[28:31], v16 offset:3072\nv_mfma_f32_16x16x32_bf16 v[32:35], v[0:3], v[44:47], 0\n" \
"ds_read_b128 v[48:51], v16 offset:35840\nv_cvt_pk_bf16_f32 v72, v72, v73\nv_mfma_f32_16x16x32_bf16 v[52:55], v[0:3], v[76:79], 0\nv_cvt_pk_bf16_f32 v73, v74, v75\n" \
"v_cvt_pk_bf16_f32 v74, v20, v21\nv_mfma_f32_16x16x32_bf16 v[56:59], v[8:11], v[44:47], 0\nv_cvt_pk_bf16_f32 v75, v22, v23\nv_mfma_f32_16x16x32_bf16 v[44:47], v[8:11], v[76:79], 0\n" \
"ds_read_b128 v[20:23], v16 offset:4096\nv_mfma_f32_16x16x32_bf16 v[32:35], v[4:7], v[84:87], v[32:35]\nds_read_b128 v[60:63], v16 offset:36864\nv_mfma_f32_16x16x32_bf16 v[52:55], v[4:7], v[88:91], v[52:55]\n" \
"buffer_store_dwordx4 v[40:43], v16, s[8:11], s19 offen offset:0 sc1\nv_mfma_f32_16x16x32_bf16 v[56:59], v[12:15], v[84:87], v[56:59]\ns_waitcnt lgkmcnt(2)\nv_mfma_f32_16x16x32_bf16 v[44:47], v[12:15], v[88:91], v[44:47]\n" \
"ds_read_b128 v[40:43], v16 offset:5120\nv_mfma_f32_16x16x32_bf16 v[32:35], a[0:3], v[24:27], v[32:35]\nds_read_b128 v[64:67], v16 offset:37888\nv_mfma_f32_16x16x32_bf16 v[52:55], a[0:3], v[36:39], v[52:55]\n" \
"v_mfma_f32_16x16x32_bf16 v[56:59], a[104:107], v[24:27], v[56:59]\nv_mfma_f32_16x16x32_bf16 v[44:47], a[104:107], v[36:39], v[44:47]\nds_read_b128 v[24:27], v16 offset:6144\nv_mfma_f32_16x16x32_bf16 v[32:35], a[4:7], v[28:31], v[32:35]\n" \
"buffer_store_dwordx4 v[72:75], v16, s[8:11], s19 offen offset:3072 sc1\nds_read_b128 v[36:39], v16 offset:38912\nv_mfma_f32_16x16x32_bf16 v[52:55], a[4:7], v[48:51], v[52:55]\nds_read_b128 v[68:71], v16 offset:7168\n" \
"v_mfma_f32_16x16x32_bf16 v[56:59], a[108:111], v[28:31], v[56:59]\nds_read_b128 v[72:75], v16 offset:39936\nv_mfma_f32_16x16x32_bf16 v[44:47], a[108:111], v[48:51], v[44:47]\ns_branch 1b\n" \
"2:\ns_cmp_lt_i32 s20, 1\ns_cbranch_scc1 3b\ns_add_u32 s30, s30, 1\n" \
"s_cmp_lt_u32 s30, 0x40000\ns_cbranch_scc0 9f\nv_max_u32 v109, v76, v78\nv_cmp_eq_u32 vcc, -1, v109\n" \
"s_cbranch_vccz 6f\nbuffer_load_dwordx4 v16, s[8:11], s43 offen offset:0 sc1 lds\n6:\nv_max_u32 v109, v88, v90\n" \
"v_cmp_eq_u32 vcc, -1, v109\ns_cbranch_vccz 6f\nbuffer_load_dwordx4 v16, s[8:11], s43 offen offset:1024 sc1 lds\n6:\n" \
"v_max_u32 v109, v92, v94\nv_cmp_eq_u32 vcc, -1, v109\ns_cbranch_vccz 6f\nbuffer_load_dwordx4 v16, s[8:11], s43 offen offset:2048 sc1 lds\n" \
"6:\nv_max_u32 v109, v72, v74\nv_cmp_eq_u32 vcc, -1, v109\ns_cbranch_vccz 6f\n" \
"buffer_load_dwordx4 v16, s[8:11], s43 offen offset:3072 sc1 lds\n6:\ns_waitcnt vmcnt(0)\nds_read_b128 v[76:79], v110 offset:0\n" \
"ds_read_b128 v[88:91], v110 offset:1024\nds_read_b128 v[92:95], v110 offset:2048\nds_read_b128 v[72:75], v110 offset:3072\ns_waitcnt lgkmcnt(0)\n" \
"v_max3_u32 v109, v76, v78, v88\nv_max3_u32 v109, v109, v90, v92\nv_max3_u32 v109, v109, v94, v72\nv_max_u32 v109, v109, v74\n" \
"v_cmp_eq_u32 vcc, -1, v109\ns_cbranch_vccnz 2b\ns_branch 3b\n4:\n" \
"s_cmp_ge_i32 s20, 64\ns_cbranch_scc1 9f\ns_cmp_ge_i32 s20, 62\ns_cbranch_scc1 5b\n" \
"v_bfe_u32 v110, v16, 4, 3\nv_lshlrev_b32 v110, 2, v110\ns_lshl_b32 s43, s20, 5\ns_add_u32 s43, s43, 64\n" \
"s_add_u32 s43, s43, s26\n7:\nv_cmp_eq_u32 vcc, -1, v111\ns_cbranch_vccz 8f\n" \
"s_add_u32 s30, s30, 1\ns_cmp_lt_u32 s30, 0x40000\ns_cbranch_scc0 9f\ns_sleep 8\n" \
"buffer_load_dword v111, v110, s[8:11], s43 offen sc1\ns_waitcnt vmcnt(0)\ns_branch 7b\n8:\n" \
"s_mov_b32 s14, s10\ns_branch 5b\n9:\ns_waitcnt vmcnt(0)\n" \
"s_endpgm\n"
#define D_LOOP \
"s_branch 4f\n.p2align 6\n1:\ns_waitcnt lgkmcnt(0)\n" \
"v_mfma_f32_16x16x32_bf16 v[44:47], a[8:11], v[8:11], v[44:47]\nds_read_b128 v[16:19], v4 offset:3072\ns_mov_b32 m0, s24\nv_mfma_f32_16x16x32_bf16 v[64:67], a[8:11], v[12:15], v[64:67]\n" \
"ds_read_b128 v[32:35], v4 offset:35840\nbuffer_load_dword v1, s[16:19], 0 offen offset:0 sc1 lds\nv_mfma_f32_16x16x32_bf16 v[48:51], a[104:107], v[8:11], v[48:51]\ns_add_u32 m0, s23, 0\n" \
"s_and_b32 s98, s20, 1\ns_lshl_b32 s98, s98, 3\nv_mfma_f32_16x16x32_bf16 v[20:23], a[104:107], v[12:15], v[20:23]\nv_mfma_f32_16x16x32_bf16 v[56:59], a[200:203], v[8:11], v[56:59]\n" \
"v_mfma_f32_16x16x32_bf16 v[24:27], a[200:203], v[12:15], v[24:27]\nv_mfma_f32_16x16x32_bf16 v[60:63], v[152:155], v[8:11], v[60:63]\ns_waitcnt lgkmcnt(1)\nv_mfma_f32_16x16x32_bf16 v[28:31], v[152:155], v[12:15], v[28:31]\n" \
"s_waitcnt lgkmcnt(0)\nds_read_b128 v[8:11], v4 offset:4096\nv_mfma_f32_16x16x32_bf16 v[44:47], a[12:15], v[16:19], v[44:47]\nds_read_b128 v[12:15], v4 offset:36864\n" \
"v_mfma_f32_16x16x32_bf16 v[64:67], a[12:15], v[32:35], v[64:67]\nv_mfma_f32_16x16x32_bf16 v[48:51], a[108:111], v[16:19], v[48:51]\nv_mfma_f32_16x16x32_bf16 v[20:23], a[108:111], v[32:35], v[20:23]\nv_mfma_f32_16x16x32_bf16 v[56:59], a[204:207], v[16:19], v[56:59]\n" \
"v_mfma_f32_16x16x32_bf16 v[24:27], a[204:207], v[32:35], v[24:27]\nv_mfma_f32_16x16x32_bf16 v[60:63], v[156:159], v[16:19], v[60:63]\ns_waitcnt lgkmcnt(0)\nv_mfma_f32_16x16x32_bf16 v[28:31], v[156:159], v[32:35], v[28:31]\n" \
"ds_read_b128 v[16:19], v4 offset:5120\nv_mfma_f32_16x16x32_bf16 v[44:47], a[16:19], v[8:11], v[44:47]\nds_read_b128 v[32:35], v4 offset:37888\nv_mfma_f32_16x16x32_bf16 v[64:67], a[16:19], v[12:15], v[64:67]\n" \
"v_mfma_f32_16x16x32_bf16 v[48:51], a[112:115], v[8:11], v[48:51]\nv_mfma_f32_16x16x32_bf16 v[20:23], a[112:115], v[12:15], v[20:23]\nv_mfma_f32_16x16x32_bf16 v[56:59], a[208:211], v[8:11], v[56:59]\nv_mfma_f32_16x16x32_bf16 v[24:27], a[208:211], v[12:15], v[24:27]\n" \
"v_mfma_f32_16x16x32_bf16 v[60:63], v[160:163], v[8:11], v[60:63]\ns_waitcnt lgkmcnt(0)\nv_mfma_f32_16x16x32_bf16 v[28:31], v[160:163], v[12:15], v[28:31]\nds_read_b128 v[8:11], v4 offset:6144\n" \
"s_add_u32 s99, s20, 1\nv_mfma_f32_16x16x32_bf16 v[44:47], a[20:23], v[16:19], v[44:47]\nds_read_b128 v[12:15], v4 offset:38912\ns_lshr_b32 s97, s99, 1\n" \
"v_mfma_f32_16x16x32_bf16 v[64:67], a[20:23], v[32:35], v[64:67]\ns_and_b32 s99, s99, 1\ns_lshl_b32 s99, s99, 3\nv_readlane_b32 s97, v3, s97\n" \
"v_mfma_f32_16x16x32_bf16 v[48:51], a[116:119], v[16:19], v[48:51]\nv_mfma_f32_16x16x32_bf16 v[20:23], a[116:119], v[32:35], v[20:23]\ns_lshr_b32 s97, s97, s99\ns_and_b32 s97, s97, 0xff\n" \
"s_mul_i32 s97, s97, 0x38000\nv_mfma_f32_16x16x32_bf16 v[56:59], a[212:215], v[16:19], v[56:59]\ns_add_u32 s97, s97, s21\nv_mfma_f32_16x16x32_bf16 v[24:27], a[212:215], v[32:35], v[24:27]\n" \
"v_mfma_f32_16x16x32_bf16 v[60:63], v[164:167], v[16:19], v[60:63]\ns_waitcnt lgkmcnt(0)\nv_mfma_f32_16x16x32_bf16 v[28:31], v[164:167], v[32:35], v[28:31]\nds_read_b128 v[16:19], v4 offset:7168\n" \
"v_mfma_f32_16x16x32_bf16 v[44:47], a[24:27], v[8:11], v[44:47]\nds_read_b128 v[32:35], v4 offset:39936\nv_mfma_f32_16x16x32_bf16 v[64:67], a[24:27], v[12:15], v[64:67]\nv_mfma_f32_16x16x32_bf16 v[48:51], a[120:123], v[8:11], v[48:51]\n" \
"v_mfma_f32_16x16x32_bf16 v[20:23], a[120:123], v[12:15], v[20:23]\nv_mfma_f32_16x16x32_bf16 v[56:59], a[216:219], v[8:11], v[56:59]\nv_mfma_f32_16x16x32_bf16 v[24:27], a[216:219], v[12:15], v[24:27]\nv_mfma_f32_16x16x32_bf16 v[60:63], v[168:171], v[8:11], v[60:63]\n" \
"s_waitcnt lgkmcnt(0)\nv_mfma_f32_16x16x32_bf16 v[28:31], v[168:171], v[12:15], v[28:31]\nds_read_b128 v[8:11], v4 offset:8192\nv_mov_b32 v111, s20\n" \
"v_mfma_f32_16x16x32_bf16 v[44:47], a[28:31], v[16:19], v[44:47]\nds_read_b128 v[12:15], v4 offset:40960\ns_add_u32 s99, s20, 2\nv_mfma_f32_16x16x32_bf16 v[64:67], a[28:31], v[32:35], v[64:67]\n" \
"v_mfma_f32_16x16x32_bf16 v[48:51], a[124:127], v[16:19], v[48:51]\nv_mfma_f32_16x16x32_bf16 v[20:23], a[124:127], v[32:35], v[20:23]\nv_mfma_f32_16x16x32_bf16 v[56:59], a[220:223], v[16:19], v[56:59]\ns_waitcnt vmcnt(0)\n" \
"s_barrier\nv_mfma_f32_16x16x32_bf16 v[24:27], a[220:223], v[32:35], v[24:27]\nbuffer_store_dword v111, v2, s[8:11], s25 offen offset:0 sc1\nv_mfma_f32_16x16x32_bf16 v[60:63], v[172:175], v[16:19], v[60:63]\n" \
"ds_read_b32 v110, v5 offset:0\ns_mov_b32 s25, 0\nv_mfma_f32_16x16x32_bf16 v[28:31], v[172:175], v[32:35], v[28:31]\ns_waitcnt lgkmcnt(1)\n" \
"ds_read_b128 v[16:19], v4 offset:9216\nv_mfma_f32_16x16x32_bf16 v[44:47], a[32:35], v[8:11], v[44:47]\nds_read_b128 v[32:35], v4 offset:41984\nv_mfma_f32_16x16x32_bf16 v[64:67], a[32:35], v[12:15], v[64:67]\n" \
"v_mfma_f32_16x16x32_bf16 v[48:51], a[128:131], v[8:11], v[48:51]\ns_waitcnt lgkmcnt(2)\nv_cmp_gt_i32 s[94:95], s99, v110\nv_mfma_f32_16x16x32_bf16 v[20:23], a[128:131], v[12:15], v[20:23]\n" \
"v_mfma_f32_16x16x32_bf16 v[56:59], a[224:227], v[8:11], v[56:59]\ns_cmp_eq_u64 s[94:95], 0\ns_cselect_b32 s14, s10, 0\nv_mfma_f32_16x16x32_bf16 v[24:27], a[224:227], v[12:15], v[24:27]\n" \
"v_mfma_f32_16x16x32_bf16 v[60:63], v[176:179], v[8:11], v[60:63]\ns_waitcnt lgkmcnt(0)\nv_mfma_f32_16x16x32_bf16 v[28:31], v[176:179], v[12:15], v[28:31]\nds_read_b128 v[8:11], v4 offset:10240\n" \
"v_mfma_f32_16x16x32_bf16 v[44:47], a[36:39], v[16:19], v[44:47]\nds_read_b128 v[12:15], v4 offset:43008\nv_mfma_f32_16x16x32_bf16 v[64:67], a[36:39], v[32:35], v[64:67]\nv_mfma_f32_16x16x32_bf16 v[48:51], a[132:135], v[16:19], v[48:51]\n" \
"v_mfma_f32_16x16x32_bf16 v[20:23], a[132:135], v[32:35], v[20:23]\nbuffer_load_dwordx4 v0, s[12:15], s97 offen offset:0 sc1 lds\nv_mfma_f32_16x16x32_bf16 v[56:59], a[228:231], v[16:19], v[56:59]\nv_mfma_f32_16x16x32_bf16 v[24:27], a[228:231], v[32:35], v[24:27]\n" \
"v_mfma_f32_16x16x32_bf16 v[60:63], v[180:183], v[16:19], v[60:63]\ns_waitcnt lgkmcnt(0)\nv_mfma_f32_16x16x32_bf16 v[28:31], v[180:183], v[32:35], v[28:31]\nbuffer_load_dwordx4 v0, s[12:15], s97 offen offset:1024 sc1 lds\n" \
"v_mfma_f32_16x16x32_bf16 v[44:47], a[40:43], v[8:11], v[44:47]\nds_read_b128 v[16:19], v4 offset:11264\nv_mfma_f32_16x16x32_bf16 v[64:67], a[40:43], v[12:15], v[64:67]\nds_read_b128 v[32:35], v4 offset:44032\n" \
"v_mfma_f32_16x16x32_bf16 v[48:51], a[136:139], v[8:11], v[48:51]\nv_mfma_f32_16x16x32_bf16 v[20:23], a[136:139], v[12:15], v[20:23]\nbuffer_load_dwordx4 v0, s[12:15], s97 offen offset:2048 sc1 lds\nv_mfma_f32_16x16x32_bf16 v[56:59], a[232:235], v[8:11], v[56:59]\n" \
"v_mfma_f32_16x16x32_bf16 v[24:27], a[232:235], v[12:15], v[24:27]\nv_mfma_f32_16x16x32_bf16 v[60:63], v[184:187], v[8:11], v[60:63]\ns_waitcnt lgkmcnt(1)\nv_mfma_f32_16x16x32_bf16 v[28:31], v[184:187], v[12:15], v[28:31]\n" \
"s_waitcnt lgkmcnt(0)\nbuffer_load_dwordx4 v0, s[12:15], s97 offen offset:3072 sc1 lds\nv_mfma_f32_16x16x32_bf16 v[44:47], a[44:47], v[16:19], v[44:47]\nds_read_b128 v[8:11], v4 offset:12288\n" \
"s_add_u32 m0, s23, 0x1000\nv_mfma_f32_16x16x32_bf16 v[64:67], a[44:47], v[32:35], v[64:67]\nds_read_b128 v[12:15], v4 offset:45056\ns_add_u32 s97, s97, 0x1000\n" \
"v_mfma_f32_16x16x32_bf16 v[48:51], a[140:143], v[16:19], v[48:51]\nv_mfma_f32_16x16x32_bf16 v[20:23], a[140:143], v[32:35], v[20:23]\nbuffer_load_dwordx4 v0, s[12:15], s97 offen offset:0 sc1 lds\nv_mfma_f32_16x16x32_bf16 v[56:59], a[236:239], v[16:19], v[56:59]\n" \
"v_mfma_f32_16x16x32_bf16 v[24:27], a[236:239], v[32:35], v[24:27]\nv_mfma_f32_16x16x32_bf16 v[60:63], v[188:191], v[16:19], v[60:63]\ns_waitcnt lgkmcnt(1)\nv_mfma_f32_16x16x32_bf16 v[28:31], v[188:191], v[32:35], v[28:31]\n" \
"s_waitcnt lgkmcnt(0)\nbuffer_load_dwordx4 v0, s[12:15], s97 offen offset:1024 sc1 lds\nv_mfma_f32_16x16x32_bf16 v[44:47], a[48:51], v[8:11], v[44:47]\nds_read_b128 v[16:19], v4 offset:13312\n" \
"v_mfma_f32_16x16x32_bf16 v[64:67], a[48:51], v[12:15], v[64:67]\nds_read_b128 v[32:35], v4 offset:46080\nv_mfma_f32_16x16x32_bf16 v[48:51], a[144:147], v[8:11], v[48:51]\nv_mfma_f32_16x16x32_bf16 v[20:23], a[144:147], v[12:15], v[20:23]\n" \
"buffer_load_dwordx4 v0, s[12:15], s97 offen offset:2048 sc1 lds\nv_mfma_f32_16x16x32_bf16 v[56:59], a[240:243], v[8:11], v[56:59]\nv_mfma_f32_16x16x32_bf16 v[24:27], a[240:243], v[12:15], v[24:27]\nv_mfma_f32_16x16x32_bf16 v[60:63], v[192:195], v[8:11], v[60:63]\n" \
"s_waitcnt lgkmcnt(1)\nv_mfma_f32_16x16x32_bf16 v[28:31], v[192:195], v[12:15], v[28:31]\ns_waitcnt lgkmcnt(0)\nbuffer_load_dwordx4 v0, s[12:15], s97 offen offset:3072 sc1 lds\n" \
"v_mfma_f32_16x16x32_bf16 v[44:47], a[52:55], v[16:19], v[44:47]\nds_read_b128 v[8:11], v4 offset:14336\ns_add_u32 m0, s23, 0x2000\nv_mfma_f32_16x16x32_bf16 v[64:67], a[52:55], v[32:35], v[64:67]\n" \
"ds_read_b128 v[12:15], v4 offset:47104\ns_add_u32 s97, s97, 0x1000\nv_mfma_f32_16x16x32_bf16 v[48:51], a[148:151], v[16:19], v[48:51]\ns_xor_b32 s23, s23, 0x10000\n" \
"v_mfma_f32_16x16x32_bf16 v[20:23], a[148:151], v[32:35], v[20:23]\nbuffer_load_dwordx4 v0, s[12:15], s97 offen offset:0 sc1 lds\nv_mfma_f32_16x16x32_bf16 v[56:59], a[244:247], v[16:19], v[56:59]\nv_mfma_f32_16x16x32_bf16 v[24:27], a[244:247], v[32:35], v[24:27]\n" \
"v_mfma_f32_16x16x32_bf16 v[60:63], v[196:199], v[16:19], v[60:63]\ns_waitcnt lgkmcnt(1)\nv_mfma_f32_16x16x32_bf16 v[28:31], v[196:199], v[32:35], v[28:31]\ns_waitcnt lgkmcnt(0)\n" \
"buffer_load_dwordx4 v0, s[12:15], s97 offen offset:1024 sc1 lds\nv_mfma_f32_16x16x32_bf16 v[44:47], a[56:59], v[8:11], v[44:47]\nds_read_b128 v[16:19], v4 offset:15360\nv_mfma_f32_16x16x32_bf16 v[64:67], a[56:59], v[12:15], v[64:67]\n" \
"ds_read_b128 v[32:35], v4 offset:48128\nv_mfma_f32_16x16x32_bf16 v[48:51], a[152:155], v[8:11], v[48:51]\nv_mfma_f32_16x16x32_bf16 v[20:23], a[152:155], v[12:15], v[20:23]\nbuffer_load_dwordx4 v0, s[12:15], s97 offen offset:2048 sc1 lds\n" \
"v_mfma_f32_16x16x32_bf16 v[56:59], a[248:251], v[8:11], v[56:59]\nv_mfma_f32_16x16x32_bf16 v[24:27], a[248:251], v[12:15], v[24:27]\nv_mfma_f32_16x16x32_bf16 v[60:63], v[200:203], v[8:11], v[60:63]\ns_waitcnt lgkmcnt(1)\n" \
"v_mfma_f32_16x16x32_bf16 v[28:31], v[200:203], v[12:15], v[28:31]\ns_waitcnt lgkmcnt(0)\nbuffer_load_dwordx4 v0, s[12:15], s97 offen offset:3072 sc1 lds\nv_mfma_f32_16x16x32_bf16 v[44:47], a[60:63], v[16:19], v[44:47]\n" \
"ds_read_b128 v[8:11], v4 offset:16384\nv_mfma_f32_16x16x32_bf16 v[64:67], a[60:63], v[32:35], v[64:67]\nds_read_b128 v[12:15], v4 offset:49152\nv_mfma_f32_16x16x32_bf16 v[48:51], a[156:159], v[16:19], v[48:51]\n" \
"v_mfma_f32_16x16x32_bf16 v[20:23], a[156:159], v[32:35], v[20:23]\nv_mfma_f32_16x16x32_bf16 v[56:59], a[252:255], v[16:19], v[56:59]\nv_mfma_f32_16x16x32_bf16 v[24:27], a[252:255], v[32:35], v[24:27]\nv_mfma_f32_16x16x32_bf16 v[60:63], v[204:207], v[16:19], v[60:63]\n" \
"s_waitcnt lgkmcnt(1)\nv_mfma_f32_16x16x32_bf16 v[28:31], v[204:207], v[32:35], v[28:31]\ns_waitcnt lgkmcnt(0)\nds_read_b128 v[16:19], v4 offset:17408\n" \
"v_mfma_f32_16x16x32_bf16 v[44:47], a[64:67], v[8:11], v[44:47]\nds_read_b128 v[32:35], v4 offset:50176\nv_mfma_f32_16x16x32_bf16 v[64:67], a[64:67], v[12:15], v[64:67]\nv_mfma_f32_16x16x32_bf16 v[48:51], a[160:163], v[8:11], v[48:51]\n" \
"v_mfma_f32_16x16x32_bf16 v[20:23], a[160:163], v[12:15], v[20:23]\nv_mfma_f32_16x16x32_bf16 v[56:59], v[112:115], v[8:11], v[56:59]\nv_mfma_f32_16x16x32_bf16 v[24:27], v[112:115], v[12:15], v[24:27]\nv_mfma_f32_16x16x32_bf16 v[60:63], v[208:211], v[8:11], v[60:63]\n" \
"s_waitcnt lgkmcnt(0)\nv_mfma_f32_16x16x32_bf16 v[28:31], v[208:211], v[12:15], v[28:31]\nds_read_b128 v[8:11], v4 offset:18432\nv_mfma_f32_16x16x32_bf16 v[44:47], a[68:71], v[16:19], v[44:47]\n" \
"ds_read_b128 v[12:15], v4 offset:51200\nv_mfma_f32_16x16x32_bf16 v[64:67], a[68:71], v[32:35], v[64:67]\nv_mfma_f32_16x16x32_bf16 v[48:51], a[164:167], v[16:19], v[48:51]\nv_mfma_f32_16x16x32_bf16 v[20:23], a[164:167], v[32:35], v[20:23]\n" \
"s_lshr_b32 s99, s20, 1\ns_add_u32 s20, s20, 1\nv_readlane_b32 s99, v3, s99\nv_mfma_f32_16x16x32_bf16 v[56:59], v[116:119], v[16:19], v[56:59]\n" \
"v_mfma_f32_16x16x32_bf16 v[24:27], v[116:119], v[32:35], v[24:27]\ns_lshr_b32 s99, s99, s98\ns_and_b32 s99, s99, 0xff\ns_mul_i32 s99, s99, 0x50000\n" \
"v_mfma_f32_16x16x32_bf16 v[60:63], v[212:215], v[16:19], v[60:63]\ns_waitcnt lgkmcnt(0)\ns_add_u32 s99, s99, s22\ns_add_u32 s98, s99, 0x8000\n" \
"v_mfma_f32_16x16x32_bf16 v[28:31], v[212:215], v[32:35], v[28:31]\nds_read_b128 v[16:19], v4 offset:19456\nv_mfma_f32_16x16x32_bf16 v[44:47], a[72:75], v[8:11], v[44:47]\nds_read_b128 v[32:35], v4 offset:52224\n" \
"v_mfma_f32_16x16x32_bf16 v[64:67], a[72:75], v[12:15], v[64:67]\nv_mfma_f32_16x16x32_bf16 v[48:51], a[168:171], v[8:11], v[48:51]\nv_mfma_f32_16x16x32_bf16 v[20:23], a[168:171], v[12:15], v[20:23]\nv_mfma_f32_16x16x32_bf16 v[56:59], v[120:123], v[8:11], v[56:59]\n" \
"v_mfma_f32_16x16x32_bf16 v[24:27], v[120:123], v[12:15], v[24:27]\nv_mfma_f32_16x16x32_bf16 v[60:63], v[216:219], v[8:11], v[60:63]\ns_waitcnt lgkmcnt(0)\nv_mfma_f32_16x16x32_bf16 v[28:31], v[216:219], v[12:15], v[28:31]\n" \
"ds_read_b128 v[8:11], v4 offset:20480\nv_mfma_f32_16x16x32_bf16 v[44:47], a[76:79], v[16:19], v[44:47]\nds_read_b128 v[12:15], v4 offset:53248\nv_mfma_f32_16x16x32_bf16 v[64:67], a[76:79], v[32:35], v[64:67]\n" \
"v_mfma_f32_16x16x32_bf16 v[48:51], a[172:175], v[16:19], v[48:51]\nv_mfma_f32_16x16x32_bf16 v[20:23], a[172:175], v[32:35], v[20:23]\nv_mfma_f32_16x16x32_bf16 v[56:59], v[124:127], v[16:19], v[56:59]\nv_mfma_f32_16x16x32_bf16 v[24:27], v[124:127], v[32:35], v[24:27]\n" \
"v_mfma_f32_16x16x32_bf16 v[60:63], v[220:223], v[16:19], v[60:63]\ns_waitcnt lgkmcnt(0)\nv_mfma_f32_16x16x32_bf16 v[28:31], v[220:223], v[32:35], v[28:31]\nds_read_b128 v[36:39], v4 offset:21504\n" \
"v_mfma_f32_16x16x32_bf16 v[44:47], a[80:83], v[8:11], v[44:47]\nds_read_b128 v[32:35], v4 offset:54272\nv_mfma_f32_16x16x32_bf16 v[64:67], a[80:83], v[12:15], v[64:67]\nv_mfma_f32_16x16x32_bf16 v[48:51], a[176:179], v[8:11], v[48:51]\n" \
"v_mfma_f32_16x16x32_bf16 v[20:23], a[176:179], v[12:15], v[20:23]\nv_mfma_f32_16x16x32_bf16 v[56:59], v[128:131], v[8:11], v[56:59]\nv_mfma_f32_16x16x32_bf16 v[24:27], v[128:131], v[12:15], v[24:27]\nv_mfma_f32_16x16x32_bf16 v[60:63], v[224:227], v[8:11], v[60:63]\n" \
"s_waitcnt lgkmcnt(0)\nv_mfma_f32_16x16x32_bf16 v[28:31], v[224:227], v[12:15], v[28:31]\nds_read_b128 v[40:43], v4 offset:22528\nv_mfma_f32_16x16x32_bf16 v[44:47], a[84:87], v[36:39], v[44:47]\n" \
"ds_read_b128 v[16:19], v4 offset:55296\nv_xor_b32 v4, 0x10000, v4\nv_mfma_f32_16x16x32_bf16 v[64:67], a[84:87], v[32:35], v[64:67]\nv_mfma_f32_16x16x32_bf16 v[48:51], a[180:183], v[36:39], v[48:51]\n" \
"v_mfma_f32_16x16x32_bf16 v[20:23], a[180:183], v[32:35], v[20:23]\nv_mfma_f32_16x16x32_bf16 v[56:59], v[132:135], v[36:39], v[56:59]\nv_mfma_f32_16x16x32_bf16 v[24:27], v[132:135], v[32:35], v[24:27]\nv_mfma_f32_16x16x32_bf16 v[60:63], v[228:231], v[36:39], v[60:63]\n" \
"s_waitcnt lgkmcnt(0)\nv_mfma_f32_16x16x32_bf16 v[28:31], v[228:231], v[32:35], v[28:31]\ns_waitcnt vmcnt(0)\ns_barrier\n" \
"v_mfma_f32_16x16x32_bf16 v[44:47], a[88:91], v[40:43], v[44:47]\nds_read_b128 v[8:11], v4 offset:0\nv_mfma_f32_16x16x32_bf16 v[48:51], a[184:187], v[40:43], v[48:51]\nds_read_b128 v[12:15], v4 offset:32768\n" \
"v_mfma_f32_16x16x32_bf16 v[56:59], v[136:139], v[40:43], v[56:59]\nv_cvt_pk_bf16_f32 v36, v44, v45\nv_cvt_pk_bf16_f32 v37, v46, v47\nv_mfma_f32_16x16x32_bf16 v[60:63], v[232:235], v[40:43], v[60:63]\n" \
"v_cvt_pk_bf16_f32 v38, v48, v49\nv_cvt_pk_bf16_f32 v39, v50, v51\nv_mfma_f32_16x16x32_bf16 v[64:67], a[88:91], v[16:19], v[64:67]\nv_cvt_pk_bf16_f32 v32, v56, v57\n" \
"v_cvt_pk_bf16_f32 v33, v58, v59\nv_mfma_f32_16x16x32_bf16 v[20:23], a[184:187], v[16:19], v[20:23]\nbuffer_store_dwordx4 v[36:39], v6, s[8:11], s99 offen offset:0 sc1\nv_cvt_pk_bf16_f32 v34, v60, v61\n" \
"v_cvt_pk_bf16_f32 v35, v62, v63\nv_mfma_f32_16x16x32_bf16 v[24:27], v[136:139], v[16:19], v[24:27]\ns_waitcnt lgkmcnt(1)\nv_cvt_pk_bf16_f32 v36, v64, v65\n" \
"v_cvt_pk_bf16_f32 v37, v66, v67\nv_mfma_f32_16x16x32_bf16 v[28:31], v[232:235], v[16:19], v[28:31]\nds_read_b128 v[40:43], v4 offset:1024\nv_cvt_pk_bf16_f32 v38, v20, v21\n" \
"v_mfma_f32_16x16x32_bf16 v[44:47], a[0:3], v[8:11], 0\nds_read_b128 v[16:19], v4 offset:33792\nv_cvt_pk_bf16_f32 v39, v22, v23\nv_mfma_f32_16x16x32_bf16 v[48:51], a[96:99], v[8:11], 0\n" \
"buffer_store_dwordx4 v[32:35], v6, s[8:11], s99 offen offset:512 sc1\nv_cvt_pk_bf16_f32 v52, v24, v25\nv_cvt_pk_bf16_f32 v53, v26, v27\nv_cvt_pk_bf16_f32 v54, v28, v29\n" \
"v_mfma_f32_16x16x32_bf16 v[56:59], a[192:195], v[8:11], 0\ns_waitcnt lgkmcnt(2)\nv_cvt_pk_bf16_f32 v55, v30, v31\nv_mfma_f32_16x16x32_bf16 v[60:63], v[144:147], v[8:11], 0\n" \
"v_mfma_f32_16x16x32_bf16 v[64:67], a[0:3], v[12:15], 0\nv_mfma_f32_16x16x32_bf16 v[20:23], a[96:99], v[12:15], 0\nbuffer_store_dwordx4 v[36:39], v6, s[8:11], s98 offen offset:0 sc1\nv_mfma_f32_16x16x32_bf16 v[24:27], a[192:195], v[12:15], 0\n" \
"s_waitcnt lgkmcnt(0)\nv_mfma_f32_16x16x32_bf16 v[28:31], v[144:147], v[12:15], 0\nds_read_b128 v[8:11], v4 offset:2048\nv_mfma_f32_16x16x32_bf16 v[44:47], a[4:7], v[40:43], v[44:47]\n" \
"ds_read_b128 v[12:15], v4 offset:34816\nv_mfma_f32_16x16x32_bf16 v[64:67], a[4:7], v[16:19], v[64:67]\nbuffer_store_dwordx4 v[52:55], v6, s[8:11], s98 offen offset:512 sc1\nv_mfma_f32_16x16x32_bf16 v[48:51], a[100:103], v[40:43], v[48:51]\n" \
"v_mfma_f32_16x16x32_bf16 v[20:23], a[100:103], v[16:19], v[20:23]\nv_mfma_f32_16x16x32_bf16 v[56:59], a[196:199], v[40:43], v[56:59]\nv_mfma_f32_16x16x32_bf16 v[24:27], a[196:199], v[16:19], v[24:27]\nv_mfma_f32_16x16x32_bf16 v[60:63], v[148:151], v[40:43], v[60:63]\n" \
"s_cmp_eq_u32 s14, 0\nv_mfma_f32_16x16x32_bf16 v[28:31], v[148:151], v[16:19], v[28:31]\ns_cbranch_scc0 1b\n4:\n" \
"buffer_load_dword v68, v1, s[8:11], 0 offen sc1\nv_mov_b32 v69, s20\ns_add_u32 s98, s20, 1\ns_waitcnt vmcnt(0)\n" \
"v_cmp_gt_i32 vcc, s98, v68\ns_cbranch_vccz 8f\nbuffer_store_dword v69, v2, s[8:11], 0 offen sc1\ns_mov_b32 s25, 0x40000000\n" \
"s_cmp_ge_u32 s20, 0x80\ns_cbranch_scc1 9f\n7:\ns_add_u32 s26, s26, 1\n" \
"s_cmp_lt_u32 s26, 0x40000\ns_cbranch_scc0 9f\ns_sleep 8\nbuffer_load_dword v68, v1, s[8:11], 0 offen sc1\n" \
"s_waitcnt vmcnt(0)\nv_cmp_gt_i32 vcc, s98, v68\ns_cbranch_vccnz 7b\n8:\n" \
"s_lshr_b32 s98, s20, 1\nv_readlane_b32 s98, v3, s98\ns_and_b32 s99, s20, 1\ns_lshl_b32 s99, s99, 3\n" \
"s_lshr_b32 s98, s98, s99\ns_and_b32 s98, s98, 0xff\ns_mul_i32 s98, s98, 0x38000\ns_add_u32 s98, s98, s21\n" \
"s_xor_b32 s99, s23, 0x10000\ns_add_u32 m0, s99, 0\ns_add_u32 s98, s98, 0\nbuffer_load_dwordx4 v0, s[8:11], s98 offen offset:0 sc1 lds\n" \
"buffer_load_dwordx4 v0, s[8:11], s98 offen offset:1024 sc1 lds\nbuffer_load_dwordx4 v0, s[8:11], s98 offen offset:2048 sc1 lds\nbuffer_load_dwordx4 v0, s[8:11], s98 offen offset:3072 sc1 lds\ns_add_u32 m0, s99, 0x1000\n" \
"s_add_u32 s98, s98, 0x1000\nbuffer_load_dwordx4 v0, s[8:11], s98 offen offset:0 sc1 lds\nbuffer_load_dwordx4 v0, s[8:11], s98 offen offset:1024 sc1 lds\nbuffer_load_dwordx4 v0, s[8:11], s98 offen offset:2048 sc1 lds\n" \
"buffer_load_dwordx4 v0, s[8:11], s98 offen offset:3072 sc1 lds\ns_add_u32 m0, s99, 0x2000\ns_add_u32 s98, s98, 0x1000\nbuffer_load_dwordx4 v0, s[8:11], s98 offen offset:0 sc1 lds\n" \
"buffer_load_dwordx4 v0, s[8:11], s98 offen offset:1024 sc1 lds\nbuffer_load_dwordx4 v0, s[8:11], s98 offen offset:2048 sc1 lds\nbuffer_load_dwordx4 v0, s[8:11], s98 offen offset:3072 sc1 lds\nbuffer_store_dword v69, v2, s[8:11], s25 offen sc1\n" \
"s_mov_b32 s25, 0x40000000\ns_waitcnt vmcnt(1) lgkmcnt(0)\ns_barrier\nds_read_b128 v[68:71], v4 offset:0\n" \
"ds_read_b128 v[72:75], v4 offset:32768\nds_read_b128 v[76:79], v4 offset:1024\nds_read_b128 v[80:83], v4 offset:33792\nds_read_b128 v[8:11], v4 offset:2048\n" \
"ds_read_b128 v[12:15], v4 offset:34816\ns_waitcnt lgkmcnt(0)\nv_mfma_f32_16x16x32_bf16 v[44:47], a[0:3], v[68:71], 0\nv_mfma_f32_16x16x32_bf16 v[48:51], a[96:99], v[68:71], 0\n" \
"v_mfma_f32_16x16x32_bf16 v[56:59], a[192:195], v[68:71], 0\nv_mfma_f32_16x16x32_bf16 v[60:63], v[144:147], v[68:71], 0\nv_mfma_f32_16x16x32_bf16 v[64:67], a[0:3], v[72:75], 0\nv_mfma_f32_16x16x32_bf16 v[20:23], a[96:99], v[72:75], 0\n" \
"v_mfma_f32_16x16x32_bf16 v[24:27], a[192:195], v[72:75], 0\nv_mfma_f32_16x16x32_bf16 v[28:31], v[144:147], v[72:75], 0\nv_mfma_f32_16x16x32_bf16 v[44:47], a[4:7], v[76:79], v[44:47]\nv_mfma_f32_16x16x32_bf16 v[48:51], a[100:103], v[76:79], v[48:51]\n" \
"v_mfma_f32_16x16x32_bf16 v[56:59], a[196:199], v[76:79], v[56:59]\nv_mfma_f32_16x16x32_bf16 v[60:63], v[148:151], v[76:79], v[60:63]\nv_mfma_f32_16x16x32_bf16 v[64:67], a[4:7], v[80:83], v[64:67]\nv_mfma_f32_16x16x32_bf16 v[20:23], a[100:103], v[80:83], v[20:23]\n" \
"v_mfma_f32_16x16x32_bf16 v[24:27], a[196:199], v[80:83], v[24:27]\nv_mfma_f32_16x16x32_bf16 v[28:31], v[148:151], v[80:83], v[28:31]\ns_branch 1b\n9:\n" \
"s_waitcnt vmcnt(0)\ns_endpgm\n"
#define G_LOOP \
"s_branch 4f\n.p2align 6\n1:\ns_waitcnt lgkmcnt(2)\n" \
"v_mfma_f32_16x16x32_bf16 v[8:11], a[104:107], v[36:39], v[8:11]\ns_lshr_b32 s99, s20, 1\ns_and_b32 s98, s20, 1\nv_mov_b32 v110, s20\n" \
"v_mfma_f32_16x16x32_bf16 v[12:15], a[104:107], v[40:43], v[12:15]\ns_add_u32 s20, s20, 1\ns_add_u32 s97, s20, 1\ns_lshr_b32 s96, s97, 1\n" \
"v_mfma_f32_16x16x32_bf16 v[16:19], a[232:235], v[36:39], v[16:19]\nds_read_b128 v[52:55], v7 offset:28672\nv_readlane_b32 s96, v6, s96\nv_mfma_f32_16x16x32_bf16 v[20:23], a[232:235], v[40:43], v[20:23]\n" \
"ds_read_b128 v[56:59], v7 offset:61440\ns_and_b32 s97, s97, 1\nv_mfma_f32_16x16x32_bf16 v[24:27], v[216:219], v[36:39], v[24:27]\ns_lshl_b32 s97, s97, 3\n" \
"s_lshr_b32 s96, s96, s97\ns_and_b32 s96, s96, 0xff\nv_mfma_f32_16x16x32_bf16 v[28:31], v[216:219], v[40:43], v[28:31]\ns_waitcnt lgkmcnt(2)\n" \
"s_lshl_b32 s97, s96, 13\nv_xor_b32 v4, 0x400, v4\nv_mfma_f32_16x16x32_bf16 v[8:11], a[108:111], v[44:47], v[8:11]\ns_lshl_b32 s98, s98, 3\n" \
"v_mov_b32 v32, v111\nv_mov_b32 v33, v111\nv_mfma_f32_16x16x32_bf16 v[12:15], a[108:111], v[48:51], v[12:15]\ns_lshl_b32 s96, s96, 16\n" \
"v_mul_f32 v252, 0xc0135761, v34\nv_mul_f32 v34, v34, v34\nv_mfma_f32_16x16x32_bf16 v[16:19], a[236:239], v[44:47], v[16:19]\nds_read_b128 v[36:39], v7 offset:29696\n" \
"v_readlane_b32 s99, v6, s99\nv_mfma_f32_16x16x32_bf16 v[20:23], a[236:239], v[48:51], v[20:23]\nds_read_b128 v[40:43], v7 offset:62464\nv_mul_f32 v34, v34, v252\n" \
"v_mfma_f32_16x16x32_bf16 v[24:27], v[220:223], v[44:47], v[24:27]\nds_read_b128 v[60:63], v7 offset:30720\nv_mul_f32 v254, v35, v35\nv_mfma_f32_16x16x32_bf16 v[28:31], v[220:223], v[48:51], v[28:31]\n" \
"s_waitcnt lgkmcnt(3)\nv_permlane16_swap_b32 v32, v33\nv_mfma_f32_16x16x32_bf16 v[8:11], a[112:115], v[52:55], v[8:11]\nds_read_b128 v[44:47], v7 offset:63488\n" \
"v_mul_f32 v253, 0x3d372713, v34\nv_mfma_f32_16x16x32_bf16 v[12:15], a[112:115], v[56:59], v[12:15]\ns_lshr_b32 s99, s99, s98\ns_and_b32 s99, s99, 0xff\n" \
"s_mul_i32 s99, s99, 0x38000\nv_mfma_f32_16x16x32_bf16 v[16:19], a[240:243], v[52:55], v[16:19]\nds_read_b128 v[48:51], v7 offset:31744\ns_or_b32 s16, s99, s18\n" \
"v_mfma_f32_16x16x32_bf16 v[20:23], a[240:243], v[56:59], v[20:23]\ns_or_b32 s17, s99, s19\ns_add_u32 s96, s96, s21\nv_mfma_f32_16x16x32_bf16 v[24:27], v[224:227], v[52:55], v[24:27]\n" \
"ds_read_b128 v[64:67], v7 offset:64512\nv_xor_b32 v7, 0x10000, v7\nv_mfma_f32_16x16x32_bf16 v[28:31], v[224:227], v[56:59], v[28:31]\ns_waitcnt lgkmcnt(3)\n" \
"buffer_load_dwordx4 v[52:55], v1, s[8:11], s97 offen offset:0 sc1\nv_mfma_f32_16x16x32_bf16 v[8:11], a[116:119], v[36:39], v[8:11]\nbuffer_load_dwordx4 v[56:59], v1, s[8:11], s97 offen offset:1024 sc1\nv_mfma_f32_16x16x32_bf16 v[12:15], a[116:119], v[40:43], v[12:15]\n" \
"v_mfma_f32_16x16x32_bf16 v[16:19], a[244:247], v[36:39], v[16:19]\ns_waitcnt vmcnt(5) lgkmcnt(0)\ns_barrier\nv_mfma_f32_16x16x32_bf16 v[20:23], a[244:247], v[40:43], v[20:23]\n" \
"ds_read_b128 v[68:71], v4 offset:0\nv_mfma_f32_16x16x32_bf16 v[24:27], v[228:231], v[36:39], v[24:27]\nds_read_b128 v[72:75], v7 offset:0\nv_mfma_f32_16x16x32_bf16 v[28:31], v[228:231], v[40:43], v[28:31]\n" \
"ds_read_b128 v[36:39], v7 offset:32768\nv_mfma_f32_16x16x32_bf16 v[8:11], a[120:123], v[60:63], v[8:11]\nds_read_b128 v[40:43], v7 offset:1024\nv_mfma_f32_16x16x32_bf16 v[12:15], a[120:123], v[44:47], v[12:15]\n" \
"s_waitcnt lgkmcnt(3)\nv_add_f32 v68, v68, v69\nv_mfma_f32_16x16x32_bf16 v[8:11], a[124:127], v[48:51], v[8:11]\nds_read_b128 v[76:79], v7 offset:33792\n" \
"v_add_f32 v70, v70, v71\nv_mfma_f32_16x16x32_bf16 v[12:15], a[124:127], v[64:67], v[12:15]\nv_cmp_u_f32 s[98:99], v68, v70\nv_mfma_f32_16x16x32_bf16 v[16:19], a[248:251], v[60:63], v[16:19]\n" \
"v_fma_f32 v68, v32, v240, v8\nv_fma_f32 v69, v32, v241, v9\nv_mfma_f32_16x16x32_bf16 v[20:23], a[248:251], v[44:47], v[20:23]\ns_waitcnt vmcnt(2)\n" \
"buffer_store_dword v110, v3, s[8:11], s23 offen offset:0 sc1\nv_mfma_f32_16x16x32_bf16 v[16:19], a[252:255], v[48:51], v[16:19]\ns_mov_b32 s23, 0\nv_fma_f32 v70, v32, v242, v10\n" \
"v_fma_f32 v71, v32, v243, v11\nv_mfma_f32_16x16x32_bf16 v[20:23], a[252:255], v[64:67], v[20:23]\ns_cmp_lt_u32 s20, 0x80\ns_cselect_b32 s98, s98, -1\n" \
"v_fma_f32 v80, v33, v240, v12\nv_mfma_f32_16x16x32_bf16 v[24:27], v[232:235], v[60:63], v[24:27]\nv_fma_f32 v81, v33, v241, v13\nv_fma_f32 v82, v33, v242, v14\n" \
"v_mfma_f32_16x16x32_bf16 v[28:31], v[232:235], v[44:47], v[28:31]\nv_fma_f32 v83, v33, v243, v15\nv_fma_f32 v44, v32, v244, v16\nv_mfma_f32_16x16x32_bf16 v[24:27], v[236:239], v[48:51], v[24:27]\n" \
"s_waitcnt lgkmcnt(0)\nv_fma_f32 v45, v32, v245, v17\nv_fma_f32 v46, v32, v246, v18\nv_mfma_f32_16x16x32_bf16 v[28:31], v[236:239], v[64:67], v[28:31]\n" \
"s_cmp_lg_u64 s[98:99], 0\ns_cbranch_scc1 4f\n5:\nv_mfma_f32_16x16x32_bf16 v[8:11], a[0:3], v[72:75], 0\n" \
"s_xor_b32 m0, s22, 0\nv_fma_f32 v47, v32, v247, v19\nv_fma_f32 v48, v33, v244, v20\nv_mfma_f32_16x16x32_bf16 v[12:15], a[0:3], v[36:39], 0\n" \
"v_fma_f32 v49, v33, v245, v21\nv_fma_f32 v50, v33, v246, v22\nv_mfma_f32_16x16x32_bf16 v[8:11], a[4:7], v[40:43], v[8:11]\nds_read_b128 v[60:63], v7 offset:2048\n" \
"v_fma_f32 v51, v33, v247, v23\nv_mfma_f32_16x16x32_bf16 v[12:15], a[4:7], v[76:79], v[12:15]\nds_read_b128 v[64:67], v7 offset:34816\nv_fma_f32 v84, v32, v248, v24\n" \
"v_mfma_f32_16x16x32_bf16 v[16:19], a[128:131], v[72:75], 0\nv_fma_f32 v85, v32, v249, v25\nv_fma_f32 v86, v32, v250, v26\nv_mfma_f32_16x16x32_bf16 v[20:23], a[128:131], v[36:39], 0\n" \
"v_fma_f32 v87, v32, v251, v27\nv_fma_f32 v88, v33, v248, v28\nv_mfma_f32_16x16x32_bf16 v[16:19], a[132:135], v[40:43], v[16:19]\nv_fma_f32 v89, v33, v249, v29\n" \
"v_fma_f32 v90, v33, v250, v30\nv_mfma_f32_16x16x32_bf16 v[20:23], a[132:135], v[76:79], v[20:23]\nv_fma_f32 v91, v33, v251, v31\nv_xor_b32 v5, 0x400, v5\n" \
"v_mfma_f32_16x16x32_bf16 v[24:27], v[112:115], v[72:75], 0\nds_read_b128 v[92:95], v7 offset:3072\nv_mfma_f32_16x16x32_bf16 v[28:31], v[112:115], v[36:39], 0\nds_read_b128 v[72:75], v7 offset:35840\n" \
"v_mfma_f32_16x16x32_bf16 v[24:27], v[116:119], v[40:43], v[24:27]\ns_waitcnt lgkmcnt(2)\nv_permlane16_swap_b32 v68, v80\nv_mfma_f32_16x16x32_bf16 v[28:31], v[116:119], v[76:79], v[28:31]\n" \
"v_permlane16_swap_b32 v69, v81\nv_mfma_f32_16x16x32_bf16 v[8:11], a[8:11], v[60:63], v[8:11]\nv_permlane16_swap_b32 v70, v82\nv_mfma_f32_16x16x32_bf16 v[12:15], a[8:11], v[64:67], v[12:15]\n" \
"v_permlane16_swap_b32 v71, v83\nv_mfma_f32_16x16x32_bf16 v[16:19], a[136:139], v[60:63], v[16:19]\nds_read_b128 v[36:39], v7 offset:4096\nv_mul_f32 v40, v68, v68\n" \
"v_mfma_f32_16x16x32_bf16 v[20:23], a[136:139], v[64:67], v[20:23]\nds_read_b128 v[76:79], v7 offset:36864\nv_mul_f32 v41, v69, v69\nv_mfma_f32_16x16x32_bf16 v[24:27], v[120:123], v[60:63], v[24:27]\n" \
"s_waitcnt lgkmcnt(2)\nv_mul_f32 v42, v70, v70\nv_mul_f32 v43, v71, v71\nv_mfma_f32_16x16x32_bf16 v[28:31], v[120:123], v[64:67], v[28:31]\n" \
"v_fma_f32 v40, v40, v253, v252\nv_fma_f32 v41, v41, v253, v252\nv_mfma_f32_16x16x32_bf16 v[8:11], a[12:15], v[92:95], v[8:11]\nv_fma_f32 v42, v42, v253, v252\n" \
"v_fma_f32 v43, v43, v253, v252\nv_mfma_f32_16x16x32_bf16 v[12:15], a[12:15], v[72:75], v[12:15]\nv_mul_f32 v40, v40, v68\nv_mul_f32 v41, v41, v69\n" \
"v_mfma_f32_16x16x32_bf16 v[16:19], a[140:143], v[92:95], v[16:19]\nds_read_b128 v[60:63], v7 offset:5120\nv_mul_f32 v42, v42, v70\nv_mfma_f32_16x16x32_bf16 v[20:23], a[140:143], v[72:75], v[20:23]\n" \
"ds_read_b128 v[64:67], v7 offset:37888\nv_mul_f32 v43, v43, v71\nv_mfma_f32_16x16x32_bf16 v[24:27], v[124:127], v[92:95], v[24:27]\ns_waitcnt lgkmcnt(2)\n" \
"v_exp_f32 v40, v40\nv_mfma_f32_16x16x32_bf16 v[28:31], v[124:127], v[72:75], v[28:31]\nv_exp_f32 v41, v41\nv_mfma_f32_16x16x32_bf16 v[8:11], a[16:19], v[36:39], v[8:11]\n" \
"v_exp_f32 v42, v42\nv_mfma_f32_16x16x32_bf16 v[12:15], a[16:19], v[76:79], v[12:15]\nv_exp_f32 v43, v43\nv_mfma_f32_16x16x32_bf16 v[16:19], a[144:147], v[36:39], v[16:19]\n" \
"ds_read_b128 v[72:75], v7 offset:6144\nv_fma_f32 v40, v40, v254, v254\nv_mfma_f32_16x16x32_bf16 v[20:23], a[144:147], v[76:79], v[20:23]\nds_read_b128 v[92:95], v7 offset:38912\n" \
"v_fma_f32 v41, v41, v254, v254\nv_mfma_f32_16x16x32_bf16 v[24:27], v[128:131], v[36:39], v[24:27]\ns_waitcnt lgkmcnt(2)\nv_rcp_f32 v40, v40\n" \
"v_mfma_f32_16x16x32_bf16 v[28:31], v[128:131], v[76:79], v[28:31]\nv_rcp_f32 v41, v41\nv_mfma_f32_16x16x32_bf16 v[8:11], a[20:23], v[60:63], v[8:11]\nv_fma_f32 v42, v42, v254, v254\n" \
"v_fma_f32 v43, v43, v254, v254\nv_mfma_f32_16x16x32_bf16 v[12:15], a[20:23], v[64:67], v[12:15]\nv_rcp_f32 v42, v42\nv_mfma_f32_16x16x32_bf16 v[16:19], a[148:151], v[60:63], v[16:19]\n" \
"ds_read_b128 v[36:39], v7 offset:7168\nv_mul_f32 v80, v68, v80\nv_mfma_f32_16x16x32_bf16 v[20:23], a[148:151], v[64:67], v[20:23]\ns_waitcnt vmcnt(1)\n" \
"v_max3_u32 v35, v52, v54, v56\nv_max_u32 v35, v35, v58\nv_mfma_f32_16x16x32_bf16 v[24:27], v[132:135], v[60:63], v[24:27]\nds_read_b128 v[76:79], v7 offset:39936\n" \
"v_cmp_eq_u32 s[98:99], -1, v35\nv_mfma_f32_16x16x32_bf16 v[28:31], v[132:135], v[64:67], v[28:31]\ns_waitcnt lgkmcnt(2)\nv_rcp_f32 v43, v43\n" \
"v_mfma_f32_16x16x32_bf16 v[8:11], a[24:27], v[72:75], v[8:11]\ns_cmp_eq_u64 s[98:99], 0\ns_cselect_b32 s14, s10, 0\nv_mul_f32 v81, v69, v81\n" \
"v_mfma_f32_16x16x32_bf16 v[12:15], a[24:27], v[92:95], v[12:15]\nbuffer_load_dwordx4 v0, s[12:15], s96 offen offset:0 sc1 lds\nv_mfma_f32_16x16x32_bf16 v[16:19], a[152:155], v[72:75], v[16:19]\nds_read_b128 v[60:63], v7 offset:8192\n" \
"v_mul_f32 v82, v70, v82\nv_mfma_f32_16x16x32_bf16 v[20:23], a[152:155], v[92:95], v[20:23]\nds_read_b128 v[64:67], v7 offset:40960\nv_mul_f32 v83, v71, v83\n" \
"v_mfma_f32_16x16x32_bf16 v[24:27], v[136:139], v[72:75], v[24:27]\ns_waitcnt lgkmcnt(2)\nv_mul_f32 v80, v80, v40\nv_mul_f32 v81, v81, v41\n" \
"v_mfma_f32_16x16x32_bf16 v[28:31], v[136:139], v[92:95], v[28:31]\nv_mul_f32 v82, v82, v42\nv_mul_f32 v83, v83, v43\nv_mfma_f32_16x16x32_bf16 v[8:11], a[28:31], v[36:39], v[8:11]\n" \
"v_cvt_pk_bf16_f32 v80, v80, v81\nv_cvt_pk_bf16_f32 v81, v82, v83\nv_mfma_f32_16x16x32_bf16 v[12:15], a[28:31], v[76:79], v[12:15]\nbuffer_load_dwordx4 v0, s[12:15], s96 offen offset:1024 sc1 lds\n" \
"v_mfma_f32_16x16x32_bf16 v[16:19], a[156:159], v[36:39], v[16:19]\nds_read_b128 v[40:43], v7 offset:9216\nv_mfma_f32_16x16x32_bf16 v[20:23], a[156:159], v[76:79], v[20:23]\nds_read_b128 v[68:71], v7 offset:41984\n" \
"v_mfma_f32_16x16x32_bf16 v[24:27], v[140:143], v[36:39], v[24:27]\ns_waitcnt lgkmcnt(2)\nv_permlane16_swap_b32 v44, v48\nv_mfma_f32_16x16x32_bf16 v[28:31], v[140:143], v[76:79], v[28:31]\n" \
"buffer_load_dwordx4 v0, s[12:15], s96 offen offset:2048 sc1 lds\nv_mfma_f32_16x16x32_bf16 v[8:11], a[32:35], v[60:63], v[8:11]\nv_permlane16_swap_b32 v45, v49\nv_mfma_f32_16x16x32_bf16 v[12:15], a[32:35], v[64:67], v[12:15]\n" \
"v_permlane16_swap_b32 v46, v50\nv_mfma_f32_16x16x32_bf16 v[16:19], a[160:163], v[60:63], v[16:19]\nds_read_b128 v[36:39], v7 offset:10240\nv_mul_f32 v72, v44, v44\n" \
"v_mfma_f32_16x16x32_bf16 v[20:23], a[160:163], v[64:67], v[20:23]\nds_read_b128 v[76:79], v7 offset:43008\nv_mul_f32 v73, v45, v45\nv_mfma_f32_16x16x32_bf16 v[24:27], v[144:147], v[60:63], v[24:27]\n" \
"s_waitcnt lgkmcnt(2)\nbuffer_load_dwordx4 v0, s[12:15], s96 offen offset:3072 sc1 lds\nv_mfma_f32_16x16x32_bf16 v[28:31], v[144:147], v[64:67], v[28:31]\ns_xor_b32 m0, s22, 0x1000\n" \
"s_xor_b32 s96, s96, 0x1000\nv_mul_f32 v74, v46, v46\nv_mfma_f32_16x16x32_bf16 v[8:11], a[36:39], v[40:43], v[8:11]\nv_permlane16_swap_b32 v47, v51\n" \
"v_mfma_f32_16x16x32_bf16 v[12:15], a[36:39], v[68:71], v[12:15]\nv_mul_f32 v75, v47, v47\nv_fma_f32 v72, v72, v253, v252\nv_mfma_f32_16x16x32_bf16 v[16:19], a[164:167], v[40:43], v[16:19]\n" \
"ds_read_b128 v[60:63], v7 offset:11264\nv_fma_f32 v73, v73, v253, v252\nv_mfma_f32_16x16x32_bf16 v[20:23], a[164:167], v[68:71], v[20:23]\nbuffer_load_dwordx4 v0, s[12:15], s96 offen offset:0 sc1 lds\n" \
"v_mfma_f32_16x16x32_bf16 v[24:27], v[148:151], v[40:43], v[24:27]\nds_read_b128 v[64:67], v7 offset:44032\nv_fma_f32 v74, v74, v253, v252\nv_mfma_f32_16x16x32_bf16 v[28:31], v[148:151], v[68:71], v[28:31]\n" \
"s_waitcnt lgkmcnt(2)\nv_fma_f32 v75, v75, v253, v252\nv_mul_f32 v72, v72, v44\nv_mfma_f32_16x16x32_bf16 v[8:11], a[40:43], v[36:39], v[8:11]\n" \
"v_exp_f32 v72, v72\nv_mfma_f32_16x16x32_bf16 v[12:15], a[40:43], v[76:79], v[12:15]\nv_mul_f32 v73, v73, v45\nv_mul_f32 v74, v74, v46\n" \
"v_mfma_f32_16x16x32_bf16 v[16:19], a[168:171], v[36:39], v[16:19]\nbuffer_load_dwordx4 v0, s[12:15], s96 offen offset:1024 sc1 lds\nv_mfma_f32_16x16x32_bf16 v[20:23], a[168:171], v[76:79], v[20:23]\nds_read_b128 v[40:43], v7 offset:12288\n" \
"v_mul_f32 v75, v75, v47\nv_mfma_f32_16x16x32_bf16 v[24:27], v[152:155], v[36:39], v[24:27]\nds_read_b128 v[68:71], v7 offset:45056\nv_fma_f32 v72, v72, v254, v254\n" \
"v_mfma_f32_16x16x32_bf16 v[28:31], v[152:155], v[76:79], v[28:31]\ns_waitcnt lgkmcnt(2)\nv_exp_f32 v73, v73\nv_mfma_f32_16x16x32_bf16 v[8:11], a[44:47], v[60:63], v[8:11]\n" \
"v_exp_f32 v74, v74\nv_mfma_f32_16x16x32_bf16 v[12:15], a[44:47], v[64:67], v[12:15]\nbuffer_load_dwordx4 v0, s[12:15], s96 offen offset:2048 sc1 lds\nv_mfma_f32_16x16x32_bf16 v[16:19], a[172:175], v[60:63], v[16:19]\n" \
"ds_read_b128 v[36:39], v7 offset:13312\nv_fma_f32 v73, v73, v254, v254\nv_mfma_f32_16x16x32_bf16 v[20:23], a[172:175], v[64:67], v[20:23]\nds_read_b128 v[76:79], v7 offset:46080\n" \
"v_fma_f32 v74, v74, v254, v254\nv_mfma_f32_16x16x32_bf16 v[24:27], v[156:159], v[60:63], v[24:27]\ns_waitcnt lgkmcnt(2)\nv_exp_f32 v75, v75\n" \
"v_mfma_f32_16x16x32_bf16 v[28:31], v[156:159], v[64:67], v[28:31]\nv_rcp_f32 v72, v72\nv_mfma_f32_16x16x32_bf16 v[8:11], a[48:51], v[40:43], v[8:11]\nbuffer_load_dwordx4 v0, s[12:15], s96 offen offset:3072 sc1 lds\n" \
"v_mfma_f32_16x16x32_bf16 v[12:15], a[48:51], v[68:71], v[12:15]\ns_xor_b32 m0, s22, 0x8000\ns_xor_b32 s96, s96, 0x9000\nv_fma_f32 v75, v75, v254, v254\n" \
"v_mfma_f32_16x16x32_bf16 v[16:19], a[176:179], v[40:43], v[16:19]\nds_read_b128 v[60:63], v7 offset:14336\nv_mul_f32 v48, v44, v48\nv_mfma_f32_16x16x32_bf16 v[20:23], a[176:179], v[68:71], v[20:23]\n" \
"ds_read_b128 v[64:67], v7 offset:47104\nv_mul_f32 v49, v45, v49\nv_mfma_f32_16x16x32_bf16 v[24:27], v[160:163], v[40:43], v[24:27]\ns_waitcnt lgkmcnt(2)\n" \
"v_rcp_f32 v73, v73\nv_mfma_f32_16x16x32_bf16 v[28:31], v[160:163], v[68:71], v[28:31]\nbuffer_load_dwordx4 v0, s[12:15], s96 offen offset:0 sc1 lds\nv_mfma_f32_16x16x32_bf16 v[8:11], a[52:55], v[36:39], v[8:11]\n" \
"v_rcp_f32 v74, v74\nv_mfma_f32_16x16x32_bf16 v[12:15], a[52:55], v[76:79], v[12:15]\nv_rcp_f32 v75, v75\nv_mfma_f32_16x16x32_bf16 v[16:19], a[180:183], v[36:39], v[16:19]\n" \
"ds_read_b128 v[40:43], v7 offset:15360\nv_mul_f32 v50, v46, v50\nv_mfma_f32_16x16x32_bf16 v[20:23], a[180:183], v[76:79], v[20:23]\nds_read_b128 v[68:71], v7 offset:48128\n" \
"v_mul_f32 v51, v47, v51\nv_mfma_f32_16x16x32_bf16 v[24:27], v[164:167], v[36:39], v[24:27]\ns_waitcnt lgkmcnt(2)\nbuffer_load_dwordx4 v0, s[12:15], s96 offen offset:1024 sc1 lds\n" \
"v_mfma_f32_16x16x32_bf16 v[28:31], v[164:167], v[76:79], v[28:31]\nv_mul_f32 v48, v48, v72\nv_mul_f32 v49, v49, v73\nv_mfma_f32_16x16x32_bf16 v[8:11], a[56:59], v[60:63], v[8:11]\n" \
"v_mul_f32 v50, v50, v74\nv_mul_f32 v51, v51, v75\nv_mfma_f32_16x16x32_bf16 v[12:15], a[56:59], v[64:67], v[12:15]\nv_cvt_pk_bf16_f32 v48, v48, v49\n" \
"v_cvt_pk_bf16_f32 v49, v50, v51\nv_mfma_f32_16x16x32_bf16 v[16:19], a[184:187], v[60:63], v[16:19]\nds_read_b128 v[36:39], v7 offset:16384\nv_mfma_f32_16x16x32_bf16 v[20:23], a[184:187], v[64:67], v[20:23]\n" \
"buffer_load_dwordx4 v0, s[12:15], s96 offen offset:2048 sc1 lds\nv_mfma_f32_16x16x32_bf16 v[24:27], v[168:171], v[60:63], v[24:27]\ns_waitcnt lgkmcnt(1)\nds_read_b128 v[44:47], v7 offset:49152\n" \
"v_mfma_f32_16x16x32_bf16 v[28:31], v[168:171], v[64:67], v[28:31]\nv_permlane16_swap_b32 v84, v88\nv_mfma_f32_16x16x32_bf16 v[8:11], a[60:63], v[40:43], v[8:11]\nv_permlane16_swap_b32 v85, v89\n" \
"v_mfma_f32_16x16x32_bf16 v[12:15], a[60:63], v[68:71], v[12:15]\nv_permlane16_swap_b32 v86, v90\nv_mfma_f32_16x16x32_bf16 v[16:19], a[188:191], v[40:43], v[16:19]\nbuffer_load_dwordx4 v0, s[12:15], s96 offen offset:3072 sc1 lds\n" \
"v_mfma_f32_16x16x32_bf16 v[20:23], a[188:191], v[68:71], v[20:23]\nds_read_b128 v[60:63], v7 offset:17408\ns_xor_b32 m0, s22, 0x9000\nv_mfma_f32_16x16x32_bf16 v[24:27], v[172:175], v[40:43], v[24:27]\n" \
"ds_read_b128 v[64:67], v7 offset:50176\ns_xor_b32 s96, s96, 0x1000\nv_mfma_f32_16x16x32_bf16 v[28:31], v[172:175], v[68:71], v[28:31]\ns_waitcnt lgkmcnt(2)\n" \
"v_permlane16_swap_b32 v87, v91\nv_mfma_f32_16x16x32_bf16 v[8:11], a[64:67], v[36:39], v[8:11]\ns_xor_b32 s22, s22, 0x10000\nv_mul_f32 v40, v84, v84\n" \
"v_mul_f32 v41, v85, v85\nv_mfma_f32_16x16x32_bf16 v[12:15], a[64:67], v[44:47], v[12:15]\nbuffer_load_dwordx4 v0, s[12:15], s96 offen offset:0 sc1 lds\nv_mfma_f32_16x16x32_bf16 v[16:19], a[192:195], v[36:39], v[16:19]\n" \
"ds_read_b128 v[68:71], v7 offset:18432\nv_mul_f32 v42, v86, v86\nv_mfma_f32_16x16x32_bf16 v[20:23], a[192:195], v[44:47], v[20:23]\nds_read_b128 v[72:75], v7 offset:51200\n" \
"v_mul_f32 v43, v87, v87\nv_mfma_f32_16x16x32_bf16 v[24:27], v[176:179], v[36:39], v[24:27]\ns_waitcnt lgkmcnt(2)\nv_fma_f32 v40, v40, v253, v252\n" \
"v_fma_f32 v41, v41, v253, v252\nv_mfma_f32_16x16x32_bf16 v[28:31], v[176:179], v[44:47], v[28:31]\nv_fma_f32 v42, v42, v253, v252\nv_fma_f32 v43, v43, v253, v252\n" \
"v_mfma_f32_16x16x32_bf16 v[8:11], a[68:71], v[60:63], v[8:11]\nbuffer_load_dwordx4 v0, s[12:15], s96 offen offset:1024 sc1 lds\nv_mfma_f32_16x16x32_bf16 v[12:15], a[68:71], v[64:67], v[12:15]\nv_mul_f32 v40, v40, v84\n" \
"v_mul_f32 v41, v41, v85\nv_mfma_f32_16x16x32_bf16 v[16:19], a[196:199], v[60:63], v[16:19]\nds_read_b128 v[36:39], v7 offset:19456\nv_mul_f32 v42, v42, v86\n" \
"v_mfma_f32_16x16x32_bf16 v[20:23], a[196:199], v[64:67], v[20:23]\nds_read_b128 v[44:47], v7 offset:52224\nv_mul_f32 v43, v43, v87\nv_mfma_f32_16x16x32_bf16 v[24:27], v[180:183], v[60:63], v[24:27]\n" \
"s_waitcnt lgkmcnt(2)\nv_exp_f32 v40, v40\nv_mfma_f32_16x16x32_bf16 v[28:31], v[180:183], v[64:67], v[28:31]\nbuffer_load_dwordx4 v0, s[12:15], s96 offen offset:2048 sc1 lds\n" \
"v_mfma_f32_16x16x32_bf16 v[8:11], a[72:75], v[68:71], v[8:11]\nv_exp_f32 v41, v41\nv_mfma_f32_16x16x32_bf16 v[12:15], a[72:75], v[72:75], v[12:15]\nv_exp_f32 v42, v42\n" \
"v_mfma_f32_16x16x32_bf16 v[16:19], a[200:203], v[68:71], v[16:19]\nds_read_b128 v[60:63], v7 offset:20480\nv_fma_f32 v40, v40, v254, v254\nv_mfma_f32_16x16x32_bf16 v[20:23], a[200:203], v[72:75], v[20:23]\n" \
"ds_read_b128 v[64:67], v7 offset:53248\nv_fma_f32 v41, v41, v254, v254\nv_mfma_f32_16x16x32_bf16 v[24:27], v[184:187], v[68:71], v[24:27]\ns_waitcnt lgkmcnt(2)\n" \
"buffer_load_dwordx4 v0, s[12:15], s96 offen offset:3072 sc1 lds\nv_mfma_f32_16x16x32_bf16 v[28:31], v[184:187], v[72:75], v[28:31]\nv_exp_f32 v43, v43\nv_mfma_f32_16x16x32_bf16 v[8:11], a[76:79], v[36:39], v[8:11]\n" \
"v_rcp_f32 v40, v40\nv_mfma_f32_16x16x32_bf16 v[12:15], a[76:79], v[44:47], v[12:15]\nv_rcp_f32 v41, v41\nv_mfma_f32_16x16x32_bf16 v[16:19], a[204:207], v[36:39], v[16:19]\n" \
"ds_read_b128 v[68:71], v7 offset:21504\nv_fma_f32 v42, v42, v254, v254\nv_mfma_f32_16x16x32_bf16 v[20:23], a[204:207], v[44:47], v[20:23]\nds_read_b128 v[72:75], v7 offset:54272\n" \
"v_fma_f32 v43, v43, v254, v254\nv_mfma_f32_16x16x32_bf16 v[24:27], v[188:191], v[36:39], v[24:27]\ns_waitcnt lgkmcnt(2)\nv_rcp_f32 v42, v42\n" \
"v_mfma_f32_16x16x32_bf16 v[28:31], v[188:191], v[44:47], v[28:31]\nv_rcp_f32 v43, v43\nv_mfma_f32_16x16x32_bf16 v[8:11], a[80:83], v[60:63], v[8:11]\nbuffer_store_dwordx2 v[80:81], v2, s[8:11], s16 offen offset:0 sc1\n" \
"v_mul_f32 v88, v84, v88\nv_mfma_f32_16x16x32_bf16 v[12:15], a[80:83], v[64:67], v[12:15]\nv_mul_f32 v89, v85, v89\nv_mul_f32 v90, v86, v90\n" \
"v_mfma_f32_16x16x32_bf16 v[16:19], a[208:211], v[60:63], v[16:19]\nbuffer_store_dwordx2 v[48:49], v2, s[8:11], s17 offen offset:256 sc1\nv_mul_f32 v91, v87, v91\nv_mfma_f32_16x16x32_bf16 v[20:23], a[208:211], v[64:67], v[20:23]\n" \
"v_mul_f32 v88, v88, v40\nv_mul_f32 v89, v89, v41\nv_mfma_f32_16x16x32_bf16 v[24:27], v[192:195], v[60:63], v[24:27]\nds_read_b128 v[36:39], v7 offset:22528\n" \
"v_mul_f32 v90, v90, v42\nv_mul_f32 v91, v91, v43\nv_mfma_f32_16x16x32_bf16 v[28:31], v[192:195], v[64:67], v[28:31]\ns_waitcnt lgkmcnt(1)\n" \
"ds_read_b128 v[40:43], v7 offset:55296\nv_cvt_pk_bf16_f32 v88, v88, v89\nv_mfma_f32_16x16x32_bf16 v[8:11], a[84:87], v[68:71], v[8:11]\nds_read_b128 v[44:47], v4 offset:0\n" \
"v_cvt_pk_bf16_f32 v89, v90, v91\nv_mfma_f32_16x16x32_bf16 v[12:15], a[84:87], v[72:75], v[12:15]\nbuffer_store_dwordx2 v[88:89], v2, s[8:11], s17 offen offset:512 sc1\nv_add_f32 v52, v52, v56\n" \
"v_mfma_f32_16x16x32_bf16 v[16:19], a[212:215], v[68:71], v[16:19]\nds_read_b128 v[48:51], v7 offset:23552\nv_add_f32 v53, v53, v57\nv_mfma_f32_16x16x32_bf16 v[20:23], a[212:215], v[72:75], v[20:23]\n" \
"ds_read_b128 v[60:63], v7 offset:56320\nv_add_f32 v54, v54, v58\nv_mfma_f32_16x16x32_bf16 v[24:27], v[196:199], v[68:71], v[24:27]\nds_read_b128 v[64:67], v4 offset:512\n" \
"v_add_f32 v55, v55, v59\nv_mfma_f32_16x16x32_bf16 v[28:31], v[196:199], v[72:75], v[28:31]\ns_waitcnt lgkmcnt(4)\nv_permlane16_swap_b32 v52, v54\n" \
"v_mfma_f32_16x16x32_bf16 v[8:11], a[88:91], v[36:39], v[8:11]\nv_permlane16_swap_b32 v53, v55\nv_mfma_f32_16x16x32_bf16 v[12:15], a[88:91], v[40:43], v[12:15]\ns_waitcnt lgkmcnt(3)\n" \
"v_add_f32 v44, v44, v45\nv_add_f32 v46, v46, v47\nv_mfma_f32_16x16x32_bf16 v[16:19], a[216:219], v[36:39], v[16:19]\nds_read_b128 v[56:59], v7 offset:24576\n" \
"v_add_f32 v44, v44, v46\nv_mfma_f32_16x16x32_bf16 v[20:23], a[216:219], v[40:43], v[20:23]\nds_read_b128 v[68:71], v7 offset:57344\nv_mul_f32 v44, 0xba800000, v44\n" \
"v_mfma_f32_16x16x32_bf16 v[24:27], v[200:203], v[36:39], v[24:27]\ns_waitcnt lgkmcnt(2)\nv_add_f32 v52, v52, v54\nv_add_f32 v53, v53, v55\n" \
"v_mfma_f32_16x16x32_bf16 v[28:31], v[200:203], v[40:43], v[28:31]\nv_add_f32 v64, v64, v65\nv_add_f32 v66, v66, v67\nv_mfma_f32_16x16x32_bf16 v[8:11], a[92:95], v[48:51], v[8:11]\n" \
"v_add_f32 v64, v64, v66\nv_mul_f32 v65, v44, -v44\nv_mfma_f32_16x16x32_bf16 v[12:15], a[92:95], v[60:63], v[12:15]\nv_fmamk_f32 v35, v64, 0x3a800000, v65\n" \
"v_cvt_pk_bf16_f32 v35, v35, v35\nv_mfma_f32_16x16x32_bf16 v[16:19], a[220:223], v[48:51], v[16:19]\nds_read_b128 v[64:67], v7 offset:25600\nv_lshlrev_b32 v35, 16, v35\n" \
"v_mfma_f32_16x16x32_bf16 v[20:23], a[220:223], v[60:63], v[20:23]\nds_read_b128 v[72:75], v7 offset:58368\nv_max_f32 v35, 0, v35\nv_mfma_f32_16x16x32_bf16 v[24:27], v[204:207], v[48:51], v[24:27]\n" \
"s_waitcnt lgkmcnt(2)\nv_add_f32 v35, 0x3727c5ac, v35\nv_cvt_pk_bf16_f32 v111, v35, v44\nv_mfma_f32_16x16x32_bf16 v[28:31], v[204:207], v[60:63], v[28:31]\n" \
"v_permlane32_swap_b32 v52, v53\nv_mfma_f32_16x16x32_bf16 v[8:11], a[96:99], v[56:59], v[8:11]\nv_lshlrev_b32 v35, 16, v111\nv_add_f32 v52, v52, v53\n" \
"v_mfma_f32_16x16x32_bf16 v[12:15], a[96:99], v[68:71], v[12:15]\nv_sqrt_f32 v35, v35\nv_mfma_f32_16x16x32_bf16 v[16:19], a[224:227], v[56:59], v[16:19]\nds_read_b128 v[36:39], v7 offset:26624\n" \
"v_cvt_pk_bf16_f32 v35, v35, v35\nv_mfma_f32_16x16x32_bf16 v[20:23], a[224:227], v[68:71], v[20:23]\nds_read_b128 v[40:43], v7 offset:59392\nv_lshlrev_b32 v35, 16, v35\n" \
"v_mfma_f32_16x16x32_bf16 v[24:27], v[208:211], v[56:59], v[24:27]\ns_waitcnt lgkmcnt(2)\nv_rcp_f32 v34, v35\nv_mfma_f32_16x16x32_bf16 v[28:31], v[208:211], v[68:71], v[28:31]\n" \
"ds_write_b32 v5, v52 offset:0\nv_mfma_f32_16x16x32_bf16 v[8:11], a[100:103], v[64:67], v[8:11]\nv_and_b32 v111, 0xffff0000, v111\nv_mfma_f32_16x16x32_bf16 v[12:15], a[100:103], v[72:75], v[12:15]\n" \
"v_mfma_f32_16x16x32_bf16 v[16:19], a[228:231], v[64:67], v[16:19]\nds_read_b128 v[44:47], v7 offset:27648\nv_mfma_f32_16x16x32_bf16 v[20:23], a[228:231], v[72:75], v[20:23]\nds_read_b128 v[48:51], v7 offset:60416\n" \
"v_mfma_f32_16x16x32_bf16 v[24:27], v[212:215], v[64:67], v[24:27]\nv_mfma_f32_16x16x32_bf16 v[28:31], v[212:215], v[72:75], v[28:31]\ns_branch 1b\n4:\n" \
"s_barrier\ns_lshr_b32 s94, s20, 1\nv_readlane_b32 s94, v6, s94\ns_and_b32 s95, s20, 1\n" \
"s_lshl_b32 s95, s95, 3\ns_lshr_b32 s94, s94, s95\ns_and_b32 s94, s94, 0xff\ns_lshl_b32 s95, s94, 13\n" \
"buffer_load_dwordx4 v[96:99], v1, s[8:11], s95 offen offset:0 sc1\nbuffer_load_dwordx4 v[100:103], v1, s[8:11], s95 offen offset:1024 sc1\nv_fma_f32 v84, v32, v240, v8\nv_fma_f32 v85, v32, v241, v9\n" \
"v_fma_f32 v86, v32, v242, v10\nv_fma_f32 v87, v32, v243, v11\nv_fma_f32 v88, v33, v240, v12\nv_fma_f32 v89, v33, v241, v13\n" \
"v_fma_f32 v90, v33, v242, v14\nv_fma_f32 v91, v33, v243, v15\nv_permlane16_swap_b32 v84, v88\nv_permlane16_swap_b32 v85, v89\n" \
"v_permlane16_swap_b32 v86, v90\nv_permlane16_swap_b32 v87, v91\nv_mul_f32 v92, v84, v84\nv_mul_f32 v93, v85, v85\n" \
"v_mul_f32 v94, v86, v86\nv_mul_f32 v95, v87, v87\nv_fma_f32 v92, v92, v253, v252\nv_fma_f32 v93, v93, v253, v252\n" \
"v_fma_f32 v94, v94, v253, v252\nv_fma_f32 v95, v95, v253, v252\nv_mul_f32 v92, v92, v84\nv_mul_f32 v93, v93, v85\n" \
"v_mul_f32 v94, v94, v86\nv_mul_f32 v95, v95, v87\nv_mul_f32 v88, v84, v88\nv_mul_f32 v89, v85, v89\n" \
"v_mul_f32 v90, v86, v90\nv_mul_f32 v91, v87, v91\nv_exp_f32 v92, v92\nv_exp_f32 v93, v93\n" \
"v_exp_f32 v94, v94\nv_exp_f32 v95, v95\nv_fma_f32 v92, v92, v254, v254\nv_fma_f32 v93, v93, v254, v254\n" \
"v_fma_f32 v94, v94, v254, v254\nv_fma_f32 v95, v95, v254, v254\nv_rcp_f32 v92, v92\nv_rcp_f32 v93, v93\n" \
"v_rcp_f32 v94, v94\nv_rcp_f32 v95, v95\nv_mul_f32 v88, v88, v92\nv_mul_f32 v89, v89, v93\n" \
"v_mul_f32 v90, v90, v94\nv_mul_f32 v91, v91, v95\nv_cvt_pk_bf16_f32 v88, v88, v89\nv_cvt_pk_bf16_f32 v89, v90, v91\n" \
"buffer_store_dwordx2 v[88:89], v2, s[8:11], s16 offen offset:0 sc1\nv_fma_f32 v84, v32, v244, v16\nv_fma_f32 v85, v32, v245, v17\nv_fma_f32 v86, v32, v246, v18\n" \
"v_fma_f32 v87, v32, v247, v19\nv_fma_f32 v88, v33, v244, v20\nv_fma_f32 v89, v33, v245, v21\nv_fma_f32 v90, v33, v246, v22\n" \
"v_fma_f32 v91, v33, v247, v23\nv_permlane16_swap_b32 v84, v88\nv_permlane16_swap_b32 v85, v89\nv_permlane16_swap_b32 v86, v90\n" \
"v_permlane16_swap_b32 v87, v91\nv_mul_f32 v92, v84, v84\nv_mul_f32 v93, v85, v85\nv_mul_f32 v94, v86, v86\n" \
"v_mul_f32 v95, v87, v87\nv_fma_f32 v92, v92, v253, v252\nv_fma_f32 v93, v93, v253, v252\nv_fma_f32 v94, v94, v253, v252\n" \
"v_fma_f32 v95, v95, v253, v252\nv_mul_f32 v92, v92, v84\nv_mul_f32 v93, v93, v85\nv_mul_f32 v94, v94, v86\n" \
"v_mul_f32 v95, v95, v87\nv_mul_f32 v88, v84, v88\nv_mul_f32 v89, v85, v89\nv_mul_f32 v90, v86, v90\n" \
"v_mul_f32 v91, v87, v91\nv_exp_f32 v92, v92\nv_exp_f32 v93, v93\nv_exp_f32 v94, v94\n" \
"v_exp_f32 v95, v95\nv_fma_f32 v92, v92, v254, v254\nv_fma_f32 v93, v93, v254, v254\nv_fma_f32 v94, v94, v254, v254\n" \
"v_fma_f32 v95, v95, v254, v254\nv_rcp_f32 v92, v92\nv_rcp_f32 v93, v93\nv_rcp_f32 v94, v94\n" \
"v_rcp_f32 v95, v95\nv_mul_f32 v88, v88, v92\nv_mul_f32 v89, v89, v93\nv_mul_f32 v90, v90, v94\n" \
"v_mul_f32 v91, v91, v95\nv_cvt_pk_bf16_f32 v88, v88, v89\nv_cvt_pk_bf16_f32 v89, v90, v91\nbuffer_store_dwordx2 v[88:89], v2, s[8:11], s17 offen offset:256 sc1\n" \
"v_fma_f32 v84, v32, v248, v24\nv_fma_f32 v85, v32, v249, v25\nv_fma_f32 v86, v32, v250, v26\nv_fma_f32 v87, v32, v251, v27\n" \
"v_fma_f32 v88, v33, v248, v28\nv_fma_f32 v89, v33, v249, v29\nv_fma_f32 v90, v33, v250, v30\nv_fma_f32 v91, v33, v251, v31\n" \
"v_permlane16_swap_b32 v84, v88\nv_permlane16_swap_b32 v85, v89\nv_permlane16_swap_b32 v86, v90\nv_permlane16_swap_b32 v87, v91\n" \
"v_mul_f32 v92, v84, v84\nv_mul_f32 v93, v85, v85\nv_mul_f32 v94, v86, v86\nv_mul_f32 v95, v87, v87\n" \
"v_fma_f32 v92, v92, v253, v252\nv_fma_f32 v93, v93, v253, v252\nv_fma_f32 v94, v94, v253, v252\nv_fma_f32 v95, v95, v253, v252\n" \
"v_mul_f32 v92, v92, v84\nv_mul_f32 v93, v93, v85\nv_mul_f32 v94, v94, v86\nv_mul_f32 v95, v95, v87\n" \
"v_mul_f32 v88, v84, v88\nv_mul_f32 v89, v85, v89\nv_mul_f32 v90, v86, v90\nv_mul_f32 v91, v87, v91\n" \
"v_exp_f32 v92, v92\nv_exp_f32 v93, v93\nv_exp_f32 v94, v94\nv_exp_f32 v95, v95\n" \
"v_fma_f32 v92, v92, v254, v254\nv_fma_f32 v93, v93, v254, v254\nv_fma_f32 v94, v94, v254, v254\nv_fma_f32 v95, v95, v254, v254\n" \
"v_rcp_f32 v92, v92\nv_rcp_f32 v93, v93\nv_rcp_f32 v94, v94\nv_rcp_f32 v95, v95\n" \
"v_mul_f32 v88, v88, v92\nv_mul_f32 v89, v89, v93\nv_mul_f32 v90, v90, v94\nv_mul_f32 v91, v91, v95\n" \
"v_cvt_pk_bf16_f32 v88, v88, v89\nv_cvt_pk_bf16_f32 v89, v90, v91\nbuffer_store_dwordx2 v[88:89], v2, s[8:11], s17 offen offset:512 sc1\ns_mov_b32 s16, 0x80000000\n" \
"s_mov_b32 s17, 0x80000000\nv_mov_b32 v84, s20\ns_cmp_ge_u32 s20, 0x80\ns_cbranch_scc1 6f\n" \
"s_waitcnt vmcnt(3)\nv_max3_u32 v85, v96, v98, v100\nv_max_u32 v85, v85, v102\nv_cmp_eq_u32 vcc, -1, v85\n" \
"s_cbranch_vccz 8f\n6:\ns_waitcnt vmcnt(0)\nbuffer_store_dword v84, v3, s[8:11], 0 offen offset:0 sc1\n" \
"s_mov_b32 s23, 0x40000000\ns_cbranch_scc1 9f\n7:\ns_add_u32 s24, s24, 1\n" \
"s_cmp_lt_u32 s24, 0x40000\ns_cbranch_scc0 9f\nbuffer_load_dwordx4 v[96:99], v1, s[8:11], s95 offen offset:0 sc1\nbuffer_load_dwordx4 v[100:103], v1, s[8:11], s95 offen offset:1024 sc1\n" \
"s_waitcnt vmcnt(0)\nv_max3_u32 v85, v96, v98, v100\nv_max_u32 v85, v85, v102\nv_cmp_eq_u32 vcc, -1, v85\n" \
"s_cbranch_vccz 8f\ns_sleep 8\ns_branch 7b\n8:\n" \
"s_lshl_b32 s94, s94, 16\ns_add_u32 s94, s94, s21\ns_xor_b32 s95, s22, 0x10000\ns_xor_b32 m0, s95, 0\n" \
"s_xor_b32 s94, s94, 0\nbuffer_load_dwordx4 v0, s[8:11], s94 offen offset:0 sc1 lds\nbuffer_load_dwordx4 v0, s[8:11], s94 offen offset:1024 sc1 lds\nbuffer_load_dwordx4 v0, s[8:11], s94 offen offset:2048 sc1 lds\n" \
"buffer_load_dwordx4 v0, s[8:11], s94 offen offset:3072 sc1 lds\ns_xor_b32 m0, s95, 0x1000\ns_xor_b32 s94, s94, 0x1000\nbuffer_load_dwordx4 v0, s[8:11], s94 offen offset:0 sc1 lds\n" \
"buffer_load_dwordx4 v0, s[8:11], s94 offen offset:1024 sc1 lds\nbuffer_load_dwordx4 v0, s[8:11], s94 offen offset:2048 sc1 lds\nbuffer_load_dwordx4 v0, s[8:11], s94 offen offset:3072 sc1 lds\ns_xor_b32 m0, s95, 0x8000\n" \
"s_xor_b32 s94, s94, 0x9000\nbuffer_load_dwordx4 v0, s[8:11], s94 offen offset:0 sc1 lds\nbuffer_load_dwordx4 v0, s[8:11], s94 offen offset:1024 sc1 lds\nbuffer_load_dwordx4 v0, s[8:11], s94 offen offset:2048 sc1 lds\n" \
"buffer_load_dwordx4 v0, s[8:11], s94 offen offset:3072 sc1 lds\ns_xor_b32 m0, s95, 0x9000\ns_xor_b32 s94, s94, 0x1000\nbuffer_load_dwordx4 v0, s[8:11], s94 offen offset:0 sc1 lds\n" \
"buffer_load_dwordx4 v0, s[8:11], s94 offen offset:1024 sc1 lds\nbuffer_load_dwordx4 v0, s[8:11], s94 offen offset:2048 sc1 lds\nbuffer_load_dwordx4 v0, s[8:11], s94 offen offset:3072 sc1 lds\nv_add_f32 v96, v96, v100\n" \
"v_add_f32 v97, v97, v101\nv_add_f32 v98, v98, v102\nv_add_f32 v99, v99, v103\ns_nop 1\n" \
"v_permlane16_swap_b32 v96, v98\nv_permlane16_swap_b32 v97, v99\nv_add_f32 v96, v96, v98\nv_add_f32 v97, v97, v99\n" \
"s_nop 1\nv_permlane32_swap_b32 v96, v97\nv_add_f32 v96, v96, v97\nds_write_b32 v5, v96\n" \
"s_add_u32 s97, s20, 1\ns_lshr_b32 s96, s97, 1\nv_readlane_b32 s96, v6, s96\ns_and_b32 s97, s97, 1\n" \
"s_lshl_b32 s97, s97, 3\ns_lshr_b32 s96, s96, s97\ns_and_b32 s96, s96, 0xff\ns_lshl_b32 s97, s96, 13\n" \
"s_lshl_b32 s96, s96, 16\ns_add_u32 s96, s96, s21\nbuffer_load_dwordx4 v[52:55], v1, s[8:11], s97 offen offset:0 sc1\nbuffer_load_dwordx4 v[56:59], v1, s[8:11], s97 offen offset:1024 sc1\n" \
"s_waitcnt vmcnt(18)\nbuffer_store_dword v84, v3, s[8:11], s23 offen offset:0 sc1\ns_mov_b32 s23, 0x40000000\ns_waitcnt vmcnt(3) lgkmcnt(0)\n" \
"s_barrier\nds_read_b128 v[72:75], v7 offset:0\nds_read_b128 v[36:39], v7 offset:32768\nds_read_b128 v[40:43], v7 offset:1024\n" \
"ds_read_b128 v[76:79], v7 offset:33792\ns_waitcnt lgkmcnt(0)\ns_branch 5b\n9:\n" \
"s_waitcnt vmcnt(0)\ns_endpgm\n"
struct WMA { static constexpr int NT = 4, NP = 16, RB = 0, LB = 0, CBI = 0;
             static constexpr int kind(int j, int t) { return (A_LDS >> (16 * j + t) & 1) ? 1 : (A_VAR >> (16 * j + t) & 1) ? 2 : 0; } };
namespace la { constexpr int L_Q = 24576, L_K = 57344, L_QM = 65536, L_KM = 66048, L_SP = 69632, L_W = 73728; }
#ifndef LEAD_B
#define LEAD_B 132
#endif
DEVI float dror8(float x) { return __uint_as_float(__builtin_amdgcn_update_dpp(0u, __float_as_uint(x), 0x128, 0xF, 0xF, true)); }
DEVI float scm(float a, float m) { float r; asm("v_fma_f32 %0, %1, %2, -%3" : "=v"(r) : "v"(a), "s"(SC_L2E), "v"(m)); return r; }
DEVI float ex2(float x) { return __builtin_amdgcn_exp2f(x); }
DEVI float max3(float a, float b, float c) { float r; asm("v_max3_f32 %0, %1, %2, %3" : "=v"(r) : "v"(a), "v"(b), "v"(c)); return r; }
constexpr u64 tri(int i) { u64 m = 0; for (int l = 0; l < 64; ++l) if (4 * (l >> 4) + i <= (l & 15)) m |= 1ull << l; return m; }
DEVI unsigned vbfe(unsigned a, int o, int n) { unsigned r; asm volatile("v_bfe_u32 %0, %1, %2, %3" : "=v"(r) : "v"(a), "i"(o), "i"(n)); return r; }
DEVI unsigned vadds(unsigned a, unsigned b) { unsigned r; asm volatile("v_add_u32 %0, %1, %2" : "=v"(r) : "s"(b), "v"(a)); return r; }
DEVI float vexp(float a) { float r; asm volatile("v_exp_f32 %0, %1" : "=v"(r) : "v"(a)); return r; }
DEVI unsigned vors(unsigned a, unsigned b) { unsigned r; asm volatile("v_or_b32 %0, %1, %2" : "=v"(r) : "s"(b), "v"(a)); return r; }
DEVI float faddv(float a, float b) { float r; asm volatile("v_add_f32 %0, %1, %2" : "=v"(r) : "v"(a), "v"(b)); return r; }
DEVI unsigned packv(float a, float b) { unsigned r; asm volatile("v_cvt_pk_bf16_f32 %0, %1, %2" : "=v"(r) : "v"(a), "v"(b)); return r; }
DEVI void setm0q(unsigned v) { asm volatile("s_mov_b32 m0, %0" :: "s"(v) : "memory"); }
template <int OFF> DEVI void st16o(v4u v, rsrc_t r, unsigned vo, unsigned so) {
    asm volatile("buffer_store_dwordx4 %0, %1, %2, %3 offen offset:%4 sc1" :: "v"(v), "v"(vo), "s"(r), "s"(so), "n"(OFF) : "memory"); }
DEVI unsigned vlsa(unsigned a, int sh, unsigned c) { unsigned r; asm volatile("v_lshl_add_u32 %0, %1, %2, %3" : "=v"(r) : "v"(a), "i"(sh), "v"(c)); return r; }
DEVI unsigned vlss(unsigned a, int sh, unsigned c) { unsigned r; asm volatile("v_lshl_add_u32 %0, %1, %2, %3" : "=v"(r) : "v"(a), "i"(sh), "s"(c)); return r; }
template <int OFF> DEVI void sts4h(unsigned p, unsigned u0, unsigned u1) {
    asm volatile("ds_write_b16 %0, %1 offset:%3\n\tds_write_b16_d16_hi %0, %1 offset:%3+16\n\tds_write_b16 %0, %2 offset:%3+8\n\tds_write_b16_d16_hi %0, %2 offset:%3+24" :: "v"(p), "v"(u0), "v"(u1), "n"(OFF) : "memory");
}
DEVI void zero4v(f32x4& d) { f32x2 a, b; asm volatile("v_mov_b64 %0, 0\n\tv_mov_b64 %1, 0" : "=v"(a), "=v"(b)); d = __builtin_shufflevector(a, b, 0, 1, 2, 3); }
DEVI float sel0(float a, u64 m) { float r; asm("v_cndmask_b32 %0, %1, 0, %2" : "=v"(r) : "v"(a), "s"(m)); return r; }
DEVI float vmul(float a, float b) { float r; asm("v_mul_f32 %0, %1, %2" : "=v"(r) : "v"(a), "v"(b)); return r; }
DEVI void swap16(float& a, float& b) { unsigned x = __float_as_uint(a), y = __float_as_uint(b); swap16u(x, y); a = __uint_as_float(x); b = __uint_as_float(y); }
#ifndef PACE
#define PACE 8
#endif
DEVI void pace(int l) { asm volatile("s_cmp_eq_u32 %0, 0\n\ts_cbranch_scc1 1f\n\ts_sleep %1\n1:" :: "s"(l), "n"(PACE) : "scc"); }
DEVI void role_A(const Args& a, int w, int l, int h, int rep) {
    asm volatile("; MARK role_A"); __builtin_amdgcn_s_waitcnt(0);
    using namespace la;
    const int lane0 = lane_id();
    const rsrc_t rs = mkrs(a.ws, WS_NEED);
    const unsigned lane16 = lane0 * 16, wl = L_W + w * (11 * 2048) + lane16;
    v8u Wc[3];
    unsigned nb;
    {
        const int lane = lane0;
        const unsigned ta = (lane >> 4) * 2048 + w * 256 + (lane & 15) * 16;
        const rsrc_t r1 = mkrs(a.Wq, WSZ(8u << 20)), r2 = mkrs(a.Wk, WSZ(8u << 20));
        const unsigned v1 = (8 * w + ((lane >> 2) & 3)) * 8192 + h * 256 + (lane >> 4) * 64 + (lane & 3) * 16;
        const unsigned lw = opqs(w * 2048);
        load_blocks<32, 4, 4, true>([&](auto B, auto S) { pace(l); sfor<0, 2>([&](auto I) { dma_blk(lw + S.v * 16384 + I.v * 8192, B.v < 16 ? r1 : r2, v1, opqs((2 * (B.v & 15) + I.v) * 32 * 8192), 2048); }); },
                                    [&](auto B, auto S) { sfor<0, 4>([&](auto T) { constexpr int o = S.v * 16384 + (T.v >> 1) * 8192 + (T.v & 1) * 8;
                                                                                   put_frag<WMA, (B.v >> 4) * 2 + (T.v & 1), 2 * (B.v & 15) + (T.v >> 1), o, o + 1024>(ta, wl, Wc); });
                                                          if constexpr (B.v == 28) nb = side(rs, l); });
        TSW(127, 2);
    }
    const unsigned spm = na(nb, N_PM + h * 512), spmm = na(nb, N_PMM + h * 128), smem = na(nb, N_MEM + rep * 16384 + w * 4096);
    const int lane = lane_id(), c = lane & 15, g = lane >> 4;
    const unsigned own = (w >> 1) * 32768 + (w & 1) * 4096, wlo = (unsigned)(uintptr_t)a.ws, whi = (unsigned)((uintptr_t)a.ws >> 32), hi = -(w >> 1);
    const v8u pv{lane16, wl, (lane & 2) ? 0xEEEEu : 0x4444u, (unsigned)(L_QM + w * 1024 + (c >> 2) * 128 + g * 32 + (c & 3) * 4), (unsigned)(L_QM + g * 128 + (c & 7) * 16),
                 w == 2 ? ((g >> 1) * 16 + crow(c & 7, c >> 3)) * 16 + (g & 1) * 8 : w < 2 ? lane * 8 : lane < 32 ? DV(lane * 512) : 1u << 30,
                 w < 2 ? c * 16 + g * 4 : c < 8 ? crow(c, g & 1) * 8 + (g >> 1) * 4 : 1u << 30, 0u};
    const v16u s0{wlo, whi, RSZ, 0x20000u, wlo, whi, 0u, 0x20000u, (unsigned)tri(0) | hi, (unsigned)(tri(0) >> 32) | hi, (unsigned)tri(1) | hi, (unsigned)(tri(1) >> 32) | hi,
                  (unsigned)tri(2) | hi, (unsigned)(tri(2) >> 32) | hi, (unsigned)tri(3) | hi, (unsigned)(tri(3) >> 32) | hi};
    const v16u s1{0xFF00FF00u, 0xFF00FF00u, 0u, ~0u, hi, hi, 0u, O_XN + l * XB + own, own, smem, w == 2 ? spm : OOB, nb + DV(N_PTX + h * 2048), w == 1 ? 0u : OOB, w == 0 ? 0u : OOB,
                  w < 2 ? w * 256 : w == 2 ? spmm - 1536 : OOB, ~hi};
    const v16u s2{w < 2 ? w * 1024 : w == 2 ? spm : spmm, 17u - w, w < 2 ? 0u : w == 2 ? 0x200u : 0x380u, w ? 0u : __float_as_uint(-3e38f), (unsigned)(L_Q + w * 1024), (unsigned)(L_Q + (w & 1) * 4096),
                  __float_as_uint(SC_L2E), __float_as_uint(-1e30f), na(nb, N_FX), w == 3 ? na(nb, N_PA + ((h >> 3) * 32 + 8 + (h & 7)) * 4) : OOB, (unsigned)(L_KM + (w >> 1) * 1024 + (w & 1) * 256),
                  0u, 0u, (unsigned)(L_SP + w * 256), O_PROF + (blockIdx.x * 4 + w) * 8192, na(nb, N_WM + h * 16384 + w * 4096)};
    asm volatile(A_LOOP :: "{v[0:7]}"(Wc[0]), "{v[8:15]}"(Wc[1]), "{v[16:23]}"(Wc[2]), "{v[24:31]}"(pv), "{s[8:23]}"(s0), "{s[24:39]}"(s1), "{s[40:55]}"(s2),
                 "{s58}"(na(nb, N_U + 8 * w * 65536 + h * 2048)) : "memory", "s99", "vcc");
}
constexpr int bkind(int n) { return (B_LDS >> n & 1) ? 1 : (B_VAR >> n & 1) ? 2 : 0; }
struct WMV { static constexpr int NT = 2, NP = 16, RB = 0, LB = 0, CBI = 0; static constexpr int kind(int j, int t) { return bkind(16 * j + t); } };
struct WMO { static constexpr int NT = 16, NP = 2, LB = __builtin_popcount((unsigned)B_LDS), CBI = __builtin_popcount((unsigned)B_VAR), RB = 8 * (32 - LB - CBI);
             static constexpr int kind(int j, int t) { return bkind(32 + 2 * j + t); } };
#ifndef B_LV
#define B_LV 24576
#endif
namespace lb { constexpr int L_V = B_LV, L_VM = B_LV + 4096, L_W = 65536; }
DEVI void role_B(const Args& a, int w, int l, int h, int rep) {
    asm volatile("; MARK role_B"); __builtin_amdgcn_s_waitcnt(0);
    using namespace lb;
    const int lane0 = lane_id();
    const rsrc_t rs = mkrs(a.ws, WS_NEED);
    const unsigned lane16 = lane0 * 16, wl = L_W + w * (12 * 2048) + lane16;
    v8u Wc[2];
    unsigned nb, fx;
    {
        const int lane = lane0;
        const unsigned ta = (lane >> 4) * 2048 + w * 256 + (lane & 15) * 16, tb = (lane >> 5) * 8192 + w * 2048 + (lane & 16) * 64 + (lane & 15) * 16;
        const rsrc_t r1 = mkrs(a.Wv, WSZ(8u << 20)), r2 = mkrs(a.Wo, WSZ(8u << 20));
        const unsigned v1 = (8 * w + ((lane >> 2) & 3)) * 8192 + h * 256 + (lane >> 4) * 64 + (lane & 3) * 16, v2 = (h * 128 + ((lane >> 2) & 7)) * 2048 + w * 512 + (lane >> 5) * 64 + (lane & 3) * 16;
        const unsigned lw = opqs(w * 2048);
        load_blocks<32, 4, 4, true>([&](auto B, auto S) {
            constexpr int b = B.v & 15;
            pace(l);
            sfor<0, 2>([&](auto I) { if constexpr (B.v < 16) dma_blk(lw + S.v * 16384 + I.v * 8192, r1, v1, opqs((2 * b + I.v) * 32 * 8192), 2048);
                                     else dma_blk(lw + S.v * 16384 + I.v * 8192, r2, v2, opqs(((b & 3) * 32 + I.v * 16) * 2048 + (b >> 2) * 128), 1024); });
        }, [&](auto B, auto S) {
            constexpr int b = B.v & 15;
            sfor<0, 4>([&](auto T) { constexpr int o = S.v * 16384 + (T.v >> 1) * (B.v < 16 ? 8192 : 512) + (T.v & 1) * 8;
                                     if constexpr (B.v < 16) put_frag<WMV, T.v & 1, 2 * b + (T.v >> 1), o, o + 1024>(ta, wl, Wc); else put_frag<WMO, (b & 12) + T.v, b & 3, o, o + 256>(tb, wl, Wc); });
            if constexpr (B.v == 28) { nb = side(rs, l); fx = ld4(rs, (lane & 7) * 4, na(nb, N_FX)); }
        });
        TSW(127, 2);
    }
    const int lane = lane_id(), c = lane & 15, g = lane >> 4;
    const unsigned own = (w >> 1) * 32768 + (w & 1) * 4096, wlo = (unsigned)(uintptr_t)a.ws, whi = (unsigned)((uintptr_t)a.ws >> 32);
    const unsigned prow = L_VM + w * 512 + ((c >> 2) & 1) * 256 + g * 64 + (c >> 3) * 32 + (c & 1) * 8 + ((c >> 1) & 1) * 4;
    const v4u pv{lane16, wl, ((lane & 2) ? 0xEEEEu : 0x4444u) | prow << 16, (unsigned)(L_VM + (g >> 1) * 512 + (lane & 3) * 64 + (g & 1) * 32 + ((lane >> 2) & 3) * 8)};
    const v16u s0{wlo, whi, RSZ, 0x20000u, wlo, whi, 0u, 0x20000u, OOB, OOB, OOB, OOB, ~1u, O_XN + l * XB + own, own, na(nb, N_MEM + rep * 16384 + w * 4096)};
    const v8u s1{na(nb, N_WM + h * 16384 + w * 4096), na(nb, N_U + h * 65536 + w * 16384), na(nb, N_FX), w == 3 ? na(nb, N_PA + ((h >> 3) * 32 + (h & 7)) * 4) : OOB,
                 (unsigned)(L_V + w * 1024), OOB, 0u, O_PROF + (blockIdx.x * 4 + w) * 8192 + 64};
    asm volatile(B_LOOP :: "{v[0:7]}"(Wc[0]), "{v[8:15]}"(Wc[1]), "{v[16:19]}"(pv), "{v20}"(fx), "{s[8:23]}"(s0), "{s[24:31]}"(s1) : "memory", "s99", "vcc");
}
template <class NB> DEVI void put_tile(v4u (&u)[8], rsrc_t rs, int tile, int w, int lane, NB&& nbf) {
    const unsigned vo = tile * 32768 + w * 8192 + lane * 16, red = (lane & 15) * 16;
    float s = 0.f;
#pragma unroll
    for (int f = 0; f < 8; ++f) {
        st16(u[f], rs, O_XIN + vo + f * 1024);
#pragma unroll
        for (int i = 0; i < 4; ++i) s += bflo(u[f][i]) + bfhi(u[f][i]);
    }
    s = red32(red16(s));
    BAR;
    if (lane < 16) *LDSP(float, red + w * 4) = s;
    WL0; BAR;
    const f32x4 sa = __builtin_bit_cast(f32x4, ldsq(red, 0));
    const float mean = ((sa[0] + sa[1]) + (sa[2] + sa[3])) * (1.0f / 1024.0f);
    float q = 0.f;
#pragma unroll
    for (int f = 0; f < 8; ++f) {
        asm volatile("" : "+v"(u[f]));
#pragma unroll
        for (int i = 0; i < 4; ++i) { const float d0 = bflo(u[f][i]) - mean, d1 = bfhi(u[f][i]) - mean; q = fmaf(d0, d0, fmaf(d1, d1, q)); }
    }
    q = red32(red16(q));
    if (lane < 16) *LDSP(float, red + 256 + w * 4) = q;
    WL0; BAR;
    const f32x4 qa = __builtin_bit_cast(f32x4, ldsq(red, 256));
    const float mb = rbf(mean), inv = 1.0f / rbf(sqrtf(rbf(rbf(((qa[0] + qa[1]) + (qa[2] + qa[3])) * (1.0f / 1024.0f)) + 1e-5f)));
#pragma unroll
    for (int f = 0; f < 8; ++f) {
        v4u o;
        asm volatile("" : "+v"(u[f]));
#pragma unroll
        for (int i = 0; i < 4; ++i) { const unsigned d = pack2(bflo(u[f][i]) - mb, bfhi(u[f][i]) - mb); o[i] = pack2(bflo(d) * inv, bfhi(d) * inv); }
        st16(o, rs, O_XN + vo + f * 1024);
    }
    const unsigned nb = nbf();
    WV0;
    if (lane == 0) st4(0u, rs, na(nb, N_FX) + tile * 16 + w * 4, 0);
}
#ifndef SXL
#define SXL 1
#endif
#ifndef PTD
#define PTD 4
#endif
#ifndef HLD
#define HLD 170
#endif
#ifndef SXH
#define SXH 30
#endif
#ifndef PTL2
#define PTL2 0
#endif
#ifndef SXP
#define SXP 1
#endif
#ifndef SXH0
#define SXH0 SXH
#endif
#ifndef WF
#define WF 0
#endif
#ifndef UIN
#define UIN 8
#endif
namespace lr { constexpr int PT1 = 192, PT0 = 224, PTM = 240, VPM = NV, VMM = NV + 16, VW = NV + 28, VX = NV + 44, L_M = 1024, L_T = 8192; }
template <int R, int N, int OFF, bool CO = true> DEVI void lda(rsrc_t rs, unsigned vo, unsigned so) {
    if constexpr (!CO && N == 4) asm volatile("buffer_load_dwordx4 a[%3:%4], %0, %1, %2 offen offset:%5" :: "v"(vo), "s"(rs), "s"(so), "n"(R), "n"(R + 3), "n"(OFF) : "memory");
    else if constexpr (!CO) asm volatile("buffer_load_dwordx2 a[%3:%4], %0, %1, %2 offen offset:%5" :: "v"(vo), "s"(rs), "s"(so), "n"(R), "n"(R + 1), "n"(OFF) : "memory");
    else if constexpr (N == 4) asm volatile("buffer_load_dwordx4 a[%3:%4], %0, %1, %2 offen offset:%5 sc1" :: "v"(vo), "s"(rs), "s"(so), "n"(R), "n"(R + 3), "n"(OFF) : "memory");
    else asm volatile("buffer_load_dwordx2 a[%3:%4], %0, %1, %2 offen offset:%5 sc1" :: "v"(vo), "s"(rs), "s"(so), "n"(R), "n"(R + 1), "n"(OFF) : "memory");
}
template <int R> DEVI unsigned rdv() { unsigned t; asm volatile("v_mov_b32 %0, v[%1]" : "=v"(t) : "n"(R)); return t; }
template <int RA, int RB, bool Z, bool AV> DEVI void mfh(f32x4& d) {
    if constexpr (AV) { if constexpr (Z) asm volatile(M16 "%0, v[%1:%2], a[%3:%4], 0" : "=v"(d) : "n"(RA), "n"(RA + 3), "n"(RB), "n"(RB + 3));
                        else asm volatile(M16 "%0, v[%1:%2], a[%3:%4], %0" : "+v"(d) : "n"(RA), "n"(RA + 3), "n"(RB), "n"(RB + 3)); }
    else { if constexpr (Z) asm volatile(M16 "%0, a[%1:%2], a[%3:%4], 0" : "=v"(d) : "n"(RA), "n"(RA + 3), "n"(RB), "n"(RB + 3));
           else asm volatile(M16 "%0, a[%1:%2], a[%3:%4], %0" : "+v"(d) : "n"(RA), "n"(RA + 3), "n"(RB), "n"(RB + 3)); }
}
template <int RA, int RB, bool Z> DEVI void mfh16(f32x4& d) {
    if constexpr (Z) asm volatile("v_mfma_f32_16x16x16_bf16 %0, a[%1:%2], a[%3:%4], 0" : "=v"(d) : "n"(RA), "n"(RA + 1), "n"(RB), "n"(RB + 1));
    else asm volatile("v_mfma_f32_16x16x16_bf16 %0, a[%1:%2], a[%3:%4], %0" : "+v"(d) : "n"(RA), "n"(RA + 1), "n"(RB), "n"(RB + 1));
}
template <int RA, int RB, bool BV> DEVI void smfh(f32x4& d, int sidx) {
    if constexpr (BV) asm volatile(S16 "%0, v[%2:%3], v[%4:%5], %1" : "+v"(d) : "v"(sidx), "n"(RA), "n"(RA + 3), "n"(RB), "n"(RB + 7));
    else asm volatile(S16 "%0, v[%2:%3], a[%4:%5], %1" : "+v"(d) : "v"(sidx), "n"(RA), "n"(RA + 3), "n"(RB), "n"(RB + 7));
}
template <int R> DEVI u64 ltv(int need) { u64 m; asm volatile("v_cmp_lt_i32 %0, v[%2], %1" : "=s"(m) : "v"(need), "n"(R)); return m; }
template <int R> DEVI void ldv4(rsrc_t rs, unsigned vo, unsigned so) { asm volatile("buffer_load_dwordx4 v[%3:%4], %0, %1, %2 offen sc1" :: "v"(vo), "s"(rs), "s"(so), "n"(R), "n"(R + 3) : "memory"); }
template <int W0, int P0>
DEVI void poll_round(unsigned vw, unsigned lane16, unsigned vmm, rsrc_t rs, unsigned sw0, unsigned sw1, unsigned sp, unsigned sm) {
    asm volatile(
#if WF
        "buffer_load_dwordx4 v[%[w0]:%[w0]+3], %[vw], %[rs], %[sw0] offen sc1\n\t"
        "buffer_load_dwordx4 v[%[w0]+4:%[w0]+7], %[vw], %[rs], %[sw1] offen sc1\n\t"
        "buffer_load_dwordx4 v[%[w0]+8:%[w0]+11], %[vw], %[rs], %[sw0] offen offset:256 sc1\n\t"
        "buffer_load_dwordx4 v[%[w0]+12:%[w0]+15], %[vw], %[rs], %[sw1] offen offset:256 sc1\n\t"
#endif
        "buffer_load_dwordx4 v[%[p0]:%[p0]+3], %[l16], %[rs], %[sp] offen sc1\n\t"
        "buffer_load_dwordx4 v[%[p0]+4:%[p0]+7], %[l16], %[rs], %[sp] offen offset:1024 sc1\n\t"
        "buffer_load_dwordx4 v[%[p0]+8:%[p0]+11], %[l16], %[rs], %[sp] offen offset:2048 sc1\n\t"
        "buffer_load_dwordx4 v[%[p0]+12:%[p0]+15], %[l16], %[rs], %[sp] offen offset:3072 sc1\n\t"
        "buffer_load_dwordx2 v[%[p0]+16:%[p0]+17], %[vm], %[rs], %[sm] offen sc1\n\t"
        "buffer_load_dwordx2 v[%[p0]+18:%[p0]+19], %[vm], %[rs], %[sm] offen offset:128 sc1\n\t"
#if !WF
        "buffer_load_dwordx4 v[%[w0]:%[w0]+3], %[vw], %[rs], %[sw0] offen sc1\n\t"
        "buffer_load_dwordx4 v[%[w0]+4:%[w0]+7], %[vw], %[rs], %[sw1] offen sc1\n\t"
        "buffer_load_dwordx4 v[%[w0]+8:%[w0]+11], %[vw], %[rs], %[sw0] offen offset:256 sc1\n\t"
        "buffer_load_dwordx4 v[%[w0]+12:%[w0]+15], %[vw], %[rs], %[sw1] offen offset:256 sc1\n\t"
#endif
        :: [w0] "n"(W0), [p0] "n"(P0), [vw] "v"(vw), [l16] "v"(lane16), [vm] "v"(vmm), [rs] "s"(rs), [sw0] "s"(sw0), [sw1] "s"(sw1), [sp] "s"(sp), [sm] "s"(sm) : "memory");
}
template <int R0, int R1> DEVI void mx3(unsigned& m) { asm volatile("v_max3_u32 %0, %0, v[%1], v[%2]" : "+v"(m) : "n"(R0), "n"(R1)); }
DEVI void wr2x2(unsigned p, float r0, float r1, float r2, float r3) {
    asm volatile("ds_write2_b32 %0, %1, %3 offset0:0 offset1:2\n\tds_write2_b32 %0, %2, %4 offset0:8 offset1:10" :: "v"(p), "v"(r0), "v"(r1), "v"(r2), "v"(r3) : "memory");
}
#ifndef DLY_X
#define DLY_X 1400
#endif
DEVI void role_R(const Args& a, int w, int l, int T2, int t0) {
    asm volatile("; MARK role_R"); __builtin_amdgcn_s_waitcnt(0);
    using namespace lr;
    const int lane = lane_id(), c = lane & 15, g = lane >> 4;
    const rsrc_t rs = mkrs(a.ws, WS_NEED);
    const unsigned lane16 = lane * 16;
#pragma nounroll
    for (int k = 0; k < 4 * l; ++k) {
        const int tile = k * 32 + T2;
        if (tile >= LN_NOW) hold(t0, DLY_X);
        v4u u[8];
#pragma unroll
        for (int f = 0; f < 8; ++f) u[f] = *(const v4u*)(a.x + (size_t)(tile * 16 + c) * 1024 + (w * 8 + f) * 32 + g * 8);
        if (tile < 2 * NSEG) put_tile(u, rs, tile, w, lane, [&]() { hold(t0, DLY_V); return side(rs, 0); });
        TSW(127, 1 + k);
    }
    if (l == 0)
#pragma unroll
        for (int i = 0; i < 2; ++i) {
            const unsigned n2 = O_N + i * 8192;
#pragma unroll
            for (int k = 0; k < 4; ++k) st16(v4u{SENT, SENT, SENT, SENT}, rs, na(n2, N_WM + T2 * 16384 + w * 4096) + k * 1024 + lane16);
            st16(v4u{SENT, SENT, SENT, SENT}, rs, w < 2 ? na(n2, N_PTX + T2 * 2048) + w * 1024 + lane16 : lane >= (w == 2 ? 32 : 8) ? OOB : na(n2, w == 2 ? N_PM + T2 * 512 : N_PMM + T2 * 128) + lane16);
        }
    hold(t0, DLY_V);
    const unsigned nb = side(rs, l), vmem = na(nb, N_MEM + 2 * w * 16384 + T2 * 512) + lane * 8;
    auto put_mem = [&](v2u v, int s) { const unsigned so = rslot(s) * (NREP * 32768); st8(v, rs, vmem, so); st8(v, rs, vmem, so + 32768); };
    {
        const unsigned* m0 = (const unsigned*)(a.mem0 + (((lane >> 3) & 3) * 2 + ((lane >> 1) & 1)) * 1024 + T2 * 32 + (lane >> 5) * 16 + (lane & 1) * 8 + ((lane >> 2) & 1) * 2);
        const v2u mv{nosent(m0[0]), m0[2]};
        put_mem(v2u{SENT, SENT}, 1); put_mem(v2u{SENT, SENT}, 2);
        WV0; BAR;
        put_mem(mv, 0);
    }
    const int sidx = opq((lane & 2) ? 0xEEEE : 0x4444);
    const unsigned vmm = (8 * w + 2 * g) * 128 + c * 8, vw = (8 * w + g) * 32768 + DV(T2 * 512) + c * 16, vptm = g * 2048 + 1536 + c * 16;
    const unsigned sfl = na(nb, N_PA + (unsigned)(T2 * 4 + w) * 128), su0 = na(nb, N_U + 8 * w * 65536 + T2 * 2048), vfl = sfl + c * 4;
    const unsigned lred = L_M + w * 1024 + (c >> 3) * 512 + g * 128 + ((c >> 1) & 1) * 64 + ((c >> 2) & 1) * 16 + (c & 1) * 4;
    auto ld_u = [&](auto B, int s) {
        const unsigned so = su0 + rslot(s) * (4u << 20);
        sfor<0, 16>([&](auto I) { constexpr int j = I.v >> 1, e = I.v & 1; lda<64 * B.v + 16 * (j >> 1) + 8 * e + 4 * (j & 1), 4, e * 1024>(rs, lane16, so + j * 131072); });
    };
    wait_prog(rs, sfl + (lane & 7) * 4, 0, 0);
    ld_u(IC<0>{}, 0);
    wait_prog(rs, vfl, 0, 1 << 24);
    v2u sxv{0u, 0u};
    unsigned sxo = OOB;
    int tpub = t0;
    auto step = [&](auto B, int s) {
        constexpr int b = B.v;
        TSD;
        TSR(0); TSK(15);
        if (s == 8) { TSC(126, 0); TSW(126, 1); }
        if (s == 56) { TSC(126, 2); TSW(126, 3); }
        const unsigned spt = na(nb, N_PTX + (unsigned)(s * 32 + 8 * w) * 2048);
        auto ptx = [&](auto N) {
            constexpr int n = N.v & 31, j = n >> 1; constexpr bool co = N.v >= 32 || !PTL2;
            if constexpr (n == 20) ldv4<VX>(rs, lane16, O_XIN + l * XB + s * 65536 + (w & 1) * 32768 + T2 * 1024);
            else if constexpr (n >= 16) lda<PTM + 4 * (n - 16), 4, (n & 1) * 256, co>(rs, vptm, spt + (j - 8) * 16384);
            else if constexpr (n & 1) lda<PT0 + 2 * j, 2, 1024, co>(rs, lane * 8, spt + DV(j * 2048));
            else lda<PT1 + 4 * j, 4, 0, co>(rs, lane16, spt + DV(j * 2048));
        };
        {
            const unsigned sw = na(nb, N_WM + rslot(s) * 524288);
            if ((int)__builtin_amdgcn_s_memrealtime() - tpub < HLD) { wvm<16 - UIN>(); st8(sxv, rs, sxo, 0); sxo = OOB; hold(tpub, HLD); }
            wait_hint(rs, sfl, vfl, s + 1, [&](int mx) { if (SXH > -99 && left(mx) > (l ? SXH : SXH0) * 256) { wvm<16 - UIN>(); st8(sxv, rs, sxo, 0); sxo = OOB; } });
            TSR(5);
            unsigned spins = 0;
            f32x4 e0, e1;
            unsigned mx;
            auto look = [&]() { zero4(e0); zero4(e1); mx = opq(0); poll_round<VW, VPM>(vw, lane16, vmm, rs, sw, sw + 131072, na(nb, N_PM + w * 4096) + rslot(s) * 32768, na(nb, N_PMM) + rslot(s) * 16384); };
            auto prod = [&](auto NY) {
                sfor<0, 10>([&](auto N) {
                    constexpr int n = N.v, p = n >> 1, e = n & 1;
                    if constexpr (n < 8 && e == 0) wvm<(n ? NY.v : 0) + (WF ? 5 : 9) - p>();
                    if constexpr (n >= 8) wvm<NY.v + (WF ? 0 : 2 - 2 * e)>();
                    if constexpr (n == 1 && NY.v) { st8(sxv, rs, sxo, 0); sxo = OOB; }
                    if constexpr (p < 4) smfh<VPM + 4 * p, 64 * b + 16 * p + 8 * e, false>(e ? e1 : e0, sidx);
                    else smfh<VMM, VW + 8 * e, true>(e ? e1 : e0, sidx);
                    if constexpr (n < 8 && e == 0) mx3<VPM + 4 * p, VPM + 4 * p + 2>(mx);
                    if constexpr (n >= 8) { mx3<VMM + 2 * e, VMM + 2 * e + 1>(mx); mx3<VW + 8 * e, VW + 8 * e + 4>(mx); }
                    SB;
                });
            };
#pragma nounroll
            for (;;) {
                look(); prod(IC<SXP>{});
                nop<3>(); fence(e0, e1);
                if (pend1(mx) == 0) break;
                spin_bail(spins);
                nop<7>();
            }
            TSR(1);
#if defined(TS) && TS != 4
            tsr[8] = spins + 1;
#endif
            const float r0 = fadd(e0[0], e0[2]), r1 = fadd(e0[1], e0[3]), r2 = fadd(e1[0], e1[2]), r3 = fadd(e1[1], e1[3]);
            wr2x2(lred, r0, r1, r2, r3);
            sfor<0, PTD>(ptx);
            WL0; BAR;
            f32x4 q[4];
#pragma unroll
            for (int k = 0; k < 4; ++k) q[k] = __builtin_bit_cast(f32x4, ldsq(lane16, L_M + k * 1024));
            float o[4];
#pragma unroll
            for (int i = 0; i < 4; ++i) o[i] = fadd(fadd(q[0][i], q[1][i]), fadd(q[2][i], q[3][i]));
            const v2u mv{nosent(pack2(o[0], o[1])), pack2(o[2], o[3])};
            put_mem(mv, s + 1);
            if (!SXP) st8(sxv, rs, sxo, 0);
            tpub = (int)__builtin_amdgcn_s_memrealtime();
            TSR(2);
        }
        sfor<PTD, 21>(ptx);
        if (PTD < 21) BAR;
        f32x4 t[2][2], u[2];
        put_mem(v2u{SENT, SENT}, s + 3);
#pragma nounroll
        for (int n = 0;; ++n) {
            if (n) { sfor<32, 52>(ptx); WV0; }
            const unsigned su = su0 + rslot(s + 1) * (4u << 20);
            auto lu = [&](auto N) { constexpr int j = N.v >> 1, e = N.v & 1; lda<16 * (j >> 1) + 8 * e + 4 * (j & 1) + 64 * (1 - b), 4, e * 1024>(rs, lane16, su + j * 131072); };
            TSR(6);
            sfor<0, 16>([&](auto N) {
                constexpr int j = N.v >> 1, e = N.v & 1, R = 64 * b + 16 * (j >> 1) + 8 * e + 4 * (j & 1), nu = N.v * UIN / 16;
                if constexpr (e == 0) wvm<21 - 2 * j + (2 * j + 1 < PTD ? 3 - SXP : 0) + nu>();
                if constexpr ((N.v + 1) * UIN / 16 > nu) lu(IC<nu>{});
                mfh<R, PT1 + 4 * j, j == 0, false>(t[1][e]); mfh16<R, PT0 + 2 * j, j == 0>(u[e]);
            });
            wvm<2 + (PTD == 21 ? 3 - SXP : 0) + UIN>();
            sfor<0, 8>([&](auto N) { constexpr int q = N.v >> 2, rt = (N.v >> 1) & 1, e = N.v & 1; mfh<VW + 8 * e + 4 * q, PTM + 4 * (2 * q + rt), N.v < 2, true>(t[rt][e]); });
            nop<7>(); fence(t[0][0], t[0][1], t[1][0], t[1][1], u[0], u[1]);
#pragma unroll
            for (int e = 0; e < 2; ++e)
#pragma unroll
                for (int i = 0; i < 4; ++i) t[0][e][i] = fadd(t[0][e][i], u[e][i]);
            if (__builtin_amdgcn_fcmpf(t[0][0][0], t[1][0][0], 8) == 0 || n == 2) break;
            TSV(s, 7, 1);
            if (n == 1) wait_prog(rs, sfl + 32 + (lane & 7) * 4, 0, (s + 2) << 24);
        }
        TSR(3);
#pragma unroll
        for (int e = 0; e < 2; ++e) { stsq(lane16 + w * 4096, L_T + e * 1024, __builtin_bit_cast(v4u, t[0][e])); stsq(lane16 + w * 4096, L_T + 2048 + e * 1024, __builtin_bit_cast(v4u, t[1][e])); }
        WL0; BAR;
        if (w < 2) {
            float o[8], s1 = 0.f, s2 = 0.f;
#pragma unroll
            for (int e = 0; e < 2; ++e) {
                f32x4 q[4];
#pragma unroll
                for (int k = 0; k < 4; ++k) q[k] = __builtin_bit_cast(f32x4, ldsq(lane16 + w * 2048, L_T + k * 4096 + e * 1024));
#pragma unroll
                for (int i = 0; i < 4; ++i) o[4 * e + i] = fadd(fadd(q[0][i], q[1][i]), fadd(q[2][i], q[3][i]));
            }
            v4u x1;
            sfor<0, 4>([&](auto I) {
                constexpr int i = I.v;
                const unsigned x = rdv<VX + i>(), at = pack2(o[2 * i], o[2 * i + 1]);
                x1[i] = pack2(bflo(x) + bflo(at), bfhi(x) + bfhi(at));
                const float lo = bflo(x1[i]), hi = bfhi(x1[i]);
                s1 += lo + hi; s2 = fmaf(lo, lo, fmaf(hi, hi, s2));
            });
            st16(x1, rs, O_XM + l * XB + s * 65536 + w * 32768 + T2 * 1024 + lane16);
            sxv = v2u{nosent(__float_as_uint(red32(red16(s1)))), __float_as_uint(red32(red16(s2)))};
            sxo = lane < 16 ? O_SX + (unsigned)((l * 64 + s) * 32 + T2) * 256 + (SXL ? w * 128 + c * 8 : c * 16 + w * 8) : OOB;
        }
        TSR(4);
        {
            const unsigned su = su0 + rslot(s + 1) * (4u << 20);
            sfor<UIN, 16>([&](auto N) { constexpr int j = N.v >> 1, e = N.v & 1; lda<16 * (j >> 1) + 8 * e + 4 * (j & 1) + 64 * (1 - b), 4, e * 1024>(rs, lane16, su + j * 131072); });
        }
#if TS != 4
        TSF(s, 0x817F);
#endif
    };
#pragma nounroll
    for (int s = 0; s < NSEG; s += 2) { step(IC<0>{}, s); step(IC<1>{}, s + 1); }
    wvm<16 - UIN>(); st8(sxv, rs, sxo, 0);
    WV0;
}
#ifndef FSIDE
#define FSIDE 1
#endif
#ifndef PDX
#define PDX 0
#endif
constexpr int NU = 2 * NSEG;
#ifndef PSF
#define PSF 40
#endif
#ifndef PSR
#define PSR 12
#endif
#ifndef RS1
#define RS1 52
#endif
constexpr int RSH = 4 * (64 + RS1);
#ifndef RPA
#define RPA 8
#endif
#ifndef GROT
#define GROT 1
#endif
#ifndef LAGK
#define LAGK 10
#endif
#ifndef LAGP
#define LAGP 0
#endif
DEVI unsigned unit_ls(unsigned u) {
    static_assert(NSEG == 64 && LAGD >= 6, "");
    static_assert(LAGP % 2 == 0 && LAGP < 2 * LAGK + 1, "");
    const unsigned d = u - LAGD + LAGP, p = d / (2 * LAGK + 1), r = d % (2 * LAGK + 1), n0 = LAGD - LAGP / 2 + p * (LAGK + 1) + (r >> 1);
    return u < LAGD || n0 >= 64 ? u : ((r & 1) || r == 2 * LAGK) ? n0 : 64 + u - n0;
}
DEVI unsigned unit_tab(int lane) { return opq(unit_ls(2 * lane) | unit_ls(2 * lane + 1) << 8); }
DEVI unsigned unit_get(unsigned tab, int u) { return ((unsigned)__builtin_amdgcn_readlane(tab, (u >> 1) & 63) >> ((u & 1) * 8)) & 255; }
struct WMG { static constexpr int NT = 3, NP = 16, RB = 0, LB = 0, CBI = 0; static constexpr int kind(int, int) { return 0; } };
struct WMD { static constexpr int NT = 4, NP = 12, RB = 0, LB = 0, CBI = 0; static constexpr int kind(int, int) { return 0; } };
namespace lg { constexpr int L_ST = 155648, L_OK = 159744; }
DEVI void role_GU(const Args& a, int w, int j) {
    asm volatile("; MARK role_GU"); __builtin_amdgcn_s_waitcnt(0);
    using namespace lg;
    const int lane = lane_id(), c = lane & 15, g = lane >> 4;
    const rsrc_t rs = mkrs(a.ws, WS_NEED);
    const unsigned lane16 = lane * 16, hc = 96 * j + 24 * w;
    {
        const unsigned ta = g * 4096 + (lane & 1) * 2048 + w * 256 + ((lane >> 2) & 3) * 64 + ((lane >> 1) & 1) * 8, tb = ta + 65536;
        const rsrc_t r1 = mkrs(a.Wg, WSZ(7168u * 1024)), r2 = mkrs(a.Wu, WSZ(7168u * 1024));
        const unsigned cb = 192 * j + g * 48 + (lane & 3) * 16, v1 = (lane & 3) == 3 || cb >= 7168 ? OOB : (8 * w + ((lane >> 2) & 3)) * 7168 + cb, lw = opqs(w * 4096), z = opqs(0);
        load_blocks<32, 8, 4, true>([&](auto B, auto S) { dma_blk(lw + S.v * 16384, r1, v1, z + B.v * 32 * 7168, 1792); dma_blk(lw + S.v * 16384 + 2048, r2, v1, z + B.v * 32 * 7168, 1792); },
                                    [&](auto B, auto S) { sfor<0, 3>([&](auto T) { constexpr int o = (S.v & 3) * 16384 + T.v * 16; put_frag<WMG, T.v, B.v, o, o + 1024>(S.v < 4 ? ta : tb, 0, nullptr); }); });
        TSW(120 - 65536, 0);
        v4u one{0x3F803F80u, 0x3F803F80u, 0x3F803F80u, 0x3F803F80u};
        asm volatile("s_nop 1" : "+v"(one));
        f32x4 cs[3];
        sfor<0, 96>([&](auto N) { constexpr int t = N.v % 3, ks = N.v / 3; mfA<8 * widx<WMG>(t, ks >> 1) + 4 * (ks & 1), ks == 0>(cs[t], one); });
        nop<7>(); fence(cs[0], cs[1], cs[2]);
        sfor<0, 3>([&](auto T) { mwr4<384 + 4 * T.v>(__builtin_bit_cast(v4u, cs[T.v])); });
        BAR;
    }
    const unsigned nf = side(rs, FSIDE);
    const unsigned wlo = (unsigned)(uintptr_t)a.ws, whi = (unsigned)((uintptr_t)a.ws >> 32);
    const unsigned xr = w * 8192 + (GROT ? (j & 1) * 4096 + (j & 2) * 16384 : 0);
    const v8u pv{lane16, O_SX + (8 * w + g) * 256 + c * 16, O_H + (g & 1) * 114688 + hc * 32 + c * 16 + (g >> 1) * 8, lane == 0 ? na(nf, N_PG) + j * 128 + w * 4 : OOB,
                 (unsigned)(L_ST + ((lane & 1) * 16 + ((lane & 31) >> 1)) * 16), L_ST + lane16 + w * 4, unit_tab(lane), lane16};
    const v16u s0{wlo, whi, RSZ, 0x20000u, wlo, whi, 0u, 0x20000u, OOB, OOB, hc < 3584 ? 0u : OOB, hc + 8 < 3584 ? 0u : OOB, 0u, O_XM + xr, 65536 + xr, 0u};
    const v2u s1{0u, O_PROF + (blockIdx.x * 4 + w) * 8192};
    asm volatile(G_LOOP :: "{v[0:7]}"(pv), "{s[8:23]}"(s0), "{s[24:25]}"(s1) : "memory", "s99", "vcc");
}
DEVI void role_DN(const Args& a, int w, int p, int cg) {
    asm volatile("; MARK role_DN"); __builtin_amdgcn_s_waitcnt(0);
    using namespace lg;
    const int lane = lane_id();
    const rsrc_t rs = mkrs(a.ws, WS_NEED);
    const unsigned lane16 = lane * 16;
    const int F0 = p < 3 ? 23 * p : (p == 3 ? 68 : 89), zb = p < 2 ? -1 : (p == 4 ? 0 : 22);
    {
        const unsigned ta = w * 18432 + (lane >> 4) * 512 + ((lane >> 2) & 3) * 64 + (lane & 3) * 16;
        const rsrc_t r1 = mkrs(a.Wd, WSZ(3584u * 2048));
        const unsigned v1 = (F0 * 32 + (lane >> 2)) * 2048 + cg * 512 + w * 128 + (lane & 3) * 16;
        load_blocks<46, 9, 2>([&](auto B, auto S) { dma_blk(w * 18432 + S.v * 2048, r1, (B.v >> 1) == zb ? OOB : v1, (B.v >> 1) * 65536 + (B.v & 1) * 64, 2048); },
                              [&](auto B, auto S) { constexpr int ks = B.v >> 1, t = 2 * (B.v & 1), o = S.v * 2048;
                                                    put_frag<WMD, t, ks, o, o + 256>(ta, 0, nullptr); put_frag<WMD, t + 1, ks, o + 8, o + 264>(ta, 0, nullptr); });
        TSW(120 - 65536, 0);
        BAR;
    }
    const int g0 = F0 / 3 * 4, gn = ((F0 + 22) / 3 + 1) * 4 - g0;
    const unsigned nf = side(rs, FSIDE);
    const unsigned wlo = (unsigned)(uintptr_t)a.ws, whi = (unsigned)((uintptr_t)a.ws >> 32);
    const v8u pv{lane16, na(nf, N_PG) + g0 * 32 + (lane < gn ? (lane >> 2) * 128 + (lane & 3) * 4 : 0), lane == 0 ? na(nf ^ PDX, N_PD) + (cg * NKF + p) * 128 + w * 4 : OOB,
                 unit_tab(lane), lane16, (unsigned)(L_OK + lane * 4), (unsigned)((lane & 8) * 2048 + (lane >> 4) * 128 + (lane & 7) * 16), 0u};
    const v16u s0{wlo, whi, RSZ, 0x20000u, wlo, whi, 0u, 0x20000u, wlo, whi, w == 0 ? RSZ : 0u, 0x20000u, 0u, O_H + (w >> 1) * 114688 + (F0 + (w & 1) * 12) * 1024,
                  O_D + p * 65536 + (cg * 8 + w * 2) * 512, (unsigned)(65536 + (w >> 1) * 32768 + (w & 1) * 12288)};
    const v4u s1{(unsigned)(L_OK + w * 256), 0u, 0u, O_PROF + (blockIdx.x * 4 + w) * 8192};
    asm volatile(D_LOOP :: "{v[0:7]}"(pv), "{s[8:23]}"(s0), "{s[24:27]}"(s1) : "memory", "s99", "vcc");
}
template <int R> DEVI void ldv1(rsrc_t rs, unsigned vo) { asm volatile("buffer_load_dword v[%2], %0, %1, 0 offen sc1" :: "v"(vo), "s"(rs), "n"(R) : "memory"); }
template <int R, int I> DEVI void unp2(f32x4& d) { float x, y; asm volatile("v_lshlrev_b32 %0, 16, v[%2]\n\tv_and_b32 %1, 0xffff0000, v[%2]" : "=v"(x), "=v"(y) : "n"(R)); d[I] = x; d[I + 1] = y; }
DEVI void role_RED(const Args& a, int w, int cu) {
    asm volatile("; MARK role_RED"); __builtin_amdgcn_s_waitcnt(0);
    const int lane0 = lane_id(), c = lane0 & 15, g = lane0 >> 4;
    const rsrc_t rs = mkrs(a.ws, WS_NEED);
    const unsigned nf = side(rs, FSIDE);
    v4u sel[2];
#pragma unroll
    for (int e = 0; e < 2; ++e)
#pragma unroll
        for (int i = 0; i < 4; ++i) sel[e][i] = (g == (c >> 2) && i == 2 * e + ((c >> 1) & 1)) ? ((c & 1) ? 0x3F800000u : 0x3F80u) : 0u;
    const unsigned vfl = na(nf ^ PDX, N_PD) + w * (NKF * 128) + (lane0 < 20 ? (lane0 >> 2) * 128 + (lane0 & 3) * 4 : 0);
    constexpr int VP = NV + 32;
    const unsigned lo = (c & 8) * 128 + g * 256 + (c & 7) * 16 + (cu & 1) * 128 + w * 8192, ld = (c & 8) * 64 + g * 128 + (c & 7) * 16 + (cu & 1) * 16384 + w * 4096;
    const unsigned lout = ((cu & 1) * 8 + (c & 7)) * 2048 + w * 512 + (c & 8) * 8 + g * 16, lw = g == (c >> 2) ? (c & 7) * 64 + w * 16 + (c & 8) : 1024 + lane0 * 8;
    const rsrc_t ro = mkrs(a.out, 4u << 20);
    v4u one{0x3F803F80u, 0x3F803F80u, 0x3F803F80u, 0x3F803F80u};
    asm volatile("" : "+v"(one));
    auto issue = [&](auto B, auto N0, auto N1, int k) {
        const unsigned ls = k >> 2, base = ((k >> 1) & 1) * 32768;
        sfor<N0.v, N1.v>([&](auto N) { constexpr int f = (N.v - 4) / 5, p = (N.v - 4) % 5;
            if constexpr (N.v < 4) ldv4<NV + 16 * B.v + 4 * N.v>(rs, lo, O_XM + ls * 65536 + base + N.v * 2048);
            else lda<80 * B.v + 20 * f + 4 * p, 4, f * 1024>(rs, ld, O_D + (ls * NKF + p) * 65536 + base); });
        if constexpr (N1.v == 24) ldv1<VP>(rs, vfl);
    };
    *LDSP(unsigned char, 2048 + unit_ls(2 * lane0)) = 2 * lane0; *LDSP(unsigned char, 2048 + unit_ls(2 * lane0 + 1)) = 2 * lane0 + 1;
    WL0; BAR;
    const unsigned tab = opq(*LDSP(const unsigned short, 2048 + 2 * lane0));
    auto next = [&](int k) { const int n = k + (k >= RSH ? 6 : k < 256 ? 4 : 2); return k < RSH && n >= (k < 256 ? 256 : RSH) ? RSH + cu : n; };
    u64 late = 1, far = 1;
    bool pre = false;
    auto task = [&](auto B, int k) {
        constexpr int b = B.v;
        const int u = unit_get(tab, k >> 2), rt = (k >> 1) & 1, kn = next(k), nx = kn < 4 * NU ? unit_get(tab, kn >> 2) + 1 : NU + 1;
        const unsigned ls = k >> 2, tile = ls * 2 + rt;
        TSD; TSR(0);
        if (!pre) {
            if (late) {
                unsigned spins = 0;
#pragma nounroll
                for (;;) {
                    if (far) __builtin_amdgcn_s_sleep(PSF); else __builtin_amdgcn_s_sleep(PSR);
                    CB;
                    const int v = (int)ld4(rs, vfl, 0);
                    if (__builtin_amdgcn_sicmp(v, u + 1, 40) == 0) break;
                    far = __builtin_amdgcn_sicmp(v, u, 40);
                    spin_bail(spins);
                }
            }
            TSR(1);
            issue(B, IC<0>{}, IC<24>{}, k);
        }
        else { TSR(1); }
        const bool l0 = ls < 64;
        const rsrc_t rd = l0 ? rs : ro;
        const unsigned vd = opq((l0 ? O_XIN + XB + lo : lout - 64 * 65536) + tile * 32768), fs = l0 ? 2048 : 128;
        v4u x[4];
        f32x4 d[4][2], S, Q;
        auto unp = [&](auto F, auto I) { unp2<NV + 16 * b + 4 * F.v + I.v, 2 * (I.v & 1)>(d[F.v][I.v >> 1]); };
        auto fin = [&](auto F) {
            constexpr int f = F.v;
            fence(d[f][0], d[f][1]);
#pragma unroll
            for (int i = 0; i < 4; ++i) x[f][i] = pack2(d[f][i >> 1][2 * (i & 1)], d[f][i >> 1][2 * (i & 1) + 1]);
            asm volatile("" : "+v"(x[f]));
        };
        auto put = [&](auto F) { constexpr int f = F.v; st16s(x[f], rd, vd, f * fs); SB; mfv<f == 0>(S, one, x[f]); mfv<f == 0>(Q, x[f], x[f]); SB; };
        wvm<24>();
        sfor<0, 4>([&](auto I) { unp(IC<0>{}, I); });
        sfor<0, 4>([&](auto F) {
            constexpr int f = F.v;
            wvm<16 - 5 * f + (f ? f - 1 : 0)>();
            nop<1>();
            sfor<0, 10>([&](auto N) {
                mfB<80 * b + 20 * f + 4 * (N.v >> 1), false>(d[f][N.v & 1], sel[N.v & 1]);
                if constexpr (N.v < 4 && f < 3) unp(IC<(f + 1) & 3>{}, N);
                if constexpr (N.v == 5 && f > 0) fin(IC<(f + 3) & 3>{});
                SB;
            });
            if constexpr (f > 0) put(IC<(f + 3) & 3>{});
        });
        wvm<3>();
        TSR(2);
#ifdef TS
        tsr[4] = pre + 1;
#endif
        const bool p1 = ltv<VP>(nx) == 0;
        if (!p1) ldv1<VP + 1>(rs, vfl);
        nop<3>();
        fin(IC<3>{});
        nop<1>();
        put(IC<3>{});
        auto second = [&](auto NS) {
            wvm<NS.v>();
            pre = ltv<VP + 1>(nx) == 0;
            if (pre) { issue(IC<1 - b>{}, IC<0>{}, IC<24>{}, kn); if constexpr (NS.v == 5) wvm<25>(); }
            else {
                if constexpr (NS.v == 5) { ldv1<VP + 1>(rs, vfl); WV0; }
                late = ltv<VP + 1>(nx); far = ltv<VP + 1>(nx - 1);
            }
        };
        if (l0) {
            nop<7>();
            fence(S, Q);
            const float qd = selm(selm(Q[0], Q[1], 0xAAAAAAAAAAAAAAAAull), selm(Q[2], Q[3], 0xAAAAAAAAAAAAAAAAull), 0xCCCCCCCCCCCCCCCCull);
            *LDSP(f32x2, lw) = f32x2{S[0], qd};
            if (p1) issue(IC<1 - b>{}, IC<0>{}, IC<RPA>{}, kn);
            f32x2 xf[4][4];
#pragma unroll
            for (int f = 0; f < 4; ++f)
#pragma unroll
                for (int i = 0; i < 4; ++i) { xf[f][i] = f32x2{bflo(x[f][i]), bfhi(x[f][i])}; asm volatile("" : "+v"(xf[f][i])); }
            TSR(5);
            WL0; BAR;
            f32x4 t[4];
#pragma unroll
            for (int i = 0; i < 4; ++i) t[i] = __builtin_bit_cast(f32x4, ldsq((c & 7) * 64, i * 16));
            const f32x4 t4 = (t[0] + t[1]) + (t[2] + t[3]);
            const float mean = (t4[0] + t4[2]) * (1.0f / 1024.0f), var = fmaxf(rbf(fmaf(t4[1] + t4[3], 1.0f / 1024.0f, -mean * mean)), 0.f);
            const float inv = __builtin_amdgcn_rcpf(rbf(__builtin_amdgcn_sqrtf(rbf(var + 1e-5f)))), mi = -rbf(mean) * inv;
#pragma unroll
            for (int f = 0; f < 4; ++f) {
                v4u o;
#pragma unroll
                for (int i = 0; i < 4; ++i) xf[f][i] = __builtin_elementwise_fma(xf[f][i], f32x2{inv, inv}, f32x2{mi, mi});
#pragma unroll
                for (int i = 0; i < 4; ++i) o[i] = pack2(xf[f][i][0], xf[f][i][1]);
                st16(o, rs, vd + (O_XN - O_XIN) + f * 2048);
            }
            TSR(6);
            if (p1) { issue(IC<1 - b>{}, IC<RPA>{}, IC<24>{}, kn); TSR(7); wvm<25 - RPA>(); pre = true; } else second(IC<5>{});
            BAR;
            st4(0u, rs, lane0 < 2 && w == 0 ? na(nf ^ (FSIDE ? 0 : 8192), N_FX) + tile * 16 + (cu & 1) * 8 + lane0 * 4 : OOB, 0);
        }
        else if (p1) { issue(IC<1 - b>{}, IC<0>{}, IC<24>{}, kn); pre = true; }
        else second(IC<1>{});
        TSR(3); TSF(u, 0xFF);
    };
#pragma nounroll
    for (int k = cu < 4 ? cu : 252 + cu;;) {
        task(IC<0>{}, k); k = next(k);
        if (k >= 4 * NU) break;
        task(IC<1>{}, k); k = next(k);
        if (k >= 4 * NU) break;
    }
}
#ifndef MAP6
#define MAP6 0
#endif
#ifndef OFFM
#define OFFM 0
#endif
#ifndef LSW
#define LSW 0
#endif
#ifndef PQ
#define PQ 1
#endif
#ifndef XFM
#define XFM 0
#endif
#ifndef DLY_A
#define DLY_A 150
#endif
#ifndef DLY_B
#define DLY_B 0
#endif
#ifndef DLY_1
#define DLY_1 1800
#endif
#ifndef DLY_G
#define DLY_G 1200
#endif
#ifndef DLY_D
#define DLY_D 1600
#endif
__global__ __launch_bounds__(256, 1) __attribute__((amdgpu_num_vgpr(NV))) void mega6(Args a) {
    asm volatile("" ::: "v255", "a255");
    const int w = __builtin_amdgcn_readfirstlane(threadIdx.x >> 6), b = blockIdx.x, xcd = b & 7, slot = b >> 3, l = (xcd >> 2) ^ LSW, idx = (slot & 7) * 4 + (xcd & 3), f = slot - 24;
    const int t0 = (int)__builtin_amdgcn_s_memrealtime(), xf = XFM ? 7 - xcd : xcd;
    TSW(127, 0);
#if MAP6
    {
        const int odd = (xcd ^ MAP6 ^ 1) & 1, e4 = xcd >> 1, hd = (e4 & 1) * 16 + (slot & 15), j = slot - 16;
        if (l && (!odd || (j < 0 && hd >= LN_NOW))) pull_all(a, w, !odd ? (slot >> 4) * 32 + hd : 64 + hd - LN_NOW, t0);
        if (PQ && odd && j >= 8) pull(a.Wq + (1 << 20), 6144, ((j - 8) * 4 + e4) * 4 + w, 128);
        if (!odd && l && slot < 3) { const int q = (slot * 2 + (e4 & 1)) * 4 + w; use(ld4(mkrs(a.ws, WS_NEED), q < 2 * NVK + 2 ? prb(q >> 1, q & 1) + lane_id() * 32 : OOB, 0)); }
        if (!odd && slot < 16) { hold(t0, l ? DLY_1 : DLY_A); role_A(a, w, l, hd, (e4 & 1) * 4 + (slot >> 3)); }
        else if (!odd) { hold(t0, l ? DLY_1 : DLY_B); role_B(a, w, l, hd, (e4 & 1) * 4 + (slot >> 3)); }
        else if (j < 0) role_R(a, w, l, hd, t0);
        else if (j < 5) {
            if (j < 4) vote(mkrs(a.ws, WS_NEED), w, e4 * 4 + j, l, t0);
            hold(t0, DLY_D); role_DN(a, w, j < 4 ? e4 : 4, j < 4 ? j : e4);
        }
        else {
            const int k = e4 * 11 + j - 5 - (e4 * 3 + 1) / 2, r = j - 14 - (e4 & 1);
            if (r < 0) { hold(t0, DLY_G); role_GU(a, w, k + (e4 & 1 ? 0 : 0)); }
            else { const int cu = (e4 * 3 + 1) / 2 + r; hold(t0, DLY_D); role_RED(a, w, cu); if (cu * 4 + w == 0) vote_end(mkrs(a.ws, WS_NEED)); }
        }
        return;
    }
#endif
    if (slot < 24 && l && (slot < 16 || idx >= LN_NOW)) pull_all(a, w, (slot >> 3) * 32 + idx - (slot < 16 ? 0 : LN_NOW), t0);
    if (PQ && f >= 0 && f < 4) pull(a.Wq + (1 << 20), 6144, (f * 8 + xcd) * 4 + w, 128);
    if (slot < 8) { hold(t0, l ? DLY_1 : DLY_A); role_A(a, w, l, idx, (xcd & 3) * 2); }
    else if (slot < 16) { hold(t0, l ? DLY_1 : DLY_B); role_B(a, w, l, idx, (xcd & 3) * 2 + 1); }
    else if (slot < 24) role_R(a, w, l, idx, t0);
    else if (f < (xf < 3 ? 6 : 4)) { hold(t0, DLY_G); if (!(OFFM & 1)) role_GU(a, w, xf < 3 ? 6 * xf + f : 6 + 4 * xf + f); }
    else if (xf < 3) {
        const int cu = xf + 3 * (f - 6), q = cu * 4 + w;
        use(ld4(mkrs(a.ws, WS_NEED), q < 2 * NVK + 2 ? prb(q >> 1, q & 1) + lane_id() * 32 : OOB, 0));
        hold(t0, DLY_D); if (!(OFFM & 4)) role_RED(a, w, cu);
        if (q == 0 && !(OFFM & 4)) vote_end(mkrs(a.ws, WS_NEED));
    }
    else {
        if (xf < 7) { vote(mkrs(a.ws, WS_NEED), w, (xf - 3) * 4 + f - 4, l, t0); }
        hold(t0, DLY_D); if (!(OFFM & 2)) role_DN(a, w, xf - 3, f - 4); }
}
extern "C" void kernel_launch(void* const* d_in, const int* in_sizes, int n_in, void* d_out, int out_size, void* d_ws, size_t ws_size, hipStream_t stream) {
    (void)in_sizes; (void)n_in; (void)out_size; (void)ws_size;
    (void)hipFuncSetAttribute((const void*)mega6, hipFuncAttributeMaxDynamicSharedMemorySize, 163840);
    d_ws = (void*)(((uintptr_t)d_ws + (2u << 20) - 1) & ~(uintptr_t)((2u << 20) - 1));
    Args a{(const u16*)d_in[0], (const u16*)d_in[1], (const u16*)d_in[2], (const u16*)d_in[3], (const u16*)d_in[4], (const u16*)d_in[5],
           (const u16*)d_in[8], (const u16*)d_in[9], (const u16*)d_in[10], (char*)d_ws, (u16*)d_out};
    mega6<<<256, 256, 163840, stream>>>(a);
}
```

```cpp
#include <hip/hip_runtime.h>
#include <stdint.h>

typedef unsigned short u16;
typedef unsigned long long u64;
typedef __attribute__((ext_vector_type(8))) __bf16 bf8;
typedef __attribute__((ext_vector_type(16))) __bf16 bf16v;
typedef __attribute__((ext_vector_type(4))) float f32x4;
typedef __attribute__((ext_vector_type(16))) float f32x16;
typedef __attribute__((ext_vector_type(2))) float f32x2;
typedef __attribute__((ext_vector_type(8))) unsigned v8u;
typedef __attribute__((ext_vector_type(4))) unsigned v4u;
typedef __attribute__((ext_vector_type(3))) unsigned v3u;
typedef __attribute__((ext_vector_type(2))) unsigned v2u;
typedef __amdgpu_buffer_rsrc_t rsrc_t;
typedef __attribute__((address_space(3))) char lds_char;
#define DEVI __device__ __forceinline__
#define LDSP(T, p) ((__attribute__((address_space(3))) T*)(p))

typedef __attribute__((ext_vector_type(2))) __bf16 bf16x2_t;
DEVI unsigned pack2(float lo, float hi) { f32x2 v = {lo, hi}; bf16x2_t b = __builtin_convertvector(v, bf16x2_t); return __builtin_bit_cast(unsigned, b); }
DEVI float bflo(unsigned u) { return __uint_as_float(u << 16); }
DEVI float bfhi(unsigned u) { return __uint_as_float(u & 0xffff0000u); }
DEVI float rbf(float x) { return bflo(pack2(x, x)); }
constexpr int NV = 112;
#define MR_(R, A_STR, V_STR, OUTS, INS, W) do { if constexpr ((R) < 256) asm volatile(A_STR : OUTS : INS, [lo] "n"(R), [hi] "n"((R) + (W) - 1)); \
                                                else asm volatile(V_STR : OUTS : INS, [lo] "n"((R) - 256 + NV), [hi] "n"((R) - 256 + NV + (W) - 1)); } while (0)
#define CM_ ,
template <int R> DEVI void mwr(unsigned x) { if constexpr (R < 256) asm volatile("v_accvgpr_write_b32 a[%1], %0" :: "v"(x), "n"(R)); else asm volatile("v_mov_b32 v[%1], %0" :: "v"(x), "n"(R - 256 + NV)); }
template <int R> DEVI void mwr4(v4u x) { mwr<R>(x[0]); mwr<R + 1>(x[1]); mwr<R + 2>(x[2]); mwr<R + 3>(x[3]); }
#define M16 "v_mfma_f32_16x16x32_bf16 "
#define M32 "v_mfma_f32_32x32x16_bf16 "
#define S16 "v_smfmac_f32_16x16x64_bf16 "
template <int R, bool Z> DEVI void mfA(f32x4& d, v4u b) {
    if constexpr (Z) MR_(R, M16 "%[d], a[%[lo]:%[hi]], %[b], 0", M16 "%[d], v[%[lo]:%[hi]], %[b], 0", [d] "=&v"(d), [b] "v"(b), 4);
    else MR_(R, M16 "%[d], a[%[lo]:%[hi]], %[b], %[d]", M16 "%[d], v[%[lo]:%[hi]], %[b], %[d]", [d] "+v"(d), [b] "v"(b), 4);
}
template <int R, bool Z> DEVI void mfB(f32x4& d, v4u a) {
    if constexpr (Z) MR_(R, M16 "%[d], %[a], a[%[lo]:%[hi]], 0", M16 "%[d], %[a], v[%[lo]:%[hi]], 0", [d] "=&v"(d), [a] "v"(a), 4);
    else MR_(R, M16 "%[d], %[a], a[%[lo]:%[hi]], %[d]", M16 "%[d], %[a], v[%[lo]:%[hi]], %[d]", [d] "+v"(d), [a] "v"(a), 4);
}
template <int R, bool Z> DEVI void mf32A(f32x16& d, v4u b) {
    if constexpr (Z) MR_(R, M32 "%[d], a[%[lo]:%[hi]], %[b], 0", M32 "%[d], v[%[lo]:%[hi]], %[b], 0", [d] "=&v"(d), [b] "v"(b), 4);
    else MR_(R, M32 "%[d], a[%[lo]:%[hi]], %[b], %[d]", M32 "%[d], v[%[lo]:%[hi]], %[b], %[d]", [d] "+v"(d), [b] "v"(b), 4);
}
template <bool Z> DEVI void mfv(f32x4& d, v4u a, v4u b) {
    if constexpr (Z) asm volatile(M16 "%0, %1, %2, 0" : "=&v"(d) : "v"(a), "v"(b)); else asm volatile(M16 "%0, %1, %2, %0" : "+v"(d) : "v"(a), "v"(b));
}
template <int R> DEVI void smf(f32x4& d, v4u a, int idx) { MR_(R, S16 "%[d], %[a], a[%[lo]:%[hi]], %[i]", S16 "%[d], %[a], v[%[lo]:%[hi]], %[i]", [d] "+v"(d), [a] "v"(a) CM_ [i] "v"(idx), 8); }
DEVI void smfv(f32x4& d, v4u a, v8u b, int idx) { asm volatile(S16 "%0, %1, %2, %3" : "+v"(d) : "v"(a), "v"(b), "v"(idx)); }
DEVI void zero4(f32x4& d) { d = f32x4{0.f, 0.f, 0.f, 0.f}; asm volatile("" : "+v"(d)); }
template <int N> DEVI void nop() { asm volatile("s_nop %0" :: "n"(N)); }
DEVI void fence(f32x4& d) { asm volatile("" : "+v"(d)); }
template <class... T> DEVI void fence(f32x4& d, T&... r) { fence(d); fence(r...); }
DEVI float selm(float a, float b, u64 m) { float r; asm("v_cndmask_b32 %0, %1, %2, %3" : "=v"(r) : "v"(a), "v"(b), "s"(m)); return r; }
DEVI int opqs(int x) { asm volatile("" : "+s"(x)); return x; }
DEVI v4u lo4(v8u v) { return __builtin_shufflevector(v, v, 0, 1, 2, 3); }
DEVI v4u hi4(v8u v) { return __builtin_shufflevector(v, v, 4, 5, 6, 7); }
DEVI v8u cat8(v4u a, v4u b) { return __builtin_shufflevector(a, b, 0, 1, 2, 3, 4, 5, 6, 7); }
constexpr int AUX_SC1 = 16;
DEVI v4u ld16(rsrc_t r, unsigned vo, unsigned so) { return __builtin_amdgcn_raw_buffer_load_b128(r, vo, so, AUX_SC1); }
DEVI v2u ld8(rsrc_t r, unsigned vo, unsigned so) { return __builtin_amdgcn_raw_buffer_load_b64(r, vo, so, AUX_SC1); }
DEVI unsigned ld4(rsrc_t r, unsigned vo, unsigned so) { return __builtin_amdgcn_raw_buffer_load_b32(r, vo, so, AUX_SC1); }
DEVI void st16(v4u v, rsrc_t r, unsigned vo) { __builtin_amdgcn_raw_buffer_store_b128(v, r, vo, 0, AUX_SC1); }
DEVI void st16s(v4u v, rsrc_t r, unsigned vo, unsigned so) { __builtin_amdgcn_raw_buffer_store_b128(v, r, vo, so, AUX_SC1); }
DEVI void st8(v2u v, rsrc_t r, unsigned vo, unsigned so) { __builtin_amdgcn_raw_buffer_store_b64(v, r, vo, so, AUX_SC1); }
DEVI void st4(unsigned v, rsrc_t r, unsigned vo, unsigned so) { __builtin_amdgcn_raw_buffer_store_b32(v, r, vo, so, AUX_SC1); }
#define CB asm volatile("" ::: "memory")
#define WL0 asm volatile("s_waitcnt lgkmcnt(0)" ::: "memory")
#define WV0 asm volatile("s_waitcnt vmcnt(0)" ::: "memory")
#define BAR do { __builtin_amdgcn_s_barrier(); CB; } while (0)
#define SB __builtin_amdgcn_sched_barrier(0)
DEVI float fadd(float a, float b) { float r; asm("v_add_f32 %0, %1, %2" : "=v"(r) : "v"(a), "v"(b)); return r; }
DEVI float vfma(float a, float b, float c) { float r; asm("v_fma_f32 %0, %1, %2, %3" : "=v"(r) : "v"(a), "v"(b), "v"(c)); return r; }
DEVI unsigned vxor(unsigned a, unsigned b) { unsigned r; asm volatile("v_xor_b32 %0, %1, %2" : "=v"(r) : "i"(b), "v"(a)); return r; }
DEVI int lane_id() { int r; asm volatile("v_mbcnt_lo_u32_b32 %0, -1, 0\n\tv_mbcnt_hi_u32_b32 %0, -1, %0" : "=v"(r)); __builtin_assume(r >= 0 && r < 64); return r; }
DEVI int opq(int x) { asm volatile("" : "+v"(x)); return x; }
DEVI float vmaxf(float a, float b) { float r; asm("v_max_f32 %0, %1, %2" : "=v"(r) : "v"(a), "v"(b)); return r; }
DEVI void swap32(float& a, float& b) { auto r = __builtin_amdgcn_permlane32_swap(__float_as_uint(a), __float_as_uint(b), false, false); a = __uint_as_float(r[0]); b = __uint_as_float(r[1]); }
DEVI void swap32u(unsigned& a, unsigned& b) { auto r = __builtin_amdgcn_permlane32_swap(a, b, false, false); a = r[0]; b = r[1]; }
DEVI void swap16u(unsigned& a, unsigned& b) { auto r = __builtin_amdgcn_permlane16_swap(a, b, false, false); a = r[0]; b = r[1]; }
DEVI float hadd(float a, float b) { swap32(a, b); return fadd(a, b); }
DEVI float red32(float x) { float y = x; swap32(x, y); return x + y; }
DEVI float red16(float x) { unsigned a = __float_as_uint(x), b = a; swap16u(a, b); return __uint_as_float(a) + __uint_as_float(b); }
DEVI float mx32(float x) { float y = x; swap32(x, y); return vmaxf(x, y); }
DEVI float mx16(float x) { unsigned a = __float_as_uint(x), b = a; swap16u(a, b); return vmaxf(__uint_as_float(a), __uint_as_float(b)); }
template <int N> struct IC { static constexpr int v = N; };
template <int I, int N, class F> DEVI void sfor(F&& f) { if constexpr (I < N) { f(IC<I>{}); sfor<I + 1, N>(f); } }
#ifndef NSEG
#define NSEG 64
#endif
#ifndef LAGD
#define LAGD 10
#endif
constexpr float SC_L2E = 0.08838834764831845f * 1.4426950408889634f;
constexpr unsigned SENT = 0xFFFFFFFFu;
#ifndef NUS_
#define NUS_ 8
#endif
#ifndef NRG_
#define NRG_ 8
#endif
#ifndef NC
#define NC 8
#endif
constexpr int NREP = 8, NUS = NUS_, NRG = NRG_, NKF = 5, NGU = 38;
constexpr unsigned XB = 4u << 20;
constexpr unsigned O_XIN = 0;
constexpr unsigned O_XN  = O_XIN + 2 * XB;
constexpr unsigned O_XM  = O_XN + 2 * XB;
constexpr unsigned O_H   = O_XM + 2 * XB;
constexpr unsigned O_D   = O_H + 2u * 64 * 224 * 1024;
constexpr unsigned O_N = O_D + 2u * 64 * NKF * 65536;
constexpr unsigned DV(unsigned o) { return ((o & ~8191u) << 1) | (o & 8191u); }
DEVI unsigned na(unsigned nb, unsigned o) { return (DV(o) + nb) ^ ((o >> 7) & 8192u); }
constexpr unsigned N_PM  = 0;
constexpr unsigned N_PMM = N_PM + NRG * 16384;
constexpr unsigned N_PA  = (1u << 20) - 16384;
constexpr unsigned N_MEM = 1u << 20;
constexpr unsigned N_WM  = 2u << 20;
constexpr unsigned N_PTX = 6u << 20;
constexpr unsigned N_U   = 10u << 20;
static_assert(NRG == 8 && NUS == 8 && NREP == 8 && O_N % (4u << 20) == 0, "");
constexpr unsigned O_FLG_END = O_N + DV(N_MEM + NREP * 16384);
constexpr unsigned N_FX  = N_PA - 8192;
constexpr unsigned N_PG  = N_FX - 8192;
constexpr unsigned N_PD  = N_PG + 40 * 128;
constexpr unsigned O_PRB = O_N + (512u << 10), O_CAL = O_N + (768u << 10);
constexpr unsigned O_SX  = O_N + DV(N_PG) - 2u * 64 * 32 * 256;
constexpr unsigned O_FLG = O_SX;
static_assert(O_PRB >= O_N + DV(N_PMM + NRG * 8192) && O_FLG >= O_CAL + 8192 && (O_FLG_END - O_FLG) % 65536 == 0, "");
constexpr unsigned OOB = 0x80000000u;
constexpr unsigned WS_NEED = O_N + DV(N_U + NUS * (2u << 20));
constexpr unsigned O_PROF = 240u << 20;
static_assert(WS_NEED <= (200u << 20), "workspace too large");

#ifdef DRY
#define WSZ(x) 0u
#else
#define WSZ(x) (x)
#endif
struct Args { const u16 *x, *mem0, *Wq, *Wk, *Wv, *Wo, *Wg, *Wu, *Wd; char* ws; u16* out; };
#ifndef SPIN_LOG
#define SPIN_LOG 18
#endif
constexpr unsigned SPIN_CAP = 1u << SPIN_LOG;
DEVI void spin_bail(unsigned& spins) {
    asm volatile("s_add_u32 %0, %0, 1\n\ts_cmp_lt_u32 %0, %1\n\ts_cbranch_scc1 1f\n\ts_endpgm\n1:" : "+s"(spins) : "n"(SPIN_CAP) : "scc");
}
template <int K> DEVI void wrl(unsigned& v, u64& t) { asm volatile("" : "+s"(t)); asm volatile("v_writelane_b32 %0, %1, %2" : "+v"(v) : "s"((unsigned)t), "n"(K)); }
#ifdef TS
#ifndef TSCLK
#define TSCLK __builtin_amdgcn_s_memrealtime
#endif
#define TSV(u, i, v) __builtin_amdgcn_raw_buffer_store_b32((unsigned)(v), mkrs(a.ws, 256u << 20), lane_id() == 0 ? O_PROF + ((blockIdx.x * 4 + w) * 128 + (u)) * 64 + (i) * 4 : OOB, 0, 0)
#define TSW(u, i) TSV(u, i, TSCLK())
#define TSC(u, i) TSV(u, i, __builtin_amdgcn_s_memtime())
#define TSD u64 tsr[16] = {}
#define TSR(k) tsr[k] = rclk()
#define TSK(k) asm volatile("s_memtime %0" : "=s"(tsr[k]))
#define TSF(u, m) do { unsigned tv_ = 0; WL0; sfor<0, 16>([&](auto K) { if constexpr (((m) >> K.v) & 1) wrl<K.v>(tv_, tsr[K.v]); }); \
    const int tl_ = lane_id(); __builtin_amdgcn_raw_buffer_store_b32(tv_, mkrs(a.ws, 256u << 20), (tl_ < 16 && (((m) >> tl_) & 1)) ? O_PROF + ((blockIdx.x * 4 + w) * 128 + (u)) * 64 + tl_ * 4 : OOB, 0, 0); } while (0)
#if TS == 4
#undef TSV
#undef TSR
#undef TSK
#define TSV(u, i, v)
#define TSR(k)
#define TSK(k)
#define FSK(G, O, D) if constexpr ((G) >= (O) && ((G) - (O)) % (D) == 0 && ((G) - (O)) / (D) < FN) asm volatile("s_memtime %0" : "=s"(tsr[((G) - (O)) / (D)]))
#define FSF(u) TSF(u, (1 << FN) - 1)
#define FSD TSD
#endif
#else
#define TSW(u, i)
#define TSC(u, i)
#define TSV(u, i, v)
#define TSD
#define TSR(k)
#define TSK(k)
#define TSF(u, m)
#endif
#if TS != 4
#define FSK(G, O, D)
#define FSF(u)
#define FSD
#endif
DEVI unsigned rslot(int s) { return (unsigned)(s & (NRG - 1)); }
DEVI u64 pend(v4u d) { return __builtin_amdgcn_uicmp(max(d[0], d[2]), SENT, 32); }
DEVI u64 pend2(v2u d) { return __builtin_amdgcn_uicmp(d[0], SENT, 32); }
DEVI u64 pend1(unsigned d) { return __builtin_amdgcn_uicmp(d, SENT, 32); }
DEVI unsigned nosent(unsigned u) { return min(u, 0xFFFFFFFEu); }
#ifndef WPOL
#define WPOL ""
#endif
template <int OFF, bool SC1> DEVI void dma1(rsrc_t rs, unsigned vo, unsigned so) {
    if constexpr (SC1) asm volatile("buffer_load_dwordx4 %0, %1, %2 offen offset:%3 sc1 lds" :: "v"(vo), "s"(rs), "s"(so), "n"(OFF) : "memory");
    else asm volatile("buffer_load_dwordx4 %0, %1, %2 offen offset:%3 " WPOL " lds" :: "v"(vo), "s"(rs), "s"(so), "n"(OFF) : "memory");
}
DEVI void setm0(unsigned v) { asm volatile("s_mov_b32 m0, %0\n\ts_nop 0" :: "s"(v) : "memory"); }
DEVI void dma4(unsigned lds, rsrc_t rs, unsigned vo, unsigned so) { setm0(lds); dma1<0, true>(rs, vo, so); dma1<1024, true>(rs, vo, so); dma1<2048, true>(rs, vo, so); dma1<3072, true>(rs, vo, so); }
DEVI void dmaw(unsigned lds, rsrc_t rs, unsigned vo, unsigned so) { setm0(lds); asm volatile("buffer_load_dword %0, %1, %2 offen sc1 lds" :: "v"(vo), "s"(rs), "s"(so) : "memory"); }
template <int N> DEVI void wvm() { asm volatile("s_waitcnt vmcnt(%0)" :: "n"(N) : "memory"); }
template <int N> DEVI void wlg() { asm volatile("s_waitcnt lgkmcnt(%0)" :: "n"(N) : "memory"); }
DEVI void dma4p(u64 c0, u64 c1, u64 c2, u64 c3, rsrc_t rs, unsigned vo, unsigned so) {
    asm volatile(
        "s_cmp_eq_u64 %[c0], 0\n\ts_cbranch_scc1 1f\n\t"
        "buffer_load_dwordx4 %[vo], %[rs], %[so] offen sc1 lds\n"
        "1:\n\ts_cmp_eq_u64 %[c1], 0\n\ts_cbranch_scc1 2f\n\t"
        "buffer_load_dwordx4 %[vo], %[rs], %[so] offen offset:1024 sc1 lds\n"
        "2:\n\ts_cmp_eq_u64 %[c2], 0\n\ts_cbranch_scc1 3f\n\t"
        "buffer_load_dwordx4 %[vo], %[rs], %[so] offen offset:2048 sc1 lds\n"
        "3:\n\ts_cmp_eq_u64 %[c3], 0\n\ts_cbranch_scc1 4f\n\t"
        "buffer_load_dwordx4 %[vo], %[rs], %[so] offen offset:3072 sc1 lds\n"
        "4:"
        :: [c0] "s"(c0), [c1] "s"(c1), [c2] "s"(c2), [c3] "s"(c3), [vo] "v"(vo), [rs] "s"(rs), [so] "s"(so)
        : "memory", "scc");
}
#ifndef PSL
#define PSL 8
#endif
DEVI void wait_prog(rsrc_t rs, unsigned vo, unsigned so, int need) {
    unsigned spins = 0;
#pragma nounroll
    for (;;) {
        CB;
        const int v = (int)ld4(rs, vo, so);
        if (__builtin_amdgcn_sicmp(v, need, 40) == 0) return;
        spin_bail(spins);
        __builtin_amdgcn_s_sleep(PSL);
    }
}

typedef __attribute__((ext_vector_type(16))) int v16i;
typedef __attribute__((ext_vector_type(16))) unsigned v16u;
#ifdef TS
constexpr unsigned RSZ = 256u << 20;
#else
constexpr unsigned RSZ = WS_NEED;
#endif
DEVI u64 rclk() { u64 t; asm volatile("s_memrealtime %0" : "=s"(t)); return t; }
DEVI unsigned tsv(u64& t) { asm volatile("s_waitcnt lgkmcnt(0)" : "+s"(t)); return (unsigned)t; }
DEVI unsigned stamp(u64& t, int tag, int lead) { asm volatile("" : "+s"(t)); return (unsigned)tag << 24 | (((unsigned)t + lead) & 0xFFFFFFu); }
#ifndef SXTH
#define SXTH 20
#endif
DEVI int left(int t) { return (int)((unsigned)(t - (int)__builtin_amdgcn_s_memrealtime()) << 8); }
DEVI void hold(int t0, int d) { for (int n = 0; n < 4096 && (int)__builtin_amdgcn_s_memrealtime() - t0 < d; ++n) __builtin_amdgcn_s_sleep(4); }
DEVI int smin(int a, int b) { int r; asm("s_min_i32 %0, %1, %2" : "=s"(r) : "s"(a), "s"(b) : "scc"); return r; }
DEVI int smax(int a, int b) { int r; asm("s_max_i32 %0, %1, %2" : "=s"(r) : "s"(a), "s"(b) : "scc"); return r; }
DEVI void smin8x2(const v16i& f, int& m0, int& m1) {
    asm("s_min_i32 %0, %2, %3\n\ts_min_i32 %1, %10, %11\n\ts_min_i32 %0, %0, %4\n\ts_min_i32 %1, %1, %12\n\ts_min_i32 %0, %0, %5\n\ts_min_i32 %1, %1, %13\n\ts_min_i32 %0, %0, %6\n\ts_min_i32 %1, %1, %14\n\t"
        "s_min_i32 %0, %0, %7\n\ts_min_i32 %1, %1, %15\n\ts_min_i32 %0, %0, %8\n\ts_min_i32 %1, %1, %16\n\ts_min_i32 %0, %0, %9\n\ts_min_i32 %1, %1, %17"
        : "=&s"(m0), "=&s"(m1) : "s"(f[0]), "s"(f[1]), "s"(f[2]), "s"(f[3]), "s"(f[4]), "s"(f[5]), "s"(f[6]), "s"(f[7]),
          "s"(f[8]), "s"(f[9]), "s"(f[10]), "s"(f[11]), "s"(f[12]), "s"(f[13]), "s"(f[14]), "s"(f[15]) : "scc");
}
DEVI void smax8x2(const v16i& f, int& m0, int& m1) {
    asm("s_max_i32 %0, %2, %3\n\ts_max_i32 %1, %10, %11\n\ts_max_i32 %0, %0, %4\n\ts_max_i32 %1, %1, %12\n\ts_max_i32 %0, %0, %5\n\ts_max_i32 %1, %1, %13\n\ts_max_i32 %0, %0, %6\n\ts_max_i32 %1, %1, %14\n\t"
        "s_max_i32 %0, %0, %7\n\ts_max_i32 %1, %1, %15\n\ts_max_i32 %0, %0, %8\n\ts_max_i32 %1, %1, %16\n\ts_max_i32 %0, %0, %9\n\ts_max_i32 %1, %1, %17"
        : "=&s"(m0), "=&s"(m1) : "s"(f[0]), "s"(f[1]), "s"(f[2]), "s"(f[3]), "s"(f[4]), "s"(f[5]), "s"(f[6]), "s"(f[7]),
          "s"(f[8]), "s"(f[9]), "s"(f[10]), "s"(f[11]), "s"(f[12]), "s"(f[13]), "s"(f[14]), "s"(f[15]) : "scc");
}
#ifndef NVK
#define NVK 8
#endif
#ifndef DLY_V
#define DLY_V 500
#endif
#ifndef PSV
#define PSV 12
#endif
#ifndef DLY_P
#define DLY_P 150
#endif
DEVI unsigned prb(int k, int b) { return O_PRB + k * 16384 + b * 8192 + (k & 1) * 4096; }
DEVI void vote(rsrc_t rs, int w, int v, int far, int t0s) {
    const int lane = lane_id();
    constexpr unsigned N = (O_FLG_END - O_FLG) / 65536;
#pragma nounroll
    for (unsigned q = 0; q < N; ++q) st16(v4u{SENT, SENT, SENT, SENT}, rs, O_FLG + ((v * 4 + w) * N + q) * 1024 + lane * 16);
    const v2u e = ld8(rs, O_CAL + (lane & 31) * 256 + 128, 0);
    static_assert(NVK % 4 == 0 && NVK < 12, "");
    int best[2] = {1 << 30, 1 << 30};
    hold(t0s, DLY_P);
#pragma nounroll
    for (int k = 0; k <= NVK / 4; ++k)
#pragma unroll
        for (int b = 0; b < 2; ++b) {
            u64 t0, t1; int d;
            asm volatile("s_memtime %0\n\ts_waitcnt lgkmcnt(0)\n\ts_buffer_load_dword %2, %3, %4 glc\n\ts_waitcnt lgkmcnt(0)\n\ts_memtime %1\n\ts_waitcnt lgkmcnt(0)"
                         : "=&s"(t0), "=&s"(t1), "=&s"(d) : "s"(rs), "s"(prb(k ? 4 * k - w : 0, b) + v * 128) : "memory");
            if (k) best[b] = smin(best[b], (int)(t1 - t0));
        }
    *LDSP(int, (lane & 1) * 16 + w * 4) = best[lane & 1];
    WV0; WL0; BAR;
#pragma unroll
    for (int b = 0; b < 2; ++b) { const v4u t = *LDSP(const v4u, b * 16); best[b] = min(min(t[0], t[1]), min(t[2], t[3])); }
    st8(v2u{((e[0] + 2) & ~1u) | ((best[1] < best[0]) ^ far), e[1] + 1}, rs, lane < 32 && w == 0 ? O_CAL + lane * 256 + v * 8 : OOB, 0);
}
DEVI void vote_end(rsrc_t rs) {
    const unsigned o = O_CAL + (lane_id() & 31) * 256 + 128;
    const v2u e = ld8(rs, o, 0);
    st8(v2u{e[0] + 2, e[1] + 1}, rs, o, 0);
}
DEVI unsigned side(rsrc_t rs, int l) {
    unsigned spins = 0;
#pragma nounroll
    for (;;) {
        v16u f[2]; v2u e;
        const unsigned o = O_CAL + (blockIdx.x & 31) * 256;
        asm volatile("s_buffer_load_dwordx16 %0, %3, %4 glc\n\ts_buffer_load_dwordx16 %1, %3, %5 glc\n\ts_buffer_load_dwordx2 %2, %3, %6 glc\n\ts_waitcnt lgkmcnt(0)"
                     : "=&s"(f[0]), "=&s"(f[1]), "=&s"(e) : "s"(rs), "s"(o), "s"(o + 64), "s"(o + 128) : "memory");
        unsigned bad = 0, sum = 0;
#pragma unroll
        for (int i = 0; i < 16; ++i) { const unsigned x = f[i >> 3][2 * i & 15]; bad |= ((x ^ (e[0] + 2)) >> 1) | (f[i >> 3][(2 * i & 15) + 1] ^ (e[1] + 1)); sum += x & 1; }
#ifdef CSIDE
        sum = CSIDE * 16;
#endif
        if (bad == 0) return opqs(O_N + ((l ^ (sum > 8)) & 1) * 8192);
        spin_bail(spins);
        __builtin_amdgcn_s_sleep(PSV);
    }
}
#ifndef PSH
#define PSH 2
#endif
#ifndef HRED
#define HRED 1
#endif
template <class F> DEVI void wait_hint(rsrc_t rs, unsigned so, unsigned vo, int tag, F&& mid) {
    int mx;
#pragma nounroll
    for (int n = 0;; ++n) {
        v16i f;
        asm volatile("s_buffer_load_dwordx16 %0, %1, %2 glc\n\ts_waitcnt lgkmcnt(0)" : "=s"(f) : "s"(rs), "s"(so) : "memory");
#if HRED
        int m0, m1;
        smin8x2(f, m0, m1);
        if (smin(m0, m1) >= tag << 24) { smax8x2(f, m0, m1); mx = smax(m0, m1); break; }
#else
        int mn = f[0]; mx = f[0];
#pragma unroll
        for (int i = 1; i < 16; ++i) { mn = smin(mn, f[i]); mx = smax(mx, f[i]); }
        if (mn >= tag << 24) break;
#endif
        if (n == 40) { wait_prog(rs, vo, 0, tag << 24); n = 0; }
        __builtin_amdgcn_s_sleep(PSH);
    }
    mid(mx);
#pragma nounroll
    for (int n = 0; n < 64; ++n) if (left(mx) <= 0) break;
}
template <class WM> constexpr int widx(int j, int t) {
    int n = 0;
    for (int q = 0; q < j * WM::NP + t; ++q) n += WM::kind(q / WM::NP, q % WM::NP) == WM::kind(j, t);
    return n;
}
template <class WM> constexpr int lds_tile(int t) { for (int j = 0; j < WM::NT; ++j) if (WM::kind(j, t) == 1) return j; return -1; }
DEVI v4u ldsq(unsigned p, int off) { return *LDSP(const v4u, LDSP(char, p) + off); }
DEVI void stsq(unsigned p, int off, v4u v) { *LDSP(v4u, LDSP(char, p) + off) = v; }
DEVI v8u ldpair(unsigned wl, int slot) { return cat8(ldsq(wl, slot * 2048), ldsq(wl, slot * 2048 + 1024)); }
DEVI rsrc_t mkrs(const void* p, unsigned bytes) { return __builtin_amdgcn_make_buffer_rsrc((void*)p, 0, (int)bytes, 0x00020000); }
#ifndef LNN
#define LNN 8
#endif
constexpr int LN_NOW = LNN, NP0 = (96 - LN_NOW) * 4;
template <int SL = 0> DEVI void pull(const void* p, int n, int q, int nq) {
    const rsrc_t r = mkrs(p, n * 1024);
    const unsigned vo = lane_id() * 16;
#pragma nounroll
    for (; q < n; q += nq) { asm volatile("buffer_load_dwordx4 a[0:3], %0, %1, %2 offen" :: "v"(vo), "s"(r), "s"(q * 1024) : "memory"); if (SL) __builtin_amdgcn_s_sleep(SL); }
}
#ifndef PQ
#define PQ 0
#endif
#ifndef DLY_F
#define DLY_F 1000
#endif
#ifndef PFS
#define PFS 5
#endif
DEVI void pull_all(const Args& a, int w, int q, int t0) {
    q = q * 4 + w;
    pull(a.Wv, 8192, q, NP0); pull(a.Wo, 8192, q, NP0); if (!PQ) pull(a.Wq, 8192, q, NP0); pull(a.Wk, 8192, q, NP0);
    if (q >= 256) return;
    hold(t0, DLY_F);
    pull<PFS>(q & 1 ? a.Wg : a.Wu, 7168, q >> 1, 128); pull<PFS>(a.Wd, 7168, q, 256);
}
template <class WM, int j, int ks, int O0, int O1> DEVI void put_frag(unsigned ta, unsigned wl, v8u* Wc) {
    constexpr int t = ks >> 1, f = ks & 1, k = WM::kind(j, t), ix = widx<WM>(j, t);
    if constexpr (k == 0) {
        constexpr int R = WM::RB + 8 * ix + 4 * f;
        if constexpr (R < 256) asm volatile("ds_read_b64_tr_b16 a[%1:%2], %0 offset:%5\n\tds_read_b64_tr_b16 a[%3:%4], %0 offset:%6" :: "v"(ta), "n"(R), "n"(R + 1), "n"(R + 2), "n"(R + 3), "n"(O0), "n"(O1) : "memory");
        else asm volatile("ds_read_b64_tr_b16 v[%1:%2], %0 offset:%5\n\tds_read_b64_tr_b16 v[%3:%4], %0 offset:%6" :: "v"(ta), "n"(R - 256 + NV), "n"(R - 255 + NV), "n"(R - 254 + NV), "n"(R - 253 + NV), "n"(O0), "n"(O1) : "memory");
    } else {
        v2u p, q;
        asm volatile("ds_read_b64_tr_b16 %0, %2 offset:%3\n\tds_read_b64_tr_b16 %1, %2 offset:%4\n\ts_waitcnt lgkmcnt(0)" : "=&v"(p), "=&v"(q) : "v"(ta), "n"(O0), "n"(O1) : "memory");
        const v4u F = v4u{p[0], p[1], q[0], q[1]};
        if constexpr (k == 1) stsq(wl, (WM::LB + ix) * 2048 + f * 1024, F);
        else if constexpr (f == 0) Wc[WM::CBI + ix] = cat8(F, F);
        else Wc[WM::CBI + ix] = cat8(lo4(Wc[WM::CBI + ix]), F);
    }
}
template <int NB, int DEPTH, int NI, bool SHARED = false, int SL = DEPTH, class ISSUE, class PUT>
DEVI void load_blocks(ISSUE&& issue, PUT&& put) {
    sfor<0, DEPTH - 1>([&](auto B) { issue(B, IC<B.v % SL>{}); });
    sfor<0, NB>([&](auto B) {
        constexpr int b = B.v, left = NB - 1 - b < DEPTH - 2 ? NB - 1 - b : DEPTH - 2;
        wvm<NI * left>();
        wlg<0>();
        if constexpr (SHARED) BAR;
        if constexpr (b + DEPTH - 1 < NB) issue(IC<b + DEPTH - 1>{}, IC<(b + DEPTH - 1) % SL>{});
        put(B, IC<b % SL>{});
    });
    wlg<0>();
    if constexpr (SHARED) BAR;
}
DEVI void dma_blk(unsigned lds, rsrc_t rw, unsigned vo, unsigned so, unsigned rowb) { setm0(lds); dma1<0, false>(rw, vo, so); setm0(lds + 1024); dma1<0, false>(rw, vo, so + 16 * rowb); }
template <class WM, int NKS, int K0, int K1, int RTS, int LATE1, class FILL>
DEVI void gemm_pass(unsigned xa, unsigned wl, const v8u* Wc, v4u (&x)[2][3], f32x4 (&acc)[WM::NT][2], FILL&& fill) {
    auto rd = [&](auto KS, auto Q) {
        constexpr int ks = KS.v, q = Q.v, jl = lds_tile<WM>(ks >> 1), il = widx<WM>(jl < 0 ? 0 : jl, ks >> 1);
        if constexpr (q == 0 && jl >= 0) x[ks & 1][2] = ldsq(wl, (WM::LB + il) * 2048 + (ks & 1) * 1024);
        if constexpr (q == 1) x[ks & 1][1] = ldsq(xa, RTS + ks * 1024);
        if constexpr (q == 2) x[ks & 1][0] = ldsq(xa, ks * 1024);
    };
    sfor<K0 * 2 * WM::NT, K1 * 2 * WM::NT>([&](auto N) {
        constexpr int ks = N.v / (2 * WM::NT), n = N.v % (2 * WM::NT), j = n >> 1, rt = n & 1, t = ks >> 1, f = ks & 1, k = WM::kind(j, t), ix = widx<WM>(j, t);
        fill(IC<ks>{}, IC<n>{});
        SB;
        const v4u xv = x[ks & 1][rt];
        if constexpr (k == 0) mfA<WM::RB + 8 * ix + 4 * f, ks == 0>(acc[j][rt], xv);
        else if constexpr (k == 1) mfv<ks == 0>(acc[j][rt], x[ks & 1][2], xv);
        else mfv<ks == 0>(acc[j][rt], f ? hi4(Wc[WM::CBI + ix]) : lo4(Wc[WM::CBI + ix]), xv);
        if constexpr (ks + 1 < NKS) rd(IC<ks + 1>{}, IC<n - (ks == 1 ? LATE1 : 0)>{});
        SB;
    });
}
template <class WM> constexpr int next_lds(int n) { for (; n < 16 * WM::NT; ++n) if (WM::kind(2 * (n >> 5) + (n & 1), (n >> 1) & 15) == 1) return n; return -1; }
template <class WM> constexpr int lds_cnt(int n) { int c = 0; for (int q = next_lds<WM>(0); q >= 0 && q < n; q = next_lds<WM>(q + 1)) ++c; return c; }
template <class WM> constexpr int lds_nth(int k) { int q = next_lds<WM>(0); for (; k > 0 && q >= 0; --k) q = next_lds<WM>(q + 1); return q; }
template <class WM, int K, int NB> DEVI void pair_next(v8u (&pb)[NB], unsigned wl) {
    constexpr int n = lds_nth<WM>(K);
    if constexpr (n >= 0) { constexpr int ix = widx<WM>(2 * (n >> 5) + (n & 1), (n >> 1) & 15); pb[K % NB] = ldpair(wl, WM::LB + ix); }
}
template <class WM, int MO, int MR, int NB, class FILL>
DEVI void spass(unsigned ma, unsigned wl, const v8u* Wc, f32x4* e, v4u (&m)[MR], v8u (&pb)[NB], int sidx, FILL&& fill) {
    sfor<0, 16 * WM::NT>([&](auto N) {
        constexpr int n = N.v, t = (n >> 1) & 15, j = 2 * (n >> 5) + (n & 1), k = WM::kind(j, t), ix = widx<WM>(j, t), tn = (t + MR - 1) & 15, r = (n >> 1) % MR;
        fill(N);
        SB;
        if constexpr (k == 0) smf<WM::RB + 8 * ix>(e[j], m[r], sidx);
        else if constexpr (k == 1) { constexpr int q = lds_cnt<WM>(n); smfv(e[j], m[r], pb[q % NB], sidx); pair_next<WM, q + NB>(pb, wl); }
        else smfv(e[j], m[r], Wc[WM::CBI + ix], sidx);
        if constexpr ((n & 1) == 0 && n + 2 * MR - 2 < 16 * WM::NT) m[(r + MR - 1) % MR] = ldsq(ma, MO + (tn >> 3) * 32768 + (tn & 7) * 1024);
        SB;
    });
}
typedef __attribute__((ext_vector_type(4))) short s16x4;
DEVI v2u ldtr(unsigned p, int off) { return __builtin_bit_cast(v2u, __builtin_amdgcn_ds_read_tr16_b64_v4i16(LDSP(s16x4, LDSP(char, p) + off))); }
DEVI v4u ldtr2(unsigned p, int off) { const v2u a = ldtr(p, off), b = ldtr(p, off + 256); return v4u{a[0], a[1], b[0], b[1]}; }
DEVI void sts1(unsigned p, int off, unsigned v) { *LDSP(unsigned, LDSP(char, p) + off) = v; }
DEVI unsigned lds1(unsigned p, int off) { return *LDSP(const unsigned, LDSP(char, p) + off); }
DEVI void use(unsigned v) { asm volatile("" :: "v"(v) : "memory"); }
DEVI int mk_sidx(int lane) { return opq(0x4444 + (lane & 2) * 0x5555); }
DEVI int crow(int r, int hb) { return 4 * (r >> 1) + 2 * hb + (r & 1); }
#define A_LDS 0x28a0114002004410ull
#define A_VAR 0x0000000400020001ull
#define A_LOOP \
"s_brev_b32 s99, 1\ns_brev_b32 s98, 1\ns_brev_b32 s97, 1\ns_brev_b32 s96, 1\n" \
"s_branch 4f\n.p2align 6\n1:\ns_waitcnt lgkmcnt(0)\n" \
"v_mfma_f32_16x16x32_bf16 v[40:43], v[72:75], v[32:35], v[40:43]\nds_read_b128 v[76:79], v25 offset:3072\nds_read_b128 v[80:83], v24 offset:21504\nv_mfma_f32_16x16x32_bf16 v[44:47], v[72:75], v[36:39], v[44:47]\n" \
"ds_read_b128 v[84:87], v24 offset:54272\nv_bfe_u32 v31, v24, 6, 3\nv_mfma_f32_16x16x32_bf16 v[48:51], a[160:163], v[32:35], v[48:51]\ns_mov_b32 m0, s50\n" \
"s_add_u32 s99, s30, 2\nv_lshlrev_b32 v31, 2, v31\nv_mfma_f32_16x16x32_bf16 v[52:55], a[160:163], v[36:39], v[52:55]\ns_min_u32 s99, s99, 63\n" \
"s_lshl_b32 s99, s99, 5\ns_add_u32 s99, s99, s48\nv_mfma_f32_16x16x32_bf16 v[56:59], v[120:123], v[32:35], v[56:59]\ns_and_b32 s94, s30, 7\n" \
"s_lshl_b32 s94, s94, 18\ns_add_u32 s94, s94, s33\nv_mfma_f32_16x16x32_bf16 v[60:63], v[120:123], v[36:39], v[60:63]\ns_add_u32 s31, s31, 0x10000\n" \
"v_mfma_f32_16x16x32_bf16 v[64:67], v[224:227], v[32:35], v[64:67]\ns_waitcnt lgkmcnt(1)\nv_mfma_f32_16x16x32_bf16 v[68:71], v[224:227], v[36:39], v[68:71]\ns_waitcnt lgkmcnt(0)\n" \
"ds_read_b128 v[32:35], v24 offset:22528\nv_mfma_f32_16x16x32_bf16 v[40:43], v[76:79], v[80:83], v[40:43]\nds_read_b128 v[36:39], v24 offset:55296\nv_mfma_f32_16x16x32_bf16 v[44:47], v[76:79], v[84:87], v[44:47]\n" \
"v_mfma_f32_16x16x32_bf16 v[48:51], a[164:167], v[80:83], v[48:51]\nds_read_b128 v[72:75], v25 offset:18432\nv_mfma_f32_16x16x32_bf16 v[52:55], a[164:167], v[84:87], v[52:55]\nv_mfma_f32_16x16x32_bf16 v[56:59], v[124:127], v[80:83], v[56:59]\n" \
"v_mfma_f32_16x16x32_bf16 v[60:63], v[124:127], v[84:87], v[60:63]\nv_mfma_f32_16x16x32_bf16 v[64:67], v[228:231], v[80:83], v[64:67]\ns_waitcnt lgkmcnt(1)\nv_mfma_f32_16x16x32_bf16 v[68:71], v[228:231], v[84:87], v[68:71]\n" \
"ds_read_b128 v[76:79], v24 offset:23552\nv_mfma_f32_16x16x32_bf16 v[40:43], a[64:67], v[32:35], v[40:43]\nds_read_b128 v[80:83], v24 offset:56320\nv_mfma_f32_16x16x32_bf16 v[44:47], a[64:67], v[36:39], v[44:47]\n" \
"v_mfma_f32_16x16x32_bf16 v[48:51], a[168:171], v[32:35], v[48:51]\nds_read_b128 v[84:87], v25 offset:19456\nv_mfma_f32_16x16x32_bf16 v[52:55], a[168:171], v[36:39], v[52:55]\nv_mfma_f32_16x16x32_bf16 v[56:59], v[128:131], v[32:35], v[56:59]\n" \
"s_waitcnt lgkmcnt(3)\nv_mfma_f32_16x16x32_bf16 v[60:63], v[128:131], v[36:39], v[60:63]\nv_mfma_f32_16x16x32_bf16 v[64:67], v[72:75], v[32:35], v[64:67]\ns_waitcnt vmcnt(8) lgkmcnt(1)\n" \
"s_barrier\nv_mfma_f32_16x16x32_bf16 v[68:71], v[72:75], v[36:39], v[68:71]\nds_read_b128 v[32:35], v24 offset:24576\nv_mfma_f32_16x16x32_bf16 v[40:43], a[68:71], v[76:79], v[40:43]\n" \
"ds_read_b128 v[36:39], v24 offset:57344\nv_mfma_f32_16x16x32_bf16 v[44:47], a[68:71], v[80:83], v[44:47]\nds_read_b128 v[72:75], v25 offset:12288\nv_mfma_f32_16x16x32_bf16 v[48:51], a[172:175], v[76:79], v[48:51]\n" \
"buffer_load_dword v31, s[8:11], s99 offen offset:0 sc1 lds\nv_mfma_f32_16x16x32_bf16 v[52:55], a[172:175], v[80:83], v[52:55]\ns_add_u32 m0, s32, 0x4000\nv_mfma_f32_16x16x32_bf16 v[56:59], v[132:135], v[76:79], v[56:59]\n" \
"s_waitcnt lgkmcnt(3)\nv_mfma_f32_16x16x32_bf16 v[60:63], v[132:135], v[80:83], v[60:63]\nv_mfma_f32_16x16x32_bf16 v[64:67], v[84:87], v[76:79], v[64:67]\ns_waitcnt lgkmcnt(1)\n" \
"v_mfma_f32_16x16x32_bf16 v[68:71], v[84:87], v[80:83], v[68:71]\nds_read_b128 v[76:79], v24 offset:25600\nv_mfma_f32_16x16x32_bf16 v[40:43], a[72:75], v[32:35], v[40:43]\nds_read_b128 v[80:83], v24 offset:58368\n" \
"v_mfma_f32_16x16x32_bf16 v[44:47], a[72:75], v[36:39], v[44:47]\nds_read_b128 v[84:87], v25 offset:13312\nv_mfma_f32_16x16x32_bf16 v[48:51], a[176:179], v[32:35], v[48:51]\ns_waitcnt lgkmcnt(3)\n" \
"v_mfma_f32_16x16x32_bf16 v[52:55], a[176:179], v[36:39], v[52:55]\nv_mfma_f32_16x16x32_bf16 v[56:59], v[72:75], v[32:35], v[56:59]\nv_mfma_f32_16x16x32_bf16 v[60:63], v[72:75], v[36:39], v[60:63]\nv_mfma_f32_16x16x32_bf16 v[64:67], v[232:235], v[32:35], v[64:67]\n" \
"s_waitcnt lgkmcnt(1)\nv_mfma_f32_16x16x32_bf16 v[68:71], v[232:235], v[36:39], v[68:71]\nbuffer_load_dwordx4 v24, s[8:11], s94 offen offset:0 sc1 lds\nv_mfma_f32_16x16x32_bf16 v[40:43], a[76:79], v[76:79], v[40:43]\n" \
"ds_read_b128 v[32:35], v24 offset:26624\nbuffer_load_dwordx4 v24, s[8:11], s94 offen offset:1024 sc1 lds\nv_mfma_f32_16x16x32_bf16 v[44:47], a[76:79], v[80:83], v[44:47]\nds_read_b128 v[36:39], v24 offset:59392\n" \
"buffer_load_dwordx4 v24, s[8:11], s94 offen offset:2048 sc1 lds\nv_mfma_f32_16x16x32_bf16 v[48:51], a[180:183], v[76:79], v[48:51]\ns_waitcnt lgkmcnt(2)\nbuffer_load_dwordx4 v24, s[8:11], s94 offen offset:3072 sc1 lds\n" \
"v_mfma_f32_16x16x32_bf16 v[52:55], a[180:183], v[80:83], v[52:55]\nds_read_b128 v[72:75], v25 offset:20480\ns_and_b32 s93, s30, 15\nv_mfma_f32_16x16x32_bf16 v[56:59], v[84:87], v[76:79], v[56:59]\n" \
"s_lshl_b32 s92, s93, 17\ns_add_u32 s92, s92, s35\ns_and_b32 s93, s93, 7\nv_mfma_f32_16x16x32_bf16 v[60:63], v[84:87], v[80:83], v[60:63]\n" \
"s_lshr_b32 s95, s30, s41\ns_and_b32 s95, s95, 1\ns_lshl_b32 s93, s93, 14\nv_mfma_f32_16x16x32_bf16 v[64:67], v[236:239], v[76:79], v[64:67]\n" \
"s_lshl1_add_u32 s99, s93, s34\ns_or_b32 s98, s92, s36\ns_or_b32 s97, s92, s37\nv_mfma_f32_16x16x32_bf16 v[68:71], v[236:239], v[80:83], v[68:71]\n" \
"s_waitcnt lgkmcnt(1)\nds_read_b128 v[76:79], v24 offset:27648\nv_mfma_f32_16x16x32_bf16 v[40:43], a[80:83], v[32:35], v[40:43]\nds_read_b128 v[80:83], v24 offset:60416\n" \
"s_and_b32 s92, s92, s39\nv_mfma_f32_16x16x32_bf16 v[44:47], a[80:83], v[36:39], v[44:47]\ns_andn2_b32 s93, s93, s39\ns_add_u32 s96, s92, s38\n" \
"s_add_u32 s96, s96, s93\nv_mfma_f32_16x16x32_bf16 v[48:51], a[184:187], v[32:35], v[48:51]\nds_read_b128 v[84:87], v25 offset:21504\ns_add_u32 s30, s30, 1\n" \
"v_mfma_f32_16x16x32_bf16 v[52:55], a[184:187], v[36:39], v[52:55]\ns_lshl_b32 s93, s30, 24\ns_cmp_le_u32 s30, 64\ns_cselect_b32 s51, s14, 0\n" \
"v_mfma_f32_16x16x32_bf16 v[56:59], v[136:139], v[32:35], v[56:59]\ns_waitcnt lgkmcnt(3)\nds_read_b128 v[88:91], v25 offset:4096\nv_mfma_f32_16x16x32_bf16 v[60:63], v[136:139], v[36:39], v[60:63]\n" \
"v_mfma_f32_16x16x32_bf16 v[64:67], v[72:75], v[32:35], v[64:67]\ns_waitcnt lgkmcnt(2)\nv_mfma_f32_16x16x32_bf16 v[68:71], v[72:75], v[36:39], v[68:71]\nds_read_b128 v[32:35], v24 offset:28672\n" \
"v_mfma_f32_16x16x32_bf16 v[40:43], a[84:87], v[76:79], v[40:43]\nds_read_b128 v[36:39], v24 offset:61440\nv_mfma_f32_16x16x32_bf16 v[44:47], a[84:87], v[80:83], v[44:47]\nv_mfma_f32_16x16x32_bf16 v[48:51], a[188:191], v[76:79], v[48:51]\n" \
"v_mfma_f32_16x16x32_bf16 v[52:55], a[188:191], v[80:83], v[52:55]\nv_mfma_f32_16x16x32_bf16 v[56:59], v[140:143], v[76:79], v[56:59]\ns_waitcnt lgkmcnt(2)\nds_read_b128 v[72:75], v25 offset:5120\n" \
"v_mfma_f32_16x16x32_bf16 v[60:63], v[140:143], v[80:83], v[60:63]\nv_mfma_f32_16x16x32_bf16 v[64:67], v[84:87], v[76:79], v[64:67]\ns_waitcnt lgkmcnt(1)\nv_mfma_f32_16x16x32_bf16 v[68:71], v[84:87], v[80:83], v[68:71]\n" \
"ds_read_b128 v[76:79], v24 offset:29696\nv_mfma_f32_16x16x32_bf16 v[40:43], v[88:91], v[32:35], v[40:43]\nds_read_b128 v[80:83], v24 offset:62464\nv_mfma_f32_16x16x32_bf16 v[44:47], v[88:91], v[36:39], v[44:47]\n" \
"v_mfma_f32_16x16x32_bf16 v[48:51], a[192:195], v[32:35], v[48:51]\nv_mfma_f32_16x16x32_bf16 v[52:55], a[192:195], v[36:39], v[52:55]\nv_mfma_f32_16x16x32_bf16 v[56:59], v[144:147], v[32:35], v[56:59]\nv_mfma_f32_16x16x32_bf16 v[60:63], v[144:147], v[36:39], v[60:63]\n" \
"v_mfma_f32_16x16x32_bf16 v[64:67], v[240:243], v[32:35], v[64:67]\ns_waitcnt lgkmcnt(0)\nv_mfma_f32_16x16x32_bf16 v[68:71], v[240:243], v[36:39], v[68:71]\nds_read_b128 v[32:35], v24 offset:30720\n" \
"v_add_u32 v31, s32, v24\nv_mfma_f32_16x16x32_bf16 v[40:43], v[72:75], v[76:79], v[40:43]\nds_read_b128 v[36:39], v24 offset:63488\nv_mfma_f32_16x16x32_bf16 v[44:47], v[72:75], v[80:83], v[44:47]\n" \
"v_mfma_f32_16x16x32_bf16 v[48:51], a[196:199], v[76:79], v[48:51]\nv_mfma_f32_16x16x32_bf16 v[52:55], a[196:199], v[80:83], v[52:55]\nv_mfma_f32_16x16x32_bf16 v[56:59], v[148:151], v[76:79], v[56:59]\nv_mfma_f32_16x16x32_bf16 v[60:63], v[148:151], v[80:83], v[60:63]\n" \
"v_mfma_f32_16x16x32_bf16 v[64:67], v[244:247], v[76:79], v[64:67]\ns_waitcnt vmcnt(0) lgkmcnt(0)\nds_read_b128 v[72:75], v31 offset:16384\nds_read_b128 v[76:79], v31 offset:17408\n" \
"v_mfma_f32_16x16x32_bf16 v[68:71], v[244:247], v[80:83], v[68:71]\nds_read_b128 v[84:87], v31 offset:18432\nds_read_b128 v[80:83], v31 offset:19456\nv_mfma_f32_16x16x32_bf16 v[40:43], a[88:91], v[32:35], v[40:43]\n" \
"ds_read_b128 v[88:91], v24 offset:31744\nv_mfma_f32_16x16x32_bf16 v[44:47], a[88:91], v[36:39], v[44:47]\nds_read_b128 v[92:95], v24 offset:64512\nv_mfma_f32_16x16x32_bf16 v[48:51], a[200:203], v[32:35], v[48:51]\n" \
"v_mfma_f32_16x16x32_bf16 v[52:55], a[200:203], v[36:39], v[52:55]\ns_waitcnt lgkmcnt(2)\nv_max3_u32 v96, v72, v76, v84\nv_max_u32 v96, v96, v80\n" \
"v_mfma_f32_16x16x32_bf16 v[56:59], v[152:155], v[32:35], v[56:59]\nds_read_b128 v[100:103], v25 offset:0\nv_cmp_eq_u32 vcc, -1, v96\nv_mfma_f32_16x16x32_bf16 v[60:63], v[152:155], v[36:39], v[60:63]\n" \
"ds_read_b128 v[104:107], v25 offset:1024\nv_mov_b64 v[108:109], 0\nv_mfma_f32_16x16x32_bf16 v[64:67], v[248:251], v[32:35], v[64:67]\ns_waitcnt lgkmcnt(3)\n" \
"v_mov_b64 v[110:111], 0\nv_mov_b64 v[32:33], 0\nv_mfma_f32_16x16x32_bf16 v[68:71], v[248:251], v[36:39], v[68:71]\ns_cbranch_vccnz 2f\n" \
"3:\nv_mfma_f32_16x16x32_bf16 v[40:43], a[92:95], v[88:91], v[40:43]\ns_waitcnt lgkmcnt(2)\ns_barrier\n" \
"v_mfma_f32_16x16x32_bf16 v[48:51], a[204:207], v[88:91], v[48:51]\ns_memrealtime s[56:57]\nds_read_b128 v[36:39], v24 offset:20480\nv_mfma_f32_16x16x32_bf16 v[56:59], v[156:159], v[88:91], v[56:59]\n" \
"ds_read_b128 v[72:75], v24 offset:21504\nv_mov_b64 v[34:35], 0\nv_mfma_f32_16x16x32_bf16 v[64:67], v[252:255], v[88:91], v[64:67]\nds_read_b128 v[76:79], v24 offset:16384\n" \
"v_add_u32 v31, s44, v24\nv_mfma_f32_16x16x32_bf16 v[44:47], a[92:95], v[92:95], v[44:47]\nds_read_b128 v[80:83], v24 offset:22528\nv_cvt_pk_bf16_f32 v40, v40, v48\n" \
"v_mfma_f32_16x16x32_bf16 v[52:55], a[204:207], v[92:95], v[52:55]\nds_read_b128 v[84:87], v24 offset:23552\nv_cvt_pk_bf16_f32 v41, v41, v49\nv_mfma_f32_16x16x32_bf16 v[60:63], v[156:159], v[92:95], v[60:63]\n" \
"ds_read_b128 v[88:91], v24 offset:17408\nv_cvt_pk_bf16_f32 v42, v42, v50\nv_mfma_f32_16x16x32_bf16 v[68:71], v[252:255], v[92:95], v[68:71]\ns_waitcnt lgkmcnt(5)\n" \
"ds_write_b64 v31, v[40:41] offset:0\nv_smfmac_f32_16x16x64_bf16 v[108:111], v[36:39], v[100:107], v26\nds_read_b128 v[92:95], v24 offset:18432\nv_cvt_pk_bf16_f32 v43, v43, v51\n" \
"v_smfmac_f32_16x16x64_bf16 v[32:35], v[36:39], a[120:127], v26\ns_waitcnt lgkmcnt(5)\nds_write_b64 v31, v[42:43] offset:8\nv_smfmac_f32_16x16x64_bf16 v[108:111], v[72:75], a[24:31], v26\n" \
"ds_read_b128 v[48:51], v24 offset:19456\nv_cvt_pk_bf16_f32 v56, v56, v64\nv_smfmac_f32_16x16x64_bf16 v[32:35], v[72:75], a[128:135], v26\ns_add_u32 s94, s56, 0x76\n" \
"s_and_b32 s94, s94, 0xffffff\nv_cvt_pk_bf16_f32 v57, v57, v65\nv_smfmac_f32_16x16x64_bf16 v[108:111], v[76:79], v[0:7], v26\nds_read_b128 v[40:43], v24 offset:49152\n" \
"v_cvt_pk_bf16_f32 v58, v58, v66\nv_smfmac_f32_16x16x64_bf16 v[32:35], v[76:79], a[96:103], v26\ns_waitcnt lgkmcnt(4)\nds_write_b64 v31, v[56:57] offset:32768\n" \
"v_smfmac_f32_16x16x64_bf16 v[108:111], v[80:83], a[32:39], v26\nds_read_b128 v[76:79], v24 offset:50176\nv_cvt_pk_bf16_f32 v59, v59, v67\nv_smfmac_f32_16x16x64_bf16 v[32:35], v[80:83], a[136:143], v26\n" \
"ds_read_b128 v[96:99], v25 offset:6144\ns_or_b32 s94, s94, s93\nv_smfmac_f32_16x16x64_bf16 v[108:111], v[84:87], a[40:47], v26\nds_read_b128 v[100:103], v25 offset:7168\n" \
"v_mov_b32 v56, s94\nv_smfmac_f32_16x16x64_bf16 v[32:35], v[84:87], a[144:151], v26\nds_read_b128 v[64:67], v24 offset:51200\nv_cvt_pk_bf16_f32 v44, v44, v52\n" \
"v_smfmac_f32_16x16x64_bf16 v[108:111], v[88:91], a[0:7], v26\nds_read_b128 v[80:83], v25 offset:2048\nv_cvt_pk_bf16_f32 v45, v45, v53\nv_smfmac_f32_16x16x64_bf16 v[32:35], v[88:91], v[8:15], v26\n" \
"s_waitcnt lgkmcnt(5)\nds_read_b128 v[84:87], v25 offset:3072\nv_smfmac_f32_16x16x64_bf16 v[108:111], v[92:95], a[8:15], v26\nds_read_b128 v[104:107], v24 offset:56320\n" \
"v_cvt_pk_bf16_f32 v46, v46, v54\nv_smfmac_f32_16x16x64_bf16 v[32:35], v[92:95], a[104:111], v26\nds_write_b64 v31, v[58:59] offset:32776\nv_smfmac_f32_16x16x64_bf16 v[108:111], v[48:51], a[16:23], v26\n" \
"buffer_store_dword v56, v29, s[8:11], s49 offen offset:0 sc1\nv_smfmac_f32_16x16x64_bf16 v[32:35], v[48:51], a[112:119], v26\nds_read_b128 v[92:95], v24 offset:52224\nv_cvt_pk_bf16_f32 v47, v47, v55\n" \
"v_smfmac_f32_16x16x64_bf16 v[108:111], v[40:43], a[48:55], v26\nds_read_b128 v[48:51], v24 offset:53248\nv_cvt_pk_bf16_f32 v60, v60, v68\nv_smfmac_f32_16x16x64_bf16 v[32:35], v[40:43], a[152:159], v26\n" \
"s_waitcnt lgkmcnt(4)\nds_write_b64 v31, v[44:45] offset:4096\nv_smfmac_f32_16x16x64_bf16 v[108:111], v[76:79], a[56:63], v26\nds_read_b128 v[52:55], v24 offset:54272\n" \
"v_cvt_pk_bf16_f32 v61, v61, v69\nv_smfmac_f32_16x16x64_bf16 v[32:35], v[76:79], v[96:103], v26\nds_read_b128 v[56:59], v24 offset:55296\nv_cvt_pk_bf16_f32 v62, v62, v70\n" \
"v_smfmac_f32_16x16x64_bf16 v[108:111], v[64:67], v[80:87], v26\nds_read_b128 v[76:79], v25 offset:4096\nv_cvt_pk_bf16_f32 v63, v63, v71\nv_smfmac_f32_16x16x64_bf16 v[32:35], v[64:67], a[160:167], v26\n" \
"s_waitcnt lgkmcnt(5)\nds_read_b128 v[80:83], v25 offset:5120\nv_smfmac_f32_16x16x64_bf16 v[108:111], v[104:107], a[88:95], v26\nds_write_b64 v31, v[46:47] offset:4104\n" \
"v_smfmac_f32_16x16x64_bf16 v[32:35], v[104:107], a[200:207], v26\nds_read_b128 v[64:67], v25 offset:14336\nv_mov_b64 v[44:45], 0\nv_smfmac_f32_16x16x64_bf16 v[108:111], v[92:95], a[64:71], v26\n" \
"ds_read_b128 v[68:71], v25 offset:15360\nv_mov_b64 v[46:47], 0\nv_smfmac_f32_16x16x64_bf16 v[32:35], v[92:95], a[168:175], v26\ns_waitcnt lgkmcnt(5)\n" \
"ds_write_b64 v31, v[60:61] offset:36864\nv_smfmac_f32_16x16x64_bf16 v[108:111], v[48:51], a[72:79], v26\nds_read_b128 v[84:87], v24 offset:51200\nv_mov_b64 v[60:61], 0\n" \
"v_smfmac_f32_16x16x64_bf16 v[32:35], v[48:51], a[176:183], v26\nds_write_b64 v31, v[62:63] offset:36872\nv_smfmac_f32_16x16x64_bf16 v[108:111], v[52:55], a[80:87], v26\nds_read_b128 v[48:51], v24 offset:18432\n" \
"v_mov_b64 v[62:63], 0\nv_smfmac_f32_16x16x64_bf16 v[32:35], v[52:55], a[184:191], v26\ns_waitcnt lgkmcnt(5)\nds_read_b32 v31, v28 offset:512\n" \
"v_smfmac_f32_16x16x64_bf16 v[108:111], v[56:59], v[76:83], v26\nv_add_u32 v52, s45, v24\nv_smfmac_f32_16x16x64_bf16 v[32:35], v[56:59], a[192:199], v26\nds_read_b128 v[76:79], v24 offset:54272\n" \
"v_cndmask_b32 v52, v52, v28, s[28:29]\nv_smfmac_f32_16x16x64_bf16 v[44:47], v[72:75], a[240:247], v26\ns_waitcnt lgkmcnt(4)\nds_read_b128 v[96:99], v25 offset:18432\n" \
"v_smfmac_f32_16x16x64_bf16 v[60:63], v[72:75], v[64:71], v26\nds_read_b128 v[100:103], v25 offset:19456\nv_add_f32 v64, v108, v110\nv_smfmac_f32_16x16x64_bf16 v[44:47], v[36:39], a[232:239], v26\n" \
"ds_read_b128 v[68:71], v25 offset:20480\nv_add_f32 v65, v32, v34\nv_smfmac_f32_16x16x64_bf16 v[60:63], v[36:39], v[192:199], v26\nds_read_b128 v[72:75], v25 offset:21504\n" \
"v_add_f32 v66, v109, v111\nv_smfmac_f32_16x16x64_bf16 v[44:47], v[84:87], v[120:127], v26\nds_read_b128 v[36:39], v24 offset:19456\nv_add_f32 v67, v33, v35\n" \
"v_smfmac_f32_16x16x64_bf16 v[60:63], v[84:87], v[224:231], v26\nds_read_b128 v[32:35], v24 offset:50176\nv_cvt_pk_bf16_f32 v54, v64, v65\nv_smfmac_f32_16x16x64_bf16 v[44:47], v[104:107], v[152:159], v26\n" \
"ds_read_b128 v[80:83], v25 offset:10240\nv_cvt_pk_bf16_f32 v55, v66, v67\nv_smfmac_f32_16x16x64_bf16 v[60:63], v[104:107], v[248:255], v26\nds_write2_b32 v27, v54, v55 offset0:0 offset1:4\n" \
"v_smfmac_f32_16x16x64_bf16 v[44:47], v[56:59], v[144:151], v26\ns_waitcnt lgkmcnt(5)\nds_read_b128 v[84:87], v25 offset:11264\nv_smfmac_f32_16x16x64_bf16 v[60:63], v[56:59], v[240:247], v26\n" \
"ds_read_b128 v[64:67], v24 offset:53248\nv_cmp_eq_u32 vcc, -1, v31\nv_smfmac_f32_16x16x64_bf16 v[44:47], v[48:51], v[16:23], v26\nv_smfmac_f32_16x16x64_bf16 v[60:63], v[48:51], v[176:183], v26\n" \
"ds_read_b128 v[56:59], v24 offset:23552\nv_smfmac_f32_16x16x64_bf16 v[44:47], v[92:95], v[128:135], v26\nds_read_b128 v[104:107], v25 offset:12288\nv_smfmac_f32_16x16x64_bf16 v[60:63], v[92:95], v[96:103], v26\n" \
"s_waitcnt lgkmcnt(4)\nds_read_b128 v[108:111], v25 offset:13312\nv_smfmac_f32_16x16x64_bf16 v[44:47], v[76:79], v[136:143], v26\ns_barrier\n" \
"v_smfmac_f32_16x16x64_bf16 v[60:63], v[76:79], v[68:75], v26\nds_read_b128 v[92:95], v25 offset:16384\nv_mov_b32 v49, s43\nv_smfmac_f32_16x16x64_bf16 v[44:47], v[36:39], a[224:231], v26\n" \
"ds_read_b128 v[96:99], v25 offset:17408\nv_cndmask_b32 v72, v49, 0, s[16:17]\nv_smfmac_f32_16x16x64_bf16 v[60:63], v[36:39], v[184:191], v26\ns_waitcnt lgkmcnt(4)\n" \
"ds_read_b128 v[36:39], v24 offset:22528\nv_smfmac_f32_16x16x64_bf16 v[44:47], v[40:43], v[80:87], v26\nds_read_b128 v[76:79], v25 offset:8192\nv_cndmask_b32 v73, v49, 0, s[18:19]\n" \
"v_smfmac_f32_16x16x64_bf16 v[60:63], v[40:43], v[208:215], v26\nds_read_b128 v[80:83], v25 offset:9216\nv_cndmask_b32 v74, v49, 0, s[20:21]\nv_smfmac_f32_16x16x64_bf16 v[44:47], v[32:35], v[112:119], v26\n" \
"ds_read_b128 v[40:43], v24 offset:16384\nv_cndmask_b32 v75, v49, 0, s[22:23]\nv_smfmac_f32_16x16x64_bf16 v[60:63], v[32:35], v[216:223], v26\nds_read_b128 v[68:71], v52 offset:0\n" \
"v_mov_b32 v48, 0xff61b1e6\nv_smfmac_f32_16x16x64_bf16 v[44:47], v[88:91], a[216:223], v26\nds_read_b128 v[32:35], v24 offset:57344\nv_cndmask_b32 v84, v48, v49, s[16:17]\n" \
"v_smfmac_f32_16x16x64_bf16 v[60:63], v[88:91], v[168:175], v26\ns_waitcnt lgkmcnt(6)\nds_read_b128 v[88:91], v24 offset:61440\nv_smfmac_f32_16x16x64_bf16 v[44:47], v[64:67], v[104:111], v26\n" \
"ds_read_b128 v[100:103], v52 offset:1024\nv_cndmask_b32 v85, v48, v49, s[18:19]\nv_smfmac_f32_16x16x64_bf16 v[60:63], v[64:67], v[232:239], v26\nds_read_b128 v[104:107], v24 offset:58368\n" \
"v_cndmask_b32 v86, v48, v49, s[20:21]\nv_smfmac_f32_16x16x64_bf16 v[44:47], v[56:59], a[248:255], v26\nds_read_b128 v[64:67], v24 offset:62464\nv_cndmask_b32 v87, v48, v49, s[22:23]\n" \
"v_smfmac_f32_16x16x64_bf16 v[60:63], v[56:59], v[92:99], v26\ns_waitcnt lgkmcnt(5)\nds_read_b128 v[56:59], v52 offset:2048\nv_smfmac_f32_16x16x64_bf16 v[44:47], v[36:39], v[76:83], v26\n" \
"ds_read_b128 v[48:51], v24 offset:59392\nv_smfmac_f32_16x16x64_bf16 v[60:63], v[36:39], v[200:207], v26\nds_read_b128 v[76:79], v24 offset:63488\nv_smfmac_f32_16x16x64_bf16 v[44:47], v[40:43], a[208:215], v26\n" \
"ds_read_b128 v[36:39], v52 offset:3072\nds_read_b128 v[52:55], v24 offset:60416\nv_smfmac_f32_16x16x64_bf16 v[60:63], v[40:43], v[160:167], v26\ns_waitcnt lgkmcnt(6)\n" \
"ds_read_b128 v[40:43], v24 offset:64512\nv_mfma_f32_16x16x32_bf16 v[72:75], v[32:35], v[68:71], v[72:75]\nds_read_b128 v[80:83], v24 offset:0\nv_add_f32 v32, v44, v46\n" \
"v_add_f32 v34, v45, v47\nv_mfma_f32_16x16x32_bf16 v[84:87], v[88:91], v[68:71], v[84:87]\ns_waitcnt lgkmcnt(6)\nv_add_f32 v33, v60, v62\n" \
"v_add_f32 v35, v61, v63\nv_cvt_pk_bf16_f32 v44, v32, v33\nv_cvt_pk_bf16_f32 v45, v34, v35\nv_mfma_f32_16x16x32_bf16 v[72:75], v[104:107], v[100:103], v[72:75]\n" \
"s_waitcnt lgkmcnt(4)\nds_write2_b32 v27, v44, v45 offset0:128 offset1:132\nv_mfma_f32_16x16x32_bf16 v[84:87], v[64:67], v[100:103], v[84:87]\nds_read_b128 v[32:35], v24 offset:32768\n" \
"v_mfma_f32_16x16x32_bf16 v[72:75], v[48:51], v[56:59], v[72:75]\ns_waitcnt lgkmcnt(4)\ns_cmp_eq_u64 vcc, 0\ns_cselect_b32 s14, s10, 0\n" \
"v_mfma_f32_16x16x32_bf16 v[84:87], v[76:79], v[56:59], v[84:87]\ns_cmp_eq_u32 s51, 0\ns_cbranch_scc1 4f\n5:\n" \
"v_mfma_f32_16x16x32_bf16 v[72:75], v[52:55], v[36:39], v[72:75]\ns_waitcnt lgkmcnt(1)\ns_barrier\nds_read_b128 v[44:47], v28 offset:512\n" \
"ds_read_b128 v[52:55], v28 offset:1536\nv_mfma_f32_16x16x32_bf16 v[84:87], v[40:43], v[36:39], v[84:87]\nds_read_b128 v[60:63], v28 offset:2560\nds_read_b128 v[64:67], v28 offset:3584\n" \
"v_mfma_f32_16x16x32_bf16 v[40:43], v[0:3], v[80:83], 0\nds_read_b128 v[76:79], v24 offset:1024\nv_max3_f32 v31, v72, v73, v74\nv_max3_f32 v31, v31, v75, s47\n" \
"v_mfma_f32_16x16x32_bf16 v[48:51], a[96:99], v[80:83], 0\ns_waitcnt lgkmcnt(4)\nds_read_b128 v[88:91], v24 offset:33792\nv_max3_f32 v92, v84, v85, v86\n" \
"v_max3_f32 v93, v31, v92, v87\nv_max3_f32 v31, v31, v92, v87\nv_mul_f32 v92, s46, v31\nv_fma_f32 v72, v72, s46, -v92\n" \
"v_mfma_f32_16x16x32_bf16 v[96:99], v[44:47], v[68:71], 0\ns_waitcnt lgkmcnt(4)\nv_permlane32_swap_b32 v31, v93\nv_exp_f32 v72, v72\n" \
"v_fma_f32 v74, v74, s46, -v92\nv_mfma_f32_16x16x32_bf16 v[96:99], v[52:55], v[100:103], v[96:99]\ns_waitcnt lgkmcnt(2)\nv_exp_f32 v74, v74\n" \
"v_mfma_f32_16x16x32_bf16 v[96:99], v[60:63], v[56:59], v[96:99]\nv_fma_f32 v73, v73, s46, -v92\nv_fma_f32 v75, v75, s46, -v92\nv_mfma_f32_16x16x32_bf16 v[96:99], v[64:67], v[36:39], v[96:99]\n" \
"v_fma_f32 v84, v84, s46, -v92\nv_fma_f32 v86, v86, s46, -v92\nv_mfma_f32_16x16x32_bf16 v[56:59], a[208:211], v[80:83], 0\nv_exp_f32 v73, v73\n" \
"v_mfma_f32_16x16x32_bf16 v[64:67], v[160:163], v[80:83], 0\nv_max3_f32 v36, v96, v97, v98\nv_max3_f32 v31, v31, v93, v99\nv_max_f32 v37, v36, v31\n" \
"v_max_f32 v36, v36, v31\nv_exp_f32 v75, v75\nv_fma_f32 v85, v85, s46, -v92\nv_fma_f32 v87, v87, s46, -v92\n" \
"v_mfma_f32_16x16x32_bf16 v[44:47], v[0:3], v[32:35], 0\nv_permlane16_swap_b32 v36, v37\nv_max_f32 v36, v36, v37\nv_exp_f32 v84, v84\n" \
"v_exp_f32 v86, v86\nv_cndmask_b32 v38, v96, v98, s[26:27]\nv_fma_f32 v92, v36, -s46, v92\nv_mul_f32 v36, s46, v36\n" \
"v_exp_f32 v85, v85\nv_exp_f32 v87, v87\nv_cndmask_b32 v39, v97, v99, s[26:27]\nv_fma_f32 v38, v38, s46, -v36\n" \
"v_add_f32 v80, v72, v74\nv_add_f32 v81, v73, v75\nv_mfma_f32_16x16x32_bf16 v[52:55], a[96:99], v[32:35], 0\nv_fma_f32 v39, v39, s46, -v36\n" \
"v_add_f32 v82, v84, v86\nv_add_f32 v80, v80, v81\nv_cndmask_b32 v96, v72, v74, s[24:25]\nv_cndmask_b32 v97, v73, v75, s[24:25]\n" \
"v_cndmask_b32 v98, v84, v86, s[24:25]\nv_cndmask_b32 v99, v85, v87, s[24:25]\nv_exp_f32 v92, v92\nv_exp_f32 v38, v38\n" \
"v_add_f32 v83, v85, v87\nv_add_f32 v80, v80, v82\nv_mfma_f32_16x16x32_bf16 v[60:63], a[208:211], v[32:35], 0\ns_waitcnt lgkmcnt(0)\n" \
"v_exp_f32 v39, v39\nv_add_f32 v80, v80, v83\nv_fma_f32 v80, v80, v92, v38\nv_mfma_f32_16x16x32_bf16 v[68:71], v[160:163], v[32:35], 0\n" \
"v_add_f32 v81, v80, v39\nv_add_f32 v80, v39, v80\nv_mfma_f32_16x16x32_bf16 v[40:43], v[4:7], v[76:79], v[40:43]\nds_read_b128 v[32:35], v24 offset:2048\n" \
"v_permlane16_swap_b32 v80, v81\nv_mfma_f32_16x16x32_bf16 v[44:47], v[4:7], v[88:91], v[44:47]\nds_read_b128 v[100:103], v24 offset:34816\nv_add_f32 v82, v80, v81\n" \
"v_add_f32 v80, v81, v80\nv_mfma_f32_16x16x32_bf16 v[48:51], a[100:103], v[76:79], v[48:51]\nv_mfma_f32_16x16x32_bf16 v[52:55], a[100:103], v[88:91], v[52:55]\nv_permlane32_swap_b32 v80, v82\n" \
"v_add_f32 v80, v80, v82\nv_mfma_f32_16x16x32_bf16 v[56:59], a[212:215], v[76:79], v[56:59]\nv_rcp_f32 v80, v80\nv_mfma_f32_16x16x32_bf16 v[60:63], a[212:215], v[88:91], v[60:63]\n" \
"v_mul_f32 v92, v92, v80\nv_mul_f32 v96, v96, v92\nv_mul_f32 v97, v97, v92\nv_mul_f32 v38, v38, v80\n" \
"v_mul_f32 v39, v39, v80\nv_mfma_f32_16x16x32_bf16 v[64:67], v[164:167], v[76:79], v[64:67]\ns_waitcnt lgkmcnt(0)\nv_mul_f32 v98, v98, v92\n" \
"v_mul_f32 v99, v99, v92\nv_cvt_pk_bf16_f32 v36, v96, v97\nv_cvt_pk_bf16_f32 v31, v38, v39\nv_cvt_pk_bf16_f32 v37, v98, v99\n" \
"v_and_or_b32 v36, v36, -2, s95\nv_and_or_b32 v31, v31, -2, s95\nv_mfma_f32_16x16x32_bf16 v[68:71], v[164:167], v[88:91], v[68:71]\nbuffer_store_dwordx2 v[36:37], v29, s[8:11], s99 offen offset:0 sc1\n" \
"buffer_store_dword v31, v30, s[8:11], s96 offen offset:1536 sc1\nv_mfma_f32_16x16x32_bf16 v[40:43], a[0:3], v[32:35], v[40:43]\nds_read_b128 v[36:39], v24 offset:3072\nv_mul_f32 v72, v72, v92\n" \
"v_mfma_f32_16x16x32_bf16 v[44:47], a[0:3], v[100:103], v[44:47]\nds_read_b128 v[76:79], v24 offset:35840\nv_mul_f32 v73, v73, v92\nv_mfma_f32_16x16x32_bf16 v[48:51], v[8:11], v[32:35], v[48:51]\n" \
"v_mul_f32 v74, v74, v92\nv_mul_f32 v75, v75, v92\nv_mfma_f32_16x16x32_bf16 v[52:55], v[8:11], v[100:103], v[52:55]\nv_cvt_pk_bf16_f32 v80, v72, v73\n" \
"v_cvt_pk_bf16_f32 v81, v74, v75\nv_mfma_f32_16x16x32_bf16 v[56:59], a[216:219], v[32:35], v[56:59]\nv_and_or_b32 v80, v80, -2, s95\nv_mul_f32 v84, v84, v92\n" \
"v_mfma_f32_16x16x32_bf16 v[60:63], a[216:219], v[100:103], v[60:63]\nbuffer_store_dwordx2 v[80:81], v29, s[8:11], s97 offen offset:1024 sc1\nv_mul_f32 v85, v85, v92\nv_mfma_f32_16x16x32_bf16 v[64:67], v[168:171], v[32:35], v[64:67]\n" \
"s_add_u32 m0, s32, 0x4000\nv_mul_f32 v86, v86, v92\nv_mul_f32 v87, v87, v92\nv_mfma_f32_16x16x32_bf16 v[68:71], v[168:171], v[100:103], v[68:71]\n" \
"s_waitcnt lgkmcnt(0)\nds_read_b128 v[32:35], v24 offset:4096\nv_mfma_f32_16x16x32_bf16 v[40:43], a[4:7], v[36:39], v[40:43]\nds_read_b128 v[72:75], v24 offset:36864\n" \
"v_cvt_pk_bf16_f32 v82, v84, v85\nv_mfma_f32_16x16x32_bf16 v[44:47], a[4:7], v[76:79], v[44:47]\ns_add_u32 s99, s31, 0x4000\nv_cvt_pk_bf16_f32 v83, v86, v87\n" \
"v_mfma_f32_16x16x32_bf16 v[48:51], v[12:15], v[36:39], v[48:51]\nbuffer_store_dwordx4 v[80:83], v24, s[8:11], s98 offen offset:0 sc1\nbuffer_load_dwordx4 v24, s[8:11], s99 offen offset:0 sc1 lds\nv_mfma_f32_16x16x32_bf16 v[52:55], v[12:15], v[76:79], v[52:55]\n" \
"v_mfma_f32_16x16x32_bf16 v[56:59], a[220:223], v[36:39], v[56:59]\nv_mfma_f32_16x16x32_bf16 v[60:63], a[220:223], v[76:79], v[60:63]\nbuffer_load_dwordx4 v24, s[8:11], s99 offen offset:1024 sc1 lds\nv_mfma_f32_16x16x32_bf16 v[64:67], v[172:175], v[36:39], v[64:67]\n" \
"s_waitcnt lgkmcnt(0)\nv_mfma_f32_16x16x32_bf16 v[68:71], v[172:175], v[76:79], v[68:71]\nds_read_b128 v[36:39], v24 offset:5120\nv_mfma_f32_16x16x32_bf16 v[40:43], a[8:11], v[32:35], v[40:43]\n" \
"ds_read_b128 v[76:79], v24 offset:37888\nv_mfma_f32_16x16x32_bf16 v[44:47], a[8:11], v[72:75], v[44:47]\nbuffer_load_dwordx4 v24, s[8:11], s99 offen offset:2048 sc1 lds\nv_mfma_f32_16x16x32_bf16 v[48:51], a[104:107], v[32:35], v[48:51]\n" \
"v_mfma_f32_16x16x32_bf16 v[52:55], a[104:107], v[72:75], v[52:55]\nv_mfma_f32_16x16x32_bf16 v[56:59], v[16:19], v[32:35], v[56:59]\nv_mfma_f32_16x16x32_bf16 v[60:63], v[16:19], v[72:75], v[60:63]\nbuffer_load_dwordx4 v24, s[8:11], s99 offen offset:3072 sc1 lds\n" \
"v_mfma_f32_16x16x32_bf16 v[64:67], v[176:179], v[32:35], v[64:67]\ns_waitcnt lgkmcnt(0)\nv_mfma_f32_16x16x32_bf16 v[68:71], v[176:179], v[72:75], v[68:71]\nds_read_b128 v[32:35], v24 offset:6144\n" \
"v_mfma_f32_16x16x32_bf16 v[40:43], a[12:15], v[36:39], v[40:43]\nds_read_b128 v[72:75], v24 offset:38912\nv_mfma_f32_16x16x32_bf16 v[44:47], a[12:15], v[76:79], v[44:47]\nv_mfma_f32_16x16x32_bf16 v[48:51], a[108:111], v[36:39], v[48:51]\n" \
"v_mfma_f32_16x16x32_bf16 v[52:55], a[108:111], v[76:79], v[52:55]\nv_mfma_f32_16x16x32_bf16 v[56:59], v[20:23], v[36:39], v[56:59]\nv_mfma_f32_16x16x32_bf16 v[60:63], v[20:23], v[76:79], v[60:63]\nv_mfma_f32_16x16x32_bf16 v[64:67], v[180:183], v[36:39], v[64:67]\n" \
"s_waitcnt lgkmcnt(0)\nv_mfma_f32_16x16x32_bf16 v[68:71], v[180:183], v[76:79], v[68:71]\nds_read_b128 v[36:39], v24 offset:7168\nv_mfma_f32_16x16x32_bf16 v[40:43], a[16:19], v[32:35], v[40:43]\n" \
"ds_read_b128 v[76:79], v24 offset:39936\nv_mfma_f32_16x16x32_bf16 v[44:47], a[16:19], v[72:75], v[44:47]\ns_add_u32 m0, s32, 0x6000\ns_add_u32 s99, s31, 0x6000\n" \
"v_mfma_f32_16x16x32_bf16 v[48:51], a[112:115], v[32:35], v[48:51]\nv_mfma_f32_16x16x32_bf16 v[52:55], a[112:115], v[72:75], v[52:55]\nv_mfma_f32_16x16x32_bf16 v[56:59], a[224:227], v[32:35], v[56:59]\nds_read_b128 v[80:83], v25 offset:0\n" \
"v_mfma_f32_16x16x32_bf16 v[60:63], a[224:227], v[72:75], v[60:63]\nbuffer_load_dwordx4 v24, s[8:11], s99 offen offset:0 sc1 lds\nv_mfma_f32_16x16x32_bf16 v[64:67], v[184:187], v[32:35], v[64:67]\ns_waitcnt lgkmcnt(1)\n" \
"v_mfma_f32_16x16x32_bf16 v[68:71], v[184:187], v[72:75], v[68:71]\nds_read_b128 v[32:35], v24 offset:8192\nv_mfma_f32_16x16x32_bf16 v[40:43], a[20:23], v[36:39], v[40:43]\nds_read_b128 v[72:75], v24 offset:40960\n" \
"v_mfma_f32_16x16x32_bf16 v[44:47], a[20:23], v[76:79], v[44:47]\nbuffer_load_dwordx4 v24, s[8:11], s99 offen offset:1024 sc1 lds\nv_mfma_f32_16x16x32_bf16 v[48:51], a[116:119], v[36:39], v[48:51]\nv_mfma_f32_16x16x32_bf16 v[52:55], a[116:119], v[76:79], v[52:55]\n" \
"v_mfma_f32_16x16x32_bf16 v[56:59], a[228:231], v[36:39], v[56:59]\nds_read_b128 v[84:87], v25 offset:1024\nv_mfma_f32_16x16x32_bf16 v[60:63], a[228:231], v[76:79], v[60:63]\nbuffer_load_dwordx4 v24, s[8:11], s99 offen offset:2048 sc1 lds\n" \
"v_mfma_f32_16x16x32_bf16 v[64:67], v[188:191], v[36:39], v[64:67]\ns_waitcnt lgkmcnt(1)\nv_mfma_f32_16x16x32_bf16 v[68:71], v[188:191], v[76:79], v[68:71]\nds_read_b128 v[36:39], v24 offset:9216\n" \
"v_mfma_f32_16x16x32_bf16 v[40:43], v[80:83], v[32:35], v[40:43]\nds_read_b128 v[76:79], v24 offset:41984\nv_mfma_f32_16x16x32_bf16 v[44:47], v[80:83], v[72:75], v[44:47]\nbuffer_load_dwordx4 v24, s[8:11], s99 offen offset:3072 sc1 lds\n" \
"v_mfma_f32_16x16x32_bf16 v[48:51], a[120:123], v[32:35], v[48:51]\nv_mfma_f32_16x16x32_bf16 v[52:55], a[120:123], v[72:75], v[52:55]\nv_mfma_f32_16x16x32_bf16 v[56:59], a[232:235], v[32:35], v[56:59]\nv_mfma_f32_16x16x32_bf16 v[60:63], a[232:235], v[72:75], v[60:63]\n" \
"v_mfma_f32_16x16x32_bf16 v[64:67], v[192:195], v[32:35], v[64:67]\ns_waitcnt lgkmcnt(0)\nv_mfma_f32_16x16x32_bf16 v[68:71], v[192:195], v[72:75], v[68:71]\nds_read_b128 v[32:35], v24 offset:10240\n" \
"v_mfma_f32_16x16x32_bf16 v[40:43], v[84:87], v[36:39], v[40:43]\nds_read_b128 v[72:75], v24 offset:43008\nv_mfma_f32_16x16x32_bf16 v[44:47], v[84:87], v[76:79], v[44:47]\nv_mfma_f32_16x16x32_bf16 v[48:51], a[124:127], v[36:39], v[48:51]\n" \
"ds_read_b128 v[80:83], v25 offset:14336\nv_mfma_f32_16x16x32_bf16 v[52:55], a[124:127], v[76:79], v[52:55]\nv_mfma_f32_16x16x32_bf16 v[56:59], a[236:239], v[36:39], v[56:59]\nv_mfma_f32_16x16x32_bf16 v[60:63], a[236:239], v[76:79], v[60:63]\n" \
"v_mfma_f32_16x16x32_bf16 v[64:67], v[196:199], v[36:39], v[64:67]\ns_waitcnt lgkmcnt(1)\nv_mfma_f32_16x16x32_bf16 v[68:71], v[196:199], v[76:79], v[68:71]\nds_read_b128 v[36:39], v24 offset:11264\n" \
"v_mfma_f32_16x16x32_bf16 v[40:43], a[24:27], v[32:35], v[40:43]\nds_read_b128 v[76:79], v24 offset:44032\nv_mfma_f32_16x16x32_bf16 v[44:47], a[24:27], v[72:75], v[44:47]\nv_mfma_f32_16x16x32_bf16 v[48:51], a[128:131], v[32:35], v[48:51]\n" \
"ds_read_b128 v[84:87], v25 offset:15360\nv_mfma_f32_16x16x32_bf16 v[52:55], a[128:131], v[72:75], v[52:55]\nv_mfma_f32_16x16x32_bf16 v[56:59], a[240:243], v[32:35], v[56:59]\ns_waitcnt lgkmcnt(3)\n" \
"v_mfma_f32_16x16x32_bf16 v[60:63], a[240:243], v[72:75], v[60:63]\nv_mfma_f32_16x16x32_bf16 v[64:67], v[80:83], v[32:35], v[64:67]\ns_waitcnt lgkmcnt(1)\nv_mfma_f32_16x16x32_bf16 v[68:71], v[80:83], v[72:75], v[68:71]\n" \
"ds_read_b128 v[32:35], v24 offset:12288\nv_mfma_f32_16x16x32_bf16 v[40:43], a[28:31], v[36:39], v[40:43]\nds_read_b128 v[72:75], v24 offset:45056\nv_mfma_f32_16x16x32_bf16 v[44:47], a[28:31], v[76:79], v[44:47]\n" \
"ds_read_b128 v[80:83], v25 offset:8192\nv_mfma_f32_16x16x32_bf16 v[48:51], a[132:135], v[36:39], v[48:51]\nv_mfma_f32_16x16x32_bf16 v[52:55], a[132:135], v[76:79], v[52:55]\nv_mfma_f32_16x16x32_bf16 v[56:59], a[244:247], v[36:39], v[56:59]\n" \
"s_waitcnt lgkmcnt(3)\nv_mfma_f32_16x16x32_bf16 v[60:63], a[244:247], v[76:79], v[60:63]\nv_mfma_f32_16x16x32_bf16 v[64:67], v[84:87], v[36:39], v[64:67]\ns_waitcnt lgkmcnt(1)\n" \
"v_mfma_f32_16x16x32_bf16 v[68:71], v[84:87], v[76:79], v[68:71]\nds_read_b128 v[36:39], v24 offset:13312\nv_mfma_f32_16x16x32_bf16 v[40:43], a[32:35], v[32:35], v[40:43]\nds_read_b128 v[76:79], v24 offset:46080\n" \
"v_mfma_f32_16x16x32_bf16 v[44:47], a[32:35], v[72:75], v[44:47]\nds_read_b128 v[84:87], v25 offset:9216\nv_mfma_f32_16x16x32_bf16 v[48:51], a[136:139], v[32:35], v[48:51]\ns_waitcnt lgkmcnt(3)\n" \
"v_mfma_f32_16x16x32_bf16 v[52:55], a[136:139], v[72:75], v[52:55]\nv_mfma_f32_16x16x32_bf16 v[56:59], v[80:83], v[32:35], v[56:59]\nv_mfma_f32_16x16x32_bf16 v[60:63], v[80:83], v[72:75], v[60:63]\nv_mfma_f32_16x16x32_bf16 v[64:67], v[200:203], v[32:35], v[64:67]\n" \
"s_waitcnt lgkmcnt(1)\nv_mfma_f32_16x16x32_bf16 v[68:71], v[200:203], v[72:75], v[68:71]\nds_read_b128 v[32:35], v24 offset:14336\nv_mfma_f32_16x16x32_bf16 v[40:43], a[36:39], v[36:39], v[40:43]\n" \
"ds_read_b128 v[72:75], v24 offset:47104\nv_mfma_f32_16x16x32_bf16 v[44:47], a[36:39], v[76:79], v[44:47]\nv_mfma_f32_16x16x32_bf16 v[48:51], a[140:143], v[36:39], v[48:51]\ns_waitcnt lgkmcnt(2)\n" \
"ds_read_b128 v[80:83], v25 offset:16384\nv_mfma_f32_16x16x32_bf16 v[52:55], a[140:143], v[76:79], v[52:55]\nv_mfma_f32_16x16x32_bf16 v[56:59], v[84:87], v[36:39], v[56:59]\nv_mfma_f32_16x16x32_bf16 v[60:63], v[84:87], v[76:79], v[60:63]\n" \
"v_mfma_f32_16x16x32_bf16 v[64:67], v[204:207], v[36:39], v[64:67]\ns_waitcnt lgkmcnt(1)\nv_mfma_f32_16x16x32_bf16 v[68:71], v[204:207], v[76:79], v[68:71]\nds_read_b128 v[36:39], v24 offset:15360\n" \
"v_mfma_f32_16x16x32_bf16 v[40:43], a[40:43], v[32:35], v[40:43]\nds_read_b128 v[76:79], v24 offset:48128\nv_mfma_f32_16x16x32_bf16 v[44:47], a[40:43], v[72:75], v[44:47]\nv_mfma_f32_16x16x32_bf16 v[48:51], a[144:147], v[32:35], v[48:51]\n" \
"ds_read_b128 v[84:87], v25 offset:17408\nv_mfma_f32_16x16x32_bf16 v[52:55], a[144:147], v[72:75], v[52:55]\nv_mfma_f32_16x16x32_bf16 v[56:59], a[248:251], v[32:35], v[56:59]\ns_waitcnt lgkmcnt(3)\n" \
"v_mfma_f32_16x16x32_bf16 v[60:63], a[248:251], v[72:75], v[60:63]\ns_waitcnt vmcnt(4) lgkmcnt(1)\ns_barrier\nv_mfma_f32_16x16x32_bf16 v[64:67], v[80:83], v[32:35], v[64:67]\n" \
"s_add_u32 m0, s32, 0\ns_add_u32 s99, s31, 0x10000\nv_mfma_f32_16x16x32_bf16 v[68:71], v[80:83], v[72:75], v[68:71]\nds_read_b128 v[32:35], v24 offset:16384\n" \
"v_mfma_f32_16x16x32_bf16 v[40:43], a[44:47], v[36:39], v[40:43]\nds_read_b128 v[72:75], v24 offset:49152\nv_mfma_f32_16x16x32_bf16 v[44:47], a[44:47], v[76:79], v[44:47]\nds_read_b128 v[80:83], v25 offset:10240\n" \
"v_mfma_f32_16x16x32_bf16 v[48:51], a[148:151], v[36:39], v[48:51]\nbuffer_load_dwordx4 v24, s[12:15], s99 offen offset:0 sc1 lds\nv_mfma_f32_16x16x32_bf16 v[52:55], a[148:151], v[76:79], v[52:55]\nv_mfma_f32_16x16x32_bf16 v[56:59], a[252:255], v[36:39], v[56:59]\n" \
"s_waitcnt lgkmcnt(3)\nv_mfma_f32_16x16x32_bf16 v[60:63], a[252:255], v[76:79], v[60:63]\nbuffer_load_dwordx4 v24, s[12:15], s99 offen offset:1024 sc1 lds\nv_mfma_f32_16x16x32_bf16 v[64:67], v[84:87], v[36:39], v[64:67]\n" \
"s_waitcnt lgkmcnt(1)\nv_mfma_f32_16x16x32_bf16 v[68:71], v[84:87], v[76:79], v[68:71]\nds_read_b128 v[36:39], v24 offset:17408\nv_mfma_f32_16x16x32_bf16 v[40:43], a[48:51], v[32:35], v[40:43]\n" \
"ds_read_b128 v[76:79], v24 offset:50176\nv_mfma_f32_16x16x32_bf16 v[44:47], a[48:51], v[72:75], v[44:47]\nds_read_b128 v[84:87], v25 offset:11264\nv_mfma_f32_16x16x32_bf16 v[48:51], a[152:155], v[32:35], v[48:51]\n" \
"s_waitcnt lgkmcnt(3)\nbuffer_load_dwordx4 v24, s[12:15], s99 offen offset:2048 sc1 lds\nv_mfma_f32_16x16x32_bf16 v[52:55], a[152:155], v[72:75], v[52:55]\nv_mfma_f32_16x16x32_bf16 v[56:59], v[80:83], v[32:35], v[56:59]\n" \
"v_mfma_f32_16x16x32_bf16 v[60:63], v[80:83], v[72:75], v[60:63]\nbuffer_load_dwordx4 v24, s[12:15], s99 offen offset:3072 sc1 lds\nv_mfma_f32_16x16x32_bf16 v[64:67], v[208:211], v[32:35], v[64:67]\ns_waitcnt lgkmcnt(1)\n" \
"ds_read_b128 v[32:35], v25 offset:6144\nv_mfma_f32_16x16x32_bf16 v[68:71], v[208:211], v[72:75], v[68:71]\nds_read_b128 v[80:83], v24 offset:18432\nv_mfma_f32_16x16x32_bf16 v[40:43], a[52:55], v[36:39], v[40:43]\n" \
"ds_read_b128 v[88:91], v24 offset:51200\ns_add_u32 m0, s32, 0x2000\nv_mfma_f32_16x16x32_bf16 v[44:47], a[52:55], v[76:79], v[44:47]\ns_add_u32 s99, s31, 0x12000\n" \
"v_mfma_f32_16x16x32_bf16 v[48:51], a[156:159], v[36:39], v[48:51]\ns_waitcnt lgkmcnt(3)\nv_mfma_f32_16x16x32_bf16 v[52:55], a[156:159], v[76:79], v[52:55]\nv_mfma_f32_16x16x32_bf16 v[56:59], v[84:87], v[36:39], v[56:59]\n" \
"buffer_load_dwordx4 v24, s[12:15], s99 offen offset:0 sc1 lds\nv_mfma_f32_16x16x32_bf16 v[60:63], v[84:87], v[76:79], v[60:63]\nv_mfma_f32_16x16x32_bf16 v[64:67], v[212:215], v[36:39], v[64:67]\ns_waitcnt lgkmcnt(0)\n" \
"ds_read_b128 v[84:87], v25 offset:7168\nv_mfma_f32_16x16x32_bf16 v[68:71], v[212:215], v[76:79], v[68:71]\nds_read_b128 v[92:95], v24 offset:19456\nv_mfma_f32_16x16x32_bf16 v[40:43], a[56:59], v[80:83], v[40:43]\n" \
"ds_read_b128 v[76:79], v24 offset:52224\nv_mfma_f32_16x16x32_bf16 v[44:47], a[56:59], v[88:91], v[44:47]\nbuffer_load_dwordx4 v24, s[12:15], s99 offen offset:1024 sc1 lds\nv_mfma_f32_16x16x32_bf16 v[48:51], v[32:35], v[80:83], v[48:51]\n" \
"v_mfma_f32_16x16x32_bf16 v[52:55], v[32:35], v[88:91], v[52:55]\nv_mfma_f32_16x16x32_bf16 v[56:59], v[112:115], v[80:83], v[56:59]\nds_read_b128 v[72:75], v25 offset:2048\nv_mfma_f32_16x16x32_bf16 v[60:63], v[112:115], v[88:91], v[60:63]\n" \
"buffer_load_dwordx4 v24, s[12:15], s99 offen offset:2048 sc1 lds\nv_mfma_f32_16x16x32_bf16 v[64:67], v[216:219], v[80:83], v[64:67]\ns_waitcnt lgkmcnt(1)\nv_mfma_f32_16x16x32_bf16 v[68:71], v[216:219], v[88:91], v[68:71]\n" \
"ds_read_b128 v[32:35], v24 offset:20480\nv_mfma_f32_16x16x32_bf16 v[40:43], a[60:63], v[92:95], v[40:43]\nds_read_b128 v[36:39], v24 offset:53248\nv_mfma_f32_16x16x32_bf16 v[44:47], a[60:63], v[76:79], v[44:47]\n" \
"buffer_load_dwordx4 v24, s[12:15], s99 offen offset:3072 sc1 lds\nv_mfma_f32_16x16x32_bf16 v[48:51], v[84:87], v[92:95], v[48:51]\nv_mfma_f32_16x16x32_bf16 v[52:55], v[84:87], v[76:79], v[52:55]\nv_mfma_f32_16x16x32_bf16 v[56:59], v[116:119], v[92:95], v[56:59]\n" \
"v_mfma_f32_16x16x32_bf16 v[60:63], v[116:119], v[76:79], v[60:63]\nv_mfma_f32_16x16x32_bf16 v[64:67], v[220:223], v[92:95], v[64:67]\nv_mfma_f32_16x16x32_bf16 v[68:71], v[220:223], v[76:79], v[68:71]\ns_branch 1b\n" \
"2:\ns_add_u32 s52, s52, 1\ns_cmp_lt_u32 s52, 0x40000\ns_cbranch_scc0 9f\n" \
"v_cmp_eq_u32 vcc, -1, v72\ns_cbranch_vccz 6f\nbuffer_load_dwordx4 v24, s[8:11], s94 offen offset:0 sc1 lds\n6:\n" \
"v_cmp_eq_u32 vcc, -1, v76\ns_cbranch_vccz 6f\nbuffer_load_dwordx4 v24, s[8:11], s94 offen offset:1024 sc1 lds\n6:\n" \
"v_cmp_eq_u32 vcc, -1, v84\ns_cbranch_vccz 6f\nbuffer_load_dwordx4 v24, s[8:11], s94 offen offset:2048 sc1 lds\n6:\n" \
"v_cmp_eq_u32 vcc, -1, v80\ns_cbranch_vccz 6f\nbuffer_load_dwordx4 v24, s[8:11], s94 offen offset:3072 sc1 lds\n6:\n" \
"s_waitcnt vmcnt(0)\nds_read_b128 v[72:75], v31 offset:16384\nds_read_b128 v[76:79], v31 offset:17408\nds_read_b128 v[84:87], v31 offset:18432\n" \
"ds_read_b128 v[80:83], v31 offset:19456\ns_waitcnt lgkmcnt(0)\nv_max3_u32 v96, v72, v76, v84\nv_max_u32 v96, v96, v80\n" \
"v_cmp_eq_u32 vcc, -1, v96\ns_cbranch_vccnz 2b\ns_branch 3b\n4:\n" \
"s_cmp_gt_u32 s30, 64\ns_cbranch_scc1 9f\ns_cmp_eq_u32 s30, 64\ns_cbranch_scc1 5b\n" \
"v_bfe_u32 v44, v24, 6, 3\nv_lshlrev_b32 v44, 2, v44\ns_lshl_b32 s94, s30, 5\ns_add_u32 s94, s94, s48\n" \
"7:\nbuffer_load_dword v45, v44, s[8:11], s94 offen sc1\ns_waitcnt vmcnt(0)\nv_cmp_eq_u32 vcc, -1, v45\n" \
"s_cbranch_vccz 8f\ns_add_u32 s52, s52, 1\ns_cmp_lt_u32 s52, 0x40000\ns_cbranch_scc0 9f\n" \
"s_sleep 8\ns_branch 7b\n8:\ns_mov_b32 m0, s53\n" \
"s_add_u32 s94, s30, 1\ns_min_u32 s94, s94, 63\ns_lshl_b32 s94, s94, 5\ns_add_u32 s94, s94, s48\n" \
"buffer_load_dword v44, s[8:11], s94 offen offset:0 sc1 lds\ns_add_u32 m0, s32, 0\ns_add_u32 s94, s31, 0\nbuffer_load_dwordx4 v24, s[8:11], s94 offen offset:0 sc1 lds\n" \
"buffer_load_dwordx4 v24, s[8:11], s94 offen offset:1024 sc1 lds\nbuffer_load_dwordx4 v24, s[8:11], s94 offen offset:2048 sc1 lds\nbuffer_load_dwordx4 v24, s[8:11], s94 offen offset:3072 sc1 lds\ns_add_u32 m0, s32, 0x2000\n" \
"s_add_u32 s94, s31, 0x2000\nbuffer_load_dwordx4 v24, s[8:11], s94 offen offset:0 sc1 lds\nbuffer_load_dwordx4 v24, s[8:11], s94 offen offset:1024 sc1 lds\nbuffer_load_dwordx4 v24, s[8:11], s94 offen offset:2048 sc1 lds\n" \
"buffer_load_dwordx4 v24, s[8:11], s94 offen offset:3072 sc1 lds\nv_lshrrev_b32 v44, 2, v24\nv_add_u32 v44, 0x11000, v44\ns_waitcnt vmcnt(0) lgkmcnt(0)\n" \
"s_barrier\nds_read_b32 v45, v44 offset:0\nds_read_b128 v[80:83], v24 offset:0\nds_read_b128 v[32:35], v24 offset:32768\n" \
"s_waitcnt lgkmcnt(0)\nv_cmp_eq_u32 vcc, -1, v45\ns_cmp_eq_u64 vcc, 0\ns_cselect_b32 s14, s10, 0\n" \
"s_branch 5b\n9:\ns_waitcnt vmcnt(0)\ns_endpgm\n"
#define B_LDS 0x44840c0202a00110ull
#define B_VAR 0x0000000000010001ull
#define B_LV 8192
#define B_LOOP \
"v_mov_b32 v33, v20\ns_branch 4f\n.p2align 6\n1:\n" \
"s_waitcnt lgkmcnt(4)\nv_mfma_f32_16x16x32_bf16 v[44:47], a[8:11], v[28:31], v[44:47]\ns_add_u32 m0, s22, 0\ns_add_u32 s43, s20, 7\n" \
"s_and_b32 s43, s43, 7\nv_mfma_f32_16x16x32_bf16 v[68:71], a[8:11], v[40:43], v[68:71]\ns_lshl_b32 s43, s43, 18\ns_add_u32 s43, s43, s23\n" \
"v_mfma_f32_16x16x32_bf16 v[72:75], a[112:115], v[28:31], v[72:75]\nds_read_b128 v[48:51], v17 offset:0\nv_mfma_f32_16x16x32_bf16 v[20:23], a[112:115], v[40:43], v[20:23]\nv_mfma_f32_16x16x32_bf16 v[44:47], a[12:15], v[36:39], v[44:47]\n" \
"s_waitcnt vmcnt(7) lgkmcnt(1)\ns_barrier\nv_mfma_f32_16x16x32_bf16 v[68:71], a[12:15], v[32:35], v[68:71]\nds_read_b128 v[28:31], v16 offset:8192\n" \
"v_mfma_f32_16x16x32_bf16 v[72:75], a[116:119], v[36:39], v[72:75]\nds_read_b128 v[40:43], v16 offset:40960\nv_mfma_f32_16x16x32_bf16 v[20:23], a[116:119], v[32:35], v[20:23]\nds_read_b128 v[36:39], v16 offset:9216\n" \
"v_mfma_f32_16x16x32_bf16 v[44:47], a[16:19], v[52:55], v[44:47]\nds_read_b128 v[32:35], v17 offset:1024\nv_mfma_f32_16x16x32_bf16 v[68:71], a[16:19], v[24:27], v[68:71]\nbuffer_load_dwordx4 v16, s[8:11], s43 offen offset:0 sc1 lds\n" \
"v_mfma_f32_16x16x32_bf16 v[72:75], a[120:123], v[52:55], v[72:75]\nds_read_b128 v[64:67], v16 offset:41984\nv_mfma_f32_16x16x32_bf16 v[20:23], a[120:123], v[24:27], v[20:23]\nds_read_b128 v[52:55], v16 offset:10240\n" \
"v_mfma_f32_16x16x32_bf16 v[44:47], a[20:23], v[56:59], v[44:47]\nbuffer_load_dwordx4 v16, s[8:11], s43 offen offset:1024 sc1 lds\nv_mfma_f32_16x16x32_bf16 v[68:71], a[20:23], v[60:63], v[68:71]\nds_read_b128 v[24:27], v16 offset:43008\n" \
"v_mfma_f32_16x16x32_bf16 v[72:75], a[124:127], v[56:59], v[72:75]\ns_waitcnt lgkmcnt(3)\nds_read_b128 v[56:59], v17 offset:4096\nv_mfma_f32_16x16x32_bf16 v[20:23], a[124:127], v[60:63], v[20:23]\n" \
"buffer_load_dwordx4 v16, s[8:11], s43 offen offset:2048 sc1 lds\nv_mfma_f32_16x16x32_bf16 v[44:47], v[48:51], v[28:31], v[44:47]\nds_read_b128 v[60:63], v16 offset:11264\nv_mfma_f32_16x16x32_bf16 v[68:71], v[48:51], v[40:43], v[68:71]\n" \
"ds_read_b128 v[76:79], v16 offset:44032\nv_mfma_f32_16x16x32_bf16 v[72:75], a[128:131], v[28:31], v[72:75]\nbuffer_load_dwordx4 v16, s[8:11], s43 offen offset:3072 sc1 lds\nv_mfma_f32_16x16x32_bf16 v[20:23], a[128:131], v[40:43], v[20:23]\n" \
"s_waitcnt lgkmcnt(3)\nds_read_b128 v[28:31], v17 offset:5120\nv_mfma_f32_16x16x32_bf16 v[44:47], v[32:35], v[36:39], v[44:47]\nds_read_b128 v[40:43], v16 offset:12288\n" \
"v_mfma_f32_16x16x32_bf16 v[68:71], v[32:35], v[64:67], v[68:71]\ns_add_u32 m0, s22, 0x6000\nds_read_b128 v[32:35], v16 offset:45056\nv_mfma_f32_16x16x32_bf16 v[72:75], a[132:135], v[36:39], v[72:75]\n" \
"s_add_u32 s42, s21, -0xa000\nv_mfma_f32_16x16x32_bf16 v[20:23], a[132:135], v[64:67], v[20:23]\nds_read_b128 v[36:39], v16 offset:13312\nv_mfma_f32_16x16x32_bf16 v[44:47], a[24:27], v[52:55], v[44:47]\n" \
"s_waitcnt lgkmcnt(4)\nds_read_b128 v[48:51], v16 offset:46080\nv_mfma_f32_16x16x32_bf16 v[68:71], a[24:27], v[24:27], v[68:71]\nbuffer_load_dwordx4 v16, s[12:15], s42 offen offset:0 sc1 lds\n" \
"v_mfma_f32_16x16x32_bf16 v[72:75], v[56:59], v[52:55], v[72:75]\nv_mfma_f32_16x16x32_bf16 v[20:23], v[56:59], v[24:27], v[20:23]\nds_read_b128 v[52:55], v16 offset:14336\nv_mfma_f32_16x16x32_bf16 v[44:47], a[28:31], v[60:63], v[44:47]\n" \
"s_waitcnt lgkmcnt(3)\nbuffer_load_dwordx4 v16, s[12:15], s42 offen offset:1024 sc1 lds\nv_mfma_f32_16x16x32_bf16 v[68:71], a[28:31], v[76:79], v[68:71]\nds_read_b128 v[24:27], v16 offset:47104\n" \
"v_mfma_f32_16x16x32_bf16 v[72:75], v[28:31], v[60:63], v[72:75]\nds_read_b128 v[56:59], v17 offset:6144\nv_mfma_f32_16x16x32_bf16 v[20:23], v[28:31], v[76:79], v[20:23]\nbuffer_load_dwordx4 v16, s[12:15], s42 offen offset:2048 sc1 lds\n" \
"v_mfma_f32_16x16x32_bf16 v[44:47], a[32:35], v[40:43], v[44:47]\nds_read_b128 v[28:31], v16 offset:15360\nv_lshlrev_b32 v60, 5, v16\nv_mfma_f32_16x16x32_bf16 v[68:71], a[32:35], v[32:35], v[68:71]\n" \
"ds_read_b128 v[62:65], v16 offset:48128\nv_and_b32 v61, 0x7000, v60\nv_mfma_f32_16x16x32_bf16 v[72:75], a[136:139], v[40:43], v[72:75]\ns_waitcnt lgkmcnt(4)\n" \
"buffer_load_dwordx4 v16, s[12:15], s42 offen offset:3072 sc1 lds\nv_mfma_f32_16x16x32_bf16 v[20:23], a[136:139], v[32:35], v[20:23]\nds_read_b128 v[40:43], v17 offset:7168\ns_add_u32 m0, s22, 0\n" \
"v_mfma_f32_16x16x32_bf16 v[44:47], a[36:39], v[36:39], v[44:47]\nds_read_b128 v[32:35], v16 offset:16384\nv_lshl_add_u32 v60, v61, 17, v60\nv_mfma_f32_16x16x32_bf16 v[68:71], a[36:39], v[48:51], v[68:71]\n" \
"ds_read_b128 v[76:79], v17 offset:2048\nv_mfma_f32_16x16x32_bf16 v[72:75], a[140:143], v[36:39], v[72:75]\nds_read_b128 v[80:83], v16 offset:49152\nv_mfma_f32_16x16x32_bf16 v[20:23], a[140:143], v[48:51], v[20:23]\n" \
"s_waitcnt lgkmcnt(4)\nds_read_b128 v[36:39], v16 offset:17408\nv_mfma_f32_16x16x32_bf16 v[44:47], a[40:43], v[52:55], v[44:47]\nds_read_b128 v[48:51], v17 offset:3072\n" \
"v_mfma_f32_16x16x32_bf16 v[68:71], a[40:43], v[24:27], v[68:71]\nds_read_b128 v[84:87], v16 offset:50176\nv_mfma_f32_16x16x32_bf16 v[72:75], v[56:59], v[52:55], v[72:75]\nv_mfma_f32_16x16x32_bf16 v[20:23], v[56:59], v[24:27], v[20:23]\n" \
"ds_read_b128 v[52:55], v16 offset:18432\nv_mfma_f32_16x16x32_bf16 v[44:47], a[44:47], v[28:31], v[44:47]\ns_waitcnt lgkmcnt(4)\nds_read_b128 v[24:27], v16 offset:51200\n" \
"v_mfma_f32_16x16x32_bf16 v[68:71], a[44:47], v[62:65], v[68:71]\nds_read_b128 v[56:59], v17 offset:8192\nv_add_u32 v61, s22, v16\nv_mfma_f32_16x16x32_bf16 v[72:75], v[40:43], v[28:31], v[72:75]\n" \
"v_mfma_f32_16x16x32_bf16 v[20:23], v[40:43], v[62:65], v[20:23]\nds_read_b128 v[28:31], v16 offset:19456\nv_mfma_f32_16x16x32_bf16 v[44:47], v[76:79], v[32:35], v[44:47]\nds_read_b128 v[40:43], v16 offset:52224\n" \
"v_mfma_f32_16x16x32_bf16 v[68:71], v[76:79], v[80:83], v[68:71]\nds_read_b128 v[62:65], v17 offset:9216\nv_mfma_f32_16x16x32_bf16 v[72:75], a[144:147], v[32:35], v[72:75]\ns_waitcnt lgkmcnt(5)\n" \
"ds_read_b128 v[92:95], v17 offset:9216\nv_mfma_f32_16x16x32_bf16 v[20:23], a[144:147], v[80:83], v[20:23]\nds_read_b128 v[32:35], v16 offset:20480\nv_mfma_f32_16x16x32_bf16 v[44:47], v[48:51], v[36:39], v[44:47]\n" \
"ds_read_b128 v[76:79], v16 offset:53248\nv_mfma_f32_16x16x32_bf16 v[68:71], v[48:51], v[84:87], v[68:71]\nds_read_b128 v[88:91], v17 offset:8192\nv_mfma_f32_16x16x32_bf16 v[72:75], a[148:151], v[36:39], v[72:75]\n" \
"s_waitcnt vmcnt(4)\nds_read_b128 v[36:39], v61 offset:0\nds_read_b128 v[80:83], v61 offset:1024\nds_read_b128 v[48:51], v61 offset:2048\n" \
"v_mfma_f32_16x16x32_bf16 v[20:23], a[148:151], v[84:87], v[20:23]\ns_waitcnt lgkmcnt(7)\nds_read_b128 v[84:87], v61 offset:3072\nv_mfma_f32_16x16x32_bf16 v[44:47], a[48:51], v[52:55], v[44:47]\n" \
"ds_read_b128 v[96:99], v17 offset:4096\nv_mfma_f32_16x16x32_bf16 v[68:71], a[48:51], v[24:27], v[68:71]\nds_read_b128 v[100:103], v17 offset:5120\nv_mov_b64 v[104:105], 0\n" \
"v_mfma_f32_16x16x32_bf16 v[72:75], v[56:59], v[52:55], v[72:75]\ns_waitcnt lgkmcnt(3)\nv_max3_u32 v52, v36, v80, v48\nv_mov_b64 v[106:107], 0\n" \
"v_mfma_f32_16x16x32_bf16 v[20:23], v[56:59], v[24:27], v[20:23]\ns_waitcnt lgkmcnt(2)\nv_max_u32 v52, v52, v84\nv_cmp_eq_u32 vcc, -1, v52\n" \
"v_mfma_f32_16x16x32_bf16 v[44:47], a[52:55], v[28:31], v[44:47]\nv_mov_b64 v[24:25], 0\nv_mov_b64 v[26:27], 0\nv_mfma_f32_16x16x32_bf16 v[68:71], a[52:55], v[40:43], v[68:71]\n" \
"s_cbranch_vccnz 2f\n3:\nv_mfma_f32_16x16x32_bf16 v[72:75], v[62:65], v[28:31], v[72:75]\ns_barrier\n" \
"ds_read_b128 v[28:31], v16 offset:2048\nv_mfma_f32_16x16x32_bf16 v[20:23], v[92:95], v[40:43], v[20:23]\ns_memrealtime s[40:41]\nds_read_b128 v[36:39], v16 offset:5120\n" \
"v_mfma_f32_16x16x32_bf16 v[44:47], a[56:59], v[32:35], v[44:47]\nds_read_b128 v[40:43], v16 offset:1024\ns_lshl_b32 s43, s20, 24\nv_mfma_f32_16x16x32_bf16 v[68:71], a[56:59], v[76:79], v[68:71]\n" \
"ds_read_b128 v[48:51], v16 offset:33792\nv_mfma_f32_16x16x32_bf16 v[72:75], a[152:155], v[32:35], v[72:75]\nv_mfma_f32_16x16x32_bf16 v[20:23], a[152:155], v[76:79], v[20:23]\ns_waitcnt lgkmcnt(3)\n" \
"ds_read_b128 v[32:35], v16 offset:3072\nv_smfmac_f32_16x16x64_bf16 v[104:107], v[28:31], a[8:15], v18\ns_waitcnt lgkmcnt(3)\nv_smfmac_f32_16x16x64_bf16 v[24:27], v[28:31], a[112:119], v18\n" \
"ds_read_b128 v[52:55], v16 offset:34816\nv_smfmac_f32_16x16x64_bf16 v[104:107], v[36:39], a[24:31], v18\ns_waitcnt lgkmcnt(3)\nds_read_b128 v[76:79], v17 offset:6144\n" \
"v_smfmac_f32_16x16x64_bf16 v[24:27], v[36:39], v[96:103], v18\nds_read_b128 v[28:31], v16 offset:36864\nv_smfmac_f32_16x16x64_bf16 v[104:107], v[40:43], a[0:7], v18\ns_waitcnt lgkmcnt(4)\n" \
"ds_read_b128 v[80:83], v17 offset:7168\nv_smfmac_f32_16x16x64_bf16 v[24:27], v[40:43], a[104:111], v18\nds_read_b128 v[36:39], v16 offset:6144\ns_add_u32 s42, s40, 0x60\n" \
"v_smfmac_f32_16x16x64_bf16 v[104:107], v[48:51], a[48:55], v18\ns_and_b32 s42, s42, 0xffffff\ns_or_b32 s42, s42, s43\nv_mov_b32 v40, s42\n" \
"v_smfmac_f32_16x16x64_bf16 v[24:27], v[48:51], v[88:95], v18\ns_waitcnt lgkmcnt(4)\nds_read_b128 v[48:51], v16 offset:7168\nv_smfmac_f32_16x16x64_bf16 v[104:107], v[32:35], a[16:23], v18\n" \
"buffer_store_dword v40, v60, s[8:11], s27 offen offset:0 sc1\nv_smfmac_f32_16x16x64_bf16 v[24:27], v[32:35], a[120:127], v18\nds_read_b128 v[56:59], v16 offset:35840\nv_smfmac_f32_16x16x64_bf16 v[104:107], v[52:55], a[56:63], v18\n" \
"s_waitcnt lgkmcnt(4)\nv_smfmac_f32_16x16x64_bf16 v[24:27], v[52:55], a[152:159], v18\nds_read_b128 v[32:35], v16 offset:0\ns_add_u32 s16, s20, 1\n" \
"v_smfmac_f32_16x16x64_bf16 v[104:107], v[28:31], a[72:79], v18\nds_read_b128 v[60:63], v17 offset:0\nv_mov_b64 v[40:41], 0\nv_smfmac_f32_16x16x64_bf16 v[24:27], v[28:31], a[168:175], v18\n" \
"s_waitcnt lgkmcnt(4)\nds_read_b128 v[28:31], v16 offset:39936\nv_smfmac_f32_16x16x64_bf16 v[104:107], v[36:39], a[32:39], v18\nds_read_b128 v[64:67], v17 offset:1024\n" \
"v_mov_b64 v[42:43], 0\nv_smfmac_f32_16x16x64_bf16 v[24:27], v[36:39], a[136:143], v18\ns_waitcnt lgkmcnt(4)\nds_read_b128 v[36:39], v16 offset:37888\n" \
"v_smfmac_f32_16x16x64_bf16 v[104:107], v[48:51], a[40:47], v18\nds_read_b128 v[84:87], v17 offset:2048\ns_and_b32 s16, s16, 7\nv_smfmac_f32_16x16x64_bf16 v[24:27], v[48:51], v[76:83], v18\n" \
"ds_read_b128 v[52:55], v16 offset:4096\ns_lshl_b32 s16, s16, 22\nv_smfmac_f32_16x16x64_bf16 v[104:107], v[56:59], a[64:71], v18\nds_read_b128 v[88:91], v17 offset:3072\n" \
"s_add_u32 s16, s16, s25\nv_smfmac_f32_16x16x64_bf16 v[24:27], v[56:59], a[160:167], v18\ns_waitcnt lgkmcnt(6)\nds_read_b128 v[48:51], v16 offset:38912\n" \
"v_smfmac_f32_16x16x64_bf16 v[104:107], v[32:35], v[0:7], v18\nds_read_b128 v[56:59], v16 offset:21504\ns_add_u32 s17, s16, 0x1000\nv_smfmac_f32_16x16x64_bf16 v[24:27], v[32:35], v[8:15], v18\n" \
"s_waitcnt lgkmcnt(6)\nds_read_b128 v[32:35], v16 offset:32768\nv_smfmac_f32_16x16x64_bf16 v[104:107], v[28:31], a[96:103], v18\nds_read_b128 v[76:79], v16 offset:54272\n" \
"s_add_u32 s18, s16, 0x4000\nv_smfmac_f32_16x16x64_bf16 v[24:27], v[28:31], a[192:199], v18\ns_waitcnt lgkmcnt(5)\nds_read_b128 v[28:31], v16 offset:22528\n" \
"v_smfmac_f32_16x16x64_bf16 v[104:107], v[36:39], a[80:87], v18\nds_read_b128 v[80:83], v16 offset:55296\nv_lshrrev_b32 v92, 16, v18\nv_smfmac_f32_16x16x64_bf16 v[24:27], v[36:39], a[176:183], v18\n" \
"ds_write_b128 v16, v[40:43] offset:45056\nv_smfmac_f32_16x16x64_bf16 v[104:107], v[52:55], v[60:67], v18\ns_add_u32 s19, s16, 0x5000\ns_and_b32 s43, s20, 2\n" \
"s_lshl_b32 s43, s43, 12\nv_smfmac_f32_16x16x64_bf16 v[24:27], v[52:55], a[128:135], v18\ns_waitcnt lgkmcnt(4)\nds_read_b128 v[36:39], v16 offset:23552\n" \
"v_smfmac_f32_16x16x64_bf16 v[104:107], v[48:51], a[88:95], v18\nds_read_b128 v[52:55], v16 offset:56320\ns_and_b32 s42, s20, 7\nv_smfmac_f32_16x16x64_bf16 v[24:27], v[48:51], a[184:191], v18\n" \
"ds_read_b128 v[60:63], v17 offset:10240\ns_lshl_b32 s42, s42, 20\nv_smfmac_f32_16x16x64_bf16 v[104:107], v[32:35], v[84:91], v18\nds_read_b128 v[64:67], v17 offset:11264\n" \
"s_add_u32 s42, s42, s24\nv_smfmac_f32_16x16x64_bf16 v[24:27], v[32:35], a[144:151], v18\ns_waitcnt lgkmcnt(4)\nv_mfma_f32_16x16x32_bf16 v[44:47], a[60:63], v[56:59], v[44:47]\n" \
"v_add_f32 v104, v104, v106\nv_add_f32 v105, v105, v107\nv_cvt_pk_bf16_f32 v104, v104, v105\nv_mfma_f32_16x16x32_bf16 v[68:71], a[60:63], v[76:79], v[68:71]\n" \
"v_add_f32 v24, v24, v26\nv_add_f32 v25, v25, v27\nv_cvt_pk_bf16_f32 v24, v24, v25\nv_mfma_f32_16x16x32_bf16 v[72:75], a[156:159], v[56:59], v[72:75]\n" \
"ds_write2_b32 v92, v104, v24 offset0:0 offset1:4\nv_mfma_f32_16x16x32_bf16 v[20:23], a[156:159], v[76:79], v[20:23]\ns_add_u32 s99, s20, 2\nv_mov_b64 v[24:25], 0\n" \
"v_mov_b64 v[26:27], 0\nv_mfma_f32_16x16x32_bf16 v[44:47], a[64:67], v[28:31], v[44:47]\ns_min_u32 s99, s99, 63\nv_bfe_u32 v32, v16, 4, 3\n" \
"v_lshlrev_b32 v32, 2, v32\nv_mfma_f32_16x16x32_bf16 v[68:71], a[64:67], v[80:83], v[68:71]\ns_waitcnt vmcnt(1) lgkmcnt(0)\ns_barrier\n" \
"ds_read_b64_tr_b16 v[48:49], v19 offset:0\nv_mfma_f32_16x16x32_bf16 v[72:75], a[160:163], v[28:31], v[72:75]\nds_read_b64_tr_b16 v[50:51], v19 offset:256\nds_read_b64_tr_b16 v[28:29], v19 offset:1024\n" \
"ds_read_b64_tr_b16 v[30:31], v19 offset:1280\nv_mfma_f32_16x16x32_bf16 v[20:23], a[160:163], v[80:83], v[20:23]\nds_read_b128 v[56:59], v16 offset:24576\nv_mov_b64 v[76:77], 0\n" \
"v_mfma_f32_16x16x32_bf16 v[44:47], a[68:71], v[36:39], v[44:47]\ns_lshl_b32 s99, s99, 5\ns_add_u32 s99, s99, s26\nv_mov_b64 v[78:79], 0\n" \
"v_mfma_f32_16x16x32_bf16 v[68:71], a[68:71], v[52:55], v[68:71]\ns_waitcnt lgkmcnt(3)\nbuffer_load_dword v33, v32, s[8:11], s99 offen sc1\nv_mfma_f32_16x16x32_bf16 v[72:75], a[164:167], v[36:39], v[72:75]\n" \
"ds_read_b128 v[80:83], v16 offset:25600\nv_mov_b64 v[34:35], 0\nv_smfmac_f32_16x16x64_bf16 v[40:43], v[48:51], a[200:207], v18\ns_waitcnt lgkmcnt(2)\n" \
"ds_read_b128 v[88:91], v17 offset:12288\nv_smfmac_f32_16x16x64_bf16 v[40:43], v[28:31], v[60:67], v18\nds_read_b128 v[84:87], v16 offset:45056\nv_mov_b64 v[36:37], 0\n" \
"v_smfmac_f32_16x16x64_bf16 v[24:27], v[48:51], a[224:231], v18\nds_read_b128 v[92:95], v17 offset:13312\nv_mfma_f32_16x16x32_bf16 v[20:23], a[164:167], v[52:55], v[20:23]\nds_read_b128 v[60:63], v17 offset:14336\n" \
"v_add_f32 v40, v40, v42\nv_smfmac_f32_16x16x64_bf16 v[24:27], v[28:31], a[232:239], v18\ns_waitcnt lgkmcnt(5)\nds_read_b128 v[52:55], v16 offset:45056\n" \
"v_mfma_f32_16x16x32_bf16 v[44:47], a[72:75], v[56:59], v[44:47]\nds_read_b128 v[64:67], v17 offset:15360\nv_add_f32 v41, v41, v43\nv_smfmac_f32_16x16x64_bf16 v[76:79], v[48:51], a[208:215], v18\n" \
"ds_read_b128 v[96:99], v16 offset:26624\nv_add_f32 v24, v24, v26\nv_smfmac_f32_16x16x64_bf16 v[76:79], v[28:31], a[216:223], v18\nds_read_b128 v[100:103], v16 offset:45056\n" \
"v_add_f32 v25, v25, v27\nv_smfmac_f32_16x16x64_bf16 v[34:37], v[48:51], a[240:247], v18\nds_read_b128 v[104:107], v17 offset:16384\nv_cvt_pk_bf16_f32 v38, v40, v41\n" \
"v_mfma_f32_16x16x32_bf16 v[72:75], a[168:171], v[56:59], v[72:75]\ns_waitcnt lgkmcnt(5)\nv_add_f32 v76, v76, v78\nv_add_f32 v77, v77, v79\n" \
"v_smfmac_f32_16x16x64_bf16 v[34:37], v[28:31], a[248:255], v18\nds_read_b128 v[56:59], v16 offset:45056\nv_cvt_pk_bf16_f32 v39, v24, v25\nv_smfmac_f32_16x16x64_bf16 v[84:87], v[48:51], v[112:119], v18\n" \
"ds_read_b128 v[108:111], v17 offset:17408\nv_cvt_pk_bf16_f32 v40, v76, v77\nv_mfma_f32_16x16x32_bf16 v[44:47], a[76:79], v[80:83], v[44:47]\ns_waitcnt lgkmcnt(4)\n" \
"v_permlane32_swap_b32 v38, v39\nv_smfmac_f32_16x16x64_bf16 v[84:87], v[28:31], v[120:127], v18\nds_read_b128 v[24:27], v16 offset:45056\nv_add_f32 v34, v34, v36\n" \
"v_smfmac_f32_16x16x64_bf16 v[52:55], v[48:51], v[128:135], v18\nds_read_b128 v[76:79], v16 offset:27648\nv_add_f32 v35, v35, v37\nv_cvt_pk_bf16_f32 v41, v34, v35\n" \
"v_smfmac_f32_16x16x64_bf16 v[52:55], v[28:31], v[136:143], v18\nds_read_b128 v[34:37], v17 offset:18432\nv_permlane32_swap_b32 v40, v41\nv_mfma_f32_16x16x32_bf16 v[72:75], a[172:175], v[80:83], v[72:75]\n" \
"s_waitcnt lgkmcnt(4)\nv_permlane16_swap_b32 v38, v40\nv_smfmac_f32_16x16x64_bf16 v[100:103], v[48:51], v[88:95], v18\nds_read_b128 v[80:83], v16 offset:45056\n" \
"v_permlane16_swap_b32 v39, v41\nv_and_or_b32 v38, v38, -2, s32\nv_smfmac_f32_16x16x64_bf16 v[100:103], v[28:31], v[60:67], v18\nds_read_b128 v[88:91], v16 offset:28672\n" \
"v_add_f32 v84, v84, v86\nv_mfma_f32_16x16x32_bf16 v[44:47], a[80:83], v[96:99], v[44:47]\nbuffer_store_dwordx4 v[38:41], v16, s[8:11], s29 offen offset:0 sc1\nv_add_f32 v85, v85, v87\n" \
"v_add_f32 v52, v52, v54\nv_add_f32 v53, v53, v55\nv_smfmac_f32_16x16x64_bf16 v[56:59], v[48:51], v[144:151], v18\nds_read_b128 v[60:63], v16 offset:45056\n" \
"v_add_f32 v100, v100, v102\nv_smfmac_f32_16x16x64_bf16 v[56:59], v[28:31], v[152:159], v18\nds_read_b128 v[38:41], v17 offset:19456\nv_add_f32 v101, v101, v103\n" \
"v_mfma_f32_16x16x32_bf16 v[72:75], a[176:179], v[96:99], v[72:75]\ns_waitcnt lgkmcnt(3)\nv_cvt_pk_bf16_f32 v64, v84, v85\nv_cvt_pk_bf16_f32 v65, v52, v53\n" \
"v_smfmac_f32_16x16x64_bf16 v[24:27], v[48:51], v[160:167], v18\nds_read_b128 v[52:55], v16 offset:45056\nv_add_f32 v56, v56, v58\nv_smfmac_f32_16x16x64_bf16 v[24:27], v[28:31], v[168:175], v18\n" \
"ds_read_b128 v[84:87], v16 offset:45056\nv_add_f32 v57, v57, v59\nv_mfma_f32_16x16x32_bf16 v[44:47], a[84:87], v[76:79], v[44:47]\nv_permlane32_swap_b32 v64, v65\n" \
"v_cvt_pk_bf16_f32 v66, v100, v101\nv_cvt_pk_bf16_f32 v67, v56, v57\nv_smfmac_f32_16x16x64_bf16 v[80:83], v[48:51], v[184:191], v18\nds_read_b128 v[56:59], v16 offset:29696\n" \
"v_permlane32_swap_b32 v66, v67\nv_smfmac_f32_16x16x64_bf16 v[80:83], v[28:31], v[192:199], v18\ns_waitcnt lgkmcnt(3)\nv_permlane16_swap_b32 v64, v66\n" \
"v_mfma_f32_16x16x32_bf16 v[72:75], a[180:183], v[76:79], v[72:75]\nv_permlane16_swap_b32 v65, v67\nv_and_or_b32 v64, v64, -2, s32\nv_smfmac_f32_16x16x64_bf16 v[60:63], v[48:51], v[104:111], v18\n" \
"ds_read_b128 v[76:79], v16 offset:45056\nv_add_f32 v24, v24, v26\nv_smfmac_f32_16x16x64_bf16 v[60:63], v[28:31], v[176:183], v18\ns_waitcnt lgkmcnt(2)\n" \
"buffer_store_dwordx4 v[64:67], v16, s[8:11], s29 offen offset:1024 sc1\nds_read_b128 v[92:95], v17 offset:20480\nv_add_f32 v25, v25, v27\nv_mfma_f32_16x16x32_bf16 v[44:47], a[88:91], v[88:91], v[44:47]\n" \
"ds_read_b128 v[96:99], v17 offset:21504\nv_add_f32 v80, v80, v82\nv_smfmac_f32_16x16x64_bf16 v[52:55], v[48:51], v[200:207], v18\nds_read_b128 v[64:67], v16 offset:45056\n" \
"v_add_f32 v81, v81, v83\nv_smfmac_f32_16x16x64_bf16 v[52:55], v[28:31], v[34:41], v18\nds_read_b128 v[100:103], v16 offset:30720\nv_add_f32 v60, v60, v62\n" \
"v_smfmac_f32_16x16x64_bf16 v[84:87], v[48:51], v[208:215], v18\nds_read_b128 v[34:37], v16 offset:45056\nv_add_f32 v61, v61, v63\nv_mfma_f32_16x16x32_bf16 v[72:75], a[184:187], v[88:91], v[72:75]\n" \
"s_waitcnt lgkmcnt(5)\nv_add_f32 v52, v52, v54\nv_add_f32 v53, v53, v55\nv_smfmac_f32_16x16x64_bf16 v[84:87], v[28:31], v[216:223], v18\n" \
"ds_read_b128 v[104:107], v17 offset:22528\nv_cvt_pk_bf16_f32 v38, v24, v25\nv_mfma_f32_16x16x32_bf16 v[44:47], a[92:95], v[56:59], v[44:47]\nv_cvt_pk_bf16_f32 v39, v80, v81\n" \
"v_cvt_pk_bf16_f32 v40, v60, v61\nv_cvt_pk_bf16_f32 v41, v52, v53\nv_smfmac_f32_16x16x64_bf16 v[76:79], v[48:51], v[232:239], v18\nds_read_b128 v[108:111], v17 offset:23552\n" \
"v_permlane32_swap_b32 v38, v39\nv_permlane32_swap_b32 v40, v41\nv_smfmac_f32_16x16x64_bf16 v[76:79], v[28:31], v[240:247], v18\nds_read_b128 v[24:27], v16 offset:31744\n" \
"v_permlane16_swap_b32 v38, v40\nv_mfma_f32_16x16x32_bf16 v[72:75], a[188:191], v[56:59], v[72:75]\ns_waitcnt lgkmcnt(3)\nv_permlane16_swap_b32 v39, v41\n" \
"v_and_or_b32 v38, v38, -2, s32\nv_smfmac_f32_16x16x64_bf16 v[64:67], v[48:51], v[92:99], v18\nds_read_b128 v[52:55], v16 offset:57344\nv_add_f32 v84, v84, v86\n" \
"v_smfmac_f32_16x16x64_bf16 v[64:67], v[28:31], v[224:231], v18\nds_read_b128 v[56:59], v16 offset:58368\nv_add_f32 v85, v85, v87\nv_mfma_f32_16x16x32_bf16 v[44:47], a[96:99], v[100:103], v[44:47]\n" \
"s_waitcnt lgkmcnt(3)\nbuffer_store_dwordx4 v[38:41], v16, s[8:11], s29 offen offset:2048 sc1\nv_add_f32 v76, v76, v78\nv_add_f32 v77, v77, v79\n" \
"v_cvt_pk_bf16_f32 v38, v84, v85\nv_smfmac_f32_16x16x64_bf16 v[34:37], v[48:51], v[104:111], v18\nds_read_b128 v[60:63], v16 offset:59392\nv_add_f32 v64, v64, v66\n" \
"v_add_f32 v65, v65, v67\nv_cvt_pk_bf16_f32 v39, v76, v77\nv_cvt_pk_bf16_f32 v40, v64, v65\nv_smfmac_f32_16x16x64_bf16 v[34:37], v[28:31], v[248:255], v18\n" \
"ds_read_b128 v[48:51], v16 offset:60416\nv_permlane32_swap_b32 v38, v39\nv_mfma_f32_16x16x32_bf16 v[72:75], a[192:195], v[100:103], v[72:75]\ns_waitcnt lgkmcnt(3)\n" \
"ds_read_b128 v[28:31], v16 offset:61440\nv_mfma_f32_16x16x32_bf16 v[44:47], a[100:103], v[24:27], v[44:47]\nv_add_f32 v34, v34, v36\nv_add_f32 v35, v35, v37\n" \
"v_cvt_pk_bf16_f32 v41, v34, v35\nv_mfma_f32_16x16x32_bf16 v[72:75], a[196:199], v[24:27], v[72:75]\nds_read_b128 v[34:37], v16 offset:62464\nv_permlane32_swap_b32 v40, v41\n" \
"v_mfma_f32_16x16x32_bf16 v[68:71], a[72:75], v[52:55], v[68:71]\ns_waitcnt lgkmcnt(3)\nv_permlane16_swap_b32 v38, v40\nv_permlane16_swap_b32 v39, v41\n" \
"v_and_or_b32 v38, v38, -2, s32\nv_mfma_f32_16x16x32_bf16 v[20:23], a[168:171], v[52:55], v[20:23]\nbuffer_store_dwordx4 v[38:41], v16, s[8:11], s29 offen offset:3072 sc1\ns_bfe_u32 s32, s20, 0x10003\n" \
"v_cvt_pk_bf16_f32 v44, v44, v45\nv_cvt_pk_bf16_f32 v45, v46, v47\nv_mfma_f32_16x16x32_bf16 v[68:71], a[76:79], v[56:59], v[68:71]\ns_xor_b32 s29, s42, s43\n" \
"v_cvt_pk_bf16_f32 v46, v72, v73\nv_cvt_pk_bf16_f32 v47, v74, v75\nv_mfma_f32_16x16x32_bf16 v[20:23], a[172:175], v[56:59], v[20:23]\nds_read_b128 v[24:27], v16 offset:63488\n" \
"v_mfma_f32_16x16x32_bf16 v[68:71], a[80:83], v[60:63], v[68:71]\ns_waitcnt vmcnt(4) lgkmcnt(1)\nv_cmp_eq_u32 s[42:43], -1, v33\nv_mfma_f32_16x16x32_bf16 v[20:23], a[176:179], v[60:63], v[20:23]\n" \
"ds_read_b128 v[38:41], v16 offset:64512\nv_mfma_f32_16x16x32_bf16 v[68:71], a[84:87], v[48:51], v[68:71]\ns_cmp_eq_u64 s[42:43], 0\ns_cselect_b32 s14, s10, 0\n" \
"s_cmp_lt_i32 s20, 62\ns_cselect_b32 s14, s14, 0\ns_cmp_eq_u32 s14, 0\ns_cbranch_scc1 4f\n" \
"5:\nv_add_u32 v32, s28, v16\nv_mfma_f32_16x16x32_bf16 v[20:23], a[180:183], v[48:51], v[20:23]\nds_write_b128 v32, v[44:47] offset:0\n" \
"s_add_u32 m0, s22, 0\nv_mfma_f32_16x16x32_bf16 v[68:71], a[88:91], v[28:31], v[68:71]\nbuffer_load_dwordx4 v16, s[12:15], s21 offen offset:0 sc1 lds\nv_mfma_f32_16x16x32_bf16 v[20:23], a[184:187], v[28:31], v[20:23]\n" \
"ds_read_b128 v[42:45], v17 offset:10240\ns_add_u32 s20, s20, 1\nv_mfma_f32_16x16x32_bf16 v[68:71], a[92:95], v[34:37], v[68:71]\ns_waitcnt lgkmcnt(1)\n" \
"s_barrier\nds_read_b128 v[28:31], v16 offset:8192\nv_mfma_f32_16x16x32_bf16 v[20:23], a[188:191], v[34:37], v[20:23]\nds_read_b128 v[48:51], v16 offset:9216\n" \
"v_mfma_f32_16x16x32_bf16 v[68:71], a[96:99], v[24:27], v[68:71]\nds_read_b128 v[64:67], v16 offset:10240\nv_add_u32 v32, s28, v16\nv_mfma_f32_16x16x32_bf16 v[20:23], a[192:195], v[24:27], v[20:23]\n" \
"buffer_load_dwordx4 v16, s[12:15], s21 offen offset:1024 sc1 lds\nds_read_b128 v[72:75], v16 offset:11264\nds_read_b128 v[24:27], v17 offset:11264\nv_mfma_f32_16x16x32_bf16 v[68:71], a[100:103], v[38:41], v[68:71]\n" \
"s_waitcnt lgkmcnt(4)\nv_mfma_f32_16x16x32_bf16 v[20:23], a[196:199], v[38:41], v[20:23]\ns_waitcnt lgkmcnt(3)\nv_mfma_f32_16x16x32_bf16 v[34:37], v[28:31], a[200:203], 0\n" \
"s_waitcnt lgkmcnt(2)\nv_cvt_pk_bf16_f32 v68, v68, v69\nv_cvt_pk_bf16_f32 v69, v70, v71\nv_mfma_f32_16x16x32_bf16 v[34:37], v[48:51], a[204:207], v[34:37]\n" \
"s_waitcnt lgkmcnt(0)\nv_cvt_pk_bf16_f32 v70, v20, v21\nv_cvt_pk_bf16_f32 v71, v22, v23\nv_mfma_f32_16x16x32_bf16 v[34:37], v[64:67], v[42:45], v[34:37]\n" \
"buffer_load_dwordx4 v16, s[12:15], s21 offen offset:2048 sc1 lds\nv_mfma_f32_16x16x32_bf16 v[34:37], v[72:75], v[24:27], v[34:37]\nds_write_b128 v32, v[68:71] offset:32768\nv_mfma_f32_16x16x32_bf16 v[20:23], v[28:31], a[208:211], 0\n" \
"v_mfma_f32_16x16x32_bf16 v[20:23], v[48:51], a[212:215], v[20:23]\nv_cvt_pk_bf16_f32 v34, v34, v35\nv_cvt_pk_bf16_f32 v35, v36, v37\nv_mfma_f32_16x16x32_bf16 v[20:23], v[64:67], a[216:219], v[20:23]\n" \
"s_add_u32 s43, s21, 0x4000\nds_read_b128 v[24:27], v17 offset:18432\nv_mfma_f32_16x16x32_bf16 v[20:23], v[72:75], a[220:223], v[20:23]\ns_waitcnt lgkmcnt(1)\n" \
"s_barrier\nv_mfma_f32_16x16x32_bf16 v[38:41], v[28:31], a[224:227], 0\nbuffer_load_dwordx4 v16, s[12:15], s21 offen offset:3072 sc1 lds\nv_mfma_f32_16x16x32_bf16 v[38:41], v[48:51], a[228:231], v[38:41]\n" \
"s_add_u32 m0, s22, 0x4000\nds_read_b128 v[52:55], v17 offset:19456\nv_mfma_f32_16x16x32_bf16 v[38:41], v[64:67], a[232:235], v[38:41]\nbuffer_load_dwordx4 v16, s[12:15], s43 offen offset:0 sc1 lds\n" \
"v_mfma_f32_16x16x32_bf16 v[38:41], v[72:75], a[236:239], v[38:41]\nds_read_b128 v[68:71], v16 offset:40960\nv_cvt_pk_bf16_f32 v20, v20, v21\nv_mfma_f32_16x16x32_bf16 v[56:59], v[28:31], v[144:147], 0\n" \
"ds_read_b128 v[76:79], v16 offset:41984\nv_cvt_pk_bf16_f32 v21, v22, v23\nv_mfma_f32_16x16x32_bf16 v[56:59], v[48:51], v[148:151], v[56:59]\nds_read_b128 v[80:83], v16 offset:43008\n" \
"v_cvt_pk_bf16_f32 v38, v38, v39\nv_mfma_f32_16x16x32_bf16 v[56:59], v[64:67], v[152:155], v[56:59]\nds_read_b128 v[60:63], v17 offset:10240\nv_cvt_pk_bf16_f32 v39, v40, v41\n" \
"v_mfma_f32_16x16x32_bf16 v[56:59], v[72:75], v[156:159], v[56:59]\nbuffer_load_dwordx4 v16, s[12:15], s43 offen offset:1024 sc1 lds\nv_mfma_f32_16x16x32_bf16 v[84:87], v[28:31], v[200:203], 0\ns_waitcnt lgkmcnt(4)\n" \
"ds_read_b128 v[44:47], v16 offset:44032\nv_mfma_f32_16x16x32_bf16 v[84:87], v[48:51], v[204:207], v[84:87]\nds_read_b128 v[40:43], v17 offset:11264\nv_cvt_pk_bf16_f32 v56, v56, v57\n" \
"v_mfma_f32_16x16x32_bf16 v[84:87], v[64:67], v[24:27], v[84:87]\ns_waitcnt lgkmcnt(4)\nv_cvt_pk_bf16_f32 v57, v58, v59\nv_mfma_f32_16x16x32_bf16 v[84:87], v[72:75], v[52:55], v[84:87]\n" \
"v_mfma_f32_16x16x32_bf16 v[22:25], v[68:71], a[200:203], 0\ns_waitcnt lgkmcnt(2)\nbuffer_load_dwordx4 v16, s[12:15], s43 offen offset:2048 sc1 lds\nv_mfma_f32_16x16x32_bf16 v[22:25], v[76:79], a[204:207], v[22:25]\n" \
"v_cvt_pk_bf16_f32 v84, v84, v85\nv_cvt_pk_bf16_f32 v85, v86, v87\nv_mfma_f32_16x16x32_bf16 v[22:25], v[80:83], v[60:63], v[22:25]\ns_waitcnt lgkmcnt(0)\n" \
"v_mfma_f32_16x16x32_bf16 v[22:25], v[44:47], v[40:43], v[22:25]\nv_mfma_f32_16x16x32_bf16 v[52:55], v[68:71], a[208:211], 0\nbuffer_load_dwordx4 v16, s[12:15], s43 offen offset:3072 sc1 lds\nv_mfma_f32_16x16x32_bf16 v[52:55], v[76:79], a[212:215], v[52:55]\n" \
"v_cvt_pk_bf16_f32 v36, v22, v23\nv_cvt_pk_bf16_f32 v37, v24, v25\nv_mfma_f32_16x16x32_bf16 v[52:55], v[80:83], a[216:219], v[52:55]\nds_read_b128 v[24:27], v17 offset:18432\n" \
"v_mfma_f32_16x16x32_bf16 v[52:55], v[44:47], a[220:223], v[52:55]\nds_read_b128 v[60:63], v17 offset:19456\nv_mfma_f32_16x16x32_bf16 v[86:89], v[68:71], a[224:227], 0\nv_mfma_f32_16x16x32_bf16 v[86:89], v[76:79], a[228:231], v[86:89]\n" \
"buffer_store_dwordx4 v[34:37], v16, s[8:11], s16 offen offset:0 sc1\nv_cvt_pk_bf16_f32 v22, v52, v53\nv_cvt_pk_bf16_f32 v23, v54, v55\nv_mfma_f32_16x16x32_bf16 v[86:89], v[80:83], a[232:235], v[86:89]\n" \
"v_mfma_f32_16x16x32_bf16 v[86:89], v[44:47], a[236:239], v[86:89]\nv_mfma_f32_16x16x32_bf16 v[32:35], v[68:71], v[144:147], 0\nv_mfma_f32_16x16x32_bf16 v[32:35], v[76:79], v[148:151], v[32:35]\nv_cvt_pk_bf16_f32 v40, v86, v87\n" \
"v_cvt_pk_bf16_f32 v41, v88, v89\nv_mfma_f32_16x16x32_bf16 v[32:35], v[80:83], v[152:155], v[32:35]\nv_mfma_f32_16x16x32_bf16 v[32:35], v[44:47], v[156:159], v[32:35]\nv_mfma_f32_16x16x32_bf16 v[52:55], v[68:71], v[200:203], 0\n" \
"s_waitcnt lgkmcnt(0)\nbuffer_store_dwordx4 v[20:23], v16, s[8:11], s16 offen offset:1024 sc1\nv_mfma_f32_16x16x32_bf16 v[52:55], v[76:79], v[204:207], v[52:55]\nv_cvt_pk_bf16_f32 v58, v32, v33\n" \
"v_cvt_pk_bf16_f32 v59, v34, v35\nv_mfma_f32_16x16x32_bf16 v[52:55], v[80:83], v[24:27], v[52:55]\nv_mfma_f32_16x16x32_bf16 v[52:55], v[44:47], v[60:63], v[52:55]\nv_mfma_f32_16x16x32_bf16 v[20:23], v[28:31], a[240:243], 0\n" \
"v_mfma_f32_16x16x32_bf16 v[24:27], v[68:71], a[240:243], 0\nv_cvt_pk_bf16_f32 v86, v52, v53\nv_cvt_pk_bf16_f32 v87, v54, v55\nv_mfma_f32_16x16x32_bf16 v[20:23], v[48:51], a[244:247], v[20:23]\n" \
"v_mfma_f32_16x16x32_bf16 v[24:27], v[76:79], a[244:247], v[24:27]\nv_mfma_f32_16x16x32_bf16 v[20:23], v[64:67], a[248:251], v[20:23]\nbuffer_store_dwordx4 v[38:41], v16, s[8:11], s16 offen offset:2048 sc1\nds_read_b128 v[32:35], v17 offset:12288\n" \
"v_mfma_f32_16x16x32_bf16 v[24:27], v[80:83], a[248:251], v[24:27]\nv_mfma_f32_16x16x32_bf16 v[20:23], v[72:75], a[252:255], v[20:23]\nds_read_b128 v[36:39], v17 offset:13312\nv_mfma_f32_16x16x32_bf16 v[24:27], v[44:47], a[252:255], v[24:27]\n" \
"v_mfma_f32_16x16x32_bf16 v[40:43], v[28:31], v[128:131], 0\nds_read_b128 v[52:55], v17 offset:14336\nv_cvt_pk_bf16_f32 v20, v20, v21\nv_mfma_f32_16x16x32_bf16 v[60:63], v[68:71], v[128:131], 0\n" \
"v_cvt_pk_bf16_f32 v21, v22, v23\nv_cvt_pk_bf16_f32 v22, v24, v25\nv_mfma_f32_16x16x32_bf16 v[40:43], v[48:51], v[132:135], v[40:43]\ns_waitcnt vmcnt(7)\n" \
"s_barrier\nv_mfma_f32_16x16x32_bf16 v[60:63], v[76:79], v[132:135], v[60:63]\nbuffer_store_dwordx4 v[56:59], v16, s[8:11], s17 offen offset:3072 sc1\nds_read_b128 v[88:91], v17 offset:15360\n" \
"v_cvt_pk_bf16_f32 v23, v26, v27\nv_mfma_f32_16x16x32_bf16 v[40:43], v[64:67], v[136:139], v[40:43]\nv_mfma_f32_16x16x32_bf16 v[60:63], v[80:83], v[136:139], v[60:63]\nv_mfma_f32_16x16x32_bf16 v[40:43], v[72:75], v[140:143], v[40:43]\n" \
"s_waitcnt lgkmcnt(1)\nv_mfma_f32_16x16x32_bf16 v[60:63], v[44:47], v[140:143], v[60:63]\nv_mfma_f32_16x16x32_bf16 v[24:27], v[28:31], v[32:35], 0\nv_cvt_pk_bf16_f32 v40, v40, v41\n" \
"v_cvt_pk_bf16_f32 v41, v42, v43\nv_mfma_f32_16x16x32_bf16 v[56:59], v[68:71], v[32:35], 0\nv_cvt_pk_bf16_f32 v42, v60, v61\nv_cvt_pk_bf16_f32 v43, v62, v63\n" \
"v_mfma_f32_16x16x32_bf16 v[24:27], v[48:51], v[36:39], v[24:27]\nv_mfma_f32_16x16x32_bf16 v[56:59], v[76:79], v[36:39], v[56:59]\nbuffer_store_dwordx4 v[84:87], v16, s[8:11], s18 offen offset:3072 sc1\ns_add_u32 m0, s22, 0x2000\n" \
"s_add_u32 s43, s21, 0x2000\ns_add_u32 s21, s21, 0x10000\nv_mfma_f32_16x16x32_bf16 v[24:27], v[64:67], v[52:55], v[24:27]\ns_waitcnt lgkmcnt(0)\n" \
"ds_read_b128 v[32:35], v17 offset:16384\nv_mfma_f32_16x16x32_bf16 v[56:59], v[80:83], v[52:55], v[56:59]\nv_mfma_f32_16x16x32_bf16 v[24:27], v[72:75], v[88:91], v[24:27]\nds_read_b128 v[36:39], v17 offset:17408\n" \
"v_mfma_f32_16x16x32_bf16 v[56:59], v[44:47], v[88:91], v[56:59]\nbuffer_load_dwordx4 v16, s[12:15], s43 offen offset:0 sc1 lds\nv_mfma_f32_16x16x32_bf16 v[52:55], v[28:31], v[112:115], 0\nv_cvt_pk_bf16_f32 v24, v24, v25\n" \
"v_cvt_pk_bf16_f32 v25, v26, v27\nv_mfma_f32_16x16x32_bf16 v[60:63], v[68:71], v[112:115], 0\nv_cvt_pk_bf16_f32 v26, v56, v57\nv_cvt_pk_bf16_f32 v27, v58, v59\n" \
"v_mfma_f32_16x16x32_bf16 v[52:55], v[48:51], v[116:119], v[52:55]\nbuffer_store_dwordx4 v[20:23], v16, s[8:11], s16 offen offset:3072 sc1\nv_mfma_f32_16x16x32_bf16 v[60:63], v[76:79], v[116:119], v[60:63]\nv_mfma_f32_16x16x32_bf16 v[52:55], v[64:67], v[120:123], v[52:55]\n" \
"v_mfma_f32_16x16x32_bf16 v[60:63], v[80:83], v[120:123], v[60:63]\nv_mfma_f32_16x16x32_bf16 v[52:55], v[72:75], v[124:127], v[52:55]\ns_waitcnt lgkmcnt(0)\nv_mfma_f32_16x16x32_bf16 v[60:63], v[44:47], v[124:127], v[60:63]\n" \
"v_mfma_f32_16x16x32_bf16 v[20:23], v[28:31], v[32:35], 0\nv_cvt_pk_bf16_f32 v52, v52, v53\nv_cvt_pk_bf16_f32 v53, v54, v55\nv_mfma_f32_16x16x32_bf16 v[56:59], v[68:71], v[32:35], 0\n" \
"buffer_store_dwordx4 v[40:43], v16, s[8:11], s17 offen offset:2048 sc1\nbuffer_load_dwordx4 v16, s[12:15], s43 offen offset:1024 sc1 lds\nv_cvt_pk_bf16_f32 v54, v60, v61\nv_mfma_f32_16x16x32_bf16 v[20:23], v[48:51], v[36:39], v[20:23]\n" \
"v_cvt_pk_bf16_f32 v55, v62, v63\nv_mfma_f32_16x16x32_bf16 v[56:59], v[76:79], v[36:39], v[56:59]\nv_mfma_f32_16x16x32_bf16 v[20:23], v[64:67], v[176:179], v[20:23]\nv_mfma_f32_16x16x32_bf16 v[56:59], v[80:83], v[176:179], v[56:59]\n" \
"v_mfma_f32_16x16x32_bf16 v[20:23], v[72:75], v[180:183], v[20:23]\nv_mfma_f32_16x16x32_bf16 v[56:59], v[44:47], v[180:183], v[56:59]\nv_mfma_f32_16x16x32_bf16 v[32:35], v[28:31], v[184:187], 0\nv_cvt_pk_bf16_f32 v20, v20, v21\n" \
"v_cvt_pk_bf16_f32 v21, v22, v23\nv_mfma_f32_16x16x32_bf16 v[36:39], v[68:71], v[184:187], 0\nbuffer_store_dwordx4 v[24:27], v16, s[8:11], s17 offen offset:1024 sc1\nv_cvt_pk_bf16_f32 v22, v56, v57\n" \
"v_cvt_pk_bf16_f32 v23, v58, v59\nv_mfma_f32_16x16x32_bf16 v[32:35], v[48:51], v[188:191], v[32:35]\nv_mfma_f32_16x16x32_bf16 v[36:39], v[76:79], v[188:191], v[36:39]\nbuffer_load_dwordx4 v16, s[12:15], s43 offen offset:2048 sc1 lds\n" \
"v_mfma_f32_16x16x32_bf16 v[32:35], v[64:67], v[192:195], v[32:35]\nv_mfma_f32_16x16x32_bf16 v[36:39], v[80:83], v[192:195], v[36:39]\nv_mfma_f32_16x16x32_bf16 v[32:35], v[72:75], v[196:199], v[32:35]\nv_mfma_f32_16x16x32_bf16 v[36:39], v[44:47], v[196:199], v[36:39]\n" \
"v_mfma_f32_16x16x32_bf16 v[24:27], v[28:31], v[160:163], 0\nbuffer_store_dwordx4 v[52:55], v16, s[8:11], s17 offen offset:0 sc1\nv_cvt_pk_bf16_f32 v32, v32, v33\nv_cvt_pk_bf16_f32 v33, v34, v35\n" \
"v_mfma_f32_16x16x32_bf16 v[40:43], v[68:71], v[160:163], 0\nv_cvt_pk_bf16_f32 v34, v36, v37\nv_cvt_pk_bf16_f32 v35, v38, v39\nv_mfma_f32_16x16x32_bf16 v[24:27], v[48:51], v[164:167], v[24:27]\n" \
"v_mfma_f32_16x16x32_bf16 v[40:43], v[76:79], v[164:167], v[40:43]\nv_mfma_f32_16x16x32_bf16 v[24:27], v[64:67], v[168:171], v[24:27]\nds_read_b128 v[36:39], v17 offset:20480\nv_mfma_f32_16x16x32_bf16 v[40:43], v[80:83], v[168:171], v[40:43]\n" \
"buffer_load_dwordx4 v16, s[12:15], s43 offen offset:3072 sc1 lds\nv_mfma_f32_16x16x32_bf16 v[24:27], v[72:75], v[172:175], v[24:27]\nds_read_b128 v[52:55], v17 offset:21504\nv_mfma_f32_16x16x32_bf16 v[40:43], v[44:47], v[172:175], v[40:43]\n" \
"v_mfma_f32_16x16x32_bf16 v[60:63], v[28:31], v[232:235], 0\nbuffer_store_dwordx4 v[20:23], v16, s[8:11], s18 offen offset:1024 sc1\nv_cvt_pk_bf16_f32 v24, v24, v25\nv_cvt_pk_bf16_f32 v25, v26, v27\n" \
"v_mfma_f32_16x16x32_bf16 v[20:23], v[68:71], v[232:235], 0\nv_cvt_pk_bf16_f32 v26, v40, v41\nv_cvt_pk_bf16_f32 v27, v42, v43\nv_mfma_f32_16x16x32_bf16 v[60:63], v[48:51], v[236:239], v[60:63]\n" \
"v_mfma_f32_16x16x32_bf16 v[20:23], v[76:79], v[236:239], v[20:23]\nv_mfma_f32_16x16x32_bf16 v[60:63], v[64:67], v[240:243], v[60:63]\nv_mfma_f32_16x16x32_bf16 v[20:23], v[80:83], v[240:243], v[20:23]\nv_mfma_f32_16x16x32_bf16 v[60:63], v[72:75], v[244:247], v[60:63]\n" \
"s_waitcnt lgkmcnt(0)\nv_mfma_f32_16x16x32_bf16 v[20:23], v[44:47], v[244:247], v[20:23]\nbuffer_store_dwordx4 v[32:35], v16, s[8:11], s18 offen offset:2048 sc1\nv_mfma_f32_16x16x32_bf16 v[84:87], v[28:31], v[36:39], 0\n" \
"v_cvt_pk_bf16_f32 v60, v60, v61\nv_cvt_pk_bf16_f32 v61, v62, v63\nv_mfma_f32_16x16x32_bf16 v[32:35], v[68:71], v[36:39], 0\nv_cvt_pk_bf16_f32 v62, v20, v21\n" \
"v_cvt_pk_bf16_f32 v63, v22, v23\nv_mfma_f32_16x16x32_bf16 v[84:87], v[48:51], v[52:55], v[84:87]\nv_mfma_f32_16x16x32_bf16 v[32:35], v[76:79], v[52:55], v[32:35]\nv_mfma_f32_16x16x32_bf16 v[84:87], v[64:67], v[224:227], v[84:87]\n" \
"ds_read_b128 v[52:55], v17 offset:22528\nv_mfma_f32_16x16x32_bf16 v[32:35], v[80:83], v[224:227], v[32:35]\nv_mfma_f32_16x16x32_bf16 v[84:87], v[72:75], v[228:231], v[84:87]\nbuffer_store_dwordx4 v[24:27], v16, s[8:11], s18 offen offset:0 sc1\n" \
"ds_read_b128 v[88:91], v17 offset:23552\nv_mfma_f32_16x16x32_bf16 v[32:35], v[44:47], v[228:231], v[32:35]\nv_mfma_f32_16x16x32_bf16 v[56:59], v[28:31], v[208:211], 0\nv_cvt_pk_bf16_f32 v84, v84, v85\n" \
"v_cvt_pk_bf16_f32 v85, v86, v87\nv_mfma_f32_16x16x32_bf16 v[24:27], v[68:71], v[208:211], 0\nv_cvt_pk_bf16_f32 v86, v32, v33\nv_cvt_pk_bf16_f32 v87, v34, v35\n" \
"v_mfma_f32_16x16x32_bf16 v[56:59], v[48:51], v[212:215], v[56:59]\nv_mfma_f32_16x16x32_bf16 v[24:27], v[76:79], v[212:215], v[24:27]\nds_read_b128 v[20:23], v16 offset:0\nv_mfma_f32_16x16x32_bf16 v[56:59], v[64:67], v[216:219], v[56:59]\n" \
"ds_read_b128 v[40:43], v16 offset:32768\nv_mfma_f32_16x16x32_bf16 v[24:27], v[80:83], v[216:219], v[24:27]\nv_mfma_f32_16x16x32_bf16 v[56:59], v[72:75], v[220:223], v[56:59]\ns_waitcnt lgkmcnt(2)\n" \
"buffer_store_dwordx4 v[60:63], v16, s[8:11], s19 offen offset:2048 sc1\nv_mfma_f32_16x16x32_bf16 v[24:27], v[44:47], v[220:223], v[24:27]\nds_read_b128 v[36:39], v16 offset:1024\nv_mfma_f32_16x16x32_bf16 v[60:63], v[28:31], v[52:55], 0\n" \
"ds_read_b128 v[32:35], v16 offset:33792\nv_cvt_pk_bf16_f32 v56, v56, v57\nv_mfma_f32_16x16x32_bf16 v[28:31], v[68:71], v[52:55], 0\nv_cvt_pk_bf16_f32 v57, v58, v59\n" \
"v_cvt_pk_bf16_f32 v58, v24, v25\nv_mfma_f32_16x16x32_bf16 v[60:63], v[48:51], v[88:91], v[60:63]\nv_cvt_pk_bf16_f32 v59, v26, v27\nv_mfma_f32_16x16x32_bf16 v[28:31], v[76:79], v[88:91], v[28:31]\n" \
"ds_read_b128 v[52:55], v16 offset:2048\nv_mfma_f32_16x16x32_bf16 v[60:63], v[64:67], v[248:251], v[60:63]\nds_read_b128 v[24:27], v16 offset:34816\nv_mfma_f32_16x16x32_bf16 v[28:31], v[80:83], v[248:251], v[28:31]\n" \
"buffer_store_dwordx4 v[84:87], v16, s[8:11], s19 offen offset:1024 sc1\nv_mfma_f32_16x16x32_bf16 v[60:63], v[72:75], v[252:255], v[60:63]\ns_waitcnt lgkmcnt(2)\nv_mfma_f32_16x16x32_bf16 v[28:31], v[44:47], v[252:255], v[28:31]\n" \
"ds_read_b128 v[48:51], v16 offset:3072\nv_mfma_f32_16x16x32_bf16 v[44:47], v[0:3], v[20:23], 0\nds_read_b128 v[64:67], v16 offset:35840\nv_cvt_pk_bf16_f32 v60, v60, v61\n" \
"v_mfma_f32_16x16x32_bf16 v[68:71], v[0:3], v[40:43], 0\nv_cvt_pk_bf16_f32 v61, v62, v63\nv_cvt_pk_bf16_f32 v62, v28, v29\nv_mfma_f32_16x16x32_bf16 v[72:75], v[8:11], v[20:23], 0\n" \
"v_cvt_pk_bf16_f32 v63, v30, v31\nv_mfma_f32_16x16x32_bf16 v[20:23], v[8:11], v[40:43], 0\nds_read_b128 v[28:31], v16 offset:4096\nv_mfma_f32_16x16x32_bf16 v[44:47], v[4:7], v[36:39], v[44:47]\n" \
"ds_read_b128 v[40:43], v16 offset:36864\nv_mfma_f32_16x16x32_bf16 v[68:71], v[4:7], v[32:35], v[68:71]\nbuffer_store_dwordx4 v[56:59], v16, s[8:11], s19 offen offset:0 sc1\nv_mfma_f32_16x16x32_bf16 v[72:75], v[12:15], v[36:39], v[72:75]\n" \
"s_waitcnt lgkmcnt(2)\nv_mfma_f32_16x16x32_bf16 v[20:23], v[12:15], v[32:35], v[20:23]\nds_read_b128 v[36:39], v16 offset:5120\nv_mfma_f32_16x16x32_bf16 v[44:47], a[0:3], v[52:55], v[44:47]\n" \
"ds_read_b128 v[32:35], v16 offset:37888\nv_mfma_f32_16x16x32_bf16 v[68:71], a[0:3], v[24:27], v[68:71]\nv_mfma_f32_16x16x32_bf16 v[72:75], a[104:107], v[52:55], v[72:75]\nv_mfma_f32_16x16x32_bf16 v[20:23], a[104:107], v[24:27], v[20:23]\n" \
"ds_read_b128 v[52:55], v16 offset:6144\nv_mfma_f32_16x16x32_bf16 v[44:47], a[4:7], v[48:51], v[44:47]\nbuffer_store_dwordx4 v[60:63], v16, s[8:11], s19 offen offset:3072 sc1\nds_read_b128 v[24:27], v16 offset:38912\n" \
"v_mfma_f32_16x16x32_bf16 v[68:71], a[4:7], v[64:67], v[68:71]\nds_read_b128 v[56:59], v16 offset:7168\nv_mfma_f32_16x16x32_bf16 v[72:75], a[108:111], v[48:51], v[72:75]\nds_read_b128 v[60:63], v16 offset:39936\n" \
"v_mfma_f32_16x16x32_bf16 v[20:23], a[108:111], v[64:67], v[20:23]\ns_branch 1b\n2:\ns_cmp_lt_i32 s20, 1\n" \
"s_cbranch_scc1 3b\ns_add_u32 s30, s30, 1\ns_cmp_lt_u32 s30, 0x40000\ns_cbranch_scc0 9f\n" \
"v_cmp_eq_u32 vcc, -1, v36\ns_cbranch_vccz 6f\nbuffer_load_dwordx4 v16, s[8:11], s43 offen offset:0 sc1 lds\n6:\n" \
"v_cmp_eq_u32 vcc, -1, v80\ns_cbranch_vccz 6f\nbuffer_load_dwordx4 v16, s[8:11], s43 offen offset:1024 sc1 lds\n6:\n" \
"v_cmp_eq_u32 vcc, -1, v48\ns_cbranch_vccz 6f\nbuffer_load_dwordx4 v16, s[8:11], s43 offen offset:2048 sc1 lds\n6:\n" \
"v_cmp_eq_u32 vcc, -1, v84\ns_cbranch_vccz 6f\nbuffer_load_dwordx4 v16, s[8:11], s43 offen offset:3072 sc1 lds\n6:\n" \
"s_waitcnt vmcnt(0)\nds_read_b128 v[36:39], v61 offset:0\nds_read_b128 v[80:83], v61 offset:1024\nds_read_b128 v[48:51], v61 offset:2048\n" \
"ds_read_b128 v[84:87], v61 offset:3072\ns_waitcnt lgkmcnt(0)\nv_max3_u32 v52, v36, v80, v48\nv_max_u32 v52, v52, v84\n" \
"v_cmp_eq_u32 vcc, -1, v52\ns_cbranch_vccnz 2b\ns_branch 3b\n4:\n" \
"s_cmp_ge_i32 s20, 64\ns_cbranch_scc1 9f\ns_cmp_ge_i32 s20, 62\ns_cbranch_scc1 5b\n" \
"v_bfe_u32 v32, v16, 4, 3\nv_lshlrev_b32 v32, 2, v32\ns_lshl_b32 s43, s20, 5\ns_add_u32 s43, s43, 64\n" \
"s_add_u32 s43, s43, s26\n7:\nv_cmp_eq_u32 vcc, -1, v33\ns_cbranch_vccz 8f\n" \
"s_add_u32 s30, s30, 1\ns_cmp_lt_u32 s30, 0x40000\ns_cbranch_scc0 9f\ns_sleep 8\n" \
"buffer_load_dword v33, v32, s[8:11], s43 offen sc1\ns_waitcnt vmcnt(0)\ns_branch 7b\n8:\n" \
"s_mov_b32 s14, s10\ns_branch 5b\n9:\ns_waitcnt vmcnt(0)\n" \
"s_endpgm\n"
#define D_LOOP \
"s_branch 4f\n.p2align 6\n1:\ns_waitcnt lgkmcnt(0)\n" \
"v_mfma_f32_16x16x32_bf16 v[44:47], a[8:11], v[8:11], v[44:47]\nds_read_b128 v[16:19], v4 offset:3072\ns_mov_b32 m0, s24\nv_mfma_f32_16x16x32_bf16 v[64:67], a[8:11], v[12:15], v[64:67]\n" \
"ds_read_b128 v[32:35], v4 offset:35840\nbuffer_load_dword v1, s[16:19], 0 offen offset:0 sc1 lds\nv_mfma_f32_16x16x32_bf16 v[48:51], a[104:107], v[8:11], v[48:51]\ns_add_u32 m0, s23, 0\n" \
"s_and_b32 s98, s20, 1\ns_lshl_b32 s98, s98, 3\nv_mfma_f32_16x16x32_bf16 v[20:23], a[104:107], v[12:15], v[20:23]\nv_mfma_f32_16x16x32_bf16 v[56:59], a[200:203], v[8:11], v[56:59]\n" \
"v_mfma_f32_16x16x32_bf16 v[24:27], a[200:203], v[12:15], v[24:27]\nv_mfma_f32_16x16x32_bf16 v[60:63], v[152:155], v[8:11], v[60:63]\ns_waitcnt lgkmcnt(1)\nv_mfma_f32_16x16x32_bf16 v[28:31], v[152:155], v[12:15], v[28:31]\n" \
"s_waitcnt lgkmcnt(0)\nds_read_b128 v[8:11], v4 offset:4096\nv_mfma_f32_16x16x32_bf16 v[44:47], a[12:15], v[16:19], v[44:47]\nds_read_b128 v[12:15], v4 offset:36864\n" \
"v_mfma_f32_16x16x32_bf16 v[64:67], a[12:15], v[32:35], v[64:67]\nv_mfma_f32_16x16x32_bf16 v[48:51], a[108:111], v[16:19], v[48:51]\nv_mfma_f32_16x16x32_bf16 v[20:23], a[108:111], v[32:35], v[20:23]\nv_mfma_f32_16x16x32_bf16 v[56:59], a[204:207], v[16:19], v[56:59]\n" \
"v_mfma_f32_16x16x32_bf16 v[24:27], a[204:207], v[32:35], v[24:27]\nv_mfma_f32_16x16x32_bf16 v[60:63], v[156:159], v[16:19], v[60:63]\ns_waitcnt lgkmcnt(0)\nv_mfma_f32_16x16x32_bf16 v[28:31], v[156:159], v[32:35], v[28:31]\n" \
"ds_read_b128 v[16:19], v4 offset:5120\nv_mfma_f32_16x16x32_bf16 v[44:47], a[16:19], v[8:11], v[44:47]\nds_read_b128 v[32:35], v4 offset:37888\nv_mfma_f32_16x16x32_bf16 v[64:67], a[16:19], v[12:15], v[64:67]\n" \
"v_mfma_f32_16x16x32_bf16 v[48:51], a[112:115], v[8:11], v[48:51]\nv_mfma_f32_16x16x32_bf16 v[20:23], a[112:115], v[12:15], v[20:23]\nv_mfma_f32_16x16x32_bf16 v[56:59], a[208:211], v[8:11], v[56:59]\nv_mfma_f32_16x16x32_bf16 v[24:27], a[208:211], v[12:15], v[24:27]\n" \
"v_mfma_f32_16x16x32_bf16 v[60:63], v[160:163], v[8:11], v[60:63]\ns_waitcnt lgkmcnt(0)\nv_mfma_f32_16x16x32_bf16 v[28:31], v[160:163], v[12:15], v[28:31]\nds_read_b128 v[8:11], v4 offset:6144\n" \
"s_add_u32 s99, s20, 1\nv_mfma_f32_16x16x32_bf16 v[44:47], a[20:23], v[16:19], v[44:47]\nds_read_b128 v[12:15], v4 offset:38912\ns_lshr_b32 s97, s99, 1\n" \
"v_mfma_f32_16x16x32_bf16 v[64:67], a[20:23], v[32:35], v[64:67]\ns_and_b32 s99, s99, 1\ns_lshl_b32 s99, s99, 3\nv_readlane_b32 s97, v3, s97\n" \
"v_mfma_f32_16x16x32_bf16 v[48:51], a[116:119], v[16:19], v[48:51]\nv_mfma_f32_16x16x32_bf16 v[20:23], a[116:119], v[32:35], v[20:23]\ns_lshr_b32 s97, s97, s99\ns_and_b32 s97, s97, 0xff\n" \
"s_mul_i32 s97, s97, 0x38000\nv_mfma_f32_16x16x32_bf16 v[56:59], a[212:215], v[16:19], v[56:59]\ns_add_u32 s97, s97, s21\nv_mfma_f32_16x16x32_bf16 v[24:27], a[212:215], v[32:35], v[24:27]\n" \
"v_mfma_f32_16x16x32_bf16 v[60:63], v[164:167], v[16:19], v[60:63]\ns_waitcnt lgkmcnt(0)\nv_mfma_f32_16x16x32_bf16 v[28:31], v[164:167], v[32:35], v[28:31]\nds_read_b128 v[16:19], v4 offset:7168\n" \
"v_mfma_f32_16x16x32_bf16 v[44:47], a[24:27], v[8:11], v[44:47]\nds_read_b128 v[32:35], v4 offset:39936\nv_mfma_f32_16x16x32_bf16 v[64:67], a[24:27], v[12:15], v[64:67]\nv_mfma_f32_16x16x32_bf16 v[48:51], a[120:123], v[8:11], v[48:51]\n" \
"v_mfma_f32_16x16x32_bf16 v[20:23], a[120:123], v[12:15], v[20:23]\nv_mfma_f32_16x16x32_bf16 v[56:59], a[216:219], v[8:11], v[56:59]\nv_mfma_f32_16x16x32_bf16 v[24:27], a[216:219], v[12:15], v[24:27]\nv_mfma_f32_16x16x32_bf16 v[60:63], v[168:171], v[8:11], v[60:63]\n" \
"s_waitcnt lgkmcnt(0)\nv_mfma_f32_16x16x32_bf16 v[28:31], v[168:171], v[12:15], v[28:31]\nds_read_b128 v[8:11], v4 offset:8192\nv_mov_b32 v111, s20\n" \
"v_mfma_f32_16x16x32_bf16 v[44:47], a[28:31], v[16:19], v[44:47]\nds_read_b128 v[12:15], v4 offset:40960\ns_add_u32 s99, s20, 2\nv_mfma_f32_16x16x32_bf16 v[64:67], a[28:31], v[32:35], v[64:67]\n" \
"v_mfma_f32_16x16x32_bf16 v[48:51], a[124:127], v[16:19], v[48:51]\nv_mfma_f32_16x16x32_bf16 v[20:23], a[124:127], v[32:35], v[20:23]\nv_mfma_f32_16x16x32_bf16 v[56:59], a[220:223], v[16:19], v[56:59]\ns_waitcnt vmcnt(0)\n" \
"s_barrier\nv_mfma_f32_16x16x32_bf16 v[24:27], a[220:223], v[32:35], v[24:27]\nbuffer_store_dword v111, v2, s[8:11], s25 offen offset:0 sc1\nv_mfma_f32_16x16x32_bf16 v[60:63], v[172:175], v[16:19], v[60:63]\n" \
"ds_read_b32 v110, v5 offset:0\ns_mov_b32 s25, 0\nv_mfma_f32_16x16x32_bf16 v[28:31], v[172:175], v[32:35], v[28:31]\ns_waitcnt lgkmcnt(1)\n" \
"ds_read_b128 v[16:19], v4 offset:9216\nv_mfma_f32_16x16x32_bf16 v[44:47], a[32:35], v[8:11], v[44:47]\nds_read_b128 v[32:35], v4 offset:41984\nv_mfma_f32_16x16x32_bf16 v[64:67], a[32:35], v[12:15], v[64:67]\n" \
"v_mfma_f32_16x16x32_bf16 v[48:51], a[128:131], v[8:11], v[48:51]\ns_waitcnt lgkmcnt(2)\nv_cmp_gt_i32 s[94:95], s99, v110\nv_mfma_f32_16x16x32_bf16 v[20:23], a[128:131], v[12:15], v[20:23]\n" \
"v_mfma_f32_16x16x32_bf16 v[56:59], a[224:227], v[8:11], v[56:59]\ns_cmp_eq_u64 s[94:95], 0\ns_cselect_b32 s14, s10, 0\nv_mfma_f32_16x16x32_bf16 v[24:27], a[224:227], v[12:15], v[24:27]\n" \
"v_mfma_f32_16x16x32_bf16 v[60:63], v[176:179], v[8:11], v[60:63]\ns_waitcnt lgkmcnt(0)\nv_mfma_f32_16x16x32_bf16 v[28:31], v[176:179], v[12:15], v[28:31]\nds_read_b128 v[8:11], v4 offset:10240\n" \
"v_mfma_f32_16x16x32_bf16 v[44:47], a[36:39], v[16:19], v[44:47]\nds_read_b128 v[12:15], v4 offset:43008\nv_mfma_f32_16x16x32_bf16 v[64:67], a[36:39], v[32:35], v[64:67]\nv_mfma_f32_16x16x32_bf16 v[48:51], a[132:135], v[16:19], v[48:51]\n" \
"v_mfma_f32_16x16x32_bf16 v[20:23], a[132:135], v[32:35], v[20:23]\nbuffer_load_dwordx4 v0, s[12:15], s97 offen offset:0 sc1 lds\nv_mfma_f32_16x16x32_bf16 v[56:59], a[228:231], v[16:19], v[56:59]\nv_mfma_f32_16x16x32_bf16 v[24:27], a[228:231], v[32:35], v[24:27]\n" \
"v_mfma_f32_16x16x32_bf16 v[60:63], v[180:183], v[16:19], v[60:63]\ns_waitcnt lgkmcnt(0)\nv_mfma_f32_16x16x32_bf16 v[28:31], v[180:183], v[32:35], v[28:31]\nbuffer_load_dwordx4 v0, s[12:15], s97 offen offset:1024 sc1 lds\n" \
"v_mfma_f32_16x16x32_bf16 v[44:47], a[40:43], v[8:11], v[44:47]\nds_read_b128 v[16:19], v4 offset:11264\nv_mfma_f32_16x16x32_bf16 v[64:67], a[40:43], v[12:15], v[64:67]\nds_read_b128 v[32:35], v4 offset:44032\n" \
"v_mfma_f32_16x16x32_bf16 v[48:51], a[136:139], v[8:11], v[48:51]\nv_mfma_f32_16x16x32_bf16 v[20:23], a[136:139], v[12:15], v[20:23]\nbuffer_load_dwordx4 v0, s[12:15], s97 offen offset:2048 sc1 lds\nv_mfma_f32_16x16x32_bf16 v[56:59], a[232:235], v[8:11], v[56:59]\n" \
"v_mfma_f32_16x16x32_bf16 v[24:27], a[232:235], v[12:15], v[24:27]\nv_mfma_f32_16x16x32_bf16 v[60:63], v[184:187], v[8:11], v[60:63]\ns_waitcnt lgkmcnt(1)\nv_mfma_f32_16x16x32_bf16 v[28:31], v[184:187], v[12:15], v[28:31]\n" \
"s_waitcnt lgkmcnt(0)\nbuffer_load_dwordx4 v0, s[12:15], s97 offen offset:3072 sc1 lds\nv_mfma_f32_16x16x32_bf16 v[44:47], a[44:47], v[16:19], v[44:47]\nds_read_b128 v[8:11], v4 offset:12288\n" \
"s_add_u32 m0, s23, 0x1000\nv_mfma_f32_16x16x32_bf16 v[64:67], a[44:47], v[32:35], v[64:67]\nds_read_b128 v[12:15], v4 offset:45056\ns_add_u32 s97, s97, 0x1000\n" \
"v_mfma_f32_16x16x32_bf16 v[48:51], a[140:143], v[16:19], v[48:51]\nv_mfma_f32_16x16x32_bf16 v[20:23], a[140:143], v[32:35], v[20:23]\nbuffer_load_dwordx4 v0, s[12:15], s97 offen offset:0 sc1 lds\nv_mfma_f32_16x16x32_bf16 v[56:59], a[236:239], v[16:19], v[56:59]\n" \
"v_mfma_f32_16x16x32_bf16 v[24:27], a[236:239], v[32:35], v[24:27]\nv_mfma_f32_16x16x32_bf16 v[60:63], v[188:191], v[16:19], v[60:63]\ns_waitcnt lgkmcnt(1)\nv_mfma_f32_16x16x32_bf16 v[28:31], v[188:191], v[32:35], v[28:31]\n" \
"s_waitcnt lgkmcnt(0)\nbuffer_load_dwordx4 v0, s[12:15], s97 offen offset:1024 sc1 lds\nv_mfma_f32_16x16x32_bf16 v[44:47], a[48:51], v[8:11], v[44:47]\nds_read_b128 v[16:19], v4 offset:13312\n" \
"v_mfma_f32_16x16x32_bf16 v[64:67], a[48:51], v[12:15], v[64:67]\nds_read_b128 v[32:35], v4 offset:46080\nv_mfma_f32_16x16x32_bf16 v[48:51], a[144:147], v[8:11], v[48:51]\nv_mfma_f32_16x16x32_bf16 v[20:23], a[144:147], v[12:15], v[20:23]\n" \
"buffer_load_dwordx4 v0, s[12:15], s97 offen offset:2048 sc1 lds\nv_mfma_f32_16x16x32_bf16 v[56:59], a[240:243], v[8:11], v[56:59]\nv_mfma_f32_16x16x32_bf16 v[24:27], a[240:243], v[12:15], v[24:27]\nv_mfma_f32_16x16x32_bf16 v[60:63], v[192:195], v[8:11], v[60:63]\n" \
"s_waitcnt lgkmcnt(1)\nv_mfma_f32_16x16x32_bf16 v[28:31], v[192:195], v[12:15], v[28:31]\ns_waitcnt lgkmcnt(0)\nbuffer_load_dwordx4 v0, s[12:15], s97 offen offset:3072 sc1 lds\n" \
"v_mfma_f32_16x16x32_bf16 v[44:47], a[52:55], v[16:19], v[44:47]\nds_read_b128 v[8:11], v4 offset:14336\ns_add_u32 m0, s23, 0x2000\nv_mfma_f32_16x16x32_bf16 v[64:67], a[52:55], v[32:35], v[64:67]\n" \
"ds_read_b128 v[12:15], v4 offset:47104\ns_add_u32 s97, s97, 0x1000\nv_mfma_f32_16x16x32_bf16 v[48:51], a[148:151], v[16:19], v[48:51]\ns_xor_b32 s23, s23, 0x10000\n" \
"v_mfma_f32_16x16x32_bf16 v[20:23], a[148:151], v[32:35], v[20:23]\nbuffer_load_dwordx4 v0, s[12:15], s97 offen offset:0 sc1 lds\nv_mfma_f32_16x16x32_bf16 v[56:59], a[244:247], v[16:19], v[56:59]\nv_mfma_f32_16x16x32_bf16 v[24:27], a[244:247], v[32:35], v[24:27]\n" \
"v_mfma_f32_16x16x32_bf16 v[60:63], v[196:199], v[16:19], v[60:63]\ns_waitcnt lgkmcnt(1)\nv_mfma_f32_16x16x32_bf16 v[28:31], v[196:199], v[32:35], v[28:31]\ns_waitcnt lgkmcnt(0)\n" \
"buffer_load_dwordx4 v0, s[12:15], s97 offen offset:1024 sc1 lds\nv_mfma_f32_16x16x32_bf16 v[44:47], a[56:59], v[8:11], v[44:47]\nds_read_b128 v[16:19], v4 offset:15360\nv_mfma_f32_16x16x32_bf16 v[64:67], a[56:59], v[12:15], v[64:67]\n" \
"ds_read_b128 v[32:35], v4 offset:48128\nv_mfma_f32_16x16x32_bf16 v[48:51], a[152:155], v[8:11], v[48:51]\nv_mfma_f32_16x16x32_bf16 v[20:23], a[152:155], v[12:15], v[20:23]\nbuffer_load_dwordx4 v0, s[12:15], s97 offen offset:2048 sc1 lds\n" \
"v_mfma_f32_16x16x32_bf16 v[56:59], a[248:251], v[8:11], v[56:59]\nv_mfma_f32_16x16x32_bf16 v[24:27], a[248:251], v[12:15], v[24:27]\nv_mfma_f32_16x16x32_bf16 v[60:63], v[200:203], v[8:11], v[60:63]\ns_waitcnt lgkmcnt(1)\n" \
"v_mfma_f32_16x16x32_bf16 v[28:31], v[200:203], v[12:15], v[28:31]\ns_waitcnt lgkmcnt(0)\nbuffer_load_dwordx4 v0, s[12:15], s97 offen offset:3072 sc1 lds\nv_mfma_f32_16x16x32_bf16 v[44:47], a[60:63], v[16:19], v[44:47]\n" \
"ds_read_b128 v[8:11], v4 offset:16384\nv_mfma_f32_16x16x32_bf16 v[64:67], a[60:63], v[32:35], v[64:67]\nds_read_b128 v[12:15], v4 offset:49152\nv_mfma_f32_16x16x32_bf16 v[48:51], a[156:159], v[16:19], v[48:51]\n" \
"v_mfma_f32_16x16x32_bf16 v[20:23], a[156:159], v[32:35], v[20:23]\nv_mfma_f32_16x16x32_bf16 v[56:59], a[252:255], v[16:19], v[56:59]\nv_mfma_f32_16x16x32_bf16 v[24:27], a[252:255], v[32:35], v[24:27]\nv_mfma_f32_16x16x32_bf16 v[60:63], v[204:207], v[16:19], v[60:63]\n" \
"s_waitcnt lgkmcnt(1)\nv_mfma_f32_16x16x32_bf16 v[28:31], v[204:207], v[32:35], v[28:31]\ns_waitcnt lgkmcnt(0)\nds_read_b128 v[16:19], v4 offset:17408\n" \
"v_mfma_f32_16x16x32_bf16 v[44:47], a[64:67], v[8:11], v[44:47]\nds_read_b128 v[32:35], v4 offset:50176\nv_mfma_f32_16x16x32_bf16 v[64:67], a[64:67], v[12:15], v[64:67]\nv_mfma_f32_16x16x32_bf16 v[48:51], a[160:163], v[8:11], v[48:51]\n" \
"v_mfma_f32_16x16x32_bf16 v[20:23], a[160:163], v[12:15], v[20:23]\nv_mfma_f32_16x16x32_bf16 v[56:59], v[112:115], v[8:11], v[56:59]\nv_mfma_f32_16x16x32_bf16 v[24:27], v[112:115], v[12:15], v[24:27]\nv_mfma_f32_16x16x32_bf16 v[60:63], v[208:211], v[8:11], v[60:63]\n" \
"s_waitcnt lgkmcnt(0)\nv_mfma_f32_16x16x32_bf16 v[28:31], v[208:211], v[12:15], v[28:31]\nds_read_b128 v[8:11], v4 offset:18432\nv_mfma_f32_16x16x32_bf16 v[44:47], a[68:71], v[16:19], v[44:47]\n" \
"ds_read_b128 v[12:15], v4 offset:51200\nv_mfma_f32_16x16x32_bf16 v[64:67], a[68:71], v[32:35], v[64:67]\nv_mfma_f32_16x16x32_bf16 v[48:51], a[164:167], v[16:19], v[48:51]\nv_mfma_f32_16x16x32_bf16 v[20:23], a[164:167], v[32:35], v[20:23]\n" \
"s_lshr_b32 s99, s20, 1\ns_add_u32 s20, s20, 1\nv_readlane_b32 s99, v3, s99\nv_mfma_f32_16x16x32_bf16 v[56:59], v[116:119], v[16:19], v[56:59]\n" \
"v_mfma_f32_16x16x32_bf16 v[24:27], v[116:119], v[32:35], v[24:27]\ns_lshr_b32 s99, s99, s98\ns_and_b32 s99, s99, 0xff\ns_mul_i32 s99, s99, 0x50000\n" \
"v_mfma_f32_16x16x32_bf16 v[60:63], v[212:215], v[16:19], v[60:63]\ns_waitcnt lgkmcnt(0)\ns_add_u32 s99, s99, s22\ns_add_u32 s98, s99, 0x8000\n" \
"v_mfma_f32_16x16x32_bf16 v[28:31], v[212:215], v[32:35], v[28:31]\nds_read_b128 v[16:19], v4 offset:19456\nv_mfma_f32_16x16x32_bf16 v[44:47], a[72:75], v[8:11], v[44:47]\nds_read_b128 v[32:35], v4 offset:52224\n" \
"v_mfma_f32_16x16x32_bf16 v[64:67], a[72:75], v[12:15], v[64:67]\nv_mfma_f32_16x16x32_bf16 v[48:51], a[168:171], v[8:11], v[48:51]\nv_mfma_f32_16x16x32_bf16 v[20:23], a[168:171], v[12:15], v[20:23]\nv_mfma_f32_16x16x32_bf16 v[56:59], v[120:123], v[8:11], v[56:59]\n" \
"v_mfma_f32_16x16x32_bf16 v[24:27], v[120:123], v[12:15], v[24:27]\nv_mfma_f32_16x16x32_bf16 v[60:63], v[216:219], v[8:11], v[60:63]\ns_waitcnt lgkmcnt(0)\nv_mfma_f32_16x16x32_bf16 v[28:31], v[216:219], v[12:15], v[28:31]\n" \
"ds_read_b128 v[8:11], v4 offset:20480\nv_mfma_f32_16x16x32_bf16 v[44:47], a[76:79], v[16:19], v[44:47]\nds_read_b128 v[12:15], v4 offset:53248\nv_mfma_f32_16x16x32_bf16 v[64:67], a[76:79], v[32:35], v[64:67]\n" \
"v_mfma_f32_16x16x32_bf16 v[48:51], a[172:175], v[16:19], v[48:51]\nv_mfma_f32_16x16x32_bf16 v[20:23], a[172:175], v[32:35], v[20:23]\nv_mfma_f32_16x16x32_bf16 v[56:59], v[124:127], v[16:19], v[56:59]\nv_mfma_f32_16x16x32_bf16 v[24:27], v[124:127], v[32:35], v[24:27]\n" \
"v_mfma_f32_16x16x32_bf16 v[60:63], v[220:223], v[16:19], v[60:63]\ns_waitcnt lgkmcnt(0)\nv_mfma_f32_16x16x32_bf16 v[28:31], v[220:223], v[32:35], v[28:31]\nds_read_b128 v[36:39], v4 offset:21504\n" \
"v_mfma_f32_16x16x32_bf16 v[44:47], a[80:83], v[8:11], v[44:47]\nds_read_b128 v[32:35], v4 offset:54272\nv_mfma_f32_16x16x32_bf16 v[64:67], a[80:83], v[12:15], v[64:67]\nv_mfma_f32_16x16x32_bf16 v[48:51], a[176:179], v[8:11], v[48:51]\n" \
"v_mfma_f32_16x16x32_bf16 v[20:23], a[176:179], v[12:15], v[20:23]\nv_mfma_f32_16x16x32_bf16 v[56:59], v[128:131], v[8:11], v[56:59]\nv_mfma_f32_16x16x32_bf16 v[24:27], v[128:131], v[12:15], v[24:27]\nv_mfma_f32_16x16x32_bf16 v[60:63], v[224:227], v[8:11], v[60:63]\n" \
"s_waitcnt lgkmcnt(0)\nv_mfma_f32_16x16x32_bf16 v[28:31], v[224:227], v[12:15], v[28:31]\nds_read_b128 v[40:43], v4 offset:22528\nv_mfma_f32_16x16x32_bf16 v[44:47], a[84:87], v[36:39], v[44:47]\n" \
"ds_read_b128 v[16:19], v4 offset:55296\nv_xor_b32 v4, 0x10000, v4\nv_mfma_f32_16x16x32_bf16 v[64:67], a[84:87], v[32:35], v[64:67]\nv_mfma_f32_16x16x32_bf16 v[48:51], a[180:183], v[36:39], v[48:51]\n" \
"v_mfma_f32_16x16x32_bf16 v[20:23], a[180:183], v[32:35], v[20:23]\nv_mfma_f32_16x16x32_bf16 v[56:59], v[132:135], v[36:39], v[56:59]\nv_mfma_f32_16x16x32_bf16 v[24:27], v[132:135], v[32:35], v[24:27]\nv_mfma_f32_16x16x32_bf16 v[60:63], v[228:231], v[36:39], v[60:63]\n" \
"s_waitcnt lgkmcnt(0)\nv_mfma_f32_16x16x32_bf16 v[28:31], v[228:231], v[32:35], v[28:31]\ns_waitcnt vmcnt(0)\ns_barrier\n" \
"v_mfma_f32_16x16x32_bf16 v[44:47], a[88:91], v[40:43], v[44:47]\nds_read_b128 v[8:11], v4 offset:0\nv_mfma_f32_16x16x32_bf16 v[48:51], a[184:187], v[40:43], v[48:51]\nds_read_b128 v[12:15], v4 offset:32768\n" \
"v_mfma_f32_16x16x32_bf16 v[56:59], v[136:139], v[40:43], v[56:59]\nv_cvt_pk_bf16_f32 v36, v44, v45\nv_cvt_pk_bf16_f32 v37, v46, v47\nv_mfma_f32_16x16x32_bf16 v[60:63], v[232:235], v[40:43], v[60:63]\n" \
"v_cvt_pk_bf16_f32 v38, v48, v49\nv_cvt_pk_bf16_f32 v39, v50, v51\nv_mfma_f32_16x16x32_bf16 v[64:67], a[88:91], v[16:19], v[64:67]\nv_cvt_pk_bf16_f32 v32, v56, v57\n" \
"v_cvt_pk_bf16_f32 v33, v58, v59\nv_mfma_f32_16x16x32_bf16 v[20:23], a[184:187], v[16:19], v[20:23]\nbuffer_store_dwordx4 v[36:39], v6, s[8:11], s99 offen offset:0 sc1\nv_cvt_pk_bf16_f32 v34, v60, v61\n" \
"v_cvt_pk_bf16_f32 v35, v62, v63\nv_mfma_f32_16x16x32_bf16 v[24:27], v[136:139], v[16:19], v[24:27]\ns_waitcnt lgkmcnt(1)\nv_cvt_pk_bf16_f32 v36, v64, v65\n" \
"v_cvt_pk_bf16_f32 v37, v66, v67\nv_mfma_f32_16x16x32_bf16 v[28:31], v[232:235], v[16:19], v[28:31]\nds_read_b128 v[40:43], v4 offset:1024\nv_cvt_pk_bf16_f32 v38, v20, v21\n" \
"v_mfma_f32_16x16x32_bf16 v[44:47], a[0:3], v[8:11], 0\nds_read_b128 v[16:19], v4 offset:33792\nv_cvt_pk_bf16_f32 v39, v22, v23\nv_mfma_f32_16x16x32_bf16 v[48:51], a[96:99], v[8:11], 0\n" \
"buffer_store_dwordx4 v[32:35], v6, s[8:11], s99 offen offset:512 sc1\nv_cvt_pk_bf16_f32 v52, v24, v25\nv_cvt_pk_bf16_f32 v53, v26, v27\nv_cvt_pk_bf16_f32 v54, v28, v29\n" \
"v_mfma_f32_16x16x32_bf16 v[56:59], a[192:195], v[8:11], 0\ns_waitcnt lgkmcnt(2)\nv_cvt_pk_bf16_f32 v55, v30, v31\nv_mfma_f32_16x16x32_bf16 v[60:63], v[144:147], v[8:11], 0\n" \
"v_mfma_f32_16x16x32_bf16 v[64:67], a[0:3], v[12:15], 0\nv_mfma_f32_16x16x32_bf16 v[20:23], a[96:99], v[12:15], 0\nbuffer_store_dwordx4 v[36:39], v6, s[8:11], s98 offen offset:0 sc1\nv_mfma_f32_16x16x32_bf16 v[24:27], a[192:195], v[12:15], 0\n" \
"s_waitcnt lgkmcnt(0)\nv_mfma_f32_16x16x32_bf16 v[28:31], v[144:147], v[12:15], 0\nds_read_b128 v[8:11], v4 offset:2048\nv_mfma_f32_16x16x32_bf16 v[44:47], a[4:7], v[40:43], v[44:47]\n" \
"ds_read_b128 v[12:15], v4 offset:34816\nv_mfma_f32_16x16x32_bf16 v[64:67], a[4:7], v[16:19], v[64:67]\nbuffer_store_dwordx4 v[52:55], v6, s[8:11], s98 offen offset:512 sc1\nv_mfma_f32_16x16x32_bf16 v[48:51], a[100:103], v[40:43], v[48:51]\n" \
"v_mfma_f32_16x16x32_bf16 v[20:23], a[100:103], v[16:19], v[20:23]\nv_mfma_f32_16x16x32_bf16 v[56:59], a[196:199], v[40:43], v[56:59]\nv_mfma_f32_16x16x32_bf16 v[24:27], a[196:199], v[16:19], v[24:27]\nv_mfma_f32_16x16x32_bf16 v[60:63], v[148:151], v[40:43], v[60:63]\n" \
"s_cmp_eq_u32 s14, 0\nv_mfma_f32_16x16x32_bf16 v[28:31], v[148:151], v[16:19], v[28:31]\ns_cbranch_scc0 1b\n4:\n" \
"buffer_load_dword v68, v1, s[8:11], 0 offen sc1\nv_mov_b32 v69, s20\ns_add_u32 s98, s20, 1\ns_waitcnt vmcnt(0)\n" \
"v_cmp_gt_i32 vcc, s98, v68\ns_cbranch_vccz 8f\nbuffer_store_dword v69, v2, s[8:11], 0 offen sc1\ns_mov_b32 s25, 0x40000000\n" \
"s_cmp_ge_u32 s20, 0x80\ns_cbranch_scc1 9f\n7:\ns_add_u32 s26, s26, 1\n" \
"s_cmp_lt_u32 s26, 0x40000\ns_cbranch_scc0 9f\ns_sleep 8\nbuffer_load_dword v68, v1, s[8:11], 0 offen sc1\n" \
"s_waitcnt vmcnt(0)\nv_cmp_gt_i32 vcc, s98, v68\ns_cbranch_vccnz 7b\n8:\n" \
"s_lshr_b32 s98, s20, 1\nv_readlane_b32 s98, v3, s98\ns_and_b32 s99, s20, 1\ns_lshl_b32 s99, s99, 3\n" \
"s_lshr_b32 s98, s98, s99\ns_and_b32 s98, s98, 0xff\ns_mul_i32 s98, s98, 0x38000\ns_add_u32 s98, s98, s21\n" \
"s_xor_b32 s99, s23, 0x10000\ns_add_u32 m0, s99, 0\ns_add_u32 s98, s98, 0\nbuffer_load_dwordx4 v0, s[8:11], s98 offen offset:0 sc1 lds\n" \
"buffer_load_dwordx4 v0, s[8:11], s98 offen offset:1024 sc1 lds\nbuffer_load_dwordx4 v0, s[8:11], s98 offen offset:2048 sc1 lds\nbuffer_load_dwordx4 v0, s[8:11], s98 offen offset:3072 sc1 lds\ns_add_u32 m0, s99, 0x1000\n" \
"s_add_u32 s98, s98, 0x1000\nbuffer_load_dwordx4 v0, s[8:11], s98 offen offset:0 sc1 lds\nbuffer_load_dwordx4 v0, s[8:11], s98 offen offset:1024 sc1 lds\nbuffer_load_dwordx4 v0, s[8:11], s98 offen offset:2048 sc1 lds\n" \
"buffer_load_dwordx4 v0, s[8:11], s98 offen offset:3072 sc1 lds\ns_add_u32 m0, s99, 0x2000\ns_add_u32 s98, s98, 0x1000\nbuffer_load_dwordx4 v0, s[8:11], s98 offen offset:0 sc1 lds\n" \
"buffer_load_dwordx4 v0, s[8:11], s98 offen offset:1024 sc1 lds\nbuffer_load_dwordx4 v0, s[8:11], s98 offen offset:2048 sc1 lds\nbuffer_load_dwordx4 v0, s[8:11], s98 offen offset:3072 sc1 lds\nbuffer_store_dword v69, v2, s[8:11], s25 offen sc1\n" \
"s_mov_b32 s25, 0x40000000\ns_waitcnt vmcnt(1) lgkmcnt(0)\ns_barrier\nds_read_b128 v[68:71], v4 offset:0\n" \
"ds_read_b128 v[72:75], v4 offset:32768\nds_read_b128 v[76:79], v4 offset:1024\nds_read_b128 v[80:83], v4 offset:33792\nds_read_b128 v[8:11], v4 offset:2048\n" \
"ds_read_b128 v[12:15], v4 offset:34816\ns_waitcnt lgkmcnt(0)\nv_mfma_f32_16x16x32_bf16 v[44:47], a[0:3], v[68:71], 0\nv_mfma_f32_16x16x32_bf16 v[48:51], a[96:99], v[68:71], 0\n" \
"v_mfma_f32_16x16x32_bf16 v[56:59], a[192:195], v[68:71], 0\nv_mfma_f32_16x16x32_bf16 v[60:63], v[144:147], v[68:71], 0\nv_mfma_f32_16x16x32_bf16 v[64:67], a[0:3], v[72:75], 0\nv_mfma_f32_16x16x32_bf16 v[20:23], a[96:99], v[72:75], 0\n" \
"v_mfma_f32_16x16x32_bf16 v[24:27], a[192:195], v[72:75], 0\nv_mfma_f32_16x16x32_bf16 v[28:31], v[144:147], v[72:75], 0\nv_mfma_f32_16x16x32_bf16 v[44:47], a[4:7], v[76:79], v[44:47]\nv_mfma_f32_16x16x32_bf16 v[48:51], a[100:103], v[76:79], v[48:51]\n" \
"v_mfma_f32_16x16x32_bf16 v[56:59], a[196:199], v[76:79], v[56:59]\nv_mfma_f32_16x16x32_bf16 v[60:63], v[148:151], v[76:79], v[60:63]\nv_mfma_f32_16x16x32_bf16 v[64:67], a[4:7], v[80:83], v[64:67]\nv_mfma_f32_16x16x32_bf16 v[20:23], a[100:103], v[80:83], v[20:23]\n" \
"v_mfma_f32_16x16x32_bf16 v[24:27], a[196:199], v[80:83], v[24:27]\nv_mfma_f32_16x16x32_bf16 v[28:31], v[148:151], v[80:83], v[28:31]\ns_branch 1b\n9:\n" \
"s_waitcnt vmcnt(0)\ns_endpgm\n"
#define G_LOOP \
"s_branch 4f\n.p2align 6\n1:\ns_waitcnt lgkmcnt(2)\n" \
"v_mfma_f32_16x16x32_bf16 v[8:11], a[104:107], v[36:39], v[8:11]\ns_lshr_b32 s99, s20, 1\ns_and_b32 s98, s20, 1\nv_mov_b32 v110, s20\n" \
"v_mfma_f32_16x16x32_bf16 v[12:15], a[104:107], v[40:43], v[12:15]\ns_add_u32 s20, s20, 1\ns_add_u32 s97, s20, 1\ns_lshr_b32 s96, s97, 1\n" \
"v_mfma_f32_16x16x32_bf16 v[16:19], a[232:235], v[36:39], v[16:19]\nds_read_b128 v[52:55], v7 offset:28672\nv_readlane_b32 s96, v6, s96\nv_mfma_f32_16x16x32_bf16 v[20:23], a[232:235], v[40:43], v[20:23]\n" \
"ds_read_b128 v[56:59], v7 offset:61440\ns_and_b32 s97, s97, 1\nv_mfma_f32_16x16x32_bf16 v[24:27], v[216:219], v[36:39], v[24:27]\ns_lshl_b32 s97, s97, 3\n" \
"s_lshr_b32 s96, s96, s97\ns_and_b32 s96, s96, 0xff\nv_mfma_f32_16x16x32_bf16 v[28:31], v[216:219], v[40:43], v[28:31]\ns_waitcnt lgkmcnt(2)\n" \
"s_lshl_b32 s97, s96, 13\nv_xor_b32 v4, 0x400, v4\nv_mfma_f32_16x16x32_bf16 v[8:11], a[108:111], v[44:47], v[8:11]\ns_lshl_b32 s98, s98, 3\n" \
"v_mov_b32 v32, v111\nv_mov_b32 v33, v111\nv_mfma_f32_16x16x32_bf16 v[12:15], a[108:111], v[48:51], v[12:15]\ns_lshl_b32 s96, s96, 16\n" \
"v_mul_f32 v252, 0xc0135761, v34\nv_mul_f32 v34, v34, v34\nv_mfma_f32_16x16x32_bf16 v[16:19], a[236:239], v[44:47], v[16:19]\nds_read_b128 v[36:39], v7 offset:29696\n" \
"v_readlane_b32 s99, v6, s99\nv_mfma_f32_16x16x32_bf16 v[20:23], a[236:239], v[48:51], v[20:23]\nds_read_b128 v[40:43], v7 offset:62464\nv_mul_f32 v34, v34, v252\n" \
"v_mfma_f32_16x16x32_bf16 v[24:27], v[220:223], v[44:47], v[24:27]\nds_read_b128 v[60:63], v7 offset:30720\nv_mul_f32 v254, v35, v35\nv_mfma_f32_16x16x32_bf16 v[28:31], v[220:223], v[48:51], v[28:31]\n" \
"s_waitcnt lgkmcnt(3)\nv_permlane16_swap_b32 v32, v33\nv_mfma_f32_16x16x32_bf16 v[8:11], a[112:115], v[52:55], v[8:11]\nds_read_b128 v[44:47], v7 offset:63488\n" \
"v_mul_f32 v253, 0x3d372713, v34\nv_mfma_f32_16x16x32_bf16 v[12:15], a[112:115], v[56:59], v[12:15]\ns_lshr_b32 s99, s99, s98\ns_and_b32 s99, s99, 0xff\n" \
"s_mul_i32 s99, s99, 0x38000\nv_mfma_f32_16x16x32_bf16 v[16:19], a[240:243], v[52:55], v[16:19]\nds_read_b128 v[48:51], v7 offset:31744\ns_or_b32 s16, s99, s18\n" \
"v_mfma_f32_16x16x32_bf16 v[20:23], a[240:243], v[56:59], v[20:23]\ns_or_b32 s17, s99, s19\ns_add_u32 s96, s96, s21\nv_mfma_f32_16x16x32_bf16 v[24:27], v[224:227], v[52:55], v[24:27]\n" \
"ds_read_b128 v[64:67], v7 offset:64512\nv_xor_b32 v7, 0x10000, v7\nv_mfma_f32_16x16x32_bf16 v[28:31], v[224:227], v[56:59], v[28:31]\ns_waitcnt lgkmcnt(3)\n" \
"buffer_load_dwordx4 v[52:55], v1, s[8:11], s97 offen offset:0 sc1\nv_mfma_f32_16x16x32_bf16 v[8:11], a[116:119], v[36:39], v[8:11]\nbuffer_load_dwordx4 v[56:59], v1, s[8:11], s97 offen offset:1024 sc1\nv_mfma_f32_16x16x32_bf16 v[12:15], a[116:119], v[40:43], v[12:15]\n" \
"v_mfma_f32_16x16x32_bf16 v[16:19], a[244:247], v[36:39], v[16:19]\ns_waitcnt vmcnt(5) lgkmcnt(0)\ns_barrier\nv_mfma_f32_16x16x32_bf16 v[20:23], a[244:247], v[40:43], v[20:23]\n" \
"ds_read_b128 v[68:71], v4 offset:0\nv_mfma_f32_16x16x32_bf16 v[24:27], v[228:231], v[36:39], v[24:27]\nds_read_b128 v[72:75], v7 offset:0\nv_mfma_f32_16x16x32_bf16 v[28:31], v[228:231], v[40:43], v[28:31]\n" \
"ds_read_b128 v[36:39], v7 offset:32768\nv_mfma_f32_16x16x32_bf16 v[8:11], a[120:123], v[60:63], v[8:11]\nds_read_b128 v[40:43], v7 offset:1024\nv_mfma_f32_16x16x32_bf16 v[12:15], a[120:123], v[44:47], v[12:15]\n" \
"s_waitcnt lgkmcnt(3)\nv_add_f32 v68, v68, v69\nv_mfma_f32_16x16x32_bf16 v[8:11], a[124:127], v[48:51], v[8:11]\nds_read_b128 v[76:79], v7 offset:33792\n" \
"v_add_f32 v70, v70, v71\nv_mfma_f32_16x16x32_bf16 v[12:15], a[124:127], v[64:67], v[12:15]\nv_cmp_u_f32 s[98:99], v68, v70\nv_mfma_f32_16x16x32_bf16 v[16:19], a[248:251], v[60:63], v[16:19]\n" \
"v_fma_f32 v68, v32, v240, v8\nv_fma_f32 v69, v32, v241, v9\nv_mfma_f32_16x16x32_bf16 v[20:23], a[248:251], v[44:47], v[20:23]\ns_waitcnt vmcnt(2)\n" \
"buffer_store_dword v110, v3, s[8:11], s23 offen offset:0 sc1\nv_mfma_f32_16x16x32_bf16 v[16:19], a[252:255], v[48:51], v[16:19]\ns_mov_b32 s23, 0\nv_fma_f32 v70, v32, v242, v10\n" \
"v_fma_f32 v71, v32, v243, v11\nv_mfma_f32_16x16x32_bf16 v[20:23], a[252:255], v[64:67], v[20:23]\ns_cmp_lt_u32 s20, 0x80\ns_cselect_b32 s98, s98, -1\n" \
"v_fma_f32 v80, v33, v240, v12\nv_mfma_f32_16x16x32_bf16 v[24:27], v[232:235], v[60:63], v[24:27]\nv_fma_f32 v81, v33, v241, v13\nv_fma_f32 v82, v33, v242, v14\n" \
"v_mfma_f32_16x16x32_bf16 v[28:31], v[232:235], v[44:47], v[28:31]\nv_fma_f32 v83, v33, v243, v15\nv_fma_f32 v44, v32, v244, v16\nv_mfma_f32_16x16x32_bf16 v[24:27], v[236:239], v[48:51], v[24:27]\n" \
"s_waitcnt lgkmcnt(0)\nv_fma_f32 v45, v32, v245, v17\nv_fma_f32 v46, v32, v246, v18\nv_mfma_f32_16x16x32_bf16 v[28:31], v[236:239], v[64:67], v[28:31]\n" \
"s_cmp_lg_u64 s[98:99], 0\ns_cbranch_scc1 4f\n5:\nv_mfma_f32_16x16x32_bf16 v[8:11], a[0:3], v[72:75], 0\n" \
"s_xor_b32 m0, s22, 0\nv_fma_f32 v47, v32, v247, v19\nv_fma_f32 v48, v33, v244, v20\nv_mfma_f32_16x16x32_bf16 v[12:15], a[0:3], v[36:39], 0\n" \
"v_fma_f32 v49, v33, v245, v21\nv_fma_f32 v50, v33, v246, v22\nv_mfma_f32_16x16x32_bf16 v[8:11], a[4:7], v[40:43], v[8:11]\nds_read_b128 v[60:63], v7 offset:2048\n" \
"v_fma_f32 v51, v33, v247, v23\nv_mfma_f32_16x16x32_bf16 v[12:15], a[4:7], v[76:79], v[12:15]\nds_read_b128 v[64:67], v7 offset:34816\nv_fma_f32 v84, v32, v248, v24\n" \
"v_mfma_f32_16x16x32_bf16 v[16:19], a[128:131], v[72:75], 0\nv_fma_f32 v85, v32, v249, v25\nv_fma_f32 v86, v32, v250, v26\nv_mfma_f32_16x16x32_bf16 v[20:23], a[128:131], v[36:39], 0\n" \
"v_fma_f32 v87, v32, v251, v27\nv_fma_f32 v88, v33, v248, v28\nv_mfma_f32_16x16x32_bf16 v[16:19], a[132:135], v[40:43], v[16:19]\nv_fma_f32 v89, v33, v249, v29\n" \
"v_fma_f32 v90, v33, v250, v30\nv_mfma_f32_16x16x32_bf16 v[20:23], a[132:135], v[76:79], v[20:23]\nv_fma_f32 v91, v33, v251, v31\nv_xor_b32 v5, 0x400, v5\n" \
"v_mfma_f32_16x16x32_bf16 v[24:27], v[112:115], v[72:75], 0\nds_read_b128 v[92:95], v7 offset:3072\nv_mfma_f32_16x16x32_bf16 v[28:31], v[112:115], v[36:39], 0\nds_read_b128 v[72:75], v7 offset:35840\n" \
"v_mfma_f32_16x16x32_bf16 v[24:27], v[116:119], v[40:43], v[24:27]\ns_waitcnt lgkmcnt(2)\nv_permlane16_swap_b32 v68, v80\nv_mfma_f32_16x16x32_bf16 v[28:31], v[116:119], v[76:79], v[28:31]\n" \
"v_permlane16_swap_b32 v69, v81\nv_mfma_f32_16x16x32_bf16 v[8:11], a[8:11], v[60:63], v[8:11]\nv_permlane16_swap_b32 v70, v82\nv_mfma_f32_16x16x32_bf16 v[12:15], a[8:11], v[64:67], v[12:15]\n" \
"v_permlane16_swap_b32 v71, v83\nv_mfma_f32_16x16x32_bf16 v[16:19], a[136:139], v[60:63], v[16:19]\nds_read_b128 v[36:39], v7 offset:4096\nv_mul_f32 v40, v68, v68\n" \
"v_mfma_f32_16x16x32_bf16 v[20:23], a[136:139], v[64:67], v[20:23]\nds_read_b128 v[76:79], v7 offset:36864\nv_mul_f32 v41, v69, v69\nv_mfma_f32_16x16x32_bf16 v[24:27], v[120:123], v[60:63], v[24:27]\n" \
"s_waitcnt lgkmcnt(2)\nv_mul_f32 v42, v70, v70\nv_mul_f32 v43, v71, v71\nv_mfma_f32_16x16x32_bf16 v[28:31], v[120:123], v[64:67], v[28:31]\n" \
"v_fma_f32 v40, v40, v253, v252\nv_fma_f32 v41, v41, v253, v252\nv_mfma_f32_16x16x32_bf16 v[8:11], a[12:15], v[92:95], v[8:11]\nv_fma_f32 v42, v42, v253, v252\n" \
"v_fma_f32 v43, v43, v253, v252\nv_mfma_f32_16x16x32_bf16 v[12:15], a[12:15], v[72:75], v[12:15]\nv_mul_f32 v40, v40, v68\nv_mul_f32 v41, v41, v69\n" \
"v_mfma_f32_16x16x32_bf16 v[16:19], a[140:143], v[92:95], v[16:19]\nds_read_b128 v[60:63], v7 offset:5120\nv_mul_f32 v42, v42, v70\nv_mfma_f32_16x16x32_bf16 v[20:23], a[140:143], v[72:75], v[20:23]\n" \
"ds_read_b128 v[64:67], v7 offset:37888\nv_mul_f32 v43, v43, v71\nv_mfma_f32_16x16x32_bf16 v[24:27], v[124:127], v[92:95], v[24:27]\ns_waitcnt lgkmcnt(2)\n" \
"v_exp_f32 v40, v40\nv_mfma_f32_16x16x32_bf16 v[28:31], v[124:127], v[72:75], v[28:31]\nv_exp_f32 v41, v41\nv_mfma_f32_16x16x32_bf16 v[8:11], a[16:19], v[36:39], v[8:11]\n" \
"v_exp_f32 v42, v42\nv_mfma_f32_16x16x32_bf16 v[12:15], a[16:19], v[76:79], v[12:15]\nv_exp_f32 v43, v43\nv_mfma_f32_16x16x32_bf16 v[16:19], a[144:147], v[36:39], v[16:19]\n" \
"ds_read_b128 v[72:75], v7 offset:6144\nv_fma_f32 v40, v40, v254, v254\nv_mfma_f32_16x16x32_bf16 v[20:23], a[144:147], v[76:79], v[20:23]\nds_read_b128 v[92:95], v7 offset:38912\n" \
"v_fma_f32 v41, v41, v254, v254\nv_mfma_f32_16x16x32_bf16 v[24:27], v[128:131], v[36:39], v[24:27]\ns_waitcnt lgkmcnt(2)\nv_rcp_f32 v40, v40\n" \
"v_mfma_f32_16x16x32_bf16 v[28:31], v[128:131], v[76:79], v[28:31]\nv_rcp_f32 v41, v41\nv_mfma_f32_16x16x32_bf16 v[8:11], a[20:23], v[60:63], v[8:11]\nv_fma_f32 v42, v42, v254, v254\n" \
"v_fma_f32 v43, v43, v254, v254\nv_mfma_f32_16x16x32_bf16 v[12:15], a[20:23], v[64:67], v[12:15]\nv_rcp_f32 v42, v42\nv_mfma_f32_16x16x32_bf16 v[16:19], a[148:151], v[60:63], v[16:19]\n" \
"ds_read_b128 v[36:39], v7 offset:7168\nv_mul_f32 v80, v68, v80\nv_mfma_f32_16x16x32_bf16 v[20:23], a[148:151], v[64:67], v[20:23]\ns_waitcnt vmcnt(1)\n" \
"v_max3_u32 v35, v52, v54, v56\nv_max_u32 v35, v35, v58\nv_mfma_f32_16x16x32_bf16 v[24:27], v[132:135], v[60:63], v[24:27]\nds_read_b128 v[76:79], v7 offset:39936\n" \
"v_cmp_eq_u32 s[98:99], -1, v35\nv_mfma_f32_16x16x32_bf16 v[28:31], v[132:135], v[64:67], v[28:31]\ns_waitcnt lgkmcnt(2)\nv_rcp_f32 v43, v43\n" \
"v_mfma_f32_16x16x32_bf16 v[8:11], a[24:27], v[72:75], v[8:11]\ns_cmp_eq_u64 s[98:99], 0\ns_cselect_b32 s14, s10, 0\nv_mul_f32 v81, v69, v81\n" \
"v_mfma_f32_16x16x32_bf16 v[12:15], a[24:27], v[92:95], v[12:15]\nbuffer_load_dwordx4 v0, s[12:15], s96 offen offset:0 sc1 lds\nv_mfma_f32_16x16x32_bf16 v[16:19], a[152:155], v[72:75], v[16:19]\nds_read_b128 v[60:63], v7 offset:8192\n" \
"v_mul_f32 v82, v70, v82\nv_mfma_f32_16x16x32_bf16 v[20:23], a[152:155], v[92:95], v[20:23]\nds_read_b128 v[64:67], v7 offset:40960\nv_mul_f32 v83, v71, v83\n" \
"v_mfma_f32_16x16x32_bf16 v[24:27], v[136:139], v[72:75], v[24:27]\ns_waitcnt lgkmcnt(2)\nv_mul_f32 v80, v80, v40\nv_mul_f32 v81, v81, v41\n" \
"v_mfma_f32_16x16x32_bf16 v[28:31], v[136:139], v[92:95], v[28:31]\nv_mul_f32 v82, v82, v42\nv_mul_f32 v83, v83, v43\nv_mfma_f32_16x16x32_bf16 v[8:11], a[28:31], v[36:39], v[8:11]\n" \
"v_cvt_pk_bf16_f32 v80, v80, v81\nv_cvt_pk_bf16_f32 v81, v82, v83\nv_mfma_f32_16x16x32_bf16 v[12:15], a[28:31], v[76:79], v[12:15]\nbuffer_load_dwordx4 v0, s[12:15], s96 offen offset:1024 sc1 lds\n" \
"v_mfma_f32_16x16x32_bf16 v[16:19], a[156:159], v[36:39], v[16:19]\nds_read_b128 v[40:43], v7 offset:9216\nv_mfma_f32_16x16x32_bf16 v[20:23], a[156:159], v[76:79], v[20:23]\nds_read_b128 v[68:71], v7 offset:41984\n" \
"v_mfma_f32_16x16x32_bf16 v[24:27], v[140:143], v[36:39], v[24:27]\ns_waitcnt lgkmcnt(2)\nv_permlane16_swap_b32 v44, v48\nv_mfma_f32_16x16x32_bf16 v[28:31], v[140:143], v[76:79], v[28:31]\n" \
"buffer_load_dwordx4 v0, s[12:15], s96 offen offset:2048 sc1 lds\nv_mfma_f32_16x16x32_bf16 v[8:11], a[32:35], v[60:63], v[8:11]\nv_permlane16_swap_b32 v45, v49\nv_mfma_f32_16x16x32_bf16 v[12:15], a[32:35], v[64:67], v[12:15]\n" \
"v_permlane16_swap_b32 v46, v50\nv_mfma_f32_16x16x32_bf16 v[16:19], a[160:163], v[60:63], v[16:19]\nds_read_b128 v[36:39], v7 offset:10240\nv_mul_f32 v72, v44, v44\n" \
"v_mfma_f32_16x16x32_bf16 v[20:23], a[160:163], v[64:67], v[20:23]\nds_read_b128 v[76:79], v7 offset:43008\nv_mul_f32 v73, v45, v45\nv_mfma_f32_16x16x32_bf16 v[24:27], v[144:147], v[60:63], v[24:27]\n" \
"s_waitcnt lgkmcnt(2)\nbuffer_load_dwordx4 v0, s[12:15], s96 offen offset:3072 sc1 lds\nv_mfma_f32_16x16x32_bf16 v[28:31], v[144:147], v[64:67], v[28:31]\ns_xor_b32 m0, s22, 0x1000\n" \
"s_xor_b32 s96, s96, 0x1000\nv_mul_f32 v74, v46, v46\nv_mfma_f32_16x16x32_bf16 v[8:11], a[36:39], v[40:43], v[8:11]\nv_permlane16_swap_b32 v47, v51\n" \
"v_mfma_f32_16x16x32_bf16 v[12:15], a[36:39], v[68:71], v[12:15]\nv_mul_f32 v75, v47, v47\nv_fma_f32 v72, v72, v253, v252\nv_mfma_f32_16x16x32_bf16 v[16:19], a[164:167], v[40:43], v[16:19]\n" \
"ds_read_b128 v[60:63], v7 offset:11264\nv_fma_f32 v73, v73, v253, v252\nv_mfma_f32_16x16x32_bf16 v[20:23], a[164:167], v[68:71], v[20:23]\nbuffer_load_dwordx4 v0, s[12:15], s96 offen offset:0 sc1 lds\n" \
"v_mfma_f32_16x16x32_bf16 v[24:27], v[148:151], v[40:43], v[24:27]\nds_read_b128 v[64:67], v7 offset:44032\nv_fma_f32 v74, v74, v253, v252\nv_mfma_f32_16x16x32_bf16 v[28:31], v[148:151], v[68:71], v[28:31]\n" \
"s_waitcnt lgkmcnt(2)\nv_fma_f32 v75, v75, v253, v252\nv_mul_f32 v72, v72, v44\nv_mfma_f32_16x16x32_bf16 v[8:11], a[40:43], v[36:39], v[8:11]\n" \
"v_exp_f32 v72, v72\nv_mfma_f32_16x16x32_bf16 v[12:15], a[40:43], v[76:79], v[12:15]\nv_mul_f32 v73, v73, v45\nv_mul_f32 v74, v74, v46\n" \
"v_mfma_f32_16x16x32_bf16 v[16:19], a[168:171], v[36:39], v[16:19]\nbuffer_load_dwordx4 v0, s[12:15], s96 offen offset:1024 sc1 lds\nv_mfma_f32_16x16x32_bf16 v[20:23], a[168:171], v[76:79], v[20:23]\nds_read_b128 v[40:43], v7 offset:12288\n" \
"v_mul_f32 v75, v75, v47\nv_mfma_f32_16x16x32_bf16 v[24:27], v[152:155], v[36:39], v[24:27]\nds_read_b128 v[68:71], v7 offset:45056\nv_fma_f32 v72, v72, v254, v254\n" \
"v_mfma_f32_16x16x32_bf16 v[28:31], v[152:155], v[76:79], v[28:31]\ns_waitcnt lgkmcnt(2)\nv_exp_f32 v73, v73\nv_mfma_f32_16x16x32_bf16 v[8:11], a[44:47], v[60:63], v[8:11]\n" \
"v_exp_f32 v74, v74\nv_mfma_f32_16x16x32_bf16 v[12:15], a[44:47], v[64:67], v[12:15]\nbuffer_load_dwordx4 v0, s[12:15], s96 offen offset:2048 sc1 lds\nv_mfma_f32_16x16x32_bf16 v[16:19], a[172:175], v[60:63], v[16:19]\n" \
"ds_read_b128 v[36:39], v7 offset:13312\nv_fma_f32 v73, v73, v254, v254\nv_mfma_f32_16x16x32_bf16 v[20:23], a[172:175], v[64:67], v[20:23]\nds_read_b128 v[76:79], v7 offset:46080\n" \
"v_fma_f32 v74, v74, v254, v254\nv_mfma_f32_16x16x32_bf16 v[24:27], v[156:159], v[60:63], v[24:27]\ns_waitcnt lgkmcnt(2)\nv_exp_f32 v75, v75\n" \
"v_mfma_f32_16x16x32_bf16 v[28:31], v[156:159], v[64:67], v[28:31]\nv_rcp_f32 v72, v72\nv_mfma_f32_16x16x32_bf16 v[8:11], a[48:51], v[40:43], v[8:11]\nbuffer_load_dwordx4 v0, s[12:15], s96 offen offset:3072 sc1 lds\n" \
"v_mfma_f32_16x16x32_bf16 v[12:15], a[48:51], v[68:71], v[12:15]\ns_xor_b32 m0, s22, 0x8000\ns_xor_b32 s96, s96, 0x9000\nv_fma_f32 v75, v75, v254, v254\n" \
"v_mfma_f32_16x16x32_bf16 v[16:19], a[176:179], v[40:43], v[16:19]\nds_read_b128 v[60:63], v7 offset:14336\nv_mul_f32 v48, v44, v48\nv_mfma_f32_16x16x32_bf16 v[20:23], a[176:179], v[68:71], v[20:23]\n" \
"ds_read_b128 v[64:67], v7 offset:47104\nv_mul_f32 v49, v45, v49\nv_mfma_f32_16x16x32_bf16 v[24:27], v[160:163], v[40:43], v[24:27]\ns_waitcnt lgkmcnt(2)\n" \
"v_rcp_f32 v73, v73\nv_mfma_f32_16x16x32_bf16 v[28:31], v[160:163], v[68:71], v[28:31]\nbuffer_load_dwordx4 v0, s[12:15], s96 offen offset:0 sc1 lds\nv_mfma_f32_16x16x32_bf16 v[8:11], a[52:55], v[36:39], v[8:11]\n" \
"v_rcp_f32 v74, v74\nv_mfma_f32_16x16x32_bf16 v[12:15], a[52:55], v[76:79], v[12:15]\nv_rcp_f32 v75, v75\nv_mfma_f32_16x16x32_bf16 v[16:19], a[180:183], v[36:39], v[16:19]\n" \
"ds_read_b128 v[40:43], v7 offset:15360\nv_mul_f32 v50, v46, v50\nv_mfma_f32_16x16x32_bf16 v[20:23], a[180:183], v[76:79], v[20:23]\nds_read_b128 v[68:71], v7 offset:48128\n" \
"v_mul_f32 v51, v47, v51\nv_mfma_f32_16x16x32_bf16 v[24:27], v[164:167], v[36:39], v[24:27]\ns_waitcnt lgkmcnt(2)\nbuffer_load_dwordx4 v0, s[12:15], s96 offen offset:1024 sc1 lds\n" \
"v_mfma_f32_16x16x32_bf16 v[28:31], v[164:167], v[76:79], v[28:31]\nv_mul_f32 v48, v48, v72\nv_mul_f32 v49, v49, v73\nv_mfma_f32_16x16x32_bf16 v[8:11], a[56:59], v[60:63], v[8:11]\n" \
"v_mul_f32 v50, v50, v74\nv_mul_f32 v51, v51, v75\nv_mfma_f32_16x16x32_bf16 v[12:15], a[56:59], v[64:67], v[12:15]\nv_cvt_pk_bf16_f32 v48, v48, v49\n" \
"v_cvt_pk_bf16_f32 v49, v50, v51\nv_mfma_f32_16x16x32_bf16 v[16:19], a[184:187], v[60:63], v[16:19]\nds_read_b128 v[36:39], v7 offset:16384\nv_mfma_f32_16x16x32_bf16 v[20:23], a[184:187], v[64:67], v[20:23]\n" \
"buffer_load_dwordx4 v0, s[12:15], s96 offen offset:2048 sc1 lds\nv_mfma_f32_16x16x32_bf16 v[24:27], v[168:171], v[60:63], v[24:27]\ns_waitcnt lgkmcnt(1)\nds_read_b128 v[44:47], v7 offset:49152\n" \
"v_mfma_f32_16x16x32_bf16 v[28:31], v[168:171], v[64:67], v[28:31]\nv_permlane16_swap_b32 v84, v88\nv_mfma_f32_16x16x32_bf16 v[8:11], a[60:63], v[40:43], v[8:11]\nv_permlane16_swap_b32 v85, v89\n" \
"v_mfma_f32_16x16x32_bf16 v[12:15], a[60:63], v[68:71], v[12:15]\nv_permlane16_swap_b32 v86, v90\nv_mfma_f32_16x16x32_bf16 v[16:19], a[188:191], v[40:43], v[16:19]\nbuffer_load_dwordx4 v0, s[12:15], s96 offen offset:3072 sc1 lds\n" \
"v_mfma_f32_16x16x32_bf16 v[20:23], a[188:191], v[68:71], v[20:23]\nds_read_b128 v[60:63], v7 offset:17408\ns_xor_b32 m0, s22, 0x9000\nv_mfma_f32_16x16x32_bf16 v[24:27], v[172:175], v[40:43], v[24:27]\n" \
"ds_read_b128 v[64:67], v7 offset:50176\ns_xor_b32 s96, s96, 0x1000\nv_mfma_f32_16x16x32_bf16 v[28:31], v[172:175], v[68:71], v[28:31]\ns_waitcnt lgkmcnt(2)\n" \
"v_permlane16_swap_b32 v87, v91\nv_mfma_f32_16x16x32_bf16 v[8:11], a[64:67], v[36:39], v[8:11]\ns_xor_b32 s22, s22, 0x10000\nv_mul_f32 v40, v84, v84\n" \
"v_mul_f32 v41, v85, v85\nv_mfma_f32_16x16x32_bf16 v[12:15], a[64:67], v[44:47], v[12:15]\nbuffer_load_dwordx4 v0, s[12:15], s96 offen offset:0 sc1 lds\nv_mfma_f32_16x16x32_bf16 v[16:19], a[192:195], v[36:39], v[16:19]\n" \
"ds_read_b128 v[68:71], v7 offset:18432\nv_mul_f32 v42, v86, v86\nv_mfma_f32_16x16x32_bf16 v[20:23], a[192:195], v[44:47], v[20:23]\nds_read_b128 v[72:75], v7 offset:51200\n" \
"v_mul_f32 v43, v87, v87\nv_mfma_f32_16x16x32_bf16 v[24:27], v[176:179], v[36:39], v[24:27]\ns_waitcnt lgkmcnt(2)\nv_fma_f32 v40, v40, v253, v252\n" \
"v_fma_f32 v41, v41, v253, v252\nv_mfma_f32_16x16x32_bf16 v[28:31], v[176:179], v[44:47], v[28:31]\nv_fma_f32 v42, v42, v253, v252\nv_fma_f32 v43, v43, v253, v252\n" \
"v_mfma_f32_16x16x32_bf16 v[8:11], a[68:71], v[60:63], v[8:11]\nbuffer_load_dwordx4 v0, s[12:15], s96 offen offset:1024 sc1 lds\nv_mfma_f32_16x16x32_bf16 v[12:15], a[68:71], v[64:67], v[12:15]\nv_mul_f32 v40, v40, v84\n" \
"v_mul_f32 v41, v41, v85\nv_mfma_f32_16x16x32_bf16 v[16:19], a[196:199], v[60:63], v[16:19]\nds_read_b128 v[36:39], v7 offset:19456\nv_mul_f32 v42, v42, v86\n" \
"v_mfma_f32_16x16x32_bf16 v[20:23], a[196:199], v[64:67], v[20:23]\nds_read_b128 v[44:47], v7 offset:52224\nv_mul_f32 v43, v43, v87\nv_mfma_f32_16x16x32_bf16 v[24:27], v[180:183], v[60:63], v[24:27]\n" \
"s_waitcnt lgkmcnt(2)\nv_exp_f32 v40, v40\nv_mfma_f32_16x16x32_bf16 v[28:31], v[180:183], v[64:67], v[28:31]\nbuffer_load_dwordx4 v0, s[12:15], s96 offen offset:2048 sc1 lds\n" \
"v_mfma_f32_16x16x32_bf16 v[8:11], a[72:75], v[68:71], v[8:11]\nv_exp_f32 v41, v41\nv_mfma_f32_16x16x32_bf16 v[12:15], a[72:75], v[72:75], v[12:15]\nv_exp_f32 v42, v42\n" \
"v_mfma_f32_16x16x32_bf16 v[16:19], a[200:203], v[68:71], v[16:19]\nds_read_b128 v[60:63], v7 offset:20480\nv_fma_f32 v40, v40, v254, v254\nv_mfma_f32_16x16x32_bf16 v[20:23], a[200:203], v[72:75], v[20:23]\n" \
"ds_read_b128 v[64:67], v7 offset:53248\nv_fma_f32 v41, v41, v254, v254\nv_mfma_f32_16x16x32_bf16 v[24:27], v[184:187], v[68:71], v[24:27]\ns_waitcnt lgkmcnt(2)\n" \
"buffer_load_dwordx4 v0, s[12:15], s96 offen offset:3072 sc1 lds\nv_mfma_f32_16x16x32_bf16 v[28:31], v[184:187], v[72:75], v[28:31]\nv_exp_f32 v43, v43\nv_mfma_f32_16x16x32_bf16 v[8:11], a[76:79], v[36:39], v[8:11]\n" \
"v_rcp_f32 v40, v40\nv_mfma_f32_16x16x32_bf16 v[12:15], a[76:79], v[44:47], v[12:15]\nv_rcp_f32 v41, v41\nv_mfma_f32_16x16x32_bf16 v[16:19], a[204:207], v[36:39], v[16:19]\n" \
"ds_read_b128 v[68:71], v7 offset:21504\nv_fma_f32 v42, v42, v254, v254\nv_mfma_f32_16x16x32_bf16 v[20:23], a[204:207], v[44:47], v[20:23]\nds_read_b128 v[72:75], v7 offset:54272\n" \
"v_fma_f32 v43, v43, v254, v254\nv_mfma_f32_16x16x32_bf16 v[24:27], v[188:191], v[36:39], v[24:27]\ns_waitcnt lgkmcnt(2)\nv_rcp_f32 v42, v42\n" \
"v_mfma_f32_16x16x32_bf16 v[28:31], v[188:191], v[44:47], v[28:31]\nv_rcp_f32 v43, v43\nv_mfma_f32_16x16x32_bf16 v[8:11], a[80:83], v[60:63], v[8:11]\nbuffer_store_dwordx2 v[80:81], v2, s[8:11], s16 offen offset:0 sc1\n" \
"v_mul_f32 v88, v84, v88\nv_mfma_f32_16x16x32_bf16 v[12:15], a[80:83], v[64:67], v[12:15]\nv_mul_f32 v89, v85, v89\nv_mul_f32 v90, v86, v90\n" \
"v_mfma_f32_16x16x32_bf16 v[16:19], a[208:211], v[60:63], v[16:19]\nbuffer_store_dwordx2 v[48:49], v2, s[8:11], s17 offen offset:256 sc1\nv_mul_f32 v91, v87, v91\nv_mfma_f32_16x16x32_bf16 v[20:23], a[208:211], v[64:67], v[20:23]\n" \
"v_mul_f32 v88, v88, v40\nv_mul_f32 v89, v89, v41\nv_mfma_f32_16x16x32_bf16 v[24:27], v[192:195], v[60:63], v[24:27]\nds_read_b128 v[36:39], v7 offset:22528\n" \
"v_mul_f32 v90, v90, v42\nv_mul_f32 v91, v91, v43\nv_mfma_f32_16x16x32_bf16 v[28:31], v[192:195], v[64:67], v[28:31]\ns_waitcnt lgkmcnt(1)\n" \
"ds_read_b128 v[40:43], v7 offset:55296\nv_cvt_pk_bf16_f32 v88, v88, v89\nv_mfma_f32_16x16x32_bf16 v[8:11], a[84:87], v[68:71], v[8:11]\nds_read_b128 v[44:47], v4 offset:0\n" \
"v_cvt_pk_bf16_f32 v89, v90, v91\nv_mfma_f32_16x16x32_bf16 v[12:15], a[84:87], v[72:75], v[12:15]\nbuffer_store_dwordx2 v[88:89], v2, s[8:11], s17 offen offset:512 sc1\nv_add_f32 v52, v52, v56\n" \
"v_mfma_f32_16x16x32_bf16 v[16:19], a[212:215], v[68:71], v[16:19]\nds_read_b128 v[48:51], v7 offset:23552\nv_add_f32 v53, v53, v57\nv_mfma_f32_16x16x32_bf16 v[20:23], a[212:215], v[72:75], v[20:23]\n" \
"ds_read_b128 v[60:63], v7 offset:56320\nv_add_f32 v54, v54, v58\nv_mfma_f32_16x16x32_bf16 v[24:27], v[196:199], v[68:71], v[24:27]\nds_read_b128 v[64:67], v4 offset:512\n" \
"v_add_f32 v55, v55, v59\nv_mfma_f32_16x16x32_bf16 v[28:31], v[196:199], v[72:75], v[28:31]\ns_waitcnt lgkmcnt(4)\nv_permlane16_swap_b32 v52, v54\n" \
"v_mfma_f32_16x16x32_bf16 v[8:11], a[88:91], v[36:39], v[8:11]\nv_permlane16_swap_b32 v53, v55\nv_mfma_f32_16x16x32_bf16 v[12:15], a[88:91], v[40:43], v[12:15]\ns_waitcnt lgkmcnt(3)\n" \
"v_add_f32 v44, v44, v45\nv_add_f32 v46, v46, v47\nv_mfma_f32_16x16x32_bf16 v[16:19], a[216:219], v[36:39], v[16:19]\nds_read_b128 v[56:59], v7 offset:24576\n" \
"v_add_f32 v44, v44, v46\nv_mfma_f32_16x16x32_bf16 v[20:23], a[216:219], v[40:43], v[20:23]\nds_read_b128 v[68:71], v7 offset:57344\nv_mul_f32 v44, 0xba800000, v44\n" \
"v_mfma_f32_16x16x32_bf16 v[24:27], v[200:203], v[36:39], v[24:27]\ns_waitcnt lgkmcnt(2)\nv_add_f32 v52, v52, v54\nv_add_f32 v53, v53, v55\n" \
"v_mfma_f32_16x16x32_bf16 v[28:31], v[200:203], v[40:43], v[28:31]\nv_add_f32 v64, v64, v65\nv_add_f32 v66, v66, v67\nv_mfma_f32_16x16x32_bf16 v[8:11], a[92:95], v[48:51], v[8:11]\n" \
"v_add_f32 v64, v64, v66\nv_mul_f32 v65, v44, -v44\nv_mfma_f32_16x16x32_bf16 v[12:15], a[92:95], v[60:63], v[12:15]\nv_fmamk_f32 v35, v64, 0x3a800000, v65\n" \
"v_cvt_pk_bf16_f32 v35, v35, v35\nv_mfma_f32_16x16x32_bf16 v[16:19], a[220:223], v[48:51], v[16:19]\nds_read_b128 v[64:67], v7 offset:25600\nv_lshlrev_b32 v35, 16, v35\n" \
"v_mfma_f32_16x16x32_bf16 v[20:23], a[220:223], v[60:63], v[20:23]\nds_read_b128 v[72:75], v7 offset:58368\nv_max_f32 v35, 0, v35\nv_mfma_f32_16x16x32_bf16 v[24:27], v[204:207], v[48:51], v[24:27]\n" \
"s_waitcnt lgkmcnt(2)\nv_add_f32 v35, 0x3727c5ac, v35\nv_cvt_pk_bf16_f32 v111, v35, v44\nv_mfma_f32_16x16x32_bf16 v[28:31], v[204:207], v[60:63], v[28:31]\n" \
"v_permlane32_swap_b32 v52, v53\nv_mfma_f32_16x16x32_bf16 v[8:11], a[96:99], v[56:59], v[8:11]\nv_lshlrev_b32 v35, 16, v111\nv_add_f32 v52, v52, v53\n" \
"v_mfma_f32_16x16x32_bf16 v[12:15], a[96:99], v[68:71], v[12:15]\nv_sqrt_f32 v35, v35\nv_mfma_f32_16x16x32_bf16 v[16:19], a[224:227], v[56:59], v[16:19]\nds_read_b128 v[36:39], v7 offset:26624\n" \
"v_cvt_pk_bf16_f32 v35, v35, v35\nv_mfma_f32_16x16x32_bf16 v[20:23], a[224:227], v[68:71], v[20:23]\nds_read_b128 v[40:43], v7 offset:59392\nv_lshlrev_b32 v35, 16, v35\n" \
"v_mfma_f32_16x16x32_bf16 v[24:27], v[208:211], v[56:59], v[24:27]\ns_waitcnt lgkmcnt(2)\nv_rcp_f32 v34, v35\nv_mfma_f32_16x16x32_bf16 v[28:31], v[208:211], v[68:71], v[28:31]\n" \
"ds_write_b32 v5, v52 offset:0\nv_mfma_f32_16x16x32_bf16 v[8:11], a[100:103], v[64:67], v[8:11]\nv_and_b32 v111, 0xffff0000, v111\nv_mfma_f32_16x16x32_bf16 v[12:15], a[100:103], v[72:75], v[12:15]\n" \
"v_mfma_f32_16x16x32_bf16 v[16:19], a[228:231], v[64:67], v[16:19]\nds_read_b128 v[44:47], v7 offset:27648\nv_mfma_f32_16x16x32_bf16 v[20:23], a[228:231], v[72:75], v[20:23]\nds_read_b128 v[48:51], v7 offset:60416\n" \
"v_mfma_f32_16x16x32_bf16 v[24:27], v[212:215], v[64:67], v[24:27]\nv_mfma_f32_16x16x32_bf16 v[28:31], v[212:215], v[72:75], v[28:31]\ns_branch 1b\n4:\n" \
"s_barrier\ns_lshr_b32 s94, s20, 1\nv_readlane_b32 s94, v6, s94\ns_and_b32 s95, s20, 1\n" \
"s_lshl_b32 s95, s95, 3\ns_lshr_b32 s94, s94, s95\ns_and_b32 s94, s94, 0xff\ns_lshl_b32 s95, s94, 13\n" \
"buffer_load_dwordx4 v[96:99], v1, s[8:11], s95 offen offset:0 sc1\nbuffer_load_dwordx4 v[100:103], v1, s[8:11], s95 offen offset:1024 sc1\nv_fma_f32 v84, v32, v240, v8\nv_fma_f32 v85, v32, v241, v9\n" \
"v_fma_f32 v86, v32, v242, v10\nv_fma_f32 v87, v32, v243, v11\nv_fma_f32 v88, v33, v240, v12\nv_fma_f32 v89, v33, v241, v13\n" \
"v_fma_f32 v90, v33, v242, v14\nv_fma_f32 v91, v33, v243, v15\nv_permlane16_swap_b32 v84, v88\nv_permlane16_swap_b32 v85, v89\n" \
"v_permlane16_swap_b32 v86, v90\nv_permlane16_swap_b32 v87, v91\nv_mul_f32 v92, v84, v84\nv_mul_f32 v93, v85, v85\n" \
"v_mul_f32 v94, v86, v86\nv_mul_f32 v95, v87, v87\nv_fma_f32 v92, v92, v253, v252\nv_fma_f32 v93, v93, v253, v252\n" \
"v_fma_f32 v94, v94, v253, v252\nv_fma_f32 v95, v95, v253, v252\nv_mul_f32 v92, v92, v84\nv_mul_f32 v93, v93, v85\n" \
"v_mul_f32 v94, v94, v86\nv_mul_f32 v95, v95, v87\nv_mul_f32 v88, v84, v88\nv_mul_f32 v89, v85, v89\n" \
"v_mul_f32 v90, v86, v90\nv_mul_f32 v91, v87, v91\nv_exp_f32 v92, v92\nv_exp_f32 v93, v93\n" \
"v_exp_f32 v94, v94\nv_exp_f32 v95, v95\nv_fma_f32 v92, v92, v254, v254\nv_fma_f32 v93, v93, v254, v254\n" \
"v_fma_f32 v94, v94, v254, v254\nv_fma_f32 v95, v95, v254, v254\nv_rcp_f32 v92, v92\nv_rcp_f32 v93, v93\n" \
"v_rcp_f32 v94, v94\nv_rcp_f32 v95, v95\nv_mul_f32 v88, v88, v92\nv_mul_f32 v89, v89, v93\n" \
"v_mul_f32 v90, v90, v94\nv_mul_f32 v91, v91, v95\nv_cvt_pk_bf16_f32 v88, v88, v89\nv_cvt_pk_bf16_f32 v89, v90, v91\n" \
"buffer_store_dwordx2 v[88:89], v2, s[8:11], s16 offen offset:0 sc1\nv_fma_f32 v84, v32, v244, v16\nv_fma_f32 v85, v32, v245, v17\nv_fma_f32 v86, v32, v246, v18\n" \
"v_fma_f32 v87, v32, v247, v19\nv_fma_f32 v88, v33, v244, v20\nv_fma_f32 v89, v33, v245, v21\nv_fma_f32 v90, v33, v246, v22\n" \
"v_fma_f32 v91, v33, v247, v23\nv_permlane16_swap_b32 v84, v88\nv_permlane16_swap_b32 v85, v89\nv_permlane16_swap_b32 v86, v90\n" \
"v_permlane16_swap_b32 v87, v91\nv_mul_f32 v92, v84, v84\nv_mul_f32 v93, v85, v85\nv_mul_f32 v94, v86, v86\n" \
"v_mul_f32 v95, v87, v87\nv_fma_f32 v92, v92, v253, v252\nv_fma_f32 v93, v93, v253, v252\nv_fma_f32 v94, v94, v253, v252\n" \
"v_fma_f32 v95, v95, v253, v252\nv_mul_f32 v92, v92, v84\nv_mul_f32 v93, v93, v85\nv_mul_f32 v94, v94, v86\n" \
"v_mul_f32 v95, v95, v87\nv_mul_f32 v88, v84, v88\nv_mul_f32 v89, v85, v89\nv_mul_f32 v90, v86, v90\n" \
"v_mul_f32 v91, v87, v91\nv_exp_f32 v92, v92\nv_exp_f32 v93, v93\nv_exp_f32 v94, v94\n" \
"v_exp_f32 v95, v95\nv_fma_f32 v92, v92, v254, v254\nv_fma_f32 v93, v93, v254, v254\nv_fma_f32 v94, v94, v254, v254\n" \
"v_fma_f32 v95, v95, v254, v254\nv_rcp_f32 v92, v92\nv_rcp_f32 v93, v93\nv_rcp_f32 v94, v94\n" \
"v_rcp_f32 v95, v95\nv_mul_f32 v88, v88, v92\nv_mul_f32 v89, v89, v93\nv_mul_f32 v90, v90, v94\n" \
"v_mul_f32 v91, v91, v95\nv_cvt_pk_bf16_f32 v88, v88, v89\nv_cvt_pk_bf16_f32 v89, v90, v91\nbuffer_store_dwordx2 v[88:89], v2, s[8:11], s17 offen offset:256 sc1\n" \
"v_fma_f32 v84, v32, v248, v24\nv_fma_f32 v85, v32, v249, v25\nv_fma_f32 v86, v32, v250, v26\nv_fma_f32 v87, v32, v251, v27\n" \
"v_fma_f32 v88, v33, v248, v28\nv_fma_f32 v89, v33, v249, v29\nv_fma_f32 v90, v33, v250, v30\nv_fma_f32 v91, v33, v251, v31\n" \
"v_permlane16_swap_b32 v84, v88\nv_permlane16_swap_b32 v85, v89\nv_permlane16_swap_b32 v86, v90\nv_permlane16_swap_b32 v87, v91\n" \
"v_mul_f32 v92, v84, v84\nv_mul_f32 v93, v85, v85\nv_mul_f32 v94, v86, v86\nv_mul_f32 v95, v87, v87\n" \
"v_fma_f32 v92, v92, v253, v252\nv_fma_f32 v93, v93, v253, v252\nv_fma_f32 v94, v94, v253, v252\nv_fma_f32 v95, v95, v253, v252\n" \
"v_mul_f32 v92, v92, v84\nv_mul_f32 v93, v93, v85\nv_mul_f32 v94, v94, v86\nv_mul_f32 v95, v95, v87\n" \
"v_mul_f32 v88, v84, v88\nv_mul_f32 v89, v85, v89\nv_mul_f32 v90, v86, v90\nv_mul_f32 v91, v87, v91\n" \
"v_exp_f32 v92, v92\nv_exp_f32 v93, v93\nv_exp_f32 v94, v94\nv_exp_f32 v95, v95\n" \
"v_fma_f32 v92, v92, v254, v254\nv_fma_f32 v93, v93, v254, v254\nv_fma_f32 v94, v94, v254, v254\nv_fma_f32 v95, v95, v254, v254\n" \
"v_rcp_f32 v92, v92\nv_rcp_f32 v93, v93\nv_rcp_f32 v94, v94\nv_rcp_f32 v95, v95\n" \
"v_mul_f32 v88, v88, v92\nv_mul_f32 v89, v89, v93\nv_mul_f32 v90, v90, v94\nv_mul_f32 v91, v91, v95\n" \
"v_cvt_pk_bf16_f32 v88, v88, v89\nv_cvt_pk_bf16_f32 v89, v90, v91\nbuffer_store_dwordx2 v[88:89], v2, s[8:11], s17 offen offset:512 sc1\ns_mov_b32 s16, 0x80000000\n" \
"s_mov_b32 s17, 0x80000000\nv_mov_b32 v84, s20\ns_cmp_ge_u32 s20, 0x80\ns_cbranch_scc1 6f\n" \
"s_waitcnt vmcnt(3)\nv_max3_u32 v85, v96, v98, v100\nv_max_u32 v85, v85, v102\nv_cmp_eq_u32 vcc, -1, v85\n" \
"s_cbranch_vccz 8f\n6:\ns_waitcnt vmcnt(0)\nbuffer_store_dword v84, v3, s[8:11], 0 offen offset:0 sc1\n" \
"s_mov_b32 s23, 0x40000000\ns_cbranch_scc1 9f\n7:\ns_add_u32 s24, s24, 1\n" \
"s_cmp_lt_u32 s24, 0x40000\ns_cbranch_scc0 9f\nbuffer_load_dwordx4 v[96:99], v1, s[8:11], s95 offen offset:0 sc1\nbuffer_load_dwordx4 v[100:103], v1, s[8:11], s95 offen offset:1024 sc1\n" \
"s_waitcnt vmcnt(0)\nv_max3_u32 v85, v96, v98, v100\nv_max_u32 v85, v85, v102\nv_cmp_eq_u32 vcc, -1, v85\n" \
"s_cbranch_vccz 8f\ns_sleep 8\ns_branch 7b\n8:\n" \
"s_lshl_b32 s94, s94, 16\ns_add_u32 s94, s94, s21\ns_xor_b32 s95, s22, 0x10000\ns_xor_b32 m0, s95, 0\n" \
"s_xor_b32 s94, s94, 0\nbuffer_load_dwordx4 v0, s[8:11], s94 offen offset:0 sc1 lds\nbuffer_load_dwordx4 v0, s[8:11], s94 offen offset:1024 sc1 lds\nbuffer_load_dwordx4 v0, s[8:11], s94 offen offset:2048 sc1 lds\n" \
"buffer_load_dwordx4 v0, s[8:11], s94 offen offset:3072 sc1 lds\ns_xor_b32 m0, s95, 0x1000\ns_xor_b32 s94, s94, 0x1000\nbuffer_load_dwordx4 v0, s[8:11], s94 offen offset:0 sc1 lds\n" \
"buffer_load_dwordx4 v0, s[8:11], s94 offen offset:1024 sc1 lds\nbuffer_load_dwordx4 v0, s[8:11], s94 offen offset:2048 sc1 lds\nbuffer_load_dwordx4 v0, s[8:11], s94 offen offset:3072 sc1 lds\ns_xor_b32 m0, s95, 0x8000\n" \
"s_xor_b32 s94, s94, 0x9000\nbuffer_load_dwordx4 v0, s[8:11], s94 offen offset:0 sc1 lds\nbuffer_load_dwordx4 v0, s[8:11], s94 offen offset:1024 sc1 lds\nbuffer_load_dwordx4 v0, s[8:11], s94 offen offset:2048 sc1 lds\n" \
"buffer_load_dwordx4 v0, s[8:11], s94 offen offset:3072 sc1 lds\ns_xor_b32 m0, s95, 0x9000\ns_xor_b32 s94, s94, 0x1000\nbuffer_load_dwordx4 v0, s[8:11], s94 offen offset:0 sc1 lds\n" \
"buffer_load_dwordx4 v0, s[8:11], s94 offen offset:1024 sc1 lds\nbuffer_load_dwordx4 v0, s[8:11], s94 offen offset:2048 sc1 lds\nbuffer_load_dwordx4 v0, s[8:11], s94 offen offset:3072 sc1 lds\nv_add_f32 v96, v96, v100\n" \
"v_add_f32 v97, v97, v101\nv_add_f32 v98, v98, v102\nv_add_f32 v99, v99, v103\ns_nop 1\n" \
"v_permlane16_swap_b32 v96, v98\nv_permlane16_swap_b32 v97, v99\nv_add_f32 v96, v96, v98\nv_add_f32 v97, v97, v99\n" \
"s_nop 1\nv_permlane32_swap_b32 v96, v97\nv_add_f32 v96, v96, v97\nds_write_b32 v5, v96\n" \
"s_add_u32 s97, s20, 1\ns_lshr_b32 s96, s97, 1\nv_readlane_b32 s96, v6, s96\ns_and_b32 s97, s97, 1\n" \
"s_lshl_b32 s97, s97, 3\ns_lshr_b32 s96, s96, s97\ns_and_b32 s96, s96, 0xff\ns_lshl_b32 s97, s96, 13\n" \
"s_lshl_b32 s96, s96, 16\ns_add_u32 s96, s96, s21\nbuffer_load_dwordx4 v[52:55], v1, s[8:11], s97 offen offset:0 sc1\nbuffer_load_dwordx4 v[56:59], v1, s[8:11], s97 offen offset:1024 sc1\n" \
"s_waitcnt vmcnt(18)\nbuffer_store_dword v84, v3, s[8:11], s23 offen offset:0 sc1\ns_mov_b32 s23, 0x40000000\ns_waitcnt vmcnt(3) lgkmcnt(0)\n" \
"s_barrier\nds_read_b128 v[72:75], v7 offset:0\nds_read_b128 v[36:39], v7 offset:32768\nds_read_b128 v[40:43], v7 offset:1024\n" \
"ds_read_b128 v[76:79], v7 offset:33792\ns_waitcnt lgkmcnt(0)\ns_branch 5b\n9:\n" \
"s_waitcnt vmcnt(0)\ns_endpgm\n"
struct WMA { static constexpr int NT = 4, NP = 16, RB = 0, LB = 0, CBI = 0;
             static constexpr int kind(int j, int t) { return (A_LDS >> (16 * j + t) & 1) ? 1 : (A_VAR >> (16 * j + t) & 1) ? 2 : 0; } };
namespace la { constexpr int L_Q = 24576, L_K = 57344, L_QM = 65536, L_KM = 66048, L_SP = 69632, L_W = 73728; }
#ifndef LEAD_B
#define LEAD_B 132
#endif
DEVI float dror8(float x) { return __uint_as_float(__builtin_amdgcn_update_dpp(0u, __float_as_uint(x), 0x128, 0xF, 0xF, true)); }
DEVI float scm(float a, float m) { float r; asm("v_fma_f32 %0, %1, %2, -%3" : "=v"(r) : "v"(a), "s"(SC_L2E), "v"(m)); return r; }
DEVI float ex2(float x) { return __builtin_amdgcn_exp2f(x); }
DEVI float max3(float a, float b, float c) { float r; asm("v_max3_f32 %0, %1, %2, %3" : "=v"(r) : "v"(a), "v"(b), "v"(c)); return r; }
constexpr u64 tri(int i) { u64 m = 0; for (int l = 0; l < 64; ++l) if (4 * (l >> 4) + i <= (l & 15)) m |= 1ull << l; return m; }
DEVI unsigned vbfe(unsigned a, int o, int n) { unsigned r; asm volatile("v_bfe_u32 %0, %1, %2, %3" : "=v"(r) : "v"(a), "i"(o), "i"(n)); return r; }
DEVI unsigned vadds(unsigned a, unsigned b) { unsigned r; asm volatile("v_add_u32 %0, %1, %2" : "=v"(r) : "s"(b), "v"(a)); return r; }
DEVI float vexp(float a) { float r; asm volatile("v_exp_f32 %0, %1" : "=v"(r) : "v"(a)); return r; }
DEVI unsigned vors(unsigned a, unsigned b) { unsigned r; asm volatile("v_or_b32 %0, %1, %2" : "=v"(r) : "s"(b), "v"(a)); return r; }
DEVI float faddv(float a, float b) { float r; asm volatile("v_add_f32 %0, %1, %2" : "=v"(r) : "v"(a), "v"(b)); return r; }
DEVI unsigned packv(float a, float b) { unsigned r; asm volatile("v_cvt_pk_bf16_f32 %0, %1, %2" : "=v"(r) : "v"(a), "v"(b)); return r; }
DEVI void setm0q(unsigned v) { asm volatile("s_mov_b32 m0, %0" :: "s"(v) : "memory"); }
template <int OFF> DEVI void st16o(v4u v, rsrc_t r, unsigned vo, unsigned so) {
    asm volatile("buffer_store_dwordx4 %0, %1, %2, %3 offen offset:%4 sc1" :: "v"(v), "v"(vo), "s"(r), "s"(so), "n"(OFF) : "memory"); }
DEVI unsigned vlsa(unsigned a, int sh, unsigned c) { unsigned r; asm volatile("v_lshl_add_u32 %0, %1, %2, %3" : "=v"(r) : "v"(a), "i"(sh), "v"(c)); return r; }
DEVI unsigned vlss(unsigned a, int sh, unsigned c) { unsigned r; asm volatile("v_lshl_add_u32 %0, %1, %2, %3" : "=v"(r) : "v"(a), "i"(sh), "s"(c)); return r; }
template <int OFF> DEVI void sts4h(unsigned p, unsigned u0, unsigned u1) {
    asm volatile("ds_write_b16 %0, %1 offset:%3\n\tds_write_b16_d16_hi %0, %1 offset:%3+16\n\tds_write_b16 %0, %2 offset:%3+8\n\tds_write_b16_d16_hi %0, %2 offset:%3+24" :: "v"(p), "v"(u0), "v"(u1), "n"(OFF) : "memory");
}
DEVI void zero4v(f32x4& d) { f32x2 a, b; asm volatile("v_mov_b64 %0, 0\n\tv_mov_b64 %1, 0" : "=v"(a), "=v"(b)); d = __builtin_shufflevector(a, b, 0, 1, 2, 3); }
DEVI float sel0(float a, u64 m) { float r; asm("v_cndmask_b32 %0, %1, 0, %2" : "=v"(r) : "v"(a), "s"(m)); return r; }
DEVI float vmul(float a, float b) { float r; asm("v_mul_f32 %0, %1, %2" : "=v"(r) : "v"(a), "v"(b)); return r; }
DEVI void swap16(float& a, float& b) { unsigned x = __float_as_uint(a), y = __float_as_uint(b); swap16u(x, y); a = __uint_as_float(x); b = __uint_as_float(y); }
#ifndef PACE
#define PACE 8
#endif
DEVI void pace(int l) { asm volatile("s_cmp_eq_u32 %0, 0\n\ts_cbranch_scc1 1f\n\ts_sleep %1\n1:" :: "s"(l), "n"(PACE) : "scc"); }
DEVI void role_A(const Args& a, int w, int l, int h, int rep) {
    asm volatile("; MARK role_A"); __builtin_amdgcn_s_waitcnt(0);
    using namespace la;
    const int lane0 = lane_id();
    const rsrc_t rs = mkrs(a.ws, WS_NEED);
    const unsigned lane16 = lane0 * 16, wl = L_W + w * (11 * 2048) + lane16;
    v8u Wc[3];
    unsigned nb;
    {
        const int lane = lane0;
        const unsigned ta = (lane >> 4) * 2048 + w * 256 + (lane & 15) * 16;
        const rsrc_t r1 = mkrs(a.Wq, WSZ(8u << 20)), r2 = mkrs(a.Wk, WSZ(8u << 20));
        const unsigned v1 = (8 * w + ((lane >> 2) & 3)) * 8192 + h * 256 + (lane >> 4) * 64 + (lane & 3) * 16;
        const unsigned lw = opqs(w * 2048);
        load_blocks<32, 4, 4, true>([&](auto B, auto S) { pace(l); sfor<0, 2>([&](auto I) { dma_blk(lw + S.v * 16384 + I.v * 8192, B.v < 16 ? r1 : r2, v1, opqs((2 * (B.v & 15) + I.v) * 32 * 8192), 2048); }); },
                                    [&](auto B, auto S) { sfor<0, 4>([&](auto T) { constexpr int o = S.v * 16384 + (T.v >> 1) * 8192 + (T.v & 1) * 8;
                                                                                   put_frag<WMA, (B.v >> 4) * 2 + (T.v & 1), 2 * (B.v & 15) + (T.v >> 1), o, o + 1024>(ta, wl, Wc); });
                                                          if constexpr (B.v == 28) nb = side(rs, l); });
        TSW(127, 2);
    }
    const unsigned spm = na(nb, N_PM + h * 512), spmm = na(nb, N_PMM + h * 128), smem = na(nb, N_MEM + rep * 16384 + w * 4096);
    const int lane = lane_id(), c = lane & 15, g = lane >> 4;
    const unsigned own = (w >> 1) * 32768 + (w & 1) * 4096, wlo = (unsigned)(uintptr_t)a.ws, whi = (unsigned)((uintptr_t)a.ws >> 32), hi = -(w >> 1);
    const v8u pv{lane16, wl, (lane & 2) ? 0xEEEEu : 0x4444u, (unsigned)(L_QM + w * 1024 + (c >> 2) * 128 + g * 32 + (c & 3) * 4), (unsigned)(L_QM + g * 128 + (c & 7) * 16),
                 w == 2 ? ((g >> 1) * 16 + crow(c & 7, c >> 3)) * 16 + (g & 1) * 8 : w < 2 ? lane * 8 : lane < NC ? DV(lane * 512) : 1u << 30,
                 w < 2 ? c * 16 + g * 4 : c < 8 ? crow(c, g & 1) * 8 + (g >> 1) * 4 : 1u << 30, 0u};
    const v16u s0{wlo, whi, RSZ, 0x20000u, wlo, whi, 0u, 0x20000u, (unsigned)tri(0) | hi, (unsigned)(tri(0) >> 32) | hi, (unsigned)tri(1) | hi, (unsigned)(tri(1) >> 32) | hi,
                  (unsigned)tri(2) | hi, (unsigned)(tri(2) >> 32) | hi, (unsigned)tri(3) | hi, (unsigned)(tri(3) >> 32) | hi};
    const v16u s1{0xFF00FF00u, 0xFF00FF00u, 0u, ~0u, hi, hi, 0u, O_XN + l * XB + own, own, smem, w == 2 ? spm : OOB, nb + DV(N_PTX + h * 2048), w == 1 ? 0u : OOB, w == 0 ? 0u : OOB,
                  w < 2 ? w * 256 : w == 2 ? spmm - 1536 : OOB, ~hi};
    const v16u s2{w < 2 ? w * 1024 : w == 2 ? spm : spmm, w < 2 ? 4u : 3u, w < 2 ? 0u : w == 2 ? 0x200u : 0x380u, w ? 0u : __float_as_uint(-3e38f), (unsigned)(L_Q + w * 1024), (unsigned)(L_Q + (w & 1) * 4096),
                  __float_as_uint(SC_L2E), __float_as_uint(-1e30f), na(nb, N_FX), w == 3 ? na(nb, N_PA + ((h >> 3) * 32 + 8 + (h & 7)) * 4) : OOB, (unsigned)(L_KM + (w >> 1) * 1024 + (w & 1) * 256),
                  0u, 0u, (unsigned)(L_SP + w * 256), O_PROF + (blockIdx.x * 4 + w) * 8192, na(nb, N_WM + h * 16384 + w * 4096)};
    asm volatile(A_LOOP :: "{v[0:7]}"(Wc[0]), "{v[8:15]}"(Wc[1]), "{v[16:23]}"(Wc[2]), "{v[24:31]}"(pv), "{s[8:23]}"(s0), "{s[24:39]}"(s1), "{s[40:55]}"(s2),
                 "{s58}"(na(nb, N_U + 8 * w * 65536 + h * 2048)) : "memory", "s99", "vcc");
}
constexpr int bkind(int n) { return (B_LDS >> n & 1) ? 1 : (B_VAR >> n & 1) ? 2 : 0; }
struct WMV { static constexpr int NT = 2, NP = 16, RB = 0, LB = 0, CBI = 0; static constexpr int kind(int j, int t) { return bkind(16 * j + t); } };
struct WMO { static constexpr int NT = 16, NP = 2, LB = __builtin_popcount((unsigned)B_LDS), CBI = __builtin_popcount((unsigned)B_VAR), RB = 8 * (32 - LB - CBI);
             static constexpr int kind(int j, int t) { return bkind(32 + 2 * j + t); } };
#ifndef B_LV
#define B_LV 24576
#endif
namespace lb { constexpr int L_V = B_LV, L_VM = B_LV + 4096, L_W = 65536; }
DEVI void role_B(const Args& a, int w, int l, int h, int rep) {
    asm volatile("; MARK role_B"); __builtin_amdgcn_s_waitcnt(0);
    using namespace lb;
    const int lane0 = lane_id();
    const rsrc_t rs = mkrs(a.ws, WS_NEED);
    const unsigned lane16 = lane0 * 16, wl = L_W + w * (12 * 2048) + lane16;
    v8u Wc[2];
    unsigned nb, fx;
    {
        const int lane = lane0;
        const unsigned ta = (lane >> 4) * 2048 + w * 256 + (lane & 15) * 16, tb = (lane >> 5) * 8192 + w * 2048 + (lane & 16) * 64 + (lane & 15) * 16;
        const rsrc_t r1 = mkrs(a.Wv, WSZ(8u << 20)), r2 = mkrs(a.Wo, WSZ(8u << 20));
        const unsigned v1 = (8 * w + ((lane >> 2) & 3)) * 8192 + h * 256 + (lane >> 4) * 64 + (lane & 3) * 16, v2 = (h * 128 + ((lane >> 2) & 7)) * 2048 + w * 512 + (lane >> 5) * 64 + (lane & 3) * 16;
        const unsigned lw = opqs(w * 2048);
        load_blocks<32, 4, 4, true>([&](auto B, auto S) {
            constexpr int b = B.v & 15;
            pace(l);
            sfor<0, 2>([&](auto I) { if constexpr (B.v < 16) dma_blk(lw + S.v * 16384 + I.v * 8192, r1, v1, opqs((2 * b + I.v) * 32 * 8192), 2048);
                                     else dma_blk(lw + S.v * 16384 + I.v * 8192, r2, v2, opqs(((b & 3) * 32 + I.v * 16) * 2048 + (b >> 2) * 128), 1024); });
        }, [&](auto B, auto S) {
            constexpr int b = B.v & 15;
            sfor<0, 4>([&](auto T) { constexpr int o = S.v * 16384 + (T.v >> 1) * (B.v < 16 ? 8192 : 512) + (T.v & 1) * 8;
                                     if constexpr (B.v < 16) put_frag<WMV, T.v & 1, 2 * b + (T.v >> 1), o, o + 1024>(ta, wl, Wc); else put_frag<WMO, (b & 12) + T.v, b & 3, o, o + 256>(tb, wl, Wc); });
            if constexpr (B.v == 28) { nb = side(rs, l); fx = ld4(rs, (lane & 7) * 4, na(nb, N_FX)); }
        });
        TSW(127, 2);
    }
    const int lane = lane_id(), c = lane & 15, g = lane >> 4;
    const unsigned own = (w >> 1) * 32768 + (w & 1) * 4096, wlo = (unsigned)(uintptr_t)a.ws, whi = (unsigned)((uintptr_t)a.ws >> 32);
    const unsigned prow = L_VM + w * 512 + ((c >> 2) & 1) * 256 + g * 64 + (c >> 3) * 32 + (c & 1) * 8 + ((c >> 1) & 1) * 4;
    const v4u pv{lane16, wl, ((lane & 2) ? 0xEEEEu : 0x4444u) | prow << 16, (unsigned)(L_VM + (g >> 1) * 512 + (lane & 3) * 64 + (g & 1) * 32 + ((lane >> 2) & 3) * 8)};
    const v16u s0{wlo, whi, RSZ, 0x20000u, wlo, whi, 0u, 0x20000u, OOB, OOB, OOB, OOB, ~1u, O_XN + l * XB + own, own, na(nb, N_MEM + rep * 16384 + w * 4096)};
    const v8u s1{na(nb, N_WM + h * 16384 + w * 4096), na(nb, N_U + h * 65536 + w * 16384), na(nb, N_FX), w == 3 ? na(nb, N_PA + ((h >> 3) * 32 + (h & 7)) * 4) : OOB,
                 (unsigned)(L_V + w * 1024), OOB, 0u, O_PROF + (blockIdx.x * 4 + w) * 8192 + 64};
    asm volatile(B_LOOP :: "{v[0:7]}"(Wc[0]), "{v[8:15]}"(Wc[1]), "{v[16:19]}"(pv), "{v20}"(fx), "{s[8:23]}"(s0), "{s[24:31]}"(s1), "{s32}"(1u) : "memory", "s99", "vcc");
}
template <class NB> DEVI void put_tile(v4u (&u)[8], rsrc_t rs, int tile, int w, int lane, NB&& nbf) {
    const unsigned vo = tile * 32768 + w * 8192 + lane * 16, red = (lane & 15) * 16;
    float s = 0.f;
#pragma unroll
    for (int f = 0; f < 8; ++f) {
#pragma unroll
        for (int i = 0; i < 4; ++i) s += bflo(u[f][i]) + bfhi(u[f][i]);
    }
    s = red32(red16(s));
    BAR;
    if (lane < 16) *LDSP(float, red + w * 4) = s;
    WL0; BAR;
    const f32x4 sa = __builtin_bit_cast(f32x4, ldsq(red, 0));
    const float mean = ((sa[0] + sa[1]) + (sa[2] + sa[3])) * (1.0f / 1024.0f);
    float q = 0.f;
#pragma unroll
    for (int f = 0; f < 8; ++f) {
        asm volatile("" : "+v"(u[f]));
#pragma unroll
        for (int i = 0; i < 4; ++i) { const float d0 = bflo(u[f][i]) - mean, d1 = bfhi(u[f][i]) - mean; q = fmaf(d0, d0, fmaf(d1, d1, q)); }
    }
    q = red32(red16(q));
    if (lane < 16) *LDSP(float, red + 256 + w * 4) = q;
    WL0; BAR;
    const f32x4 qa = __builtin_bit_cast(f32x4, ldsq(red, 256));
    const float mb = rbf(mean), inv = 1.0f / rbf(sqrtf(rbf(rbf(((qa[0] + qa[1]) + (qa[2] + qa[3])) * (1.0f / 1024.0f)) + 1e-5f)));
#pragma unroll
    for (int f = 0; f < 8; ++f) {
        v4u o;
        asm volatile("" : "+v"(u[f]));
#pragma unroll
        for (int i = 0; i < 4; ++i) { const unsigned d = pack2(bflo(u[f][i]) - mb, bfhi(u[f][i]) - mb); o[i] = pack2(bflo(d) * inv, bfhi(d) * inv); }
        st16(o, rs, O_XN + vo + f * 1024);
    }
    const unsigned nb = nbf();
    WV0;
    if (lane == 0) st4(0u, rs, na(nb, N_FX) + tile * 16 + w * 4, 0);
}
#ifndef SXL
#define SXL 1
#endif
#ifndef PTD
#define PTD 4
#endif
#ifndef HLD
#define HLD 170
#endif
#ifndef SXH
#define SXH 30
#endif
#ifndef SXP
#define SXP 1
#endif
#ifndef SXH0
#define SXH0 SXH
#endif
#ifndef WF
#define WF 0
#endif
#ifndef UIN
#define UIN 8
#endif
namespace lr { constexpr int PT1 = NV + 48, PT0 = NV + 80, PTM = NV + 96, VPM = NV, VMM = NV + 16, VW = NV + 28, VX = NV + 44, L_M = 1024, MD = 288, MW = 4 * MD, L_T = 8192; }
template <int R, int N, int OFF> DEVI void lda(rsrc_t rs, unsigned vo, unsigned so) {
    if constexpr (N == 4) asm volatile("buffer_load_dwordx4 a[%3:%4], %0, %1, %2 offen offset:%5 sc1" :: "v"(vo), "s"(rs), "s"(so), "n"(R), "n"(R + 3), "n"(OFF) : "memory");
    else asm volatile("buffer_load_dwordx2 a[%3:%4], %0, %1, %2 offen offset:%5 sc1" :: "v"(vo), "s"(rs), "s"(so), "n"(R), "n"(R + 1), "n"(OFF) : "memory");
}
template <int R, int N, int OFF> DEVI void ldv(rsrc_t rs, unsigned vo, unsigned so) {
    if constexpr (N == 4) asm volatile("buffer_load_dwordx4 v[%3:%4], %0, %1, %2 offen offset:%5 sc1" :: "v"(vo), "s"(rs), "s"(so), "n"(R), "n"(R + 3), "n"(OFF) : "memory");
    else asm volatile("buffer_load_dwordx2 v[%3:%4], %0, %1, %2 offen offset:%5 sc1" :: "v"(vo), "s"(rs), "s"(so), "n"(R), "n"(R + 1), "n"(OFF) : "memory");
}
template <int R> DEVI unsigned rdv() { unsigned t; asm volatile("v_mov_b32 %0, v[%1]" : "=v"(t) : "n"(R)); return t; }
template <int RA, int RB, bool Z, bool AV> DEVI void mfh(f32x4& d) {
    if constexpr (AV) { if constexpr (Z) asm volatile(M16 "%0, v[%1:%2], v[%3:%4], 0" : "=v"(d) : "n"(RA), "n"(RA + 3), "n"(RB), "n"(RB + 3));
                        else asm volatile(M16 "%0, v[%1:%2], v[%3:%4], %0" : "+v"(d) : "n"(RA), "n"(RA + 3), "n"(RB), "n"(RB + 3)); }
    else { if constexpr (Z) asm volatile(M16 "%0, a[%1:%2], v[%3:%4], 0" : "=v"(d) : "n"(RA), "n"(RA + 3), "n"(RB), "n"(RB + 3));
           else asm volatile(M16 "%0, a[%1:%2], v[%3:%4], %0" : "+v"(d) : "n"(RA), "n"(RA + 3), "n"(RB), "n"(RB + 3)); }
}
template <int RA, int RB, bool Z> DEVI void mfh16(f32x4& d) {
    if constexpr (Z) asm volatile("v_mfma_f32_16x16x16_bf16 %0, a[%1:%2], v[%3:%4], 0" : "=v"(d) : "n"(RA), "n"(RA + 1), "n"(RB), "n"(RB + 1));
    else asm volatile("v_mfma_f32_16x16x16_bf16 %0, a[%1:%2], v[%3:%4], %0" : "+v"(d) : "n"(RA), "n"(RA + 1), "n"(RB), "n"(RB + 1));
}
template <int RA, int RB, bool BV> DEVI void smfh(f32x4& d, int sidx) {
    if constexpr (BV) asm volatile(S16 "%0, v[%2:%3], v[%4:%5], %1" : "+v"(d) : "v"(sidx), "n"(RA), "n"(RA + 3), "n"(RB), "n"(RB + 7));
    else asm volatile(S16 "%0, v[%2:%3], a[%4:%5], %1" : "+v"(d) : "v"(sidx), "n"(RA), "n"(RA + 3), "n"(RB), "n"(RB + 7));
}
template <int R> DEVI u64 ltv(int need) { u64 m; asm volatile("v_cmp_lt_i32 %0, v[%2], %1" : "=s"(m) : "v"(need), "n"(R)); return m; }
template <int R> DEVI void ldv4(rsrc_t rs, unsigned vo, unsigned so) { asm volatile("buffer_load_dwordx4 v[%3:%4], %0, %1, %2 offen sc1" :: "v"(vo), "s"(rs), "s"(so), "n"(R), "n"(R + 3) : "memory"); }
template <int W0, int P0>
DEVI void poll_round(unsigned vw, unsigned lane16, unsigned vmm, rsrc_t rs, unsigned sw0, unsigned sw1, unsigned sp, unsigned sm) {
    asm volatile(
#if WF
        "buffer_load_dwordx4 v[%[w0]:%[w0]+3], %[vw], %[rs], %[sw0] offen sc1\n\t"
        "buffer_load_dwordx4 v[%[w0]+4:%[w0]+7], %[vw], %[rs], %[sw1] offen sc1\n\t"
        "buffer_load_dwordx4 v[%[w0]+8:%[w0]+11], %[vw], %[rs], %[sw0] offen offset:256 sc1\n\t"
        "buffer_load_dwordx4 v[%[w0]+12:%[w0]+15], %[vw], %[rs], %[sw1] offen offset:256 sc1\n\t"
#endif
        "buffer_load_dwordx4 v[%[p0]:%[p0]+3], %[l16], %[rs], %[sp] offen sc1\n\t"
        "buffer_load_dwordx4 v[%[p0]+4:%[p0]+7], %[l16], %[rs], %[sp] offen offset:1024 sc1\n\t"
        "buffer_load_dwordx4 v[%[p0]+8:%[p0]+11], %[l16], %[rs], %[sp] offen offset:2048 sc1\n\t"
        "buffer_load_dwordx4 v[%[p0]+12:%[p0]+15], %[l16], %[rs], %[sp] offen offset:3072 sc1\n\t"
        "buffer_load_dwordx2 v[%[p0]+16:%[p0]+17], %[vm], %[rs], %[sm] offen sc1\n\t"
        "buffer_load_dwordx2 v[%[p0]+18:%[p0]+19], %[vm], %[rs], %[sm] offen offset:128 sc1\n\t"
#if !WF
        "buffer_load_dwordx4 v[%[w0]:%[w0]+3], %[vw], %[rs], %[sw0] offen sc1\n\t"
        "buffer_load_dwordx4 v[%[w0]+4:%[w0]+7], %[vw], %[rs], %[sw1] offen sc1\n\t"
        "buffer_load_dwordx4 v[%[w0]+8:%[w0]+11], %[vw], %[rs], %[sw0] offen offset:256 sc1\n\t"
        "buffer_load_dwordx4 v[%[w0]+12:%[w0]+15], %[vw], %[rs], %[sw1] offen offset:256 sc1\n\t"
#endif
        :: [w0] "n"(W0), [p0] "n"(P0), [vw] "v"(vw), [l16] "v"(lane16), [vm] "v"(vmm), [rs] "s"(rs), [sw0] "s"(sw0), [sw1] "s"(sw1), [sp] "s"(sp), [sm] "s"(sm) : "memory");
}
template <int R0, int R1> DEVI void mx3(unsigned& m) { asm volatile("v_max3_u32 %0, %0, v[%1], v[%2]" : "+v"(m) : "n"(R0), "n"(R1)); }
template <int R> DEVI void tagd(unsigned& m, unsigned t) { asm volatile("v_bitop3_b32 %0, %0, v[%2], %1 bitop3:0xf6" : "+v"(m) : "s"(t), "n"(R)); }
template <int O> DEVI void wr2(unsigned p, float r0, float r1) { asm volatile("ds_write2_b32 %0, %1, %2 offset0:%3 offset1:%4" :: "v"(p), "v"(r0), "v"(r1), "n"(O), "n"(O + 2) : "memory"); }
#ifndef DLY_X
#define DLY_X 1300
#endif
DEVI void role_R(const Args& a, int w, int l, int T2, int t0) {
    asm volatile("; MARK role_R"); __builtin_amdgcn_s_waitcnt(0);
    using namespace lr;
    const int lane = lane_id(), c = lane & 15, g = lane >> 4;
    const rsrc_t rs = mkrs(a.ws, WS_NEED);
    const unsigned lane16 = lane * 16;
#pragma nounroll
    for (int k = 0; k < 4 * l; ++k) {
        const int tile = k * 32 + T2;
        if (tile >= LN_NOW) hold(t0, DLY_X);
        v4u u[8];
#pragma unroll
        for (int f = 0; f < 8; ++f) u[f] = *(const v4u*)(a.x + (size_t)(tile * 16 + c) * 1024 + (w * 8 + f) * 32 + g * 8);
        if (tile < 2 * NSEG) put_tile(u, rs, tile, w, lane, [&]() { hold(t0, DLY_V); return side(rs, 0); });
        TSW(127, 1 + k);
    }
    auto arm = [&](unsigned n2, int s) {
        if (s < 8)
#pragma unroll
            for (int k = 0; k < 4; ++k) st16(v4u{SENT, SENT, SENT, SENT}, rs, na(n2, N_WM + s * 524288 + T2 * 16384 + w * 4096) + k * 1024 + lane16);
        st16(v4u{SENT, SENT, SENT, SENT}, rs, w < 2 ? na(n2, N_PTX + s * 65536 + T2 * 2048) + w * 1024 + lane16 : lane >= (w == 2 ? 32 : 8) || s >= 8 ? OOB :
                                              na(n2, w == 2 ? N_PM + s * 16384 + T2 * 512 : N_PMM + s * 8192 + T2 * 128) + lane16);
    };
    if (l == 0) { arm(O_N, 0); arm(O_N + 8192, 0); }
    hold(t0, DLY_V);
    const unsigned nb = side(rs, l), vmem = c < 8 ? na(nb, N_MEM + T2 * 512) + g * 65536 + w * 128 + c * 16 : OOB;
    auto put_mem = [&](v4u v, int s) { st16(v, rs, vmem + rslot(s) * (NREP * 32768)); };
    {
        const unsigned* m0 = (const unsigned*)(a.mem0 + ((w & 1) * 4 + ((lane >> 2) & 1) * 2 + (lane & 1)) * 1024 + T2 * 32 + (w >> 1) * 16 + ((lane >> 1) & 1) * 2);
        const v4u mv{nosent(m0[0]), m0[2], m0[4], m0[6]};
        put_mem(v4u{SENT, SENT, SENT, SENT}, 1); put_mem(v4u{SENT, SENT, SENT, SENT}, 2);
        arm(nb, 1);
        WV0; BAR;
        put_mem(mv, 0);
    }
    const int sidx = opq((lane & 2) ? 0xEEEE : 0x4444);
    const unsigned vmm = (8 * w + 2 * g) * 128 + c * 8, vw = (8 * w + g) * 32768 + DV(T2 * 512) + c * 16, vptm = g * 2048 + 1536 + c * 16;
    const unsigned sfl = na(nb, N_PA + (unsigned)((T2 & (NC - 1)) * 4 + w) * 128), su0 = na(nb, N_U + 8 * w * 65536 + T2 * 2048), vfl = sfl + c * 4;
    const unsigned lred = L_M + w * MW + ((c >> 2) & 1) * 2 * MD + (c >> 3) * 128 + g * 32 + ((c >> 1) & 1) * 16 + (c & 1) * 4, lrr = L_M + g * MD + w * 64 + (c & 7) * 8;
    auto ld_u = [&](auto B, int s) {
        const unsigned so = su0 + rslot(s) * (4u << 20);
        sfor<0, 16>([&](auto I) { constexpr int j = I.v >> 1, e = I.v & 1; lda<64 * B.v + 16 * (j >> 1) + 8 * e + 4 * (j & 1), 4, e * 1024>(rs, lane16, so + j * 131072); });
    };
    wait_prog(rs, sfl + (lane & 7) * 4, 0, 0);
    ld_u(IC<0>{}, 0);
    wait_prog(rs, vfl, 0, 1 << 24);
    const rsrc_t rxi = l ? rs : mkrs(a.x, XB);
    const unsigned vxi = l ? lane16 : c * 2048 + g * 16, sxi = (w & 1) * 32768 + (l ? O_XIN + XB + T2 * 1024 : T2 * 64);
    v2u sxv{0u, 0u};
    unsigned sxo = OOB;
    int tpub = t0;
    auto step = [&](auto B, int s) {
        constexpr int b = B.v;
        TSD;
        TSR(0); TSK(15);
        if (s == 8) { TSC(126, 0); TSW(126, 1); }
        if (s == 56) { TSC(126, 2); TSW(126, 3); }
        const unsigned spt = na(nb, N_PTX + (unsigned)((s & 15) * 32 + 8 * w) * 2048);
        auto ptx = [&](auto N) {
            constexpr int n = N.v & 31, j = n >> 1;
            if constexpr (n == 20) ldv4<VX>(rxi, vxi, sxi + s * 65536);
            else if constexpr (n >= 16) ldv<PTM + 4 * (n - 16), 4, (n & 1) * 256>(rs, vptm, spt + (j - 8) * 16384);
            else if constexpr (n & 1) ldv<PT0 + 2 * j, 2, 1024>(rs, lane * 8, spt + DV(j * 2048));
            else ldv<PT1 + 4 * j, 4, 0>(rs, lane16, spt + DV(j * 2048));
        };
        {
            const unsigned sw = na(nb, N_WM + rslot(s) * 524288), vpub = opq(vmem + rslot(s + 1) * (NREP * 32768));
            if ((int)__builtin_amdgcn_s_memrealtime() - tpub < HLD) { wvm<16 - UIN>(); st8(sxv, rs, sxo, 0); sxo = OOB; hold(tpub, HLD); }
            wait_hint(rs, sfl, vfl, s + 1, [&](int mx) { if (SXH > -99 && left(mx) > (l ? SXH : SXH0) * 256) { wvm<16 - UIN>(); st8(sxv, rs, sxo, 0); sxo = OOB; } });
            TSR(5);
            unsigned spins = 0;
            f32x4 e0, e1;
            unsigned mw;
            const unsigned tg = (s >> 3) & 1;
            auto look = [&]() { zero4(e0); zero4(e1); mw = opq(0); poll_round<VW, VPM>(vw, lane16, vmm, rs, sw, sw + 131072, na(nb, N_PM + w * 4096) + rslot(s) * 32768, na(nb, N_PMM) + rslot(s) * 16384); };
            auto prod = [&](auto NY) {
                sfor<0, 10>([&](auto N) {
                    constexpr int n = N.v, p = n >> 1, e = n & 1;
                    if constexpr (n < 8 && e == 0) wvm<(n ? NY.v : 0) + (WF ? 5 : 9) - p>();
                    if constexpr (n >= 8) wvm<NY.v + (WF ? 0 : 2 - 2 * e)>();
                    if constexpr (n == 1 && NY.v) { st8(sxv, rs, sxo, 0); sxo = OOB; }
                    if constexpr (p < 4) smfh<VPM + 4 * p, 64 * b + 16 * p + 8 * e, false>(e ? e1 : e0, sidx);
                    else smfh<VMM, VW + 8 * e, true>(e ? e1 : e0, sidx);
                    if constexpr (n < 8) tagd<VPM + 4 * p + 2 * e>(mw, tg);
                    if constexpr (n >= 8) { if constexpr (e == 0) sfor<0, 4>([&](auto I) { tagd<VMM + I.v>(mw, tg); }); tagd<VW + 8 * e>(mw, tg); tagd<VW + 8 * e + 4>(mw, tg); }
                    SB;
                });
            };
#pragma nounroll
            for (;;) {
                look(); prod(IC<SXP>{});
                nop<3>(); fence(e0, e1);
                if (__builtin_amdgcn_uicmp(mw & 1, 0u, 33) == 0) break;
                spin_bail(spins);
                nop<7>();
            }
            TSR(1);
#if defined(TS) && TS != 4
            tsr[8] = spins + 1;
#endif
            wr2<0>(lred, fadd(e0[0], e0[2]), fadd(e0[1], e0[3])); wr2<MD / 4>(lred, fadd(e1[0], e1[2]), fadd(e1[1], e1[3]));
            sfor<0, PTD>(ptx);
            WL0; BAR;
            f32x2 q[4];
#pragma unroll
            for (int k = 0; k < 4; ++k) q[k] = *LDSP(const f32x2, LDSP(char, lrr) + k * MW);
            unsigned m0 = nosent(pack2(fadd(fadd(q[0][0], q[1][0]), fadd(q[2][0], q[3][0])), fadd(fadd(q[0][1], q[1][1]), fadd(q[2][1], q[3][1])))), m1 = m0;
            swap16u(m0, m1);
            unsigned m2 = m0, m3 = m1;
            swap32u(m0, m2); swap32u(m1, m3);
            st16(v4u{m0, m1, m2, m3}, rs, vpub);
            if (!SXP) st8(sxv, rs, sxo, 0);
            tpub = (int)__builtin_amdgcn_s_memrealtime();
            TSR(2);
        }
        sfor<PTD, 21>(ptx);
        if (PTD < 21) BAR;
        f32x4 t[2][2], u[2];
        put_mem(v4u{SENT, SENT, SENT, SENT}, s + 3);
#pragma nounroll
        for (int n = 0;; ++n) {
            if (n) { sfor<32, 52>(ptx); WV0; }
            const unsigned su = su0 + rslot(s + 1) * (4u << 20);
            auto lu = [&](auto N) { constexpr int j = N.v >> 1, e = N.v & 1; lda<16 * (j >> 1) + 8 * e + 4 * (j & 1) + 64 * (1 - b), 4, e * 1024>(rs, lane16, su + j * 131072); };
            TSR(6);
            unsigned mt = opq(0);
            const unsigned tp = (s >> 4) & 1;
            sfor<0, 16>([&](auto N) {
                constexpr int j = N.v >> 1, e = N.v & 1, R = 64 * b + 16 * (j >> 1) + 8 * e + 4 * (j & 1), nu = N.v * UIN / 16;
                if constexpr (e == 0) wvm<21 - 2 * j + (2 * j + 1 < PTD ? 2 - SXP : 0) + nu - 1>();
                if constexpr ((N.v + 1) * UIN / 16 > nu) lu(IC<nu>{});
                mfh<R, PT1 + 4 * j, j == 0, false>(t[1][e]); tagd<e ? PT0 + 2 * j : PT1 + 4 * j>(mt, tp); mfh16<R, PT0 + 2 * j, j == 0>(u[e]);
            });
            wvm<1 + (PTD == 21 ? 2 - SXP : 0) + UIN>();
            sfor<0, 8>([&](auto N) { constexpr int q = N.v >> 2, rt = (N.v >> 1) & 1, e = N.v & 1; mfh<VW + 8 * e + 4 * q, PTM + 4 * (2 * q + rt), N.v < 2, true>(t[rt][e]);
                                     tagd<PTM + 2 * N.v>(mt, tp); tagd<PTM + 2 * N.v + 1>(mt, tp); });
            nop<7>(); fence(t[0][0], t[0][1], t[1][0], t[1][1], u[0], u[1]);
#pragma unroll
            for (int e = 0; e < 2; ++e)
#pragma unroll
                for (int i = 0; i < 4; ++i) t[0][e][i] = fadd(t[0][e][i], u[e][i]);
            if (__builtin_amdgcn_uicmp(mt & 1, 0u, 33) == 0 || n == 2) break;
            TSV(s, 7, 1);
            if (n == 1) wait_prog(rs, sfl + 32 + (lane & 7) * 4, 0, (s + 2) << 24);
        }
        TSR(3);
#pragma unroll
        for (int e = 0; e < 2; ++e) { stsq(lane16 + w * 4096, L_T + e * 1024, __builtin_bit_cast(v4u, t[0][e])); stsq(lane16 + w * 4096, L_T + 2048 + e * 1024, __builtin_bit_cast(v4u, t[1][e])); }
        WL0; BAR;
        if (w < 2) {
            float o[8], s1 = 0.f, s2 = 0.f;
#pragma unroll
            for (int e = 0; e < 2; ++e) {
                f32x4 q[4];
#pragma unroll
                for (int k = 0; k < 4; ++k) q[k] = __builtin_bit_cast(f32x4, ldsq(lane16 + w * 2048, L_T + k * 4096 + e * 1024));
#pragma unroll
                for (int i = 0; i < 4; ++i) o[4 * e + i] = fadd(fadd(q[0][i], q[1][i]), fadd(q[2][i], q[3][i]));
            }
            v4u x1;
            sfor<0, 4>([&](auto I) {
                constexpr int i = I.v;
                const unsigned x = rdv<VX + i>(), at = pack2(o[2 * i], o[2 * i + 1]);
                x1[i] = pack2(bflo(x) + bflo(at), bfhi(x) + bfhi(at));
                const float lo = bflo(x1[i]), hi = bfhi(x1[i]);
                s1 += lo + hi; s2 = fmaf(lo, lo, fmaf(hi, hi, s2));
            });
            st16(x1, rs, O_XM + l * XB + s * 65536 + w * 32768 + T2 * 1024 + lane16);
            sxv = v2u{nosent(__float_as_uint(red32(red16(s1)))), __float_as_uint(red32(red16(s2)))};
            sxo = lane < 16 ? O_SX + (unsigned)((l * 64 + s) * 32 + T2) * 256 + (SXL ? w * 128 + c * 8 : c * 16 + w * 8) : OOB;
        }
        TSR(4);
        {
            const unsigned su = su0 + rslot(s + 1) * (4u << 20);
            sfor<UIN, 16>([&](auto N) { constexpr int j = N.v >> 1, e = N.v & 1; lda<16 * (j >> 1) + 8 * e + 4 * (j & 1) + 64 * (1 - b), 4, e * 1024>(rs, lane16, su + j * 131072); });
        }
        if (s < 14) arm(nb, s + 2);
#if TS != 4
        TSF(s, 0x817F);
#endif
    };
#pragma nounroll
    for (int s = 0; s < NSEG; s += 2) { step(IC<0>{}, s); step(IC<1>{}, s + 1); }
    wvm<16 - UIN>(); st8(sxv, rs, sxo, 0);
    WV0;
}
#ifndef FSIDE
#define FSIDE 1
#endif
#ifndef PDX
#define PDX 0
#endif
constexpr int NU = 2 * NSEG;
#ifndef PSF
#define PSF 40
#endif
#ifndef PSR
#define PSR 12
#endif
#ifndef RS1
#define RS1 52
#endif
constexpr int RSH = 4 * (64 + RS1);
#ifndef RPA
#define RPA 8
#endif
#ifndef GROT
#define GROT 1
#endif
#ifndef LAGK
#define LAGK 10
#endif
#ifndef LAGP
#define LAGP 0
#endif
DEVI unsigned unit_ls(unsigned u) {
    static_assert(NSEG == 64 && LAGD >= 6, "");
    static_assert(LAGP % 2 == 0 && LAGP < 2 * LAGK + 1, "");
    const unsigned d = u - LAGD + LAGP, p = d / (2 * LAGK + 1), r = d % (2 * LAGK + 1), n0 = LAGD - LAGP / 2 + p * (LAGK + 1) + (r >> 1);
    return u < LAGD || n0 >= 64 ? u : ((r & 1) || r == 2 * LAGK) ? n0 : 64 + u - n0;
}
DEVI unsigned unit_tab(int lane) { return opq(unit_ls(2 * lane) | unit_ls(2 * lane + 1) << 8); }
DEVI unsigned unit_get(unsigned tab, int u) { return ((unsigned)__builtin_amdgcn_readlane(tab, (u >> 1) & 63) >> ((u & 1) * 8)) & 255; }
struct WMG { static constexpr int NT = 3, NP = 16, RB = 0, LB = 0, CBI = 0; static constexpr int kind(int, int) { return 0; } };
struct WMD { static constexpr int NT = 4, NP = 12, RB = 0, LB = 0, CBI = 0; static constexpr int kind(int, int) { return 0; } };
namespace lg { constexpr int L_ST = 155648, L_OK = 159744; }
DEVI void role_GU(const Args& a, int w, int j) {
    asm volatile("; MARK role_GU"); __builtin_amdgcn_s_waitcnt(0);
    using namespace lg;
    const int lane = lane_id(), c = lane & 15, g = lane >> 4;
    const rsrc_t rs = mkrs(a.ws, WS_NEED);
    const unsigned lane16 = lane * 16, hc = 96 * j + 24 * w;
    {
        const unsigned ta = g * 4096 + (lane & 1) * 2048 + w * 256 + ((lane >> 2) & 3) * 64 + ((lane >> 1) & 1) * 8, tb = ta + 65536;
        const rsrc_t r1 = mkrs(a.Wg, WSZ(7168u * 1024)), r2 = mkrs(a.Wu, WSZ(7168u * 1024));
        const unsigned cb = 192 * j + g * 48 + (lane & 3) * 16, v1 = (lane & 3) == 3 || cb >= 7168 ? OOB : (8 * w + ((lane >> 2) & 3)) * 7168 + cb, lw = opqs(w * 4096), z = opqs(0);
        load_blocks<32, 8, 4, true>([&](auto B, auto S) { dma_blk(lw + S.v * 16384, r1, v1, z + B.v * 32 * 7168, 1792); dma_blk(lw + S.v * 16384 + 2048, r2, v1, z + B.v * 32 * 7168, 1792); },
                                    [&](auto B, auto S) { sfor<0, 3>([&](auto T) { constexpr int o = (S.v & 3) * 16384 + T.v * 16; put_frag<WMG, T.v, B.v, o, o + 1024>(S.v < 4 ? ta : tb, 0, nullptr); }); });
        TSW(120 - 65536, 0);
        v4u one{0x3F803F80u, 0x3F803F80u, 0x3F803F80u, 0x3F803F80u};
        asm volatile("s_nop 1" : "+v"(one));
        f32x4 cs[3];
        sfor<0, 96>([&](auto N) { constexpr int t = N.v % 3, ks = N.v / 3; mfA<8 * widx<WMG>(t, ks >> 1) + 4 * (ks & 1), ks == 0>(cs[t], one); });
        nop<7>(); fence(cs[0], cs[1], cs[2]);
        sfor<0, 3>([&](auto T) { mwr4<384 + 4 * T.v>(__builtin_bit_cast(v4u, cs[T.v])); });
        BAR;
    }
    const unsigned nf = side(rs, FSIDE);
    const unsigned wlo = (unsigned)(uintptr_t)a.ws, whi = (unsigned)((uintptr_t)a.ws >> 32);
    const unsigned xr = w * 8192 + (GROT ? (j & 1) * 4096 + (j & 2) * 16384 : 0);
    const v8u pv{lane16, O_SX + (8 * w + g) * 256 + c * 16, O_H + (g & 1) * 114688 + hc * 32 + c * 16 + (g >> 1) * 8, lane == 0 ? na(nf, N_PG) + j * 128 + w * 4 : OOB,
                 (unsigned)(L_ST + ((lane & 1) * 16 + ((lane & 31) >> 1)) * 16), L_ST + lane16 + w * 4, unit_tab(lane), lane16};
    const v16u s0{wlo, whi, RSZ, 0x20000u, wlo, whi, 0u, 0x20000u, OOB, OOB, hc < 3584 ? 0u : OOB, hc + 8 < 3584 ? 0u : OOB, 0u, O_XM + xr, 65536 + xr, 0u};
    const v2u s1{0u, O_PROF + (blockIdx.x * 4 + w) * 8192};
    asm volatile(G_LOOP :: "{v[0:7]}"(pv), "{s[8:23]}"(s0), "{s[24:25]}"(s1) : "memory", "s99", "vcc");
}
DEVI void role_DN(const Args& a, int w, int p, int cg) {
    asm volatile("; MARK role_DN"); __builtin_amdgcn_s_waitcnt(0);
    using namespace lg;
    const int lane = lane_id();
    const rsrc_t rs = mkrs(a.ws, WS_NEED);
    const unsigned lane16 = lane * 16;
    const int F0 = p < 3 ? 23 * p : (p == 3 ? 68 : 89), zb = p < 2 ? -1 : (p == 4 ? 0 : 22);
    {
        const unsigned ta = w * 18432 + (lane >> 4) * 512 + ((lane >> 2) & 3) * 64 + (lane & 3) * 16;
        const rsrc_t r1 = mkrs(a.Wd, WSZ(3584u * 2048));
        const unsigned v1 = (F0 * 32 + (lane >> 2)) * 2048 + cg * 512 + w * 128 + (lane & 3) * 16;
        load_blocks<46, 9, 2>([&](auto B, auto S) { dma_blk(w * 18432 + S.v * 2048, r1, (B.v >> 1) == zb ? OOB : v1, (B.v >> 1) * 65536 + (B.v & 1) * 64, 2048); },
                              [&](auto B, auto S) { constexpr int ks = B.v >> 1, t = 2 * (B.v & 1), o = S.v * 2048;
                                                    put_frag<WMD, t, ks, o, o + 256>(ta, 0, nullptr); put_frag<WMD, t + 1, ks, o + 8, o + 264>(ta, 0, nullptr); });
        TSW(120 - 65536, 0);
        BAR;
    }
    const int g0 = F0 / 3 * 4, gn = ((F0 + 22) / 3 + 1) * 4 - g0;
    const unsigned nf = side(rs, FSIDE);
    const unsigned wlo = (unsigned)(uintptr_t)a.ws, whi = (unsigned)((uintptr_t)a.ws >> 32);
    const v8u pv{lane16, na(nf, N_PG) + g0 * 32 + (lane < gn ? (lane >> 2) * 128 + (lane & 3) * 4 : 0), lane == 0 ? na(nf ^ PDX, N_PD) + (cg * NKF + p) * 128 + w * 4 : OOB,
                 unit_tab(lane), lane16, (unsigned)(L_OK + lane * 4), (unsigned)((lane & 8) * 2048 + (lane >> 4) * 128 + (lane & 7) * 16), 0u};
    const v16u s0{wlo, whi, RSZ, 0x20000u, wlo, whi, 0u, 0x20000u, wlo, whi, w == 0 ? RSZ : 0u, 0x20000u, 0u, O_H + (w >> 1) * 114688 + (F0 + (w & 1) * 12) * 1024,
                  O_D + p * 65536 + (cg * 8 + w * 2) * 512, (unsigned)(65536 + (w >> 1) * 32768 + (w & 1) * 12288)};
    const v4u s1{(unsigned)(L_OK + w * 256), 0u, 0u, O_PROF + (blockIdx.x * 4 + w) * 8192};
    asm volatile(D_LOOP :: "{v[0:7]}"(pv), "{s[8:23]}"(s0), "{s[24:27]}"(s1) : "memory", "s99", "vcc");
}
template <int R> DEVI void ldv1(rsrc_t rs, unsigned vo) { asm volatile("buffer_load_dword v[%2], %0, %1, 0 offen sc1" :: "v"(vo), "s"(rs), "n"(R) : "memory"); }
template <int R, int I> DEVI void unp2(f32x4& d) { float x, y; asm volatile("v_lshlrev_b32 %0, 16, v[%2]\n\tv_and_b32 %1, 0xffff0000, v[%2]" : "=v"(x), "=v"(y) : "n"(R)); d[I] = x; d[I + 1] = y; }
DEVI void role_RED(const Args& a, int w, int cu) {
    asm volatile("; MARK role_RED"); __builtin_amdgcn_s_waitcnt(0);
    const int lane0 = lane_id(), c = lane0 & 15, g = lane0 >> 4;
    const rsrc_t rs = mkrs(a.ws, WS_NEED);
    const unsigned nf = side(rs, FSIDE);
    v4u sel[2];
#pragma unroll
    for (int e = 0; e < 2; ++e)
#pragma unroll
        for (int i = 0; i < 4; ++i) sel[e][i] = (g == (c >> 2) && i == 2 * e + ((c >> 1) & 1)) ? ((c & 1) ? 0x3F800000u : 0x3F80u) : 0u;
    const unsigned vfl = na(nf ^ PDX, N_PD) + w * (NKF * 128) + (lane0 < 20 ? (lane0 >> 2) * 128 + (lane0 & 3) * 4 : 0);
    constexpr int VP = NV + 32;
    const unsigned lo = (c & 8) * 128 + g * 256 + (c & 7) * 16 + (cu & 1) * 128 + w * 8192, ld = (c & 8) * 64 + g * 128 + (c & 7) * 16 + (cu & 1) * 16384 + w * 4096;
    const unsigned lout = ((cu & 1) * 8 + (c & 7)) * 2048 + w * 512 + (c & 8) * 8 + g * 16, lw = g == (c >> 2) ? (c & 7) * 64 + w * 16 + (c & 8) : 1024 + lane0 * 8;
    const rsrc_t ro = mkrs(a.out, 4u << 20);
    v4u one{0x3F803F80u, 0x3F803F80u, 0x3F803F80u, 0x3F803F80u};
    asm volatile("" : "+v"(one));
    auto issue = [&](auto B, auto N0, auto N1, int k) {
        const unsigned ls = k >> 2, base = ((k >> 1) & 1) * 32768;
        sfor<N0.v, N1.v>([&](auto N) { constexpr int f = (N.v - 4) / 5, p = (N.v - 4) % 5;
            if constexpr (N.v < 4) ldv4<NV + 16 * B.v + 4 * N.v>(rs, lo, O_XM + ls * 65536 + base + N.v * 2048);
            else lda<80 * B.v + 20 * f + 4 * p, 4, f * 1024>(rs, ld, O_D + (ls * NKF + p) * 65536 + base); });
        if constexpr (N1.v == 24) ldv1<VP>(rs, vfl);
    };
    *LDSP(unsigned char, 2048 + unit_ls(2 * lane0)) = 2 * lane0; *LDSP(unsigned char, 2048 + unit_ls(2 * lane0 + 1)) = 2 * lane0 + 1;
    WL0; BAR;
    const unsigned tab = opq(*LDSP(const unsigned short, 2048 + 2 * lane0));
    auto next = [&](int k) { const int n = k + (k >= RSH ? 6 : k < 256 ? 4 : 2); return k < RSH && n >= (k < 256 ? 256 : RSH) ? RSH + cu : n; };
    u64 late = 1, far = 1;
    bool pre = false;
    auto task = [&](auto B, int k) {
        constexpr int b = B.v;
        const int u = unit_get(tab, k >> 2), rt = (k >> 1) & 1, kn = next(k), nx = kn < 4 * NU ? unit_get(tab, kn >> 2) + 1 : NU + 1;
        const unsigned ls = k >> 2, tile = ls * 2 + rt;
        TSD; TSR(0);
        if (!pre) {
            if (late) {
                unsigned spins = 0;
#pragma nounroll
                for (;;) {
                    if (far) __builtin_amdgcn_s_sleep(PSF); else __builtin_amdgcn_s_sleep(PSR);
                    CB;
                    const int v = (int)ld4(rs, vfl, 0);
                    if (__builtin_amdgcn_sicmp(v, u + 1, 40) == 0) break;
                    far = __builtin_amdgcn_sicmp(v, u, 40);
                    spin_bail(spins);
                }
            }
            TSR(1);
            issue(B, IC<0>{}, IC<24>{}, k);
        }
        else { TSR(1); }
        const bool l0 = ls < 64;
        const rsrc_t rd = l0 ? rs : ro;
        const unsigned vd = opq((l0 ? O_XIN + XB + lo : lout - 64 * 65536) + tile * 32768), fs = l0 ? 2048 : 128;
        v4u x[4];
        f32x4 d[4][2], S, Q;
        auto unp = [&](auto F, auto I) { unp2<NV + 16 * b + 4 * F.v + I.v, 2 * (I.v & 1)>(d[F.v][I.v >> 1]); };
        auto fin = [&](auto F) {
            constexpr int f = F.v;
            fence(d[f][0], d[f][1]);
#pragma unroll
            for (int i = 0; i < 4; ++i) x[f][i] = pack2(d[f][i >> 1][2 * (i & 1)], d[f][i >> 1][2 * (i & 1) + 1]);
            asm volatile("" : "+v"(x[f]));
        };
        auto put = [&](auto F) { constexpr int f = F.v; st16s(x[f], rd, vd, f * fs); SB; mfv<f == 0>(S, one, x[f]); mfv<f == 0>(Q, x[f], x[f]); SB; };
        wvm<24>();
        sfor<0, 4>([&](auto I) { unp(IC<0>{}, I); });
        sfor<0, 4>([&](auto F) {
            constexpr int f = F.v;
            wvm<16 - 5 * f + (f ? f - 1 : 0)>();
            nop<1>();
            sfor<0, 10>([&](auto N) {
                mfB<80 * b + 20 * f + 4 * (N.v >> 1), false>(d[f][N.v & 1], sel[N.v & 1]);
                if constexpr (N.v < 4 && f < 3) unp(IC<(f + 1) & 3>{}, N);
                if constexpr (N.v == 5 && f > 0) fin(IC<(f + 3) & 3>{});
                SB;
            });
            if constexpr (f > 0) put(IC<(f + 3) & 3>{});
        });
        wvm<3>();
        TSR(2);
#ifdef TS
        tsr[4] = pre + 1;
#endif
        const bool p1 = ltv<VP>(nx) == 0;
        if (!p1) ldv1<VP + 1>(rs, vfl);
        nop<3>();
        fin(IC<3>{});
        nop<1>();
        put(IC<3>{});
        auto second = [&](auto NS) {
            wvm<NS.v>();
            pre = ltv<VP + 1>(nx) == 0;
            if (pre) { issue(IC<1 - b>{}, IC<0>{}, IC<24>{}, kn); if constexpr (NS.v == 5) wvm<25>(); }
            else {
                if constexpr (NS.v == 5) { ldv1<VP + 1>(rs, vfl); WV0; }
                late = ltv<VP + 1>(nx); far = ltv<VP + 1>(nx - 1);
            }
        };
        if (l0) {
            nop<7>();
            fence(S, Q);
            const float qd = selm(selm(Q[0], Q[1], 0xAAAAAAAAAAAAAAAAull), selm(Q[2], Q[3], 0xAAAAAAAAAAAAAAAAull), 0xCCCCCCCCCCCCCCCCull);
            *LDSP(f32x2, lw) = f32x2{S[0], qd};
            if (p1) issue(IC<1 - b>{}, IC<0>{}, IC<RPA>{}, kn);
            f32x2 xf[4][4];
#pragma unroll
            for (int f = 0; f < 4; ++f)
#pragma unroll
                for (int i = 0; i < 4; ++i) { xf[f][i] = f32x2{bflo(x[f][i]), bfhi(x[f][i])}; asm volatile("" : "+v"(xf[f][i])); }
            TSR(5);
            WL0; BAR;
            f32x4 t[4];
#pragma unroll
            for (int i = 0; i < 4; ++i) t[i] = __builtin_bit_cast(f32x4, ldsq((c & 7) * 64, i * 16));
            const f32x4 t4 = (t[0] + t[1]) + (t[2] + t[3]);
            const float mean = (t4[0] + t4[2]) * (1.0f / 1024.0f), var = fmaxf(rbf(fmaf(t4[1] + t4[3], 1.0f / 1024.0f, -mean * mean)), 0.f);
            const float inv = __builtin_amdgcn_rcpf(rbf(__builtin_amdgcn_sqrtf(rbf(var + 1e-5f)))), mi = -rbf(mean) * inv;
#pragma unroll
            for (int f = 0; f < 4; ++f) {
                v4u o;
#pragma unroll
                for (int i = 0; i < 4; ++i) xf[f][i] = __builtin_elementwise_fma(xf[f][i], f32x2{inv, inv}, f32x2{mi, mi});
#pragma unroll
                for (int i = 0; i < 4; ++i) o[i] = pack2(xf[f][i][0], xf[f][i][1]);
                st16(o, rs, vd + (O_XN - O_XIN) + f * 2048);
            }
            TSR(6);
            if (p1) { issue(IC<1 - b>{}, IC<RPA>{}, IC<24>{}, kn); TSR(7); wvm<25 - RPA>(); pre = true; } else second(IC<5>{});
            BAR;
            st4(0u, rs, lane0 < 2 && w == 0 ? na(nf ^ (FSIDE ? 0 : 8192), N_FX) + tile * 16 + (cu & 1) * 8 + lane0 * 4 : OOB, 0);
        }
        else if (p1) { issue(IC<1 - b>{}, IC<0>{}, IC<24>{}, kn); pre = true; }
        else second(IC<1>{});
        TSR(3); TSF(u, 0xFF);
    };
#pragma nounroll
    for (int k = cu < 4 ? cu : 252 + cu;;) {
        task(IC<0>{}, k); k = next(k);
        if (k >= 4 * NU) break;
        task(IC<1>{}, k); k = next(k);
        if (k >= 4 * NU) break;
    }
}
#ifndef OFFM
#define OFFM 0
#endif
#ifndef LSW
#define LSW 0
#endif
#ifndef PQD
#define PQD 0
#endif
#ifndef PQ0
#define PQ0 2048
#endif
#ifndef XFM
#define XFM 0
#endif
#ifndef DLY_A
#define DLY_A 450
#endif
#ifndef DLY_B
#define DLY_B 0
#endif
#ifndef DLY_1
#define DLY_1 1800
#endif
#ifndef DLY_G
#define DLY_G 1400
#endif
#ifndef DLY_D
#define DLY_D 1600
#endif
__global__ __launch_bounds__(256, 1) __attribute__((amdgpu_num_vgpr(NV))) void mega6(Args a) {
    asm volatile("" ::: "v255", "a255");
    const int w = __builtin_amdgcn_readfirstlane(threadIdx.x >> 6), b = blockIdx.x, xcd = b & 7, slot = b >> 3, l = (xcd >> 2) ^ LSW, idx = (slot & 7) * 4 + (xcd & 3), f = slot - 24;
    const int t0 = (int)__builtin_amdgcn_s_memrealtime(), xf = XFM ? 7 - xcd : xcd;
    TSW(127, 0);
    if (slot < 24 && l && (slot < 16 || idx >= LN_NOW)) pull_all(a, w, (slot >> 3) * 32 + idx - (slot < 16 ? 0 : LN_NOW), t0);
    if (PQ && f >= 0 && f < 4) { hold(t0, PQD); pull(a.Wq + PQ0 * 512, 8192 - PQ0, (f * 8 + xcd) * 4 + w, 128); }
    if (slot < 8) { hold(t0, l ? DLY_1 : DLY_A); role_A(a, w, l, idx, (xcd & 3) * 2); }
    else if (slot < 16) { hold(t0, l ? DLY_1 : DLY_B); role_B(a, w, l, idx, (xcd & 3) * 2); }
    else if (slot < 24) role_R(a, w, l, idx, t0);
    else if (f < (xf < 3 ? 6 : 4)) { hold(t0, DLY_G); if (!(OFFM & 1)) role_GU(a, w, xf < 3 ? 6 * xf + f : 6 + 4 * xf + f); }
    else if (xf < 3) {
        const int cu = xf + 3 * (f - 6), q = cu * 4 + w;
        use(ld4(mkrs(a.ws, WS_NEED), q < 2 * NVK + 2 ? prb(q >> 1, q & 1) + lane_id() * 32 : OOB, 0));
        hold(t0, DLY_D); if (!(OFFM & 4)) role_RED(a, w, cu);
        if (q == 0 && !(OFFM & 4)) vote_end(mkrs(a.ws, WS_NEED));
    }
    else {
        if (xf < 7) { vote(mkrs(a.ws, WS_NEED), w, (xf - 3) * 4 + f - 4, l, t0); }
        hold(t0, DLY_D); if (!(OFFM & 2)) role_DN(a, w, xf - 3, f - 4); }
}
extern "C" void kernel_launch(void* const* d_in, const int* in_sizes, int n_in, void* d_out, int out_size, void* d_ws, size_t ws_size, hipStream_t stream) {
    (void)in_sizes; (void)n_in; (void)out_size; (void)ws_size;
    (void)hipFuncSetAttribute((const void*)mega6, hipFuncAttributeMaxDynamicSharedMemorySize, 163840);
    d_ws = (void*)(((uintptr_t)d_ws + (2u << 20) - 1) & ~(uintptr_t)((2u << 20) - 1));
    Args a{(const u16*)d_in[0], (const u16*)d_in[1], (const u16*)d_in[2], (const u16*)d_in[3], (const u16*)d_in[4], (const u16*)d_in[5],
           (const u16*)d_in[8], (const u16*)d_in[9], (const u16*)d_in[10], (char*)d_ws, (u16*)d_out};
    mega6<<<256, 256, 163840, stream>>>(a);
}
```

```cpp
#include <hip/hip_runtime.h>
#include <stdint.h>

typedef unsigned short u16;
typedef unsigned long long u64;
typedef __attribute__((ext_vector_type(8))) __bf16 bf8;
typedef __attribute__((ext_vector_type(16))) __bf16 bf16v;
typedef __attribute__((ext_vector_type(4))) float f32x4;
typedef __attribute__((ext_vector_type(16))) float f32x16;
typedef __attribute__((ext_vector_type(2))) float f32x2;
typedef __attribute__((ext_vector_type(8))) unsigned v8u;
typedef __attribute__((ext_vector_type(4))) unsigned v4u;
typedef __attribute__((ext_vector_type(3))) unsigned v3u;
typedef __attribute__((ext_vector_type(2))) unsigned v2u;
typedef __amdgpu_buffer_rsrc_t rsrc_t;
typedef __attribute__((address_space(3))) char lds_char;
#define DEVI __device__ __forceinline__
#define LDSP(T, p) ((__attribute__((address_space(3))) T*)(p))

typedef __attribute__((ext_vector_type(2))) __bf16 bf16x2_t;
DEVI unsigned pack2(float lo, float hi) { f32x2 v = {lo, hi}; bf16x2_t b = __builtin_convertvector(v, bf16x2_t); return __builtin_bit_cast(unsigned, b); }
DEVI float bflo(unsigned u) { return __uint_as_float(u << 16); }
DEVI float bfhi(unsigned u) { return __uint_as_float(u & 0xffff0000u); }
DEVI float rbf(float x) { return bflo(pack2(x, x)); }
constexpr int NV = 112;
#define MR_(R, A_STR, V_STR, OUTS, INS, W) do { if constexpr ((R) < 256) asm volatile(A_STR : OUTS : INS, [lo] "n"(R), [hi] "n"((R) + (W) - 1)); \
                                                else asm volatile(V_STR : OUTS : INS, [lo] "n"((R) - 256 + NV), [hi] "n"((R) - 256 + NV + (W) - 1)); } while (0)
#define CM_ ,
template <int R> DEVI void mwr(unsigned x) { if constexpr (R < 256) asm volatile("v_accvgpr_write_b32 a[%1], %0" :: "v"(x), "n"(R)); else asm volatile("v_mov_b32 v[%1], %0" :: "v"(x), "n"(R - 256 + NV)); }
template <int R> DEVI void mwr4(v4u x) { mwr<R>(x[0]); mwr<R + 1>(x[1]); mwr<R + 2>(x[2]); mwr<R + 3>(x[3]); }
#define M16 "v_mfma_f32_16x16x32_bf16 "
#define M32 "v_mfma_f32_32x32x16_bf16 "
#define S16 "v_smfmac_f32_16x16x64_bf16 "
template <int R, bool Z> DEVI void mfA(f32x4& d, v4u b) {
    if constexpr (Z) MR_(R, M16 "%[d], a[%[lo]:%[hi]], %[b], 0", M16 "%[d], v[%[lo]:%[hi]], %[b], 0", [d] "=&v"(d), [b] "v"(b), 4);
    else MR_(R, M16 "%[d], a[%[lo]:%[hi]], %[b], %[d]", M16 "%[d], v[%[lo]:%[hi]], %[b], %[d]", [d] "+v"(d), [b] "v"(b), 4);
}
template <int R, bool Z> DEVI void mfB(f32x4& d, v4u a) {
    if constexpr (Z) MR_(R, M16 "%[d], %[a], a[%[lo]:%[hi]], 0", M16 "%[d], %[a], v[%[lo]:%[hi]], 0", [d] "=&v"(d), [a] "v"(a), 4);
    else MR_(R, M16 "%[d], %[a], a[%[lo]:%[hi]], %[d]", M16 "%[d], %[a], v[%[lo]:%[hi]], %[d]", [d] "+v"(d), [a] "v"(a), 4);
}
template <int R, bool Z> DEVI void mf32A(f32x16& d, v4u b) {
    if constexpr (Z) MR_(R, M32 "%[d], a[%[lo]:%[hi]], %[b], 0", M32 "%[d], v[%[lo]:%[hi]], %[b], 0", [d] "=&v"(d), [b] "v"(b), 4);
    else MR_(R, M32 "%[d], a[%[lo]:%[hi]], %[b], %[d]", M32 "%[d], v[%[lo]:%[hi]], %[b], %[d]", [d] "+v"(d), [b] "v"(b), 4);
}
template <bool Z> DEVI void mfv(f32x4& d, v4u a, v4u b) {
    if constexpr (Z) asm volatile(M16 "%0, %1, %2, 0" : "=&v"(d) : "v"(a), "v"(b)); else asm volatile(M16 "%0, %1, %2, %0" : "+v"(d) : "v"(a), "v"(b));
}
template <int R> DEVI void smf(f32x4& d, v4u a, int idx) { MR_(R, S16 "%[d], %[a], a[%[lo]:%[hi]], %[i]", S16 "%[d], %[a], v[%[lo]:%[hi]], %[i]", [d] "+v"(d), [a] "v"(a) CM_ [i] "v"(idx), 8); }
DEVI void smfv(f32x4& d, v4u a, v8u b, int idx) { asm volatile(S16 "%0, %1, %2, %3" : "+v"(d) : "v"(a), "v"(b), "v"(idx)); }
DEVI void zero4(f32x4& d) { d = f32x4{0.f, 0.f, 0.f, 0.f}; asm volatile("" : "+v"(d)); }
template <int N> DEVI void nop() { asm volatile("s_nop %0" :: "n"(N)); }
DEVI void fence(f32x4& d) { asm volatile("" : "+v"(d)); }
template <class... T> DEVI void fence(f32x4& d, T&... r) { fence(d); fence(r...); }
DEVI float selm(float a, float b, u64 m) { float r; asm("v_cndmask_b32 %0, %1, %2, %3" : "=v"(r) : "v"(a), "v"(b), "s"(m)); return r; }
DEVI int opqs(int x) { asm volatile("" : "+s"(x)); return x; }
DEVI v4u lo4(v8u v) { return __builtin_shufflevector(v, v, 0, 1, 2, 3); }
DEVI v4u hi4(v8u v) { return __builtin_shufflevector(v, v, 4, 5, 6, 7); }
DEVI v8u cat8(v4u a, v4u b) { return __builtin_shufflevector(a, b, 0, 1, 2, 3, 4, 5, 6, 7); }
constexpr int AUX_SC1 = 16;
DEVI v4u ld16(rsrc_t r, unsigned vo, unsigned so) { return __builtin_amdgcn_raw_buffer_load_b128(r, vo, so, AUX_SC1); }
DEVI v2u ld8(rsrc_t r, unsigned vo, unsigned so) { return __builtin_amdgcn_raw_buffer_load_b64(r, vo, so, AUX_SC1); }
DEVI unsigned ld4(rsrc_t r, unsigned vo, unsigned so) { return __builtin_amdgcn_raw_buffer_load_b32(r, vo, so, AUX_SC1); }
DEVI void st16(v4u v, rsrc_t r, unsigned vo) { __builtin_amdgcn_raw_buffer_store_b128(v, r, vo, 0, AUX_SC1); }
DEVI void st16s(v4u v, rsrc_t r, unsigned vo, unsigned so) { __builtin_amdgcn_raw_buffer_store_b128(v, r, vo, so, AUX_SC1); }
DEVI void st8(v2u v, rsrc_t r, unsigned vo, unsigned so) { __builtin_amdgcn_raw_buffer_store_b64(v, r, vo, so, AUX_SC1); }
DEVI void st4(unsigned v, rsrc_t r, unsigned vo, unsigned so) { __builtin_amdgcn_raw_buffer_store_b32(v, r, vo, so, AUX_SC1); }
#define CB asm volatile("" ::: "memory")
#define WL0 asm volatile("s_waitcnt lgkmcnt(0)" ::: "memory")
#define WV0 asm volatile("s_waitcnt vmcnt(0)" ::: "memory")
#define BAR do { __builtin_amdgcn_s_barrier(); CB; } while (0)
#define SB __builtin_amdgcn_sched_barrier(0)
DEVI float fadd(float a, float b) { float r; asm("v_add_f32 %0, %1, %2" : "=v"(r) : "v"(a), "v"(b)); return r; }
DEVI float vfma(float a, float b, float c) { float r; asm("v_fma_f32 %0, %1, %2, %3" : "=v"(r) : "v"(a), "v"(b), "v"(c)); return r; }
DEVI unsigned vxor(unsigned a, unsigned b) { unsigned r; asm volatile("v_xor_b32 %0, %1, %2" : "=v"(r) : "i"(b), "v"(a)); return r; }
DEVI int lane_id() { int r; asm volatile("v_mbcnt_lo_u32_b32 %0, -1, 0\n\tv_mbcnt_hi_u32_b32 %0, -1, %0" : "=v"(r)); __builtin_assume(r >= 0 && r < 64); return r; }
DEVI int opq(int x) { asm volatile("" : "+v"(x)); return x; }
DEVI float vmaxf(float a, float b) { float r; asm("v_max_f32 %0, %1, %2" : "=v"(r) : "v"(a), "v"(b)); return r; }
DEVI void swap32(float& a, float& b) { auto r = __builtin_amdgcn_permlane32_swap(__float_as_uint(a), __float_as_uint(b), false, false); a = __uint_as_float(r[0]); b = __uint_as_float(r[1]); }
DEVI void swap32u(unsigned& a, unsigned& b) { auto r = __builtin_amdgcn_permlane32_swap(a, b, false, false); a = r[0]; b = r[1]; }
DEVI void swap16u(unsigned& a, unsigned& b) { auto r = __builtin_amdgcn_permlane16_swap(a, b, false, false); a = r[0]; b = r[1]; }
DEVI float hadd(float a, float b) { swap32(a, b); return fadd(a, b); }
DEVI float red32(float x) { float y = x; swap32(x, y); return x + y; }
DEVI float red16(float x) { unsigned a = __float_as_uint(x), b = a; swap16u(a, b); return __uint_as_float(a) + __uint_as_float(b); }
DEVI float mx32(float x) { float y = x; swap32(x, y); return vmaxf(x, y); }
DEVI float mx16(float x) { unsigned a = __float_as_uint(x), b = a; swap16u(a, b); return vmaxf(__uint_as_float(a), __uint_as_float(b)); }
template <int N> struct IC { static constexpr int v = N; };
template <int I, int N, class F> DEVI void sfor(F&& f) { if constexpr (I < N) { f(IC<I>{}); sfor<I + 1, N>(f); } }
#ifndef NSEG
#define NSEG 64
#endif
#ifndef LAGD
#define LAGD 10
#endif
constexpr float SC_L2E = 0.08838834764831845f * 1.4426950408889634f;
constexpr unsigned SENT = 0xFFFFFFFFu;
#ifndef NUS_
#define NUS_ 8
#endif
#ifndef NRG_
#define NRG_ 8
#endif
#ifndef NC
#define NC 8
#endif
constexpr int NREP = 8, NUS = NUS_, NRG = NRG_, NKF = 5, NGU = 38;
constexpr unsigned XB = 4u << 20;
constexpr unsigned O_XIN = 0;
constexpr unsigned O_XN  = O_XIN + 2 * XB;
constexpr unsigned O_XM  = O_XN + 2 * XB;
constexpr unsigned O_H   = O_XM + 2 * XB;
constexpr unsigned O_D   = O_H + 2u * 64 * 224 * 1024;
constexpr unsigned O_N = O_D + 2u * 64 * NKF * 65536;
constexpr unsigned DV(unsigned o) { return ((o & ~8191u) << 1) | (o & 8191u); }
DEVI unsigned na(unsigned nb, unsigned o) { return (DV(o) + nb) ^ ((o >> 7) & 8192u); }
constexpr unsigned N_PM  = 0;
constexpr unsigned N_PMM = N_PM + NRG * 16384;
constexpr unsigned N_PA  = (1u << 20) - 16384;
constexpr unsigned N_MEM = 1u << 20;
constexpr unsigned N_WM  = 2u << 20;
constexpr unsigned N_PTX = 6u << 20;
constexpr unsigned N_U   = 10u << 20;
static_assert(NRG == 8 && NUS == 8 && NREP == 8 && O_N % (4u << 20) == 0, "");
constexpr unsigned O_FLG_END = O_N + DV(N_MEM + NREP * 16384);
constexpr unsigned N_FX  = N_PA - 8192;
constexpr unsigned N_PG  = N_FX - 8192;
constexpr unsigned N_PD  = N_PG + 40 * 128;
constexpr unsigned O_PRB = O_N + (512u << 10), O_CAL = O_N + (768u << 10);
constexpr unsigned O_SX  = O_N + DV(N_PG) - 2u * 64 * 32 * 256;
constexpr unsigned O_FLG = O_SX;
static_assert(O_PRB >= O_N + DV(N_PMM + NRG * 8192) && O_FLG >= O_CAL + 8192 && (O_FLG_END - O_FLG) % 65536 == 0, "");
constexpr unsigned OOB = 0x80000000u;
constexpr unsigned WS_NEED = O_N + DV(N_U + NUS * (2u << 20));
constexpr unsigned O_PROF = 240u << 20;
static_assert(WS_NEED <= (200u << 20), "workspace too large");

#ifdef DRY
#define WSZ(x) 0u
#else
#define WSZ(x) (x)
#endif
struct Args { const u16 *x, *mem0, *Wq, *Wk, *Wv, *Wo, *Wg, *Wu, *Wd; char* ws; u16* out; };
#ifndef SPIN_LOG
#define SPIN_LOG 18
#endif
constexpr unsigned SPIN_CAP = 1u << SPIN_LOG;
DEVI void spin_bail(unsigned& spins) {
    asm volatile("s_add_u32 %0, %0, 1\n\ts_cmp_lt_u32 %0, %1\n\ts_cbranch_scc1 1f\n\ts_endpgm\n1:" : "+s"(spins) : "n"(SPIN_CAP) : "scc");
}
template <int K> DEVI void wrl(unsigned& v, u64& t) { asm volatile("" : "+s"(t)); asm volatile("v_writelane_b32 %0, %1, %2" : "+v"(v) : "s"((unsigned)t), "n"(K)); }
#ifdef TS
#ifndef TSCLK
#define TSCLK __builtin_amdgcn_s_memrealtime
#endif
#define TSV(u, i, v) __builtin_amdgcn_raw_buffer_store_b32((unsigned)(v), mkrs(a.ws, 256u << 20), lane_id() == 0 ? O_PROF + ((blockIdx.x * 4 + w) * 128 + (u)) * 64 + (i) * 4 : OOB, 0, 0)
#define TSW(u, i) TSV(u, i, TSCLK())
#define TSC(u, i) TSV(u, i, __builtin_amdgcn_s_memtime())
#define TSD u64 tsr[16] = {}
#define TSR(k) tsr[k] = rclk()
#define TSK(k) asm volatile("s_memtime %0" : "=s"(tsr[k]))
#define TSF(u, m) do { unsigned tv_ = 0; WL0; sfor<0, 16>([&](auto K) { if constexpr (((m) >> K.v) & 1) wrl<K.v>(tv_, tsr[K.v]); }); \
    const int tl_ = lane_id(); __builtin_amdgcn_raw_buffer_store_b32(tv_, mkrs(a.ws, 256u << 20), (tl_ < 16 && (((m) >> tl_) & 1)) ? O_PROF + ((blockIdx.x * 4 + w) * 128 + (u)) * 64 + tl_ * 4 : OOB, 0, 0); } while (0)
#if TS == 4
#undef TSV
#undef TSR
#undef TSK
#define TSV(u, i, v)
#define TSR(k)
#define TSK(k)
#define FSK(G, O, D) if constexpr ((G) >= (O) && ((G) - (O)) % (D) == 0 && ((G) - (O)) / (D) < FN) asm volatile("s_memtime %0" : "=s"(tsr[((G) - (O)) / (D)]))
#define FSF(u) TSF(u, (1 << FN) - 1)
#define FSD TSD
#endif
#else
#define TSW(u, i)
#define TSC(u, i)
#define TSV(u, i, v)
#define TSD
#define TSR(k)
#define TSK(k)
#define TSF(u, m)
#endif
#if TS != 4
#define FSK(G, O, D)
#define FSF(u)
#define FSD
#endif
DEVI unsigned rslot(int s) { return (unsigned)(s & (NRG - 1)); }
DEVI u64 pend(v4u d) { return __builtin_amdgcn_uicmp(max(d[0], d[2]), SENT, 32); }
DEVI u64 pend2(v2u d) { return __builtin_amdgcn_uicmp(d[0], SENT, 32); }
DEVI u64 pend1(unsigned d) { return __builtin_amdgcn_uicmp(d, SENT, 32); }
DEVI unsigned nosent(unsigned u) { return min(u, 0xFFFFFFFEu); }
#ifndef WPOL
#define WPOL ""
#endif
template <int OFF, bool SC1> DEVI void dma1(rsrc_t rs, unsigned vo, unsigned so) {
    if constexpr (SC1) asm volatile("buffer_load_dwordx4 %0, %1, %2 offen offset:%3 sc1 lds" :: "v"(vo), "s"(rs), "s"(so), "n"(OFF) : "memory");
    else asm volatile("buffer_load_dwordx4 %0, %1, %2 offen offset:%3 " WPOL " lds" :: "v"(vo), "s"(rs), "s"(so), "n"(OFF) : "memory");
}
DEVI void setm0(unsigned v) { asm volatile("s_mov_b32 m0, %0\n\ts_nop 0" :: "s"(v) : "memory"); }
DEVI void dma4(unsigned lds, rsrc_t rs, unsigned vo, unsigned so) { setm0(lds); dma1<0, true>(rs, vo, so); dma1<1024, true>(rs, vo, so); dma1<2048, true>(rs, vo, so); dma1<3072, true>(rs, vo, so); }
DEVI void dmaw(unsigned lds, rsrc_t rs, unsigned vo, unsigned so) { setm0(lds); asm volatile("buffer_load_dword %0, %1, %2 offen sc1 lds" :: "v"(vo), "s"(rs), "s"(so) : "memory"); }
template <int N> DEVI void wvm() { asm volatile("s_waitcnt vmcnt(%0)" :: "n"(N) : "memory"); }
template <int N> DEVI void wlg() { asm volatile("s_waitcnt lgkmcnt(%0)" :: "n"(N) : "memory"); }
DEVI void dma4p(u64 c0, u64 c1, u64 c2, u64 c3, rsrc_t rs, unsigned vo, unsigned so) {
    asm volatile(
        "s_cmp_eq_u64 %[c0], 0\n\ts_cbranch_scc1 1f\n\t"
        "buffer_load_dwordx4 %[vo], %[rs], %[so] offen sc1 lds\n"
        "1:\n\ts_cmp_eq_u64 %[c1], 0\n\ts_cbranch_scc1 2f\n\t"
        "buffer_load_dwordx4 %[vo], %[rs], %[so] offen offset:1024 sc1 lds\n"
        "2:\n\ts_cmp_eq_u64 %[c2], 0\n\ts_cbranch_scc1 3f\n\t"
        "buffer_load_dwordx4 %[vo], %[rs], %[so] offen offset:2048 sc1 lds\n"
        "3:\n\ts_cmp_eq_u64 %[c3], 0\n\ts_cbranch_scc1 4f\n\t"
        "buffer_load_dwordx4 %[vo], %[rs], %[so] offen offset:3072 sc1 lds\n"
        "4:"
        :: [c0] "s"(c0), [c1] "s"(c1), [c2] "s"(c2), [c3] "s"(c3), [vo] "v"(vo), [rs] "s"(rs), [so] "s"(so)
        : "memory", "scc");
}
#ifndef PSL
#define PSL 8
#endif
DEVI void wait_prog(rsrc_t rs, unsigned vo, unsigned so, int need) {
    unsigned spins = 0;
#pragma nounroll
    for (;;) {
        CB;
        const int v = (int)ld4(rs, vo, so);
        if (__builtin_amdgcn_sicmp(v, need, 40) == 0) return;
        spin_bail(spins);
        __builtin_amdgcn_s_sleep(PSL);
    }
}

typedef __attribute__((ext_vector_type(16))) int v16i;
typedef __attribute__((ext_vector_type(16))) unsigned v16u;
#ifdef TS
constexpr unsigned RSZ = 256u << 20;
#else
constexpr unsigned RSZ = WS_NEED;
#endif
DEVI u64 rclk() { u64 t; asm volatile("s_memrealtime %0" : "=s"(t)); return t; }
DEVI unsigned tsv(u64& t) { asm volatile("s_waitcnt lgkmcnt(0)" : "+s"(t)); return (unsigned)t; }
DEVI unsigned stamp(u64& t, int tag, int lead) { asm volatile("" : "+s"(t)); return (unsigned)tag << 24 | (((unsigned)t + lead) & 0xFFFFFFu); }
#ifndef SXTH
#define SXTH 20
#endif
DEVI int left(int t) { return (int)((unsigned)(t - (int)__builtin_amdgcn_s_memrealtime()) << 8); }
DEVI void hold(int t0, int d) { for (int n = 0; n < 4096 && (int)__builtin_amdgcn_s_memrealtime() - t0 < d; ++n) __builtin_amdgcn_s_sleep(4); }
DEVI int smin(int a, int b) { int r; asm("s_min_i32 %0, %1, %2" : "=s"(r) : "s"(a), "s"(b) : "scc"); return r; }
DEVI int smax(int a, int b) { int r; asm("s_max_i32 %0, %1, %2" : "=s"(r) : "s"(a), "s"(b) : "scc"); return r; }
DEVI void smin8x2(const v16i& f, int& m0, int& m1) {
    asm("s_min_i32 %0, %2, %3\n\ts_min_i32 %1, %10, %11\n\ts_min_i32 %0, %0, %4\n\ts_min_i32 %1, %1, %12\n\ts_min_i32 %0, %0, %5\n\ts_min_i32 %1, %1, %13\n\ts_min_i32 %0, %0, %6\n\ts_min_i32 %1, %1, %14\n\t"
        "s_min_i32 %0, %0, %7\n\ts_min_i32 %1, %1, %15\n\ts_min_i32 %0, %0, %8\n\ts_min_i32 %1, %1, %16\n\ts_min_i32 %0, %0, %9\n\ts_min_i32 %1, %1, %17"
        : "=&s"(m0), "=&s"(m1) : "s"(f[0]), "s"(f[1]), "s"(f[2]), "s"(f[3]), "s"(f[4]), "s"(f[5]), "s"(f[6]), "s"(f[7]),
          "s"(f[8]), "s"(f[9]), "s"(f[10]), "s"(f[11]), "s"(f[12]), "s"(f[13]), "s"(f[14]), "s"(f[15]) : "scc");
}
DEVI void smax8x2(const v16i& f, int& m0, int& m1) {
    asm("s_max_i32 %0, %2, %3\n\ts_max_i32 %1, %10, %11\n\ts_max_i32 %0, %0, %4\n\ts_max_i32 %1, %1, %12\n\ts_max_i32 %0, %0, %5\n\ts_max_i32 %1, %1, %13\n\ts_max_i32 %0, %0, %6\n\ts_max_i32 %1, %1, %14\n\t"
        "s_max_i32 %0, %0, %7\n\ts_max_i32 %1, %1, %15\n\ts_max_i32 %0, %0, %8\n\ts_max_i32 %1, %1, %16\n\ts_max_i32 %0, %0, %9\n\ts_max_i32 %1, %1, %17"
        : "=&s"(m0), "=&s"(m1) : "s"(f[0]), "s"(f[1]), "s"(f[2]), "s"(f[3]), "s"(f[4]), "s"(f[5]), "s"(f[6]), "s"(f[7]),
          "s"(f[8]), "s"(f[9]), "s"(f[10]), "s"(f[11]), "s"(f[12]), "s"(f[13]), "s"(f[14]), "s"(f[15]) : "scc");
}
#ifndef NVK
#define NVK 8
#endif
#ifndef DLY_V
#define DLY_V 500
#endif
#ifndef PSV
#define PSV 12
#endif
#ifndef DLY_P
#define DLY_P 150
#endif
DEVI unsigned prb(int k, int b) { return O_PRB + k * 16384 + b * 8192 + (k & 1) * 4096; }
DEVI void vote(rsrc_t rs, int w, int v, int far, int t0s) {
    const int lane = lane_id();
    constexpr unsigned N = (O_FLG_END - O_FLG) / 65536;
#pragma nounroll
    for (unsigned q = 0; q < N; ++q) st16(v4u{SENT, SENT, SENT, SENT}, rs, O_FLG + ((v * 4 + w) * N + q) * 1024 + lane * 16);
    const v2u e = ld8(rs, O_CAL + (lane & 31) * 256 + 128, 0);
    static_assert(NVK % 4 == 0 && NVK < 12, "");
    int best[2] = {1 << 30, 1 << 30};
    hold(t0s, DLY_P);
#pragma nounroll
    for (int k = 0; k <= NVK / 4; ++k)
#pragma unroll
        for (int b = 0; b < 2; ++b) {
            u64 t0, t1; int d;
            asm volatile("s_memtime %0\n\ts_waitcnt lgkmcnt(0)\n\ts_buffer_load_dword %2, %3, %4 glc\n\ts_waitcnt lgkmcnt(0)\n\ts_memtime %1\n\ts_waitcnt lgkmcnt(0)"
                         : "=&s"(t0), "=&s"(t1), "=&s"(d) : "s"(rs), "s"(prb(k ? 4 * k - w : 0, b) + v * 128) : "memory");
            if (k) best[b] = smin(best[b], (int)(t1 - t0));
        }
    *LDSP(int, (lane & 1) * 16 + w * 4) = best[lane & 1];
    WV0; WL0; BAR;
#pragma unroll
    for (int b = 0; b < 2; ++b) { const v4u t = *LDSP(const v4u, b * 16); best[b] = min(min(t[0], t[1]), min(t[2], t[3])); }
    st8(v2u{((e[0] + 2) & ~1u) | ((best[1] < best[0]) ^ far), e[1] + 1}, rs, lane < 32 && w == 0 ? O_CAL + lane * 256 + v * 8 : OOB, 0);
}
DEVI void vote_end(rsrc_t rs) {
    const unsigned o = O_CAL + (lane_id() & 31) * 256 + 128;
    const v2u e = ld8(rs, o, 0);
    st8(v2u{e[0] + 2, e[1] + 1}, rs, o, 0);
}
DEVI unsigned side(rsrc_t rs, int l) {
    unsigned spins = 0;
#pragma nounroll
    for (;;) {
        v16u f[2]; v2u e;
        const unsigned o = O_CAL + (blockIdx.x & 31) * 256;
        asm volatile("s_buffer_load_dwordx16 %0, %3, %4 glc\n\ts_buffer_load_dwordx16 %1, %3, %5 glc\n\ts_buffer_load_dwordx2 %2, %3, %6 glc\n\ts_waitcnt lgkmcnt(0)"
                     : "=&s"(f[0]), "=&s"(f[1]), "=&s"(e) : "s"(rs), "s"(o), "s"(o + 64), "s"(o + 128) : "memory");
        unsigned bad = 0, sum = 0;
#pragma unroll
        for (int i = 0; i < 16; ++i) { const unsigned x = f[i >> 3][2 * i & 15]; bad |= ((x ^ (e[0] + 2)) >> 1) | (f[i >> 3][(2 * i & 15) + 1] ^ (e[1] + 1)); sum += x & 1; }
#ifdef CSIDE
        sum = CSIDE * 16;
#endif
        if (bad == 0) return opqs(O_N + ((l ^ (sum > 8)) & 1) * 8192);
        spin_bail(spins);
        __builtin_amdgcn_s_sleep(PSV);
    }
}
#ifndef PSH
#define PSH 2
#endif
DEVI int wait_hint(rsrc_t rs, unsigned so, unsigned vo, int tag) {
#pragma nounroll
    for (int n = 0;; ++n) {
        v16i f;
        asm volatile("s_buffer_load_dwordx16 %0, %1, %2 glc\n\ts_waitcnt lgkmcnt(0)" : "=s"(f) : "s"(rs), "s"(so) : "memory");
        int m0, m1;
        smin8x2(f, m0, m1);
        if (smin(m0, m1) >= tag << 24) { smax8x2(f, m0, m1); return smax(m0, m1); }
        if (n == 40) { wait_prog(rs, vo, 0, tag << 24); n = 0; }
        __builtin_amdgcn_s_sleep(PSH);
    }
}
template <class WM> constexpr int widx(int j, int t) {
    int n = 0;
    for (int q = 0; q < j * WM::NP + t; ++q) n += WM::kind(q / WM::NP, q % WM::NP) == WM::kind(j, t);
    return n;
}
template <class WM> constexpr int lds_tile(int t) { for (int j = 0; j < WM::NT; ++j) if (WM::kind(j, t) == 1) return j; return -1; }
DEVI v4u ldsq(unsigned p, int off) { return *LDSP(const v4u, LDSP(char, p) + off); }
DEVI void stsq(unsigned p, int off, v4u v) { *LDSP(v4u, LDSP(char, p) + off) = v; }
DEVI v8u ldpair(unsigned wl, int slot) { return cat8(ldsq(wl, slot * 2048), ldsq(wl, slot * 2048 + 1024)); }
DEVI rsrc_t mkrs(const void* p, unsigned bytes) { return __builtin_amdgcn_make_buffer_rsrc((void*)p, 0, (int)bytes, 0x00020000); }
#ifndef NPL
#define NPL 72
#endif
#ifndef LNN
#define LNN 8
#endif
constexpr int LN_NOW = LNN, NP0 = NPL * 4;
template <int SL = 0> DEVI void pull(const void* p, int n, int q, int nq) {
    const rsrc_t r = mkrs(p, n * 1024);
    const unsigned vo = lane_id() * 16;
#pragma nounroll
    for (; q < n; q += nq) { asm volatile("buffer_load_dwordx4 a[0:3], %0, %1, %2 offen" :: "v"(vo), "s"(r), "s"(q * 1024) : "memory"); if (SL) __builtin_amdgcn_s_sleep(SL); }
}
#ifndef PQ
#define PQ 0
#endif
#ifndef DLY_F
#define DLY_F 1000
#endif
#ifndef PFS
#define PFS 5
#endif
DEVI void pull_all(const Args& a, int w, int q, int t0) {
    q = q * 4 + w;
    if (q < NP0) {
    pull(a.Wv, 8192, q, NP0); pull(a.Wo, 8192, q, NP0); if (!PQ) pull(a.Wq, 8192, q, NP0); pull(a.Wk, 8192, q, NP0);
    }
    if (q >= 256) return;
    hold(t0, DLY_F);
    pull<PFS>(q & 1 ? a.Wg : a.Wu, 7168, q >> 1, 128); pull<PFS>(a.Wd, 7168, q, 256);
}
template <class WM, int j, int ks, int O0, int O1> DEVI void put_frag(unsigned ta, unsigned wl, v8u* Wc) {
    constexpr int t = ks >> 1, f = ks & 1, k = WM::kind(j, t), ix = widx<WM>(j, t);
    if constexpr (k == 0) {
        constexpr int R = WM::RB + 8 * ix + 4 * f;
        if constexpr (R < 256) asm volatile("ds_read_b64_tr_b16 a[%1:%2], %0 offset:%5\n\tds_read_b64_tr_b16 a[%3:%4], %0 offset:%6" :: "v"(ta), "n"(R), "n"(R + 1), "n"(R + 2), "n"(R + 3), "n"(O0), "n"(O1) : "memory");
        else asm volatile("ds_read_b64_tr_b16 v[%1:%2], %0 offset:%5\n\tds_read_b64_tr_b16 v[%3:%4], %0 offset:%6" :: "v"(ta), "n"(R - 256 + NV), "n"(R - 255 + NV), "n"(R - 254 + NV), "n"(R - 253 + NV), "n"(O0), "n"(O1) : "memory");
    } else {
        v2u p, q;
        asm volatile("ds_read_b64_tr_b16 %0, %2 offset:%3\n\tds_read_b64_tr_b16 %1, %2 offset:%4\n\ts_waitcnt lgkmcnt(0)" : "=&v"(p), "=&v"(q) : "v"(ta), "n"(O0), "n"(O1) : "memory");
        const v4u F = v4u{p[0], p[1], q[0], q[1]};
        if constexpr (k == 1) stsq(wl, (WM::LB + ix) * 2048 + f * 1024, F);
        else if constexpr (f == 0) Wc[WM::CBI + ix] = cat8(F, F);
        else Wc[WM::CBI + ix] = cat8(lo4(Wc[WM::CBI + ix]), F);
    }
}
template <int NB, int DEPTH, int NI, bool SHARED = false, int SL = DEPTH, class ISSUE, class PUT>
DEVI void load_blocks(ISSUE&& issue, PUT&& put) {
    sfor<0, DEPTH - 1>([&](auto B) { issue(B, IC<B.v % SL>{}); });
    sfor<0, NB>([&](auto B) {
        constexpr int b = B.v, left = NB - 1 - b < DEPTH - 2 ? NB - 1 - b : DEPTH - 2;
        wvm<NI * left>();
        wlg<0>();
        if constexpr (SHARED) BAR;
        if constexpr (b + DEPTH - 1 < NB) issue(IC<b + DEPTH - 1>{}, IC<(b + DEPTH - 1) % SL>{});
        put(B, IC<b % SL>{});
    });
    wlg<0>();
    if constexpr (SHARED) BAR;
}
DEVI void dma_blk(unsigned lds, rsrc_t rw, unsigned vo, unsigned so, unsigned rowb) { setm0(lds); dma1<0, false>(rw, vo, so); setm0(lds + 1024); dma1<0, false>(rw, vo, so + 16 * rowb); }
template <class WM, int NKS, int K0, int K1, int RTS, int LATE1, class FILL>
DEVI void gemm_pass(unsigned xa, unsigned wl, const v8u* Wc, v4u (&x)[2][3], f32x4 (&acc)[WM::NT][2], FILL&& fill) {
    auto rd = [&](auto KS, auto Q) {
        constexpr int ks = KS.v, q = Q.v, jl = lds_tile<WM>(ks >> 1), il = widx<WM>(jl < 0 ? 0 : jl, ks >> 1);
        if constexpr (q == 0 && jl >= 0) x[ks & 1][2] = ldsq(wl, (WM::LB + il) * 2048 + (ks & 1) * 1024);
        if constexpr (q == 1) x[ks & 1][1] = ldsq(xa, RTS + ks * 1024);
        if constexpr (q == 2) x[ks & 1][0] = ldsq(xa, ks * 1024);
    };
    sfor<K0 * 2 * WM::NT, K1 * 2 * WM::NT>([&](auto N) {
        constexpr int ks = N.v / (2 * WM::NT), n = N.v % (2 * WM::NT), j = n >> 1, rt = n & 1, t = ks >> 1, f = ks & 1, k = WM::kind(j, t), ix = widx<WM>(j, t);
        fill(IC<ks>{}, IC<n>{});
        SB;
        const v4u xv = x[ks & 1][rt];
        if constexpr (k == 0) mfA<WM::RB + 8 * ix + 4 * f, ks == 0>(acc[j][rt], xv);
        else if constexpr (k == 1) mfv<ks == 0>(acc[j][rt], x[ks & 1][2], xv);
        else mfv<ks == 0>(acc[j][rt], f ? hi4(Wc[WM::CBI + ix]) : lo4(Wc[WM::CBI + ix]), xv);
        if constexpr (ks + 1 < NKS) rd(IC<ks + 1>{}, IC<n - (ks == 1 ? LATE1 : 0)>{});
        SB;
    });
}
template <class WM> constexpr int next_lds(int n) { for (; n < 16 * WM::NT; ++n) if (WM::kind(2 * (n >> 5) + (n & 1), (n >> 1) & 15) == 1) return n; return -1; }
template <class WM> constexpr int lds_cnt(int n) { int c = 0; for (int q = next_lds<WM>(0); q >= 0 && q < n; q = next_lds<WM>(q + 1)) ++c; return c; }
template <class WM> constexpr int lds_nth(int k) { int q = next_lds<WM>(0); for (; k > 0 && q >= 0; --k) q = next_lds<WM>(q + 1); return q; }
template <class WM, int K, int NB> DEVI void pair_next(v8u (&pb)[NB], unsigned wl) {
    constexpr int n = lds_nth<WM>(K);
    if constexpr (n >= 0) { constexpr int ix = widx<WM>(2 * (n >> 5) + (n & 1), (n >> 1) & 15); pb[K % NB] = ldpair(wl, WM::LB + ix); }
}
template <class WM, int MO, int MR, int NB, class FILL>
DEVI void spass(unsigned ma, unsigned wl, const v8u* Wc, f32x4* e, v4u (&m)[MR], v8u (&pb)[NB], int sidx, FILL&& fill) {
    sfor<0, 16 * WM::NT>([&](auto N) {
        constexpr int n = N.v, t = (n >> 1) & 15, j = 2 * (n >> 5) + (n & 1), k = WM::kind(j, t), ix = widx<WM>(j, t), tn = (t + MR - 1) & 15, r = (n >> 1) % MR;
        fill(N);
        SB;
        if constexpr (k == 0) smf<WM::RB + 8 * ix>(e[j], m[r], sidx);
        else if constexpr (k == 1) { constexpr int q = lds_cnt<WM>(n); smfv(e[j], m[r], pb[q % NB], sidx); pair_next<WM, q + NB>(pb, wl); }
        else smfv(e[j], m[r], Wc[WM::CBI + ix], sidx);
        if constexpr ((n & 1) == 0 && n + 2 * MR - 2 < 16 * WM::NT) m[(r + MR - 1) % MR] = ldsq(ma, MO + (tn >> 3) * 32768 + (tn & 7) * 1024);
        SB;
    });
}
typedef __attribute__((ext_vector_type(4))) short s16x4;
DEVI v2u ldtr(unsigned p, int off) { return __builtin_bit_cast(v2u, __builtin_amdgcn_ds_read_tr16_b64_v4i16(LDSP(s16x4, LDSP(char, p) + off))); }
DEVI v4u ldtr2(unsigned p, int off) { const v2u a = ldtr(p, off), b = ldtr(p, off + 256); return v4u{a[0], a[1], b[0], b[1]}; }
DEVI void sts1(unsigned p, int off, unsigned v) { *LDSP(unsigned, LDSP(char, p) + off) = v; }
DEVI unsigned lds1(unsigned p, int off) { return *LDSP(const unsigned, LDSP(char, p) + off); }
DEVI void use(unsigned v) { asm volatile("" :: "v"(v) : "memory"); }
DEVI int mk_sidx(int lane) { return opq(0x4444 + (lane & 2) * 0x5555); }
DEVI int crow(int r, int hb) { return 4 * (r >> 1) + 2 * hb + (r & 1); }
#define A_LDS 0x28a0114002004410ull
#define A_VAR 0x0000000400020001ull
#define A_LOOP \
"s_brev_b32 s99, 1\ns_brev_b32 s98, 1\ns_brev_b32 s97, 1\ns_brev_b32 s96, 1\n" \
"s_branch 4f\n.p2align 6\n1:\ns_waitcnt lgkmcnt(0)\n" \
"v_mfma_f32_16x16x32_bf16 v[44:47], v[76:79], v[80:83], v[44:47]\nds_read_b128 v[36:39], v25 offset:3072\nds_read_b128 v[40:43], v24 offset:21504\nv_mfma_f32_16x16x32_bf16 v[48:51], v[76:79], v[32:35], v[48:51]\n" \
"ds_read_b128 v[84:87], v24 offset:54272\nv_bfe_u32 v31, v24, 6, 3\nv_mfma_f32_16x16x32_bf16 v[52:55], a[160:163], v[80:83], v[52:55]\ns_mov_b32 m0, s50\n" \
"s_add_u32 s99, s30, 2\nv_lshlrev_b32 v31, 2, v31\nv_mfma_f32_16x16x32_bf16 v[56:59], a[160:163], v[32:35], v[56:59]\ns_min_u32 s99, s99, 63\n" \
"s_lshl_b32 s99, s99, 5\ns_add_u32 s99, s99, s48\nv_mfma_f32_16x16x32_bf16 v[60:63], v[120:123], v[80:83], v[60:63]\ns_and_b32 s94, s30, 7\n" \
"s_lshl_b32 s94, s94, 18\ns_add_u32 s94, s94, s33\nv_mfma_f32_16x16x32_bf16 v[64:67], v[120:123], v[32:35], v[64:67]\ns_add_u32 s31, s31, 0x10000\n" \
"v_mfma_f32_16x16x32_bf16 v[68:71], v[224:227], v[80:83], v[68:71]\ns_waitcnt lgkmcnt(1)\nv_mfma_f32_16x16x32_bf16 v[72:75], v[224:227], v[32:35], v[72:75]\ns_waitcnt lgkmcnt(0)\n" \
"ds_read_b128 v[32:35], v24 offset:22528\nv_mfma_f32_16x16x32_bf16 v[44:47], v[36:39], v[40:43], v[44:47]\nds_read_b128 v[76:79], v24 offset:55296\nv_mfma_f32_16x16x32_bf16 v[48:51], v[36:39], v[84:87], v[48:51]\n" \
"v_mfma_f32_16x16x32_bf16 v[52:55], a[164:167], v[40:43], v[52:55]\nds_read_b128 v[36:39], v25 offset:18432\nv_mfma_f32_16x16x32_bf16 v[56:59], a[164:167], v[84:87], v[56:59]\nv_mfma_f32_16x16x32_bf16 v[60:63], v[124:127], v[40:43], v[60:63]\n" \
"v_mfma_f32_16x16x32_bf16 v[64:67], v[124:127], v[84:87], v[64:67]\nv_mfma_f32_16x16x32_bf16 v[68:71], v[228:231], v[40:43], v[68:71]\ns_waitcnt lgkmcnt(1)\nv_mfma_f32_16x16x32_bf16 v[72:75], v[228:231], v[84:87], v[72:75]\n" \
"ds_read_b128 v[40:43], v24 offset:23552\nv_mfma_f32_16x16x32_bf16 v[44:47], a[64:67], v[32:35], v[44:47]\nds_read_b128 v[80:83], v24 offset:56320\nv_mfma_f32_16x16x32_bf16 v[48:51], a[64:67], v[76:79], v[48:51]\n" \
"v_mfma_f32_16x16x32_bf16 v[52:55], a[168:171], v[32:35], v[52:55]\nds_read_b128 v[84:87], v25 offset:19456\nv_mfma_f32_16x16x32_bf16 v[56:59], a[168:171], v[76:79], v[56:59]\nv_mfma_f32_16x16x32_bf16 v[60:63], v[128:131], v[32:35], v[60:63]\n" \
"s_waitcnt lgkmcnt(3)\nv_mfma_f32_16x16x32_bf16 v[64:67], v[128:131], v[76:79], v[64:67]\nv_mfma_f32_16x16x32_bf16 v[68:71], v[36:39], v[32:35], v[68:71]\ns_waitcnt vmcnt(8) lgkmcnt(1)\n" \
"s_barrier\nv_mfma_f32_16x16x32_bf16 v[72:75], v[36:39], v[76:79], v[72:75]\nds_read_b128 v[32:35], v24 offset:24576\nv_mfma_f32_16x16x32_bf16 v[44:47], a[68:71], v[40:43], v[44:47]\n" \
"ds_read_b128 v[36:39], v24 offset:57344\nv_mfma_f32_16x16x32_bf16 v[48:51], a[68:71], v[80:83], v[48:51]\nds_read_b128 v[76:79], v25 offset:12288\nv_mfma_f32_16x16x32_bf16 v[52:55], a[172:175], v[40:43], v[52:55]\n" \
"buffer_load_dword v31, s[8:11], s99 offen offset:0 sc1 lds\nv_mfma_f32_16x16x32_bf16 v[56:59], a[172:175], v[80:83], v[56:59]\ns_add_u32 m0, s32, 0x4000\nv_mfma_f32_16x16x32_bf16 v[60:63], v[132:135], v[40:43], v[60:63]\n" \
"s_waitcnt lgkmcnt(3)\nv_mfma_f32_16x16x32_bf16 v[64:67], v[132:135], v[80:83], v[64:67]\nv_mfma_f32_16x16x32_bf16 v[68:71], v[84:87], v[40:43], v[68:71]\ns_waitcnt lgkmcnt(1)\n" \
"v_mfma_f32_16x16x32_bf16 v[72:75], v[84:87], v[80:83], v[72:75]\nds_read_b128 v[40:43], v24 offset:25600\nv_mfma_f32_16x16x32_bf16 v[44:47], a[72:75], v[32:35], v[44:47]\nds_read_b128 v[80:83], v24 offset:58368\n" \
"v_mfma_f32_16x16x32_bf16 v[48:51], a[72:75], v[36:39], v[48:51]\nds_read_b128 v[84:87], v25 offset:13312\nv_mfma_f32_16x16x32_bf16 v[52:55], a[176:179], v[32:35], v[52:55]\ns_waitcnt lgkmcnt(3)\n" \
"v_mfma_f32_16x16x32_bf16 v[56:59], a[176:179], v[36:39], v[56:59]\nv_mfma_f32_16x16x32_bf16 v[60:63], v[76:79], v[32:35], v[60:63]\nv_mfma_f32_16x16x32_bf16 v[64:67], v[76:79], v[36:39], v[64:67]\nv_mfma_f32_16x16x32_bf16 v[68:71], v[232:235], v[32:35], v[68:71]\n" \
"s_waitcnt lgkmcnt(1)\nv_mfma_f32_16x16x32_bf16 v[72:75], v[232:235], v[36:39], v[72:75]\nbuffer_load_dwordx4 v24, s[8:11], s94 offen offset:0 sc1 lds\nv_mfma_f32_16x16x32_bf16 v[44:47], a[76:79], v[40:43], v[44:47]\n" \
"ds_read_b128 v[32:35], v24 offset:26624\nv_mfma_f32_16x16x32_bf16 v[48:51], a[76:79], v[80:83], v[48:51]\nds_read_b128 v[36:39], v24 offset:59392\nv_mfma_f32_16x16x32_bf16 v[52:55], a[180:183], v[40:43], v[52:55]\n" \
"s_waitcnt lgkmcnt(2)\nbuffer_load_dwordx4 v24, s[8:11], s94 offen offset:1024 sc1 lds\nv_mfma_f32_16x16x32_bf16 v[56:59], a[180:183], v[80:83], v[56:59]\nbuffer_load_dwordx4 v24, s[8:11], s94 offen offset:2048 sc1 lds\n" \
"v_mfma_f32_16x16x32_bf16 v[60:63], v[84:87], v[40:43], v[60:63]\nbuffer_load_dwordx4 v24, s[8:11], s94 offen offset:3072 sc1 lds\nv_mfma_f32_16x16x32_bf16 v[64:67], v[84:87], v[80:83], v[64:67]\nds_read_b128 v[76:79], v25 offset:20480\n" \
"s_and_b32 s93, s30, 15\nv_mfma_f32_16x16x32_bf16 v[68:71], v[236:239], v[40:43], v[68:71]\ns_lshl_b32 s92, s93, 17\ns_add_u32 s92, s92, s35\n" \
"s_and_b32 s93, s93, 7\nv_mfma_f32_16x16x32_bf16 v[72:75], v[236:239], v[80:83], v[72:75]\ns_waitcnt lgkmcnt(1)\nds_read_b128 v[40:43], v24 offset:27648\n" \
"v_mfma_f32_16x16x32_bf16 v[44:47], a[80:83], v[32:35], v[44:47]\nds_read_b128 v[80:83], v24 offset:60416\ns_lshr_b32 s95, s30, s41\nv_mfma_f32_16x16x32_bf16 v[48:51], a[80:83], v[36:39], v[48:51]\n" \
"s_and_b32 s95, s95, 1\ns_lshl_b32 s93, s93, 14\ns_lshl1_add_u32 s99, s93, s34\nv_mfma_f32_16x16x32_bf16 v[52:55], a[184:187], v[32:35], v[52:55]\n" \
"ds_read_b128 v[84:87], v25 offset:21504\ns_or_b32 s98, s92, s36\nv_mfma_f32_16x16x32_bf16 v[56:59], a[184:187], v[36:39], v[56:59]\ns_or_b32 s97, s92, s37\n" \
"s_and_b32 s92, s92, s39\ns_andn2_b32 s93, s93, s39\nv_mfma_f32_16x16x32_bf16 v[60:63], v[136:139], v[32:35], v[60:63]\nds_read_b128 v[88:91], v25 offset:4096\n" \
"s_add_u32 s96, s92, s38\nv_mfma_f32_16x16x32_bf16 v[64:67], v[136:139], v[36:39], v[64:67]\ns_waitcnt lgkmcnt(4)\ns_add_u32 s96, s96, s93\n" \
"s_add_u32 s30, s30, 1\nv_mfma_f32_16x16x32_bf16 v[68:71], v[76:79], v[32:35], v[68:71]\ns_lshl_b32 s93, s30, 24\ns_cmp_le_u32 s30, 64\n" \
"s_cselect_b32 s51, s14, 0\nv_mfma_f32_16x16x32_bf16 v[72:75], v[76:79], v[36:39], v[72:75]\ns_waitcnt lgkmcnt(2)\nds_read_b128 v[32:35], v24 offset:28672\n" \
"v_mfma_f32_16x16x32_bf16 v[44:47], a[84:87], v[40:43], v[44:47]\nds_read_b128 v[36:39], v24 offset:61440\nv_mfma_f32_16x16x32_bf16 v[48:51], a[84:87], v[80:83], v[48:51]\nv_mfma_f32_16x16x32_bf16 v[52:55], a[188:191], v[40:43], v[52:55]\n" \
"v_mfma_f32_16x16x32_bf16 v[56:59], a[188:191], v[80:83], v[56:59]\nv_mfma_f32_16x16x32_bf16 v[60:63], v[140:143], v[40:43], v[60:63]\ns_waitcnt lgkmcnt(2)\nds_read_b128 v[76:79], v25 offset:5120\n" \
"v_mfma_f32_16x16x32_bf16 v[64:67], v[140:143], v[80:83], v[64:67]\nv_mfma_f32_16x16x32_bf16 v[68:71], v[84:87], v[40:43], v[68:71]\ns_waitcnt lgkmcnt(1)\nv_mfma_f32_16x16x32_bf16 v[72:75], v[84:87], v[80:83], v[72:75]\n" \
"ds_read_b128 v[40:43], v24 offset:29696\nv_mfma_f32_16x16x32_bf16 v[44:47], v[88:91], v[32:35], v[44:47]\nds_read_b128 v[80:83], v24 offset:62464\nv_mfma_f32_16x16x32_bf16 v[48:51], v[88:91], v[36:39], v[48:51]\n" \
"v_mfma_f32_16x16x32_bf16 v[52:55], a[192:195], v[32:35], v[52:55]\nv_mfma_f32_16x16x32_bf16 v[56:59], a[192:195], v[36:39], v[56:59]\nv_mfma_f32_16x16x32_bf16 v[60:63], v[144:147], v[32:35], v[60:63]\nv_mfma_f32_16x16x32_bf16 v[64:67], v[144:147], v[36:39], v[64:67]\n" \
"v_mfma_f32_16x16x32_bf16 v[68:71], v[240:243], v[32:35], v[68:71]\ns_waitcnt lgkmcnt(0)\nv_mfma_f32_16x16x32_bf16 v[72:75], v[240:243], v[36:39], v[72:75]\nds_read_b128 v[32:35], v24 offset:30720\n" \
"v_add_u32 v31, s32, v24\nv_mfma_f32_16x16x32_bf16 v[44:47], v[76:79], v[40:43], v[44:47]\nds_read_b128 v[36:39], v24 offset:63488\nv_mfma_f32_16x16x32_bf16 v[48:51], v[76:79], v[80:83], v[48:51]\n" \
"v_mfma_f32_16x16x32_bf16 v[52:55], a[196:199], v[40:43], v[52:55]\nv_mfma_f32_16x16x32_bf16 v[56:59], a[196:199], v[80:83], v[56:59]\nv_mfma_f32_16x16x32_bf16 v[60:63], v[148:151], v[40:43], v[60:63]\nv_mfma_f32_16x16x32_bf16 v[64:67], v[148:151], v[80:83], v[64:67]\n" \
"v_mfma_f32_16x16x32_bf16 v[68:71], v[244:247], v[40:43], v[68:71]\ns_waitcnt vmcnt(0) lgkmcnt(0)\nds_read_b128 v[40:43], v31 offset:16384\nds_read_b128 v[76:79], v31 offset:17408\n" \
"ds_read_b128 v[84:87], v31 offset:18432\nv_mfma_f32_16x16x32_bf16 v[72:75], v[244:247], v[80:83], v[72:75]\nds_read_b128 v[88:91], v31 offset:19456\nv_mfma_f32_16x16x32_bf16 v[44:47], a[88:91], v[32:35], v[44:47]\n" \
"ds_read_b128 v[80:83], v24 offset:31744\nv_mfma_f32_16x16x32_bf16 v[48:51], a[88:91], v[36:39], v[48:51]\nds_read_b128 v[92:95], v24 offset:64512\nv_mfma_f32_16x16x32_bf16 v[52:55], a[200:203], v[32:35], v[52:55]\n" \
"v_mfma_f32_16x16x32_bf16 v[56:59], a[200:203], v[36:39], v[56:59]\ns_waitcnt lgkmcnt(3)\nv_max3_u32 v96, v40, v76, v84\nv_mov_b64 v[100:101], 0\n" \
"v_mfma_f32_16x16x32_bf16 v[60:63], v[152:155], v[32:35], v[60:63]\nds_read_b128 v[104:107], v25 offset:0\nv_mov_b64 v[102:103], 0\nv_mfma_f32_16x16x32_bf16 v[64:67], v[152:155], v[36:39], v[64:67]\n" \
"s_waitcnt lgkmcnt(3)\nv_max_u32 v96, v96, v88\nv_cmp_eq_u32 vcc, -1, v96\nv_mfma_f32_16x16x32_bf16 v[68:71], v[248:251], v[32:35], v[68:71]\n" \
"ds_read_b128 v[108:111], v25 offset:1024\nv_mov_b64 v[32:33], 0\nv_mfma_f32_16x16x32_bf16 v[72:75], v[248:251], v[36:39], v[72:75]\ns_waitcnt lgkmcnt(2)\n" \
"s_cbranch_vccnz 2f\n3:\nv_mfma_f32_16x16x32_bf16 v[44:47], a[92:95], v[80:83], v[44:47]\ns_barrier\n" \
"s_memrealtime s[56:57]\nv_mfma_f32_16x16x32_bf16 v[52:55], a[204:207], v[80:83], v[52:55]\nds_read_b128 v[36:39], v24 offset:20480\nv_mov_b64 v[34:35], 0\n" \
"v_mfma_f32_16x16x32_bf16 v[60:63], v[156:159], v[80:83], v[60:63]\nds_read_b128 v[40:43], v24 offset:21504\nv_add_u32 v31, s44, v24\nv_mfma_f32_16x16x32_bf16 v[68:71], v[252:255], v[80:83], v[68:71]\n" \
"ds_read_b128 v[76:79], v24 offset:16384\nv_cvt_pk_bf16_f32 v44, v44, v52\nv_mfma_f32_16x16x32_bf16 v[48:51], a[92:95], v[92:95], v[48:51]\nds_read_b128 v[80:83], v24 offset:22528\n" \
"v_cvt_pk_bf16_f32 v45, v45, v53\nv_mfma_f32_16x16x32_bf16 v[56:59], a[204:207], v[92:95], v[56:59]\nds_read_b128 v[84:87], v24 offset:23552\nv_cvt_pk_bf16_f32 v46, v46, v54\n" \
"v_mfma_f32_16x16x32_bf16 v[64:67], v[156:159], v[92:95], v[64:67]\nds_read_b128 v[88:91], v24 offset:17408\nv_cvt_pk_bf16_f32 v47, v47, v55\nv_mfma_f32_16x16x32_bf16 v[72:75], v[252:255], v[92:95], v[72:75]\n" \
"s_waitcnt lgkmcnt(5)\nds_write_b64 v31, v[44:45] offset:0\nv_smfmac_f32_16x16x64_bf16 v[100:103], v[36:39], v[104:111], v26\nds_read_b128 v[52:55], v24 offset:18432\n" \
"v_cvt_pk_bf16_f32 v60, v60, v68\nv_smfmac_f32_16x16x64_bf16 v[32:35], v[36:39], a[120:127], v26\ns_waitcnt lgkmcnt(5)\nds_write_b64 v31, v[46:47] offset:8\n" \
"v_smfmac_f32_16x16x64_bf16 v[100:103], v[40:43], a[24:31], v26\nds_read_b128 v[92:95], v24 offset:19456\nv_cvt_pk_bf16_f32 v61, v61, v69\nv_smfmac_f32_16x16x64_bf16 v[32:35], v[40:43], a[128:135], v26\n" \
"ds_write_b64 v31, v[60:61] offset:32768\nv_smfmac_f32_16x16x64_bf16 v[100:103], v[76:79], v[0:7], v26\nds_read_b128 v[44:47], v24 offset:49152\nv_cvt_pk_bf16_f32 v62, v62, v70\n" \
"v_smfmac_f32_16x16x64_bf16 v[32:35], v[76:79], a[96:103], v26\ns_waitcnt lgkmcnt(5)\ns_add_u32 s94, s56, 0x7e\nv_cvt_pk_bf16_f32 v63, v63, v71\n" \
"v_smfmac_f32_16x16x64_bf16 v[100:103], v[80:83], a[32:39], v26\nds_read_b128 v[68:71], v24 offset:50176\ns_and_b32 s94, s94, 0xffffff\nv_smfmac_f32_16x16x64_bf16 v[32:35], v[80:83], a[136:143], v26\n" \
"ds_read_b128 v[76:79], v25 offset:6144\ns_or_b32 s94, s94, s93\nv_smfmac_f32_16x16x64_bf16 v[100:103], v[84:87], a[40:47], v26\nds_read_b128 v[80:83], v25 offset:7168\n" \
"v_mov_b32 v60, s94\nv_smfmac_f32_16x16x64_bf16 v[32:35], v[84:87], a[144:151], v26\nds_read_b128 v[96:99], v24 offset:51200\nv_cvt_pk_bf16_f32 v48, v48, v56\n" \
"v_smfmac_f32_16x16x64_bf16 v[100:103], v[88:91], a[0:7], v26\nds_read_b128 v[104:107], v25 offset:2048\nv_cvt_pk_bf16_f32 v49, v49, v57\nv_smfmac_f32_16x16x64_bf16 v[32:35], v[88:91], v[8:15], v26\n" \
"s_waitcnt lgkmcnt(5)\nds_read_b128 v[108:111], v25 offset:3072\nv_smfmac_f32_16x16x64_bf16 v[100:103], v[52:55], a[8:15], v26\nds_read_b128 v[84:87], v24 offset:56320\n" \
"v_cvt_pk_bf16_f32 v50, v50, v58\nv_smfmac_f32_16x16x64_bf16 v[32:35], v[52:55], a[104:111], v26\nds_write_b64 v31, v[62:63] offset:32776\nv_smfmac_f32_16x16x64_bf16 v[100:103], v[92:95], a[16:23], v26\n" \
"buffer_store_dword v60, v29, s[8:11], s49 offen offset:0 sc1\nv_smfmac_f32_16x16x64_bf16 v[32:35], v[92:95], a[112:119], v26\nds_read_b128 v[52:55], v24 offset:52224\nv_cvt_pk_bf16_f32 v51, v51, v59\n" \
"v_smfmac_f32_16x16x64_bf16 v[100:103], v[44:47], a[48:55], v26\nds_read_b128 v[56:59], v24 offset:53248\nv_cvt_pk_bf16_f32 v64, v64, v72\nv_smfmac_f32_16x16x64_bf16 v[32:35], v[44:47], a[152:159], v26\n" \
"s_waitcnt lgkmcnt(4)\nds_write_b64 v31, v[48:49] offset:4096\nv_smfmac_f32_16x16x64_bf16 v[100:103], v[68:71], a[56:63], v26\nds_read_b128 v[60:63], v24 offset:54272\n" \
"v_cvt_pk_bf16_f32 v65, v65, v73\nv_smfmac_f32_16x16x64_bf16 v[32:35], v[68:71], v[76:83], v26\nds_read_b128 v[92:95], v24 offset:55296\nv_cvt_pk_bf16_f32 v66, v66, v74\n" \
"v_smfmac_f32_16x16x64_bf16 v[100:103], v[96:99], v[104:111], v26\nds_read_b128 v[68:71], v25 offset:4096\nv_cvt_pk_bf16_f32 v67, v67, v75\nv_smfmac_f32_16x16x64_bf16 v[32:35], v[96:99], a[160:167], v26\n" \
"s_waitcnt lgkmcnt(5)\nds_read_b128 v[72:75], v25 offset:5120\nv_smfmac_f32_16x16x64_bf16 v[100:103], v[84:87], a[88:95], v26\nds_write_b64 v31, v[50:51] offset:4104\n" \
"v_smfmac_f32_16x16x64_bf16 v[32:35], v[84:87], a[200:207], v26\nds_read_b128 v[76:79], v25 offset:14336\nv_mov_b64 v[48:49], 0\nv_smfmac_f32_16x16x64_bf16 v[100:103], v[52:55], a[64:71], v26\n" \
"ds_read_b128 v[80:83], v25 offset:15360\nv_mov_b64 v[50:51], 0\nv_smfmac_f32_16x16x64_bf16 v[32:35], v[52:55], a[168:175], v26\ns_waitcnt lgkmcnt(5)\n" \
"ds_write_b64 v31, v[64:65] offset:36864\nv_smfmac_f32_16x16x64_bf16 v[100:103], v[56:59], a[72:79], v26\nds_read_b128 v[96:99], v24 offset:51200\nv_mov_b64 v[64:65], 0\n" \
"v_smfmac_f32_16x16x64_bf16 v[32:35], v[56:59], a[176:183], v26\nds_write_b64 v31, v[66:67] offset:36872\nv_smfmac_f32_16x16x64_bf16 v[100:103], v[60:63], a[80:87], v26\nds_read_b128 v[56:59], v24 offset:18432\n" \
"v_mov_b64 v[66:67], 0\nv_smfmac_f32_16x16x64_bf16 v[32:35], v[60:63], a[184:191], v26\ns_waitcnt lgkmcnt(5)\nds_read_b32 v31, v28 offset:512\n" \
"v_smfmac_f32_16x16x64_bf16 v[100:103], v[92:95], v[68:75], v26\nv_add_u32 v60, s45, v24\nv_smfmac_f32_16x16x64_bf16 v[32:35], v[92:95], a[192:199], v26\nds_read_b128 v[68:71], v24 offset:54272\n" \
"v_cndmask_b32 v60, v60, v28, s[28:29]\nv_smfmac_f32_16x16x64_bf16 v[48:51], v[40:43], a[240:247], v26\ns_waitcnt lgkmcnt(4)\nds_read_b128 v[104:107], v25 offset:18432\n" \
"v_smfmac_f32_16x16x64_bf16 v[64:67], v[40:43], v[76:83], v26\nds_read_b128 v[108:111], v25 offset:19456\nv_add_f32 v40, v100, v102\nv_smfmac_f32_16x16x64_bf16 v[48:51], v[36:39], a[232:239], v26\n" \
"ds_read_b128 v[72:75], v25 offset:20480\nv_add_f32 v41, v32, v34\nv_smfmac_f32_16x16x64_bf16 v[64:67], v[36:39], v[192:199], v26\nds_read_b128 v[76:79], v25 offset:21504\n" \
"v_add_f32 v42, v101, v103\nv_smfmac_f32_16x16x64_bf16 v[48:51], v[96:99], v[120:127], v26\nds_read_b128 v[36:39], v24 offset:19456\nv_add_f32 v43, v33, v35\n" \
"v_smfmac_f32_16x16x64_bf16 v[64:67], v[96:99], v[224:231], v26\nds_read_b128 v[32:35], v24 offset:50176\nv_cvt_pk_bf16_f32 v62, v40, v41\nv_smfmac_f32_16x16x64_bf16 v[48:51], v[84:87], v[152:159], v26\n" \
"ds_read_b128 v[80:83], v25 offset:10240\nv_cvt_pk_bf16_f32 v63, v42, v43\nv_smfmac_f32_16x16x64_bf16 v[64:67], v[84:87], v[248:255], v26\nds_write2_b32 v27, v62, v63 offset0:0 offset1:4\n" \
"v_smfmac_f32_16x16x64_bf16 v[48:51], v[92:95], v[144:151], v26\ns_waitcnt lgkmcnt(5)\nds_read_b128 v[84:87], v25 offset:11264\nv_smfmac_f32_16x16x64_bf16 v[64:67], v[92:95], v[240:247], v26\n" \
"ds_read_b128 v[40:43], v24 offset:53248\nv_cmp_eq_u32 vcc, -1, v31\nv_smfmac_f32_16x16x64_bf16 v[48:51], v[56:59], v[16:23], v26\nv_smfmac_f32_16x16x64_bf16 v[64:67], v[56:59], v[176:183], v26\n" \
"ds_read_b128 v[92:95], v24 offset:23552\nv_smfmac_f32_16x16x64_bf16 v[48:51], v[52:55], v[128:135], v26\nds_read_b128 v[96:99], v25 offset:12288\nv_smfmac_f32_16x16x64_bf16 v[64:67], v[52:55], v[104:111], v26\n" \
"s_waitcnt lgkmcnt(4)\nds_read_b128 v[100:103], v25 offset:13312\nv_smfmac_f32_16x16x64_bf16 v[48:51], v[68:71], v[136:143], v26\ns_barrier\n" \
"v_smfmac_f32_16x16x64_bf16 v[64:67], v[68:71], v[72:79], v26\nds_read_b128 v[52:55], v25 offset:16384\nv_mov_b32 v63, s43\nv_smfmac_f32_16x16x64_bf16 v[48:51], v[36:39], a[224:231], v26\n" \
"ds_read_b128 v[56:59], v25 offset:17408\nv_cndmask_b32 v76, v63, 0, s[16:17]\nv_smfmac_f32_16x16x64_bf16 v[64:67], v[36:39], v[184:191], v26\ns_waitcnt lgkmcnt(4)\n" \
"ds_read_b128 v[36:39], v24 offset:22528\nv_smfmac_f32_16x16x64_bf16 v[48:51], v[44:47], v[80:87], v26\nds_read_b128 v[68:71], v25 offset:8192\nv_cndmask_b32 v77, v63, 0, s[18:19]\n" \
"v_smfmac_f32_16x16x64_bf16 v[64:67], v[44:47], v[208:215], v26\nds_read_b128 v[72:75], v25 offset:9216\nv_cndmask_b32 v78, v63, 0, s[20:21]\nv_smfmac_f32_16x16x64_bf16 v[48:51], v[32:35], v[112:119], v26\n" \
"ds_read_b128 v[44:47], v24 offset:16384\nv_cndmask_b32 v79, v63, 0, s[22:23]\nv_smfmac_f32_16x16x64_bf16 v[64:67], v[32:35], v[216:223], v26\nds_read_b128 v[80:83], v60 offset:0\n" \
"v_mov_b32 v62, 0xff61b1e6\nv_smfmac_f32_16x16x64_bf16 v[48:51], v[88:91], a[216:223], v26\nds_read_b128 v[32:35], v24 offset:57344\nv_cndmask_b32 v84, v62, v63, s[16:17]\n" \
"v_smfmac_f32_16x16x64_bf16 v[64:67], v[88:91], v[168:175], v26\ns_waitcnt lgkmcnt(6)\nds_read_b128 v[88:91], v24 offset:61440\nv_smfmac_f32_16x16x64_bf16 v[48:51], v[40:43], v[96:103], v26\n" \
"ds_read_b128 v[104:107], v60 offset:1024\nv_cndmask_b32 v85, v62, v63, s[18:19]\nv_smfmac_f32_16x16x64_bf16 v[64:67], v[40:43], v[232:239], v26\nds_read_b128 v[96:99], v24 offset:58368\n" \
"v_cndmask_b32 v86, v62, v63, s[20:21]\nv_smfmac_f32_16x16x64_bf16 v[48:51], v[92:95], a[248:255], v26\nds_read_b128 v[40:43], v24 offset:62464\nv_cndmask_b32 v87, v62, v63, s[22:23]\n" \
"v_smfmac_f32_16x16x64_bf16 v[64:67], v[92:95], v[52:59], v26\ns_waitcnt lgkmcnt(5)\nds_read_b128 v[56:59], v60 offset:2048\nv_smfmac_f32_16x16x64_bf16 v[48:51], v[36:39], v[68:75], v26\n" \
"ds_read_b128 v[52:55], v24 offset:59392\nv_smfmac_f32_16x16x64_bf16 v[64:67], v[36:39], v[200:207], v26\nds_read_b128 v[68:71], v24 offset:63488\nv_smfmac_f32_16x16x64_bf16 v[48:51], v[44:47], a[208:215], v26\n" \
"ds_read_b128 v[36:39], v60 offset:3072\nds_read_b128 v[60:63], v24 offset:60416\nv_smfmac_f32_16x16x64_bf16 v[64:67], v[44:47], v[160:167], v26\ns_waitcnt lgkmcnt(6)\n" \
"ds_read_b128 v[44:47], v24 offset:64512\nv_mfma_f32_16x16x32_bf16 v[76:79], v[32:35], v[80:83], v[76:79]\nds_read_b128 v[72:75], v24 offset:0\nv_add_f32 v32, v48, v50\n" \
"v_add_f32 v34, v49, v51\nv_mfma_f32_16x16x32_bf16 v[84:87], v[88:91], v[80:83], v[84:87]\ns_waitcnt lgkmcnt(6)\nv_add_f32 v33, v64, v66\n" \
"v_add_f32 v35, v65, v67\nv_cvt_pk_bf16_f32 v48, v32, v33\nv_cvt_pk_bf16_f32 v49, v34, v35\nv_mfma_f32_16x16x32_bf16 v[76:79], v[96:99], v[104:107], v[76:79]\n" \
"s_waitcnt lgkmcnt(4)\nds_write2_b32 v27, v48, v49 offset0:128 offset1:132\nv_mfma_f32_16x16x32_bf16 v[84:87], v[40:43], v[104:107], v[84:87]\nds_read_b128 v[32:35], v24 offset:32768\n" \
"v_mfma_f32_16x16x32_bf16 v[76:79], v[52:55], v[56:59], v[76:79]\ns_waitcnt lgkmcnt(4)\ns_cmp_eq_u64 vcc, 0\ns_cselect_b32 s14, s10, 0\n" \
"v_mfma_f32_16x16x32_bf16 v[84:87], v[68:71], v[56:59], v[84:87]\ns_cmp_eq_u32 s51, 0\ns_cbranch_scc1 4f\n5:\n" \
"v_mfma_f32_16x16x32_bf16 v[76:79], v[60:63], v[36:39], v[76:79]\ns_waitcnt lgkmcnt(1)\ns_barrier\nds_read_b128 v[40:43], v28 offset:512\n" \
"ds_read_b128 v[48:51], v28 offset:1536\nv_mfma_f32_16x16x32_bf16 v[84:87], v[44:47], v[36:39], v[84:87]\nds_read_b128 v[64:67], v28 offset:2560\nds_read_b128 v[88:91], v28 offset:3584\n" \
"v_mfma_f32_16x16x32_bf16 v[44:47], v[0:3], v[72:75], 0\nds_read_b128 v[92:95], v24 offset:1024\nv_max3_f32 v31, v76, v77, v78\nv_max3_f32 v31, v31, v79, s47\n" \
"v_mfma_f32_16x16x32_bf16 v[52:55], a[96:99], v[72:75], 0\nds_read_b128 v[96:99], v24 offset:33792\nv_max3_f32 v100, v84, v85, v86\nv_max3_f32 v101, v31, v100, v87\n" \
"v_max3_f32 v31, v31, v100, v87\nv_mfma_f32_16x16x32_bf16 v[60:63], a[208:211], v[72:75], 0\ns_waitcnt lgkmcnt(6)\nv_mul_f32 v100, s46, v31\n" \
"v_fma_f32 v76, v76, s46, -v100\nv_exp_f32 v76, v76\nv_mfma_f32_16x16x32_bf16 v[68:71], v[160:163], v[72:75], 0\ns_waitcnt lgkmcnt(4)\n" \
"v_fma_f32 v78, v78, s46, -v100\nv_exp_f32 v78, v78\nv_fma_f32 v77, v77, s46, -v100\nv_fma_f32 v79, v79, s46, -v100\n" \
"v_mfma_f32_16x16x32_bf16 v[108:111], v[40:43], v[80:83], 0\nv_exp_f32 v77, v77\nv_fma_f32 v84, v84, s46, -v100\nv_fma_f32 v86, v86, s46, -v100\n" \
"v_fma_f32 v85, v85, s46, -v100\nv_mfma_f32_16x16x32_bf16 v[108:111], v[48:51], v[104:107], v[108:111]\ns_waitcnt lgkmcnt(0)\nv_fma_f32 v87, v87, s46, -v100\n" \
"v_cndmask_b32 v40, v76, v78, s[24:25]\nv_mfma_f32_16x16x32_bf16 v[108:111], v[64:67], v[56:59], v[108:111]\nv_exp_f32 v79, v79\nv_mfma_f32_16x16x32_bf16 v[108:111], v[88:91], v[36:39], v[108:111]\n" \
"v_exp_f32 v84, v84\nv_mfma_f32_16x16x32_bf16 v[48:51], v[0:3], v[32:35], 0\nv_exp_f32 v86, v86\nv_exp_f32 v85, v85\n" \
"v_mfma_f32_16x16x32_bf16 v[56:59], a[96:99], v[32:35], 0\nv_exp_f32 v87, v87\nv_cndmask_b32 v41, v77, v79, s[24:25]\nv_cndmask_b32 v42, v84, v86, s[24:25]\n" \
"v_mfma_f32_16x16x32_bf16 v[64:67], a[208:211], v[32:35], 0\nv_cndmask_b32 v43, v85, v87, s[24:25]\nv_permlane32_swap_b32 v31, v101\nv_mfma_f32_16x16x32_bf16 v[72:75], v[160:163], v[32:35], 0\n" \
"v_max3_f32 v36, v108, v109, v110\nv_max3_f32 v31, v31, v101, v111\nv_max_f32 v37, v36, v31\nv_max_f32 v36, v36, v31\n" \
"v_mfma_f32_16x16x32_bf16 v[44:47], v[4:7], v[92:95], v[44:47]\nds_read_b128 v[32:35], v24 offset:2048\nv_permlane16_swap_b32 v36, v37\nv_mfma_f32_16x16x32_bf16 v[48:51], v[4:7], v[96:99], v[48:51]\n" \
"ds_read_b128 v[80:83], v24 offset:34816\nv_max_f32 v36, v36, v37\nv_mfma_f32_16x16x32_bf16 v[52:55], a[100:103], v[92:95], v[52:55]\nv_cndmask_b32 v38, v108, v110, s[26:27]\n" \
"v_fma_f32 v100, v36, -s46, v100\nv_mul_f32 v36, s46, v36\nv_cndmask_b32 v39, v109, v111, s[26:27]\nv_fma_f32 v38, v38, s46, -v36\n" \
"v_mfma_f32_16x16x32_bf16 v[56:59], a[100:103], v[96:99], v[56:59]\nv_add_f32 v88, v76, v78\nv_add_f32 v89, v77, v79\nv_fma_f32 v39, v39, s46, -v36\n" \
"v_add_f32 v90, v84, v86\nv_add_f32 v88, v88, v89\nv_mfma_f32_16x16x32_bf16 v[60:63], a[212:215], v[92:95], v[60:63]\nv_exp_f32 v100, v100\n" \
"v_exp_f32 v38, v38\nv_add_f32 v91, v85, v87\nv_mfma_f32_16x16x32_bf16 v[64:67], a[212:215], v[96:99], v[64:67]\nv_add_f32 v88, v88, v90\n" \
"v_exp_f32 v39, v39\nv_add_f32 v88, v88, v91\nv_fma_f32 v88, v88, v100, v38\nv_mfma_f32_16x16x32_bf16 v[68:71], v[164:167], v[92:95], v[68:71]\n" \
"s_waitcnt lgkmcnt(0)\nv_add_f32 v89, v88, v39\nv_add_f32 v88, v39, v88\nv_mfma_f32_16x16x32_bf16 v[72:75], v[164:167], v[96:99], v[72:75]\n" \
"ds_read_b128 v[92:95], v24 offset:3072\nv_permlane16_swap_b32 v88, v89\nv_add_f32 v90, v88, v89\nv_add_f32 v88, v89, v88\n" \
"v_mfma_f32_16x16x32_bf16 v[44:47], a[0:3], v[32:35], v[44:47]\nds_read_b128 v[96:99], v24 offset:35840\nv_permlane32_swap_b32 v88, v90\nv_mfma_f32_16x16x32_bf16 v[48:51], a[0:3], v[80:83], v[48:51]\n" \
"v_add_f32 v88, v88, v90\nv_mfma_f32_16x16x32_bf16 v[52:55], v[8:11], v[32:35], v[52:55]\nv_rcp_f32 v88, v88\nv_mfma_f32_16x16x32_bf16 v[56:59], v[8:11], v[80:83], v[56:59]\n" \
"v_mul_f32 v100, v100, v88\nv_mul_f32 v40, v40, v100\nv_mul_f32 v41, v41, v100\nv_mul_f32 v38, v38, v88\n" \
"v_mul_f32 v39, v39, v88\nv_mfma_f32_16x16x32_bf16 v[60:63], a[216:219], v[32:35], v[60:63]\nv_mul_f32 v42, v42, v100\nv_mul_f32 v43, v43, v100\n" \
"v_cvt_pk_bf16_f32 v36, v40, v41\nv_cvt_pk_bf16_f32 v31, v38, v39\nv_cvt_pk_bf16_f32 v37, v42, v43\nv_mfma_f32_16x16x32_bf16 v[64:67], a[216:219], v[80:83], v[64:67]\n" \
"v_and_or_b32 v36, v36, -2, s95\nv_and_or_b32 v31, v31, -2, s95\nv_mfma_f32_16x16x32_bf16 v[68:71], v[168:171], v[32:35], v[68:71]\ns_waitcnt lgkmcnt(0)\n" \
"buffer_store_dwordx2 v[36:37], v29, s[8:11], s99 offen offset:0 sc1\nbuffer_store_dword v31, v30, s[8:11], s96 offen offset:1536 sc1\nv_mfma_f32_16x16x32_bf16 v[72:75], v[168:171], v[80:83], v[72:75]\nds_read_b128 v[88:91], v24 offset:4096\n" \
"v_mul_f32 v76, v76, v100\nv_mfma_f32_16x16x32_bf16 v[44:47], a[4:7], v[92:95], v[44:47]\nds_read_b128 v[32:35], v24 offset:36864\nv_mul_f32 v77, v77, v100\n" \
"v_mfma_f32_16x16x32_bf16 v[48:51], a[4:7], v[96:99], v[48:51]\nv_mul_f32 v78, v78, v100\nv_mul_f32 v79, v79, v100\nv_mfma_f32_16x16x32_bf16 v[52:55], v[12:15], v[92:95], v[52:55]\n" \
"v_cvt_pk_bf16_f32 v80, v76, v77\nv_cvt_pk_bf16_f32 v81, v78, v79\nv_mfma_f32_16x16x32_bf16 v[56:59], v[12:15], v[96:99], v[56:59]\ns_add_u32 m0, s32, 0x4000\n" \
"v_and_or_b32 v80, v80, -2, s95\nv_mul_f32 v84, v84, v100\nv_mfma_f32_16x16x32_bf16 v[60:63], a[220:223], v[92:95], v[60:63]\nbuffer_store_dwordx2 v[80:81], v29, s[8:11], s97 offen offset:1024 sc1\n" \
"v_mul_f32 v85, v85, v100\nv_mfma_f32_16x16x32_bf16 v[64:67], a[220:223], v[96:99], v[64:67]\ns_add_u32 s99, s31, 0x4000\nv_mul_f32 v86, v86, v100\n" \
"v_mul_f32 v87, v87, v100\nv_mfma_f32_16x16x32_bf16 v[68:71], v[172:175], v[92:95], v[68:71]\ns_waitcnt lgkmcnt(0)\nv_cvt_pk_bf16_f32 v82, v84, v85\n" \
"v_cvt_pk_bf16_f32 v83, v86, v87\nv_mfma_f32_16x16x32_bf16 v[72:75], v[172:175], v[96:99], v[72:75]\nds_read_b128 v[36:39], v24 offset:5120\nv_mfma_f32_16x16x32_bf16 v[44:47], a[8:11], v[88:91], v[44:47]\n" \
"ds_read_b128 v[40:43], v24 offset:37888\nv_mfma_f32_16x16x32_bf16 v[48:51], a[8:11], v[32:35], v[48:51]\nbuffer_store_dwordx4 v[80:83], v24, s[8:11], s98 offen offset:0 sc1\nbuffer_load_dwordx4 v24, s[8:11], s99 offen offset:0 sc1 lds\n" \
"v_mfma_f32_16x16x32_bf16 v[52:55], a[104:107], v[88:91], v[52:55]\nv_mfma_f32_16x16x32_bf16 v[56:59], a[104:107], v[32:35], v[56:59]\nbuffer_load_dwordx4 v24, s[8:11], s99 offen offset:1024 sc1 lds\nv_mfma_f32_16x16x32_bf16 v[60:63], v[16:19], v[88:91], v[60:63]\n" \
"v_mfma_f32_16x16x32_bf16 v[64:67], v[16:19], v[32:35], v[64:67]\nv_mfma_f32_16x16x32_bf16 v[68:71], v[176:179], v[88:91], v[68:71]\ns_waitcnt lgkmcnt(0)\nv_mfma_f32_16x16x32_bf16 v[72:75], v[176:179], v[32:35], v[72:75]\n" \
"ds_read_b128 v[76:79], v24 offset:6144\nv_mfma_f32_16x16x32_bf16 v[44:47], a[12:15], v[36:39], v[44:47]\nds_read_b128 v[32:35], v24 offset:38912\nv_mfma_f32_16x16x32_bf16 v[48:51], a[12:15], v[40:43], v[48:51]\n" \
"buffer_load_dwordx4 v24, s[8:11], s99 offen offset:2048 sc1 lds\nv_mfma_f32_16x16x32_bf16 v[52:55], a[108:111], v[36:39], v[52:55]\nv_mfma_f32_16x16x32_bf16 v[56:59], a[108:111], v[40:43], v[56:59]\nbuffer_load_dwordx4 v24, s[8:11], s99 offen offset:3072 sc1 lds\n" \
"v_mfma_f32_16x16x32_bf16 v[60:63], v[20:23], v[36:39], v[60:63]\nv_mfma_f32_16x16x32_bf16 v[64:67], v[20:23], v[40:43], v[64:67]\nv_mfma_f32_16x16x32_bf16 v[68:71], v[180:183], v[36:39], v[68:71]\ns_waitcnt lgkmcnt(0)\n" \
"v_mfma_f32_16x16x32_bf16 v[72:75], v[180:183], v[40:43], v[72:75]\nds_read_b128 v[36:39], v24 offset:7168\nv_mfma_f32_16x16x32_bf16 v[44:47], a[16:19], v[76:79], v[44:47]\nds_read_b128 v[40:43], v24 offset:39936\n" \
"v_mfma_f32_16x16x32_bf16 v[48:51], a[16:19], v[32:35], v[48:51]\ns_add_u32 m0, s32, 0x6000\ns_add_u32 s99, s31, 0x6000\nv_mfma_f32_16x16x32_bf16 v[52:55], a[112:115], v[76:79], v[52:55]\n" \
"v_mfma_f32_16x16x32_bf16 v[56:59], a[112:115], v[32:35], v[56:59]\nv_mfma_f32_16x16x32_bf16 v[60:63], a[224:227], v[76:79], v[60:63]\nds_read_b128 v[80:83], v25 offset:0\nv_mfma_f32_16x16x32_bf16 v[64:67], a[224:227], v[32:35], v[64:67]\n" \
"buffer_load_dwordx4 v24, s[8:11], s99 offen offset:0 sc1 lds\nv_mfma_f32_16x16x32_bf16 v[68:71], v[184:187], v[76:79], v[68:71]\ns_waitcnt lgkmcnt(1)\nv_mfma_f32_16x16x32_bf16 v[72:75], v[184:187], v[32:35], v[72:75]\n" \
"ds_read_b128 v[76:79], v24 offset:8192\nv_mfma_f32_16x16x32_bf16 v[44:47], a[20:23], v[36:39], v[44:47]\nds_read_b128 v[32:35], v24 offset:40960\nv_mfma_f32_16x16x32_bf16 v[48:51], a[20:23], v[40:43], v[48:51]\n" \
"buffer_load_dwordx4 v24, s[8:11], s99 offen offset:1024 sc1 lds\nv_mfma_f32_16x16x32_bf16 v[52:55], a[116:119], v[36:39], v[52:55]\nv_mfma_f32_16x16x32_bf16 v[56:59], a[116:119], v[40:43], v[56:59]\nv_mfma_f32_16x16x32_bf16 v[60:63], a[228:231], v[36:39], v[60:63]\n" \
"ds_read_b128 v[84:87], v25 offset:1024\nv_mfma_f32_16x16x32_bf16 v[64:67], a[228:231], v[40:43], v[64:67]\nbuffer_load_dwordx4 v24, s[8:11], s99 offen offset:2048 sc1 lds\nv_mfma_f32_16x16x32_bf16 v[68:71], v[188:191], v[36:39], v[68:71]\n" \
"s_waitcnt lgkmcnt(1)\nv_mfma_f32_16x16x32_bf16 v[72:75], v[188:191], v[40:43], v[72:75]\nds_read_b128 v[36:39], v24 offset:9216\nv_mfma_f32_16x16x32_bf16 v[44:47], v[80:83], v[76:79], v[44:47]\n" \
"ds_read_b128 v[40:43], v24 offset:41984\nv_mfma_f32_16x16x32_bf16 v[48:51], v[80:83], v[32:35], v[48:51]\nbuffer_load_dwordx4 v24, s[8:11], s99 offen offset:3072 sc1 lds\nv_mfma_f32_16x16x32_bf16 v[52:55], a[120:123], v[76:79], v[52:55]\n" \
"v_mfma_f32_16x16x32_bf16 v[56:59], a[120:123], v[32:35], v[56:59]\nv_mfma_f32_16x16x32_bf16 v[60:63], a[232:235], v[76:79], v[60:63]\nv_mfma_f32_16x16x32_bf16 v[64:67], a[232:235], v[32:35], v[64:67]\nv_mfma_f32_16x16x32_bf16 v[68:71], v[192:195], v[76:79], v[68:71]\n" \
"s_waitcnt lgkmcnt(0)\nv_mfma_f32_16x16x32_bf16 v[72:75], v[192:195], v[32:35], v[72:75]\nds_read_b128 v[76:79], v24 offset:10240\nv_mfma_f32_16x16x32_bf16 v[44:47], v[84:87], v[36:39], v[44:47]\n" \
"ds_read_b128 v[32:35], v24 offset:43008\nv_mfma_f32_16x16x32_bf16 v[48:51], v[84:87], v[40:43], v[48:51]\nv_mfma_f32_16x16x32_bf16 v[52:55], a[124:127], v[36:39], v[52:55]\nds_read_b128 v[80:83], v25 offset:14336\n" \
"v_mfma_f32_16x16x32_bf16 v[56:59], a[124:127], v[40:43], v[56:59]\nv_mfma_f32_16x16x32_bf16 v[60:63], a[236:239], v[36:39], v[60:63]\nv_mfma_f32_16x16x32_bf16 v[64:67], a[236:239], v[40:43], v[64:67]\nv_mfma_f32_16x16x32_bf16 v[68:71], v[196:199], v[36:39], v[68:71]\n" \
"s_waitcnt lgkmcnt(1)\nv_mfma_f32_16x16x32_bf16 v[72:75], v[196:199], v[40:43], v[72:75]\nds_read_b128 v[36:39], v24 offset:11264\nv_mfma_f32_16x16x32_bf16 v[44:47], a[24:27], v[76:79], v[44:47]\n" \
"ds_read_b128 v[40:43], v24 offset:44032\nv_mfma_f32_16x16x32_bf16 v[48:51], a[24:27], v[32:35], v[48:51]\nv_mfma_f32_16x16x32_bf16 v[52:55], a[128:131], v[76:79], v[52:55]\nds_read_b128 v[84:87], v25 offset:15360\n" \
"v_mfma_f32_16x16x32_bf16 v[56:59], a[128:131], v[32:35], v[56:59]\nv_mfma_f32_16x16x32_bf16 v[60:63], a[240:243], v[76:79], v[60:63]\ns_waitcnt lgkmcnt(3)\nv_mfma_f32_16x16x32_bf16 v[64:67], a[240:243], v[32:35], v[64:67]\n" \
"v_mfma_f32_16x16x32_bf16 v[68:71], v[80:83], v[76:79], v[68:71]\ns_waitcnt lgkmcnt(1)\nv_mfma_f32_16x16x32_bf16 v[72:75], v[80:83], v[32:35], v[72:75]\nds_read_b128 v[76:79], v24 offset:12288\n" \
"v_mfma_f32_16x16x32_bf16 v[44:47], a[28:31], v[36:39], v[44:47]\nds_read_b128 v[32:35], v24 offset:45056\nv_mfma_f32_16x16x32_bf16 v[48:51], a[28:31], v[40:43], v[48:51]\nds_read_b128 v[80:83], v25 offset:8192\n" \
"v_mfma_f32_16x16x32_bf16 v[52:55], a[132:135], v[36:39], v[52:55]\nv_mfma_f32_16x16x32_bf16 v[56:59], a[132:135], v[40:43], v[56:59]\nv_mfma_f32_16x16x32_bf16 v[60:63], a[244:247], v[36:39], v[60:63]\ns_waitcnt lgkmcnt(3)\n" \
"v_mfma_f32_16x16x32_bf16 v[64:67], a[244:247], v[40:43], v[64:67]\nv_mfma_f32_16x16x32_bf16 v[68:71], v[84:87], v[36:39], v[68:71]\ns_waitcnt lgkmcnt(1)\nv_mfma_f32_16x16x32_bf16 v[72:75], v[84:87], v[40:43], v[72:75]\n" \
"ds_read_b128 v[36:39], v24 offset:13312\nv_mfma_f32_16x16x32_bf16 v[44:47], a[32:35], v[76:79], v[44:47]\nds_read_b128 v[40:43], v24 offset:46080\nv_mfma_f32_16x16x32_bf16 v[48:51], a[32:35], v[32:35], v[48:51]\n" \
"ds_read_b128 v[84:87], v25 offset:9216\nv_mfma_f32_16x16x32_bf16 v[52:55], a[136:139], v[76:79], v[52:55]\ns_waitcnt lgkmcnt(3)\nv_mfma_f32_16x16x32_bf16 v[56:59], a[136:139], v[32:35], v[56:59]\n" \
"v_mfma_f32_16x16x32_bf16 v[60:63], v[80:83], v[76:79], v[60:63]\nv_mfma_f32_16x16x32_bf16 v[64:67], v[80:83], v[32:35], v[64:67]\nv_mfma_f32_16x16x32_bf16 v[68:71], v[200:203], v[76:79], v[68:71]\ns_waitcnt lgkmcnt(1)\n" \
"v_mfma_f32_16x16x32_bf16 v[72:75], v[200:203], v[32:35], v[72:75]\nds_read_b128 v[76:79], v24 offset:14336\nv_mfma_f32_16x16x32_bf16 v[44:47], a[36:39], v[36:39], v[44:47]\nds_read_b128 v[32:35], v24 offset:47104\n" \
"v_mfma_f32_16x16x32_bf16 v[48:51], a[36:39], v[40:43], v[48:51]\nv_mfma_f32_16x16x32_bf16 v[52:55], a[140:143], v[36:39], v[52:55]\ns_waitcnt lgkmcnt(2)\nds_read_b128 v[80:83], v25 offset:16384\n" \
"v_mfma_f32_16x16x32_bf16 v[56:59], a[140:143], v[40:43], v[56:59]\nv_mfma_f32_16x16x32_bf16 v[60:63], v[84:87], v[36:39], v[60:63]\nv_mfma_f32_16x16x32_bf16 v[64:67], v[84:87], v[40:43], v[64:67]\nv_mfma_f32_16x16x32_bf16 v[68:71], v[204:207], v[36:39], v[68:71]\n" \
"s_waitcnt lgkmcnt(1)\nv_mfma_f32_16x16x32_bf16 v[72:75], v[204:207], v[40:43], v[72:75]\nds_read_b128 v[36:39], v24 offset:15360\nv_mfma_f32_16x16x32_bf16 v[44:47], a[40:43], v[76:79], v[44:47]\n" \
"ds_read_b128 v[40:43], v24 offset:48128\nv_mfma_f32_16x16x32_bf16 v[48:51], a[40:43], v[32:35], v[48:51]\nv_mfma_f32_16x16x32_bf16 v[52:55], a[144:147], v[76:79], v[52:55]\nds_read_b128 v[84:87], v25 offset:17408\n" \
"v_mfma_f32_16x16x32_bf16 v[56:59], a[144:147], v[32:35], v[56:59]\nv_mfma_f32_16x16x32_bf16 v[60:63], a[248:251], v[76:79], v[60:63]\ns_waitcnt lgkmcnt(3)\nv_mfma_f32_16x16x32_bf16 v[64:67], a[248:251], v[32:35], v[64:67]\n" \
"s_waitcnt vmcnt(4) lgkmcnt(1)\ns_barrier\nv_mfma_f32_16x16x32_bf16 v[68:71], v[80:83], v[76:79], v[68:71]\ns_add_u32 m0, s32, 0\n" \
"s_add_u32 s99, s31, 0x10000\nv_mfma_f32_16x16x32_bf16 v[72:75], v[80:83], v[32:35], v[72:75]\nds_read_b128 v[76:79], v24 offset:16384\nv_mfma_f32_16x16x32_bf16 v[44:47], a[44:47], v[36:39], v[44:47]\n" \
"ds_read_b128 v[32:35], v24 offset:49152\nv_mfma_f32_16x16x32_bf16 v[48:51], a[44:47], v[40:43], v[48:51]\nds_read_b128 v[80:83], v25 offset:10240\nv_mfma_f32_16x16x32_bf16 v[52:55], a[148:151], v[36:39], v[52:55]\n" \
"buffer_load_dwordx4 v24, s[12:15], s99 offen offset:0 sc1 lds\nv_mfma_f32_16x16x32_bf16 v[56:59], a[148:151], v[40:43], v[56:59]\nv_mfma_f32_16x16x32_bf16 v[60:63], a[252:255], v[36:39], v[60:63]\ns_waitcnt lgkmcnt(3)\n" \
"v_mfma_f32_16x16x32_bf16 v[64:67], a[252:255], v[40:43], v[64:67]\nbuffer_load_dwordx4 v24, s[12:15], s99 offen offset:1024 sc1 lds\nv_mfma_f32_16x16x32_bf16 v[68:71], v[84:87], v[36:39], v[68:71]\ns_waitcnt lgkmcnt(1)\n" \
"v_mfma_f32_16x16x32_bf16 v[72:75], v[84:87], v[40:43], v[72:75]\nds_read_b128 v[36:39], v24 offset:17408\nv_mfma_f32_16x16x32_bf16 v[44:47], a[48:51], v[76:79], v[44:47]\nds_read_b128 v[40:43], v24 offset:50176\n" \
"v_mfma_f32_16x16x32_bf16 v[48:51], a[48:51], v[32:35], v[48:51]\nds_read_b128 v[84:87], v25 offset:11264\nv_mfma_f32_16x16x32_bf16 v[52:55], a[152:155], v[76:79], v[52:55]\ns_waitcnt lgkmcnt(3)\n" \
"buffer_load_dwordx4 v24, s[12:15], s99 offen offset:2048 sc1 lds\nv_mfma_f32_16x16x32_bf16 v[56:59], a[152:155], v[32:35], v[56:59]\nv_mfma_f32_16x16x32_bf16 v[60:63], v[80:83], v[76:79], v[60:63]\nv_mfma_f32_16x16x32_bf16 v[64:67], v[80:83], v[32:35], v[64:67]\n" \
"buffer_load_dwordx4 v24, s[12:15], s99 offen offset:3072 sc1 lds\nv_mfma_f32_16x16x32_bf16 v[68:71], v[208:211], v[76:79], v[68:71]\ns_waitcnt lgkmcnt(1)\nds_read_b128 v[76:79], v25 offset:6144\n" \
"v_mfma_f32_16x16x32_bf16 v[72:75], v[208:211], v[32:35], v[72:75]\nds_read_b128 v[80:83], v24 offset:18432\nv_mfma_f32_16x16x32_bf16 v[44:47], a[52:55], v[36:39], v[44:47]\nds_read_b128 v[32:35], v24 offset:51200\n" \
"s_add_u32 m0, s32, 0x2000\nv_mfma_f32_16x16x32_bf16 v[48:51], a[52:55], v[40:43], v[48:51]\ns_add_u32 s99, s31, 0x12000\nv_mfma_f32_16x16x32_bf16 v[52:55], a[156:159], v[36:39], v[52:55]\n" \
"s_waitcnt lgkmcnt(3)\nv_mfma_f32_16x16x32_bf16 v[56:59], a[156:159], v[40:43], v[56:59]\nv_mfma_f32_16x16x32_bf16 v[60:63], v[84:87], v[36:39], v[60:63]\nbuffer_load_dwordx4 v24, s[12:15], s99 offen offset:0 sc1 lds\n" \
"v_mfma_f32_16x16x32_bf16 v[64:67], v[84:87], v[40:43], v[64:67]\nv_mfma_f32_16x16x32_bf16 v[68:71], v[212:215], v[36:39], v[68:71]\ns_waitcnt lgkmcnt(0)\nds_read_b128 v[36:39], v25 offset:7168\n" \
"v_mfma_f32_16x16x32_bf16 v[72:75], v[212:215], v[40:43], v[72:75]\nds_read_b128 v[84:87], v24 offset:19456\nv_mfma_f32_16x16x32_bf16 v[44:47], a[56:59], v[80:83], v[44:47]\nds_read_b128 v[40:43], v24 offset:52224\n" \
"v_mfma_f32_16x16x32_bf16 v[48:51], a[56:59], v[32:35], v[48:51]\nbuffer_load_dwordx4 v24, s[12:15], s99 offen offset:1024 sc1 lds\nv_mfma_f32_16x16x32_bf16 v[52:55], v[76:79], v[80:83], v[52:55]\nv_mfma_f32_16x16x32_bf16 v[56:59], v[76:79], v[32:35], v[56:59]\n" \
"v_mfma_f32_16x16x32_bf16 v[60:63], v[112:115], v[80:83], v[60:63]\nds_read_b128 v[76:79], v25 offset:2048\nv_mfma_f32_16x16x32_bf16 v[64:67], v[112:115], v[32:35], v[64:67]\nbuffer_load_dwordx4 v24, s[12:15], s99 offen offset:2048 sc1 lds\n" \
"v_mfma_f32_16x16x32_bf16 v[68:71], v[216:219], v[80:83], v[68:71]\ns_waitcnt lgkmcnt(1)\nv_mfma_f32_16x16x32_bf16 v[72:75], v[216:219], v[32:35], v[72:75]\nds_read_b128 v[80:83], v24 offset:20480\n" \
"v_mfma_f32_16x16x32_bf16 v[44:47], a[60:63], v[84:87], v[44:47]\nds_read_b128 v[32:35], v24 offset:53248\nv_mfma_f32_16x16x32_bf16 v[48:51], a[60:63], v[40:43], v[48:51]\nbuffer_load_dwordx4 v24, s[12:15], s99 offen offset:3072 sc1 lds\n" \
"v_mfma_f32_16x16x32_bf16 v[52:55], v[36:39], v[84:87], v[52:55]\nv_mfma_f32_16x16x32_bf16 v[56:59], v[36:39], v[40:43], v[56:59]\nv_mfma_f32_16x16x32_bf16 v[60:63], v[116:119], v[84:87], v[60:63]\nv_mfma_f32_16x16x32_bf16 v[64:67], v[116:119], v[40:43], v[64:67]\n" \
"v_mfma_f32_16x16x32_bf16 v[68:71], v[220:223], v[84:87], v[68:71]\nv_mfma_f32_16x16x32_bf16 v[72:75], v[220:223], v[40:43], v[72:75]\ns_branch 1b\n2:\n" \
"s_add_u32 s52, s52, 1\ns_cmp_lt_u32 s52, 0x40000\ns_cbranch_scc0 9f\nv_cmp_eq_u32 vcc, -1, v40\n" \
"s_cbranch_vccz 6f\nbuffer_load_dwordx4 v24, s[8:11], s94 offen offset:0 sc1 lds\n6:\nv_cmp_eq_u32 vcc, -1, v76\n" \
"s_cbranch_vccz 6f\nbuffer_load_dwordx4 v24, s[8:11], s94 offen offset:1024 sc1 lds\n6:\nv_cmp_eq_u32 vcc, -1, v84\n" \
"s_cbranch_vccz 6f\nbuffer_load_dwordx4 v24, s[8:11], s94 offen offset:2048 sc1 lds\n6:\nv_cmp_eq_u32 vcc, -1, v88\n" \
"s_cbranch_vccz 6f\nbuffer_load_dwordx4 v24, s[8:11], s94 offen offset:3072 sc1 lds\n6:\ns_waitcnt vmcnt(0)\n" \
"ds_read_b128 v[40:43], v31 offset:16384\nds_read_b128 v[76:79], v31 offset:17408\nds_read_b128 v[84:87], v31 offset:18432\nds_read_b128 v[88:91], v31 offset:19456\n" \
"s_waitcnt lgkmcnt(0)\nv_max3_u32 v96, v40, v76, v84\nv_max_u32 v96, v96, v88\nv_cmp_eq_u32 vcc, -1, v96\n" \
"s_cbranch_vccnz 2b\ns_branch 3b\n4:\ns_cmp_gt_u32 s30, 64\n" \
"s_cbranch_scc1 9f\ns_cmp_eq_u32 s30, 64\ns_cbranch_scc1 5b\nv_bfe_u32 v40, v24, 6, 3\n" \
"v_lshlrev_b32 v40, 2, v40\ns_lshl_b32 s94, s30, 5\ns_add_u32 s94, s94, s48\n7:\n" \
"buffer_load_dword v41, v40, s[8:11], s94 offen sc1\ns_waitcnt vmcnt(0)\nv_cmp_eq_u32 vcc, -1, v41\ns_cbranch_vccz 8f\n" \
"s_add_u32 s52, s52, 1\ns_cmp_lt_u32 s52, 0x40000\ns_cbranch_scc0 9f\ns_sleep 8\n" \
"s_branch 7b\n8:\ns_mov_b32 m0, s53\ns_add_u32 s94, s30, 1\n" \
"s_min_u32 s94, s94, 63\ns_lshl_b32 s94, s94, 5\ns_add_u32 s94, s94, s48\nbuffer_load_dword v40, s[8:11], s94 offen offset:0 sc1 lds\n" \
"s_add_u32 m0, s32, 0\ns_add_u32 s94, s31, 0\nbuffer_load_dwordx4 v24, s[8:11], s94 offen offset:0 sc1 lds\nbuffer_load_dwordx4 v24, s[8:11], s94 offen offset:1024 sc1 lds\n" \
"buffer_load_dwordx4 v24, s[8:11], s94 offen offset:2048 sc1 lds\nbuffer_load_dwordx4 v24, s[8:11], s94 offen offset:3072 sc1 lds\ns_add_u32 m0, s32, 0x2000\ns_add_u32 s94, s31, 0x2000\n" \
"buffer_load_dwordx4 v24, s[8:11], s94 offen offset:0 sc1 lds\nbuffer_load_dwordx4 v24, s[8:11], s94 offen offset:1024 sc1 lds\nbuffer_load_dwordx4 v24, s[8:11], s94 offen offset:2048 sc1 lds\nbuffer_load_dwordx4 v24, s[8:11], s94 offen offset:3072 sc1 lds\n" \
"v_lshrrev_b32 v40, 2, v24\nv_add_u32 v40, 0x11000, v40\ns_waitcnt vmcnt(0) lgkmcnt(0)\ns_barrier\n" \
"ds_read_b32 v41, v40 offset:0\nds_read_b128 v[72:75], v24 offset:0\nds_read_b128 v[32:35], v24 offset:32768\ns_waitcnt lgkmcnt(0)\n" \
"v_cmp_eq_u32 vcc, -1, v41\ns_cmp_eq_u64 vcc, 0\ns_cselect_b32 s14, s10, 0\ns_branch 5b\n" \
"9:\ns_waitcnt vmcnt(0)\ns_endpgm\n"
#define B_LDS 0x44840c0202a00110ull
#define B_VAR 0x0000000000010001ull
#define B_LV 8192
#define B_LOOP \
"v_mov_b32 v33, v20\ns_branch 4f\n.p2align 6\n1:\n" \
"s_waitcnt lgkmcnt(4)\nv_mfma_f32_16x16x32_bf16 v[44:47], a[8:11], v[28:31], v[44:47]\ns_add_u32 m0, s22, 0\ns_add_u32 s43, s20, 7\n" \
"s_and_b32 s43, s43, 7\nv_mfma_f32_16x16x32_bf16 v[68:71], a[8:11], v[40:43], v[68:71]\ns_lshl_b32 s43, s43, 18\ns_add_u32 s43, s43, s23\n" \
"v_mfma_f32_16x16x32_bf16 v[72:75], a[112:115], v[28:31], v[72:75]\nds_read_b128 v[48:51], v17 offset:0\nv_mfma_f32_16x16x32_bf16 v[20:23], a[112:115], v[40:43], v[20:23]\nv_mfma_f32_16x16x32_bf16 v[44:47], a[12:15], v[36:39], v[44:47]\n" \
"s_waitcnt vmcnt(7) lgkmcnt(1)\ns_barrier\nv_mfma_f32_16x16x32_bf16 v[68:71], a[12:15], v[32:35], v[68:71]\nds_read_b128 v[28:31], v16 offset:8192\n" \
"v_mfma_f32_16x16x32_bf16 v[72:75], a[116:119], v[36:39], v[72:75]\nds_read_b128 v[40:43], v16 offset:40960\nv_mfma_f32_16x16x32_bf16 v[20:23], a[116:119], v[32:35], v[20:23]\nds_read_b128 v[36:39], v16 offset:9216\n" \
"v_mfma_f32_16x16x32_bf16 v[44:47], a[16:19], v[52:55], v[44:47]\nds_read_b128 v[32:35], v17 offset:1024\nv_mfma_f32_16x16x32_bf16 v[68:71], a[16:19], v[24:27], v[68:71]\nbuffer_load_dwordx4 v16, s[8:11], s43 offen offset:0 sc1 lds\n" \
"v_mfma_f32_16x16x32_bf16 v[72:75], a[120:123], v[52:55], v[72:75]\nds_read_b128 v[64:67], v16 offset:41984\nv_mfma_f32_16x16x32_bf16 v[20:23], a[120:123], v[24:27], v[20:23]\nds_read_b128 v[52:55], v16 offset:10240\n" \
"v_mfma_f32_16x16x32_bf16 v[44:47], a[20:23], v[56:59], v[44:47]\nbuffer_load_dwordx4 v16, s[8:11], s43 offen offset:1024 sc1 lds\nv_mfma_f32_16x16x32_bf16 v[68:71], a[20:23], v[60:63], v[68:71]\nds_read_b128 v[24:27], v16 offset:43008\n" \
"v_mfma_f32_16x16x32_bf16 v[72:75], a[124:127], v[56:59], v[72:75]\ns_waitcnt lgkmcnt(3)\nds_read_b128 v[56:59], v17 offset:4096\nv_mfma_f32_16x16x32_bf16 v[20:23], a[124:127], v[60:63], v[20:23]\n" \
"buffer_load_dwordx4 v16, s[8:11], s43 offen offset:2048 sc1 lds\nv_mfma_f32_16x16x32_bf16 v[44:47], v[48:51], v[28:31], v[44:47]\nds_read_b128 v[60:63], v16 offset:11264\nv_mfma_f32_16x16x32_bf16 v[68:71], v[48:51], v[40:43], v[68:71]\n" \
"ds_read_b128 v[76:79], v16 offset:44032\nv_mfma_f32_16x16x32_bf16 v[72:75], a[128:131], v[28:31], v[72:75]\nbuffer_load_dwordx4 v16, s[8:11], s43 offen offset:3072 sc1 lds\nv_mfma_f32_16x16x32_bf16 v[20:23], a[128:131], v[40:43], v[20:23]\n" \
"s_waitcnt lgkmcnt(3)\nds_read_b128 v[28:31], v17 offset:5120\nv_mfma_f32_16x16x32_bf16 v[44:47], v[32:35], v[36:39], v[44:47]\nds_read_b128 v[40:43], v16 offset:12288\n" \
"v_mfma_f32_16x16x32_bf16 v[68:71], v[32:35], v[64:67], v[68:71]\ns_add_u32 m0, s22, 0x6000\nds_read_b128 v[32:35], v16 offset:45056\nv_mfma_f32_16x16x32_bf16 v[72:75], a[132:135], v[36:39], v[72:75]\n" \
"s_add_u32 s42, s21, -0xa000\nv_mfma_f32_16x16x32_bf16 v[20:23], a[132:135], v[64:67], v[20:23]\nds_read_b128 v[36:39], v16 offset:13312\nv_mfma_f32_16x16x32_bf16 v[44:47], a[24:27], v[52:55], v[44:47]\n" \
"s_waitcnt lgkmcnt(4)\nds_read_b128 v[48:51], v16 offset:46080\nv_mfma_f32_16x16x32_bf16 v[68:71], a[24:27], v[24:27], v[68:71]\nbuffer_load_dwordx4 v16, s[12:15], s42 offen offset:0 sc1 lds\n" \
"v_mfma_f32_16x16x32_bf16 v[72:75], v[56:59], v[52:55], v[72:75]\nv_mfma_f32_16x16x32_bf16 v[20:23], v[56:59], v[24:27], v[20:23]\nds_read_b128 v[52:55], v16 offset:14336\nv_mfma_f32_16x16x32_bf16 v[44:47], a[28:31], v[60:63], v[44:47]\n" \
"s_waitcnt lgkmcnt(3)\nbuffer_load_dwordx4 v16, s[12:15], s42 offen offset:1024 sc1 lds\nv_mfma_f32_16x16x32_bf16 v[68:71], a[28:31], v[76:79], v[68:71]\nds_read_b128 v[24:27], v16 offset:47104\n" \
"v_mfma_f32_16x16x32_bf16 v[72:75], v[28:31], v[60:63], v[72:75]\nds_read_b128 v[56:59], v17 offset:6144\nv_mfma_f32_16x16x32_bf16 v[20:23], v[28:31], v[76:79], v[20:23]\nbuffer_load_dwordx4 v16, s[12:15], s42 offen offset:2048 sc1 lds\n" \
"v_mfma_f32_16x16x32_bf16 v[44:47], a[32:35], v[40:43], v[44:47]\nds_read_b128 v[28:31], v16 offset:15360\nv_lshlrev_b32 v60, 5, v16\nv_mfma_f32_16x16x32_bf16 v[68:71], a[32:35], v[32:35], v[68:71]\n" \
"ds_read_b128 v[62:65], v16 offset:48128\nv_and_b32 v61, 0x7000, v60\nv_mfma_f32_16x16x32_bf16 v[72:75], a[136:139], v[40:43], v[72:75]\ns_waitcnt lgkmcnt(4)\n" \
"buffer_load_dwordx4 v16, s[12:15], s42 offen offset:3072 sc1 lds\nv_mfma_f32_16x16x32_bf16 v[20:23], a[136:139], v[32:35], v[20:23]\nds_read_b128 v[40:43], v17 offset:7168\ns_add_u32 m0, s22, 0\n" \
"v_mfma_f32_16x16x32_bf16 v[44:47], a[36:39], v[36:39], v[44:47]\nds_read_b128 v[32:35], v16 offset:16384\nv_lshl_add_u32 v60, v61, 17, v60\nv_mfma_f32_16x16x32_bf16 v[68:71], a[36:39], v[48:51], v[68:71]\n" \
"ds_read_b128 v[76:79], v17 offset:2048\nv_mfma_f32_16x16x32_bf16 v[72:75], a[140:143], v[36:39], v[72:75]\nds_read_b128 v[80:83], v16 offset:49152\nv_mfma_f32_16x16x32_bf16 v[20:23], a[140:143], v[48:51], v[20:23]\n" \
"s_waitcnt lgkmcnt(4)\nds_read_b128 v[36:39], v16 offset:17408\nv_mfma_f32_16x16x32_bf16 v[44:47], a[40:43], v[52:55], v[44:47]\nds_read_b128 v[48:51], v17 offset:3072\n" \
"v_mfma_f32_16x16x32_bf16 v[68:71], a[40:43], v[24:27], v[68:71]\nds_read_b128 v[84:87], v16 offset:50176\nv_mfma_f32_16x16x32_bf16 v[72:75], v[56:59], v[52:55], v[72:75]\nv_mfma_f32_16x16x32_bf16 v[20:23], v[56:59], v[24:27], v[20:23]\n" \
"ds_read_b128 v[52:55], v16 offset:18432\nv_mfma_f32_16x16x32_bf16 v[44:47], a[44:47], v[28:31], v[44:47]\ns_waitcnt lgkmcnt(4)\nds_read_b128 v[24:27], v16 offset:51200\n" \
"v_mfma_f32_16x16x32_bf16 v[68:71], a[44:47], v[62:65], v[68:71]\nds_read_b128 v[56:59], v17 offset:8192\nv_add_u32 v61, s22, v16\nv_mfma_f32_16x16x32_bf16 v[72:75], v[40:43], v[28:31], v[72:75]\n" \
"v_mfma_f32_16x16x32_bf16 v[20:23], v[40:43], v[62:65], v[20:23]\nds_read_b128 v[28:31], v16 offset:19456\nv_mfma_f32_16x16x32_bf16 v[44:47], v[76:79], v[32:35], v[44:47]\nds_read_b128 v[40:43], v16 offset:52224\n" \
"v_mfma_f32_16x16x32_bf16 v[68:71], v[76:79], v[80:83], v[68:71]\nds_read_b128 v[62:65], v17 offset:9216\nv_mfma_f32_16x16x32_bf16 v[72:75], a[144:147], v[32:35], v[72:75]\ns_waitcnt lgkmcnt(5)\n" \
"ds_read_b128 v[92:95], v17 offset:9216\nv_mfma_f32_16x16x32_bf16 v[20:23], a[144:147], v[80:83], v[20:23]\nds_read_b128 v[32:35], v16 offset:20480\nv_mfma_f32_16x16x32_bf16 v[44:47], v[48:51], v[36:39], v[44:47]\n" \
"ds_read_b128 v[76:79], v16 offset:53248\nv_mfma_f32_16x16x32_bf16 v[68:71], v[48:51], v[84:87], v[68:71]\nds_read_b128 v[88:91], v17 offset:8192\nv_mfma_f32_16x16x32_bf16 v[72:75], a[148:151], v[36:39], v[72:75]\n" \
"s_waitcnt vmcnt(4)\nds_read_b128 v[36:39], v61 offset:0\nds_read_b128 v[80:83], v61 offset:1024\nds_read_b128 v[48:51], v61 offset:2048\n" \
"v_mfma_f32_16x16x32_bf16 v[20:23], a[148:151], v[84:87], v[20:23]\ns_waitcnt lgkmcnt(7)\nds_read_b128 v[84:87], v61 offset:3072\nv_mfma_f32_16x16x32_bf16 v[44:47], a[48:51], v[52:55], v[44:47]\n" \
"ds_read_b128 v[96:99], v17 offset:4096\nv_mfma_f32_16x16x32_bf16 v[68:71], a[48:51], v[24:27], v[68:71]\nds_read_b128 v[100:103], v17 offset:5120\nv_mov_b64 v[104:105], 0\n" \
"v_mfma_f32_16x16x32_bf16 v[72:75], v[56:59], v[52:55], v[72:75]\ns_waitcnt lgkmcnt(3)\nv_max3_u32 v52, v36, v80, v48\nv_mov_b64 v[106:107], 0\n" \
"v_mfma_f32_16x16x32_bf16 v[20:23], v[56:59], v[24:27], v[20:23]\ns_waitcnt lgkmcnt(2)\nv_max_u32 v52, v52, v84\nv_cmp_eq_u32 vcc, -1, v52\n" \
"v_mfma_f32_16x16x32_bf16 v[44:47], a[52:55], v[28:31], v[44:47]\nv_mov_b64 v[24:25], 0\nv_mov_b64 v[26:27], 0\nv_mfma_f32_16x16x32_bf16 v[68:71], a[52:55], v[40:43], v[68:71]\n" \
"s_cbranch_vccnz 2f\n3:\nv_mfma_f32_16x16x32_bf16 v[72:75], v[62:65], v[28:31], v[72:75]\ns_barrier\n" \
"ds_read_b128 v[28:31], v16 offset:2048\nv_mfma_f32_16x16x32_bf16 v[20:23], v[92:95], v[40:43], v[20:23]\ns_memrealtime s[40:41]\nds_read_b128 v[36:39], v16 offset:5120\n" \
"v_mfma_f32_16x16x32_bf16 v[44:47], a[56:59], v[32:35], v[44:47]\nds_read_b128 v[40:43], v16 offset:1024\ns_lshl_b32 s43, s20, 24\nv_mfma_f32_16x16x32_bf16 v[68:71], a[56:59], v[76:79], v[68:71]\n" \
"ds_read_b128 v[48:51], v16 offset:33792\nv_mfma_f32_16x16x32_bf16 v[72:75], a[152:155], v[32:35], v[72:75]\nv_mfma_f32_16x16x32_bf16 v[20:23], a[152:155], v[76:79], v[20:23]\ns_waitcnt lgkmcnt(3)\n" \
"ds_read_b128 v[32:35], v16 offset:3072\nv_smfmac_f32_16x16x64_bf16 v[104:107], v[28:31], a[8:15], v18\ns_waitcnt lgkmcnt(3)\nv_smfmac_f32_16x16x64_bf16 v[24:27], v[28:31], a[112:119], v18\n" \
"ds_read_b128 v[52:55], v16 offset:34816\nv_smfmac_f32_16x16x64_bf16 v[104:107], v[36:39], a[24:31], v18\ns_waitcnt lgkmcnt(3)\nds_read_b128 v[76:79], v17 offset:6144\n" \
"v_smfmac_f32_16x16x64_bf16 v[24:27], v[36:39], v[96:103], v18\nds_read_b128 v[28:31], v16 offset:36864\nv_smfmac_f32_16x16x64_bf16 v[104:107], v[40:43], a[0:7], v18\ns_waitcnt lgkmcnt(4)\n" \
"ds_read_b128 v[80:83], v17 offset:7168\nv_smfmac_f32_16x16x64_bf16 v[24:27], v[40:43], a[104:111], v18\nds_read_b128 v[36:39], v16 offset:6144\ns_add_u32 s42, s40, 0x67\n" \
"v_smfmac_f32_16x16x64_bf16 v[104:107], v[48:51], a[48:55], v18\ns_and_b32 s42, s42, 0xffffff\ns_or_b32 s42, s42, s43\nv_mov_b32 v40, s42\n" \
"v_smfmac_f32_16x16x64_bf16 v[24:27], v[48:51], v[88:95], v18\ns_waitcnt lgkmcnt(4)\nds_read_b128 v[48:51], v16 offset:7168\nv_smfmac_f32_16x16x64_bf16 v[104:107], v[32:35], a[16:23], v18\n" \
"buffer_store_dword v40, v60, s[8:11], s27 offen offset:0 sc1\nv_smfmac_f32_16x16x64_bf16 v[24:27], v[32:35], a[120:127], v18\nds_read_b128 v[56:59], v16 offset:35840\nv_smfmac_f32_16x16x64_bf16 v[104:107], v[52:55], a[56:63], v18\n" \
"s_waitcnt lgkmcnt(4)\nv_smfmac_f32_16x16x64_bf16 v[24:27], v[52:55], a[152:159], v18\nds_read_b128 v[32:35], v16 offset:0\ns_add_u32 s16, s20, 1\n" \
"v_smfmac_f32_16x16x64_bf16 v[104:107], v[28:31], a[72:79], v18\nds_read_b128 v[60:63], v17 offset:0\nv_mov_b64 v[40:41], 0\nv_smfmac_f32_16x16x64_bf16 v[24:27], v[28:31], a[168:175], v18\n" \
"s_waitcnt lgkmcnt(4)\nds_read_b128 v[28:31], v16 offset:39936\nv_smfmac_f32_16x16x64_bf16 v[104:107], v[36:39], a[32:39], v18\nds_read_b128 v[64:67], v17 offset:1024\n" \
"v_mov_b64 v[42:43], 0\nv_smfmac_f32_16x16x64_bf16 v[24:27], v[36:39], a[136:143], v18\ns_waitcnt lgkmcnt(4)\nds_read_b128 v[36:39], v16 offset:37888\n" \
"v_smfmac_f32_16x16x64_bf16 v[104:107], v[48:51], a[40:47], v18\nds_read_b128 v[84:87], v17 offset:2048\ns_and_b32 s16, s16, 7\nv_smfmac_f32_16x16x64_bf16 v[24:27], v[48:51], v[76:83], v18\n" \
"ds_read_b128 v[52:55], v16 offset:4096\ns_lshl_b32 s16, s16, 22\nv_smfmac_f32_16x16x64_bf16 v[104:107], v[56:59], a[64:71], v18\nds_read_b128 v[88:91], v17 offset:3072\n" \
"s_add_u32 s16, s16, s25\nv_smfmac_f32_16x16x64_bf16 v[24:27], v[56:59], a[160:167], v18\ns_waitcnt lgkmcnt(6)\nds_read_b128 v[48:51], v16 offset:38912\n" \
"v_smfmac_f32_16x16x64_bf16 v[104:107], v[32:35], v[0:7], v18\nds_read_b128 v[56:59], v16 offset:21504\ns_add_u32 s17, s16, 0x1000\nv_smfmac_f32_16x16x64_bf16 v[24:27], v[32:35], v[8:15], v18\n" \
"s_waitcnt lgkmcnt(6)\nds_read_b128 v[32:35], v16 offset:32768\nv_smfmac_f32_16x16x64_bf16 v[104:107], v[28:31], a[96:103], v18\nds_read_b128 v[76:79], v16 offset:54272\n" \
"s_add_u32 s18, s16, 0x4000\nv_smfmac_f32_16x16x64_bf16 v[24:27], v[28:31], a[192:199], v18\ns_waitcnt lgkmcnt(5)\nds_read_b128 v[28:31], v16 offset:22528\n" \
"v_smfmac_f32_16x16x64_bf16 v[104:107], v[36:39], a[80:87], v18\nds_read_b128 v[80:83], v16 offset:55296\nv_lshrrev_b32 v92, 16, v18\nv_smfmac_f32_16x16x64_bf16 v[24:27], v[36:39], a[176:183], v18\n" \
"ds_write_b128 v16, v[40:43] offset:45056\nv_smfmac_f32_16x16x64_bf16 v[104:107], v[52:55], v[60:67], v18\ns_add_u32 s19, s16, 0x5000\ns_and_b32 s43, s20, 2\n" \
"s_lshl_b32 s43, s43, 12\nv_smfmac_f32_16x16x64_bf16 v[24:27], v[52:55], a[128:135], v18\ns_waitcnt lgkmcnt(4)\nds_read_b128 v[36:39], v16 offset:23552\n" \
"v_smfmac_f32_16x16x64_bf16 v[104:107], v[48:51], a[88:95], v18\nds_read_b128 v[52:55], v16 offset:56320\ns_and_b32 s42, s20, 7\nv_smfmac_f32_16x16x64_bf16 v[24:27], v[48:51], a[184:191], v18\n" \
"ds_read_b128 v[60:63], v17 offset:10240\ns_lshl_b32 s42, s42, 20\nv_smfmac_f32_16x16x64_bf16 v[104:107], v[32:35], v[84:91], v18\nds_read_b128 v[64:67], v17 offset:11264\n" \
"s_add_u32 s42, s42, s24\nv_smfmac_f32_16x16x64_bf16 v[24:27], v[32:35], a[144:151], v18\ns_waitcnt lgkmcnt(4)\nv_mfma_f32_16x16x32_bf16 v[44:47], a[60:63], v[56:59], v[44:47]\n" \
"v_add_f32 v104, v104, v106\nv_add_f32 v105, v105, v107\nv_cvt_pk_bf16_f32 v104, v104, v105\nv_mfma_f32_16x16x32_bf16 v[68:71], a[60:63], v[76:79], v[68:71]\n" \
"v_add_f32 v24, v24, v26\nv_add_f32 v25, v25, v27\nv_cvt_pk_bf16_f32 v24, v24, v25\nv_mfma_f32_16x16x32_bf16 v[72:75], a[156:159], v[56:59], v[72:75]\n" \
"ds_write2_b32 v92, v104, v24 offset0:0 offset1:4\nv_mfma_f32_16x16x32_bf16 v[20:23], a[156:159], v[76:79], v[20:23]\ns_add_u32 s99, s20, 2\nv_mov_b64 v[24:25], 0\n" \
"v_mov_b64 v[26:27], 0\nv_mfma_f32_16x16x32_bf16 v[44:47], a[64:67], v[28:31], v[44:47]\ns_min_u32 s99, s99, 63\nv_bfe_u32 v32, v16, 4, 3\n" \
"v_lshlrev_b32 v32, 2, v32\nv_mfma_f32_16x16x32_bf16 v[68:71], a[64:67], v[80:83], v[68:71]\ns_waitcnt vmcnt(1) lgkmcnt(0)\ns_barrier\n" \
"ds_read_b64_tr_b16 v[48:49], v19 offset:0\nv_mfma_f32_16x16x32_bf16 v[72:75], a[160:163], v[28:31], v[72:75]\nds_read_b64_tr_b16 v[50:51], v19 offset:256\nds_read_b64_tr_b16 v[28:29], v19 offset:1024\n" \
"ds_read_b64_tr_b16 v[30:31], v19 offset:1280\nv_mfma_f32_16x16x32_bf16 v[20:23], a[160:163], v[80:83], v[20:23]\nds_read_b128 v[56:59], v16 offset:24576\nv_mov_b64 v[76:77], 0\n" \
"v_mfma_f32_16x16x32_bf16 v[44:47], a[68:71], v[36:39], v[44:47]\ns_lshl_b32 s99, s99, 5\ns_add_u32 s99, s99, s26\nv_mov_b64 v[78:79], 0\n" \
"v_mfma_f32_16x16x32_bf16 v[68:71], a[68:71], v[52:55], v[68:71]\ns_waitcnt lgkmcnt(3)\nbuffer_load_dword v33, v32, s[8:11], s99 offen sc1\nv_mfma_f32_16x16x32_bf16 v[72:75], a[164:167], v[36:39], v[72:75]\n" \
"ds_read_b128 v[80:83], v16 offset:25600\nv_mov_b64 v[34:35], 0\nv_smfmac_f32_16x16x64_bf16 v[40:43], v[48:51], a[200:207], v18\ns_waitcnt lgkmcnt(2)\n" \
"ds_read_b128 v[88:91], v17 offset:12288\nv_smfmac_f32_16x16x64_bf16 v[40:43], v[28:31], v[60:67], v18\nds_read_b128 v[84:87], v16 offset:45056\nv_mov_b64 v[36:37], 0\n" \
"v_smfmac_f32_16x16x64_bf16 v[24:27], v[48:51], a[224:231], v18\nds_read_b128 v[92:95], v17 offset:13312\nv_mfma_f32_16x16x32_bf16 v[20:23], a[164:167], v[52:55], v[20:23]\nds_read_b128 v[60:63], v17 offset:14336\n" \
"v_add_f32 v40, v40, v42\nv_smfmac_f32_16x16x64_bf16 v[24:27], v[28:31], a[232:239], v18\ns_waitcnt lgkmcnt(5)\nds_read_b128 v[52:55], v16 offset:45056\n" \
"v_mfma_f32_16x16x32_bf16 v[44:47], a[72:75], v[56:59], v[44:47]\nds_read_b128 v[64:67], v17 offset:15360\nv_add_f32 v41, v41, v43\nv_smfmac_f32_16x16x64_bf16 v[76:79], v[48:51], a[208:215], v18\n" \
"ds_read_b128 v[96:99], v16 offset:26624\nv_add_f32 v24, v24, v26\nv_smfmac_f32_16x16x64_bf16 v[76:79], v[28:31], a[216:223], v18\nds_read_b128 v[100:103], v16 offset:45056\n" \
"v_add_f32 v25, v25, v27\nv_smfmac_f32_16x16x64_bf16 v[34:37], v[48:51], a[240:247], v18\nds_read_b128 v[104:107], v17 offset:16384\nv_cvt_pk_bf16_f32 v38, v40, v41\n" \
"v_mfma_f32_16x16x32_bf16 v[72:75], a[168:171], v[56:59], v[72:75]\ns_waitcnt lgkmcnt(5)\nv_add_f32 v76, v76, v78\nv_add_f32 v77, v77, v79\n" \
"v_smfmac_f32_16x16x64_bf16 v[34:37], v[28:31], a[248:255], v18\nds_read_b128 v[56:59], v16 offset:45056\nv_cvt_pk_bf16_f32 v39, v24, v25\nv_smfmac_f32_16x16x64_bf16 v[84:87], v[48:51], v[112:119], v18\n" \
"ds_read_b128 v[108:111], v17 offset:17408\nv_cvt_pk_bf16_f32 v40, v76, v77\nv_mfma_f32_16x16x32_bf16 v[44:47], a[76:79], v[80:83], v[44:47]\ns_waitcnt lgkmcnt(4)\n" \
"v_permlane32_swap_b32 v38, v39\nv_smfmac_f32_16x16x64_bf16 v[84:87], v[28:31], v[120:127], v18\nds_read_b128 v[24:27], v16 offset:45056\nv_add_f32 v34, v34, v36\n" \
"v_smfmac_f32_16x16x64_bf16 v[52:55], v[48:51], v[128:135], v18\nds_read_b128 v[76:79], v16 offset:27648\nv_add_f32 v35, v35, v37\nv_cvt_pk_bf16_f32 v41, v34, v35\n" \
"v_smfmac_f32_16x16x64_bf16 v[52:55], v[28:31], v[136:143], v18\nds_read_b128 v[34:37], v17 offset:18432\nv_permlane32_swap_b32 v40, v41\nv_mfma_f32_16x16x32_bf16 v[72:75], a[172:175], v[80:83], v[72:75]\n" \
"s_waitcnt lgkmcnt(4)\nv_permlane16_swap_b32 v38, v40\nv_smfmac_f32_16x16x64_bf16 v[100:103], v[48:51], v[88:95], v18\nds_read_b128 v[80:83], v16 offset:45056\n" \
"v_permlane16_swap_b32 v39, v41\nv_and_or_b32 v38, v38, -2, s32\nv_smfmac_f32_16x16x64_bf16 v[100:103], v[28:31], v[60:67], v18\nds_read_b128 v[88:91], v16 offset:28672\n" \
"v_add_f32 v84, v84, v86\nv_mfma_f32_16x16x32_bf16 v[44:47], a[80:83], v[96:99], v[44:47]\nbuffer_store_dwordx4 v[38:41], v16, s[8:11], s29 offen offset:0 sc1\nv_add_f32 v85, v85, v87\n" \
"v_add_f32 v52, v52, v54\nv_add_f32 v53, v53, v55\nv_smfmac_f32_16x16x64_bf16 v[56:59], v[48:51], v[144:151], v18\nds_read_b128 v[60:63], v16 offset:45056\n" \
"v_add_f32 v100, v100, v102\nv_smfmac_f32_16x16x64_bf16 v[56:59], v[28:31], v[152:159], v18\nds_read_b128 v[38:41], v17 offset:19456\nv_add_f32 v101, v101, v103\n" \
"v_mfma_f32_16x16x32_bf16 v[72:75], a[176:179], v[96:99], v[72:75]\ns_waitcnt lgkmcnt(3)\nv_cvt_pk_bf16_f32 v64, v84, v85\nv_cvt_pk_bf16_f32 v65, v52, v53\n" \
"v_smfmac_f32_16x16x64_bf16 v[24:27], v[48:51], v[160:167], v18\nds_read_b128 v[52:55], v16 offset:45056\nv_add_f32 v56, v56, v58\nv_smfmac_f32_16x16x64_bf16 v[24:27], v[28:31], v[168:175], v18\n" \
"ds_read_b128 v[84:87], v16 offset:45056\nv_add_f32 v57, v57, v59\nv_mfma_f32_16x16x32_bf16 v[44:47], a[84:87], v[76:79], v[44:47]\nv_permlane32_swap_b32 v64, v65\n" \
"v_cvt_pk_bf16_f32 v66, v100, v101\nv_cvt_pk_bf16_f32 v67, v56, v57\nv_smfmac_f32_16x16x64_bf16 v[80:83], v[48:51], v[184:191], v18\nds_read_b128 v[56:59], v16 offset:29696\n" \
"v_permlane32_swap_b32 v66, v67\nv_smfmac_f32_16x16x64_bf16 v[80:83], v[28:31], v[192:199], v18\ns_waitcnt lgkmcnt(3)\nv_permlane16_swap_b32 v64, v66\n" \
"v_mfma_f32_16x16x32_bf16 v[72:75], a[180:183], v[76:79], v[72:75]\nv_permlane16_swap_b32 v65, v67\nv_and_or_b32 v64, v64, -2, s32\nv_smfmac_f32_16x16x64_bf16 v[60:63], v[48:51], v[104:111], v18\n" \
"ds_read_b128 v[76:79], v16 offset:45056\nv_add_f32 v24, v24, v26\nv_smfmac_f32_16x16x64_bf16 v[60:63], v[28:31], v[176:183], v18\ns_waitcnt lgkmcnt(2)\n" \
"buffer_store_dwordx4 v[64:67], v16, s[8:11], s29 offen offset:1024 sc1\nds_read_b128 v[92:95], v17 offset:20480\nv_add_f32 v25, v25, v27\nv_mfma_f32_16x16x32_bf16 v[44:47], a[88:91], v[88:91], v[44:47]\n" \
"ds_read_b128 v[96:99], v17 offset:21504\nv_add_f32 v80, v80, v82\nv_smfmac_f32_16x16x64_bf16 v[52:55], v[48:51], v[200:207], v18\nds_read_b128 v[64:67], v16 offset:45056\n" \
"v_add_f32 v81, v81, v83\nv_smfmac_f32_16x16x64_bf16 v[52:55], v[28:31], v[34:41], v18\nds_read_b128 v[100:103], v16 offset:30720\nv_add_f32 v60, v60, v62\n" \
"v_smfmac_f32_16x16x64_bf16 v[84:87], v[48:51], v[208:215], v18\nds_read_b128 v[34:37], v16 offset:45056\nv_add_f32 v61, v61, v63\nv_mfma_f32_16x16x32_bf16 v[72:75], a[184:187], v[88:91], v[72:75]\n" \
"s_waitcnt lgkmcnt(5)\nv_add_f32 v52, v52, v54\nv_add_f32 v53, v53, v55\nv_smfmac_f32_16x16x64_bf16 v[84:87], v[28:31], v[216:223], v18\n" \
"ds_read_b128 v[104:107], v17 offset:22528\nv_cvt_pk_bf16_f32 v38, v24, v25\nv_mfma_f32_16x16x32_bf16 v[44:47], a[92:95], v[56:59], v[44:47]\nv_cvt_pk_bf16_f32 v39, v80, v81\n" \
"v_cvt_pk_bf16_f32 v40, v60, v61\nv_cvt_pk_bf16_f32 v41, v52, v53\nv_smfmac_f32_16x16x64_bf16 v[76:79], v[48:51], v[232:239], v18\nds_read_b128 v[108:111], v17 offset:23552\n" \
"v_permlane32_swap_b32 v38, v39\nv_permlane32_swap_b32 v40, v41\nv_smfmac_f32_16x16x64_bf16 v[76:79], v[28:31], v[240:247], v18\nds_read_b128 v[24:27], v16 offset:31744\n" \
"v_permlane16_swap_b32 v38, v40\nv_mfma_f32_16x16x32_bf16 v[72:75], a[188:191], v[56:59], v[72:75]\ns_waitcnt lgkmcnt(3)\nv_permlane16_swap_b32 v39, v41\n" \
"v_and_or_b32 v38, v38, -2, s32\nv_smfmac_f32_16x16x64_bf16 v[64:67], v[48:51], v[92:99], v18\nds_read_b128 v[52:55], v16 offset:57344\nv_add_f32 v84, v84, v86\n" \
"v_smfmac_f32_16x16x64_bf16 v[64:67], v[28:31], v[224:231], v18\nds_read_b128 v[56:59], v16 offset:58368\nv_add_f32 v85, v85, v87\nv_mfma_f32_16x16x32_bf16 v[44:47], a[96:99], v[100:103], v[44:47]\n" \
"s_waitcnt lgkmcnt(3)\nbuffer_store_dwordx4 v[38:41], v16, s[8:11], s29 offen offset:2048 sc1\nv_add_f32 v76, v76, v78\nv_add_f32 v77, v77, v79\n" \
"v_cvt_pk_bf16_f32 v38, v84, v85\nv_smfmac_f32_16x16x64_bf16 v[34:37], v[48:51], v[104:111], v18\nds_read_b128 v[60:63], v16 offset:59392\nv_add_f32 v64, v64, v66\n" \
"v_add_f32 v65, v65, v67\nv_cvt_pk_bf16_f32 v39, v76, v77\nv_cvt_pk_bf16_f32 v40, v64, v65\nv_smfmac_f32_16x16x64_bf16 v[34:37], v[28:31], v[248:255], v18\n" \
"ds_read_b128 v[48:51], v16 offset:60416\nv_permlane32_swap_b32 v38, v39\nv_mfma_f32_16x16x32_bf16 v[72:75], a[192:195], v[100:103], v[72:75]\ns_waitcnt lgkmcnt(3)\n" \
"ds_read_b128 v[28:31], v16 offset:61440\nv_mfma_f32_16x16x32_bf16 v[44:47], a[100:103], v[24:27], v[44:47]\nv_add_f32 v34, v34, v36\nv_add_f32 v35, v35, v37\n" \
"v_cvt_pk_bf16_f32 v41, v34, v35\nv_mfma_f32_16x16x32_bf16 v[72:75], a[196:199], v[24:27], v[72:75]\nds_read_b128 v[34:37], v16 offset:62464\nv_permlane32_swap_b32 v40, v41\n" \
"v_mfma_f32_16x16x32_bf16 v[68:71], a[72:75], v[52:55], v[68:71]\ns_waitcnt lgkmcnt(3)\nv_permlane16_swap_b32 v38, v40\nv_permlane16_swap_b32 v39, v41\n" \
"v_and_or_b32 v38, v38, -2, s32\nv_mfma_f32_16x16x32_bf16 v[20:23], a[168:171], v[52:55], v[20:23]\nbuffer_store_dwordx4 v[38:41], v16, s[8:11], s29 offen offset:3072 sc1\ns_bfe_u32 s32, s20, 0x10003\n" \
"v_cvt_pk_bf16_f32 v44, v44, v45\nv_cvt_pk_bf16_f32 v45, v46, v47\nv_mfma_f32_16x16x32_bf16 v[68:71], a[76:79], v[56:59], v[68:71]\ns_xor_b32 s29, s42, s43\n" \
"v_cvt_pk_bf16_f32 v46, v72, v73\nv_cvt_pk_bf16_f32 v47, v74, v75\nv_mfma_f32_16x16x32_bf16 v[20:23], a[172:175], v[56:59], v[20:23]\nds_read_b128 v[24:27], v16 offset:63488\n" \
"v_mfma_f32_16x16x32_bf16 v[68:71], a[80:83], v[60:63], v[68:71]\ns_waitcnt vmcnt(4) lgkmcnt(1)\nv_cmp_eq_u32 s[42:43], -1, v33\nv_mfma_f32_16x16x32_bf16 v[20:23], a[176:179], v[60:63], v[20:23]\n" \
"ds_read_b128 v[38:41], v16 offset:64512\nv_mfma_f32_16x16x32_bf16 v[68:71], a[84:87], v[48:51], v[68:71]\ns_cmp_eq_u64 s[42:43], 0\ns_cselect_b32 s14, s10, 0\n" \
"s_cmp_lt_i32 s20, 62\ns_cselect_b32 s14, s14, 0\ns_cmp_eq_u32 s14, 0\ns_cbranch_scc1 4f\n" \
"5:\nv_add_u32 v32, s28, v16\nv_mfma_f32_16x16x32_bf16 v[20:23], a[180:183], v[48:51], v[20:23]\nds_write_b128 v32, v[44:47] offset:0\n" \
"s_add_u32 m0, s22, 0\nv_mfma_f32_16x16x32_bf16 v[68:71], a[88:91], v[28:31], v[68:71]\nbuffer_load_dwordx4 v16, s[12:15], s21 offen offset:0 sc1 lds\nv_mfma_f32_16x16x32_bf16 v[20:23], a[184:187], v[28:31], v[20:23]\n" \
"ds_read_b128 v[42:45], v17 offset:10240\ns_add_u32 s20, s20, 1\nv_mfma_f32_16x16x32_bf16 v[68:71], a[92:95], v[34:37], v[68:71]\ns_waitcnt lgkmcnt(1)\n" \
"s_barrier\nds_read_b128 v[28:31], v16 offset:8192\nv_mfma_f32_16x16x32_bf16 v[20:23], a[188:191], v[34:37], v[20:23]\nds_read_b128 v[48:51], v16 offset:9216\n" \
"v_mfma_f32_16x16x32_bf16 v[68:71], a[96:99], v[24:27], v[68:71]\nds_read_b128 v[64:67], v16 offset:10240\nv_add_u32 v32, s28, v16\nv_mfma_f32_16x16x32_bf16 v[20:23], a[192:195], v[24:27], v[20:23]\n" \
"buffer_load_dwordx4 v16, s[12:15], s21 offen offset:1024 sc1 lds\nds_read_b128 v[72:75], v16 offset:11264\nds_read_b128 v[24:27], v17 offset:11264\nv_mfma_f32_16x16x32_bf16 v[68:71], a[100:103], v[38:41], v[68:71]\n" \
"s_waitcnt lgkmcnt(4)\nv_mfma_f32_16x16x32_bf16 v[20:23], a[196:199], v[38:41], v[20:23]\ns_waitcnt lgkmcnt(3)\nv_mfma_f32_16x16x32_bf16 v[34:37], v[28:31], a[200:203], 0\n" \
"s_waitcnt lgkmcnt(2)\nv_cvt_pk_bf16_f32 v68, v68, v69\nv_cvt_pk_bf16_f32 v69, v70, v71\nv_mfma_f32_16x16x32_bf16 v[34:37], v[48:51], a[204:207], v[34:37]\n" \
"s_waitcnt lgkmcnt(0)\nv_cvt_pk_bf16_f32 v70, v20, v21\nv_cvt_pk_bf16_f32 v71, v22, v23\nv_mfma_f32_16x16x32_bf16 v[34:37], v[64:67], v[42:45], v[34:37]\n" \
"buffer_load_dwordx4 v16, s[12:15], s21 offen offset:2048 sc1 lds\nv_mfma_f32_16x16x32_bf16 v[34:37], v[72:75], v[24:27], v[34:37]\nds_write_b128 v32, v[68:71] offset:32768\nv_mfma_f32_16x16x32_bf16 v[20:23], v[28:31], a[208:211], 0\n" \
"v_mfma_f32_16x16x32_bf16 v[20:23], v[48:51], a[212:215], v[20:23]\nv_cvt_pk_bf16_f32 v34, v34, v35\nv_cvt_pk_bf16_f32 v35, v36, v37\nv_mfma_f32_16x16x32_bf16 v[20:23], v[64:67], a[216:219], v[20:23]\n" \
"s_add_u32 s43, s21, 0x4000\nds_read_b128 v[24:27], v17 offset:18432\nv_mfma_f32_16x16x32_bf16 v[20:23], v[72:75], a[220:223], v[20:23]\ns_waitcnt lgkmcnt(1)\n" \
"s_barrier\nv_mfma_f32_16x16x32_bf16 v[38:41], v[28:31], a[224:227], 0\nbuffer_load_dwordx4 v16, s[12:15], s21 offen offset:3072 sc1 lds\nv_mfma_f32_16x16x32_bf16 v[38:41], v[48:51], a[228:231], v[38:41]\n" \
"s_add_u32 m0, s22, 0x4000\nds_read_b128 v[52:55], v17 offset:19456\nv_mfma_f32_16x16x32_bf16 v[38:41], v[64:67], a[232:235], v[38:41]\nbuffer_load_dwordx4 v16, s[12:15], s43 offen offset:0 sc1 lds\n" \
"v_mfma_f32_16x16x32_bf16 v[38:41], v[72:75], a[236:239], v[38:41]\nds_read_b128 v[68:71], v16 offset:40960\nv_cvt_pk_bf16_f32 v20, v20, v21\nv_mfma_f32_16x16x32_bf16 v[56:59], v[28:31], v[144:147], 0\n" \
"ds_read_b128 v[76:79], v16 offset:41984\nv_cvt_pk_bf16_f32 v21, v22, v23\nv_mfma_f32_16x16x32_bf16 v[56:59], v[48:51], v[148:151], v[56:59]\nds_read_b128 v[80:83], v16 offset:43008\n" \
"v_cvt_pk_bf16_f32 v38, v38, v39\nv_mfma_f32_16x16x32_bf16 v[56:59], v[64:67], v[152:155], v[56:59]\nds_read_b128 v[60:63], v17 offset:10240\nv_cvt_pk_bf16_f32 v39, v40, v41\n" \
"v_mfma_f32_16x16x32_bf16 v[56:59], v[72:75], v[156:159], v[56:59]\nbuffer_load_dwordx4 v16, s[12:15], s43 offen offset:1024 sc1 lds\nv_mfma_f32_16x16x32_bf16 v[84:87], v[28:31], v[200:203], 0\ns_waitcnt lgkmcnt(4)\n" \
"ds_read_b128 v[44:47], v16 offset:44032\nv_mfma_f32_16x16x32_bf16 v[84:87], v[48:51], v[204:207], v[84:87]\nds_read_b128 v[40:43], v17 offset:11264\nv_cvt_pk_bf16_f32 v56, v56, v57\n" \
"v_mfma_f32_16x16x32_bf16 v[84:87], v[64:67], v[24:27], v[84:87]\ns_waitcnt lgkmcnt(4)\nv_cvt_pk_bf16_f32 v57, v58, v59\nv_mfma_f32_16x16x32_bf16 v[84:87], v[72:75], v[52:55], v[84:87]\n" \
"v_mfma_f32_16x16x32_bf16 v[22:25], v[68:71], a[200:203], 0\ns_waitcnt lgkmcnt(2)\nbuffer_load_dwordx4 v16, s[12:15], s43 offen offset:2048 sc1 lds\nv_mfma_f32_16x16x32_bf16 v[22:25], v[76:79], a[204:207], v[22:25]\n" \
"v_cvt_pk_bf16_f32 v84, v84, v85\nv_cvt_pk_bf16_f32 v85, v86, v87\nv_mfma_f32_16x16x32_bf16 v[22:25], v[80:83], v[60:63], v[22:25]\ns_waitcnt lgkmcnt(0)\n" \
"v_mfma_f32_16x16x32_bf16 v[22:25], v[44:47], v[40:43], v[22:25]\nv_mfma_f32_16x16x32_bf16 v[52:55], v[68:71], a[208:211], 0\nbuffer_load_dwordx4 v16, s[12:15], s43 offen offset:3072 sc1 lds\nv_mfma_f32_16x16x32_bf16 v[52:55], v[76:79], a[212:215], v[52:55]\n" \
"v_cvt_pk_bf16_f32 v36, v22, v23\nv_cvt_pk_bf16_f32 v37, v24, v25\nv_mfma_f32_16x16x32_bf16 v[52:55], v[80:83], a[216:219], v[52:55]\nds_read_b128 v[24:27], v17 offset:18432\n" \
"v_mfma_f32_16x16x32_bf16 v[52:55], v[44:47], a[220:223], v[52:55]\nds_read_b128 v[60:63], v17 offset:19456\nv_mfma_f32_16x16x32_bf16 v[86:89], v[68:71], a[224:227], 0\nv_mfma_f32_16x16x32_bf16 v[86:89], v[76:79], a[228:231], v[86:89]\n" \
"buffer_store_dwordx4 v[34:37], v16, s[8:11], s16 offen offset:0 sc1\nv_cvt_pk_bf16_f32 v22, v52, v53\nv_cvt_pk_bf16_f32 v23, v54, v55\nv_mfma_f32_16x16x32_bf16 v[86:89], v[80:83], a[232:235], v[86:89]\n" \
"v_mfma_f32_16x16x32_bf16 v[86:89], v[44:47], a[236:239], v[86:89]\nv_mfma_f32_16x16x32_bf16 v[32:35], v[68:71], v[144:147], 0\nv_mfma_f32_16x16x32_bf16 v[32:35], v[76:79], v[148:151], v[32:35]\nv_cvt_pk_bf16_f32 v40, v86, v87\n" \
"v_cvt_pk_bf16_f32 v41, v88, v89\nv_mfma_f32_16x16x32_bf16 v[32:35], v[80:83], v[152:155], v[32:35]\nv_mfma_f32_16x16x32_bf16 v[32:35], v[44:47], v[156:159], v[32:35]\nv_mfma_f32_16x16x32_bf16 v[52:55], v[68:71], v[200:203], 0\n" \
"s_waitcnt lgkmcnt(0)\nbuffer_store_dwordx4 v[20:23], v16, s[8:11], s16 offen offset:1024 sc1\nv_mfma_f32_16x16x32_bf16 v[52:55], v[76:79], v[204:207], v[52:55]\nv_cvt_pk_bf16_f32 v58, v32, v33\n" \
"v_cvt_pk_bf16_f32 v59, v34, v35\nv_mfma_f32_16x16x32_bf16 v[52:55], v[80:83], v[24:27], v[52:55]\nv_mfma_f32_16x16x32_bf16 v[52:55], v[44:47], v[60:63], v[52:55]\nv_mfma_f32_16x16x32_bf16 v[20:23], v[28:31], a[240:243], 0\n" \
"v_mfma_f32_16x16x32_bf16 v[24:27], v[68:71], a[240:243], 0\nv_cvt_pk_bf16_f32 v86, v52, v53\nv_cvt_pk_bf16_f32 v87, v54, v55\nv_mfma_f32_16x16x32_bf16 v[20:23], v[48:51], a[244:247], v[20:23]\n" \
"v_mfma_f32_16x16x32_bf16 v[24:27], v[76:79], a[244:247], v[24:27]\nv_mfma_f32_16x16x32_bf16 v[20:23], v[64:67], a[248:251], v[20:23]\nbuffer_store_dwordx4 v[38:41], v16, s[8:11], s16 offen offset:2048 sc1\nds_read_b128 v[32:35], v17 offset:12288\n" \
"v_mfma_f32_16x16x32_bf16 v[24:27], v[80:83], a[248:251], v[24:27]\nv_mfma_f32_16x16x32_bf16 v[20:23], v[72:75], a[252:255], v[20:23]\nds_read_b128 v[36:39], v17 offset:13312\nv_mfma_f32_16x16x32_bf16 v[24:27], v[44:47], a[252:255], v[24:27]\n" \
"v_mfma_f32_16x16x32_bf16 v[40:43], v[28:31], v[128:131], 0\nds_read_b128 v[52:55], v17 offset:14336\nv_cvt_pk_bf16_f32 v20, v20, v21\nv_mfma_f32_16x16x32_bf16 v[60:63], v[68:71], v[128:131], 0\n" \
"v_cvt_pk_bf16_f32 v21, v22, v23\nv_cvt_pk_bf16_f32 v22, v24, v25\nv_mfma_f32_16x16x32_bf16 v[40:43], v[48:51], v[132:135], v[40:43]\ns_waitcnt vmcnt(7)\n" \
"s_barrier\nv_mfma_f32_16x16x32_bf16 v[60:63], v[76:79], v[132:135], v[60:63]\nbuffer_store_dwordx4 v[56:59], v16, s[8:11], s17 offen offset:3072 sc1\nds_read_b128 v[88:91], v17 offset:15360\n" \
"v_cvt_pk_bf16_f32 v23, v26, v27\nv_mfma_f32_16x16x32_bf16 v[40:43], v[64:67], v[136:139], v[40:43]\nv_mfma_f32_16x16x32_bf16 v[60:63], v[80:83], v[136:139], v[60:63]\nv_mfma_f32_16x16x32_bf16 v[40:43], v[72:75], v[140:143], v[40:43]\n" \
"s_waitcnt lgkmcnt(1)\nv_mfma_f32_16x16x32_bf16 v[60:63], v[44:47], v[140:143], v[60:63]\nv_mfma_f32_16x16x32_bf16 v[24:27], v[28:31], v[32:35], 0\nv_cvt_pk_bf16_f32 v40, v40, v41\n" \
"v_cvt_pk_bf16_f32 v41, v42, v43\nv_mfma_f32_16x16x32_bf16 v[56:59], v[68:71], v[32:35], 0\nv_cvt_pk_bf16_f32 v42, v60, v61\nv_cvt_pk_bf16_f32 v43, v62, v63\n" \
"v_mfma_f32_16x16x32_bf16 v[24:27], v[48:51], v[36:39], v[24:27]\nv_mfma_f32_16x16x32_bf16 v[56:59], v[76:79], v[36:39], v[56:59]\nbuffer_store_dwordx4 v[84:87], v16, s[8:11], s18 offen offset:3072 sc1\ns_add_u32 m0, s22, 0x2000\n" \
"s_add_u32 s43, s21, 0x2000\ns_add_u32 s21, s21, 0x10000\nv_mfma_f32_16x16x32_bf16 v[24:27], v[64:67], v[52:55], v[24:27]\ns_waitcnt lgkmcnt(0)\n" \
"ds_read_b128 v[32:35], v17 offset:16384\nv_mfma_f32_16x16x32_bf16 v[56:59], v[80:83], v[52:55], v[56:59]\nv_mfma_f32_16x16x32_bf16 v[24:27], v[72:75], v[88:91], v[24:27]\nds_read_b128 v[36:39], v17 offset:17408\n" \
"v_mfma_f32_16x16x32_bf16 v[56:59], v[44:47], v[88:91], v[56:59]\nbuffer_load_dwordx4 v16, s[12:15], s43 offen offset:0 sc1 lds\nv_mfma_f32_16x16x32_bf16 v[52:55], v[28:31], v[112:115], 0\nv_cvt_pk_bf16_f32 v24, v24, v25\n" \
"v_cvt_pk_bf16_f32 v25, v26, v27\nv_mfma_f32_16x16x32_bf16 v[60:63], v[68:71], v[112:115], 0\nv_cvt_pk_bf16_f32 v26, v56, v57\nv_cvt_pk_bf16_f32 v27, v58, v59\n" \
"v_mfma_f32_16x16x32_bf16 v[52:55], v[48:51], v[116:119], v[52:55]\nbuffer_store_dwordx4 v[20:23], v16, s[8:11], s16 offen offset:3072 sc1\nv_mfma_f32_16x16x32_bf16 v[60:63], v[76:79], v[116:119], v[60:63]\nv_mfma_f32_16x16x32_bf16 v[52:55], v[64:67], v[120:123], v[52:55]\n" \
"v_mfma_f32_16x16x32_bf16 v[60:63], v[80:83], v[120:123], v[60:63]\nv_mfma_f32_16x16x32_bf16 v[52:55], v[72:75], v[124:127], v[52:55]\ns_waitcnt lgkmcnt(0)\nv_mfma_f32_16x16x32_bf16 v[60:63], v[44:47], v[124:127], v[60:63]\n" \
"v_mfma_f32_16x16x32_bf16 v[20:23], v[28:31], v[32:35], 0\nv_cvt_pk_bf16_f32 v52, v52, v53\nv_cvt_pk_bf16_f32 v53, v54, v55\nv_mfma_f32_16x16x32_bf16 v[56:59], v[68:71], v[32:35], 0\n" \
"buffer_store_dwordx4 v[40:43], v16, s[8:11], s17 offen offset:2048 sc1\nbuffer_load_dwordx4 v16, s[12:15], s43 offen offset:1024 sc1 lds\nv_cvt_pk_bf16_f32 v54, v60, v61\nv_mfma_f32_16x16x32_bf16 v[20:23], v[48:51], v[36:39], v[20:23]\n" \
"v_cvt_pk_bf16_f32 v55, v62, v63\nv_mfma_f32_16x16x32_bf16 v[56:59], v[76:79], v[36:39], v[56:59]\nv_mfma_f32_16x16x32_bf16 v[20:23], v[64:67], v[176:179], v[20:23]\nv_mfma_f32_16x16x32_bf16 v[56:59], v[80:83], v[176:179], v[56:59]\n" \
"v_mfma_f32_16x16x32_bf16 v[20:23], v[72:75], v[180:183], v[20:23]\nv_mfma_f32_16x16x32_bf16 v[56:59], v[44:47], v[180:183], v[56:59]\nv_mfma_f32_16x16x32_bf16 v[32:35], v[28:31], v[184:187], 0\nv_cvt_pk_bf16_f32 v20, v20, v21\n" \
"v_cvt_pk_bf16_f32 v21, v22, v23\nv_mfma_f32_16x16x32_bf16 v[36:39], v[68:71], v[184:187], 0\nbuffer_store_dwordx4 v[24:27], v16, s[8:11], s17 offen offset:1024 sc1\nv_cvt_pk_bf16_f32 v22, v56, v57\n" \
"v_cvt_pk_bf16_f32 v23, v58, v59\nv_mfma_f32_16x16x32_bf16 v[32:35], v[48:51], v[188:191], v[32:35]\nv_mfma_f32_16x16x32_bf16 v[36:39], v[76:79], v[188:191], v[36:39]\nbuffer_load_dwordx4 v16, s[12:15], s43 offen offset:2048 sc1 lds\n" \
"v_mfma_f32_16x16x32_bf16 v[32:35], v[64:67], v[192:195], v[32:35]\nv_mfma_f32_16x16x32_bf16 v[36:39], v[80:83], v[192:195], v[36:39]\nv_mfma_f32_16x16x32_bf16 v[32:35], v[72:75], v[196:199], v[32:35]\nv_mfma_f32_16x16x32_bf16 v[36:39], v[44:47], v[196:199], v[36:39]\n" \
"v_mfma_f32_16x16x32_bf16 v[24:27], v[28:31], v[160:163], 0\nbuffer_store_dwordx4 v[52:55], v16, s[8:11], s17 offen offset:0 sc1\nv_cvt_pk_bf16_f32 v32, v32, v33\nv_cvt_pk_bf16_f32 v33, v34, v35\n" \
"v_mfma_f32_16x16x32_bf16 v[40:43], v[68:71], v[160:163], 0\nv_cvt_pk_bf16_f32 v34, v36, v37\nv_cvt_pk_bf16_f32 v35, v38, v39\nv_mfma_f32_16x16x32_bf16 v[24:27], v[48:51], v[164:167], v[24:27]\n" \
"v_mfma_f32_16x16x32_bf16 v[40:43], v[76:79], v[164:167], v[40:43]\nv_mfma_f32_16x16x32_bf16 v[24:27], v[64:67], v[168:171], v[24:27]\nds_read_b128 v[36:39], v17 offset:20480\nv_mfma_f32_16x16x32_bf16 v[40:43], v[80:83], v[168:171], v[40:43]\n" \
"buffer_load_dwordx4 v16, s[12:15], s43 offen offset:3072 sc1 lds\nv_mfma_f32_16x16x32_bf16 v[24:27], v[72:75], v[172:175], v[24:27]\nds_read_b128 v[52:55], v17 offset:21504\nv_mfma_f32_16x16x32_bf16 v[40:43], v[44:47], v[172:175], v[40:43]\n" \
"v_mfma_f32_16x16x32_bf16 v[60:63], v[28:31], v[232:235], 0\nbuffer_store_dwordx4 v[20:23], v16, s[8:11], s18 offen offset:1024 sc1\nv_cvt_pk_bf16_f32 v24, v24, v25\nv_cvt_pk_bf16_f32 v25, v26, v27\n" \
"v_mfma_f32_16x16x32_bf16 v[20:23], v[68:71], v[232:235], 0\nv_cvt_pk_bf16_f32 v26, v40, v41\nv_cvt_pk_bf16_f32 v27, v42, v43\nv_mfma_f32_16x16x32_bf16 v[60:63], v[48:51], v[236:239], v[60:63]\n" \
"v_mfma_f32_16x16x32_bf16 v[20:23], v[76:79], v[236:239], v[20:23]\nv_mfma_f32_16x16x32_bf16 v[60:63], v[64:67], v[240:243], v[60:63]\nv_mfma_f32_16x16x32_bf16 v[20:23], v[80:83], v[240:243], v[20:23]\nv_mfma_f32_16x16x32_bf16 v[60:63], v[72:75], v[244:247], v[60:63]\n" \
"s_waitcnt lgkmcnt(0)\nv_mfma_f32_16x16x32_bf16 v[20:23], v[44:47], v[244:247], v[20:23]\nbuffer_store_dwordx4 v[32:35], v16, s[8:11], s18 offen offset:2048 sc1\nv_mfma_f32_16x16x32_bf16 v[84:87], v[28:31], v[36:39], 0\n" \
"v_cvt_pk_bf16_f32 v60, v60, v61\nv_cvt_pk_bf16_f32 v61, v62, v63\nv_mfma_f32_16x16x32_bf16 v[32:35], v[68:71], v[36:39], 0\nv_cvt_pk_bf16_f32 v62, v20, v21\n" \
"v_cvt_pk_bf16_f32 v63, v22, v23\nv_mfma_f32_16x16x32_bf16 v[84:87], v[48:51], v[52:55], v[84:87]\nv_mfma_f32_16x16x32_bf16 v[32:35], v[76:79], v[52:55], v[32:35]\nv_mfma_f32_16x16x32_bf16 v[84:87], v[64:67], v[224:227], v[84:87]\n" \
"ds_read_b128 v[52:55], v17 offset:22528\nv_mfma_f32_16x16x32_bf16 v[32:35], v[80:83], v[224:227], v[32:35]\nv_mfma_f32_16x16x32_bf16 v[84:87], v[72:75], v[228:231], v[84:87]\nbuffer_store_dwordx4 v[24:27], v16, s[8:11], s18 offen offset:0 sc1\n" \
"ds_read_b128 v[88:91], v17 offset:23552\nv_mfma_f32_16x16x32_bf16 v[32:35], v[44:47], v[228:231], v[32:35]\nv_mfma_f32_16x16x32_bf16 v[56:59], v[28:31], v[208:211], 0\nv_cvt_pk_bf16_f32 v84, v84, v85\n" \
"v_cvt_pk_bf16_f32 v85, v86, v87\nv_mfma_f32_16x16x32_bf16 v[24:27], v[68:71], v[208:211], 0\nv_cvt_pk_bf16_f32 v86, v32, v33\nv_cvt_pk_bf16_f32 v87, v34, v35\n" \
"v_mfma_f32_16x16x32_bf16 v[56:59], v[48:51], v[212:215], v[56:59]\nv_mfma_f32_16x16x32_bf16 v[24:27], v[76:79], v[212:215], v[24:27]\nds_read_b128 v[20:23], v16 offset:0\nv_mfma_f32_16x16x32_bf16 v[56:59], v[64:67], v[216:219], v[56:59]\n" \
"ds_read_b128 v[40:43], v16 offset:32768\nv_mfma_f32_16x16x32_bf16 v[24:27], v[80:83], v[216:219], v[24:27]\nv_mfma_f32_16x16x32_bf16 v[56:59], v[72:75], v[220:223], v[56:59]\ns_waitcnt lgkmcnt(2)\n" \
"buffer_store_dwordx4 v[60:63], v16, s[8:11], s19 offen offset:2048 sc1\nv_mfma_f32_16x16x32_bf16 v[24:27], v[44:47], v[220:223], v[24:27]\nds_read_b128 v[36:39], v16 offset:1024\nv_mfma_f32_16x16x32_bf16 v[60:63], v[28:31], v[52:55], 0\n" \
"ds_read_b128 v[32:35], v16 offset:33792\nv_cvt_pk_bf16_f32 v56, v56, v57\nv_mfma_f32_16x16x32_bf16 v[28:31], v[68:71], v[52:55], 0\nv_cvt_pk_bf16_f32 v57, v58, v59\n" \
"v_cvt_pk_bf16_f32 v58, v24, v25\nv_mfma_f32_16x16x32_bf16 v[60:63], v[48:51], v[88:91], v[60:63]\nv_cvt_pk_bf16_f32 v59, v26, v27\nv_mfma_f32_16x16x32_bf16 v[28:31], v[76:79], v[88:91], v[28:31]\n" \
"ds_read_b128 v[52:55], v16 offset:2048\nv_mfma_f32_16x16x32_bf16 v[60:63], v[64:67], v[248:251], v[60:63]\nds_read_b128 v[24:27], v16 offset:34816\nv_mfma_f32_16x16x32_bf16 v[28:31], v[80:83], v[248:251], v[28:31]\n" \
"buffer_store_dwordx4 v[84:87], v16, s[8:11], s19 offen offset:1024 sc1\nv_mfma_f32_16x16x32_bf16 v[60:63], v[72:75], v[252:255], v[60:63]\ns_waitcnt lgkmcnt(2)\nv_mfma_f32_16x16x32_bf16 v[28:31], v[44:47], v[252:255], v[28:31]\n" \
"ds_read_b128 v[48:51], v16 offset:3072\nv_mfma_f32_16x16x32_bf16 v[44:47], v[0:3], v[20:23], 0\nds_read_b128 v[64:67], v16 offset:35840\nv_cvt_pk_bf16_f32 v60, v60, v61\n" \
"v_mfma_f32_16x16x32_bf16 v[68:71], v[0:3], v[40:43], 0\nv_cvt_pk_bf16_f32 v61, v62, v63\nv_cvt_pk_bf16_f32 v62, v28, v29\nv_mfma_f32_16x16x32_bf16 v[72:75], v[8:11], v[20:23], 0\n" \
"v_cvt_pk_bf16_f32 v63, v30, v31\nv_mfma_f32_16x16x32_bf16 v[20:23], v[8:11], v[40:43], 0\nds_read_b128 v[28:31], v16 offset:4096\nv_mfma_f32_16x16x32_bf16 v[44:47], v[4:7], v[36:39], v[44:47]\n" \
"ds_read_b128 v[40:43], v16 offset:36864\nv_mfma_f32_16x16x32_bf16 v[68:71], v[4:7], v[32:35], v[68:71]\nbuffer_store_dwordx4 v[56:59], v16, s[8:11], s19 offen offset:0 sc1\nv_mfma_f32_16x16x32_bf16 v[72:75], v[12:15], v[36:39], v[72:75]\n" \
"s_waitcnt lgkmcnt(2)\nv_mfma_f32_16x16x32_bf16 v[20:23], v[12:15], v[32:35], v[20:23]\nds_read_b128 v[36:39], v16 offset:5120\nv_mfma_f32_16x16x32_bf16 v[44:47], a[0:3], v[52:55], v[44:47]\n" \
"ds_read_b128 v[32:35], v16 offset:37888\nv_mfma_f32_16x16x32_bf16 v[68:71], a[0:3], v[24:27], v[68:71]\nv_mfma_f32_16x16x32_bf16 v[72:75], a[104:107], v[52:55], v[72:75]\nv_mfma_f32_16x16x32_bf16 v[20:23], a[104:107], v[24:27], v[20:23]\n" \
"ds_read_b128 v[52:55], v16 offset:6144\nv_mfma_f32_16x16x32_bf16 v[44:47], a[4:7], v[48:51], v[44:47]\nbuffer_store_dwordx4 v[60:63], v16, s[8:11], s19 offen offset:3072 sc1\nds_read_b128 v[24:27], v16 offset:38912\n" \
"v_mfma_f32_16x16x32_bf16 v[68:71], a[4:7], v[64:67], v[68:71]\nds_read_b128 v[56:59], v16 offset:7168\nv_mfma_f32_16x16x32_bf16 v[72:75], a[108:111], v[48:51], v[72:75]\nds_read_b128 v[60:63], v16 offset:39936\n" \
"v_mfma_f32_16x16x32_bf16 v[20:23], a[108:111], v[64:67], v[20:23]\ns_branch 1b\n2:\ns_cmp_lt_i32 s20, 1\n" \
"s_cbranch_scc1 3b\ns_add_u32 s30, s30, 1\ns_cmp_lt_u32 s30, 0x40000\ns_cbranch_scc0 9f\n" \
"v_cmp_eq_u32 vcc, -1, v36\ns_cbranch_vccz 6f\nbuffer_load_dwordx4 v16, s[8:11], s43 offen offset:0 sc1 lds\n6:\n" \
"v_cmp_eq_u32 vcc, -1, v80\ns_cbranch_vccz 6f\nbuffer_load_dwordx4 v16, s[8:11], s43 offen offset:1024 sc1 lds\n6:\n" \
"v_cmp_eq_u32 vcc, -1, v48\ns_cbranch_vccz 6f\nbuffer_load_dwordx4 v16, s[8:11], s43 offen offset:2048 sc1 lds\n6:\n" \
"v_cmp_eq_u32 vcc, -1, v84\ns_cbranch_vccz 6f\nbuffer_load_dwordx4 v16, s[8:11], s43 offen offset:3072 sc1 lds\n6:\n" \
"s_waitcnt vmcnt(0)\nds_read_b128 v[36:39], v61 offset:0\nds_read_b128 v[80:83], v61 offset:1024\nds_read_b128 v[48:51], v61 offset:2048\n" \
"ds_read_b128 v[84:87], v61 offset:3072\ns_waitcnt lgkmcnt(0)\nv_max3_u32 v52, v36, v80, v48\nv_max_u32 v52, v52, v84\n" \
"v_cmp_eq_u32 vcc, -1, v52\ns_cbranch_vccnz 2b\ns_branch 3b\n4:\n" \
"s_cmp_ge_i32 s20, 64\ns_cbranch_scc1 9f\ns_cmp_ge_i32 s20, 62\ns_cbranch_scc1 5b\n" \
"v_bfe_u32 v32, v16, 4, 3\nv_lshlrev_b32 v32, 2, v32\ns_lshl_b32 s43, s20, 5\ns_add_u32 s43, s43, 64\n" \
"s_add_u32 s43, s43, s26\n7:\nv_cmp_eq_u32 vcc, -1, v33\ns_cbranch_vccz 8f\n" \
"s_add_u32 s30, s30, 1\ns_cmp_lt_u32 s30, 0x40000\ns_cbranch_scc0 9f\ns_sleep 8\n" \
"buffer_load_dword v33, v32, s[8:11], s43 offen sc1\ns_waitcnt vmcnt(0)\ns_branch 7b\n8:\n" \
"s_mov_b32 s14, s10\ns_branch 5b\n9:\ns_waitcnt vmcnt(0)\n" \
"s_endpgm\n"
#define D_LOOP \
"s_branch 4f\n.p2align 6\n1:\ns_waitcnt lgkmcnt(0)\n" \
"v_mfma_f32_16x16x32_bf16 v[44:47], a[8:11], v[8:11], v[44:47]\nds_read_b128 v[16:19], v4 offset:3072\ns_mov_b32 m0, s24\nv_mfma_f32_16x16x32_bf16 v[64:67], a[8:11], v[12:15], v[64:67]\n" \
"ds_read_b128 v[32:35], v4 offset:35840\nbuffer_load_dword v1, s[16:19], 0 offen offset:0 sc1 lds\nv_mfma_f32_16x16x32_bf16 v[48:51], a[104:107], v[8:11], v[48:51]\ns_add_u32 m0, s23, 0\n" \
"s_and_b32 s98, s20, 1\ns_lshl_b32 s98, s98, 3\nv_mfma_f32_16x16x32_bf16 v[20:23], a[104:107], v[12:15], v[20:23]\nv_mfma_f32_16x16x32_bf16 v[56:59], a[200:203], v[8:11], v[56:59]\n" \
"v_mfma_f32_16x16x32_bf16 v[24:27], a[200:203], v[12:15], v[24:27]\nv_mfma_f32_16x16x32_bf16 v[60:63], v[152:155], v[8:11], v[60:63]\ns_waitcnt lgkmcnt(1)\nv_mfma_f32_16x16x32_bf16 v[28:31], v[152:155], v[12:15], v[28:31]\n" \
"s_waitcnt lgkmcnt(0)\nds_read_b128 v[8:11], v4 offset:4096\nv_mfma_f32_16x16x32_bf16 v[44:47], a[12:15], v[16:19], v[44:47]\nds_read_b128 v[12:15], v4 offset:36864\n" \
"v_mfma_f32_16x16x32_bf16 v[64:67], a[12:15], v[32:35], v[64:67]\nv_mfma_f32_16x16x32_bf16 v[48:51], a[108:111], v[16:19], v[48:51]\nv_mfma_f32_16x16x32_bf16 v[20:23], a[108:111], v[32:35], v[20:23]\nv_mfma_f32_16x16x32_bf16 v[56:59], a[204:207], v[16:19], v[56:59]\n" \
"v_mfma_f32_16x16x32_bf16 v[24:27], a[204:207], v[32:35], v[24:27]\nv_mfma_f32_16x16x32_bf16 v[60:63], v[156:159], v[16:19], v[60:63]\ns_waitcnt lgkmcnt(0)\nv_mfma_f32_16x16x32_bf16 v[28:31], v[156:159], v[32:35], v[28:31]\n" \
"ds_read_b128 v[16:19], v4 offset:5120\nv_mfma_f32_16x16x32_bf16 v[44:47], a[16:19], v[8:11], v[44:47]\nds_read_b128 v[32:35], v4 offset:37888\nv_mfma_f32_16x16x32_bf16 v[64:67], a[16:19], v[12:15], v[64:67]\n" \
"v_mfma_f32_16x16x32_bf16 v[48:51], a[112:115], v[8:11], v[48:51]\nv_mfma_f32_16x16x32_bf16 v[20:23], a[112:115], v[12:15], v[20:23]\nv_mfma_f32_16x16x32_bf16 v[56:59], a[208:211], v[8:11], v[56:59]\nv_mfma_f32_16x16x32_bf16 v[24:27], a[208:211], v[12:15], v[24:27]\n" \
"v_mfma_f32_16x16x32_bf16 v[60:63], v[160:163], v[8:11], v[60:63]\ns_waitcnt lgkmcnt(0)\nv_mfma_f32_16x16x32_bf16 v[28:31], v[160:163], v[12:15], v[28:31]\nds_read_b128 v[8:11], v4 offset:6144\n" \
"s_add_u32 s99, s20, 1\nv_mfma_f32_16x16x32_bf16 v[44:47], a[20:23], v[16:19], v[44:47]\nds_read_b128 v[12:15], v4 offset:38912\ns_lshr_b32 s97, s99, 1\n" \
"v_mfma_f32_16x16x32_bf16 v[64:67], a[20:23], v[32:35], v[64:67]\ns_and_b32 s99, s99, 1\ns_lshl_b32 s99, s99, 3\nv_readlane_b32 s97, v3, s97\n" \
"v_mfma_f32_16x16x32_bf16 v[48:51], a[116:119], v[16:19], v[48:51]\nv_mfma_f32_16x16x32_bf16 v[20:23], a[116:119], v[32:35], v[20:23]\ns_lshr_b32 s97, s97, s99\ns_and_b32 s97, s97, 0xff\n" \
"s_mul_i32 s97, s97, 0x38000\nv_mfma_f32_16x16x32_bf16 v[56:59], a[212:215], v[16:19], v[56:59]\ns_add_u32 s97, s97, s21\nv_mfma_f32_16x16x32_bf16 v[24:27], a[212:215], v[32:35], v[24:27]\n" \
"v_mfma_f32_16x16x32_bf16 v[60:63], v[164:167], v[16:19], v[60:63]\ns_waitcnt lgkmcnt(0)\nv_mfma_f32_16x16x32_bf16 v[28:31], v[164:167], v[32:35], v[28:31]\nds_read_b128 v[16:19], v4 offset:7168\n" \
"v_mfma_f32_16x16x32_bf16 v[44:47], a[24:27], v[8:11], v[44:47]\nds_read_b128 v[32:35], v4 offset:39936\nv_mfma_f32_16x16x32_bf16 v[64:67], a[24:27], v[12:15], v[64:67]\nv_mfma_f32_16x16x32_bf16 v[48:51], a[120:123], v[8:11], v[48:51]\n" \
"v_mfma_f32_16x16x32_bf16 v[20:23], a[120:123], v[12:15], v[20:23]\nv_mfma_f32_16x16x32_bf16 v[56:59], a[216:219], v[8:11], v[56:59]\nv_mfma_f32_16x16x32_bf16 v[24:27], a[216:219], v[12:15], v[24:27]\nv_mfma_f32_16x16x32_bf16 v[60:63], v[168:171], v[8:11], v[60:63]\n" \
"s_waitcnt lgkmcnt(0)\nv_mfma_f32_16x16x32_bf16 v[28:31], v[168:171], v[12:15], v[28:31]\nds_read_b128 v[8:11], v4 offset:8192\nv_mov_b32 v111, s20\n" \
"v_mfma_f32_16x16x32_bf16 v[44:47], a[28:31], v[16:19], v[44:47]\nds_read_b128 v[12:15], v4 offset:40960\ns_add_u32 s99, s20, 2\nv_mfma_f32_16x16x32_bf16 v[64:67], a[28:31], v[32:35], v[64:67]\n" \
"v_mfma_f32_16x16x32_bf16 v[48:51], a[124:127], v[16:19], v[48:51]\nv_mfma_f32_16x16x32_bf16 v[20:23], a[124:127], v[32:35], v[20:23]\nv_mfma_f32_16x16x32_bf16 v[56:59], a[220:223], v[16:19], v[56:59]\ns_waitcnt vmcnt(0)\n" \
"s_barrier\nv_mfma_f32_16x16x32_bf16 v[24:27], a[220:223], v[32:35], v[24:27]\nbuffer_store_dword v111, v2, s[8:11], s25 offen offset:0 sc1\nv_mfma_f32_16x16x32_bf16 v[60:63], v[172:175], v[16:19], v[60:63]\n" \
"ds_read_b32 v110, v5 offset:0\ns_mov_b32 s25, 0\nv_mfma_f32_16x16x32_bf16 v[28:31], v[172:175], v[32:35], v[28:31]\ns_waitcnt lgkmcnt(1)\n" \
"ds_read_b128 v[16:19], v4 offset:9216\nv_mfma_f32_16x16x32_bf16 v[44:47], a[32:35], v[8:11], v[44:47]\nds_read_b128 v[32:35], v4 offset:41984\nv_mfma_f32_16x16x32_bf16 v[64:67], a[32:35], v[12:15], v[64:67]\n" \
"v_mfma_f32_16x16x32_bf16 v[48:51], a[128:131], v[8:11], v[48:51]\ns_waitcnt lgkmcnt(2)\nv_cmp_gt_i32 s[94:95], s99, v110\nv_mfma_f32_16x16x32_bf16 v[20:23], a[128:131], v[12:15], v[20:23]\n" \
"v_mfma_f32_16x16x32_bf16 v[56:59], a[224:227], v[8:11], v[56:59]\ns_cmp_eq_u64 s[94:95], 0\ns_cselect_b32 s14, s10, 0\nv_mfma_f32_16x16x32_bf16 v[24:27], a[224:227], v[12:15], v[24:27]\n" \
"v_mfma_f32_16x16x32_bf16 v[60:63], v[176:179], v[8:11], v[60:63]\ns_waitcnt lgkmcnt(0)\nv_mfma_f32_16x16x32_bf16 v[28:31], v[176:179], v[12:15], v[28:31]\nds_read_b128 v[8:11], v4 offset:10240\n" \
"v_mfma_f32_16x16x32_bf16 v[44:47], a[36:39], v[16:19], v[44:47]\nds_read_b128 v[12:15], v4 offset:43008\nv_mfma_f32_16x16x32_bf16 v[64:67], a[36:39], v[32:35], v[64:67]\nv_mfma_f32_16x16x32_bf16 v[48:51], a[132:135], v[16:19], v[48:51]\n" \
"v_mfma_f32_16x16x32_bf16 v[20:23], a[132:135], v[32:35], v[20:23]\nbuffer_load_dwordx4 v0, s[12:15], s97 offen offset:0 sc1 lds\nv_mfma_f32_16x16x32_bf16 v[56:59], a[228:231], v[16:19], v[56:59]\nv_mfma_f32_16x16x32_bf16 v[24:27], a[228:231], v[32:35], v[24:27]\n" \
"v_mfma_f32_16x16x32_bf16 v[60:63], v[180:183], v[16:19], v[60:63]\ns_waitcnt lgkmcnt(0)\nv_mfma_f32_16x16x32_bf16 v[28:31], v[180:183], v[32:35], v[28:31]\nbuffer_load_dwordx4 v0, s[12:15], s97 offen offset:1024 sc1 lds\n" \
"v_mfma_f32_16x16x32_bf16 v[44:47], a[40:43], v[8:11], v[44:47]\nds_read_b128 v[16:19], v4 offset:11264\nv_mfma_f32_16x16x32_bf16 v[64:67], a[40:43], v[12:15], v[64:67]\nds_read_b128 v[32:35], v4 offset:44032\n" \
"v_mfma_f32_16x16x32_bf16 v[48:51], a[136:139], v[8:11], v[48:51]\nv_mfma_f32_16x16x32_bf16 v[20:23], a[136:139], v[12:15], v[20:23]\nbuffer_load_dwordx4 v0, s[12:15], s97 offen offset:2048 sc1 lds\nv_mfma_f32_16x16x32_bf16 v[56:59], a[232:235], v[8:11], v[56:59]\n" \
"v_mfma_f32_16x16x32_bf16 v[24:27], a[232:235], v[12:15], v[24:27]\nv_mfma_f32_16x16x32_bf16 v[60:63], v[184:187], v[8:11], v[60:63]\ns_waitcnt lgkmcnt(1)\nv_mfma_f32_16x16x32_bf16 v[28:31], v[184:187], v[12:15], v[28:31]\n" \
"s_waitcnt lgkmcnt(0)\nbuffer_load_dwordx4 v0, s[12:15], s97 offen offset:3072 sc1 lds\nv_mfma_f32_16x16x32_bf16 v[44:47], a[44:47], v[16:19], v[44:47]\nds_read_b128 v[8:11], v4 offset:12288\n" \
"s_add_u32 m0, s23, 0x1000\nv_mfma_f32_16x16x32_bf16 v[64:67], a[44:47], v[32:35], v[64:67]\nds_read_b128 v[12:15], v4 offset:45056\ns_add_u32 s97, s97, 0x1000\n" \
"v_mfma_f32_16x16x32_bf16 v[48:51], a[140:143], v[16:19], v[48:51]\nv_mfma_f32_16x16x32_bf16 v[20:23], a[140:143], v[32:35], v[20:23]\nbuffer_load_dwordx4 v0, s[12:15], s97 offen offset:0 sc1 lds\nv_mfma_f32_16x16x32_bf16 v[56:59], a[236:239], v[16:19], v[56:59]\n" \
"v_mfma_f32_16x16x32_bf16 v[24:27], a[236:239], v[32:35], v[24:27]\nv_mfma_f32_16x16x32_bf16 v[60:63], v[188:191], v[16:19], v[60:63]\ns_waitcnt lgkmcnt(1)\nv_mfma_f32_16x16x32_bf16 v[28:31], v[188:191], v[32:35], v[28:31]\n" \
"s_waitcnt lgkmcnt(0)\nbuffer_load_dwordx4 v0, s[12:15], s97 offen offset:1024 sc1 lds\nv_mfma_f32_16x16x32_bf16 v[44:47], a[48:51], v[8:11], v[44:47]\nds_read_b128 v[16:19], v4 offset:13312\n" \
"v_mfma_f32_16x16x32_bf16 v[64:67], a[48:51], v[12:15], v[64:67]\nds_read_b128 v[32:35], v4 offset:46080\nv_mfma_f32_16x16x32_bf16 v[48:51], a[144:147], v[8:11], v[48:51]\nv_mfma_f32_16x16x32_bf16 v[20:23], a[144:147], v[12:15], v[20:23]\n" \
"buffer_load_dwordx4 v0, s[12:15], s97 offen offset:2048 sc1 lds\nv_mfma_f32_16x16x32_bf16 v[56:59], a[240:243], v[8:11], v[56:59]\nv_mfma_f32_16x16x32_bf16 v[24:27], a[240:243], v[12:15], v[24:27]\nv_mfma_f32_16x16x32_bf16 v[60:63], v[192:195], v[8:11], v[60:63]\n" \
"s_waitcnt lgkmcnt(1)\nv_mfma_f32_16x16x32_bf16 v[28:31], v[192:195], v[12:15], v[28:31]\ns_waitcnt lgkmcnt(0)\nbuffer_load_dwordx4 v0, s[12:15], s97 offen offset:3072 sc1 lds\n" \
"v_mfma_f32_16x16x32_bf16 v[44:47], a[52:55], v[16:19], v[44:47]\nds_read_b128 v[8:11], v4 offset:14336\ns_add_u32 m0, s23, 0x2000\nv_mfma_f32_16x16x32_bf16 v[64:67], a[52:55], v[32:35], v[64:67]\n" \
"ds_read_b128 v[12:15], v4 offset:47104\ns_add_u32 s97, s97, 0x1000\nv_mfma_f32_16x16x32_bf16 v[48:51], a[148:151], v[16:19], v[48:51]\ns_xor_b32 s23, s23, 0x10000\n" \
"v_mfma_f32_16x16x32_bf16 v[20:23], a[148:151], v[32:35], v[20:23]\nbuffer_load_dwordx4 v0, s[12:15], s97 offen offset:0 sc1 lds\nv_mfma_f32_16x16x32_bf16 v[56:59], a[244:247], v[16:19], v[56:59]\nv_mfma_f32_16x16x32_bf16 v[24:27], a[244:247], v[32:35], v[24:27]\n" \
"v_mfma_f32_16x16x32_bf16 v[60:63], v[196:199], v[16:19], v[60:63]\ns_waitcnt lgkmcnt(1)\nv_mfma_f32_16x16x32_bf16 v[28:31], v[196:199], v[32:35], v[28:31]\ns_waitcnt lgkmcnt(0)\n" \
"buffer_load_dwordx4 v0, s[12:15], s97 offen offset:1024 sc1 lds\nv_mfma_f32_16x16x32_bf16 v[44:47], a[56:59], v[8:11], v[44:47]\nds_read_b128 v[16:19], v4 offset:15360\nv_mfma_f32_16x16x32_bf16 v[64:67], a[56:59], v[12:15], v[64:67]\n" \
"ds_read_b128 v[32:35], v4 offset:48128\nv_mfma_f32_16x16x32_bf16 v[48:51], a[152:155], v[8:11], v[48:51]\nv_mfma_f32_16x16x32_bf16 v[20:23], a[152:155], v[12:15], v[20:23]\nbuffer_load_dwordx4 v0, s[12:15], s97 offen offset:2048 sc1 lds\n" \
"v_mfma_f32_16x16x32_bf16 v[56:59], a[248:251], v[8:11], v[56:59]\nv_mfma_f32_16x16x32_bf16 v[24:27], a[248:251], v[12:15], v[24:27]\nv_mfma_f32_16x16x32_bf16 v[60:63], v[200:203], v[8:11], v[60:63]\ns_waitcnt lgkmcnt(1)\n" \
"v_mfma_f32_16x16x32_bf16 v[28:31], v[200:203], v[12:15], v[28:31]\ns_waitcnt lgkmcnt(0)\nbuffer_load_dwordx4 v0, s[12:15], s97 offen offset:3072 sc1 lds\nv_mfma_f32_16x16x32_bf16 v[44:47], a[60:63], v[16:19], v[44:47]\n" \
"ds_read_b128 v[8:11], v4 offset:16384\nv_mfma_f32_16x16x32_bf16 v[64:67], a[60:63], v[32:35], v[64:67]\nds_read_b128 v[12:15], v4 offset:49152\nv_mfma_f32_16x16x32_bf16 v[48:51], a[156:159], v[16:19], v[48:51]\n" \
"v_mfma_f32_16x16x32_bf16 v[20:23], a[156:159], v[32:35], v[20:23]\nv_mfma_f32_16x16x32_bf16 v[56:59], a[252:255], v[16:19], v[56:59]\nv_mfma_f32_16x16x32_bf16 v[24:27], a[252:255], v[32:35], v[24:27]\nv_mfma_f32_16x16x32_bf16 v[60:63], v[204:207], v[16:19], v[60:63]\n" \
"s_waitcnt lgkmcnt(1)\nv_mfma_f32_16x16x32_bf16 v[28:31], v[204:207], v[32:35], v[28:31]\ns_waitcnt lgkmcnt(0)\nds_read_b128 v[16:19], v4 offset:17408\n" \
"v_mfma_f32_16x16x32_bf16 v[44:47], a[64:67], v[8:11], v[44:47]\nds_read_b128 v[32:35], v4 offset:50176\nv_mfma_f32_16x16x32_bf16 v[64:67], a[64:67], v[12:15], v[64:67]\nv_mfma_f32_16x16x32_bf16 v[48:51], a[160:163], v[8:11], v[48:51]\n" \
"v_mfma_f32_16x16x32_bf16 v[20:23], a[160:163], v[12:15], v[20:23]\nv_mfma_f32_16x16x32_bf16 v[56:59], v[112:115], v[8:11], v[56:59]\nv_mfma_f32_16x16x32_bf16 v[24:27], v[112:115], v[12:15], v[24:27]\nv_mfma_f32_16x16x32_bf16 v[60:63], v[208:211], v[8:11], v[60:63]\n" \
"s_waitcnt lgkmcnt(0)\nv_mfma_f32_16x16x32_bf16 v[28:31], v[208:211], v[12:15], v[28:31]\nds_read_b128 v[8:11], v4 offset:18432\nv_mfma_f32_16x16x32_bf16 v[44:47], a[68:71], v[16:19], v[44:47]\n" \
"ds_read_b128 v[12:15], v4 offset:51200\nv_mfma_f32_16x16x32_bf16 v[64:67], a[68:71], v[32:35], v[64:67]\nv_mfma_f32_16x16x32_bf16 v[48:51], a[164:167], v[16:19], v[48:51]\nv_mfma_f32_16x16x32_bf16 v[20:23], a[164:167], v[32:35], v[20:23]\n" \
"s_lshr_b32 s99, s20, 1\ns_add_u32 s20, s20, 1\nv_readlane_b32 s99, v3, s99\nv_mfma_f32_16x16x32_bf16 v[56:59], v[116:119], v[16:19], v[56:59]\n" \
"v_mfma_f32_16x16x32_bf16 v[24:27], v[116:119], v[32:35], v[24:27]\ns_lshr_b32 s99, s99, s98\ns_and_b32 s99, s99, 0xff\ns_mul_i32 s99, s99, 0x50000\n" \
"v_mfma_f32_16x16x32_bf16 v[60:63], v[212:215], v[16:19], v[60:63]\ns_waitcnt lgkmcnt(0)\ns_add_u32 s99, s99, s22\ns_add_u32 s98, s99, 0x8000\n" \
"v_mfma_f32_16x16x32_bf16 v[28:31], v[212:215], v[32:35], v[28:31]\nds_read_b128 v[16:19], v4 offset:19456\nv_mfma_f32_16x16x32_bf16 v[44:47], a[72:75], v[8:11], v[44:47]\nds_read_b128 v[32:35], v4 offset:52224\n" \
"v_mfma_f32_16x16x32_bf16 v[64:67], a[72:75], v[12:15], v[64:67]\nv_mfma_f32_16x16x32_bf16 v[48:51], a[168:171], v[8:11], v[48:51]\nv_mfma_f32_16x16x32_bf16 v[20:23], a[168:171], v[12:15], v[20:23]\nv_mfma_f32_16x16x32_bf16 v[56:59], v[120:123], v[8:11], v[56:59]\n" \
"v_mfma_f32_16x16x32_bf16 v[24:27], v[120:123], v[12:15], v[24:27]\nv_mfma_f32_16x16x32_bf16 v[60:63], v[216:219], v[8:11], v[60:63]\ns_waitcnt lgkmcnt(0)\nv_mfma_f32_16x16x32_bf16 v[28:31], v[216:219], v[12:15], v[28:31]\n" \
"ds_read_b128 v[8:11], v4 offset:20480\nv_mfma_f32_16x16x32_bf16 v[44:47], a[76:79], v[16:19], v[44:47]\nds_read_b128 v[12:15], v4 offset:53248\nv_mfma_f32_16x16x32_bf16 v[64:67], a[76:79], v[32:35], v[64:67]\n" \
"v_mfma_f32_16x16x32_bf16 v[48:51], a[172:175], v[16:19], v[48:51]\nv_mfma_f32_16x16x32_bf16 v[20:23], a[172:175], v[32:35], v[20:23]\nv_mfma_f32_16x16x32_bf16 v[56:59], v[124:127], v[16:19], v[56:59]\nv_mfma_f32_16x16x32_bf16 v[24:27], v[124:127], v[32:35], v[24:27]\n" \
"v_mfma_f32_16x16x32_bf16 v[60:63], v[220:223], v[16:19], v[60:63]\ns_waitcnt lgkmcnt(0)\nv_mfma_f32_16x16x32_bf16 v[28:31], v[220:223], v[32:35], v[28:31]\nds_read_b128 v[36:39], v4 offset:21504\n" \
"v_mfma_f32_16x16x32_bf16 v[44:47], a[80:83], v[8:11], v[44:47]\nds_read_b128 v[32:35], v4 offset:54272\nv_mfma_f32_16x16x32_bf16 v[64:67], a[80:83], v[12:15], v[64:67]\nv_mfma_f32_16x16x32_bf16 v[48:51], a[176:179], v[8:11], v[48:51]\n" \
"v_mfma_f32_16x16x32_bf16 v[20:23], a[176:179], v[12:15], v[20:23]\nv_mfma_f32_16x16x32_bf16 v[56:59], v[128:131], v[8:11], v[56:59]\nv_mfma_f32_16x16x32_bf16 v[24:27], v[128:131], v[12:15], v[24:27]\nv_mfma_f32_16x16x32_bf16 v[60:63], v[224:227], v[8:11], v[60:63]\n" \
"s_waitcnt lgkmcnt(0)\nv_mfma_f32_16x16x32_bf16 v[28:31], v[224:227], v[12:15], v[28:31]\nds_read_b128 v[40:43], v4 offset:22528\nv_mfma_f32_16x16x32_bf16 v[44:47], a[84:87], v[36:39], v[44:47]\n" \
"ds_read_b128 v[16:19], v4 offset:55296\nv_xor_b32 v4, 0x10000, v4\nv_mfma_f32_16x16x32_bf16 v[64:67], a[84:87], v[32:35], v[64:67]\nv_mfma_f32_16x16x32_bf16 v[48:51], a[180:183], v[36:39], v[48:51]\n" \
"v_mfma_f32_16x16x32_bf16 v[20:23], a[180:183], v[32:35], v[20:23]\nv_mfma_f32_16x16x32_bf16 v[56:59], v[132:135], v[36:39], v[56:59]\nv_mfma_f32_16x16x32_bf16 v[24:27], v[132:135], v[32:35], v[24:27]\nv_mfma_f32_16x16x32_bf16 v[60:63], v[228:231], v[36:39], v[60:63]\n" \
"s_waitcnt lgkmcnt(0)\nv_mfma_f32_16x16x32_bf16 v[28:31], v[228:231], v[32:35], v[28:31]\ns_waitcnt vmcnt(0)\ns_barrier\n" \
"v_mfma_f32_16x16x32_bf16 v[44:47], a[88:91], v[40:43], v[44:47]\nds_read_b128 v[8:11], v4 offset:0\nv_mfma_f32_16x16x32_bf16 v[48:51], a[184:187], v[40:43], v[48:51]\nds_read_b128 v[12:15], v4 offset:32768\n" \
"v_mfma_f32_16x16x32_bf16 v[56:59], v[136:139], v[40:43], v[56:59]\nv_cvt_pk_bf16_f32 v36, v44, v45\nv_cvt_pk_bf16_f32 v37, v46, v47\nv_mfma_f32_16x16x32_bf16 v[60:63], v[232:235], v[40:43], v[60:63]\n" \
"v_cvt_pk_bf16_f32 v38, v48, v49\nv_cvt_pk_bf16_f32 v39, v50, v51\nv_mfma_f32_16x16x32_bf16 v[64:67], a[88:91], v[16:19], v[64:67]\nv_cvt_pk_bf16_f32 v32, v56, v57\n" \
"v_cvt_pk_bf16_f32 v33, v58, v59\nv_mfma_f32_16x16x32_bf16 v[20:23], a[184:187], v[16:19], v[20:23]\nbuffer_store_dwordx4 v[36:39], v6, s[8:11], s99 offen offset:0 sc1\nv_cvt_pk_bf16_f32 v34, v60, v61\n" \
"v_cvt_pk_bf16_f32 v35, v62, v63\nv_mfma_f32_16x16x32_bf16 v[24:27], v[136:139], v[16:19], v[24:27]\ns_waitcnt lgkmcnt(1)\nv_cvt_pk_bf16_f32 v36, v64, v65\n" \
"v_cvt_pk_bf16_f32 v37, v66, v67\nv_mfma_f32_16x16x32_bf16 v[28:31], v[232:235], v[16:19], v[28:31]\nds_read_b128 v[40:43], v4 offset:1024\nv_cvt_pk_bf16_f32 v38, v20, v21\n" \
"v_mfma_f32_16x16x32_bf16 v[44:47], a[0:3], v[8:11], 0\nds_read_b128 v[16:19], v4 offset:33792\nv_cvt_pk_bf16_f32 v39, v22, v23\nv_mfma_f32_16x16x32_bf16 v[48:51], a[96:99], v[8:11], 0\n" \
"buffer_store_dwordx4 v[32:35], v6, s[8:11], s99 offen offset:512 sc1\nv_cvt_pk_bf16_f32 v52, v24, v25\nv_cvt_pk_bf16_f32 v53, v26, v27\nv_cvt_pk_bf16_f32 v54, v28, v29\n" \
"v_mfma_f32_16x16x32_bf16 v[56:59], a[192:195], v[8:11], 0\ns_waitcnt lgkmcnt(2)\nv_cvt_pk_bf16_f32 v55, v30, v31\nv_mfma_f32_16x16x32_bf16 v[60:63], v[144:147], v[8:11], 0\n" \
"v_mfma_f32_16x16x32_bf16 v[64:67], a[0:3], v[12:15], 0\nv_mfma_f32_16x16x32_bf16 v[20:23], a[96:99], v[12:15], 0\nbuffer_store_dwordx4 v[36:39], v6, s[8:11], s98 offen offset:0 sc1\nv_mfma_f32_16x16x32_bf16 v[24:27], a[192:195], v[12:15], 0\n" \
"s_waitcnt lgkmcnt(0)\nv_mfma_f32_16x16x32_bf16 v[28:31], v[144:147], v[12:15], 0\nds_read_b128 v[8:11], v4 offset:2048\nv_mfma_f32_16x16x32_bf16 v[44:47], a[4:7], v[40:43], v[44:47]\n" \
"ds_read_b128 v[12:15], v4 offset:34816\nv_mfma_f32_16x16x32_bf16 v[64:67], a[4:7], v[16:19], v[64:67]\nbuffer_store_dwordx4 v[52:55], v6, s[8:11], s98 offen offset:512 sc1\nv_mfma_f32_16x16x32_bf16 v[48:51], a[100:103], v[40:43], v[48:51]\n" \
"v_mfma_f32_16x16x32_bf16 v[20:23], a[100:103], v[16:19], v[20:23]\nv_mfma_f32_16x16x32_bf16 v[56:59], a[196:199], v[40:43], v[56:59]\nv_mfma_f32_16x16x32_bf16 v[24:27], a[196:199], v[16:19], v[24:27]\nv_mfma_f32_16x16x32_bf16 v[60:63], v[148:151], v[40:43], v[60:63]\n" \
"s_cmp_eq_u32 s14, 0\nv_mfma_f32_16x16x32_bf16 v[28:31], v[148:151], v[16:19], v[28:31]\ns_cbranch_scc0 1b\n4:\n" \
"buffer_load_dword v68, v1, s[8:11], 0 offen sc1\nv_mov_b32 v69, s20\ns_add_u32 s98, s20, 1\ns_waitcnt vmcnt(0)\n" \
"v_cmp_gt_i32 vcc, s98, v68\ns_cbranch_vccz 8f\nbuffer_store_dword v69, v2, s[8:11], 0 offen sc1\ns_mov_b32 s25, 0x40000000\n" \
"s_cmp_ge_u32 s20, 0x80\ns_cbranch_scc1 9f\n7:\ns_add_u32 s26, s26, 1\n" \
"s_cmp_lt_u32 s26, 0x40000\ns_cbranch_scc0 9f\ns_sleep 8\nbuffer_load_dword v68, v1, s[8:11], 0 offen sc1\n" \
"s_waitcnt vmcnt(0)\nv_cmp_gt_i32 vcc, s98, v68\ns_cbranch_vccnz 7b\n8:\n" \
"s_lshr_b32 s98, s20, 1\nv_readlane_b32 s98, v3, s98\ns_and_b32 s99, s20, 1\ns_lshl_b32 s99, s99, 3\n" \
"s_lshr_b32 s98, s98, s99\ns_and_b32 s98, s98, 0xff\ns_mul_i32 s98, s98, 0x38000\ns_add_u32 s98, s98, s21\n" \
"s_xor_b32 s99, s23, 0x10000\ns_add_u32 m0, s99, 0\ns_add_u32 s98, s98, 0\nbuffer_load_dwordx4 v0, s[8:11], s98 offen offset:0 sc1 lds\n" \
"buffer_load_dwordx4 v0, s[8:11], s98 offen offset:1024 sc1 lds\nbuffer_load_dwordx4 v0, s[8:11], s98 offen offset:2048 sc1 lds\nbuffer_load_dwordx4 v0, s[8:11], s98 offen offset:3072 sc1 lds\ns_add_u32 m0, s99, 0x1000\n" \
"s_add_u32 s98, s98, 0x1000\nbuffer_load_dwordx4 v0, s[8:11], s98 offen offset:0 sc1 lds\nbuffer_load_dwordx4 v0, s[8:11], s98 offen offset:1024 sc1 lds\nbuffer_load_dwordx4 v0, s[8:11], s98 offen offset:2048 sc1 lds\n" \
"buffer_load_dwordx4 v0, s[8:11], s98 offen offset:3072 sc1 lds\ns_add_u32 m0, s99, 0x2000\ns_add_u32 s98, s98, 0x1000\nbuffer_load_dwordx4 v0, s[8:11], s98 offen offset:0 sc1 lds\n" \
"buffer_load_dwordx4 v0, s[8:11], s98 offen offset:1024 sc1 lds\nbuffer_load_dwordx4 v0, s[8:11], s98 offen offset:2048 sc1 lds\nbuffer_load_dwordx4 v0, s[8:11], s98 offen offset:3072 sc1 lds\nbuffer_store_dword v69, v2, s[8:11], s25 offen sc1\n" \
"s_mov_b32 s25, 0x40000000\ns_waitcnt vmcnt(1) lgkmcnt(0)\ns_barrier\nds_read_b128 v[68:71], v4 offset:0\n" \
"ds_read_b128 v[72:75], v4 offset:32768\nds_read_b128 v[76:79], v4 offset:1024\nds_read_b128 v[80:83], v4 offset:33792\nds_read_b128 v[8:11], v4 offset:2048\n" \
"ds_read_b128 v[12:15], v4 offset:34816\ns_waitcnt lgkmcnt(0)\nv_mfma_f32_16x16x32_bf16 v[44:47], a[0:3], v[68:71], 0\nv_mfma_f32_16x16x32_bf16 v[48:51], a[96:99], v[68:71], 0\n" \
"v_mfma_f32_16x16x32_bf16 v[56:59], a[192:195], v[68:71], 0\nv_mfma_f32_16x16x32_bf16 v[60:63], v[144:147], v[68:71], 0\nv_mfma_f32_16x16x32_bf16 v[64:67], a[0:3], v[72:75], 0\nv_mfma_f32_16x16x32_bf16 v[20:23], a[96:99], v[72:75], 0\n" \
"v_mfma_f32_16x16x32_bf16 v[24:27], a[192:195], v[72:75], 0\nv_mfma_f32_16x16x32_bf16 v[28:31], v[144:147], v[72:75], 0\nv_mfma_f32_16x16x32_bf16 v[44:47], a[4:7], v[76:79], v[44:47]\nv_mfma_f32_16x16x32_bf16 v[48:51], a[100:103], v[76:79], v[48:51]\n" \
"v_mfma_f32_16x16x32_bf16 v[56:59], a[196:199], v[76:79], v[56:59]\nv_mfma_f32_16x16x32_bf16 v[60:63], v[148:151], v[76:79], v[60:63]\nv_mfma_f32_16x16x32_bf16 v[64:67], a[4:7], v[80:83], v[64:67]\nv_mfma_f32_16x16x32_bf16 v[20:23], a[100:103], v[80:83], v[20:23]\n" \
"v_mfma_f32_16x16x32_bf16 v[24:27], a[196:199], v[80:83], v[24:27]\nv_mfma_f32_16x16x32_bf16 v[28:31], v[148:151], v[80:83], v[28:31]\ns_branch 1b\n9:\n" \
"s_waitcnt vmcnt(0)\ns_endpgm\n"
#define G_LOOP \
"s_branch 4f\n.p2align 6\n1:\ns_waitcnt lgkmcnt(2)\n" \
"v_mfma_f32_16x16x32_bf16 v[8:11], a[104:107], v[36:39], v[8:11]\ns_lshr_b32 s99, s20, 1\ns_and_b32 s98, s20, 1\nv_mov_b32 v110, s20\n" \
"v_mfma_f32_16x16x32_bf16 v[12:15], a[104:107], v[40:43], v[12:15]\ns_add_u32 s20, s20, 1\ns_add_u32 s97, s20, 1\ns_lshr_b32 s96, s97, 1\n" \
"v_mfma_f32_16x16x32_bf16 v[16:19], a[232:235], v[36:39], v[16:19]\nds_read_b128 v[52:55], v7 offset:28672\nv_readlane_b32 s96, v6, s96\nv_mfma_f32_16x16x32_bf16 v[20:23], a[232:235], v[40:43], v[20:23]\n" \
"ds_read_b128 v[56:59], v7 offset:61440\ns_and_b32 s97, s97, 1\nv_mfma_f32_16x16x32_bf16 v[24:27], v[216:219], v[36:39], v[24:27]\ns_lshl_b32 s97, s97, 3\n" \
"s_lshr_b32 s96, s96, s97\ns_and_b32 s96, s96, 0xff\nv_mfma_f32_16x16x32_bf16 v[28:31], v[216:219], v[40:43], v[28:31]\ns_waitcnt lgkmcnt(2)\n" \
"s_lshl_b32 s97, s96, 13\nv_xor_b32 v4, 0x400, v4\nv_mfma_f32_16x16x32_bf16 v[8:11], a[108:111], v[44:47], v[8:11]\ns_lshl_b32 s98, s98, 3\n" \
"v_mov_b32 v32, v111\nv_mov_b32 v33, v111\nv_mfma_f32_16x16x32_bf16 v[12:15], a[108:111], v[48:51], v[12:15]\ns_lshl_b32 s96, s96, 16\n" \
"v_mul_f32 v252, 0xc0135761, v34\nv_mul_f32 v34, v34, v34\nv_mfma_f32_16x16x32_bf16 v[16:19], a[236:239], v[44:47], v[16:19]\nds_read_b128 v[36:39], v7 offset:29696\n" \
"v_readlane_b32 s99, v6, s99\nv_mfma_f32_16x16x32_bf16 v[20:23], a[236:239], v[48:51], v[20:23]\nds_read_b128 v[40:43], v7 offset:62464\nv_mul_f32 v34, v34, v252\n" \
"v_mfma_f32_16x16x32_bf16 v[24:27], v[220:223], v[44:47], v[24:27]\nds_read_b128 v[60:63], v7 offset:30720\nv_mul_f32 v254, v35, v35\nv_mfma_f32_16x16x32_bf16 v[28:31], v[220:223], v[48:51], v[28:31]\n" \
"s_waitcnt lgkmcnt(3)\nv_permlane16_swap_b32 v32, v33\nv_mfma_f32_16x16x32_bf16 v[8:11], a[112:115], v[52:55], v[8:11]\nds_read_b128 v[44:47], v7 offset:63488\n" \
"v_mul_f32 v253, 0x3d372713, v34\nv_mfma_f32_16x16x32_bf16 v[12:15], a[112:115], v[56:59], v[12:15]\ns_lshr_b32 s99, s99, s98\ns_and_b32 s99, s99, 0xff\n" \
"s_mul_i32 s99, s99, 0x38000\nv_mfma_f32_16x16x32_bf16 v[16:19], a[240:243], v[52:55], v[16:19]\nds_read_b128 v[48:51], v7 offset:31744\ns_or_b32 s16, s99, s18\n" \
"v_mfma_f32_16x16x32_bf16 v[20:23], a[240:243], v[56:59], v[20:23]\ns_or_b32 s17, s99, s19\ns_add_u32 s96, s96, s21\nv_mfma_f32_16x16x32_bf16 v[24:27], v[224:227], v[52:55], v[24:27]\n" \
"ds_read_b128 v[64:67], v7 offset:64512\nv_xor_b32 v7, 0x10000, v7\nv_mfma_f32_16x16x32_bf16 v[28:31], v[224:227], v[56:59], v[28:31]\ns_waitcnt lgkmcnt(3)\n" \
"buffer_load_dwordx4 v[52:55], v1, s[8:11], s97 offen offset:0 sc1\nv_mfma_f32_16x16x32_bf16 v[8:11], a[116:119], v[36:39], v[8:11]\nbuffer_load_dwordx4 v[56:59], v1, s[8:11], s97 offen offset:1024 sc1\nv_mfma_f32_16x16x32_bf16 v[12:15], a[116:119], v[40:43], v[12:15]\n" \
"v_mfma_f32_16x16x32_bf16 v[16:19], a[244:247], v[36:39], v[16:19]\ns_waitcnt vmcnt(5) lgkmcnt(0)\ns_barrier\nv_mfma_f32_16x16x32_bf16 v[20:23], a[244:247], v[40:43], v[20:23]\n" \
"ds_read_b128 v[68:71], v4 offset:0\nv_mfma_f32_16x16x32_bf16 v[24:27], v[228:231], v[36:39], v[24:27]\nds_read_b128 v[72:75], v7 offset:0\nv_mfma_f32_16x16x32_bf16 v[28:31], v[228:231], v[40:43], v[28:31]\n" \
"ds_read_b128 v[36:39], v7 offset:32768\nv_mfma_f32_16x16x32_bf16 v[8:11], a[120:123], v[60:63], v[8:11]\nds_read_b128 v[40:43], v7 offset:1024\nv_mfma_f32_16x16x32_bf16 v[12:15], a[120:123], v[44:47], v[12:15]\n" \
"s_waitcnt lgkmcnt(3)\nv_add_f32 v68, v68, v69\nv_mfma_f32_16x16x32_bf16 v[8:11], a[124:127], v[48:51], v[8:11]\nds_read_b128 v[76:79], v7 offset:33792\n" \
"v_add_f32 v70, v70, v71\nv_mfma_f32_16x16x32_bf16 v[12:15], a[124:127], v[64:67], v[12:15]\nv_cmp_u_f32 s[98:99], v68, v70\nv_mfma_f32_16x16x32_bf16 v[16:19], a[248:251], v[60:63], v[16:19]\n" \
"v_fma_f32 v68, v32, v240, v8\nv_fma_f32 v69, v32, v241, v9\nv_mfma_f32_16x16x32_bf16 v[20:23], a[248:251], v[44:47], v[20:23]\ns_waitcnt vmcnt(2)\n" \
"buffer_store_dword v110, v3, s[8:11], s23 offen offset:0 sc1\nv_mfma_f32_16x16x32_bf16 v[16:19], a[252:255], v[48:51], v[16:19]\ns_mov_b32 s23, 0\nv_fma_f32 v70, v32, v242, v10\n" \
"v_fma_f32 v71, v32, v243, v11\nv_mfma_f32_16x16x32_bf16 v[20:23], a[252:255], v[64:67], v[20:23]\ns_cmp_lt_u32 s20, 0x80\ns_cselect_b32 s98, s98, -1\n" \
"v_fma_f32 v80, v33, v240, v12\nv_mfma_f32_16x16x32_bf16 v[24:27], v[232:235], v[60:63], v[24:27]\nv_fma_f32 v81, v33, v241, v13\nv_fma_f32 v82, v33, v242, v14\n" \
"v_mfma_f32_16x16x32_bf16 v[28:31], v[232:235], v[44:47], v[28:31]\nv_fma_f32 v83, v33, v243, v15\nv_fma_f32 v44, v32, v244, v16\nv_mfma_f32_16x16x32_bf16 v[24:27], v[236:239], v[48:51], v[24:27]\n" \
"s_waitcnt lgkmcnt(0)\nv_fma_f32 v45, v32, v245, v17\nv_fma_f32 v46, v32, v246, v18\nv_mfma_f32_16x16x32_bf16 v[28:31], v[236:239], v[64:67], v[28:31]\n" \
"s_cmp_lg_u64 s[98:99], 0\ns_cbranch_scc1 4f\n5:\nv_mfma_f32_16x16x32_bf16 v[8:11], a[0:3], v[72:75], 0\n" \
"s_xor_b32 m0, s22, 0\nv_fma_f32 v47, v32, v247, v19\nv_fma_f32 v48, v33, v244, v20\nv_mfma_f32_16x16x32_bf16 v[12:15], a[0:3], v[36:39], 0\n" \
"v_fma_f32 v49, v33, v245, v21\nv_fma_f32 v50, v33, v246, v22\nv_mfma_f32_16x16x32_bf16 v[8:11], a[4:7], v[40:43], v[8:11]\nds_read_b128 v[60:63], v7 offset:2048\n" \
"v_fma_f32 v51, v33, v247, v23\nv_mfma_f32_16x16x32_bf16 v[12:15], a[4:7], v[76:79], v[12:15]\nds_read_b128 v[64:67], v7 offset:34816\nv_fma_f32 v84, v32, v248, v24\n" \
"v_mfma_f32_16x16x32_bf16 v[16:19], a[128:131], v[72:75], 0\nv_fma_f32 v85, v32, v249, v25\nv_fma_f32 v86, v32, v250, v26\nv_mfma_f32_16x16x32_bf16 v[20:23], a[128:131], v[36:39], 0\n" \
"v_fma_f32 v87, v32, v251, v27\nv_fma_f32 v88, v33, v248, v28\nv_mfma_f32_16x16x32_bf16 v[16:19], a[132:135], v[40:43], v[16:19]\nv_fma_f32 v89, v33, v249, v29\n" \
"v_fma_f32 v90, v33, v250, v30\nv_mfma_f32_16x16x32_bf16 v[20:23], a[132:135], v[76:79], v[20:23]\nv_fma_f32 v91, v33, v251, v31\nv_xor_b32 v5, 0x400, v5\n" \
"v_mfma_f32_16x16x32_bf16 v[24:27], v[112:115], v[72:75], 0\nds_read_b128 v[92:95], v7 offset:3072\nv_mfma_f32_16x16x32_bf16 v[28:31], v[112:115], v[36:39], 0\nds_read_b128 v[72:75], v7 offset:35840\n" \
"v_mfma_f32_16x16x32_bf16 v[24:27], v[116:119], v[40:43], v[24:27]\ns_waitcnt lgkmcnt(2)\nv_permlane16_swap_b32 v68, v80\nv_mfma_f32_16x16x32_bf16 v[28:31], v[116:119], v[76:79], v[28:31]\n" \
"v_permlane16_swap_b32 v69, v81\nv_mfma_f32_16x16x32_bf16 v[8:11], a[8:11], v[60:63], v[8:11]\nv_permlane16_swap_b32 v70, v82\nv_mfma_f32_16x16x32_bf16 v[12:15], a[8:11], v[64:67], v[12:15]\n" \
"v_permlane16_swap_b32 v71, v83\nv_mfma_f32_16x16x32_bf16 v[16:19], a[136:139], v[60:63], v[16:19]\nds_read_b128 v[36:39], v7 offset:4096\nv_mul_f32 v40, v68, v68\n" \
"v_mfma_f32_16x16x32_bf16 v[20:23], a[136:139], v[64:67], v[20:23]\nds_read_b128 v[76:79], v7 offset:36864\nv_mul_f32 v41, v69, v69\nv_mfma_f32_16x16x32_bf16 v[24:27], v[120:123], v[60:63], v[24:27]\n" \
"s_waitcnt lgkmcnt(2)\nv_mul_f32 v42, v70, v70\nv_mul_f32 v43, v71, v71\nv_mfma_f32_16x16x32_bf16 v[28:31], v[120:123], v[64:67], v[28:31]\n" \
"v_fma_f32 v40, v40, v253, v252\nv_fma_f32 v41, v41, v253, v252\nv_mfma_f32_16x16x32_bf16 v[8:11], a[12:15], v[92:95], v[8:11]\nv_fma_f32 v42, v42, v253, v252\n" \
"v_fma_f32 v43, v43, v253, v252\nv_mfma_f32_16x16x32_bf16 v[12:15], a[12:15], v[72:75], v[12:15]\nv_mul_f32 v40, v40, v68\nv_mul_f32 v41, v41, v69\n" \
"v_mfma_f32_16x16x32_bf16 v[16:19], a[140:143], v[92:95], v[16:19]\nds_read_b128 v[60:63], v7 offset:5120\nv_mul_f32 v42, v42, v70\nv_mfma_f32_16x16x32_bf16 v[20:23], a[140:143], v[72:75], v[20:23]\n" \
"ds_read_b128 v[64:67], v7 offset:37888\nv_mul_f32 v43, v43, v71\nv_mfma_f32_16x16x32_bf16 v[24:27], v[124:127], v[92:95], v[24:27]\ns_waitcnt lgkmcnt(2)\n" \
"v_exp_f32 v40, v40\nv_mfma_f32_16x16x32_bf16 v[28:31], v[124:127], v[72:75], v[28:31]\nv_exp_f32 v41, v41\nv_mfma_f32_16x16x32_bf16 v[8:11], a[16:19], v[36:39], v[8:11]\n" \
"v_exp_f32 v42, v42\nv_mfma_f32_16x16x32_bf16 v[12:15], a[16:19], v[76:79], v[12:15]\nv_exp_f32 v43, v43\nv_mfma_f32_16x16x32_bf16 v[16:19], a[144:147], v[36:39], v[16:19]\n" \
"ds_read_b128 v[72:75], v7 offset:6144\nv_fma_f32 v40, v40, v254, v254\nv_mfma_f32_16x16x32_bf16 v[20:23], a[144:147], v[76:79], v[20:23]\nds_read_b128 v[92:95], v7 offset:38912\n" \
"v_fma_f32 v41, v41, v254, v254\nv_mfma_f32_16x16x32_bf16 v[24:27], v[128:131], v[36:39], v[24:27]\ns_waitcnt lgkmcnt(2)\nv_rcp_f32 v40, v40\n" \
"v_mfma_f32_16x16x32_bf16 v[28:31], v[128:131], v[76:79], v[28:31]\nv_rcp_f32 v41, v41\nv_mfma_f32_16x16x32_bf16 v[8:11], a[20:23], v[60:63], v[8:11]\nv_fma_f32 v42, v42, v254, v254\n" \
"v_fma_f32 v43, v43, v254, v254\nv_mfma_f32_16x16x32_bf16 v[12:15], a[20:23], v[64:67], v[12:15]\nv_rcp_f32 v42, v42\nv_mfma_f32_16x16x32_bf16 v[16:19], a[148:151], v[60:63], v[16:19]\n" \
"ds_read_b128 v[36:39], v7 offset:7168\nv_mul_f32 v80, v68, v80\nv_mfma_f32_16x16x32_bf16 v[20:23], a[148:151], v[64:67], v[20:23]\ns_waitcnt vmcnt(1)\n" \
"v_max3_u32 v35, v52, v54, v56\nv_max_u32 v35, v35, v58\nv_mfma_f32_16x16x32_bf16 v[24:27], v[132:135], v[60:63], v[24:27]\nds_read_b128 v[76:79], v7 offset:39936\n" \
"v_cmp_eq_u32 s[98:99], -1, v35\nv_mfma_f32_16x16x32_bf16 v[28:31], v[132:135], v[64:67], v[28:31]\ns_waitcnt lgkmcnt(2)\nv_rcp_f32 v43, v43\n" \
"v_mfma_f32_16x16x32_bf16 v[8:11], a[24:27], v[72:75], v[8:11]\ns_cmp_eq_u64 s[98:99], 0\ns_cselect_b32 s14, s10, 0\nv_mul_f32 v81, v69, v81\n" \
"v_mfma_f32_16x16x32_bf16 v[12:15], a[24:27], v[92:95], v[12:15]\nbuffer_load_dwordx4 v0, s[12:15], s96 offen offset:0 sc1 lds\nv_mfma_f32_16x16x32_bf16 v[16:19], a[152:155], v[72:75], v[16:19]\nds_read_b128 v[60:63], v7 offset:8192\n" \
"v_mul_f32 v82, v70, v82\nv_mfma_f32_16x16x32_bf16 v[20:23], a[152:155], v[92:95], v[20:23]\nds_read_b128 v[64:67], v7 offset:40960\nv_mul_f32 v83, v71, v83\n" \
"v_mfma_f32_16x16x32_bf16 v[24:27], v[136:139], v[72:75], v[24:27]\ns_waitcnt lgkmcnt(2)\nv_mul_f32 v80, v80, v40\nv_mul_f32 v81, v81, v41\n" \
"v_mfma_f32_16x16x32_bf16 v[28:31], v[136:139], v[92:95], v[28:31]\nv_mul_f32 v82, v82, v42\nv_mul_f32 v83, v83, v43\nv_mfma_f32_16x16x32_bf16 v[8:11], a[28:31], v[36:39], v[8:11]\n" \
"v_cvt_pk_bf16_f32 v80, v80, v81\nv_cvt_pk_bf16_f32 v81, v82, v83\nv_mfma_f32_16x16x32_bf16 v[12:15], a[28:31], v[76:79], v[12:15]\nbuffer_load_dwordx4 v0, s[12:15], s96 offen offset:1024 sc1 lds\n" \
"v_mfma_f32_16x16x32_bf16 v[16:19], a[156:159], v[36:39], v[16:19]\nds_read_b128 v[40:43], v7 offset:9216\nv_mfma_f32_16x16x32_bf16 v[20:23], a[156:159], v[76:79], v[20:23]\nds_read_b128 v[68:71], v7 offset:41984\n" \
"v_mfma_f32_16x16x32_bf16 v[24:27], v[140:143], v[36:39], v[24:27]\ns_waitcnt lgkmcnt(2)\nv_permlane16_swap_b32 v44, v48\nv_mfma_f32_16x16x32_bf16 v[28:31], v[140:143], v[76:79], v[28:31]\n" \
"buffer_load_dwordx4 v0, s[12:15], s96 offen offset:2048 sc1 lds\nv_mfma_f32_16x16x32_bf16 v[8:11], a[32:35], v[60:63], v[8:11]\nv_permlane16_swap_b32 v45, v49\nv_mfma_f32_16x16x32_bf16 v[12:15], a[32:35], v[64:67], v[12:15]\n" \
"v_permlane16_swap_b32 v46, v50\nv_mfma_f32_16x16x32_bf16 v[16:19], a[160:163], v[60:63], v[16:19]\nds_read_b128 v[36:39], v7 offset:10240\nv_mul_f32 v72, v44, v44\n" \
"v_mfma_f32_16x16x32_bf16 v[20:23], a[160:163], v[64:67], v[20:23]\nds_read_b128 v[76:79], v7 offset:43008\nv_mul_f32 v73, v45, v45\nv_mfma_f32_16x16x32_bf16 v[24:27], v[144:147], v[60:63], v[24:27]\n" \
"s_waitcnt lgkmcnt(2)\nbuffer_load_dwordx4 v0, s[12:15], s96 offen offset:3072 sc1 lds\nv_mfma_f32_16x16x32_bf16 v[28:31], v[144:147], v[64:67], v[28:31]\ns_xor_b32 m0, s22, 0x1000\n" \
"s_xor_b32 s96, s96, 0x1000\nv_mul_f32 v74, v46, v46\nv_mfma_f32_16x16x32_bf16 v[8:11], a[36:39], v[40:43], v[8:11]\nv_permlane16_swap_b32 v47, v51\n" \
"v_mfma_f32_16x16x32_bf16 v[12:15], a[36:39], v[68:71], v[12:15]\nv_mul_f32 v75, v47, v47\nv_fma_f32 v72, v72, v253, v252\nv_mfma_f32_16x16x32_bf16 v[16:19], a[164:167], v[40:43], v[16:19]\n" \
"ds_read_b128 v[60:63], v7 offset:11264\nv_fma_f32 v73, v73, v253, v252\nv_mfma_f32_16x16x32_bf16 v[20:23], a[164:167], v[68:71], v[20:23]\nbuffer_load_dwordx4 v0, s[12:15], s96 offen offset:0 sc1 lds\n" \
"v_mfma_f32_16x16x32_bf16 v[24:27], v[148:151], v[40:43], v[24:27]\nds_read_b128 v[64:67], v7 offset:44032\nv_fma_f32 v74, v74, v253, v252\nv_mfma_f32_16x16x32_bf16 v[28:31], v[148:151], v[68:71], v[28:31]\n" \
"s_waitcnt lgkmcnt(2)\nv_fma_f32 v75, v75, v253, v252\nv_mul_f32 v72, v72, v44\nv_mfma_f32_16x16x32_bf16 v[8:11], a[40:43], v[36:39], v[8:11]\n" \
"v_exp_f32 v72, v72\nv_mfma_f32_16x16x32_bf16 v[12:15], a[40:43], v[76:79], v[12:15]\nv_mul_f32 v73, v73, v45\nv_mul_f32 v74, v74, v46\n" \
"v_mfma_f32_16x16x32_bf16 v[16:19], a[168:171], v[36:39], v[16:19]\nbuffer_load_dwordx4 v0, s[12:15], s96 offen offset:1024 sc1 lds\nv_mfma_f32_16x16x32_bf16 v[20:23], a[168:171], v[76:79], v[20:23]\nds_read_b128 v[40:43], v7 offset:12288\n" \
"v_mul_f32 v75, v75, v47\nv_mfma_f32_16x16x32_bf16 v[24:27], v[152:155], v[36:39], v[24:27]\nds_read_b128 v[68:71], v7 offset:45056\nv_fma_f32 v72, v72, v254, v254\n" \
"v_mfma_f32_16x16x32_bf16 v[28:31], v[152:155], v[76:79], v[28:31]\ns_waitcnt lgkmcnt(2)\nv_exp_f32 v73, v73\nv_mfma_f32_16x16x32_bf16 v[8:11], a[44:47], v[60:63], v[8:11]\n" \
"v_exp_f32 v74, v74\nv_mfma_f32_16x16x32_bf16 v[12:15], a[44:47], v[64:67], v[12:15]\nbuffer_load_dwordx4 v0, s[12:15], s96 offen offset:2048 sc1 lds\nv_mfma_f32_16x16x32_bf16 v[16:19], a[172:175], v[60:63], v[16:19]\n" \
"ds_read_b128 v[36:39], v7 offset:13312\nv_fma_f32 v73, v73, v254, v254\nv_mfma_f32_16x16x32_bf16 v[20:23], a[172:175], v[64:67], v[20:23]\nds_read_b128 v[76:79], v7 offset:46080\n" \
"v_fma_f32 v74, v74, v254, v254\nv_mfma_f32_16x16x32_bf16 v[24:27], v[156:159], v[60:63], v[24:27]\ns_waitcnt lgkmcnt(2)\nv_exp_f32 v75, v75\n" \
"v_mfma_f32_16x16x32_bf16 v[28:31], v[156:159], v[64:67], v[28:31]\nv_rcp_f32 v72, v72\nv_mfma_f32_16x16x32_bf16 v[8:11], a[48:51], v[40:43], v[8:11]\nbuffer_load_dwordx4 v0, s[12:15], s96 offen offset:3072 sc1 lds\n" \
"v_mfma_f32_16x16x32_bf16 v[12:15], a[48:51], v[68:71], v[12:15]\ns_xor_b32 m0, s22, 0x8000\ns_xor_b32 s96, s96, 0x9000\nv_fma_f32 v75, v75, v254, v254\n" \
"v_mfma_f32_16x16x32_bf16 v[16:19], a[176:179], v[40:43], v[16:19]\nds_read_b128 v[60:63], v7 offset:14336\nv_mul_f32 v48, v44, v48\nv_mfma_f32_16x16x32_bf16 v[20:23], a[176:179], v[68:71], v[20:23]\n" \
"ds_read_b128 v[64:67], v7 offset:47104\nv_mul_f32 v49, v45, v49\nv_mfma_f32_16x16x32_bf16 v[24:27], v[160:163], v[40:43], v[24:27]\ns_waitcnt lgkmcnt(2)\n" \
"v_rcp_f32 v73, v73\nv_mfma_f32_16x16x32_bf16 v[28:31], v[160:163], v[68:71], v[28:31]\nbuffer_load_dwordx4 v0, s[12:15], s96 offen offset:0 sc1 lds\nv_mfma_f32_16x16x32_bf16 v[8:11], a[52:55], v[36:39], v[8:11]\n" \
"v_rcp_f32 v74, v74\nv_mfma_f32_16x16x32_bf16 v[12:15], a[52:55], v[76:79], v[12:15]\nv_rcp_f32 v75, v75\nv_mfma_f32_16x16x32_bf16 v[16:19], a[180:183], v[36:39], v[16:19]\n" \
"ds_read_b128 v[40:43], v7 offset:15360\nv_mul_f32 v50, v46, v50\nv_mfma_f32_16x16x32_bf16 v[20:23], a[180:183], v[76:79], v[20:23]\nds_read_b128 v[68:71], v7 offset:48128\n" \
"v_mul_f32 v51, v47, v51\nv_mfma_f32_16x16x32_bf16 v[24:27], v[164:167], v[36:39], v[24:27]\ns_waitcnt lgkmcnt(2)\nbuffer_load_dwordx4 v0, s[12:15], s96 offen offset:1024 sc1 lds\n" \
"v_mfma_f32_16x16x32_bf16 v[28:31], v[164:167], v[76:79], v[28:31]\nv_mul_f32 v48, v48, v72\nv_mul_f32 v49, v49, v73\nv_mfma_f32_16x16x32_bf16 v[8:11], a[56:59], v[60:63], v[8:11]\n" \
"v_mul_f32 v50, v50, v74\nv_mul_f32 v51, v51, v75\nv_mfma_f32_16x16x32_bf16 v[12:15], a[56:59], v[64:67], v[12:15]\nv_cvt_pk_bf16_f32 v48, v48, v49\n" \
"v_cvt_pk_bf16_f32 v49, v50, v51\nv_mfma_f32_16x16x32_bf16 v[16:19], a[184:187], v[60:63], v[16:19]\nds_read_b128 v[36:39], v7 offset:16384\nv_mfma_f32_16x16x32_bf16 v[20:23], a[184:187], v[64:67], v[20:23]\n" \
"buffer_load_dwordx4 v0, s[12:15], s96 offen offset:2048 sc1 lds\nv_mfma_f32_16x16x32_bf16 v[24:27], v[168:171], v[60:63], v[24:27]\ns_waitcnt lgkmcnt(1)\nds_read_b128 v[44:47], v7 offset:49152\n" \
"v_mfma_f32_16x16x32_bf16 v[28:31], v[168:171], v[64:67], v[28:31]\nv_permlane16_swap_b32 v84, v88\nv_mfma_f32_16x16x32_bf16 v[8:11], a[60:63], v[40:43], v[8:11]\nv_permlane16_swap_b32 v85, v89\n" \
"v_mfma_f32_16x16x32_bf16 v[12:15], a[60:63], v[68:71], v[12:15]\nv_permlane16_swap_b32 v86, v90\nv_mfma_f32_16x16x32_bf16 v[16:19], a[188:191], v[40:43], v[16:19]\nbuffer_load_dwordx4 v0, s[12:15], s96 offen offset:3072 sc1 lds\n" \
"v_mfma_f32_16x16x32_bf16 v[20:23], a[188:191], v[68:71], v[20:23]\nds_read_b128 v[60:63], v7 offset:17408\ns_xor_b32 m0, s22, 0x9000\nv_mfma_f32_16x16x32_bf16 v[24:27], v[172:175], v[40:43], v[24:27]\n" \
"ds_read_b128 v[64:67], v7 offset:50176\ns_xor_b32 s96, s96, 0x1000\nv_mfma_f32_16x16x32_bf16 v[28:31], v[172:175], v[68:71], v[28:31]\ns_waitcnt lgkmcnt(2)\n" \
"v_permlane16_swap_b32 v87, v91\nv_mfma_f32_16x16x32_bf16 v[8:11], a[64:67], v[36:39], v[8:11]\ns_xor_b32 s22, s22, 0x10000\nv_mul_f32 v40, v84, v84\n" \
"v_mul_f32 v41, v85, v85\nv_mfma_f32_16x16x32_bf16 v[12:15], a[64:67], v[44:47], v[12:15]\nbuffer_load_dwordx4 v0, s[12:15], s96 offen offset:0 sc1 lds\nv_mfma_f32_16x16x32_bf16 v[16:19], a[192:195], v[36:39], v[16:19]\n" \
"ds_read_b128 v[68:71], v7 offset:18432\nv_mul_f32 v42, v86, v86\nv_mfma_f32_16x16x32_bf16 v[20:23], a[192:195], v[44:47], v[20:23]\nds_read_b128 v[72:75], v7 offset:51200\n" \
"v_mul_f32 v43, v87, v87\nv_mfma_f32_16x16x32_bf16 v[24:27], v[176:179], v[36:39], v[24:27]\ns_waitcnt lgkmcnt(2)\nv_fma_f32 v40, v40, v253, v252\n" \
"v_fma_f32 v41, v41, v253, v252\nv_mfma_f32_16x16x32_bf16 v[28:31], v[176:179], v[44:47], v[28:31]\nv_fma_f32 v42, v42, v253, v252\nv_fma_f32 v43, v43, v253, v252\n" \
"v_mfma_f32_16x16x32_bf16 v[8:11], a[68:71], v[60:63], v[8:11]\nbuffer_load_dwordx4 v0, s[12:15], s96 offen offset:1024 sc1 lds\nv_mfma_f32_16x16x32_bf16 v[12:15], a[68:71], v[64:67], v[12:15]\nv_mul_f32 v40, v40, v84\n" \
"v_mul_f32 v41, v41, v85\nv_mfma_f32_16x16x32_bf16 v[16:19], a[196:199], v[60:63], v[16:19]\nds_read_b128 v[36:39], v7 offset:19456\nv_mul_f32 v42, v42, v86\n" \
"v_mfma_f32_16x16x32_bf16 v[20:23], a[196:199], v[64:67], v[20:23]\nds_read_b128 v[44:47], v7 offset:52224\nv_mul_f32 v43, v43, v87\nv_mfma_f32_16x16x32_bf16 v[24:27], v[180:183], v[60:63], v[24:27]\n" \
"s_waitcnt lgkmcnt(2)\nv_exp_f32 v40, v40\nv_mfma_f32_16x16x32_bf16 v[28:31], v[180:183], v[64:67], v[28:31]\nbuffer_load_dwordx4 v0, s[12:15], s96 offen offset:2048 sc1 lds\n" \
"v_mfma_f32_16x16x32_bf16 v[8:11], a[72:75], v[68:71], v[8:11]\nv_exp_f32 v41, v41\nv_mfma_f32_16x16x32_bf16 v[12:15], a[72:75], v[72:75], v[12:15]\nv_exp_f32 v42, v42\n" \
"v_mfma_f32_16x16x32_bf16 v[16:19], a[200:203], v[68:71], v[16:19]\nds_read_b128 v[60:63], v7 offset:20480\nv_fma_f32 v40, v40, v254, v254\nv_mfma_f32_16x16x32_bf16 v[20:23], a[200:203], v[72:75], v[20:23]\n" \
"ds_read_b128 v[64:67], v7 offset:53248\nv_fma_f32 v41, v41, v254, v254\nv_mfma_f32_16x16x32_bf16 v[24:27], v[184:187], v[68:71], v[24:27]\ns_waitcnt lgkmcnt(2)\n" \
"buffer_load_dwordx4 v0, s[12:15], s96 offen offset:3072 sc1 lds\nv_mfma_f32_16x16x32_bf16 v[28:31], v[184:187], v[72:75], v[28:31]\nv_exp_f32 v43, v43\nv_mfma_f32_16x16x32_bf16 v[8:11], a[76:79], v[36:39], v[8:11]\n" \
"v_rcp_f32 v40, v40\nv_mfma_f32_16x16x32_bf16 v[12:15], a[76:79], v[44:47], v[12:15]\nv_rcp_f32 v41, v41\nv_mfma_f32_16x16x32_bf16 v[16:19], a[204:207], v[36:39], v[16:19]\n" \
"ds_read_b128 v[68:71], v7 offset:21504\nv_fma_f32 v42, v42, v254, v254\nv_mfma_f32_16x16x32_bf16 v[20:23], a[204:207], v[44:47], v[20:23]\nds_read_b128 v[72:75], v7 offset:54272\n" \
"v_fma_f32 v43, v43, v254, v254\nv_mfma_f32_16x16x32_bf16 v[24:27], v[188:191], v[36:39], v[24:27]\ns_waitcnt lgkmcnt(2)\nv_rcp_f32 v42, v42\n" \
"v_mfma_f32_16x16x32_bf16 v[28:31], v[188:191], v[44:47], v[28:31]\nv_rcp_f32 v43, v43\nv_mfma_f32_16x16x32_bf16 v[8:11], a[80:83], v[60:63], v[8:11]\nbuffer_store_dwordx2 v[80:81], v2, s[8:11], s16 offen offset:0 sc1\n" \
"v_mul_f32 v88, v84, v88\nv_mfma_f32_16x16x32_bf16 v[12:15], a[80:83], v[64:67], v[12:15]\nv_mul_f32 v89, v85, v89\nv_mul_f32 v90, v86, v90\n" \
"v_mfma_f32_16x16x32_bf16 v[16:19], a[208:211], v[60:63], v[16:19]\nbuffer_store_dwordx2 v[48:49], v2, s[8:11], s17 offen offset:256 sc1\nv_mul_f32 v91, v87, v91\nv_mfma_f32_16x16x32_bf16 v[20:23], a[208:211], v[64:67], v[20:23]\n" \
"v_mul_f32 v88, v88, v40\nv_mul_f32 v89, v89, v41\nv_mfma_f32_16x16x32_bf16 v[24:27], v[192:195], v[60:63], v[24:27]\nds_read_b128 v[36:39], v7 offset:22528\n" \
"v_mul_f32 v90, v90, v42\nv_mul_f32 v91, v91, v43\nv_mfma_f32_16x16x32_bf16 v[28:31], v[192:195], v[64:67], v[28:31]\ns_waitcnt lgkmcnt(1)\n" \
"ds_read_b128 v[40:43], v7 offset:55296\nv_cvt_pk_bf16_f32 v88, v88, v89\nv_mfma_f32_16x16x32_bf16 v[8:11], a[84:87], v[68:71], v[8:11]\nds_read_b128 v[44:47], v4 offset:0\n" \
"v_cvt_pk_bf16_f32 v89, v90, v91\nv_mfma_f32_16x16x32_bf16 v[12:15], a[84:87], v[72:75], v[12:15]\nbuffer_store_dwordx2 v[88:89], v2, s[8:11], s17 offen offset:512 sc1\nv_add_f32 v52, v52, v56\n" \
"v_mfma_f32_16x16x32_bf16 v[16:19], a[212:215], v[68:71], v[16:19]\nds_read_b128 v[48:51], v7 offset:23552\nv_add_f32 v53, v53, v57\nv_mfma_f32_16x16x32_bf16 v[20:23], a[212:215], v[72:75], v[20:23]\n" \
"ds_read_b128 v[60:63], v7 offset:56320\nv_add_f32 v54, v54, v58\nv_mfma_f32_16x16x32_bf16 v[24:27], v[196:199], v[68:71], v[24:27]\nds_read_b128 v[64:67], v4 offset:512\n" \
"v_add_f32 v55, v55, v59\nv_mfma_f32_16x16x32_bf16 v[28:31], v[196:199], v[72:75], v[28:31]\ns_waitcnt lgkmcnt(4)\nv_permlane16_swap_b32 v52, v54\n" \
"v_mfma_f32_16x16x32_bf16 v[8:11], a[88:91], v[36:39], v[8:11]\nv_permlane16_swap_b32 v53, v55\nv_mfma_f32_16x16x32_bf16 v[12:15], a[88:91], v[40:43], v[12:15]\ns_waitcnt lgkmcnt(3)\n" \
"v_add_f32 v44, v44, v45\nv_add_f32 v46, v46, v47\nv_mfma_f32_16x16x32_bf16 v[16:19], a[216:219], v[36:39], v[16:19]\nds_read_b128 v[56:59], v7 offset:24576\n" \
"v_add_f32 v44, v44, v46\nv_mfma_f32_16x16x32_bf16 v[20:23], a[216:219], v[40:43], v[20:23]\nds_read_b128 v[68:71], v7 offset:57344\nv_mul_f32 v44, 0xba800000, v44\n" \
"v_mfma_f32_16x16x32_bf16 v[24:27], v[200:203], v[36:39], v[24:27]\ns_waitcnt lgkmcnt(2)\nv_add_f32 v52, v52, v54\nv_add_f32 v53, v53, v55\n" \
"v_mfma_f32_16x16x32_bf16 v[28:31], v[200:203], v[40:43], v[28:31]\nv_add_f32 v64, v64, v65\nv_add_f32 v66, v66, v67\nv_mfma_f32_16x16x32_bf16 v[8:11], a[92:95], v[48:51], v[8:11]\n" \
"v_add_f32 v64, v64, v66\nv_mul_f32 v65, v44, -v44\nv_mfma_f32_16x16x32_bf16 v[12:15], a[92:95], v[60:63], v[12:15]\nv_fmamk_f32 v35, v64, 0x3a800000, v65\n" \
"v_cvt_pk_bf16_f32 v35, v35, v35\nv_mfma_f32_16x16x32_bf16 v[16:19], a[220:223], v[48:51], v[16:19]\nds_read_b128 v[64:67], v7 offset:25600\nv_lshlrev_b32 v35, 16, v35\n" \
"v_mfma_f32_16x16x32_bf16 v[20:23], a[220:223], v[60:63], v[20:23]\nds_read_b128 v[72:75], v7 offset:58368\nv_max_f32 v35, 0, v35\nv_mfma_f32_16x16x32_bf16 v[24:27], v[204:207], v[48:51], v[24:27]\n" \
"s_waitcnt lgkmcnt(2)\nv_add_f32 v35, 0x3727c5ac, v35\nv_cvt_pk_bf16_f32 v111, v35, v44\nv_mfma_f32_16x16x32_bf16 v[28:31], v[204:207], v[60:63], v[28:31]\n" \
"v_permlane32_swap_b32 v52, v53\nv_mfma_f32_16x16x32_bf16 v[8:11], a[96:99], v[56:59], v[8:11]\nv_lshlrev_b32 v35, 16, v111\nv_add_f32 v52, v52, v53\n" \
"v_mfma_f32_16x16x32_bf16 v[12:15], a[96:99], v[68:71], v[12:15]\nv_sqrt_f32 v35, v35\nv_mfma_f32_16x16x32_bf16 v[16:19], a[224:227], v[56:59], v[16:19]\nds_read_b128 v[36:39], v7 offset:26624\n" \
"v_cvt_pk_bf16_f32 v35, v35, v35\nv_mfma_f32_16x16x32_bf16 v[20:23], a[224:227], v[68:71], v[20:23]\nds_read_b128 v[40:43], v7 offset:59392\nv_lshlrev_b32 v35, 16, v35\n" \
"v_mfma_f32_16x16x32_bf16 v[24:27], v[208:211], v[56:59], v[24:27]\ns_waitcnt lgkmcnt(2)\nv_rcp_f32 v34, v35\nv_mfma_f32_16x16x32_bf16 v[28:31], v[208:211], v[68:71], v[28:31]\n" \
"ds_write_b32 v5, v52 offset:0\nv_mfma_f32_16x16x32_bf16 v[8:11], a[100:103], v[64:67], v[8:11]\nv_and_b32 v111, 0xffff0000, v111\nv_mfma_f32_16x16x32_bf16 v[12:15], a[100:103], v[72:75], v[12:15]\n" \
"v_mfma_f32_16x16x32_bf16 v[16:19], a[228:231], v[64:67], v[16:19]\nds_read_b128 v[44:47], v7 offset:27648\nv_mfma_f32_16x16x32_bf16 v[20:23], a[228:231], v[72:75], v[20:23]\nds_read_b128 v[48:51], v7 offset:60416\n" \
"v_mfma_f32_16x16x32_bf16 v[24:27], v[212:215], v[64:67], v[24:27]\nv_mfma_f32_16x16x32_bf16 v[28:31], v[212:215], v[72:75], v[28:31]\ns_branch 1b\n4:\n" \
"s_barrier\ns_lshr_b32 s94, s20, 1\nv_readlane_b32 s94, v6, s94\ns_and_b32 s95, s20, 1\n" \
"s_lshl_b32 s95, s95, 3\ns_lshr_b32 s94, s94, s95\ns_and_b32 s94, s94, 0xff\ns_lshl_b32 s95, s94, 13\n" \
"buffer_load_dwordx4 v[96:99], v1, s[8:11], s95 offen offset:0 sc1\nbuffer_load_dwordx4 v[100:103], v1, s[8:11], s95 offen offset:1024 sc1\nv_fma_f32 v84, v32, v240, v8\nv_fma_f32 v85, v32, v241, v9\n" \
"v_fma_f32 v86, v32, v242, v10\nv_fma_f32 v87, v32, v243, v11\nv_fma_f32 v88, v33, v240, v12\nv_fma_f32 v89, v33, v241, v13\n" \
"v_fma_f32 v90, v33, v242, v14\nv_fma_f32 v91, v33, v243, v15\nv_permlane16_swap_b32 v84, v88\nv_permlane16_swap_b32 v85, v89\n" \
"v_permlane16_swap_b32 v86, v90\nv_permlane16_swap_b32 v87, v91\nv_mul_f32 v92, v84, v84\nv_mul_f32 v93, v85, v85\n" \
"v_mul_f32 v94, v86, v86\nv_mul_f32 v95, v87, v87\nv_fma_f32 v92, v92, v253, v252\nv_fma_f32 v93, v93, v253, v252\n" \
"v_fma_f32 v94, v94, v253, v252\nv_fma_f32 v95, v95, v253, v252\nv_mul_f32 v92, v92, v84\nv_mul_f32 v93, v93, v85\n" \
"v_mul_f32 v94, v94, v86\nv_mul_f32 v95, v95, v87\nv_mul_f32 v88, v84, v88\nv_mul_f32 v89, v85, v89\n" \
"v_mul_f32 v90, v86, v90\nv_mul_f32 v91, v87, v91\nv_exp_f32 v92, v92\nv_exp_f32 v93, v93\n" \
"v_exp_f32 v94, v94\nv_exp_f32 v95, v95\nv_fma_f32 v92, v92, v254, v254\nv_fma_f32 v93, v93, v254, v254\n" \
"v_fma_f32 v94, v94, v254, v254\nv_fma_f32 v95, v95, v254, v254\nv_rcp_f32 v92, v92\nv_rcp_f32 v93, v93\n" \
"v_rcp_f32 v94, v94\nv_rcp_f32 v95, v95\nv_mul_f32 v88, v88, v92\nv_mul_f32 v89, v89, v93\n" \
"v_mul_f32 v90, v90, v94\nv_mul_f32 v91, v91, v95\nv_cvt_pk_bf16_f32 v88, v88, v89\nv_cvt_pk_bf16_f32 v89, v90, v91\n" \
"buffer_store_dwordx2 v[88:89], v2, s[8:11], s16 offen offset:0 sc1\nv_fma_f32 v84, v32, v244, v16\nv_fma_f32 v85, v32, v245, v17\nv_fma_f32 v86, v32, v246, v18\n" \
"v_fma_f32 v87, v32, v247, v19\nv_fma_f32 v88, v33, v244, v20\nv_fma_f32 v89, v33, v245, v21\nv_fma_f32 v90, v33, v246, v22\n" \
"v_fma_f32 v91, v33, v247, v23\nv_permlane16_swap_b32 v84, v88\nv_permlane16_swap_b32 v85, v89\nv_permlane16_swap_b32 v86, v90\n" \
"v_permlane16_swap_b32 v87, v91\nv_mul_f32 v92, v84, v84\nv_mul_f32 v93, v85, v85\nv_mul_f32 v94, v86, v86\n" \
"v_mul_f32 v95, v87, v87\nv_fma_f32 v92, v92, v253, v252\nv_fma_f32 v93, v93, v253, v252\nv_fma_f32 v94, v94, v253, v252\n" \
"v_fma_f32 v95, v95, v253, v252\nv_mul_f32 v92, v92, v84\nv_mul_f32 v93, v93, v85\nv_mul_f32 v94, v94, v86\n" \
"v_mul_f32 v95, v95, v87\nv_mul_f32 v88, v84, v88\nv_mul_f32 v89, v85, v89\nv_mul_f32 v90, v86, v90\n" \
"v_mul_f32 v91, v87, v91\nv_exp_f32 v92, v92\nv_exp_f32 v93, v93\nv_exp_f32 v94, v94\n" \
"v_exp_f32 v95, v95\nv_fma_f32 v92, v92, v254, v254\nv_fma_f32 v93, v93, v254, v254\nv_fma_f32 v94, v94, v254, v254\n" \
"v_fma_f32 v95, v95, v254, v254\nv_rcp_f32 v92, v92\nv_rcp_f32 v93, v93\nv_rcp_f32 v94, v94\n" \
"v_rcp_f32 v95, v95\nv_mul_f32 v88, v88, v92\nv_mul_f32 v89, v89, v93\nv_mul_f32 v90, v90, v94\n" \
"v_mul_f32 v91, v91, v95\nv_cvt_pk_bf16_f32 v88, v88, v89\nv_cvt_pk_bf16_f32 v89, v90, v91\nbuffer_store_dwordx2 v[88:89], v2, s[8:11], s17 offen offset:256 sc1\n" \
"v_fma_f32 v84, v32, v248, v24\nv_fma_f32 v85, v32, v249, v25\nv_fma_f32 v86, v32, v250, v26\nv_fma_f32 v87, v32, v251, v27\n" \
"v_fma_f32 v88, v33, v248, v28\nv_fma_f32 v89, v33, v249, v29\nv_fma_f32 v90, v33, v250, v30\nv_fma_f32 v91, v33, v251, v31\n" \
"v_permlane16_swap_b32 v84, v88\nv_permlane16_swap_b32 v85, v89\nv_permlane16_swap_b32 v86, v90\nv_permlane16_swap_b32 v87, v91\n" \
"v_mul_f32 v92, v84, v84\nv_mul_f32 v93, v85, v85\nv_mul_f32 v94, v86, v86\nv_mul_f32 v95, v87, v87\n" \
"v_fma_f32 v92, v92, v253, v252\nv_fma_f32 v93, v93, v253, v252\nv_fma_f32 v94, v94, v253, v252\nv_fma_f32 v95, v95, v253, v252\n" \
"v_mul_f32 v92, v92, v84\nv_mul_f32 v93, v93, v85\nv_mul_f32 v94, v94, v86\nv_mul_f32 v95, v95, v87\n" \
"v_mul_f32 v88, v84, v88\nv_mul_f32 v89, v85, v89\nv_mul_f32 v90, v86, v90\nv_mul_f32 v91, v87, v91\n" \
"v_exp_f32 v92, v92\nv_exp_f32 v93, v93\nv_exp_f32 v94, v94\nv_exp_f32 v95, v95\n" \
"v_fma_f32 v92, v92, v254, v254\nv_fma_f32 v93, v93, v254, v254\nv_fma_f32 v94, v94, v254, v254\nv_fma_f32 v95, v95, v254, v254\n" \
"v_rcp_f32 v92, v92\nv_rcp_f32 v93, v93\nv_rcp_f32 v94, v94\nv_rcp_f32 v95, v95\n" \
"v_mul_f32 v88, v88, v92\nv_mul_f32 v89, v89, v93\nv_mul_f32 v90, v90, v94\nv_mul_f32 v91, v91, v95\n" \
"v_cvt_pk_bf16_f32 v88, v88, v89\nv_cvt_pk_bf16_f32 v89, v90, v91\nbuffer_store_dwordx2 v[88:89], v2, s[8:11], s17 offen offset:512 sc1\ns_mov_b32 s16, 0x80000000\n" \
"s_mov_b32 s17, 0x80000000\nv_mov_b32 v84, s20\ns_cmp_ge_u32 s20, 0x80\ns_cbranch_scc1 6f\n" \
"s_waitcnt vmcnt(3)\nv_max3_u32 v85, v96, v98, v100\nv_max_u32 v85, v85, v102\nv_cmp_eq_u32 vcc, -1, v85\n" \
"s_cbranch_vccz 8f\n6:\ns_waitcnt vmcnt(0)\nbuffer_store_dword v84, v3, s[8:11], 0 offen offset:0 sc1\n" \
"s_mov_b32 s23, 0x40000000\ns_cbranch_scc1 9f\n7:\ns_add_u32 s24, s24, 1\n" \
"s_cmp_lt_u32 s24, 0x40000\ns_cbranch_scc0 9f\nbuffer_load_dwordx4 v[96:99], v1, s[8:11], s95 offen offset:0 sc1\nbuffer_load_dwordx4 v[100:103], v1, s[8:11], s95 offen offset:1024 sc1\n" \
"s_waitcnt vmcnt(0)\nv_max3_u32 v85, v96, v98, v100\nv_max_u32 v85, v85, v102\nv_cmp_eq_u32 vcc, -1, v85\n" \
"s_cbranch_vccz 8f\ns_sleep 8\ns_branch 7b\n8:\n" \
"s_lshl_b32 s94, s94, 16\ns_add_u32 s94, s94, s21\ns_xor_b32 s95, s22, 0x10000\ns_xor_b32 m0, s95, 0\n" \
"s_xor_b32 s94, s94, 0\nbuffer_load_dwordx4 v0, s[8:11], s94 offen offset:0 sc1 lds\nbuffer_load_dwordx4 v0, s[8:11], s94 offen offset:1024 sc1 lds\nbuffer_load_dwordx4 v0, s[8:11], s94 offen offset:2048 sc1 lds\n" \
"buffer_load_dwordx4 v0, s[8:11], s94 offen offset:3072 sc1 lds\ns_xor_b32 m0, s95, 0x1000\ns_xor_b32 s94, s94, 0x1000\nbuffer_load_dwordx4 v0, s[8:11], s94 offen offset:0 sc1 lds\n" \
"buffer_load_dwordx4 v0, s[8:11], s94 offen offset:1024 sc1 lds\nbuffer_load_dwordx4 v0, s[8:11], s94 offen offset:2048 sc1 lds\nbuffer_load_dwordx4 v0, s[8:11], s94 offen offset:3072 sc1 lds\ns_xor_b32 m0, s95, 0x8000\n" \
"s_xor_b32 s94, s94, 0x9000\nbuffer_load_dwordx4 v0, s[8:11], s94 offen offset:0 sc1 lds\nbuffer_load_dwordx4 v0, s[8:11], s94 offen offset:1024 sc1 lds\nbuffer_load_dwordx4 v0, s[8:11], s94 offen offset:2048 sc1 lds\n" \
"buffer_load_dwordx4 v0, s[8:11], s94 offen offset:3072 sc1 lds\ns_xor_b32 m0, s95, 0x9000\ns_xor_b32 s94, s94, 0x1000\nbuffer_load_dwordx4 v0, s[8:11], s94 offen offset:0 sc1 lds\n" \
"buffer_load_dwordx4 v0, s[8:11], s94 offen offset:1024 sc1 lds\nbuffer_load_dwordx4 v0, s[8:11], s94 offen offset:2048 sc1 lds\nbuffer_load_dwordx4 v0, s[8:11], s94 offen offset:3072 sc1 lds\nv_add_f32 v96, v96, v100\n" \
"v_add_f32 v97, v97, v101\nv_add_f32 v98, v98, v102\nv_add_f32 v99, v99, v103\ns_nop 1\n" \
"v_permlane16_swap_b32 v96, v98\nv_permlane16_swap_b32 v97, v99\nv_add_f32 v96, v96, v98\nv_add_f32 v97, v97, v99\n" \
"s_nop 1\nv_permlane32_swap_b32 v96, v97\nv_add_f32 v96, v96, v97\nds_write_b32 v5, v96\n" \
"s_add_u32 s97, s20, 1\ns_lshr_b32 s96, s97, 1\nv_readlane_b32 s96, v6, s96\ns_and_b32 s97, s97, 1\n" \
"s_lshl_b32 s97, s97, 3\ns_lshr_b32 s96, s96, s97\ns_and_b32 s96, s96, 0xff\ns_lshl_b32 s97, s96, 13\n" \
"s_lshl_b32 s96, s96, 16\ns_add_u32 s96, s96, s21\nbuffer_load_dwordx4 v[52:55], v1, s[8:11], s97 offen offset:0 sc1\nbuffer_load_dwordx4 v[56:59], v1, s[8:11], s97 offen offset:1024 sc1\n" \
"s_waitcnt vmcnt(18)\nbuffer_store_dword v84, v3, s[8:11], s23 offen offset:0 sc1\ns_mov_b32 s23, 0x40000000\ns_waitcnt vmcnt(3) lgkmcnt(0)\n" \
"s_barrier\nds_read_b128 v[72:75], v7 offset:0\nds_read_b128 v[36:39], v7 offset:32768\nds_read_b128 v[40:43], v7 offset:1024\n" \
"ds_read_b128 v[76:79], v7 offset:33792\ns_waitcnt lgkmcnt(0)\ns_branch 5b\n9:\n" \
"s_waitcnt vmcnt(0)\ns_endpgm\n"
struct WMA { static constexpr int NT = 4, NP = 16, RB = 0, LB = 0, CBI = 0;
             static constexpr int kind(int j, int t) { return (A_LDS >> (16 * j + t) & 1) ? 1 : (A_VAR >> (16 * j + t) & 1) ? 2 : 0; } };
namespace la { constexpr int L_Q = 24576, L_K = 57344, L_QM = 65536, L_KM = 66048, L_SP = 69632, L_W = 73728; }
#ifndef LEAD_B
#define LEAD_B 132
#endif
DEVI float dror8(float x) { return __uint_as_float(__builtin_amdgcn_update_dpp(0u, __float_as_uint(x), 0x128, 0xF, 0xF, true)); }
DEVI float scm(float a, float m) { float r; asm("v_fma_f32 %0, %1, %2, -%3" : "=v"(r) : "v"(a), "s"(SC_L2E), "v"(m)); return r; }
DEVI float ex2(float x) { return __builtin_amdgcn_exp2f(x); }
DEVI float max3(float a, float b, float c) { float r; asm("v_max3_f32 %0, %1, %2, %3" : "=v"(r) : "v"(a), "v"(b), "v"(c)); return r; }
constexpr u64 tri(int i) { u64 m = 0; for (int l = 0; l < 64; ++l) if (4 * (l >> 4) + i <= (l & 15)) m |= 1ull << l; return m; }
DEVI unsigned vbfe(unsigned a, int o, int n) { unsigned r; asm volatile("v_bfe_u32 %0, %1, %2, %3" : "=v"(r) : "v"(a), "i"(o), "i"(n)); return r; }
DEVI unsigned vadds(unsigned a, unsigned b) { unsigned r; asm volatile("v_add_u32 %0, %1, %2" : "=v"(r) : "s"(b), "v"(a)); return r; }
DEVI float vexp(float a) { float r; asm volatile("v_exp_f32 %0, %1" : "=v"(r) : "v"(a)); return r; }
DEVI unsigned vors(unsigned a, unsigned b) { unsigned r; asm volatile("v_or_b32 %0, %1, %2" : "=v"(r) : "s"(b), "v"(a)); return r; }
DEVI float faddv(float a, float b) { float r; asm volatile("v_add_f32 %0, %1, %2" : "=v"(r) : "v"(a), "v"(b)); return r; }
DEVI unsigned packv(float a, float b) { unsigned r; asm volatile("v_cvt_pk_bf16_f32 %0, %1, %2" : "=v"(r) : "v"(a), "v"(b)); return r; }
DEVI void setm0q(unsigned v) { asm volatile("s_mov_b32 m0, %0" :: "s"(v) : "memory"); }
template <int OFF> DEVI void st16o(v4u v, rsrc_t r, unsigned vo, unsigned so) {
    asm volatile("buffer_store_dwordx4 %0, %1, %2, %3 offen offset:%4 sc1" :: "v"(v), "v"(vo), "s"(r), "s"(so), "n"(OFF) : "memory"); }
DEVI unsigned vlsa(unsigned a, int sh, unsigned c) { unsigned r; asm volatile("v_lshl_add_u32 %0, %1, %2, %3" : "=v"(r) : "v"(a), "i"(sh), "v"(c)); return r; }
DEVI unsigned vlss(unsigned a, int sh, unsigned c) { unsigned r; asm volatile("v_lshl_add_u32 %0, %1, %2, %3" : "=v"(r) : "v"(a), "i"(sh), "s"(c)); return r; }
template <int OFF> DEVI void sts4h(unsigned p, unsigned u0, unsigned u1) {
    asm volatile("ds_write_b16 %0, %1 offset:%3\n\tds_write_b16_d16_hi %0, %1 offset:%3+16\n\tds_write_b16 %0, %2 offset:%3+8\n\tds_write_b16_d16_hi %0, %2 offset:%3+24" :: "v"(p), "v"(u0), "v"(u1), "n"(OFF) : "memory");
}
DEVI void zero4v(f32x4& d) { f32x2 a, b; asm volatile("v_mov_b64 %0, 0\n\tv_mov_b64 %1, 0" : "=v"(a), "=v"(b)); d = __builtin_shufflevector(a, b, 0, 1, 2, 3); }
DEVI float sel0(float a, u64 m) { float r; asm("v_cndmask_b32 %0, %1, 0, %2" : "=v"(r) : "v"(a), "s"(m)); return r; }
DEVI float vmul(float a, float b) { float r; asm("v_mul_f32 %0, %1, %2" : "=v"(r) : "v"(a), "v"(b)); return r; }
DEVI void swap16(float& a, float& b) { unsigned x = __float_as_uint(a), y = __float_as_uint(b); swap16u(x, y); a = __uint_as_float(x); b = __uint_as_float(y); }
#ifndef PACE
#define PACE 8
#endif
DEVI void pace(int l) { asm volatile("s_cmp_eq_u32 %0, 0\n\ts_cbranch_scc1 1f\n\ts_sleep %1\n1:" :: "s"(l), "n"(PACE) : "scc"); }
DEVI void role_A(const Args& a, int w, int l, int h, int rep) {
    asm volatile("; MARK role_A"); __builtin_amdgcn_s_waitcnt(0);
    using namespace la;
    const int lane0 = lane_id();
    const rsrc_t rs = mkrs(a.ws, WS_NEED);
    const unsigned lane16 = lane0 * 16, wl = L_W + w * (11 * 2048) + lane16;
    v8u Wc[3];
    unsigned nb;
    {
        const int lane = lane0;
        const unsigned ta = (lane >> 4) * 2048 + w * 256 + (lane & 15) * 16;
        const rsrc_t r1 = mkrs(a.Wq, WSZ(8u << 20)), r2 = mkrs(a.Wk, WSZ(8u << 20));
        const unsigned v1 = (8 * w + ((lane >> 2) & 3)) * 8192 + h * 256 + (lane >> 4) * 64 + (lane & 3) * 16;
        const unsigned lw = opqs(w * 2048);
        load_blocks<32, 4, 4, true>([&](auto B, auto S) { pace(l); sfor<0, 2>([&](auto I) { dma_blk(lw + S.v * 16384 + I.v * 8192, B.v < 16 ? r1 : r2, v1, opqs((2 * (B.v & 15) + I.v) * 32 * 8192), 2048); }); },
                                    [&](auto B, auto S) { sfor<0, 4>([&](auto T) { constexpr int o = S.v * 16384 + (T.v >> 1) * 8192 + (T.v & 1) * 8;
                                                                                   put_frag<WMA, (B.v >> 4) * 2 + (T.v & 1), 2 * (B.v & 15) + (T.v >> 1), o, o + 1024>(ta, wl, Wc); });
                                                          if constexpr (B.v == 28) nb = side(rs, l); });
        TSW(127, 2);
    }
    const unsigned spm = na(nb, N_PM + h * 512), spmm = na(nb, N_PMM + h * 128), smem = na(nb, N_MEM + rep * 16384 + w * 4096);
    const int lane = lane_id(), c = lane & 15, g = lane >> 4;
    const unsigned own = (w >> 1) * 32768 + (w & 1) * 4096, wlo = (unsigned)(uintptr_t)a.ws, whi = (unsigned)((uintptr_t)a.ws >> 32), hi = -(w >> 1);
    const v8u pv{lane16, wl, (lane & 2) ? 0xEEEEu : 0x4444u, (unsigned)(L_QM + w * 1024 + (c >> 2) * 128 + g * 32 + (c & 3) * 4), (unsigned)(L_QM + g * 128 + (c & 7) * 16),
                 w == 2 ? ((g >> 1) * 16 + crow(c & 7, c >> 3)) * 16 + (g & 1) * 8 : w < 2 ? lane * 8 : lane < NC ? DV(lane * 512) : 1u << 30,
                 w < 2 ? c * 16 + g * 4 : c < 8 ? crow(c, g & 1) * 8 + (g >> 1) * 4 : 1u << 30, 0u};
    const v16u s0{wlo, whi, RSZ, 0x20000u, wlo, whi, 0u, 0x20000u, (unsigned)tri(0) | hi, (unsigned)(tri(0) >> 32) | hi, (unsigned)tri(1) | hi, (unsigned)(tri(1) >> 32) | hi,
                  (unsigned)tri(2) | hi, (unsigned)(tri(2) >> 32) | hi, (unsigned)tri(3) | hi, (unsigned)(tri(3) >> 32) | hi};
    const v16u s1{0xFF00FF00u, 0xFF00FF00u, 0u, ~0u, hi, hi, 0u, O_XN + l * XB + own, own, smem, w == 2 ? spm : OOB, nb + DV(N_PTX + h * 2048), w == 1 ? 0u : OOB, w == 0 ? 0u : OOB,
                  w < 2 ? w * 256 : w == 2 ? spmm - 1536 : OOB, ~hi};
    const v16u s2{w < 2 ? w * 1024 : w == 2 ? spm : spmm, w < 2 ? 4u : 3u, w < 2 ? 0u : w == 2 ? 0x200u : 0x380u, w ? 0u : __float_as_uint(-3e38f), (unsigned)(L_Q + w * 1024), (unsigned)(L_Q + (w & 1) * 4096),
                  __float_as_uint(SC_L2E), __float_as_uint(-1e30f), na(nb, N_FX), w == 3 ? na(nb, N_PA + ((h >> 3) * 32 + 8 + (h & 7)) * 4) : OOB, (unsigned)(L_KM + (w >> 1) * 1024 + (w & 1) * 256),
                  0u, 0u, (unsigned)(L_SP + w * 256), O_PROF + (blockIdx.x * 4 + w) * 8192, na(nb, N_WM + h * 16384 + w * 4096)};
    asm volatile(A_LOOP :: "{v[0:7]}"(Wc[0]), "{v[8:15]}"(Wc[1]), "{v[16:23]}"(Wc[2]), "{v[24:31]}"(pv), "{s[8:23]}"(s0), "{s[24:39]}"(s1), "{s[40:55]}"(s2),
                 "{s58}"(na(nb, N_U + 8 * w * 65536 + h * 2048)) : "memory", "s99", "vcc");
}
constexpr int bkind(int n) { return (B_LDS >> n & 1) ? 1 : (B_VAR >> n & 1) ? 2 : 0; }
struct WMV { static constexpr int NT = 2, NP = 16, RB = 0, LB = 0, CBI = 0; static constexpr int kind(int j, int t) { return bkind(16 * j + t); } };
struct WMO { static constexpr int NT = 16, NP = 2, LB = __builtin_popcount((unsigned)B_LDS), CBI = __builtin_popcount((unsigned)B_VAR), RB = 8 * (32 - LB - CBI);
             static constexpr int kind(int j, int t) { return bkind(32 + 2 * j + t); } };
#ifndef B_LV
#define B_LV 24576
#endif
namespace lb { constexpr int L_V = B_LV, L_VM = B_LV + 4096, L_W = 65536; }
DEVI void role_B(const Args& a, int w, int l, int h, int rep) {
    asm volatile("; MARK role_B"); __builtin_amdgcn_s_waitcnt(0);
    using namespace lb;
    const int lane0 = lane_id();
    const rsrc_t rs = mkrs(a.ws, WS_NEED);
    const unsigned lane16 = lane0 * 16, wl = L_W + w * (12 * 2048) + lane16;
    v8u Wc[2];
    unsigned nb, fx;
    {
        const int lane = lane0;
        const unsigned ta = (lane >> 4) * 2048 + w * 256 + (lane & 15) * 16, tb = (lane >> 5) * 8192 + w * 2048 + (lane & 16) * 64 + (lane & 15) * 16;
        const rsrc_t r1 = mkrs(a.Wv, WSZ(8u << 20)), r2 = mkrs(a.Wo, WSZ(8u << 20));
        const unsigned v1 = (8 * w + ((lane >> 2) & 3)) * 8192 + h * 256 + (lane >> 4) * 64 + (lane & 3) * 16, v2 = (h * 128 + ((lane >> 2) & 7)) * 2048 + w * 512 + (lane >> 5) * 64 + (lane & 3) * 16;
        const unsigned lw = opqs(w * 2048);
        load_blocks<32, 4, 4, true>([&](auto B, auto S) {
            constexpr int b = B.v & 15;
            pace(l);
            sfor<0, 2>([&](auto I) { if constexpr (B.v < 16) dma_blk(lw + S.v * 16384 + I.v * 8192, r1, v1, opqs((2 * b + I.v) * 32 * 8192), 2048);
                                     else dma_blk(lw + S.v * 16384 + I.v * 8192, r2, v2, opqs(((b & 3) * 32 + I.v * 16) * 2048 + (b >> 2) * 128), 1024); });
        }, [&](auto B, auto S) {
            constexpr int b = B.v & 15;
            sfor<0, 4>([&](auto T) { constexpr int o = S.v * 16384 + (T.v >> 1) * (B.v < 16 ? 8192 : 512) + (T.v & 1) * 8;
                                     if constexpr (B.v < 16) put_frag<WMV, T.v & 1, 2 * b + (T.v >> 1), o, o + 1024>(ta, wl, Wc); else put_frag<WMO, (b & 12) + T.v, b & 3, o, o + 256>(tb, wl, Wc); });
            if constexpr (B.v == 28) { nb = side(rs, l); fx = ld4(rs, (lane & 7) * 4, na(nb, N_FX)); }
        });
        TSW(127, 2);
    }
    const int lane = lane_id(), c = lane & 15, g = lane >> 4;
    const unsigned own = (w >> 1) * 32768 + (w & 1) * 4096, wlo = (unsigned)(uintptr_t)a.ws, whi = (unsigned)((uintptr_t)a.ws >> 32);
    const unsigned prow = L_VM + w * 512 + ((c >> 2) & 1) * 256 + g * 64 + (c >> 3) * 32 + (c & 1) * 8 + ((c >> 1) & 1) * 4;
    const v4u pv{lane16, wl, ((lane & 2) ? 0xEEEEu : 0x4444u) | prow << 16, (unsigned)(L_VM + (g >> 1) * 512 + (lane & 3) * 64 + (g & 1) * 32 + ((lane >> 2) & 3) * 8)};
    const v16u s0{wlo, whi, RSZ, 0x20000u, wlo, whi, 0u, 0x20000u, OOB, OOB, OOB, OOB, ~1u, O_XN + l * XB + own, own, na(nb, N_MEM + rep * 16384 + w * 4096)};
    const v8u s1{na(nb, N_WM + h * 16384 + w * 4096), na(nb, N_U + h * 65536 + w * 16384), na(nb, N_FX), w == 3 ? na(nb, N_PA + ((h >> 3) * 32 + (h & 7)) * 4) : OOB,
                 (unsigned)(L_V + w * 1024), OOB, 0u, O_PROF + (blockIdx.x * 4 + w) * 8192 + 64};
    asm volatile(B_LOOP :: "{v[0:7]}"(Wc[0]), "{v[8:15]}"(Wc[1]), "{v[16:19]}"(pv), "{v20}"(fx), "{s[8:23]}"(s0), "{s[24:31]}"(s1), "{s32}"(1u) : "memory", "s99", "vcc");
}
template <class NB> DEVI void put_tile(v4u (&u)[8], rsrc_t rs, int tile, int w, int lane, NB&& nbf) {
    const unsigned vo = tile * 32768 + w * 8192 + lane * 16, red = (lane & 15) * 16;
    float s = 0.f;
#pragma unroll
    for (int f = 0; f < 8; ++f) {
#pragma unroll
        for (int i = 0; i < 4; ++i) s += bflo(u[f][i]) + bfhi(u[f][i]);
    }
    s = red32(red16(s));
    BAR;
    if (lane < 16) *LDSP(float, red + w * 4) = s;
    WL0; BAR;
    const f32x4 sa = __builtin_bit_cast(f32x4, ldsq(red, 0));
    const float mean = ((sa[0] + sa[1]) + (sa[2] + sa[3])) * (1.0f / 1024.0f);
    float q = 0.f;
#pragma unroll
    for (int f = 0; f < 8; ++f) {
        asm volatile("" : "+v"(u[f]));
#pragma unroll
        for (int i = 0; i < 4; ++i) { const float d0 = bflo(u[f][i]) - mean, d1 = bfhi(u[f][i]) - mean; q = fmaf(d0, d0, fmaf(d1, d1, q)); }
    }
    q = red32(red16(q));
    if (lane < 16) *LDSP(float, red + 256 + w * 4) = q;
    WL0; BAR;
    const f32x4 qa = __builtin_bit_cast(f32x4, ldsq(red, 256));
    const float mb = rbf(mean), inv = 1.0f / rbf(sqrtf(rbf(rbf(((qa[0] + qa[1]) + (qa[2] + qa[3])) * (1.0f / 1024.0f)) + 1e-5f)));
#pragma unroll
    for (int f = 0; f < 8; ++f) {
        v4u o;
        asm volatile("" : "+v"(u[f]));
#pragma unroll
        for (int i = 0; i < 4; ++i) { const unsigned d = pack2(bflo(u[f][i]) - mb, bfhi(u[f][i]) - mb); o[i] = pack2(bflo(d) * inv, bfhi(d) * inv); }
        st16(o, rs, O_XN + vo + f * 1024);
    }
    const unsigned nb = nbf();
    WV0;
    if (lane == 0) st4(0u, rs, na(nb, N_FX) + tile * 16 + w * 4, 0);
}
#ifndef SXL
#define SXL 1
#endif
#ifndef HLD
#define HLD 170
#endif
#ifndef SXH
#define SXH 30
#endif
#ifndef UIN
#define UIN 8
#endif
namespace lr { constexpr int PT1 = NV + 48, PT0 = NV + 80, PTM = NV + 96, VPM = NV, VMM = NV + 16, VW = NV + 28, VX = NV + 44, L_M = 1024, MD = 288, MW = 1536, L_T = 8192, VE = NV + 112; }
template <int R, int N, int OFF> DEVI void lda(rsrc_t rs, unsigned vo, unsigned so) {
    if constexpr (N == 4) asm volatile("buffer_load_dwordx4 a[%3:%4], %0, %1, %2 offen offset:%5 sc1" :: "v"(vo), "s"(rs), "s"(so), "n"(R), "n"(R + 3), "n"(OFF) : "memory");
    else asm volatile("buffer_load_dwordx2 a[%3:%4], %0, %1, %2 offen offset:%5 sc1" :: "v"(vo), "s"(rs), "s"(so), "n"(R), "n"(R + 1), "n"(OFF) : "memory");
}
template <int R, int N, int OFF> DEVI void ldv(rsrc_t rs, unsigned vo, unsigned so) {
    if constexpr (N == 4) asm volatile("buffer_load_dwordx4 v[%3:%4], %0, %1, %2 offen offset:%5 sc1" :: "v"(vo), "s"(rs), "s"(so), "n"(R), "n"(R + 3), "n"(OFF) : "memory");
    else asm volatile("buffer_load_dwordx2 v[%3:%4], %0, %1, %2 offen offset:%5 sc1" :: "v"(vo), "s"(rs), "s"(so), "n"(R), "n"(R + 1), "n"(OFF) : "memory");
}
template <int R> DEVI unsigned rdv() { unsigned t; asm volatile("v_mov_b32 %0, v[%1]" : "=v"(t) : "n"(R)); return t; }
template <int RA, int RB, bool Z, bool AV> DEVI void mfh(f32x4& d) {
    if constexpr (AV) { if constexpr (Z) asm volatile(M16 "%0, v[%1:%2], v[%3:%4], 0" : "=v"(d) : "n"(RA), "n"(RA + 3), "n"(RB), "n"(RB + 3));
                        else asm volatile(M16 "%0, v[%1:%2], v[%3:%4], %0" : "+v"(d) : "n"(RA), "n"(RA + 3), "n"(RB), "n"(RB + 3)); }
    else { if constexpr (Z) asm volatile(M16 "%0, a[%1:%2], v[%3:%4], 0" : "=v"(d) : "n"(RA), "n"(RA + 3), "n"(RB), "n"(RB + 3));
           else asm volatile(M16 "%0, a[%1:%2], v[%3:%4], %0" : "+v"(d) : "n"(RA), "n"(RA + 3), "n"(RB), "n"(RB + 3)); }
}
template <int RA, int RB, bool Z> DEVI void mfh16(f32x4& d) {
    if constexpr (Z) asm volatile("v_mfma_f32_16x16x16_bf16 %0, a[%1:%2], v[%3:%4], 0" : "=v"(d) : "n"(RA), "n"(RA + 1), "n"(RB), "n"(RB + 1));
    else asm volatile("v_mfma_f32_16x16x16_bf16 %0, a[%1:%2], v[%3:%4], %0" : "+v"(d) : "n"(RA), "n"(RA + 1), "n"(RB), "n"(RB + 1));
}
template <int R> DEVI u64 ltv(int need) { u64 m; asm volatile("v_cmp_lt_i32 %0, v[%2], %1" : "=s"(m) : "v"(need), "n"(R)); return m; }
template <int R> DEVI void ldv4(rsrc_t rs, unsigned vo, unsigned so) { asm volatile("buffer_load_dwordx4 v[%3:%4], %0, %1, %2 offen sc1" :: "v"(vo), "s"(rs), "s"(so), "n"(R), "n"(R + 3) : "memory"); }
#define SM_(E, A, B) "v_smfmac_f32_16x16x64_bf16 v[%[e]+" #E ":%[e]+" #E "+3], " A ", " B ", %[si]\n\t"
#define TG_(M, R) "v_bitop3_b32 v[%[e]+" #M "], v[%[e]+" #M "], v[" R "], %[tg] bitop3:0xf6\n\t"
#define PP_(P, N) "s_waitcnt vmcnt(" #N ")\n\t" SM_(0, "v[%[p]+4*" #P ":%[p]+4*" #P "+3]", "a[%[u]+16*" #P ":%[u]+16*" #P "+7]") TG_(8, "%[p]+4*" #P)
#define PQ_(P) SM_(4, "v[%[p]+4*" #P ":%[p]+4*" #P "+3]", "a[%[u]+16*" #P "+8:%[u]+16*" #P "+15]") TG_(9, "%[p]+4*" #P "+2")
#define ZE_ "v_mov_b64 v[%[e]:%[e]+1], 0\n\tv_mov_b64 v[%[e]+2:%[e]+3], 0\n\tv_mov_b64 v[%[e]+4:%[e]+5], 0\n\tv_mov_b64 v[%[e]+6:%[e]+7], 0\n\tv_mov_b64 v[%[e]+8:%[e]+9], 0\n\t"
template <int U0> DEVI unsigned chain(rsrc_t rs, int tgo, unsigned sp, unsigned sm, unsigned sw0, unsigned sw1, unsigned tg, unsigned spt, unsigned lane16, unsigned lane8, unsigned vmm, unsigned vw, int sidx,
                                      unsigned lred, unsigned lrr, unsigned vpub, v2u sxv, unsigned sxo) {
    using namespace lr;
    unsigned n, d;
    asm volatile(ZE_ "s_mov_b32 %[n], 0\n\ts_memrealtime s[98:99]\n\ts_add_u32 %[d], %[pt], 0x800\n\ts_waitcnt lgkmcnt(0)\n\ts_sub_i32 s99, %[t], s98\n\ts_lshl_b32 s99, s99, 8\n\ts_sub_u32 s99, s99, 1\n\t"
        "s_cmp_lt_u32 s99, 0x10000\n\ts_cbranch_scc1 2f\n\ts_branch 3f\n"
        "1:\n\ts_add_u32 %[n], %[n], 1\n\ts_cmp_lt_u32 %[n], %[cap]\n\ts_cbranch_scc1 4f\n\ts_endpgm\n4:\n\t" ZE_ "v_bfrev_b32 %[so], 1\n\ts_branch 3f\n"
        "2:\n\ts_memrealtime s[98:99]\n\ts_waitcnt lgkmcnt(0)\n\ts_sub_i32 s99, %[t], s98\n\ts_lshl_b32 s99, s99, 8\n\ts_cmp_gt_i32 s99, 0\n\ts_cbranch_scc1 2b\n"
        "3:\n\t"
        "buffer_load_dwordx4 v[%[p]:%[p]+3], %[l16], %[rs], %[sp] offen sc1\n\t"
        "buffer_load_dwordx4 v[%[p]+4:%[p]+7], %[l16], %[rs], %[sp] offen offset:1024 sc1\n\t"
        "buffer_load_dwordx4 v[%[p]+8:%[p]+11], %[l16], %[rs], %[sp] offen offset:2048 sc1\n\t"
        "buffer_load_dwordx4 v[%[p]+12:%[p]+15], %[l16], %[rs], %[sp] offen offset:3072 sc1\n\t"
        "buffer_load_dwordx2 v[%[p]+16:%[p]+17], %[vm], %[rs], %[sm] offen sc1\n\t"
        "buffer_load_dwordx2 v[%[p]+18:%[p]+19], %[vm], %[rs], %[sm] offen offset:128 sc1\n\t"
        "buffer_load_dwordx4 v[%[w]:%[w]+3], %[vw], %[rs], %[sw0] offen sc1\n\t"
        "buffer_load_dwordx4 v[%[w]+4:%[w]+7], %[vw], %[rs], %[sw1] offen sc1\n\t"
        "buffer_load_dwordx4 v[%[w]+8:%[w]+11], %[vw], %[rs], %[sw0] offen offset:256 sc1\n\t"
        "buffer_load_dwordx4 v[%[w]+12:%[w]+15], %[vw], %[rs], %[sw1] offen offset:256 sc1\n\t"
        PP_(0, 9) "buffer_store_dwordx2 %[sx], %[so], %[rs], 0 offen sc1\n\t" PQ_(0) PP_(1, 9) PQ_(1) PP_(2, 8) PQ_(2) PP_(3, 7) PQ_(3)
        "s_waitcnt vmcnt(5)\n\t" TG_(8, "%[p]+16") TG_(9, "%[p]+17") TG_(8, "%[p]+18") TG_(9, "%[p]+19")
        "s_waitcnt vmcnt(3)\n\t" SM_(0, "v[%[p]+16:%[p]+19]", "v[%[w]:%[w]+7]") TG_(8, "%[w]") TG_(9, "%[w]+4")
        "s_waitcnt vmcnt(1)\n\t" SM_(4, "v[%[p]+16:%[p]+19]", "v[%[w]+8:%[w]+15]") TG_(8, "%[w]+8") TG_(9, "%[w]+12")
        "v_bitop3_b32 v[%[e]+8], v[%[e]+8], v[%[e]+9], 1 bitop3:0xa8\n\tv_cmp_ne_u32 vcc, 0, v[%[e]+8]\n\ts_nop 0\n\t"
        "v_add_f32 v[%[e]], v[%[e]], v[%[e]+2]\n\tv_add_f32 v[%[e]+1], v[%[e]+1], v[%[e]+3]\n\tds_write2_b32 %[lw], v[%[e]], v[%[e]+1] offset1:2\n\ts_nop 0\n\t"
        "v_add_f32 v[%[e]+4], v[%[e]+4], v[%[e]+6]\n\tv_add_f32 v[%[e]+5], v[%[e]+5], v[%[e]+7]\n\tds_write2_b32 %[lw], v[%[e]+4], v[%[e]+5] offset0:%[md] offset1:%[md]+2\n\t"
        "s_cbranch_vccnz 1b\n\t"
        "buffer_load_dwordx4 v[%[t1]:%[t1]+3], %[l16], %[rs], %[pt] offen sc1\n\tbuffer_load_dwordx2 v[%[t0]:%[t0]+1], %[l8], %[rs], %[pt] offen offset:1024 sc1\n\t"
        "buffer_load_dwordx4 v[%[t1]+4:%[t1]+7], %[l16], %[rs], %[d] offen sc1\n\tbuffer_load_dwordx2 v[%[t0]+2:%[t0]+3], %[l8], %[rs], %[d] offen offset:1024 sc1\n\t"
        "s_waitcnt lgkmcnt(0)\n\ts_barrier\n\t"
        "ds_read2st64_b64 v[%[e]:%[e]+3], %[lr] offset1:3\n\tds_read2st64_b64 v[%[e]+4:%[e]+7], %[lr] offset0:6 offset1:9\n\ts_waitcnt lgkmcnt(1)\n\t"
        "v_add_f32 v[%[e]], v[%[e]], v[%[e]+2]\n\tv_add_f32 v[%[e]+1], v[%[e]+1], v[%[e]+3]\n\ts_waitcnt lgkmcnt(0)\n\t"
        "v_add_f32 v[%[e]+4], v[%[e]+4], v[%[e]+6]\n\tv_add_f32 v[%[e]+5], v[%[e]+5], v[%[e]+7]\n\tv_add_f32 v[%[e]], v[%[e]], v[%[e]+4]\n\tv_add_f32 v[%[e]+1], v[%[e]+1], v[%[e]+5]\n\t"
        "v_cvt_pk_bf16_f32 v[%[e]+2], v[%[e]], v[%[e]+1]\n\tv_min_u32 v[%[e]], -2, v[%[e]+2]\n\tv_min_u32 v[%[e]+1], -2, v[%[e]+2]\n\ts_nop 1\n\t"
        "v_permlane16_swap_b32 v[%[e]], v[%[e]+1]\n\tv_mov_b64 v[%[e]+2:%[e]+3], v[%[e]:%[e]+1]\n\ts_nop 1\n\t"
        "v_permlane32_swap_b32 v[%[e]], v[%[e]+2]\n\tv_permlane32_swap_b32 v[%[e]+1], v[%[e]+3]\n\t"
        "buffer_store_dwordx4 v[%[e]:%[e]+3], %[vp], %[rs], 0 offen sc1"
        : [n] "=&s"(n), [d] "=&s"(d), [so] "+v"(sxo)
        : [rs] "s"(rs), [t] "s"(tgo), [sp] "s"(sp), [sm] "s"(sm), [sw0] "s"(sw0), [sw1] "s"(sw1), [tg] "s"(tg), [pt] "s"(spt), [l16] "v"(lane16), [l8] "v"(lane8), [vm] "v"(vmm), [vw] "v"(vw), [si] "v"(sidx),
          [lw] "v"(lred), [lr] "v"(lrr), [vp] "v"(vpub), [sx] "v"(sxv), [e] "n"(VE), [p] "n"(VPM), [w] "n"(VW), [u] "n"(U0), [t1] "n"(PT1), [t0] "n"(PT0), [md] "n"(MD / 4), [cap] "n"(SPIN_CAP)
        : "memory", "scc", "vcc", "s98", "s99");
    return n;
}
template <int R> DEVI void tagd(unsigned& m, unsigned t) { asm volatile("v_bitop3_b32 %0, %0, v[%2], %1 bitop3:0xf6" : "+v"(m) : "s"(t), "n"(R)); }
#ifndef DLY_R
#define DLY_R 1200
#endif
#ifndef DLY_X
#define DLY_X 1300
#endif
DEVI void role_R(const Args& a, int w, int l, int T2, int t0) {
    asm volatile("; MARK role_R"); __builtin_amdgcn_s_waitcnt(0);
    using namespace lr;
    const int lane = lane_id(), c = lane & 15, g = lane >> 4;
    const rsrc_t rs = mkrs(a.ws, WS_NEED);
    const unsigned lane16 = lane * 16;
#pragma nounroll
    for (int k = 0; k < 4 * l; ++k) {
        const int tile = k * 32 + T2;
        if (tile >= LN_NOW) hold(t0, DLY_X);
        v4u u[8];
#pragma unroll
        for (int f = 0; f < 8; ++f) u[f] = *(const v4u*)(a.x + (size_t)(tile * 16 + c) * 1024 + (w * 8 + f) * 32 + g * 8);
        if (tile < 2 * NSEG) put_tile(u, rs, tile, w, lane, [&]() { hold(t0, DLY_V); return side(rs, 0); });
        TSW(127, 1 + k);
    }
    auto arm = [&](unsigned n2, int s) {
        if (s < 8)
#pragma unroll
            for (int k = 0; k < 4; ++k) st16(v4u{SENT, SENT, SENT, SENT}, rs, na(n2, N_WM + s * 524288 + T2 * 16384 + w * 4096) + k * 1024 + lane16);
        st16(v4u{SENT, SENT, SENT, SENT}, rs, w < 2 ? na(n2, N_PTX + s * 65536 + T2 * 2048) + w * 1024 + lane16 : lane >= (w == 2 ? 32 : 8) || s >= 8 ? OOB :
                                              na(n2, w == 2 ? N_PM + s * 16384 + T2 * 512 : N_PMM + s * 8192 + T2 * 128) + lane16);
    };
    hold(t0, DLY_R);
    const unsigned nb = side(rs, l), vmem = c < 8 ? na(nb, N_MEM + T2 * 512) + g * 65536 + w * 128 + c * 16 : OOB;
    auto put_mem = [&](v4u v, int s) { st16(v, rs, vmem + rslot(s) * (NREP * 32768)); };
    {
        const unsigned* m0 = (const unsigned*)(a.mem0 + ((w & 1) * 4 + ((lane >> 2) & 1) * 2 + (lane & 1)) * 1024 + T2 * 32 + (w >> 1) * 16 + ((lane >> 1) & 1) * 2);
        const v4u mv{nosent(m0[0]), m0[2], m0[4], m0[6]};
        put_mem(v4u{SENT, SENT, SENT, SENT}, 1); put_mem(v4u{SENT, SENT, SENT, SENT}, 2);
#pragma nounroll
        for (int k = 0; k < 16; ++k) arm(nb, k);
        WV0; BAR;
        put_mem(mv, 0);
    }
    const int sidx = opq((lane & 2) ? 0xEEEE : 0x4444);
    const unsigned vmm = (8 * w + 2 * g) * 128 + c * 8, vw = (8 * w + g) * 32768 + DV(T2 * 512) + c * 16, vptm = g * 2048 + 1536 + c * 16;
    const unsigned sfl = na(nb, N_PA + (unsigned)((T2 & (NC - 1)) * 4 + w) * 128), su0 = na(nb, N_U + 8 * w * 65536 + T2 * 2048), vfl = sfl + c * 4;
    const unsigned lred = L_M + w * MW + ((c >> 2) & 1) * 2 * MD + (c >> 3) * 128 + g * 32 + ((c >> 1) & 1) * 16 + (c & 1) * 4, lrr = L_M + g * MD + w * 64 + (c & 7) * 8;
    auto ld_u = [&](auto B, int s) {
        const unsigned so = su0 + rslot(s) * (4u << 20);
        sfor<0, 16>([&](auto I) { constexpr int j = I.v >> 1, e = I.v & 1; lda<64 * B.v + 16 * (j >> 1) + 8 * e + 4 * (j & 1), 4, e * 1024>(rs, lane16, so + j * 131072); });
    };
    wait_prog(rs, sfl + (lane & 7) * 4, 0, 0);
    ld_u(IC<0>{}, 0);
    const rsrc_t rxi = l ? rs : mkrs(a.x, XB);
    const unsigned vxi = l ? lane16 : c * 2048 + g * 16, sxi = (w & 1) * 32768 + (l ? O_XIN + XB + T2 * 1024 : T2 * 64);
    v2u sxv{0u, 0u};
    unsigned sxo = OOB;
    int tpub = t0;
    auto step = [&](auto B, int s) {
        constexpr int b = B.v;
        TSD;
        TSR(0); TSK(15);
        if (s == 8) { TSC(126, 0); TSW(126, 1); }
        if (s == 56) { TSC(126, 2); TSW(126, 3); }
        const unsigned spt = na(nb, N_PTX + (unsigned)((s & 15) * 32 + 8 * w) * 2048);
        auto ptx = [&](auto N) {
            constexpr int n = N.v & 31, j = n >> 1;
            if constexpr (n == 20) ldv4<VX>(rxi, vxi, sxi + s * 65536);
            else if constexpr (n >= 16) ldv<PTM + 4 * (n - 16), 4, (n & 1) * 256>(rs, vptm, spt + (j - 8) * 16384);
            else if constexpr (n & 1) ldv<PT0 + 2 * j, 2, 1024>(rs, lane * 8, spt + DV(j * 2048));
            else ldv<PT1 + 4 * j, 4, 0>(rs, lane16, spt + DV(j * 2048));
        };
        {
            const unsigned sw = opqs(na(nb, N_WM + rslot(s) * 524288)), sw1 = opqs(sw + 131072), sp = opqs(na(nb, N_PM + w * 4096) + rslot(s) * 32768), sm = opqs(na(nb, N_PMM) + rslot(s) * 16384);
            const unsigned vpub = opq(vmem + rslot(s + 1) * (NREP * 32768)), tg = opqs((s >> 3) & 1), spq = opqs(spt);
            SB;
            if ((int)__builtin_amdgcn_s_memrealtime() - tpub < HLD) { wvm<16 - UIN>(); st8(sxv, rs, sxo, 0); sxo = OOB; hold(tpub, HLD); }
            const int mx = wait_hint(rs, sfl, vfl, s + 1);
            if (left(mx) > SXH * 256) { wvm<16 - UIN>(); st8(sxv, rs, sxo, 0); sxo = OOB; }
            TSR(5);
            const unsigned spins = chain<64 * b>(rs, mx, sp, sm, sw, sw1, tg, spq, lane16, lane * 8, vmm, vw, sidx, lred, lrr, vpub, sxv, sxo);
#if defined(TS) && TS != 4
            tsr[8] = __builtin_amdgcn_readfirstlane(spins) + 1;
#endif
            tpub = (int)__builtin_amdgcn_s_memrealtime();
            TSR(2);
        }
        sfor<4, 21>(ptx);
        BAR;
        f32x4 t[2][2], u[2];
        const unsigned su = s + 1 < NSEG ? su0 + rslot(s + 1) * (4u << 20) : OOB;
        put_mem(v4u{SENT, SENT, SENT, SENT}, s + 3);
#pragma nounroll
        for (int n = 0;; ++n) {
            if (n) { sfor<32, 52>(ptx); WV0; }
            auto lu = [&](auto N) { constexpr int j = N.v >> 1, e = N.v & 1; lda<16 * (j >> 1) + 8 * e + 4 * (j & 1) + 64 * (1 - b), 4, e * 1024>(rs, lane16, su + j * 131072); };
            TSR(6);
            unsigned mt = opq(0);
            const unsigned tp = (s >> 4) & 1;
            sfor<0, 16>([&](auto N) {
                constexpr int j = N.v >> 1, e = N.v & 1, R = 64 * b + 16 * (j >> 1) + 8 * e + 4 * (j & 1), nu = N.v * UIN / 16;
                if constexpr (e == 0) wvm<21 - 2 * j + (j < 2) + nu - 1>();
                if constexpr ((N.v + 1) * UIN / 16 > nu) lu(IC<nu>{});
                mfh<R, PT1 + 4 * j, j == 0, false>(t[1][e]); tagd<e ? PT0 + 2 * j : PT1 + 4 * j>(mt, tp); mfh16<R, PT0 + 2 * j, j == 0>(u[e]);
            });
            wvm<1 + UIN>();
            sfor<0, 8>([&](auto N) { constexpr int q = N.v >> 2, rt = (N.v >> 1) & 1, e = N.v & 1; mfh<VW + 8 * e + 4 * q, PTM + 4 * (2 * q + rt), N.v < 2, true>(t[rt][e]);
                                     tagd<PTM + 2 * N.v>(mt, tp); tagd<PTM + 2 * N.v + 1>(mt, tp); });
            nop<7>(); fence(t[0][0], t[0][1], t[1][0], t[1][1], u[0], u[1]);
#pragma unroll
            for (int e = 0; e < 2; ++e)
#pragma unroll
                for (int i = 0; i < 4; ++i) t[0][e][i] = fadd(t[0][e][i], u[e][i]);
            if (__builtin_amdgcn_uicmp(mt & 1, 0u, 33) == 0 || n == 2) break;
            TSV(s, 7, 1);
            if (n == 1) wait_prog(rs, sfl + 32 + (lane & 7) * 4, 0, (s + 2) << 24);
        }
        TSR(3);
#pragma unroll
        for (int e = 0; e < 2; ++e) { stsq(lane16 + w * 4096, L_T + e * 1024, __builtin_bit_cast(v4u, t[0][e])); stsq(lane16 + w * 4096, L_T + 2048 + e * 1024, __builtin_bit_cast(v4u, t[1][e])); }
        WL0; BAR;
        if (w < 2) {
            float o[8], s1 = 0.f, s2 = 0.f;
#pragma unroll
            for (int e = 0; e < 2; ++e) {
                f32x4 q[4];
#pragma unroll
                for (int k = 0; k < 4; ++k) q[k] = __builtin_bit_cast(f32x4, ldsq(lane16 + w * 2048, L_T + k * 4096 + e * 1024));
#pragma unroll
                for (int i = 0; i < 4; ++i) o[4 * e + i] = fadd(fadd(q[0][i], q[1][i]), fadd(q[2][i], q[3][i]));
            }
            v4u x1;
            sfor<0, 4>([&](auto I) {
                constexpr int i = I.v;
                const unsigned x = rdv<VX + i>(), at = pack2(o[2 * i], o[2 * i + 1]);
                x1[i] = pack2(bflo(x) + bflo(at), bfhi(x) + bfhi(at));
                const float lo = bflo(x1[i]), hi = bfhi(x1[i]);
                s1 += lo + hi; s2 = fmaf(lo, lo, fmaf(hi, hi, s2));
            });
            st16(x1, rs, O_XM + l * XB + s * 65536 + w * 32768 + T2 * 1024 + lane16);
            sxv = v2u{nosent(__float_as_uint(red32(red16(s1)))), __float_as_uint(red32(red16(s2)))};
            sxo = lane < 16 ? O_SX + (unsigned)((l * 64 + s) * 32 + T2) * 256 + (SXL ? w * 128 + c * 8 : c * 16 + w * 8) : OOB;
        }
        TSR(4);
        {
            sfor<UIN, 16>([&](auto N) { constexpr int j = N.v >> 1, e = N.v & 1; lda<16 * (j >> 1) + 8 * e + 4 * (j & 1) + 64 * (1 - b), 4, e * 1024>(rs, lane16, su + j * 131072); });
        }
#if TS != 4
        TSF(s, 0x817D);
#endif
    };
#pragma nounroll
    for (int s = 0; s < NSEG; s += 2) { step(IC<0>{}, s); step(IC<1>{}, s + 1); }
    wvm<16 - UIN>(); st8(sxv, rs, sxo, 0);
    WV0;
}
#ifndef FSIDE
#define FSIDE 1
#endif
#ifndef PDX
#define PDX 0
#endif
constexpr int NU = 2 * NSEG;
#ifndef PSF
#define PSF 40
#endif
#ifndef PSR
#define PSR 12
#endif
#ifndef RS1
#define RS1 49
#endif
constexpr int RSH = 4 * (64 + RS1);
#ifndef RPA
#define RPA 8
#endif
#ifndef GROT
#define GROT 1
#endif
#ifndef LAGK
#define LAGK 7
#endif
#ifndef LAGP
#define LAGP 0
#endif
DEVI unsigned unit_ls(unsigned u) {
    static_assert(NSEG == 64 && LAGD >= 6, "");
    static_assert(LAGP % 2 == 0 && LAGP < 2 * LAGK + 1, "");
    const unsigned d = u - LAGD + LAGP, p = d / (2 * LAGK + 1), r = d % (2 * LAGK + 1), n0 = LAGD - LAGP / 2 + p * (LAGK + 1) + (r >> 1);
    return u < LAGD || n0 >= 64 ? u : ((r & 1) || r == 2 * LAGK) ? n0 : 64 + u - n0;
}
DEVI unsigned unit_tab(int lane) { return opq(unit_ls(2 * lane) | unit_ls(2 * lane + 1) << 8); }
DEVI unsigned unit_get(unsigned tab, int u) { return ((unsigned)__builtin_amdgcn_readlane(tab, (u >> 1) & 63) >> ((u & 1) * 8)) & 255; }
struct WMG { static constexpr int NT = 3, NP = 16, RB = 0, LB = 0, CBI = 0; static constexpr int kind(int, int) { return 0; } };
struct WMD { static constexpr int NT = 4, NP = 12, RB = 0, LB = 0, CBI = 0; static constexpr int kind(int, int) { return 0; } };
namespace lg { constexpr int L_ST = 155648, L_OK = 159744; }
DEVI void role_GU(const Args& a, int w, int j) {
    asm volatile("; MARK role_GU"); __builtin_amdgcn_s_waitcnt(0);
    using namespace lg;
    const int lane = lane_id(), c = lane & 15, g = lane >> 4;
    const rsrc_t rs = mkrs(a.ws, WS_NEED);
    const unsigned lane16 = lane * 16, hc = 96 * j + 24 * w;
    {
        const unsigned ta = g * 4096 + (lane & 1) * 2048 + w * 256 + ((lane >> 2) & 3) * 64 + ((lane >> 1) & 1) * 8, tb = ta + 65536;
        const rsrc_t r1 = mkrs(a.Wg, WSZ(7168u * 1024)), r2 = mkrs(a.Wu, WSZ(7168u * 1024));
        const unsigned cb = 192 * j + g * 48 + (lane & 3) * 16, v1 = (lane & 3) == 3 || cb >= 7168 ? OOB : (8 * w + ((lane >> 2) & 3)) * 7168 + cb, lw = opqs(w * 4096), z = opqs(0);
        load_blocks<32, 8, 4, true>([&](auto B, auto S) { dma_blk(lw + S.v * 16384, r1, v1, z + B.v * 32 * 7168, 1792); dma_blk(lw + S.v * 16384 + 2048, r2, v1, z + B.v * 32 * 7168, 1792); },
                                    [&](auto B, auto S) { sfor<0, 3>([&](auto T) { constexpr int o = (S.v & 3) * 16384 + T.v * 16; put_frag<WMG, T.v, B.v, o, o + 1024>(S.v < 4 ? ta : tb, 0, nullptr); }); });
        TSW(120 - 65536, 0);
        v4u one{0x3F803F80u, 0x3F803F80u, 0x3F803F80u, 0x3F803F80u};
        asm volatile("s_nop 1" : "+v"(one));
        f32x4 cs[3];
        sfor<0, 96>([&](auto N) { constexpr int t = N.v % 3, ks = N.v / 3; mfA<8 * widx<WMG>(t, ks >> 1) + 4 * (ks & 1), ks == 0>(cs[t], one); });
        nop<7>(); fence(cs[0], cs[1], cs[2]);
        sfor<0, 3>([&](auto T) { mwr4<384 + 4 * T.v>(__builtin_bit_cast(v4u, cs[T.v])); });
        BAR;
    }
    const unsigned nf = side(rs, FSIDE);
    const unsigned wlo = (unsigned)(uintptr_t)a.ws, whi = (unsigned)((uintptr_t)a.ws >> 32);
    const unsigned xr = w * 8192 + (GROT ? (j & 1) * 4096 + (j & 2) * 16384 : 0);
    const v8u pv{lane16, O_SX + (8 * w + g) * 256 + c * 16, O_H + (g & 1) * 114688 + hc * 32 + c * 16 + (g >> 1) * 8, lane == 0 ? na(nf, N_PG) + j * 128 + w * 4 : OOB,
                 (unsigned)(L_ST + ((lane & 1) * 16 + ((lane & 31) >> 1)) * 16), L_ST + lane16 + w * 4, unit_tab(lane), lane16};
    const v16u s0{wlo, whi, RSZ, 0x20000u, wlo, whi, 0u, 0x20000u, OOB, OOB, hc < 3584 ? 0u : OOB, hc + 8 < 3584 ? 0u : OOB, 0u, O_XM + xr, 65536 + xr, 0u};
    const v2u s1{0u, O_PROF + (blockIdx.x * 4 + w) * 8192};
    asm volatile(G_LOOP :: "{v[0:7]}"(pv), "{s[8:23]}"(s0), "{s[24:25]}"(s1) : "memory", "s99", "vcc");
}
DEVI void role_DN(const Args& a, int w, int p, int cg) {
    asm volatile("; MARK role_DN"); __builtin_amdgcn_s_waitcnt(0);
    using namespace lg;
    const int lane = lane_id();
    const rsrc_t rs = mkrs(a.ws, WS_NEED);
    const unsigned lane16 = lane * 16;
    const int F0 = p < 3 ? 23 * p : (p == 3 ? 68 : 89), zb = p < 2 ? -1 : (p == 4 ? 0 : 22);
    {
        const unsigned ta = w * 18432 + (lane >> 4) * 512 + ((lane >> 2) & 3) * 64 + (lane & 3) * 16;
        const rsrc_t r1 = mkrs(a.Wd, WSZ(3584u * 2048));
        const unsigned v1 = (F0 * 32 + (lane >> 2)) * 2048 + cg * 512 + w * 128 + (lane & 3) * 16;
        load_blocks<46, 9, 2>([&](auto B, auto S) { dma_blk(w * 18432 + S.v * 2048, r1, (B.v >> 1) == zb ? OOB : v1, (B.v >> 1) * 65536 + (B.v & 1) * 64, 2048); },
                              [&](auto B, auto S) { constexpr int ks = B.v >> 1, t = 2 * (B.v & 1), o = S.v * 2048;
                                                    put_frag<WMD, t, ks, o, o + 256>(ta, 0, nullptr); put_frag<WMD, t + 1, ks, o + 8, o + 264>(ta, 0, nullptr); });
        TSW(120 - 65536, 0);
        BAR;
    }
    const int g0 = F0 / 3 * 4, gn = ((F0 + 22) / 3 + 1) * 4 - g0;
    const unsigned nf = side(rs, FSIDE);
    const unsigned wlo = (unsigned)(uintptr_t)a.ws, whi = (unsigned)((uintptr_t)a.ws >> 32);
    const v8u pv{lane16, na(nf, N_PG) + g0 * 32 + (lane < gn ? (lane >> 2) * 128 + (lane & 3) * 4 : 0), lane == 0 ? na(nf ^ PDX, N_PD) + (cg * NKF + p) * 128 + w * 4 : OOB,
                 unit_tab(lane), lane16, (unsigned)(L_OK + lane * 4), (unsigned)((lane & 8) * 2048 + (lane >> 4) * 128 + (lane & 7) * 16), 0u};
    const v16u s0{wlo, whi, RSZ, 0x20000u, wlo, whi, 0u, 0x20000u, wlo, whi, w == 0 ? RSZ : 0u, 0x20000u, 0u, O_H + (w >> 1) * 114688 + (F0 + (w & 1) * 12) * 1024,
                  O_D + p * 65536 + (cg * 8 + w * 2) * 512, (unsigned)(65536 + (w >> 1) * 32768 + (w & 1) * 12288)};
    const v4u s1{(unsigned)(L_OK + w * 256), 0u, 0u, O_PROF + (blockIdx.x * 4 + w) * 8192};
    asm volatile(D_LOOP :: "{v[0:7]}"(pv), "{s[8:23]}"(s0), "{s[24:27]}"(s1) : "memory", "s99", "vcc");
}
template <int R> DEVI void ldv1(rsrc_t rs, unsigned vo) { asm volatile("buffer_load_dword v[%2], %0, %1, 0 offen sc1" :: "v"(vo), "s"(rs), "n"(R) : "memory"); }
template <int R, int I> DEVI void unp2(f32x4& d) { float x, y; asm volatile("v_lshlrev_b32 %0, 16, v[%2]\n\tv_and_b32 %1, 0xffff0000, v[%2]" : "=v"(x), "=v"(y) : "n"(R)); d[I] = x; d[I + 1] = y; }
DEVI void role_RED(const Args& a, int w, int cu) {
    asm volatile("; MARK role_RED"); __builtin_amdgcn_s_waitcnt(0);
    const int lane0 = lane_id(), c = lane0 & 15, g = lane0 >> 4;
    const rsrc_t rs = mkrs(a.ws, WS_NEED);
    const unsigned nf = side(rs, FSIDE);
    v4u sel[2];
#pragma unroll
    for (int e = 0; e < 2; ++e)
#pragma unroll
        for (int i = 0; i < 4; ++i) sel[e][i] = (g == (c >> 2) && i == 2 * e + ((c >> 1) & 1)) ? ((c & 1) ? 0x3F800000u : 0x3F80u) : 0u;
    const unsigned vfl = na(nf ^ PDX, N_PD) + w * (NKF * 128) + (lane0 < 20 ? (lane0 >> 2) * 128 + (lane0 & 3) * 4 : 0);
    constexpr int VP = NV + 32;
    const unsigned lo = (c & 8) * 128 + g * 256 + (c & 7) * 16 + (cu & 1) * 128 + w * 8192, ld = (c & 8) * 64 + g * 128 + (c & 7) * 16 + (cu & 1) * 16384 + w * 4096;
    const unsigned lout = ((cu & 1) * 8 + (c & 7)) * 2048 + w * 512 + (c & 8) * 8 + g * 16, lw = g == (c >> 2) ? (c & 7) * 64 + w * 16 + (c & 8) : 1024 + lane0 * 8;
    const rsrc_t ro = mkrs(a.out, 4u << 20);
    v4u one{0x3F803F80u, 0x3F803F80u, 0x3F803F80u, 0x3F803F80u};
    asm volatile("" : "+v"(one));
    auto issue = [&](auto B, auto N0, auto N1, int k) {
        const unsigned ls = k >> 2, base = ((k >> 1) & 1) * 32768;
        sfor<N0.v, N1.v>([&](auto N) { constexpr int f = (N.v - 4) / 5, p = (N.v - 4) % 5;
            if constexpr (N.v < 4) ldv4<NV + 16 * B.v + 4 * N.v>(rs, lo, O_XM + ls * 65536 + base + N.v * 2048);
            else lda<80 * B.v + 20 * f + 4 * p, 4, f * 1024>(rs, ld, O_D + (ls * NKF + p) * 65536 + base); });
        if constexpr (N1.v == 24) ldv1<VP>(rs, vfl);
    };
    *LDSP(unsigned char, 2048 + unit_ls(2 * lane0)) = 2 * lane0; *LDSP(unsigned char, 2048 + unit_ls(2 * lane0 + 1)) = 2 * lane0 + 1;
    WL0; BAR;
    const unsigned tab = opq(*LDSP(const unsigned short, 2048 + 2 * lane0));
    auto next = [&](int k) { const int n = k + (k >= RSH ? 6 : k < 256 ? 4 : 2); return cu > 3 && k < 4 ? 252 + cu : k < RSH && n >= (k < 256 ? 256 : RSH) ? RSH + cu : n; };
    u64 late = 1, far = 1;
    bool pre = false;
    auto task = [&](auto B, int k) {
        constexpr int b = B.v;
        const int u = unit_get(tab, k >> 2), rt = (k >> 1) & 1, kn = next(k), nx = kn < 4 * NU ? unit_get(tab, kn >> 2) + 1 : NU + 1;
        const unsigned ls = k >> 2, tile = ls * 2 + rt;
        TSD; TSR(0);
        if (!pre) {
            if (late) {
                unsigned spins = 0;
#pragma nounroll
                for (;;) {
                    if (far) __builtin_amdgcn_s_sleep(PSF); else __builtin_amdgcn_s_sleep(PSR);
                    CB;
                    const int v = (int)ld4(rs, vfl, 0);
                    if (__builtin_amdgcn_sicmp(v, u + 1, 40) == 0) break;
                    far = __builtin_amdgcn_sicmp(v, u, 40);
                    spin_bail(spins);
                }
            }
            TSR(1);
            issue(B, IC<0>{}, IC<24>{}, k);
        }
        else { TSR(1); }
        const bool l0 = ls < 64;
        const rsrc_t rd = l0 ? rs : ro;
        const unsigned vd = opq((l0 ? O_XIN + XB + lo : lout - 64 * 65536) + tile * 32768), fs = l0 ? 2048 : 128;
        v4u x[4];
        f32x4 d[4][2], S, Q;
        auto unp = [&](auto F, auto I) { unp2<NV + 16 * b + 4 * F.v + I.v, 2 * (I.v & 1)>(d[F.v][I.v >> 1]); };
        auto fin = [&](auto F) {
            constexpr int f = F.v;
            fence(d[f][0], d[f][1]);
#pragma unroll
            for (int i = 0; i < 4; ++i) x[f][i] = pack2(d[f][i >> 1][2 * (i & 1)], d[f][i >> 1][2 * (i & 1) + 1]);
            asm volatile("" : "+v"(x[f]));
        };
        auto put = [&](auto F) { constexpr int f = F.v; st16s(x[f], rd, vd, f * fs); SB; mfv<f == 0>(S, one, x[f]); mfv<f == 0>(Q, x[f], x[f]); SB; };
        wvm<24>();
        sfor<0, 4>([&](auto I) { unp(IC<0>{}, I); });
        sfor<0, 4>([&](auto F) {
            constexpr int f = F.v;
            wvm<16 - 5 * f + (f ? f - 1 : 0)>();
            nop<1>();
            sfor<0, 10>([&](auto N) {
                mfB<80 * b + 20 * f + 4 * (N.v >> 1), false>(d[f][N.v & 1], sel[N.v & 1]);
                if constexpr (N.v < 4 && f < 3) unp(IC<(f + 1) & 3>{}, N);
                if constexpr (N.v == 5 && f > 0) fin(IC<(f + 3) & 3>{});
                SB;
            });
            if constexpr (f > 0) put(IC<(f + 3) & 3>{});
        });
        wvm<3>();
        TSR(2);
#ifdef TS
        tsr[4] = pre + 1;
#endif
        const bool p1 = ltv<VP>(nx) == 0;
        if (!p1) ldv1<VP + 1>(rs, vfl);
        nop<3>();
        fin(IC<3>{});
        nop<1>();
        put(IC<3>{});
        auto second = [&](auto NS) {
            wvm<NS.v>();
            pre = ltv<VP + 1>(nx) == 0;
            if (pre) { issue(IC<1 - b>{}, IC<0>{}, IC<24>{}, kn); if constexpr (NS.v == 5) wvm<25>(); }
            else {
                if constexpr (NS.v == 5) { ldv1<VP + 1>(rs, vfl); WV0; }
                late = ltv<VP + 1>(nx); far = ltv<VP + 1>(nx - 1);
            }
        };
        if (l0) {
            nop<7>();
            fence(S, Q);
            const float qd = selm(selm(Q[0], Q[1], 0xAAAAAAAAAAAAAAAAull), selm(Q[2], Q[3], 0xAAAAAAAAAAAAAAAAull), 0xCCCCCCCCCCCCCCCCull);
            *LDSP(f32x2, lw) = f32x2{S[0], qd};
            if (p1) issue(IC<1 - b>{}, IC<0>{}, IC<RPA>{}, kn);
            f32x2 xf[4][4];
#pragma unroll
            for (int f = 0; f < 4; ++f)
#pragma unroll
                for (int i = 0; i < 4; ++i) { xf[f][i] = f32x2{bflo(x[f][i]), bfhi(x[f][i])}; asm volatile("" : "+v"(xf[f][i])); }
            TSR(5);
            WL0; BAR;
            f32x4 t[4];
#pragma unroll
            for (int i = 0; i < 4; ++i) t[i] = __builtin_bit_cast(f32x4, ldsq((c & 7) * 64, i * 16));
            const f32x4 t4 = (t[0] + t[1]) + (t[2] + t[3]);
            const float mean = (t4[0] + t4[2]) * (1.0f / 1024.0f), var = fmaxf(rbf(fmaf(t4[1] + t4[3], 1.0f / 1024.0f, -mean * mean)), 0.f);
            const float inv = __builtin_amdgcn_rcpf(rbf(__builtin_amdgcn_sqrtf(rbf(var + 1e-5f)))), mi = -rbf(mean) * inv;
#pragma unroll
            for (int f = 0; f < 4; ++f) {
                v4u o;
#pragma unroll
                for (int i = 0; i < 4; ++i) xf[f][i] = __builtin_elementwise_fma(xf[f][i], f32x2{inv, inv}, f32x2{mi, mi});
#pragma unroll
                for (int i = 0; i < 4; ++i) o[i] = pack2(xf[f][i][0], xf[f][i][1]);
                st16(o, rs, vd + (O_XN - O_XIN) + f * 2048);
            }
            TSR(6);
            if (p1) { issue(IC<1 - b>{}, IC<RPA>{}, IC<24>{}, kn); TSR(7); wvm<25 - RPA>(); pre = true; } else second(IC<5>{});
            BAR;
            st4(0u, rs, lane0 < 2 && w == 0 ? na(nf ^ (FSIDE ? 0 : 8192), N_FX) + tile * 16 + (cu & 1) * 8 + lane0 * 4 : OOB, 0);
        }
        else if (p1) { issue(IC<1 - b>{}, IC<0>{}, IC<24>{}, kn); pre = true; }
        else second(IC<1>{});
        TSR(3); TSF(u, 0xFF);
    };
#pragma nounroll
    for (int k = cu < 2 ? cu + 4 : cu & 3;;) {
        task(IC<0>{}, k); k = next(k);
        if (k >= 4 * NU) break;
        task(IC<1>{}, k); k = next(k);
        if (k >= 4 * NU) break;
    }
}
#ifndef OFFM
#define OFFM 0
#endif
#ifndef LSW
#define LSW 0
#endif
#ifndef PQD
#define PQD 0
#endif
#ifndef PQ0
#define PQ0 2048
#endif
#ifndef DLY_A
#define DLY_A 400
#endif
#ifndef DLY_B
#define DLY_B 0
#endif
#ifndef DLY_1
#define DLY_1 1800
#endif
#ifndef DLY_G
#define DLY_G 1400
#endif
#ifndef DLY_D
#define DLY_D 1600
#endif
__global__ __launch_bounds__(256, 1) __attribute__((amdgpu_num_vgpr(NV))) void mega6(Args a) {
    asm volatile("" ::: "v255", "a255");
    const int w = __builtin_amdgcn_readfirstlane(threadIdx.x >> 6), b = blockIdx.x, xcd = b & 7, slot = b >> 3, l = (xcd >> 2) ^ LSW, idx = (slot & 7) * 4 + (xcd & 3), f = slot - 24;
    const int t0 = (int)__builtin_amdgcn_s_memrealtime(), xf = xcd;
    TSW(127, 0);
    if (slot < 24 && l && (slot < 16 || idx >= LN_NOW)) pull_all(a, w, (slot >> 3) * 32 + idx - (slot < 16 ? 0 : LN_NOW), t0);
    if (PQ && f >= 0 && f < 4) { hold(t0, PQD); pull(a.Wq + PQ0 * 512, 8192 - PQ0, (f * 8 + xcd) * 4 + w, 128); }
    if (slot < 8) { hold(t0, l ? DLY_1 : DLY_A); role_A(a, w, l, idx, (xcd & 3) * 2); }
    else if (slot < 16) { hold(t0, l ? DLY_1 : DLY_B); role_B(a, w, l, idx, (xcd & 3) * 2); }
    else if (slot < 24) { role_R(a, w, l, idx, t0); if (l && idx + w == 0) vote_end(mkrs(a.ws, WS_NEED)); }
    else if (f < (xf < 3 ? 6 : 4)) { hold(t0, DLY_G); if (!(OFFM & 1)) role_GU(a, w, xf < 3 ? 6 * xf + f : 6 + 4 * xf + f); }
    else if (xf < 3) {
        const int cu = xf + 3 * (f - 6), q = cu * 4 + w;
        use(ld4(mkrs(a.ws, WS_NEED), q < 2 * NVK + 2 ? prb(q >> 1, q & 1) + lane_id() * 32 : OOB, 0));
        hold(t0, DLY_D); if (!(OFFM & 4)) role_RED(a, w, cu);
    }
    else {
        if (xf < 7) { vote(mkrs(a.ws, WS_NEED), w, (xf - 3) * 4 + f - 4, l, t0); }
        hold(t0, DLY_D); if (!(OFFM & 2)) role_DN(a, w, xf - 3, f - 4); }
}
extern "C" void kernel_launch(void* const* d_in, const int* in_sizes, int n_in, void* d_out, int out_size, void* d_ws, size_t ws_size, hipStream_t stream) {
    (void)in_sizes; (void)n_in; (void)out_size; (void)ws_size;
    (void)hipFuncSetAttribute((const void*)mega6, hipFuncAttributeMaxDynamicSharedMemorySize, 163840);
    d_ws = (void*)(((uintptr_t)d_ws + (2u << 20) - 1) & ~(uintptr_t)((2u << 20) - 1));
    Args a{(const u16*)d_in[0], (const u16*)d_in[1], (const u16*)d_in[2], (const u16*)d_in[3], (const u16*)d_in[4], (const u16*)d_in[5],
           (const u16*)d_in[8], (const u16*)d_in[9], (const u16*)d_in[10], (char*)d_ws, (u16*)d_out};
    mega6<<<256, 256, 163840, stream>>>(a);
}
```

```cpp
#include <hip/hip_runtime.h>
#include <stdint.h>

typedef unsigned short u16;
typedef unsigned long long u64;
typedef __attribute__((ext_vector_type(8))) __bf16 bf8;
typedef __attribute__((ext_vector_type(16))) __bf16 bf16v;
typedef __attribute__((ext_vector_type(4))) float f32x4;
typedef __attribute__((ext_vector_type(16))) float f32x16;
typedef __attribute__((ext_vector_type(2))) float f32x2;
typedef __attribute__((ext_vector_type(8))) unsigned v8u;
typedef __attribute__((ext_vector_type(4))) unsigned v4u;
typedef __attribute__((ext_vector_type(3))) unsigned v3u;
typedef __attribute__((ext_vector_type(2))) unsigned v2u;
typedef __amdgpu_buffer_rsrc_t rsrc_t;
typedef __attribute__((address_space(3))) char lds_char;
#define DEVI __device__ __forceinline__
#define LDSP(T, p) ((__attribute__((address_space(3))) T*)(p))

typedef __attribute__((ext_vector_type(2))) __bf16 bf16x2_t;
DEVI unsigned pack2(float lo, float hi) { f32x2 v = {lo, hi}; bf16x2_t b = __builtin_convertvector(v, bf16x2_t); return __builtin_bit_cast(unsigned, b); }
DEVI float bflo(unsigned u) { return __uint_as_float(u << 16); }
DEVI float bfhi(unsigned u) { return __uint_as_float(u & 0xffff0000u); }
DEVI float rbf(float x) { return bflo(pack2(x, x)); }
constexpr int NV = 112;
#define MR_(R, A_STR, V_STR, OUTS, INS, W) do { if constexpr ((R) < 256) asm volatile(A_STR : OUTS : INS, [lo] "n"(R), [hi] "n"((R) + (W) - 1)); \
                                                else asm volatile(V_STR : OUTS : INS, [lo] "n"((R) - 256 + NV), [hi] "n"((R) - 256 + NV + (W) - 1)); } while (0)
#define CM_ ,
template <int R> DEVI void mwr(unsigned x) { if constexpr (R < 256) asm volatile("v_accvgpr_write_b32 a[%1], %0" :: "v"(x), "n"(R)); else asm volatile("v_mov_b32 v[%1], %0" :: "v"(x), "n"(R - 256 + NV)); }
template <int R> DEVI void mwr4(v4u x) { mwr<R>(x[0]); mwr<R + 1>(x[1]); mwr<R + 2>(x[2]); mwr<R + 3>(x[3]); }
#define M16 "v_mfma_f32_16x16x32_bf16 "
#define M32 "v_mfma_f32_32x32x16_bf16 "
#define S16 "v_smfmac_f32_16x16x64_bf16 "
template <int R, bool Z> DEVI void mfA(f32x4& d, v4u b) {
    if constexpr (Z) MR_(R, M16 "%[d], a[%[lo]:%[hi]], %[b], 0", M16 "%[d], v[%[lo]:%[hi]], %[b], 0", [d] "=&v"(d), [b] "v"(b), 4);
    else MR_(R, M16 "%[d], a[%[lo]:%[hi]], %[b], %[d]", M16 "%[d], v[%[lo]:%[hi]], %[b], %[d]", [d] "+v"(d), [b] "v"(b), 4);
}
template <int R, bool Z> DEVI void mfB(f32x4& d, v4u a) {
    if constexpr (Z) MR_(R, M16 "%[d], %[a], a[%[lo]:%[hi]], 0", M16 "%[d], %[a], v[%[lo]:%[hi]], 0", [d] "=&v"(d), [a] "v"(a), 4);
    else MR_(R, M16 "%[d], %[a], a[%[lo]:%[hi]], %[d]", M16 "%[d], %[a], v[%[lo]:%[hi]], %[d]", [d] "+v"(d), [a] "v"(a), 4);
}
template <int R, bool Z> DEVI void mf32A(f32x16& d, v4u b) {
    if constexpr (Z) MR_(R, M32 "%[d], a[%[lo]:%[hi]], %[b], 0", M32 "%[d], v[%[lo]:%[hi]], %[b], 0", [d] "=&v"(d), [b] "v"(b), 4);
    else MR_(R, M32 "%[d], a[%[lo]:%[hi]], %[b], %[d]", M32 "%[d], v[%[lo]:%[hi]], %[b], %[d]", [d] "+v"(d), [b] "v"(b), 4);
}
template <bool Z> DEVI void mfv(f32x4& d, v4u a, v4u b) {
    if constexpr (Z) asm volatile(M16 "%0, %1, %2, 0" : "=&v"(d) : "v"(a), "v"(b)); else asm volatile(M16 "%0, %1, %2, %0" : "+v"(d) : "v"(a), "v"(b));
}
template <int R> DEVI void smf(f32x4& d, v4u a, int idx) { MR_(R, S16 "%[d], %[a], a[%[lo]:%[hi]], %[i]", S16 "%[d], %[a], v[%[lo]:%[hi]], %[i]", [d] "+v"(d), [a] "v"(a) CM_ [i] "v"(idx), 8); }
DEVI void smfv(f32x4& d, v4u a, v8u b, int idx) { asm volatile(S16 "%0, %1, %2, %3" : "+v"(d) : "v"(a), "v"(b), "v"(idx)); }
DEVI void zero4(f32x4& d) { d = f32x4{0.f, 0.f, 0.f, 0.f}; asm volatile("" : "+v"(d)); }
template <int N> DEVI void nop() { asm volatile("s_nop %0" :: "n"(N)); }
DEVI void fence(f32x4& d) { asm volatile("" : "+v"(d)); }
template <class... T> DEVI void fence(f32x4& d, T&... r) { fence(d); fence(r...); }
DEVI float selm(float a, float b, u64 m) { float r; asm("v_cndmask_b32 %0, %1, %2, %3" : "=v"(r) : "v"(a), "v"(b), "s"(m)); return r; }
DEVI int opqs(int x) { asm volatile("" : "+s"(x)); return x; }
DEVI v4u lo4(v8u v) { return __builtin_shufflevector(v, v, 0, 1, 2, 3); }
DEVI v4u hi4(v8u v) { return __builtin_shufflevector(v, v, 4, 5, 6, 7); }
DEVI v8u cat8(v4u a, v4u b) { return __builtin_shufflevector(a, b, 0, 1, 2, 3, 4, 5, 6, 7); }
constexpr int AUX_SC1 = 16;
DEVI v4u ld16(rsrc_t r, unsigned vo, unsigned so) { return __builtin_amdgcn_raw_buffer_load_b128(r, vo, so, AUX_SC1); }
DEVI v2u ld8(rsrc_t r, unsigned vo, unsigned so) { return __builtin_amdgcn_raw_buffer_load_b64(r, vo, so, AUX_SC1); }
DEVI unsigned ld4(rsrc_t r, unsigned vo, unsigned so) { return __builtin_amdgcn_raw_buffer_load_b32(r, vo, so, AUX_SC1); }
DEVI void st16(v4u v, rsrc_t r, unsigned vo) { __builtin_amdgcn_raw_buffer_store_b128(v, r, vo, 0, AUX_SC1); }
DEVI void st16s(v4u v, rsrc_t r, unsigned vo, unsigned so) { __builtin_amdgcn_raw_buffer_store_b128(v, r, vo, so, AUX_SC1); }
DEVI void st8(v2u v, rsrc_t r, unsigned vo, unsigned so) { __builtin_amdgcn_raw_buffer_store_b64(v, r, vo, so, AUX_SC1); }
DEVI void st4(unsigned v, rsrc_t r, unsigned vo, unsigned so) { __builtin_amdgcn_raw_buffer_store_b32(v, r, vo, so, AUX_SC1); }
#define CB asm volatile("" ::: "memory")
#define WL0 asm volatile("s_waitcnt lgkmcnt(0)" ::: "memory")
#define WV0 asm volatile("s_waitcnt vmcnt(0)" ::: "memory")
#define BAR do { __builtin_amdgcn_s_barrier(); CB; } while (0)
#define SB __builtin_amdgcn_sched_barrier(0)
DEVI float fadd(float a, float b) { float r; asm("v_add_f32 %0, %1, %2" : "=v"(r) : "v"(a), "v"(b)); return r; }
DEVI float vfma(float a, float b, float c) { float r; asm("v_fma_f32 %0, %1, %2, %3" : "=v"(r) : "v"(a), "v"(b), "v"(c)); return r; }
DEVI unsigned vxor(unsigned a, unsigned b) { unsigned r; asm volatile("v_xor_b32 %0, %1, %2" : "=v"(r) : "i"(b), "v"(a)); return r; }
DEVI int lane_id() { int r; asm volatile("v_mbcnt_lo_u32_b32 %0, -1, 0\n\tv_mbcnt_hi_u32_b32 %0, -1, %0" : "=v"(r)); __builtin_assume(r >= 0 && r < 64); return r; }
DEVI int opq(int x) { asm volatile("" : "+v"(x)); return x; }
DEVI float vmaxf(float a, float b) { float r; asm("v_max_f32 %0, %1, %2" : "=v"(r) : "v"(a), "v"(b)); return r; }
DEVI void swap32(float& a, float& b) { auto r = __builtin_amdgcn_permlane32_swap(__float_as_uint(a), __float_as_uint(b), false, false); a = __uint_as_float(r[0]); b = __uint_as_float(r[1]); }
DEVI void swap32u(unsigned& a, unsigned& b) { auto r = __builtin_amdgcn_permlane32_swap(a, b, false, false); a = r[0]; b = r[1]; }
DEVI void swap16u(unsigned& a, unsigned& b) { auto r = __builtin_amdgcn_permlane16_swap(a, b, false, false); a = r[0]; b = r[1]; }
DEVI float hadd(float a, float b) { swap32(a, b); return fadd(a, b); }
DEVI float red32(float x) { float y = x; swap32(x, y); return x + y; }
DEVI float red16(float x) { unsigned a = __float_as_uint(x), b = a; swap16u(a, b); return __uint_as_float(a) + __uint_as_float(b); }
DEVI float mx32(float x) { float y = x; swap32(x, y); return vmaxf(x, y); }
DEVI float mx16(float x) { unsigned a = __float_as_uint(x), b = a; swap16u(a, b); return vmaxf(__uint_as_float(a), __uint_as_float(b)); }
template <int N> struct IC { static constexpr int v = N; };
template <int I, int N, class F> DEVI void sfor(F&& f) { if constexpr (I < N) { f(IC<I>{}); sfor<I + 1, N>(f); } }
#ifndef NSEG
#define NSEG 64
#endif
#ifndef LAGD
#define LAGD 10
#endif
constexpr float SC_L2E = 0.08838834764831845f * 1.4426950408889634f;
constexpr unsigned SENT = 0xFFFFFFFFu;
#ifndef NUS_
#define NUS_ 8
#endif
#ifndef NRG_
#define NRG_ 8
#endif
#ifndef NC
#define NC 8
#endif
constexpr int NREP = 8, NUS = NUS_, NRG = NRG_, NKF = 5, NGU = 38;
constexpr unsigned XB = 4u << 20;
constexpr unsigned O_XIN = 0;
constexpr unsigned O_XN  = O_XIN + 2 * XB;
constexpr unsigned O_XM  = O_XN + 2 * XB;
constexpr unsigned O_H   = O_XM + 2 * XB;
constexpr unsigned O_D   = O_H + 2u * 64 * 224 * 1024;
constexpr unsigned O_N = O_D + 2u * 64 * NKF * 65536;
constexpr unsigned DV(unsigned o) { return ((o & ~8191u) << 1) | (o & 8191u); }
DEVI unsigned na(unsigned nb, unsigned o) { return (DV(o) + nb) ^ ((o >> 7) & 8192u); }
constexpr unsigned N_PM  = 0;
constexpr unsigned N_PMM = N_PM + NRG * 16384;
constexpr unsigned N_PA  = (1u << 20) - 16384;
constexpr unsigned N_MEM = 1u << 20;
constexpr unsigned N_WM  = 2u << 20;
constexpr unsigned N_PTX = 6u << 20;
constexpr unsigned N_U   = 10u << 20;
static_assert(NRG == 8 && NUS == 8 && NREP == 8 && O_N % (4u << 20) == 0, "");
constexpr unsigned O_FLG_END = O_N + DV(N_MEM + NREP * 16384);
constexpr unsigned N_FX  = N_PA - 8192;
constexpr unsigned N_PG  = N_FX - 8192;
constexpr unsigned N_PD  = N_PG + 40 * 128;
constexpr unsigned PG2 = DV(N_PA + 8192 - N_PG);
constexpr unsigned O_PRB = O_N + (512u << 10), O_CAL = O_N + (768u << 10);
constexpr unsigned O_SX  = O_N + DV(N_PG) - 2u * 64 * 32 * 256;
constexpr unsigned O_FLG = O_SX;
static_assert(O_PRB >= O_N + DV(N_PMM + NRG * 8192) && O_FLG >= O_CAL + 8192 && (O_FLG_END - O_FLG) % 65536 == 0, "");
constexpr unsigned OOB = 0x80000000u;
constexpr unsigned WS_NEED = O_N + DV(N_U + NUS * (2u << 20));
constexpr unsigned O_PROF = 240u << 20;
static_assert(WS_NEED <= (200u << 20), "workspace too large");

#ifdef DRY
#define WSZ(x) 0u
#else
#define WSZ(x) (x)
#endif
struct Args { const u16 *x, *mem0, *Wq, *Wk, *Wv, *Wo, *Wg, *Wu, *Wd; char* ws; u16* out; };
#ifndef SPIN_LOG
#define SPIN_LOG 18
#endif
constexpr unsigned SPIN_CAP = 1u << SPIN_LOG;
DEVI void spin_bail(unsigned& spins) {
    asm volatile("s_add_u32 %0, %0, 1\n\ts_cmp_lt_u32 %0, %1\n\ts_cbranch_scc1 1f\n\ts_endpgm\n1:" : "+s"(spins) : "n"(SPIN_CAP) : "scc");
}
template <int K> DEVI void wrl(unsigned& v, u64& t) { asm volatile("" : "+s"(t)); asm volatile("v_writelane_b32 %0, %1, %2" : "+v"(v) : "s"((unsigned)t), "n"(K)); }
#ifdef TS
#ifndef TSCLK
#define TSCLK __builtin_amdgcn_s_memrealtime
#endif
#define TSV(u, i, v) __builtin_amdgcn_raw_buffer_store_b32((unsigned)(v), mkrs(a.ws, 256u << 20), lane_id() == 0 ? O_PROF + ((blockIdx.x * 4 + w) * 128 + (u)) * 64 + (i) * 4 : OOB, 0, 0)
#define TSW(u, i) TSV(u, i, TSCLK())
#define TSC(u, i) TSV(u, i, __builtin_amdgcn_s_memtime())
#define TSD u64 tsr[16] = {}
#define TSR(k) tsr[k] = rclk()
#define TSK(k) asm volatile("s_memtime %0" : "=s"(tsr[k]))
#define TSF(u, m) do { unsigned tv_ = 0; WL0; sfor<0, 16>([&](auto K) { if constexpr (((m) >> K.v) & 1) wrl<K.v>(tv_, tsr[K.v]); }); \
    const int tl_ = lane_id(); __builtin_amdgcn_raw_buffer_store_b32(tv_, mkrs(a.ws, 256u << 20), (tl_ < 16 && (((m) >> tl_) & 1)) ? O_PROF + ((blockIdx.x * 4 + w) * 128 + (u)) * 64 + tl_ * 4 : OOB, 0, 0); } while (0)
#if TS == 4
#undef TSV
#undef TSR
#undef TSK
#define TSV(u, i, v)
#define TSR(k)
#define TSK(k)
#define FSK(G, O, D) if constexpr ((G) >= (O) && ((G) - (O)) % (D) == 0 && ((G) - (O)) / (D) < FN) asm volatile("s_memtime %0" : "=s"(tsr[((G) - (O)) / (D)]))
#define FSF(u) TSF(u, (1 << FN) - 1)
#define FSD TSD
#endif
#else
#define TSW(u, i)
#define TSC(u, i)
#define TSV(u, i, v)
#define TSD
#define TSR(k)
#define TSK(k)
#define TSF(u, m)
#endif
#if TS != 4
#define FSK(G, O, D)
#define FSF(u)
#define FSD
#endif
DEVI unsigned rslot(int s) { return (unsigned)(s & (NRG - 1)); }
DEVI u64 pend(v4u d) { return __builtin_amdgcn_uicmp(max(d[0], d[2]), SENT, 32); }
DEVI u64 pend2(v2u d) { return __builtin_amdgcn_uicmp(d[0], SENT, 32); }
DEVI u64 pend1(unsigned d) { return __builtin_amdgcn_uicmp(d, SENT, 32); }
DEVI unsigned nosent(unsigned u) { return min(u, 0xFFFFFFFEu); }
#ifndef WPOL
#define WPOL ""
#endif
template <int OFF, bool SC1> DEVI void dma1(rsrc_t rs, unsigned vo, unsigned so) {
    if constexpr (SC1) asm volatile("buffer_load_dwordx4 %0, %1, %2 offen offset:%3 sc1 lds" :: "v"(vo), "s"(rs), "s"(so), "n"(OFF) : "memory");
    else asm volatile("buffer_load_dwordx4 %0, %1, %2 offen offset:%3 " WPOL " lds" :: "v"(vo), "s"(rs), "s"(so), "n"(OFF) : "memory");
}
DEVI void setm0(unsigned v) { asm volatile("s_mov_b32 m0, %0\n\ts_nop 0" :: "s"(v) : "memory"); }
DEVI void dma4(unsigned lds, rsrc_t rs, unsigned vo, unsigned so) { setm0(lds); dma1<0, true>(rs, vo, so); dma1<1024, true>(rs, vo, so); dma1<2048, true>(rs, vo, so); dma1<3072, true>(rs, vo, so); }
DEVI void dmaw(unsigned lds, rsrc_t rs, unsigned vo, unsigned so) { setm0(lds); asm volatile("buffer_load_dword %0, %1, %2 offen sc1 lds" :: "v"(vo), "s"(rs), "s"(so) : "memory"); }
template <int N> DEVI void wvm() { asm volatile("s_waitcnt vmcnt(%0)" :: "n"(N) : "memory"); }
template <int N> DEVI void wlg() { asm volatile("s_waitcnt lgkmcnt(%0)" :: "n"(N) : "memory"); }
DEVI void dma4p(u64 c0, u64 c1, u64 c2, u64 c3, rsrc_t rs, unsigned vo, unsigned so) {
    asm volatile(
        "s_cmp_eq_u64 %[c0], 0\n\ts_cbranch_scc1 1f\n\t"
        "buffer_load_dwordx4 %[vo], %[rs], %[so] offen sc1 lds\n"
        "1:\n\ts_cmp_eq_u64 %[c1], 0\n\ts_cbranch_scc1 2f\n\t"
        "buffer_load_dwordx4 %[vo], %[rs], %[so] offen offset:1024 sc1 lds\n"
        "2:\n\ts_cmp_eq_u64 %[c2], 0\n\ts_cbranch_scc1 3f\n\t"
        "buffer_load_dwordx4 %[vo], %[rs], %[so] offen offset:2048 sc1 lds\n"
        "3:\n\ts_cmp_eq_u64 %[c3], 0\n\ts_cbranch_scc1 4f\n\t"
        "buffer_load_dwordx4 %[vo], %[rs], %[so] offen offset:3072 sc1 lds\n"
        "4:"
        :: [c0] "s"(c0), [c1] "s"(c1), [c2] "s"(c2), [c3] "s"(c3), [vo] "v"(vo), [rs] "s"(rs), [so] "s"(so)
        : "memory", "scc");
}
#ifndef PSL
#define PSL 8
#endif
DEVI void wait_prog(rsrc_t rs, unsigned vo, unsigned so, int need) {
    unsigned spins = 0;
#pragma nounroll
    for (;;) {
        CB;
        const int v = (int)ld4(rs, vo, so);
        if (__builtin_amdgcn_sicmp(v, need, 40) == 0) return;
        spin_bail(spins);
        __builtin_amdgcn_s_sleep(PSL);
    }
}

typedef __attribute__((ext_vector_type(16))) int v16i;
typedef __attribute__((ext_vector_type(16))) unsigned v16u;
#ifdef TS
constexpr unsigned RSZ = 256u << 20;
#else
constexpr unsigned RSZ = WS_NEED;
#endif
DEVI u64 rclk() { u64 t; asm volatile("s_memrealtime %0" : "=s"(t)); return t; }
DEVI unsigned tsv(u64& t) { asm volatile("s_waitcnt lgkmcnt(0)" : "+s"(t)); return (unsigned)t; }
DEVI unsigned stamp(u64& t, int tag, int lead) { asm volatile("" : "+s"(t)); return (unsigned)tag << 24 | (((unsigned)t + lead) & 0xFFFFFFu); }
#ifndef SXTH
#define SXTH 20
#endif
DEVI int left(int t) { return (int)((unsigned)(t - (int)__builtin_amdgcn_s_memrealtime()) << 8); }
DEVI void hold(int t0, int d) { for (int n = 0; n < 4096 && (int)__builtin_amdgcn_s_memrealtime() - t0 < d; ++n) __builtin_amdgcn_s_sleep(4); }
DEVI int smin(int a, int b) { int r; asm("s_min_i32 %0, %1, %2" : "=s"(r) : "s"(a), "s"(b) : "scc"); return r; }
DEVI int smax(int a, int b) { int r; asm("s_max_i32 %0, %1, %2" : "=s"(r) : "s"(a), "s"(b) : "scc"); return r; }
DEVI void smin8x2(const v16i& f, int& m0, int& m1) {
    asm("s_min_i32 %0, %2, %3\n\ts_min_i32 %1, %10, %11\n\ts_min_i32 %0, %0, %4\n\ts_min_i32 %1, %1, %12\n\ts_min_i32 %0, %0, %5\n\ts_min_i32 %1, %1, %13\n\ts_min_i32 %0, %0, %6\n\ts_min_i32 %1, %1, %14\n\t"
        "s_min_i32 %0, %0, %7\n\ts_min_i32 %1, %1, %15\n\ts_min_i32 %0, %0, %8\n\ts_min_i32 %1, %1, %16\n\ts_min_i32 %0, %0, %9\n\ts_min_i32 %1, %1, %17"
        : "=&s"(m0), "=&s"(m1) : "s"(f[0]), "s"(f[1]), "s"(f[2]), "s"(f[3]), "s"(f[4]), "s"(f[5]), "s"(f[6]), "s"(f[7]),
          "s"(f[8]), "s"(f[9]), "s"(f[10]), "s"(f[11]), "s"(f[12]), "s"(f[13]), "s"(f[14]), "s"(f[15]) : "scc");
}
DEVI void smax8x2(const v16i& f, int& m0, int& m1) {
    asm("s_max_i32 %0, %2, %3\n\ts_max_i32 %1, %10, %11\n\ts_max_i32 %0, %0, %4\n\ts_max_i32 %1, %1, %12\n\ts_max_i32 %0, %0, %5\n\ts_max_i32 %1, %1, %13\n\ts_max_i32 %0, %0, %6\n\ts_max_i32 %1, %1, %14\n\t"
        "s_max_i32 %0, %0, %7\n\ts_max_i32 %1, %1, %15\n\ts_max_i32 %0, %0, %8\n\ts_max_i32 %1, %1, %16\n\ts_max_i32 %0, %0, %9\n\ts_max_i32 %1, %1, %17"
        : "=&s"(m0), "=&s"(m1) : "s"(f[0]), "s"(f[1]), "s"(f[2]), "s"(f[3]), "s"(f[4]), "s"(f[5]), "s"(f[6]), "s"(f[7]),
          "s"(f[8]), "s"(f[9]), "s"(f[10]), "s"(f[11]), "s"(f[12]), "s"(f[13]), "s"(f[14]), "s"(f[15]) : "scc");
}
#ifndef NVK
#define NVK 8
#endif
#ifndef DLY_V
#define DLY_V 500
#endif
#ifndef PSV
#define PSV 12
#endif
#ifndef DLY_P
#define DLY_P 150
#endif
DEVI unsigned prb(int k, int b) { return O_PRB + k * 16384 + b * 8192 + (k & 1) * 4096; }
DEVI void vote(rsrc_t rs, int w, int v, int far, int t0s) {
    const int lane = lane_id();
    constexpr unsigned N = (O_FLG_END - O_FLG) / 65536;
#pragma nounroll
    for (unsigned q = 0; q < N; ++q) st16(v4u{SENT, SENT, SENT, SENT}, rs, O_FLG + ((v * 4 + w) * N + q) * 1024 + lane * 16);
    const v2u e = ld8(rs, O_CAL + (lane & 31) * 256 + 128, 0);
    static_assert(NVK % 4 == 0 && NVK < 12, "");
    int best[2] = {1 << 30, 1 << 30};
    hold(t0s, DLY_P);
#pragma nounroll
    for (int k = 0; k <= NVK / 4; ++k)
#pragma unroll
        for (int b = 0; b < 2; ++b) {
            u64 t0, t1; int d;
            asm volatile("s_memtime %0\n\ts_waitcnt lgkmcnt(0)\n\ts_buffer_load_dword %2, %3, %4 glc\n\ts_waitcnt lgkmcnt(0)\n\ts_memtime %1\n\ts_waitcnt lgkmcnt(0)"
                         : "=&s"(t0), "=&s"(t1), "=&s"(d) : "s"(rs), "s"(prb(k ? 4 * k - w : 0, b) + v * 128) : "memory");
            if (k) best[b] = smin(best[b], (int)(t1 - t0));
        }
    *LDSP(int, (lane & 1) * 16 + w * 4) = best[lane & 1];
    WV0; WL0; BAR;
#pragma unroll
    for (int b = 0; b < 2; ++b) { const v4u t = *LDSP(const v4u, b * 16); best[b] = min(min(t[0], t[1]), min(t[2], t[3])); }
    st8(v2u{((e[0] + 2) & ~1u) | ((best[1] < best[0]) ^ far), e[1] + 1}, rs, lane < 32 && w == 0 ? O_CAL + lane * 256 + v * 8 : OOB, 0);
}
DEVI void vote_end(rsrc_t rs) {
    const unsigned o = O_CAL + (lane_id() & 31) * 256 + 128;
    const v2u e = ld8(rs, o, 0);
    st8(v2u{e[0] + 2, e[1] + 1}, rs, o, 0);
}
DEVI unsigned side(rsrc_t rs, int l) {
    unsigned spins = 0;
#pragma nounroll
    for (;;) {
        v16u f[2]; v2u e;
        const unsigned o = O_CAL + (blockIdx.x & 31) * 256;
        asm volatile("s_buffer_load_dwordx16 %0, %3, %4 glc\n\ts_buffer_load_dwordx16 %1, %3, %5 glc\n\ts_buffer_load_dwordx2 %2, %3, %6 glc\n\ts_waitcnt lgkmcnt(0)"
                     : "=&s"(f[0]), "=&s"(f[1]), "=&s"(e) : "s"(rs), "s"(o), "s"(o + 64), "s"(o + 128) : "memory");
        unsigned bad = 0, sum = 0;
#pragma unroll
        for (int i = 0; i < 16; ++i) { const unsigned x = f[i >> 3][2 * i & 15]; bad |= ((x ^ (e[0] + 2)) >> 1) | (f[i >> 3][(2 * i & 15) + 1] ^ (e[1] + 1)); sum += x & 1; }
#ifdef CSIDE
        sum = CSIDE * 16;
#endif
        if (bad == 0) return opqs(O_N + ((l ^ (sum > 8)) & 1) * 8192);
        spin_bail(spins);
        __builtin_amdgcn_s_sleep(PSV);
    }
}
#ifndef PSH
#define PSH 2
#endif
DEVI int wait_hint(rsrc_t rs, unsigned so, unsigned vo, int tag) {
#pragma nounroll
    for (int n = 0;; ++n) {
        v16i f;
        asm volatile("s_buffer_load_dwordx16 %0, %1, %2 glc\n\ts_waitcnt lgkmcnt(0)" : "=s"(f) : "s"(rs), "s"(so) : "memory");
        int m0, m1;
        smin8x2(f, m0, m1);
        if (smin(m0, m1) >= tag << 24) { smax8x2(f, m0, m1); return smax(m0, m1); }
        if (n == 40) { wait_prog(rs, vo, 0, tag << 24); n = 0; }
        __builtin_amdgcn_s_sleep(PSH);
    }
}
template <class WM> constexpr int widx(int j, int t) {
    int n = 0;
    for (int q = 0; q < j * WM::NP + t; ++q) n += WM::kind(q / WM::NP, q % WM::NP) == WM::kind(j, t);
    return n;
}
template <class WM> constexpr int lds_tile(int t) { for (int j = 0; j < WM::NT; ++j) if (WM::kind(j, t) == 1) return j; return -1; }
DEVI v4u ldsq(unsigned p, int off) { return *LDSP(const v4u, LDSP(char, p) + off); }
DEVI void stsq(unsigned p, int off, v4u v) { *LDSP(v4u, LDSP(char, p) + off) = v; }
DEVI v8u ldpair(unsigned wl, int slot) { return cat8(ldsq(wl, slot * 2048), ldsq(wl, slot * 2048 + 1024)); }
DEVI rsrc_t mkrs(const void* p, unsigned bytes) { return __builtin_amdgcn_make_buffer_rsrc((void*)p, 0, (int)bytes, 0x00020000); }
#ifndef NPL
#define NPL 72
#endif
#ifndef LNN
#define LNN 8
#endif
constexpr int LN_NOW = LNN, NP0 = NPL * 4;
template <int SL = 0> DEVI void pull(const void* p, int n, int q, int nq) {
    const rsrc_t r = mkrs(p, n * 1024);
    const unsigned vo = lane_id() * 16;
#pragma nounroll
    for (; q < n; q += nq) { asm volatile("buffer_load_dwordx4 a[0:3], %0, %1, %2 offen" :: "v"(vo), "s"(r), "s"(q * 1024) : "memory"); if (SL) __builtin_amdgcn_s_sleep(SL); }
}
#ifndef PQ
#define PQ 0
#endif
#ifndef DLY_F
#define DLY_F 1000
#endif
#ifndef PFS
#define PFS 5
#endif
DEVI void pull_all(const Args& a, int w, int q, int t0) {
    q = q * 4 + w;
    if (q < NP0) {
    pull(a.Wv, 8192, q, NP0); pull(a.Wo, 8192, q, NP0); if (!PQ) pull(a.Wq, 8192, q, NP0); pull(a.Wk, 8192, q, NP0);
    }
    if (q >= 256) return;
    hold(t0, DLY_F);
    pull<PFS>(q & 1 ? a.Wg : a.Wu, 7168, q >> 1, 128); pull<PFS>(a.Wd, 7168, q, 256);
}
template <class WM, int j, int ks, int O0, int O1> DEVI void put_frag(unsigned ta, unsigned wl, v8u* Wc) {
    constexpr int t = ks >> 1, f = ks & 1, k = WM::kind(j, t), ix = widx<WM>(j, t);
    if constexpr (k == 0) {
        constexpr int R = WM::RB + 8 * ix + 4 * f;
        if constexpr (R < 256) asm volatile("ds_read_b64_tr_b16 a[%1:%2], %0 offset:%5\n\tds_read_b64_tr_b16 a[%3:%4], %0 offset:%6" :: "v"(ta), "n"(R), "n"(R + 1), "n"(R + 2), "n"(R + 3), "n"(O0), "n"(O1) : "memory");
        else asm volatile("ds_read_b64_tr_b16 v[%1:%2], %0 offset:%5\n\tds_read_b64_tr_b16 v[%3:%4], %0 offset:%6" :: "v"(ta), "n"(R - 256 + NV), "n"(R - 255 + NV), "n"(R - 254 + NV), "n"(R - 253 + NV), "n"(O0), "n"(O1) : "memory");
    } else {
        v2u p, q;
        asm volatile("ds_read_b64_tr_b16 %0, %2 offset:%3\n\tds_read_b64_tr_b16 %1, %2 offset:%4\n\ts_waitcnt lgkmcnt(0)" : "=&v"(p), "=&v"(q) : "v"(ta), "n"(O0), "n"(O1) : "memory");
        const v4u F = v4u{p[0], p[1], q[0], q[1]};
        if constexpr (k == 1) stsq(wl, (WM::LB + ix) * 2048 + f * 1024, F);
        else if constexpr (f == 0) Wc[WM::CBI + ix] = cat8(F, F);
        else Wc[WM::CBI + ix] = cat8(lo4(Wc[WM::CBI + ix]), F);
    }
}
template <int NB, int DEPTH, int NI, bool SHARED = false, int NX = 0, class ISSUE, class PUT>
DEVI void load_blocks(ISSUE&& issue, PUT&& put) {
    sfor<0, DEPTH - 1>([&](auto B) { issue(B, IC<B.v % DEPTH>{}); });
    sfor<0, NB>([&](auto B) {
        constexpr int b = B.v, left = NB + NX - 1 - b < DEPTH - 2 ? NB + NX - 1 - b : DEPTH - 2;
        wvm<NI * left>();
        wlg<0>();
        if constexpr (SHARED) BAR;
        if constexpr (b + DEPTH - 1 < NB + NX) issue(IC<b + DEPTH - 1>{}, IC<(b + DEPTH - 1) % DEPTH>{});
        put(B, IC<b % DEPTH>{});
    });
    wlg<0>();
    if constexpr (SHARED) BAR;
}
DEVI void first_look(unsigned& fx, rsrc_t rs, unsigned nb) { asm volatile("buffer_load_dword %0, %1, %2, %3 offen sc1" : "=v"(fx) : "v"((lane_id() & 7) * 4), "s"(rs), "s"(na(nb, N_FX)) : "memory"); }
DEVI unsigned first_there(unsigned& fx) { asm volatile("" : "+v"(fx)); return pend1(fx) == 0 ? WS_NEED : 0u; }
DEVI void dma_blk(unsigned lds, rsrc_t rw, unsigned vo, unsigned so, unsigned rowb) { setm0(lds); dma1<0, false>(rw, vo, so); setm0(lds + 1024); dma1<0, false>(rw, vo, so + 16 * rowb); }
template <class WM, int NKS, int K0, int K1, int RTS, int LATE1, class FILL>
DEVI void gemm_pass(unsigned xa, unsigned wl, const v8u* Wc, v4u (&x)[2][3], f32x4 (&acc)[WM::NT][2], FILL&& fill) {
    auto rd = [&](auto KS, auto Q) {
        constexpr int ks = KS.v, q = Q.v, jl = lds_tile<WM>(ks >> 1), il = widx<WM>(jl < 0 ? 0 : jl, ks >> 1);
        if constexpr (q == 0 && jl >= 0) x[ks & 1][2] = ldsq(wl, (WM::LB + il) * 2048 + (ks & 1) * 1024);
        if constexpr (q == 1) x[ks & 1][1] = ldsq(xa, RTS + ks * 1024);
        if constexpr (q == 2) x[ks & 1][0] = ldsq(xa, ks * 1024);
    };
    sfor<K0 * 2 * WM::NT, K1 * 2 * WM::NT>([&](auto N) {
        constexpr int ks = N.v / (2 * WM::NT), n = N.v % (2 * WM::NT), j = n >> 1, rt = n & 1, t = ks >> 1, f = ks & 1, k = WM::kind(j, t), ix = widx<WM>(j, t);
        fill(IC<ks>{}, IC<n>{});
        SB;
        const v4u xv = x[ks & 1][rt];
        if constexpr (k == 0) mfA<WM::RB + 8 * ix + 4 * f, ks == 0>(acc[j][rt], xv);
        else if constexpr (k == 1) mfv<ks == 0>(acc[j][rt], x[ks & 1][2], xv);
        else mfv<ks == 0>(acc[j][rt], f ? hi4(Wc[WM::CBI + ix]) : lo4(Wc[WM::CBI + ix]), xv);
        if constexpr (ks + 1 < NKS) rd(IC<ks + 1>{}, IC<n - (ks == 1 ? LATE1 : 0)>{});
        SB;
    });
}
template <class WM> constexpr int next_lds(int n) { for (; n < 16 * WM::NT; ++n) if (WM::kind(2 * (n >> 5) + (n & 1), (n >> 1) & 15) == 1) return n; return -1; }
template <class WM> constexpr int lds_cnt(int n) { int c = 0; for (int q = next_lds<WM>(0); q >= 0 && q < n; q = next_lds<WM>(q + 1)) ++c; return c; }
template <class WM> constexpr int lds_nth(int k) { int q = next_lds<WM>(0); for (; k > 0 && q >= 0; --k) q = next_lds<WM>(q + 1); return q; }
template <class WM, int K, int NB> DEVI void pair_next(v8u (&pb)[NB], unsigned wl) {
    constexpr int n = lds_nth<WM>(K);
    if constexpr (n >= 0) { constexpr int ix = widx<WM>(2 * (n >> 5) + (n & 1), (n >> 1) & 15); pb[K % NB] = ldpair(wl, WM::LB + ix); }
}
template <class WM, int MO, int MR, int NB, class FILL>
DEVI void spass(unsigned ma, unsigned wl, const v8u* Wc, f32x4* e, v4u (&m)[MR], v8u (&pb)[NB], int sidx, FILL&& fill) {
    sfor<0, 16 * WM::NT>([&](auto N) {
        constexpr int n = N.v, t = (n >> 1) & 15, j = 2 * (n >> 5) + (n & 1), k = WM::kind(j, t), ix = widx<WM>(j, t), tn = (t + MR - 1) & 15, r = (n >> 1) % MR;
        fill(N);
        SB;
        if constexpr (k == 0) smf<WM::RB + 8 * ix>(e[j], m[r], sidx);
        else if constexpr (k == 1) { constexpr int q = lds_cnt<WM>(n); smfv(e[j], m[r], pb[q % NB], sidx); pair_next<WM, q + NB>(pb, wl); }
        else smfv(e[j], m[r], Wc[WM::CBI + ix], sidx);
        if constexpr ((n & 1) == 0 && n + 2 * MR - 2 < 16 * WM::NT) m[(r + MR - 1) % MR] = ldsq(ma, MO + (tn >> 3) * 32768 + (tn & 7) * 1024);
        SB;
    });
}
typedef __attribute__((ext_vector_type(4))) short s16x4;
DEVI v2u ldtr(unsigned p, int off) { return __builtin_bit_cast(v2u, __builtin_amdgcn_ds_read_tr16_b64_v4i16(LDSP(s16x4, LDSP(char, p) + off))); }
DEVI v4u ldtr2(unsigned p, int off) { const v2u a = ldtr(p, off), b = ldtr(p, off + 256); return v4u{a[0], a[1], b[0], b[1]}; }
DEVI void sts1(unsigned p, int off, unsigned v) { *LDSP(unsigned, LDSP(char, p) + off) = v; }
DEVI unsigned lds1(unsigned p, int off) { return *LDSP(const unsigned, LDSP(char, p) + off); }
DEVI void use(unsigned v) { asm volatile("" :: "v"(v) : "memory"); }
DEVI int mk_sidx(int lane) { return opq(0x4444 + (lane & 2) * 0x5555); }
DEVI int crow(int r, int hb) { return 4 * (r >> 1) + 2 * hb + (r & 1); }
DEVI v4u again(int id, unsigned nb, int w) { const u64 k = (u64)__builtin_amdgcn_kernarg_segment_ptr(); return v4u{(unsigned)k, (unsigned)(k >> 32), id < 0 ? ~0u : 256 + id + ((nb >> 4) & 512), (unsigned)w * 64}; }
#define A_LDS 0x28a0114002004410ull
#define A_VAR 0x0000000400020001ull
#define A_LOOP \
"s_brev_b32 s99, 1\ns_brev_b32 s98, 1\ns_brev_b32 s97, 1\ns_brev_b32 s96, 1\n" \
"s_branch 4f\n.p2align 6\n1:\ns_waitcnt lgkmcnt(0)\n" \
"v_mfma_f32_16x16x32_bf16 v[44:47], v[76:79], v[80:83], v[44:47]\nds_read_b128 v[36:39], v25 offset:3072\nds_read_b128 v[40:43], v24 offset:21504\nv_mfma_f32_16x16x32_bf16 v[48:51], v[76:79], v[32:35], v[48:51]\n" \
"ds_read_b128 v[84:87], v24 offset:54272\nv_bfe_u32 v31, v24, 6, 3\nv_mfma_f32_16x16x32_bf16 v[52:55], a[160:163], v[80:83], v[52:55]\ns_mov_b32 m0, s50\n" \
"s_add_u32 s99, s30, 2\nv_lshlrev_b32 v31, 2, v31\nv_mfma_f32_16x16x32_bf16 v[56:59], a[160:163], v[32:35], v[56:59]\ns_min_u32 s99, s99, 63\n" \
"s_lshl_b32 s99, s99, 5\ns_add_u32 s99, s99, s48\nv_mfma_f32_16x16x32_bf16 v[60:63], v[120:123], v[80:83], v[60:63]\ns_and_b32 s94, s30, 7\n" \
"s_lshl_b32 s94, s94, 18\ns_add_u32 s94, s94, s33\nv_mfma_f32_16x16x32_bf16 v[64:67], v[120:123], v[32:35], v[64:67]\ns_add_u32 s31, s31, 0x10000\n" \
"v_mfma_f32_16x16x32_bf16 v[68:71], v[224:227], v[80:83], v[68:71]\ns_waitcnt lgkmcnt(1)\nv_mfma_f32_16x16x32_bf16 v[72:75], v[224:227], v[32:35], v[72:75]\ns_waitcnt lgkmcnt(0)\n" \
"ds_read_b128 v[32:35], v24 offset:22528\nv_mfma_f32_16x16x32_bf16 v[44:47], v[36:39], v[40:43], v[44:47]\nds_read_b128 v[76:79], v24 offset:55296\nv_mfma_f32_16x16x32_bf16 v[48:51], v[36:39], v[84:87], v[48:51]\n" \
"v_mfma_f32_16x16x32_bf16 v[52:55], a[164:167], v[40:43], v[52:55]\nds_read_b128 v[36:39], v25 offset:18432\nv_mfma_f32_16x16x32_bf16 v[56:59], a[164:167], v[84:87], v[56:59]\nv_mfma_f32_16x16x32_bf16 v[60:63], v[124:127], v[40:43], v[60:63]\n" \
"v_mfma_f32_16x16x32_bf16 v[64:67], v[124:127], v[84:87], v[64:67]\nv_mfma_f32_16x16x32_bf16 v[68:71], v[228:231], v[40:43], v[68:71]\ns_waitcnt lgkmcnt(1)\nv_mfma_f32_16x16x32_bf16 v[72:75], v[228:231], v[84:87], v[72:75]\n" \
"ds_read_b128 v[40:43], v24 offset:23552\nv_mfma_f32_16x16x32_bf16 v[44:47], a[64:67], v[32:35], v[44:47]\nds_read_b128 v[80:83], v24 offset:56320\nv_mfma_f32_16x16x32_bf16 v[48:51], a[64:67], v[76:79], v[48:51]\n" \
"v_mfma_f32_16x16x32_bf16 v[52:55], a[168:171], v[32:35], v[52:55]\nds_read_b128 v[84:87], v25 offset:19456\nv_mfma_f32_16x16x32_bf16 v[56:59], a[168:171], v[76:79], v[56:59]\nv_mfma_f32_16x16x32_bf16 v[60:63], v[128:131], v[32:35], v[60:63]\n" \
"s_waitcnt lgkmcnt(3)\nv_mfma_f32_16x16x32_bf16 v[64:67], v[128:131], v[76:79], v[64:67]\nv_mfma_f32_16x16x32_bf16 v[68:71], v[36:39], v[32:35], v[68:71]\ns_waitcnt vmcnt(8) lgkmcnt(1)\n" \
"s_barrier\nv_mfma_f32_16x16x32_bf16 v[72:75], v[36:39], v[76:79], v[72:75]\nds_read_b128 v[32:35], v24 offset:24576\nv_mfma_f32_16x16x32_bf16 v[44:47], a[68:71], v[40:43], v[44:47]\n" \
"ds_read_b128 v[36:39], v24 offset:57344\nv_mfma_f32_16x16x32_bf16 v[48:51], a[68:71], v[80:83], v[48:51]\nds_read_b128 v[76:79], v25 offset:12288\nv_mfma_f32_16x16x32_bf16 v[52:55], a[172:175], v[40:43], v[52:55]\n" \
"buffer_load_dword v31, s[8:11], s99 offen offset:0 sc1 lds\nv_mfma_f32_16x16x32_bf16 v[56:59], a[172:175], v[80:83], v[56:59]\ns_add_u32 m0, s32, 0x4000\nv_mfma_f32_16x16x32_bf16 v[60:63], v[132:135], v[40:43], v[60:63]\n" \
"s_waitcnt lgkmcnt(3)\nv_mfma_f32_16x16x32_bf16 v[64:67], v[132:135], v[80:83], v[64:67]\nv_mfma_f32_16x16x32_bf16 v[68:71], v[84:87], v[40:43], v[68:71]\ns_waitcnt lgkmcnt(1)\n" \
"v_mfma_f32_16x16x32_bf16 v[72:75], v[84:87], v[80:83], v[72:75]\nds_read_b128 v[40:43], v24 offset:25600\nv_mfma_f32_16x16x32_bf16 v[44:47], a[72:75], v[32:35], v[44:47]\nds_read_b128 v[80:83], v24 offset:58368\n" \
"v_mfma_f32_16x16x32_bf16 v[48:51], a[72:75], v[36:39], v[48:51]\nds_read_b128 v[84:87], v25 offset:13312\nv_mfma_f32_16x16x32_bf16 v[52:55], a[176:179], v[32:35], v[52:55]\ns_waitcnt lgkmcnt(3)\n" \
"v_mfma_f32_16x16x32_bf16 v[56:59], a[176:179], v[36:39], v[56:59]\nv_mfma_f32_16x16x32_bf16 v[60:63], v[76:79], v[32:35], v[60:63]\nv_mfma_f32_16x16x32_bf16 v[64:67], v[76:79], v[36:39], v[64:67]\nv_mfma_f32_16x16x32_bf16 v[68:71], v[232:235], v[32:35], v[68:71]\n" \
"s_waitcnt lgkmcnt(1)\nv_mfma_f32_16x16x32_bf16 v[72:75], v[232:235], v[36:39], v[72:75]\nbuffer_load_dwordx4 v24, s[8:11], s94 offen offset:0 sc1 lds\nv_mfma_f32_16x16x32_bf16 v[44:47], a[76:79], v[40:43], v[44:47]\n" \
"ds_read_b128 v[32:35], v24 offset:26624\nv_mfma_f32_16x16x32_bf16 v[48:51], a[76:79], v[80:83], v[48:51]\nds_read_b128 v[36:39], v24 offset:59392\nv_mfma_f32_16x16x32_bf16 v[52:55], a[180:183], v[40:43], v[52:55]\n" \
"s_waitcnt lgkmcnt(2)\nbuffer_load_dwordx4 v24, s[8:11], s94 offen offset:1024 sc1 lds\nv_mfma_f32_16x16x32_bf16 v[56:59], a[180:183], v[80:83], v[56:59]\nbuffer_load_dwordx4 v24, s[8:11], s94 offen offset:2048 sc1 lds\n" \
"v_mfma_f32_16x16x32_bf16 v[60:63], v[84:87], v[40:43], v[60:63]\nbuffer_load_dwordx4 v24, s[8:11], s94 offen offset:3072 sc1 lds\nv_mfma_f32_16x16x32_bf16 v[64:67], v[84:87], v[80:83], v[64:67]\nds_read_b128 v[76:79], v25 offset:20480\n" \
"s_and_b32 s93, s30, 15\nv_mfma_f32_16x16x32_bf16 v[68:71], v[236:239], v[40:43], v[68:71]\ns_lshl_b32 s92, s93, 17\ns_add_u32 s92, s92, s35\n" \
"s_and_b32 s93, s93, 7\nv_mfma_f32_16x16x32_bf16 v[72:75], v[236:239], v[80:83], v[72:75]\ns_waitcnt lgkmcnt(1)\nds_read_b128 v[40:43], v24 offset:27648\n" \
"v_mfma_f32_16x16x32_bf16 v[44:47], a[80:83], v[32:35], v[44:47]\nds_read_b128 v[80:83], v24 offset:60416\ns_lshr_b32 s95, s30, s41\nv_mfma_f32_16x16x32_bf16 v[48:51], a[80:83], v[36:39], v[48:51]\n" \
"s_and_b32 s95, s95, 1\ns_lshl_b32 s93, s93, 14\ns_lshl1_add_u32 s99, s93, s34\nv_mfma_f32_16x16x32_bf16 v[52:55], a[184:187], v[32:35], v[52:55]\n" \
"ds_read_b128 v[84:87], v25 offset:21504\ns_or_b32 s98, s92, s36\nv_mfma_f32_16x16x32_bf16 v[56:59], a[184:187], v[36:39], v[56:59]\ns_or_b32 s97, s92, s37\n" \
"s_and_b32 s92, s92, s39\ns_andn2_b32 s93, s93, s39\nv_mfma_f32_16x16x32_bf16 v[60:63], v[136:139], v[32:35], v[60:63]\nds_read_b128 v[88:91], v25 offset:4096\n" \
"s_add_u32 s96, s92, s38\nv_mfma_f32_16x16x32_bf16 v[64:67], v[136:139], v[36:39], v[64:67]\ns_waitcnt lgkmcnt(4)\ns_add_u32 s96, s96, s93\n" \
"s_add_u32 s30, s30, 1\nv_mfma_f32_16x16x32_bf16 v[68:71], v[76:79], v[32:35], v[68:71]\ns_lshl_b32 s93, s30, 24\ns_cmp_le_u32 s30, 64\n" \
"s_cselect_b32 s51, s14, 0\nv_mfma_f32_16x16x32_bf16 v[72:75], v[76:79], v[36:39], v[72:75]\ns_waitcnt lgkmcnt(2)\nds_read_b128 v[32:35], v24 offset:28672\n" \
"v_mfma_f32_16x16x32_bf16 v[44:47], a[84:87], v[40:43], v[44:47]\nds_read_b128 v[36:39], v24 offset:61440\nv_mfma_f32_16x16x32_bf16 v[48:51], a[84:87], v[80:83], v[48:51]\nv_mfma_f32_16x16x32_bf16 v[52:55], a[188:191], v[40:43], v[52:55]\n" \
"v_mfma_f32_16x16x32_bf16 v[56:59], a[188:191], v[80:83], v[56:59]\nv_mfma_f32_16x16x32_bf16 v[60:63], v[140:143], v[40:43], v[60:63]\ns_waitcnt lgkmcnt(2)\nds_read_b128 v[76:79], v25 offset:5120\n" \
"v_mfma_f32_16x16x32_bf16 v[64:67], v[140:143], v[80:83], v[64:67]\nv_mfma_f32_16x16x32_bf16 v[68:71], v[84:87], v[40:43], v[68:71]\ns_waitcnt lgkmcnt(1)\nv_mfma_f32_16x16x32_bf16 v[72:75], v[84:87], v[80:83], v[72:75]\n" \
"ds_read_b128 v[40:43], v24 offset:29696\nv_mfma_f32_16x16x32_bf16 v[44:47], v[88:91], v[32:35], v[44:47]\nds_read_b128 v[80:83], v24 offset:62464\nv_mfma_f32_16x16x32_bf16 v[48:51], v[88:91], v[36:39], v[48:51]\n" \
"v_mfma_f32_16x16x32_bf16 v[52:55], a[192:195], v[32:35], v[52:55]\nv_mfma_f32_16x16x32_bf16 v[56:59], a[192:195], v[36:39], v[56:59]\nv_mfma_f32_16x16x32_bf16 v[60:63], v[144:147], v[32:35], v[60:63]\nv_mfma_f32_16x16x32_bf16 v[64:67], v[144:147], v[36:39], v[64:67]\n" \
"v_mfma_f32_16x16x32_bf16 v[68:71], v[240:243], v[32:35], v[68:71]\ns_waitcnt lgkmcnt(0)\nv_mfma_f32_16x16x32_bf16 v[72:75], v[240:243], v[36:39], v[72:75]\nds_read_b128 v[32:35], v24 offset:30720\n" \
"v_add_u32 v31, s32, v24\nv_mfma_f32_16x16x32_bf16 v[44:47], v[76:79], v[40:43], v[44:47]\nds_read_b128 v[36:39], v24 offset:63488\nv_mfma_f32_16x16x32_bf16 v[48:51], v[76:79], v[80:83], v[48:51]\n" \
"v_mfma_f32_16x16x32_bf16 v[52:55], a[196:199], v[40:43], v[52:55]\nv_mfma_f32_16x16x32_bf16 v[56:59], a[196:199], v[80:83], v[56:59]\nv_mfma_f32_16x16x32_bf16 v[60:63], v[148:151], v[40:43], v[60:63]\nv_mfma_f32_16x16x32_bf16 v[64:67], v[148:151], v[80:83], v[64:67]\n" \
"v_mfma_f32_16x16x32_bf16 v[68:71], v[244:247], v[40:43], v[68:71]\ns_waitcnt vmcnt(0) lgkmcnt(0)\nds_read_b128 v[40:43], v31 offset:16384\nds_read_b128 v[76:79], v31 offset:17408\n" \
"ds_read_b128 v[84:87], v31 offset:18432\nv_mfma_f32_16x16x32_bf16 v[72:75], v[244:247], v[80:83], v[72:75]\nds_read_b128 v[88:91], v31 offset:19456\nv_mfma_f32_16x16x32_bf16 v[44:47], a[88:91], v[32:35], v[44:47]\n" \
"ds_read_b128 v[80:83], v24 offset:31744\nv_mfma_f32_16x16x32_bf16 v[48:51], a[88:91], v[36:39], v[48:51]\nds_read_b128 v[92:95], v24 offset:64512\nv_mfma_f32_16x16x32_bf16 v[52:55], a[200:203], v[32:35], v[52:55]\n" \
"v_mfma_f32_16x16x32_bf16 v[56:59], a[200:203], v[36:39], v[56:59]\ns_waitcnt lgkmcnt(3)\nv_max3_u32 v96, v40, v76, v84\nv_mov_b64 v[100:101], 0\n" \
"v_mfma_f32_16x16x32_bf16 v[60:63], v[152:155], v[32:35], v[60:63]\nds_read_b128 v[104:107], v25 offset:0\nv_mov_b64 v[102:103], 0\nv_mfma_f32_16x16x32_bf16 v[64:67], v[152:155], v[36:39], v[64:67]\n" \
"s_waitcnt lgkmcnt(3)\nv_max_u32 v96, v96, v88\nv_cmp_eq_u32 vcc, -1, v96\nv_mfma_f32_16x16x32_bf16 v[68:71], v[248:251], v[32:35], v[68:71]\n" \
"ds_read_b128 v[108:111], v25 offset:1024\nv_mov_b64 v[32:33], 0\nv_mfma_f32_16x16x32_bf16 v[72:75], v[248:251], v[36:39], v[72:75]\ns_waitcnt lgkmcnt(2)\n" \
"s_cbranch_vccnz 2f\n3:\nv_mfma_f32_16x16x32_bf16 v[44:47], a[92:95], v[80:83], v[44:47]\ns_barrier\n" \
"s_memrealtime s[56:57]\nv_mfma_f32_16x16x32_bf16 v[52:55], a[204:207], v[80:83], v[52:55]\nds_read_b128 v[36:39], v24 offset:20480\nv_mov_b64 v[34:35], 0\n" \
"v_mfma_f32_16x16x32_bf16 v[60:63], v[156:159], v[80:83], v[60:63]\nds_read_b128 v[40:43], v24 offset:21504\nv_add_u32 v31, s44, v24\nv_mfma_f32_16x16x32_bf16 v[68:71], v[252:255], v[80:83], v[68:71]\n" \
"ds_read_b128 v[76:79], v24 offset:16384\nv_cvt_pk_bf16_f32 v44, v44, v52\nv_mfma_f32_16x16x32_bf16 v[48:51], a[92:95], v[92:95], v[48:51]\nds_read_b128 v[80:83], v24 offset:22528\n" \
"v_cvt_pk_bf16_f32 v45, v45, v53\nv_mfma_f32_16x16x32_bf16 v[56:59], a[204:207], v[92:95], v[56:59]\nds_read_b128 v[84:87], v24 offset:23552\nv_cvt_pk_bf16_f32 v46, v46, v54\n" \
"v_mfma_f32_16x16x32_bf16 v[64:67], v[156:159], v[92:95], v[64:67]\nds_read_b128 v[88:91], v24 offset:17408\nv_cvt_pk_bf16_f32 v47, v47, v55\nv_mfma_f32_16x16x32_bf16 v[72:75], v[252:255], v[92:95], v[72:75]\n" \
"s_waitcnt lgkmcnt(5)\nds_write_b64 v31, v[44:45] offset:0\nv_smfmac_f32_16x16x64_bf16 v[100:103], v[36:39], v[104:111], v26\nds_read_b128 v[52:55], v24 offset:18432\n" \
"v_cvt_pk_bf16_f32 v60, v60, v68\nv_smfmac_f32_16x16x64_bf16 v[32:35], v[36:39], a[120:127], v26\ns_waitcnt lgkmcnt(5)\nds_write_b64 v31, v[46:47] offset:8\n" \
"v_smfmac_f32_16x16x64_bf16 v[100:103], v[40:43], a[24:31], v26\nds_read_b128 v[92:95], v24 offset:19456\nv_cvt_pk_bf16_f32 v61, v61, v69\nv_smfmac_f32_16x16x64_bf16 v[32:35], v[40:43], a[128:135], v26\n" \
"ds_write_b64 v31, v[60:61] offset:32768\nv_smfmac_f32_16x16x64_bf16 v[100:103], v[76:79], v[0:7], v26\nds_read_b128 v[44:47], v24 offset:49152\nv_cvt_pk_bf16_f32 v62, v62, v70\n" \
"v_smfmac_f32_16x16x64_bf16 v[32:35], v[76:79], a[96:103], v26\ns_waitcnt lgkmcnt(5)\ns_add_u32 s94, s56, 0x7e\nv_cvt_pk_bf16_f32 v63, v63, v71\n" \
"v_smfmac_f32_16x16x64_bf16 v[100:103], v[80:83], a[32:39], v26\nds_read_b128 v[68:71], v24 offset:50176\ns_and_b32 s94, s94, 0xffffff\nv_smfmac_f32_16x16x64_bf16 v[32:35], v[80:83], a[136:143], v26\n" \
"ds_read_b128 v[76:79], v25 offset:6144\ns_or_b32 s94, s94, s93\nv_smfmac_f32_16x16x64_bf16 v[100:103], v[84:87], a[40:47], v26\nds_read_b128 v[80:83], v25 offset:7168\n" \
"v_mov_b32 v60, s94\nv_smfmac_f32_16x16x64_bf16 v[32:35], v[84:87], a[144:151], v26\nds_read_b128 v[96:99], v24 offset:51200\nv_cvt_pk_bf16_f32 v48, v48, v56\n" \
"v_smfmac_f32_16x16x64_bf16 v[100:103], v[88:91], a[0:7], v26\nds_read_b128 v[104:107], v25 offset:2048\nv_cvt_pk_bf16_f32 v49, v49, v57\nv_smfmac_f32_16x16x64_bf16 v[32:35], v[88:91], v[8:15], v26\n" \
"s_waitcnt lgkmcnt(5)\nds_read_b128 v[108:111], v25 offset:3072\nv_smfmac_f32_16x16x64_bf16 v[100:103], v[52:55], a[8:15], v26\nds_read_b128 v[84:87], v24 offset:56320\n" \
"v_cvt_pk_bf16_f32 v50, v50, v58\nv_smfmac_f32_16x16x64_bf16 v[32:35], v[52:55], a[104:111], v26\nds_write_b64 v31, v[62:63] offset:32776\nv_smfmac_f32_16x16x64_bf16 v[100:103], v[92:95], a[16:23], v26\n" \
"buffer_store_dword v60, v29, s[8:11], s49 offen offset:0 sc1\nv_smfmac_f32_16x16x64_bf16 v[32:35], v[92:95], a[112:119], v26\nds_read_b128 v[52:55], v24 offset:52224\nv_cvt_pk_bf16_f32 v51, v51, v59\n" \
"v_smfmac_f32_16x16x64_bf16 v[100:103], v[44:47], a[48:55], v26\nds_read_b128 v[56:59], v24 offset:53248\nv_cvt_pk_bf16_f32 v64, v64, v72\nv_smfmac_f32_16x16x64_bf16 v[32:35], v[44:47], a[152:159], v26\n" \
"s_waitcnt lgkmcnt(4)\nds_write_b64 v31, v[48:49] offset:4096\nv_smfmac_f32_16x16x64_bf16 v[100:103], v[68:71], a[56:63], v26\nds_read_b128 v[60:63], v24 offset:54272\n" \
"v_cvt_pk_bf16_f32 v65, v65, v73\nv_smfmac_f32_16x16x64_bf16 v[32:35], v[68:71], v[76:83], v26\nds_read_b128 v[92:95], v24 offset:55296\nv_cvt_pk_bf16_f32 v66, v66, v74\n" \
"v_smfmac_f32_16x16x64_bf16 v[100:103], v[96:99], v[104:111], v26\nds_read_b128 v[68:71], v25 offset:4096\nv_cvt_pk_bf16_f32 v67, v67, v75\nv_smfmac_f32_16x16x64_bf16 v[32:35], v[96:99], a[160:167], v26\n" \
"s_waitcnt lgkmcnt(5)\nds_read_b128 v[72:75], v25 offset:5120\nv_smfmac_f32_16x16x64_bf16 v[100:103], v[84:87], a[88:95], v26\nds_write_b64 v31, v[50:51] offset:4104\n" \
"v_smfmac_f32_16x16x64_bf16 v[32:35], v[84:87], a[200:207], v26\nds_read_b128 v[76:79], v25 offset:14336\nv_mov_b64 v[48:49], 0\nv_smfmac_f32_16x16x64_bf16 v[100:103], v[52:55], a[64:71], v26\n" \
"ds_read_b128 v[80:83], v25 offset:15360\nv_mov_b64 v[50:51], 0\nv_smfmac_f32_16x16x64_bf16 v[32:35], v[52:55], a[168:175], v26\ns_waitcnt lgkmcnt(5)\n" \
"ds_write_b64 v31, v[64:65] offset:36864\nv_smfmac_f32_16x16x64_bf16 v[100:103], v[56:59], a[72:79], v26\nds_read_b128 v[96:99], v24 offset:51200\nv_mov_b64 v[64:65], 0\n" \
"v_smfmac_f32_16x16x64_bf16 v[32:35], v[56:59], a[176:183], v26\nds_write_b64 v31, v[66:67] offset:36872\nv_smfmac_f32_16x16x64_bf16 v[100:103], v[60:63], a[80:87], v26\nds_read_b128 v[56:59], v24 offset:18432\n" \
"v_mov_b64 v[66:67], 0\nv_smfmac_f32_16x16x64_bf16 v[32:35], v[60:63], a[184:191], v26\ns_waitcnt lgkmcnt(5)\nds_read_b32 v31, v28 offset:512\n" \
"v_smfmac_f32_16x16x64_bf16 v[100:103], v[92:95], v[68:75], v26\nv_add_u32 v60, s45, v24\nv_smfmac_f32_16x16x64_bf16 v[32:35], v[92:95], a[192:199], v26\nds_read_b128 v[68:71], v24 offset:54272\n" \
"v_cndmask_b32 v60, v60, v28, s[28:29]\nv_smfmac_f32_16x16x64_bf16 v[48:51], v[40:43], a[240:247], v26\ns_waitcnt lgkmcnt(4)\nds_read_b128 v[104:107], v25 offset:18432\n" \
"v_smfmac_f32_16x16x64_bf16 v[64:67], v[40:43], v[76:83], v26\nds_read_b128 v[108:111], v25 offset:19456\nv_add_f32 v40, v100, v102\nv_smfmac_f32_16x16x64_bf16 v[48:51], v[36:39], a[232:239], v26\n" \
"ds_read_b128 v[72:75], v25 offset:20480\nv_add_f32 v41, v32, v34\nv_smfmac_f32_16x16x64_bf16 v[64:67], v[36:39], v[192:199], v26\nds_read_b128 v[76:79], v25 offset:21504\n" \
"v_add_f32 v42, v101, v103\nv_smfmac_f32_16x16x64_bf16 v[48:51], v[96:99], v[120:127], v26\nds_read_b128 v[36:39], v24 offset:19456\nv_add_f32 v43, v33, v35\n" \
"v_smfmac_f32_16x16x64_bf16 v[64:67], v[96:99], v[224:231], v26\nds_read_b128 v[32:35], v24 offset:50176\nv_cvt_pk_bf16_f32 v62, v40, v41\nv_smfmac_f32_16x16x64_bf16 v[48:51], v[84:87], v[152:159], v26\n" \
"ds_read_b128 v[80:83], v25 offset:10240\nv_cvt_pk_bf16_f32 v63, v42, v43\nv_smfmac_f32_16x16x64_bf16 v[64:67], v[84:87], v[248:255], v26\nds_write2_b32 v27, v62, v63 offset0:0 offset1:4\n" \
"v_smfmac_f32_16x16x64_bf16 v[48:51], v[92:95], v[144:151], v26\ns_waitcnt lgkmcnt(5)\nds_read_b128 v[84:87], v25 offset:11264\nv_smfmac_f32_16x16x64_bf16 v[64:67], v[92:95], v[240:247], v26\n" \
"ds_read_b128 v[40:43], v24 offset:53248\nv_cmp_eq_u32 vcc, -1, v31\nv_smfmac_f32_16x16x64_bf16 v[48:51], v[56:59], v[16:23], v26\nv_smfmac_f32_16x16x64_bf16 v[64:67], v[56:59], v[176:183], v26\n" \
"ds_read_b128 v[92:95], v24 offset:23552\nv_smfmac_f32_16x16x64_bf16 v[48:51], v[52:55], v[128:135], v26\nds_read_b128 v[96:99], v25 offset:12288\nv_smfmac_f32_16x16x64_bf16 v[64:67], v[52:55], v[104:111], v26\n" \
"s_waitcnt lgkmcnt(4)\nds_read_b128 v[100:103], v25 offset:13312\nv_smfmac_f32_16x16x64_bf16 v[48:51], v[68:71], v[136:143], v26\ns_barrier\n" \
"v_smfmac_f32_16x16x64_bf16 v[64:67], v[68:71], v[72:79], v26\nds_read_b128 v[52:55], v25 offset:16384\nv_mov_b32 v63, s43\nv_smfmac_f32_16x16x64_bf16 v[48:51], v[36:39], a[224:231], v26\n" \
"ds_read_b128 v[56:59], v25 offset:17408\nv_cndmask_b32 v76, v63, 0, s[16:17]\nv_smfmac_f32_16x16x64_bf16 v[64:67], v[36:39], v[184:191], v26\ns_waitcnt lgkmcnt(4)\n" \
"ds_read_b128 v[36:39], v24 offset:22528\nv_smfmac_f32_16x16x64_bf16 v[48:51], v[44:47], v[80:87], v26\nds_read_b128 v[68:71], v25 offset:8192\nv_cndmask_b32 v77, v63, 0, s[18:19]\n" \
"v_smfmac_f32_16x16x64_bf16 v[64:67], v[44:47], v[208:215], v26\nds_read_b128 v[72:75], v25 offset:9216\nv_cndmask_b32 v78, v63, 0, s[20:21]\nv_smfmac_f32_16x16x64_bf16 v[48:51], v[32:35], v[112:119], v26\n" \
"ds_read_b128 v[44:47], v24 offset:16384\nv_cndmask_b32 v79, v63, 0, s[22:23]\nv_smfmac_f32_16x16x64_bf16 v[64:67], v[32:35], v[216:223], v26\nds_read_b128 v[80:83], v60 offset:0\n" \
"v_mov_b32 v62, 0xff61b1e6\nv_smfmac_f32_16x16x64_bf16 v[48:51], v[88:91], a[216:223], v26\nds_read_b128 v[32:35], v24 offset:57344\nv_cndmask_b32 v84, v62, v63, s[16:17]\n" \
"v_smfmac_f32_16x16x64_bf16 v[64:67], v[88:91], v[168:175], v26\ns_waitcnt lgkmcnt(6)\nds_read_b128 v[88:91], v24 offset:61440\nv_smfmac_f32_16x16x64_bf16 v[48:51], v[40:43], v[96:103], v26\n" \
"ds_read_b128 v[104:107], v60 offset:1024\nv_cndmask_b32 v85, v62, v63, s[18:19]\nv_smfmac_f32_16x16x64_bf16 v[64:67], v[40:43], v[232:239], v26\nds_read_b128 v[96:99], v24 offset:58368\n" \
"v_cndmask_b32 v86, v62, v63, s[20:21]\nv_smfmac_f32_16x16x64_bf16 v[48:51], v[92:95], a[248:255], v26\nds_read_b128 v[40:43], v24 offset:62464\nv_cndmask_b32 v87, v62, v63, s[22:23]\n" \
"v_smfmac_f32_16x16x64_bf16 v[64:67], v[92:95], v[52:59], v26\ns_waitcnt lgkmcnt(5)\nds_read_b128 v[56:59], v60 offset:2048\nv_smfmac_f32_16x16x64_bf16 v[48:51], v[36:39], v[68:75], v26\n" \
"ds_read_b128 v[52:55], v24 offset:59392\nv_smfmac_f32_16x16x64_bf16 v[64:67], v[36:39], v[200:207], v26\nds_read_b128 v[68:71], v24 offset:63488\nv_smfmac_f32_16x16x64_bf16 v[48:51], v[44:47], a[208:215], v26\n" \
"ds_read_b128 v[36:39], v60 offset:3072\nds_read_b128 v[60:63], v24 offset:60416\nv_smfmac_f32_16x16x64_bf16 v[64:67], v[44:47], v[160:167], v26\ns_waitcnt lgkmcnt(6)\n" \
"ds_read_b128 v[44:47], v24 offset:64512\nv_mfma_f32_16x16x32_bf16 v[76:79], v[32:35], v[80:83], v[76:79]\nds_read_b128 v[72:75], v24 offset:0\nv_add_f32 v32, v48, v50\n" \
"v_add_f32 v34, v49, v51\nv_mfma_f32_16x16x32_bf16 v[84:87], v[88:91], v[80:83], v[84:87]\ns_waitcnt lgkmcnt(6)\nv_add_f32 v33, v64, v66\n" \
"v_add_f32 v35, v65, v67\nv_cvt_pk_bf16_f32 v48, v32, v33\nv_cvt_pk_bf16_f32 v49, v34, v35\nv_mfma_f32_16x16x32_bf16 v[76:79], v[96:99], v[104:107], v[76:79]\n" \
"s_waitcnt lgkmcnt(4)\nds_write2_b32 v27, v48, v49 offset0:128 offset1:132\nv_mfma_f32_16x16x32_bf16 v[84:87], v[40:43], v[104:107], v[84:87]\nds_read_b128 v[32:35], v24 offset:32768\n" \
"v_mfma_f32_16x16x32_bf16 v[76:79], v[52:55], v[56:59], v[76:79]\ns_waitcnt lgkmcnt(4)\ns_cmp_eq_u64 vcc, 0\ns_cselect_b32 s14, s10, 0\n" \
"v_mfma_f32_16x16x32_bf16 v[84:87], v[68:71], v[56:59], v[84:87]\ns_cmp_eq_u32 s51, 0\ns_cbranch_scc1 4f\n5:\n" \
"v_mfma_f32_16x16x32_bf16 v[76:79], v[60:63], v[36:39], v[76:79]\ns_waitcnt lgkmcnt(1)\ns_barrier\nds_read_b128 v[40:43], v28 offset:512\n" \
"ds_read_b128 v[48:51], v28 offset:1536\nv_mfma_f32_16x16x32_bf16 v[84:87], v[44:47], v[36:39], v[84:87]\nds_read_b128 v[64:67], v28 offset:2560\nds_read_b128 v[88:91], v28 offset:3584\n" \
"v_mfma_f32_16x16x32_bf16 v[44:47], v[0:3], v[72:75], 0\nds_read_b128 v[92:95], v24 offset:1024\nv_max3_f32 v31, v76, v77, v78\nv_max3_f32 v31, v31, v79, s47\n" \
"v_mfma_f32_16x16x32_bf16 v[52:55], a[96:99], v[72:75], 0\nds_read_b128 v[96:99], v24 offset:33792\nv_max3_f32 v100, v84, v85, v86\nv_max3_f32 v101, v31, v100, v87\n" \
"v_max3_f32 v31, v31, v100, v87\nv_mfma_f32_16x16x32_bf16 v[60:63], a[208:211], v[72:75], 0\ns_waitcnt lgkmcnt(6)\nv_mul_f32 v100, s46, v31\n" \
"v_fma_f32 v76, v76, s46, -v100\nv_exp_f32 v76, v76\nv_mfma_f32_16x16x32_bf16 v[68:71], v[160:163], v[72:75], 0\ns_waitcnt lgkmcnt(4)\n" \
"v_fma_f32 v78, v78, s46, -v100\nv_exp_f32 v78, v78\nv_fma_f32 v77, v77, s46, -v100\nv_fma_f32 v79, v79, s46, -v100\n" \
"v_mfma_f32_16x16x32_bf16 v[108:111], v[40:43], v[80:83], 0\nv_exp_f32 v77, v77\nv_fma_f32 v84, v84, s46, -v100\nv_fma_f32 v86, v86, s46, -v100\n" \
"v_fma_f32 v85, v85, s46, -v100\nv_mfma_f32_16x16x32_bf16 v[108:111], v[48:51], v[104:107], v[108:111]\ns_waitcnt lgkmcnt(0)\nv_fma_f32 v87, v87, s46, -v100\n" \
"v_cndmask_b32 v40, v76, v78, s[24:25]\nv_mfma_f32_16x16x32_bf16 v[108:111], v[64:67], v[56:59], v[108:111]\nv_exp_f32 v79, v79\nv_mfma_f32_16x16x32_bf16 v[108:111], v[88:91], v[36:39], v[108:111]\n" \
"v_exp_f32 v84, v84\nv_mfma_f32_16x16x32_bf16 v[48:51], v[0:3], v[32:35], 0\nv_exp_f32 v86, v86\nv_exp_f32 v85, v85\n" \
"v_mfma_f32_16x16x32_bf16 v[56:59], a[96:99], v[32:35], 0\nv_exp_f32 v87, v87\nv_cndmask_b32 v41, v77, v79, s[24:25]\nv_cndmask_b32 v42, v84, v86, s[24:25]\n" \
"v_mfma_f32_16x16x32_bf16 v[64:67], a[208:211], v[32:35], 0\nv_cndmask_b32 v43, v85, v87, s[24:25]\nv_permlane32_swap_b32 v31, v101\nv_mfma_f32_16x16x32_bf16 v[72:75], v[160:163], v[32:35], 0\n" \
"v_max3_f32 v36, v108, v109, v110\nv_max3_f32 v31, v31, v101, v111\nv_max_f32 v37, v36, v31\nv_max_f32 v36, v36, v31\n" \
"v_mfma_f32_16x16x32_bf16 v[44:47], v[4:7], v[92:95], v[44:47]\nds_read_b128 v[32:35], v24 offset:2048\nv_permlane16_swap_b32 v36, v37\nv_mfma_f32_16x16x32_bf16 v[48:51], v[4:7], v[96:99], v[48:51]\n" \
"ds_read_b128 v[80:83], v24 offset:34816\nv_max_f32 v36, v36, v37\nv_mfma_f32_16x16x32_bf16 v[52:55], a[100:103], v[92:95], v[52:55]\nv_cndmask_b32 v38, v108, v110, s[26:27]\n" \
"v_fma_f32 v100, v36, -s46, v100\nv_mul_f32 v36, s46, v36\nv_cndmask_b32 v39, v109, v111, s[26:27]\nv_fma_f32 v38, v38, s46, -v36\n" \
"v_mfma_f32_16x16x32_bf16 v[56:59], a[100:103], v[96:99], v[56:59]\nv_add_f32 v88, v76, v78\nv_add_f32 v89, v77, v79\nv_fma_f32 v39, v39, s46, -v36\n" \
"v_add_f32 v90, v84, v86\nv_add_f32 v88, v88, v89\nv_mfma_f32_16x16x32_bf16 v[60:63], a[212:215], v[92:95], v[60:63]\nv_exp_f32 v100, v100\n" \
"v_exp_f32 v38, v38\nv_add_f32 v91, v85, v87\nv_mfma_f32_16x16x32_bf16 v[64:67], a[212:215], v[96:99], v[64:67]\nv_add_f32 v88, v88, v90\n" \
"v_exp_f32 v39, v39\nv_add_f32 v88, v88, v91\nv_fma_f32 v88, v88, v100, v38\nv_mfma_f32_16x16x32_bf16 v[68:71], v[164:167], v[92:95], v[68:71]\n" \
"s_waitcnt lgkmcnt(0)\nv_add_f32 v89, v88, v39\nv_add_f32 v88, v39, v88\nv_mfma_f32_16x16x32_bf16 v[72:75], v[164:167], v[96:99], v[72:75]\n" \
"ds_read_b128 v[92:95], v24 offset:3072\nv_permlane16_swap_b32 v88, v89\nv_add_f32 v90, v88, v89\nv_add_f32 v88, v89, v88\n" \
"v_mfma_f32_16x16x32_bf16 v[44:47], a[0:3], v[32:35], v[44:47]\nds_read_b128 v[96:99], v24 offset:35840\nv_permlane32_swap_b32 v88, v90\nv_mfma_f32_16x16x32_bf16 v[48:51], a[0:3], v[80:83], v[48:51]\n" \
"v_add_f32 v88, v88, v90\nv_mfma_f32_16x16x32_bf16 v[52:55], v[8:11], v[32:35], v[52:55]\nv_rcp_f32 v88, v88\nv_mfma_f32_16x16x32_bf16 v[56:59], v[8:11], v[80:83], v[56:59]\n" \
"v_mul_f32 v100, v100, v88\nv_mul_f32 v40, v40, v100\nv_mul_f32 v41, v41, v100\nv_mul_f32 v38, v38, v88\n" \
"v_mul_f32 v39, v39, v88\nv_mfma_f32_16x16x32_bf16 v[60:63], a[216:219], v[32:35], v[60:63]\nv_mul_f32 v42, v42, v100\nv_mul_f32 v43, v43, v100\n" \
"v_cvt_pk_bf16_f32 v36, v40, v41\nv_cvt_pk_bf16_f32 v31, v38, v39\nv_cvt_pk_bf16_f32 v37, v42, v43\nv_mfma_f32_16x16x32_bf16 v[64:67], a[216:219], v[80:83], v[64:67]\n" \
"v_and_or_b32 v36, v36, -2, s95\nv_and_or_b32 v31, v31, -2, s95\nv_mfma_f32_16x16x32_bf16 v[68:71], v[168:171], v[32:35], v[68:71]\ns_waitcnt lgkmcnt(0)\n" \
"buffer_store_dwordx2 v[36:37], v29, s[8:11], s99 offen offset:0 sc1\nbuffer_store_dword v31, v30, s[8:11], s96 offen offset:1536 sc1\nv_mfma_f32_16x16x32_bf16 v[72:75], v[168:171], v[80:83], v[72:75]\nds_read_b128 v[88:91], v24 offset:4096\n" \
"v_mul_f32 v76, v76, v100\nv_mfma_f32_16x16x32_bf16 v[44:47], a[4:7], v[92:95], v[44:47]\nds_read_b128 v[32:35], v24 offset:36864\nv_mul_f32 v77, v77, v100\n" \
"v_mfma_f32_16x16x32_bf16 v[48:51], a[4:7], v[96:99], v[48:51]\nv_mul_f32 v78, v78, v100\nv_mul_f32 v79, v79, v100\nv_mfma_f32_16x16x32_bf16 v[52:55], v[12:15], v[92:95], v[52:55]\n" \
"v_cvt_pk_bf16_f32 v80, v76, v77\nv_cvt_pk_bf16_f32 v81, v78, v79\nv_mfma_f32_16x16x32_bf16 v[56:59], v[12:15], v[96:99], v[56:59]\ns_add_u32 m0, s32, 0x4000\n" \
"v_and_or_b32 v80, v80, -2, s95\nv_mul_f32 v84, v84, v100\nv_mfma_f32_16x16x32_bf16 v[60:63], a[220:223], v[92:95], v[60:63]\nbuffer_store_dwordx2 v[80:81], v29, s[8:11], s97 offen offset:1024 sc1\n" \
"v_mul_f32 v85, v85, v100\nv_mfma_f32_16x16x32_bf16 v[64:67], a[220:223], v[96:99], v[64:67]\ns_add_u32 s99, s31, 0x4000\nv_mul_f32 v86, v86, v100\n" \
"v_mul_f32 v87, v87, v100\nv_mfma_f32_16x16x32_bf16 v[68:71], v[172:175], v[92:95], v[68:71]\ns_waitcnt lgkmcnt(0)\nv_cvt_pk_bf16_f32 v82, v84, v85\n" \
"v_cvt_pk_bf16_f32 v83, v86, v87\nv_mfma_f32_16x16x32_bf16 v[72:75], v[172:175], v[96:99], v[72:75]\nds_read_b128 v[36:39], v24 offset:5120\nv_mfma_f32_16x16x32_bf16 v[44:47], a[8:11], v[88:91], v[44:47]\n" \
"ds_read_b128 v[40:43], v24 offset:37888\nv_mfma_f32_16x16x32_bf16 v[48:51], a[8:11], v[32:35], v[48:51]\nbuffer_store_dwordx4 v[80:83], v24, s[8:11], s98 offen offset:0 sc1\nbuffer_load_dwordx4 v24, s[8:11], s99 offen offset:0 sc1 lds\n" \
"v_mfma_f32_16x16x32_bf16 v[52:55], a[104:107], v[88:91], v[52:55]\nv_mfma_f32_16x16x32_bf16 v[56:59], a[104:107], v[32:35], v[56:59]\nbuffer_load_dwordx4 v24, s[8:11], s99 offen offset:1024 sc1 lds\nv_mfma_f32_16x16x32_bf16 v[60:63], v[16:19], v[88:91], v[60:63]\n" \
"v_mfma_f32_16x16x32_bf16 v[64:67], v[16:19], v[32:35], v[64:67]\nv_mfma_f32_16x16x32_bf16 v[68:71], v[176:179], v[88:91], v[68:71]\ns_waitcnt lgkmcnt(0)\nv_mfma_f32_16x16x32_bf16 v[72:75], v[176:179], v[32:35], v[72:75]\n" \
"ds_read_b128 v[76:79], v24 offset:6144\nv_mfma_f32_16x16x32_bf16 v[44:47], a[12:15], v[36:39], v[44:47]\nds_read_b128 v[32:35], v24 offset:38912\nv_mfma_f32_16x16x32_bf16 v[48:51], a[12:15], v[40:43], v[48:51]\n" \
"buffer_load_dwordx4 v24, s[8:11], s99 offen offset:2048 sc1 lds\nv_mfma_f32_16x16x32_bf16 v[52:55], a[108:111], v[36:39], v[52:55]\nv_mfma_f32_16x16x32_bf16 v[56:59], a[108:111], v[40:43], v[56:59]\nbuffer_load_dwordx4 v24, s[8:11], s99 offen offset:3072 sc1 lds\n" \
"v_mfma_f32_16x16x32_bf16 v[60:63], v[20:23], v[36:39], v[60:63]\nv_mfma_f32_16x16x32_bf16 v[64:67], v[20:23], v[40:43], v[64:67]\nv_mfma_f32_16x16x32_bf16 v[68:71], v[180:183], v[36:39], v[68:71]\ns_waitcnt lgkmcnt(0)\n" \
"v_mfma_f32_16x16x32_bf16 v[72:75], v[180:183], v[40:43], v[72:75]\nds_read_b128 v[36:39], v24 offset:7168\nv_mfma_f32_16x16x32_bf16 v[44:47], a[16:19], v[76:79], v[44:47]\nds_read_b128 v[40:43], v24 offset:39936\n" \
"v_mfma_f32_16x16x32_bf16 v[48:51], a[16:19], v[32:35], v[48:51]\ns_add_u32 m0, s32, 0x6000\ns_add_u32 s99, s31, 0x6000\nv_mfma_f32_16x16x32_bf16 v[52:55], a[112:115], v[76:79], v[52:55]\n" \
"v_mfma_f32_16x16x32_bf16 v[56:59], a[112:115], v[32:35], v[56:59]\nv_mfma_f32_16x16x32_bf16 v[60:63], a[224:227], v[76:79], v[60:63]\nds_read_b128 v[80:83], v25 offset:0\nv_mfma_f32_16x16x32_bf16 v[64:67], a[224:227], v[32:35], v[64:67]\n" \
"buffer_load_dwordx4 v24, s[8:11], s99 offen offset:0 sc1 lds\nv_mfma_f32_16x16x32_bf16 v[68:71], v[184:187], v[76:79], v[68:71]\ns_waitcnt lgkmcnt(1)\nv_mfma_f32_16x16x32_bf16 v[72:75], v[184:187], v[32:35], v[72:75]\n" \
"ds_read_b128 v[76:79], v24 offset:8192\nv_mfma_f32_16x16x32_bf16 v[44:47], a[20:23], v[36:39], v[44:47]\nds_read_b128 v[32:35], v24 offset:40960\nv_mfma_f32_16x16x32_bf16 v[48:51], a[20:23], v[40:43], v[48:51]\n" \
"buffer_load_dwordx4 v24, s[8:11], s99 offen offset:1024 sc1 lds\nv_mfma_f32_16x16x32_bf16 v[52:55], a[116:119], v[36:39], v[52:55]\nv_mfma_f32_16x16x32_bf16 v[56:59], a[116:119], v[40:43], v[56:59]\nv_mfma_f32_16x16x32_bf16 v[60:63], a[228:231], v[36:39], v[60:63]\n" \
"ds_read_b128 v[84:87], v25 offset:1024\nv_mfma_f32_16x16x32_bf16 v[64:67], a[228:231], v[40:43], v[64:67]\nbuffer_load_dwordx4 v24, s[8:11], s99 offen offset:2048 sc1 lds\nv_mfma_f32_16x16x32_bf16 v[68:71], v[188:191], v[36:39], v[68:71]\n" \
"s_waitcnt lgkmcnt(1)\nv_mfma_f32_16x16x32_bf16 v[72:75], v[188:191], v[40:43], v[72:75]\nds_read_b128 v[36:39], v24 offset:9216\nv_mfma_f32_16x16x32_bf16 v[44:47], v[80:83], v[76:79], v[44:47]\n" \
"ds_read_b128 v[40:43], v24 offset:41984\nv_mfma_f32_16x16x32_bf16 v[48:51], v[80:83], v[32:35], v[48:51]\nbuffer_load_dwordx4 v24, s[8:11], s99 offen offset:3072 sc1 lds\nv_mfma_f32_16x16x32_bf16 v[52:55], a[120:123], v[76:79], v[52:55]\n" \
"v_mfma_f32_16x16x32_bf16 v[56:59], a[120:123], v[32:35], v[56:59]\nv_mfma_f32_16x16x32_bf16 v[60:63], a[232:235], v[76:79], v[60:63]\nv_mfma_f32_16x16x32_bf16 v[64:67], a[232:235], v[32:35], v[64:67]\nv_mfma_f32_16x16x32_bf16 v[68:71], v[192:195], v[76:79], v[68:71]\n" \
"s_waitcnt lgkmcnt(0)\nv_mfma_f32_16x16x32_bf16 v[72:75], v[192:195], v[32:35], v[72:75]\nds_read_b128 v[76:79], v24 offset:10240\nv_mfma_f32_16x16x32_bf16 v[44:47], v[84:87], v[36:39], v[44:47]\n" \
"ds_read_b128 v[32:35], v24 offset:43008\nv_mfma_f32_16x16x32_bf16 v[48:51], v[84:87], v[40:43], v[48:51]\nv_mfma_f32_16x16x32_bf16 v[52:55], a[124:127], v[36:39], v[52:55]\nds_read_b128 v[80:83], v25 offset:14336\n" \
"v_mfma_f32_16x16x32_bf16 v[56:59], a[124:127], v[40:43], v[56:59]\nv_mfma_f32_16x16x32_bf16 v[60:63], a[236:239], v[36:39], v[60:63]\nv_mfma_f32_16x16x32_bf16 v[64:67], a[236:239], v[40:43], v[64:67]\nv_mfma_f32_16x16x32_bf16 v[68:71], v[196:199], v[36:39], v[68:71]\n" \
"s_waitcnt lgkmcnt(1)\nv_mfma_f32_16x16x32_bf16 v[72:75], v[196:199], v[40:43], v[72:75]\nds_read_b128 v[36:39], v24 offset:11264\nv_mfma_f32_16x16x32_bf16 v[44:47], a[24:27], v[76:79], v[44:47]\n" \
"ds_read_b128 v[40:43], v24 offset:44032\nv_mfma_f32_16x16x32_bf16 v[48:51], a[24:27], v[32:35], v[48:51]\nv_mfma_f32_16x16x32_bf16 v[52:55], a[128:131], v[76:79], v[52:55]\nds_read_b128 v[84:87], v25 offset:15360\n" \
"v_mfma_f32_16x16x32_bf16 v[56:59], a[128:131], v[32:35], v[56:59]\nv_mfma_f32_16x16x32_bf16 v[60:63], a[240:243], v[76:79], v[60:63]\ns_waitcnt lgkmcnt(3)\nv_mfma_f32_16x16x32_bf16 v[64:67], a[240:243], v[32:35], v[64:67]\n" \
"v_mfma_f32_16x16x32_bf16 v[68:71], v[80:83], v[76:79], v[68:71]\ns_waitcnt lgkmcnt(1)\nv_mfma_f32_16x16x32_bf16 v[72:75], v[80:83], v[32:35], v[72:75]\nds_read_b128 v[76:79], v24 offset:12288\n" \
"v_mfma_f32_16x16x32_bf16 v[44:47], a[28:31], v[36:39], v[44:47]\nds_read_b128 v[32:35], v24 offset:45056\nv_mfma_f32_16x16x32_bf16 v[48:51], a[28:31], v[40:43], v[48:51]\nds_read_b128 v[80:83], v25 offset:8192\n" \
"v_mfma_f32_16x16x32_bf16 v[52:55], a[132:135], v[36:39], v[52:55]\nv_mfma_f32_16x16x32_bf16 v[56:59], a[132:135], v[40:43], v[56:59]\nv_mfma_f32_16x16x32_bf16 v[60:63], a[244:247], v[36:39], v[60:63]\ns_waitcnt lgkmcnt(3)\n" \
"v_mfma_f32_16x16x32_bf16 v[64:67], a[244:247], v[40:43], v[64:67]\nv_mfma_f32_16x16x32_bf16 v[68:71], v[84:87], v[36:39], v[68:71]\ns_waitcnt lgkmcnt(1)\nv_mfma_f32_16x16x32_bf16 v[72:75], v[84:87], v[40:43], v[72:75]\n" \
"ds_read_b128 v[36:39], v24 offset:13312\nv_mfma_f32_16x16x32_bf16 v[44:47], a[32:35], v[76:79], v[44:47]\nds_read_b128 v[40:43], v24 offset:46080\nv_mfma_f32_16x16x32_bf16 v[48:51], a[32:35], v[32:35], v[48:51]\n" \
"ds_read_b128 v[84:87], v25 offset:9216\nv_mfma_f32_16x16x32_bf16 v[52:55], a[136:139], v[76:79], v[52:55]\ns_waitcnt lgkmcnt(3)\nv_mfma_f32_16x16x32_bf16 v[56:59], a[136:139], v[32:35], v[56:59]\n" \
"v_mfma_f32_16x16x32_bf16 v[60:63], v[80:83], v[76:79], v[60:63]\nv_mfma_f32_16x16x32_bf16 v[64:67], v[80:83], v[32:35], v[64:67]\nv_mfma_f32_16x16x32_bf16 v[68:71], v[200:203], v[76:79], v[68:71]\ns_waitcnt lgkmcnt(1)\n" \
"v_mfma_f32_16x16x32_bf16 v[72:75], v[200:203], v[32:35], v[72:75]\nds_read_b128 v[76:79], v24 offset:14336\nv_mfma_f32_16x16x32_bf16 v[44:47], a[36:39], v[36:39], v[44:47]\nds_read_b128 v[32:35], v24 offset:47104\n" \
"v_mfma_f32_16x16x32_bf16 v[48:51], a[36:39], v[40:43], v[48:51]\nv_mfma_f32_16x16x32_bf16 v[52:55], a[140:143], v[36:39], v[52:55]\ns_waitcnt lgkmcnt(2)\nds_read_b128 v[80:83], v25 offset:16384\n" \
"v_mfma_f32_16x16x32_bf16 v[56:59], a[140:143], v[40:43], v[56:59]\nv_mfma_f32_16x16x32_bf16 v[60:63], v[84:87], v[36:39], v[60:63]\nv_mfma_f32_16x16x32_bf16 v[64:67], v[84:87], v[40:43], v[64:67]\nv_mfma_f32_16x16x32_bf16 v[68:71], v[204:207], v[36:39], v[68:71]\n" \
"s_waitcnt lgkmcnt(1)\nv_mfma_f32_16x16x32_bf16 v[72:75], v[204:207], v[40:43], v[72:75]\nds_read_b128 v[36:39], v24 offset:15360\nv_mfma_f32_16x16x32_bf16 v[44:47], a[40:43], v[76:79], v[44:47]\n" \
"ds_read_b128 v[40:43], v24 offset:48128\nv_mfma_f32_16x16x32_bf16 v[48:51], a[40:43], v[32:35], v[48:51]\nv_mfma_f32_16x16x32_bf16 v[52:55], a[144:147], v[76:79], v[52:55]\nds_read_b128 v[84:87], v25 offset:17408\n" \
"v_mfma_f32_16x16x32_bf16 v[56:59], a[144:147], v[32:35], v[56:59]\nv_mfma_f32_16x16x32_bf16 v[60:63], a[248:251], v[76:79], v[60:63]\ns_waitcnt lgkmcnt(3)\nv_mfma_f32_16x16x32_bf16 v[64:67], a[248:251], v[32:35], v[64:67]\n" \
"s_waitcnt vmcnt(4) lgkmcnt(1)\ns_barrier\nv_mfma_f32_16x16x32_bf16 v[68:71], v[80:83], v[76:79], v[68:71]\ns_add_u32 m0, s32, 0\n" \
"s_add_u32 s99, s31, 0x10000\nv_mfma_f32_16x16x32_bf16 v[72:75], v[80:83], v[32:35], v[72:75]\nds_read_b128 v[76:79], v24 offset:16384\nv_mfma_f32_16x16x32_bf16 v[44:47], a[44:47], v[36:39], v[44:47]\n" \
"ds_read_b128 v[32:35], v24 offset:49152\nv_mfma_f32_16x16x32_bf16 v[48:51], a[44:47], v[40:43], v[48:51]\nds_read_b128 v[80:83], v25 offset:10240\nv_mfma_f32_16x16x32_bf16 v[52:55], a[148:151], v[36:39], v[52:55]\n" \
"buffer_load_dwordx4 v24, s[12:15], s99 offen offset:0 sc1 lds\nv_mfma_f32_16x16x32_bf16 v[56:59], a[148:151], v[40:43], v[56:59]\nv_mfma_f32_16x16x32_bf16 v[60:63], a[252:255], v[36:39], v[60:63]\ns_waitcnt lgkmcnt(3)\n" \
"v_mfma_f32_16x16x32_bf16 v[64:67], a[252:255], v[40:43], v[64:67]\nbuffer_load_dwordx4 v24, s[12:15], s99 offen offset:1024 sc1 lds\nv_mfma_f32_16x16x32_bf16 v[68:71], v[84:87], v[36:39], v[68:71]\ns_waitcnt lgkmcnt(1)\n" \
"v_mfma_f32_16x16x32_bf16 v[72:75], v[84:87], v[40:43], v[72:75]\nds_read_b128 v[36:39], v24 offset:17408\nv_mfma_f32_16x16x32_bf16 v[44:47], a[48:51], v[76:79], v[44:47]\nds_read_b128 v[40:43], v24 offset:50176\n" \
"v_mfma_f32_16x16x32_bf16 v[48:51], a[48:51], v[32:35], v[48:51]\nds_read_b128 v[84:87], v25 offset:11264\nv_mfma_f32_16x16x32_bf16 v[52:55], a[152:155], v[76:79], v[52:55]\ns_waitcnt lgkmcnt(3)\n" \
"buffer_load_dwordx4 v24, s[12:15], s99 offen offset:2048 sc1 lds\nv_mfma_f32_16x16x32_bf16 v[56:59], a[152:155], v[32:35], v[56:59]\nv_mfma_f32_16x16x32_bf16 v[60:63], v[80:83], v[76:79], v[60:63]\nv_mfma_f32_16x16x32_bf16 v[64:67], v[80:83], v[32:35], v[64:67]\n" \
"buffer_load_dwordx4 v24, s[12:15], s99 offen offset:3072 sc1 lds\nv_mfma_f32_16x16x32_bf16 v[68:71], v[208:211], v[76:79], v[68:71]\ns_waitcnt lgkmcnt(1)\nds_read_b128 v[76:79], v25 offset:6144\n" \
"v_mfma_f32_16x16x32_bf16 v[72:75], v[208:211], v[32:35], v[72:75]\nds_read_b128 v[80:83], v24 offset:18432\nv_mfma_f32_16x16x32_bf16 v[44:47], a[52:55], v[36:39], v[44:47]\nds_read_b128 v[32:35], v24 offset:51200\n" \
"s_add_u32 m0, s32, 0x2000\nv_mfma_f32_16x16x32_bf16 v[48:51], a[52:55], v[40:43], v[48:51]\ns_add_u32 s99, s31, 0x12000\nv_mfma_f32_16x16x32_bf16 v[52:55], a[156:159], v[36:39], v[52:55]\n" \
"s_waitcnt lgkmcnt(3)\nv_mfma_f32_16x16x32_bf16 v[56:59], a[156:159], v[40:43], v[56:59]\nv_mfma_f32_16x16x32_bf16 v[60:63], v[84:87], v[36:39], v[60:63]\nbuffer_load_dwordx4 v24, s[12:15], s99 offen offset:0 sc1 lds\n" \
"v_mfma_f32_16x16x32_bf16 v[64:67], v[84:87], v[40:43], v[64:67]\nv_mfma_f32_16x16x32_bf16 v[68:71], v[212:215], v[36:39], v[68:71]\ns_waitcnt lgkmcnt(0)\nds_read_b128 v[36:39], v25 offset:7168\n" \
"v_mfma_f32_16x16x32_bf16 v[72:75], v[212:215], v[40:43], v[72:75]\nds_read_b128 v[84:87], v24 offset:19456\nv_mfma_f32_16x16x32_bf16 v[44:47], a[56:59], v[80:83], v[44:47]\nds_read_b128 v[40:43], v24 offset:52224\n" \
"v_mfma_f32_16x16x32_bf16 v[48:51], a[56:59], v[32:35], v[48:51]\nbuffer_load_dwordx4 v24, s[12:15], s99 offen offset:1024 sc1 lds\nv_mfma_f32_16x16x32_bf16 v[52:55], v[76:79], v[80:83], v[52:55]\nv_mfma_f32_16x16x32_bf16 v[56:59], v[76:79], v[32:35], v[56:59]\n" \
"v_mfma_f32_16x16x32_bf16 v[60:63], v[112:115], v[80:83], v[60:63]\nds_read_b128 v[76:79], v25 offset:2048\nv_mfma_f32_16x16x32_bf16 v[64:67], v[112:115], v[32:35], v[64:67]\nbuffer_load_dwordx4 v24, s[12:15], s99 offen offset:2048 sc1 lds\n" \
"v_mfma_f32_16x16x32_bf16 v[68:71], v[216:219], v[80:83], v[68:71]\ns_waitcnt lgkmcnt(1)\nv_mfma_f32_16x16x32_bf16 v[72:75], v[216:219], v[32:35], v[72:75]\nds_read_b128 v[80:83], v24 offset:20480\n" \
"v_mfma_f32_16x16x32_bf16 v[44:47], a[60:63], v[84:87], v[44:47]\nds_read_b128 v[32:35], v24 offset:53248\nv_mfma_f32_16x16x32_bf16 v[48:51], a[60:63], v[40:43], v[48:51]\nbuffer_load_dwordx4 v24, s[12:15], s99 offen offset:3072 sc1 lds\n" \
"v_mfma_f32_16x16x32_bf16 v[52:55], v[36:39], v[84:87], v[52:55]\nv_mfma_f32_16x16x32_bf16 v[56:59], v[36:39], v[40:43], v[56:59]\nv_mfma_f32_16x16x32_bf16 v[60:63], v[116:119], v[84:87], v[60:63]\nv_mfma_f32_16x16x32_bf16 v[64:67], v[116:119], v[40:43], v[64:67]\n" \
"v_mfma_f32_16x16x32_bf16 v[68:71], v[220:223], v[84:87], v[68:71]\nv_mfma_f32_16x16x32_bf16 v[72:75], v[220:223], v[40:43], v[72:75]\ns_branch 1b\n2:\n" \
"s_add_u32 s52, s52, 1\ns_cmp_lt_u32 s52, 0x40000\ns_cbranch_scc0 9f\nv_cmp_eq_u32 vcc, -1, v40\n" \
"s_cbranch_vccz 6f\nbuffer_load_dwordx4 v24, s[8:11], s94 offen offset:0 sc1 lds\n6:\nv_cmp_eq_u32 vcc, -1, v76\n" \
"s_cbranch_vccz 6f\nbuffer_load_dwordx4 v24, s[8:11], s94 offen offset:1024 sc1 lds\n6:\nv_cmp_eq_u32 vcc, -1, v84\n" \
"s_cbranch_vccz 6f\nbuffer_load_dwordx4 v24, s[8:11], s94 offen offset:2048 sc1 lds\n6:\nv_cmp_eq_u32 vcc, -1, v88\n" \
"s_cbranch_vccz 6f\nbuffer_load_dwordx4 v24, s[8:11], s94 offen offset:3072 sc1 lds\n6:\ns_waitcnt vmcnt(0)\n" \
"ds_read_b128 v[40:43], v31 offset:16384\nds_read_b128 v[76:79], v31 offset:17408\nds_read_b128 v[84:87], v31 offset:18432\nds_read_b128 v[88:91], v31 offset:19456\n" \
"s_waitcnt lgkmcnt(0)\nv_max3_u32 v96, v40, v76, v84\nv_max_u32 v96, v96, v88\nv_cmp_eq_u32 vcc, -1, v96\n" \
"s_cbranch_vccnz 2b\ns_branch 3b\n4:\ns_cmp_gt_u32 s30, 64\n" \
"s_cbranch_scc1 10f\ns_cmp_eq_u32 s30, 64\ns_cbranch_scc1 5b\nv_bfe_u32 v40, v24, 6, 3\n" \
"v_lshlrev_b32 v40, 2, v40\ns_lshl_b32 s94, s30, 5\ns_add_u32 s94, s94, s48\ns_cmp_lg_u32 s58, 0\n" \
"s_cbranch_scc1 8f\n7:\nbuffer_load_dword v41, v40, s[8:11], s94 offen sc1\ns_waitcnt vmcnt(0)\n" \
"v_cmp_eq_u32 vcc, -1, v41\ns_cbranch_vccz 8f\ns_add_u32 s52, s52, 1\ns_cmp_lt_u32 s52, 0x40000\n" \
"s_cbranch_scc0 9f\ns_sleep 8\ns_branch 7b\n8:\n" \
"s_mov_b32 m0, s53\ns_add_u32 s94, s30, 1\ns_min_u32 s94, s94, 63\ns_lshl_b32 s94, s94, 5\n" \
"s_add_u32 s94, s94, s48\nbuffer_load_dword v40, s[8:11], s94 offen offset:0 sc1 lds\ns_cmp_lg_u32 s58, 0\ns_mov_b32 s58, 0\n" \
"s_cbranch_scc1 6f\ns_add_u32 m0, s32, 0\ns_add_u32 s94, s31, 0\nbuffer_load_dwordx4 v24, s[8:11], s94 offen offset:0 sc1 lds\n" \
"buffer_load_dwordx4 v24, s[8:11], s94 offen offset:1024 sc1 lds\nbuffer_load_dwordx4 v24, s[8:11], s94 offen offset:2048 sc1 lds\nbuffer_load_dwordx4 v24, s[8:11], s94 offen offset:3072 sc1 lds\ns_add_u32 m0, s32, 0x2000\n" \
"s_add_u32 s94, s31, 0x2000\nbuffer_load_dwordx4 v24, s[8:11], s94 offen offset:0 sc1 lds\nbuffer_load_dwordx4 v24, s[8:11], s94 offen offset:1024 sc1 lds\nbuffer_load_dwordx4 v24, s[8:11], s94 offen offset:2048 sc1 lds\n" \
"buffer_load_dwordx4 v24, s[8:11], s94 offen offset:3072 sc1 lds\n6:\nv_lshrrev_b32 v40, 2, v24\nv_add_u32 v40, 0x11000, v40\n" \
"s_waitcnt vmcnt(0) lgkmcnt(0)\ns_barrier\nds_read_b32 v41, v40 offset:0\nds_read_b128 v[72:75], v24 offset:0\n" \
"ds_read_b128 v[32:35], v24 offset:32768\ns_waitcnt lgkmcnt(0)\nv_cmp_eq_u32 vcc, -1, v41\ns_cmp_eq_u64 vcc, 0\n" \
"s_cselect_b32 s14, s10, 0\ns_branch 5b\n10:\ns_waitcnt vmcnt(0) lgkmcnt(0)\n" \
"s_cmp_lt_i32 s6, 0\ns_cbranch_scc1 9f\ns_mov_b64 s[0:1], s[4:5]\ns_mov_b32 s2, s6\n" \
"v_lshrrev_b32 v0, 4, v24\nv_add_u32 v0, s7, v0\ns_barrier\ns_getpc_b64 s[8:9]\n" \
"11:\ns_add_u32 s8, s8, _Z5mega64Args-11b\ns_addc_u32 s9, s9, -1\ns_setpc_b64 s[8:9]\n" \
"9:\ns_waitcnt vmcnt(0)\ns_endpgm\n"
#define B_LDS 0x44840c0202a00110ull
#define B_VAR 0x0000000000010001ull
#define B_LV 8192
#define B_LOOP \
"v_mov_b32 v33, v20\nv_cmp_eq_u32 vcc, -1, v33\nv_bfe_u32 v32, v16, 4, 3\nv_lshlrev_b32 v32, 2, v32\n" \
"s_nop 2\ns_cbranch_vccz 6f\n7:\ns_add_u32 s30, s30, 1\n" \
"s_cmp_lt_u32 s30, 0x40000\ns_cbranch_scc0 9f\ns_sleep 8\nbuffer_load_dword v33, v32, s[8:11], s26 offen sc1\n" \
"s_waitcnt vmcnt(0)\nv_cmp_eq_u32 vcc, -1, v33\ns_nop 4\ns_cbranch_vccnz 7b\n" \
"s_add_u32 m0, s22, 0\ns_add_u32 s43, s21, 0\nbuffer_load_dwordx4 v16, s[8:11], s43 offen offset:0 sc1 lds\nbuffer_load_dwordx4 v16, s[8:11], s43 offen offset:1024 sc1 lds\n" \
"buffer_load_dwordx4 v16, s[8:11], s43 offen offset:2048 sc1 lds\nbuffer_load_dwordx4 v16, s[8:11], s43 offen offset:3072 sc1 lds\ns_add_u32 m0, s22, 0x2000\ns_add_u32 s43, s21, 0x2000\n" \
"buffer_load_dwordx4 v16, s[8:11], s43 offen offset:0 sc1 lds\nbuffer_load_dwordx4 v16, s[8:11], s43 offen offset:1024 sc1 lds\nbuffer_load_dwordx4 v16, s[8:11], s43 offen offset:2048 sc1 lds\nbuffer_load_dwordx4 v16, s[8:11], s43 offen offset:3072 sc1 lds\n" \
"s_add_u32 m0, s22, 0x4000\ns_add_u32 s43, s21, 0x4000\nbuffer_load_dwordx4 v16, s[8:11], s43 offen offset:0 sc1 lds\nbuffer_load_dwordx4 v16, s[8:11], s43 offen offset:1024 sc1 lds\n" \
"buffer_load_dwordx4 v16, s[8:11], s43 offen offset:2048 sc1 lds\nbuffer_load_dwordx4 v16, s[8:11], s43 offen offset:3072 sc1 lds\n6:\ns_waitcnt vmcnt(0)\n" \
"s_barrier\nds_read_b128 v[48:51], v16 offset:0\nds_read_b128 v[64:67], v16 offset:32768\nds_read_b128 v[76:79], v16 offset:1024\n" \
"ds_read_b128 v[80:83], v16 offset:33792\nds_read_b128 v[84:87], v16 offset:2048\nds_read_b128 v[88:91], v16 offset:34816\nds_read_b128 v[92:95], v16 offset:3072\n" \
"ds_read_b128 v[96:99], v16 offset:35840\nds_read_b128 v[28:31], v16 offset:4096\nds_read_b128 v[40:43], v16 offset:36864\nds_read_b128 v[36:39], v16 offset:5120\n" \
"ds_read_b128 v[32:35], v16 offset:37888\nds_read_b128 v[52:55], v16 offset:6144\nds_read_b128 v[24:27], v16 offset:38912\nds_read_b128 v[56:59], v16 offset:7168\n" \
"ds_read_b128 v[60:63], v16 offset:39936\ns_mov_b32 s20, -1\ns_add_u32 s21, s21, 0x10000\ns_mov_b32 s14, s10\n" \
"s_waitcnt lgkmcnt(0)\nv_mfma_f32_16x16x32_bf16 v[44:47], v[0:3], v[48:51], 0\nv_mfma_f32_16x16x32_bf16 v[68:71], v[0:3], v[64:67], 0\nv_mfma_f32_16x16x32_bf16 v[72:75], v[8:11], v[48:51], 0\n" \
"v_mfma_f32_16x16x32_bf16 v[20:23], v[8:11], v[64:67], 0\nv_mfma_f32_16x16x32_bf16 v[44:47], v[4:7], v[76:79], v[44:47]\nv_mfma_f32_16x16x32_bf16 v[68:71], v[4:7], v[80:83], v[68:71]\nv_mfma_f32_16x16x32_bf16 v[72:75], v[12:15], v[76:79], v[72:75]\n" \
"v_mfma_f32_16x16x32_bf16 v[20:23], v[12:15], v[80:83], v[20:23]\nv_mfma_f32_16x16x32_bf16 v[44:47], a[0:3], v[84:87], v[44:47]\nv_mfma_f32_16x16x32_bf16 v[68:71], a[0:3], v[88:91], v[68:71]\nv_mfma_f32_16x16x32_bf16 v[72:75], a[104:107], v[84:87], v[72:75]\n" \
"v_mfma_f32_16x16x32_bf16 v[20:23], a[104:107], v[88:91], v[20:23]\nv_mfma_f32_16x16x32_bf16 v[44:47], a[4:7], v[92:95], v[44:47]\nv_mfma_f32_16x16x32_bf16 v[68:71], a[4:7], v[96:99], v[68:71]\nv_mfma_f32_16x16x32_bf16 v[72:75], a[108:111], v[92:95], v[72:75]\n" \
"v_mfma_f32_16x16x32_bf16 v[20:23], a[108:111], v[96:99], v[20:23]\ns_branch 1f\n.p2align 6\n1:\n" \
"s_waitcnt lgkmcnt(4)\nv_mfma_f32_16x16x32_bf16 v[44:47], a[8:11], v[28:31], v[44:47]\ns_add_u32 m0, s22, 0\ns_add_u32 s43, s20, 7\n" \
"s_and_b32 s43, s43, 7\nv_mfma_f32_16x16x32_bf16 v[68:71], a[8:11], v[40:43], v[68:71]\ns_lshl_b32 s43, s43, 18\ns_add_u32 s43, s43, s23\n" \
"v_mfma_f32_16x16x32_bf16 v[72:75], a[112:115], v[28:31], v[72:75]\nds_read_b128 v[48:51], v17 offset:0\nv_mfma_f32_16x16x32_bf16 v[20:23], a[112:115], v[40:43], v[20:23]\nv_mfma_f32_16x16x32_bf16 v[44:47], a[12:15], v[36:39], v[44:47]\n" \
"s_waitcnt vmcnt(7) lgkmcnt(1)\ns_barrier\nv_mfma_f32_16x16x32_bf16 v[68:71], a[12:15], v[32:35], v[68:71]\nds_read_b128 v[28:31], v16 offset:8192\n" \
"v_mfma_f32_16x16x32_bf16 v[72:75], a[116:119], v[36:39], v[72:75]\nds_read_b128 v[40:43], v16 offset:40960\nv_mfma_f32_16x16x32_bf16 v[20:23], a[116:119], v[32:35], v[20:23]\nds_read_b128 v[36:39], v16 offset:9216\n" \
"v_mfma_f32_16x16x32_bf16 v[44:47], a[16:19], v[52:55], v[44:47]\nds_read_b128 v[32:35], v17 offset:1024\nv_mfma_f32_16x16x32_bf16 v[68:71], a[16:19], v[24:27], v[68:71]\nbuffer_load_dwordx4 v16, s[8:11], s43 offen offset:0 sc1 lds\n" \
"v_mfma_f32_16x16x32_bf16 v[72:75], a[120:123], v[52:55], v[72:75]\nds_read_b128 v[64:67], v16 offset:41984\nv_mfma_f32_16x16x32_bf16 v[20:23], a[120:123], v[24:27], v[20:23]\nds_read_b128 v[52:55], v16 offset:10240\n" \
"v_mfma_f32_16x16x32_bf16 v[44:47], a[20:23], v[56:59], v[44:47]\nbuffer_load_dwordx4 v16, s[8:11], s43 offen offset:1024 sc1 lds\nv_mfma_f32_16x16x32_bf16 v[68:71], a[20:23], v[60:63], v[68:71]\nds_read_b128 v[24:27], v16 offset:43008\n" \
"v_mfma_f32_16x16x32_bf16 v[72:75], a[124:127], v[56:59], v[72:75]\ns_waitcnt lgkmcnt(3)\nds_read_b128 v[56:59], v17 offset:4096\nv_mfma_f32_16x16x32_bf16 v[20:23], a[124:127], v[60:63], v[20:23]\n" \
"buffer_load_dwordx4 v16, s[8:11], s43 offen offset:2048 sc1 lds\nv_mfma_f32_16x16x32_bf16 v[44:47], v[48:51], v[28:31], v[44:47]\nds_read_b128 v[60:63], v16 offset:11264\nv_mfma_f32_16x16x32_bf16 v[68:71], v[48:51], v[40:43], v[68:71]\n" \
"ds_read_b128 v[76:79], v16 offset:44032\nv_mfma_f32_16x16x32_bf16 v[72:75], a[128:131], v[28:31], v[72:75]\nbuffer_load_dwordx4 v16, s[8:11], s43 offen offset:3072 sc1 lds\nv_mfma_f32_16x16x32_bf16 v[20:23], a[128:131], v[40:43], v[20:23]\n" \
"s_waitcnt lgkmcnt(3)\nds_read_b128 v[28:31], v17 offset:5120\nv_mfma_f32_16x16x32_bf16 v[44:47], v[32:35], v[36:39], v[44:47]\nds_read_b128 v[40:43], v16 offset:12288\n" \
"v_mfma_f32_16x16x32_bf16 v[68:71], v[32:35], v[64:67], v[68:71]\ns_add_u32 m0, s22, 0x6000\nds_read_b128 v[32:35], v16 offset:45056\nv_mfma_f32_16x16x32_bf16 v[72:75], a[132:135], v[36:39], v[72:75]\n" \
"s_add_u32 s42, s21, -0xa000\nv_mfma_f32_16x16x32_bf16 v[20:23], a[132:135], v[64:67], v[20:23]\nds_read_b128 v[36:39], v16 offset:13312\nv_mfma_f32_16x16x32_bf16 v[44:47], a[24:27], v[52:55], v[44:47]\n" \
"s_waitcnt lgkmcnt(4)\nds_read_b128 v[48:51], v16 offset:46080\nv_mfma_f32_16x16x32_bf16 v[68:71], a[24:27], v[24:27], v[68:71]\nbuffer_load_dwordx4 v16, s[12:15], s42 offen offset:0 sc1 lds\n" \
"v_mfma_f32_16x16x32_bf16 v[72:75], v[56:59], v[52:55], v[72:75]\nv_mfma_f32_16x16x32_bf16 v[20:23], v[56:59], v[24:27], v[20:23]\nds_read_b128 v[52:55], v16 offset:14336\nv_mfma_f32_16x16x32_bf16 v[44:47], a[28:31], v[60:63], v[44:47]\n" \
"s_waitcnt lgkmcnt(3)\nbuffer_load_dwordx4 v16, s[12:15], s42 offen offset:1024 sc1 lds\nv_mfma_f32_16x16x32_bf16 v[68:71], a[28:31], v[76:79], v[68:71]\nds_read_b128 v[24:27], v16 offset:47104\n" \
"v_mfma_f32_16x16x32_bf16 v[72:75], v[28:31], v[60:63], v[72:75]\nds_read_b128 v[56:59], v17 offset:6144\nv_mfma_f32_16x16x32_bf16 v[20:23], v[28:31], v[76:79], v[20:23]\nbuffer_load_dwordx4 v16, s[12:15], s42 offen offset:2048 sc1 lds\n" \
"v_mfma_f32_16x16x32_bf16 v[44:47], a[32:35], v[40:43], v[44:47]\nds_read_b128 v[28:31], v16 offset:15360\nv_lshlrev_b32 v60, 5, v16\nv_mfma_f32_16x16x32_bf16 v[68:71], a[32:35], v[32:35], v[68:71]\n" \
"ds_read_b128 v[62:65], v16 offset:48128\nv_and_b32 v61, 0x7000, v60\nv_mfma_f32_16x16x32_bf16 v[72:75], a[136:139], v[40:43], v[72:75]\ns_waitcnt lgkmcnt(4)\n" \
"buffer_load_dwordx4 v16, s[12:15], s42 offen offset:3072 sc1 lds\nv_mfma_f32_16x16x32_bf16 v[20:23], a[136:139], v[32:35], v[20:23]\nds_read_b128 v[40:43], v17 offset:7168\ns_add_u32 m0, s22, 0\n" \
"v_mfma_f32_16x16x32_bf16 v[44:47], a[36:39], v[36:39], v[44:47]\nds_read_b128 v[32:35], v16 offset:16384\nv_lshl_add_u32 v60, v61, 17, v60\nv_mfma_f32_16x16x32_bf16 v[68:71], a[36:39], v[48:51], v[68:71]\n" \
"ds_read_b128 v[76:79], v17 offset:2048\nv_mfma_f32_16x16x32_bf16 v[72:75], a[140:143], v[36:39], v[72:75]\nds_read_b128 v[80:83], v16 offset:49152\nv_mfma_f32_16x16x32_bf16 v[20:23], a[140:143], v[48:51], v[20:23]\n" \
"s_waitcnt lgkmcnt(4)\nds_read_b128 v[36:39], v16 offset:17408\nv_mfma_f32_16x16x32_bf16 v[44:47], a[40:43], v[52:55], v[44:47]\nds_read_b128 v[48:51], v17 offset:3072\n" \
"v_mfma_f32_16x16x32_bf16 v[68:71], a[40:43], v[24:27], v[68:71]\nds_read_b128 v[84:87], v16 offset:50176\nv_mfma_f32_16x16x32_bf16 v[72:75], v[56:59], v[52:55], v[72:75]\nv_mfma_f32_16x16x32_bf16 v[20:23], v[56:59], v[24:27], v[20:23]\n" \
"ds_read_b128 v[52:55], v16 offset:18432\nv_mfma_f32_16x16x32_bf16 v[44:47], a[44:47], v[28:31], v[44:47]\ns_waitcnt lgkmcnt(4)\nds_read_b128 v[24:27], v16 offset:51200\n" \
"v_mfma_f32_16x16x32_bf16 v[68:71], a[44:47], v[62:65], v[68:71]\nds_read_b128 v[56:59], v17 offset:8192\nv_add_u32 v61, s22, v16\nv_mfma_f32_16x16x32_bf16 v[72:75], v[40:43], v[28:31], v[72:75]\n" \
"v_mfma_f32_16x16x32_bf16 v[20:23], v[40:43], v[62:65], v[20:23]\nds_read_b128 v[28:31], v16 offset:19456\nv_mfma_f32_16x16x32_bf16 v[44:47], v[76:79], v[32:35], v[44:47]\nds_read_b128 v[40:43], v16 offset:52224\n" \
"v_mfma_f32_16x16x32_bf16 v[68:71], v[76:79], v[80:83], v[68:71]\nds_read_b128 v[62:65], v17 offset:9216\nv_mfma_f32_16x16x32_bf16 v[72:75], a[144:147], v[32:35], v[72:75]\ns_waitcnt lgkmcnt(5)\n" \
"ds_read_b128 v[92:95], v17 offset:9216\nv_mfma_f32_16x16x32_bf16 v[20:23], a[144:147], v[80:83], v[20:23]\nds_read_b128 v[32:35], v16 offset:20480\nv_mfma_f32_16x16x32_bf16 v[44:47], v[48:51], v[36:39], v[44:47]\n" \
"ds_read_b128 v[76:79], v16 offset:53248\nv_mfma_f32_16x16x32_bf16 v[68:71], v[48:51], v[84:87], v[68:71]\nds_read_b128 v[88:91], v17 offset:8192\nv_mfma_f32_16x16x32_bf16 v[72:75], a[148:151], v[36:39], v[72:75]\n" \
"s_waitcnt vmcnt(4)\nds_read_b128 v[36:39], v61 offset:0\nds_read_b128 v[80:83], v61 offset:1024\nds_read_b128 v[48:51], v61 offset:2048\n" \
"v_mfma_f32_16x16x32_bf16 v[20:23], a[148:151], v[84:87], v[20:23]\ns_waitcnt lgkmcnt(7)\nds_read_b128 v[84:87], v61 offset:3072\nv_mfma_f32_16x16x32_bf16 v[44:47], a[48:51], v[52:55], v[44:47]\n" \
"ds_read_b128 v[96:99], v17 offset:4096\nv_mfma_f32_16x16x32_bf16 v[68:71], a[48:51], v[24:27], v[68:71]\nds_read_b128 v[100:103], v17 offset:5120\nv_mov_b64 v[104:105], 0\n" \
"v_mfma_f32_16x16x32_bf16 v[72:75], v[56:59], v[52:55], v[72:75]\ns_waitcnt lgkmcnt(3)\nv_max3_u32 v52, v36, v80, v48\nv_mov_b64 v[106:107], 0\n" \
"v_mfma_f32_16x16x32_bf16 v[20:23], v[56:59], v[24:27], v[20:23]\ns_waitcnt lgkmcnt(2)\nv_max_u32 v52, v52, v84\nv_cmp_eq_u32 vcc, -1, v52\n" \
"v_mfma_f32_16x16x32_bf16 v[44:47], a[52:55], v[28:31], v[44:47]\nv_mov_b64 v[24:25], 0\nv_mov_b64 v[26:27], 0\nv_mfma_f32_16x16x32_bf16 v[68:71], a[52:55], v[40:43], v[68:71]\n" \
"s_cbranch_vccnz 2f\n3:\nv_mfma_f32_16x16x32_bf16 v[72:75], v[62:65], v[28:31], v[72:75]\ns_barrier\n" \
"ds_read_b128 v[28:31], v16 offset:2048\nv_mfma_f32_16x16x32_bf16 v[20:23], v[92:95], v[40:43], v[20:23]\ns_memrealtime s[40:41]\nds_read_b128 v[36:39], v16 offset:5120\n" \
"v_mfma_f32_16x16x32_bf16 v[44:47], a[56:59], v[32:35], v[44:47]\nds_read_b128 v[40:43], v16 offset:1024\ns_lshl_b32 s43, s20, 24\nv_mfma_f32_16x16x32_bf16 v[68:71], a[56:59], v[76:79], v[68:71]\n" \
"ds_read_b128 v[48:51], v16 offset:33792\nv_mfma_f32_16x16x32_bf16 v[72:75], a[152:155], v[32:35], v[72:75]\nv_mfma_f32_16x16x32_bf16 v[20:23], a[152:155], v[76:79], v[20:23]\ns_waitcnt lgkmcnt(3)\n" \
"ds_read_b128 v[32:35], v16 offset:3072\nv_smfmac_f32_16x16x64_bf16 v[104:107], v[28:31], a[8:15], v18\ns_waitcnt lgkmcnt(3)\nv_smfmac_f32_16x16x64_bf16 v[24:27], v[28:31], a[112:119], v18\n" \
"ds_read_b128 v[52:55], v16 offset:34816\nv_smfmac_f32_16x16x64_bf16 v[104:107], v[36:39], a[24:31], v18\ns_waitcnt lgkmcnt(3)\nds_read_b128 v[76:79], v17 offset:6144\n" \
"v_smfmac_f32_16x16x64_bf16 v[24:27], v[36:39], v[96:103], v18\nds_read_b128 v[28:31], v16 offset:36864\nv_smfmac_f32_16x16x64_bf16 v[104:107], v[40:43], a[0:7], v18\ns_waitcnt lgkmcnt(4)\n" \
"ds_read_b128 v[80:83], v17 offset:7168\nv_smfmac_f32_16x16x64_bf16 v[24:27], v[40:43], a[104:111], v18\nds_read_b128 v[36:39], v16 offset:6144\ns_add_u32 s42, s40, 0x67\n" \
"v_smfmac_f32_16x16x64_bf16 v[104:107], v[48:51], a[48:55], v18\ns_and_b32 s42, s42, 0xffffff\ns_or_b32 s42, s42, s43\nv_mov_b32 v40, s42\n" \
"v_smfmac_f32_16x16x64_bf16 v[24:27], v[48:51], v[88:95], v18\ns_waitcnt lgkmcnt(4)\nds_read_b128 v[48:51], v16 offset:7168\nv_smfmac_f32_16x16x64_bf16 v[104:107], v[32:35], a[16:23], v18\n" \
"buffer_store_dword v40, v60, s[8:11], s27 offen offset:0 sc1\nv_smfmac_f32_16x16x64_bf16 v[24:27], v[32:35], a[120:127], v18\nds_read_b128 v[56:59], v16 offset:35840\nv_smfmac_f32_16x16x64_bf16 v[104:107], v[52:55], a[56:63], v18\n" \
"s_waitcnt lgkmcnt(4)\nv_smfmac_f32_16x16x64_bf16 v[24:27], v[52:55], a[152:159], v18\nds_read_b128 v[32:35], v16 offset:0\ns_add_u32 s16, s20, 1\n" \
"v_smfmac_f32_16x16x64_bf16 v[104:107], v[28:31], a[72:79], v18\nds_read_b128 v[60:63], v17 offset:0\nv_mov_b64 v[40:41], 0\nv_smfmac_f32_16x16x64_bf16 v[24:27], v[28:31], a[168:175], v18\n" \
"s_waitcnt lgkmcnt(4)\nds_read_b128 v[28:31], v16 offset:39936\nv_smfmac_f32_16x16x64_bf16 v[104:107], v[36:39], a[32:39], v18\nds_read_b128 v[64:67], v17 offset:1024\n" \
"v_mov_b64 v[42:43], 0\nv_smfmac_f32_16x16x64_bf16 v[24:27], v[36:39], a[136:143], v18\ns_waitcnt lgkmcnt(4)\nds_read_b128 v[36:39], v16 offset:37888\n" \
"v_smfmac_f32_16x16x64_bf16 v[104:107], v[48:51], a[40:47], v18\nds_read_b128 v[84:87], v17 offset:2048\ns_and_b32 s16, s16, 7\nv_smfmac_f32_16x16x64_bf16 v[24:27], v[48:51], v[76:83], v18\n" \
"ds_read_b128 v[52:55], v16 offset:4096\ns_lshl_b32 s16, s16, 22\nv_smfmac_f32_16x16x64_bf16 v[104:107], v[56:59], a[64:71], v18\nds_read_b128 v[88:91], v17 offset:3072\n" \
"s_add_u32 s16, s16, s25\nv_smfmac_f32_16x16x64_bf16 v[24:27], v[56:59], a[160:167], v18\ns_waitcnt lgkmcnt(6)\nds_read_b128 v[48:51], v16 offset:38912\n" \
"v_smfmac_f32_16x16x64_bf16 v[104:107], v[32:35], v[0:7], v18\nds_read_b128 v[56:59], v16 offset:21504\ns_add_u32 s17, s16, 0x1000\nv_smfmac_f32_16x16x64_bf16 v[24:27], v[32:35], v[8:15], v18\n" \
"s_waitcnt lgkmcnt(6)\nds_read_b128 v[32:35], v16 offset:32768\nv_smfmac_f32_16x16x64_bf16 v[104:107], v[28:31], a[96:103], v18\nds_read_b128 v[76:79], v16 offset:54272\n" \
"s_add_u32 s18, s16, 0x4000\nv_smfmac_f32_16x16x64_bf16 v[24:27], v[28:31], a[192:199], v18\ns_waitcnt lgkmcnt(5)\nds_read_b128 v[28:31], v16 offset:22528\n" \
"v_smfmac_f32_16x16x64_bf16 v[104:107], v[36:39], a[80:87], v18\nds_read_b128 v[80:83], v16 offset:55296\nv_lshrrev_b32 v92, 16, v18\nv_smfmac_f32_16x16x64_bf16 v[24:27], v[36:39], a[176:183], v18\n" \
"ds_write_b128 v16, v[40:43] offset:45056\nv_smfmac_f32_16x16x64_bf16 v[104:107], v[52:55], v[60:67], v18\ns_add_u32 s19, s16, 0x5000\ns_and_b32 s43, s20, 2\n" \
"s_lshl_b32 s43, s43, 12\nv_smfmac_f32_16x16x64_bf16 v[24:27], v[52:55], a[128:135], v18\ns_waitcnt lgkmcnt(4)\nds_read_b128 v[36:39], v16 offset:23552\n" \
"v_smfmac_f32_16x16x64_bf16 v[104:107], v[48:51], a[88:95], v18\nds_read_b128 v[52:55], v16 offset:56320\ns_and_b32 s42, s20, 7\nv_smfmac_f32_16x16x64_bf16 v[24:27], v[48:51], a[184:191], v18\n" \
"ds_read_b128 v[60:63], v17 offset:10240\ns_lshl_b32 s42, s42, 20\nv_smfmac_f32_16x16x64_bf16 v[104:107], v[32:35], v[84:91], v18\nds_read_b128 v[64:67], v17 offset:11264\n" \
"s_add_u32 s42, s42, s24\nv_smfmac_f32_16x16x64_bf16 v[24:27], v[32:35], a[144:151], v18\ns_waitcnt lgkmcnt(4)\nv_mfma_f32_16x16x32_bf16 v[44:47], a[60:63], v[56:59], v[44:47]\n" \
"v_add_f32 v104, v104, v106\nv_add_f32 v105, v105, v107\nv_cvt_pk_bf16_f32 v104, v104, v105\nv_mfma_f32_16x16x32_bf16 v[68:71], a[60:63], v[76:79], v[68:71]\n" \
"v_add_f32 v24, v24, v26\nv_add_f32 v25, v25, v27\nv_cvt_pk_bf16_f32 v24, v24, v25\nv_mfma_f32_16x16x32_bf16 v[72:75], a[156:159], v[56:59], v[72:75]\n" \
"ds_write2_b32 v92, v104, v24 offset0:0 offset1:4\nv_mfma_f32_16x16x32_bf16 v[20:23], a[156:159], v[76:79], v[20:23]\ns_add_u32 s99, s20, 2\nv_mov_b64 v[24:25], 0\n" \
"v_mov_b64 v[26:27], 0\nv_mfma_f32_16x16x32_bf16 v[44:47], a[64:67], v[28:31], v[44:47]\ns_min_u32 s99, s99, 63\nv_bfe_u32 v32, v16, 4, 3\n" \
"v_lshlrev_b32 v32, 2, v32\nv_mfma_f32_16x16x32_bf16 v[68:71], a[64:67], v[80:83], v[68:71]\ns_waitcnt vmcnt(1) lgkmcnt(0)\ns_barrier\n" \
"ds_read_b64_tr_b16 v[48:49], v19 offset:0\nv_mfma_f32_16x16x32_bf16 v[72:75], a[160:163], v[28:31], v[72:75]\nds_read_b64_tr_b16 v[50:51], v19 offset:256\nds_read_b64_tr_b16 v[28:29], v19 offset:1024\n" \
"ds_read_b64_tr_b16 v[30:31], v19 offset:1280\nv_mfma_f32_16x16x32_bf16 v[20:23], a[160:163], v[80:83], v[20:23]\nds_read_b128 v[56:59], v16 offset:24576\nv_mov_b64 v[76:77], 0\n" \
"v_mfma_f32_16x16x32_bf16 v[44:47], a[68:71], v[36:39], v[44:47]\ns_lshl_b32 s99, s99, 5\ns_add_u32 s99, s99, s26\nv_mov_b64 v[78:79], 0\n" \
"v_mfma_f32_16x16x32_bf16 v[68:71], a[68:71], v[52:55], v[68:71]\ns_waitcnt lgkmcnt(3)\nbuffer_load_dword v33, v32, s[8:11], s99 offen sc1\nv_mfma_f32_16x16x32_bf16 v[72:75], a[164:167], v[36:39], v[72:75]\n" \
"ds_read_b128 v[80:83], v16 offset:25600\nv_mov_b64 v[34:35], 0\nv_smfmac_f32_16x16x64_bf16 v[40:43], v[48:51], a[200:207], v18\ns_waitcnt lgkmcnt(2)\n" \
"ds_read_b128 v[88:91], v17 offset:12288\nv_smfmac_f32_16x16x64_bf16 v[40:43], v[28:31], v[60:67], v18\nds_read_b128 v[84:87], v16 offset:45056\nv_mov_b64 v[36:37], 0\n" \
"v_smfmac_f32_16x16x64_bf16 v[24:27], v[48:51], a[224:231], v18\nds_read_b128 v[92:95], v17 offset:13312\nv_mfma_f32_16x16x32_bf16 v[20:23], a[164:167], v[52:55], v[20:23]\nds_read_b128 v[60:63], v17 offset:14336\n" \
"v_add_f32 v40, v40, v42\nv_smfmac_f32_16x16x64_bf16 v[24:27], v[28:31], a[232:239], v18\ns_waitcnt lgkmcnt(5)\nds_read_b128 v[52:55], v16 offset:45056\n" \
"v_mfma_f32_16x16x32_bf16 v[44:47], a[72:75], v[56:59], v[44:47]\nds_read_b128 v[64:67], v17 offset:15360\nv_add_f32 v41, v41, v43\nv_smfmac_f32_16x16x64_bf16 v[76:79], v[48:51], a[208:215], v18\n" \
"ds_read_b128 v[96:99], v16 offset:26624\nv_add_f32 v24, v24, v26\nv_smfmac_f32_16x16x64_bf16 v[76:79], v[28:31], a[216:223], v18\nds_read_b128 v[100:103], v16 offset:45056\n" \
"v_add_f32 v25, v25, v27\nv_smfmac_f32_16x16x64_bf16 v[34:37], v[48:51], a[240:247], v18\nds_read_b128 v[104:107], v17 offset:16384\nv_cvt_pk_bf16_f32 v38, v40, v41\n" \
"v_mfma_f32_16x16x32_bf16 v[72:75], a[168:171], v[56:59], v[72:75]\ns_waitcnt lgkmcnt(5)\nv_add_f32 v76, v76, v78\nv_add_f32 v77, v77, v79\n" \
"v_smfmac_f32_16x16x64_bf16 v[34:37], v[28:31], a[248:255], v18\nds_read_b128 v[56:59], v16 offset:45056\nv_cvt_pk_bf16_f32 v39, v24, v25\nv_smfmac_f32_16x16x64_bf16 v[84:87], v[48:51], v[112:119], v18\n" \
"ds_read_b128 v[108:111], v17 offset:17408\nv_cvt_pk_bf16_f32 v40, v76, v77\nv_mfma_f32_16x16x32_bf16 v[44:47], a[76:79], v[80:83], v[44:47]\ns_waitcnt lgkmcnt(4)\n" \
"v_permlane32_swap_b32 v38, v39\nv_smfmac_f32_16x16x64_bf16 v[84:87], v[28:31], v[120:127], v18\nds_read_b128 v[24:27], v16 offset:45056\nv_add_f32 v34, v34, v36\n" \
"v_smfmac_f32_16x16x64_bf16 v[52:55], v[48:51], v[128:135], v18\nds_read_b128 v[76:79], v16 offset:27648\nv_add_f32 v35, v35, v37\nv_cvt_pk_bf16_f32 v41, v34, v35\n" \
"v_smfmac_f32_16x16x64_bf16 v[52:55], v[28:31], v[136:143], v18\nds_read_b128 v[34:37], v17 offset:18432\nv_permlane32_swap_b32 v40, v41\nv_mfma_f32_16x16x32_bf16 v[72:75], a[172:175], v[80:83], v[72:75]\n" \
"s_waitcnt lgkmcnt(4)\nv_permlane16_swap_b32 v38, v40\nv_smfmac_f32_16x16x64_bf16 v[100:103], v[48:51], v[88:95], v18\nds_read_b128 v[80:83], v16 offset:45056\n" \
"v_permlane16_swap_b32 v39, v41\nv_and_or_b32 v38, v38, -2, s32\nv_smfmac_f32_16x16x64_bf16 v[100:103], v[28:31], v[60:67], v18\nds_read_b128 v[88:91], v16 offset:28672\n" \
"v_add_f32 v84, v84, v86\nv_mfma_f32_16x16x32_bf16 v[44:47], a[80:83], v[96:99], v[44:47]\nbuffer_store_dwordx4 v[38:41], v16, s[8:11], s29 offen offset:0 sc1\nv_add_f32 v85, v85, v87\n" \
"v_add_f32 v52, v52, v54\nv_add_f32 v53, v53, v55\nv_smfmac_f32_16x16x64_bf16 v[56:59], v[48:51], v[144:151], v18\nds_read_b128 v[60:63], v16 offset:45056\n" \
"v_add_f32 v100, v100, v102\nv_smfmac_f32_16x16x64_bf16 v[56:59], v[28:31], v[152:159], v18\nds_read_b128 v[38:41], v17 offset:19456\nv_add_f32 v101, v101, v103\n" \
"v_mfma_f32_16x16x32_bf16 v[72:75], a[176:179], v[96:99], v[72:75]\ns_waitcnt lgkmcnt(3)\nv_cvt_pk_bf16_f32 v64, v84, v85\nv_cvt_pk_bf16_f32 v65, v52, v53\n" \
"v_smfmac_f32_16x16x64_bf16 v[24:27], v[48:51], v[160:167], v18\nds_read_b128 v[52:55], v16 offset:45056\nv_add_f32 v56, v56, v58\nv_smfmac_f32_16x16x64_bf16 v[24:27], v[28:31], v[168:175], v18\n" \
"ds_read_b128 v[84:87], v16 offset:45056\nv_add_f32 v57, v57, v59\nv_mfma_f32_16x16x32_bf16 v[44:47], a[84:87], v[76:79], v[44:47]\nv_permlane32_swap_b32 v64, v65\n" \
"v_cvt_pk_bf16_f32 v66, v100, v101\nv_cvt_pk_bf16_f32 v67, v56, v57\nv_smfmac_f32_16x16x64_bf16 v[80:83], v[48:51], v[184:191], v18\nds_read_b128 v[56:59], v16 offset:29696\n" \
"v_permlane32_swap_b32 v66, v67\nv_smfmac_f32_16x16x64_bf16 v[80:83], v[28:31], v[192:199], v18\ns_waitcnt lgkmcnt(3)\nv_permlane16_swap_b32 v64, v66\n" \
"v_mfma_f32_16x16x32_bf16 v[72:75], a[180:183], v[76:79], v[72:75]\nv_permlane16_swap_b32 v65, v67\nv_and_or_b32 v64, v64, -2, s32\nv_smfmac_f32_16x16x64_bf16 v[60:63], v[48:51], v[104:111], v18\n" \
"ds_read_b128 v[76:79], v16 offset:45056\nv_add_f32 v24, v24, v26\nv_smfmac_f32_16x16x64_bf16 v[60:63], v[28:31], v[176:183], v18\ns_waitcnt lgkmcnt(2)\n" \
"buffer_store_dwordx4 v[64:67], v16, s[8:11], s29 offen offset:1024 sc1\nds_read_b128 v[92:95], v17 offset:20480\nv_add_f32 v25, v25, v27\nv_mfma_f32_16x16x32_bf16 v[44:47], a[88:91], v[88:91], v[44:47]\n" \
"ds_read_b128 v[96:99], v17 offset:21504\nv_add_f32 v80, v80, v82\nv_smfmac_f32_16x16x64_bf16 v[52:55], v[48:51], v[200:207], v18\nds_read_b128 v[64:67], v16 offset:45056\n" \
"v_add_f32 v81, v81, v83\nv_smfmac_f32_16x16x64_bf16 v[52:55], v[28:31], v[34:41], v18\nds_read_b128 v[100:103], v16 offset:30720\nv_add_f32 v60, v60, v62\n" \
"v_smfmac_f32_16x16x64_bf16 v[84:87], v[48:51], v[208:215], v18\nds_read_b128 v[34:37], v16 offset:45056\nv_add_f32 v61, v61, v63\nv_mfma_f32_16x16x32_bf16 v[72:75], a[184:187], v[88:91], v[72:75]\n" \
"s_waitcnt lgkmcnt(5)\nv_add_f32 v52, v52, v54\nv_add_f32 v53, v53, v55\nv_smfmac_f32_16x16x64_bf16 v[84:87], v[28:31], v[216:223], v18\n" \
"ds_read_b128 v[104:107], v17 offset:22528\nv_cvt_pk_bf16_f32 v38, v24, v25\nv_mfma_f32_16x16x32_bf16 v[44:47], a[92:95], v[56:59], v[44:47]\nv_cvt_pk_bf16_f32 v39, v80, v81\n" \
"v_cvt_pk_bf16_f32 v40, v60, v61\nv_cvt_pk_bf16_f32 v41, v52, v53\nv_smfmac_f32_16x16x64_bf16 v[76:79], v[48:51], v[232:239], v18\nds_read_b128 v[108:111], v17 offset:23552\n" \
"v_permlane32_swap_b32 v38, v39\nv_permlane32_swap_b32 v40, v41\nv_smfmac_f32_16x16x64_bf16 v[76:79], v[28:31], v[240:247], v18\nds_read_b128 v[24:27], v16 offset:31744\n" \
"v_permlane16_swap_b32 v38, v40\nv_mfma_f32_16x16x32_bf16 v[72:75], a[188:191], v[56:59], v[72:75]\ns_waitcnt lgkmcnt(3)\nv_permlane16_swap_b32 v39, v41\n" \
"v_and_or_b32 v38, v38, -2, s32\nv_smfmac_f32_16x16x64_bf16 v[64:67], v[48:51], v[92:99], v18\nds_read_b128 v[52:55], v16 offset:57344\nv_add_f32 v84, v84, v86\n" \
"v_smfmac_f32_16x16x64_bf16 v[64:67], v[28:31], v[224:231], v18\nds_read_b128 v[56:59], v16 offset:58368\nv_add_f32 v85, v85, v87\nv_mfma_f32_16x16x32_bf16 v[44:47], a[96:99], v[100:103], v[44:47]\n" \
"s_waitcnt lgkmcnt(3)\nbuffer_store_dwordx4 v[38:41], v16, s[8:11], s29 offen offset:2048 sc1\nv_add_f32 v76, v76, v78\nv_add_f32 v77, v77, v79\n" \
"v_cvt_pk_bf16_f32 v38, v84, v85\nv_smfmac_f32_16x16x64_bf16 v[34:37], v[48:51], v[104:111], v18\nds_read_b128 v[60:63], v16 offset:59392\nv_add_f32 v64, v64, v66\n" \
"v_add_f32 v65, v65, v67\nv_cvt_pk_bf16_f32 v39, v76, v77\nv_cvt_pk_bf16_f32 v40, v64, v65\nv_smfmac_f32_16x16x64_bf16 v[34:37], v[28:31], v[248:255], v18\n" \
"ds_read_b128 v[48:51], v16 offset:60416\nv_permlane32_swap_b32 v38, v39\nv_mfma_f32_16x16x32_bf16 v[72:75], a[192:195], v[100:103], v[72:75]\ns_waitcnt lgkmcnt(3)\n" \
"ds_read_b128 v[28:31], v16 offset:61440\nv_mfma_f32_16x16x32_bf16 v[44:47], a[100:103], v[24:27], v[44:47]\nv_add_f32 v34, v34, v36\nv_add_f32 v35, v35, v37\n" \
"v_cvt_pk_bf16_f32 v41, v34, v35\nv_mfma_f32_16x16x32_bf16 v[72:75], a[196:199], v[24:27], v[72:75]\nds_read_b128 v[34:37], v16 offset:62464\nv_permlane32_swap_b32 v40, v41\n" \
"v_mfma_f32_16x16x32_bf16 v[68:71], a[72:75], v[52:55], v[68:71]\ns_waitcnt lgkmcnt(3)\nv_permlane16_swap_b32 v38, v40\nv_permlane16_swap_b32 v39, v41\n" \
"v_and_or_b32 v38, v38, -2, s32\nv_mfma_f32_16x16x32_bf16 v[20:23], a[168:171], v[52:55], v[20:23]\nbuffer_store_dwordx4 v[38:41], v16, s[8:11], s29 offen offset:3072 sc1\ns_bfe_u32 s32, s20, 0x10003\n" \
"v_cvt_pk_bf16_f32 v44, v44, v45\nv_cvt_pk_bf16_f32 v45, v46, v47\nv_mfma_f32_16x16x32_bf16 v[68:71], a[76:79], v[56:59], v[68:71]\ns_xor_b32 s29, s42, s43\n" \
"v_cvt_pk_bf16_f32 v46, v72, v73\nv_cvt_pk_bf16_f32 v47, v74, v75\nv_mfma_f32_16x16x32_bf16 v[20:23], a[172:175], v[56:59], v[20:23]\nds_read_b128 v[24:27], v16 offset:63488\n" \
"v_mfma_f32_16x16x32_bf16 v[68:71], a[80:83], v[60:63], v[68:71]\ns_waitcnt vmcnt(4) lgkmcnt(1)\nv_cmp_eq_u32 s[42:43], -1, v33\nv_mfma_f32_16x16x32_bf16 v[20:23], a[176:179], v[60:63], v[20:23]\n" \
"ds_read_b128 v[38:41], v16 offset:64512\nv_mfma_f32_16x16x32_bf16 v[68:71], a[84:87], v[48:51], v[68:71]\ns_cmp_eq_u64 s[42:43], 0\ns_cselect_b32 s14, s10, 0\n" \
"s_cmp_lt_i32 s20, 62\ns_cselect_b32 s14, s14, 0\ns_cmp_eq_u32 s14, 0\ns_cbranch_scc1 4f\n" \
"5:\nv_add_u32 v32, s28, v16\nv_mfma_f32_16x16x32_bf16 v[20:23], a[180:183], v[48:51], v[20:23]\nds_write_b128 v32, v[44:47] offset:0\n" \
"s_add_u32 m0, s22, 0\nv_mfma_f32_16x16x32_bf16 v[68:71], a[88:91], v[28:31], v[68:71]\nbuffer_load_dwordx4 v16, s[12:15], s21 offen offset:0 sc1 lds\nv_mfma_f32_16x16x32_bf16 v[20:23], a[184:187], v[28:31], v[20:23]\n" \
"ds_read_b128 v[42:45], v17 offset:10240\ns_add_u32 s20, s20, 1\nv_mfma_f32_16x16x32_bf16 v[68:71], a[92:95], v[34:37], v[68:71]\ns_waitcnt lgkmcnt(1)\n" \
"s_barrier\nds_read_b128 v[28:31], v16 offset:8192\nv_mfma_f32_16x16x32_bf16 v[20:23], a[188:191], v[34:37], v[20:23]\nds_read_b128 v[48:51], v16 offset:9216\n" \
"v_mfma_f32_16x16x32_bf16 v[68:71], a[96:99], v[24:27], v[68:71]\nds_read_b128 v[64:67], v16 offset:10240\nv_add_u32 v32, s28, v16\nv_mfma_f32_16x16x32_bf16 v[20:23], a[192:195], v[24:27], v[20:23]\n" \
"buffer_load_dwordx4 v16, s[12:15], s21 offen offset:1024 sc1 lds\nds_read_b128 v[72:75], v16 offset:11264\nds_read_b128 v[24:27], v17 offset:11264\nv_mfma_f32_16x16x32_bf16 v[68:71], a[100:103], v[38:41], v[68:71]\n" \
"s_waitcnt lgkmcnt(4)\nv_mfma_f32_16x16x32_bf16 v[20:23], a[196:199], v[38:41], v[20:23]\ns_waitcnt lgkmcnt(3)\nv_mfma_f32_16x16x32_bf16 v[34:37], v[28:31], a[200:203], 0\n" \
"s_waitcnt lgkmcnt(2)\nv_cvt_pk_bf16_f32 v68, v68, v69\nv_cvt_pk_bf16_f32 v69, v70, v71\nv_mfma_f32_16x16x32_bf16 v[34:37], v[48:51], a[204:207], v[34:37]\n" \
"s_waitcnt lgkmcnt(0)\nv_cvt_pk_bf16_f32 v70, v20, v21\nv_cvt_pk_bf16_f32 v71, v22, v23\nv_mfma_f32_16x16x32_bf16 v[34:37], v[64:67], v[42:45], v[34:37]\n" \
"buffer_load_dwordx4 v16, s[12:15], s21 offen offset:2048 sc1 lds\nv_mfma_f32_16x16x32_bf16 v[34:37], v[72:75], v[24:27], v[34:37]\nds_write_b128 v32, v[68:71] offset:32768\nv_mfma_f32_16x16x32_bf16 v[20:23], v[28:31], a[208:211], 0\n" \
"v_mfma_f32_16x16x32_bf16 v[20:23], v[48:51], a[212:215], v[20:23]\nv_cvt_pk_bf16_f32 v34, v34, v35\nv_cvt_pk_bf16_f32 v35, v36, v37\nv_mfma_f32_16x16x32_bf16 v[20:23], v[64:67], a[216:219], v[20:23]\n" \
"s_add_u32 s43, s21, 0x4000\nds_read_b128 v[24:27], v17 offset:18432\nv_mfma_f32_16x16x32_bf16 v[20:23], v[72:75], a[220:223], v[20:23]\ns_waitcnt lgkmcnt(1)\n" \
"s_barrier\nv_mfma_f32_16x16x32_bf16 v[38:41], v[28:31], a[224:227], 0\nbuffer_load_dwordx4 v16, s[12:15], s21 offen offset:3072 sc1 lds\nv_mfma_f32_16x16x32_bf16 v[38:41], v[48:51], a[228:231], v[38:41]\n" \
"s_add_u32 m0, s22, 0x4000\nds_read_b128 v[52:55], v17 offset:19456\nv_mfma_f32_16x16x32_bf16 v[38:41], v[64:67], a[232:235], v[38:41]\nbuffer_load_dwordx4 v16, s[12:15], s43 offen offset:0 sc1 lds\n" \
"v_mfma_f32_16x16x32_bf16 v[38:41], v[72:75], a[236:239], v[38:41]\nds_read_b128 v[68:71], v16 offset:40960\nv_cvt_pk_bf16_f32 v20, v20, v21\nv_mfma_f32_16x16x32_bf16 v[56:59], v[28:31], v[144:147], 0\n" \
"ds_read_b128 v[76:79], v16 offset:41984\nv_cvt_pk_bf16_f32 v21, v22, v23\nv_mfma_f32_16x16x32_bf16 v[56:59], v[48:51], v[148:151], v[56:59]\nds_read_b128 v[80:83], v16 offset:43008\n" \
"v_cvt_pk_bf16_f32 v38, v38, v39\nv_mfma_f32_16x16x32_bf16 v[56:59], v[64:67], v[152:155], v[56:59]\nds_read_b128 v[60:63], v17 offset:10240\nv_cvt_pk_bf16_f32 v39, v40, v41\n" \
"v_mfma_f32_16x16x32_bf16 v[56:59], v[72:75], v[156:159], v[56:59]\nbuffer_load_dwordx4 v16, s[12:15], s43 offen offset:1024 sc1 lds\nv_mfma_f32_16x16x32_bf16 v[84:87], v[28:31], v[200:203], 0\ns_waitcnt lgkmcnt(4)\n" \
"ds_read_b128 v[44:47], v16 offset:44032\nv_mfma_f32_16x16x32_bf16 v[84:87], v[48:51], v[204:207], v[84:87]\nds_read_b128 v[40:43], v17 offset:11264\nv_cvt_pk_bf16_f32 v56, v56, v57\n" \
"v_mfma_f32_16x16x32_bf16 v[84:87], v[64:67], v[24:27], v[84:87]\ns_waitcnt lgkmcnt(4)\nv_cvt_pk_bf16_f32 v57, v58, v59\nv_mfma_f32_16x16x32_bf16 v[84:87], v[72:75], v[52:55], v[84:87]\n" \
"v_mfma_f32_16x16x32_bf16 v[22:25], v[68:71], a[200:203], 0\ns_waitcnt lgkmcnt(2)\nbuffer_load_dwordx4 v16, s[12:15], s43 offen offset:2048 sc1 lds\nv_mfma_f32_16x16x32_bf16 v[22:25], v[76:79], a[204:207], v[22:25]\n" \
"v_cvt_pk_bf16_f32 v84, v84, v85\nv_cvt_pk_bf16_f32 v85, v86, v87\nv_mfma_f32_16x16x32_bf16 v[22:25], v[80:83], v[60:63], v[22:25]\ns_waitcnt lgkmcnt(0)\n" \
"v_mfma_f32_16x16x32_bf16 v[22:25], v[44:47], v[40:43], v[22:25]\nv_mfma_f32_16x16x32_bf16 v[52:55], v[68:71], a[208:211], 0\nbuffer_load_dwordx4 v16, s[12:15], s43 offen offset:3072 sc1 lds\nv_mfma_f32_16x16x32_bf16 v[52:55], v[76:79], a[212:215], v[52:55]\n" \
"v_cvt_pk_bf16_f32 v36, v22, v23\nv_cvt_pk_bf16_f32 v37, v24, v25\nv_mfma_f32_16x16x32_bf16 v[52:55], v[80:83], a[216:219], v[52:55]\nds_read_b128 v[24:27], v17 offset:18432\n" \
"v_mfma_f32_16x16x32_bf16 v[52:55], v[44:47], a[220:223], v[52:55]\nds_read_b128 v[60:63], v17 offset:19456\nv_mfma_f32_16x16x32_bf16 v[86:89], v[68:71], a[224:227], 0\nv_mfma_f32_16x16x32_bf16 v[86:89], v[76:79], a[228:231], v[86:89]\n" \
"buffer_store_dwordx4 v[34:37], v16, s[8:11], s16 offen offset:0 sc1\nv_cvt_pk_bf16_f32 v22, v52, v53\nv_cvt_pk_bf16_f32 v23, v54, v55\nv_mfma_f32_16x16x32_bf16 v[86:89], v[80:83], a[232:235], v[86:89]\n" \
"v_mfma_f32_16x16x32_bf16 v[86:89], v[44:47], a[236:239], v[86:89]\nv_mfma_f32_16x16x32_bf16 v[32:35], v[68:71], v[144:147], 0\nv_mfma_f32_16x16x32_bf16 v[32:35], v[76:79], v[148:151], v[32:35]\nv_cvt_pk_bf16_f32 v40, v86, v87\n" \
"v_cvt_pk_bf16_f32 v41, v88, v89\nv_mfma_f32_16x16x32_bf16 v[32:35], v[80:83], v[152:155], v[32:35]\nv_mfma_f32_16x16x32_bf16 v[32:35], v[44:47], v[156:159], v[32:35]\nv_mfma_f32_16x16x32_bf16 v[52:55], v[68:71], v[200:203], 0\n" \
"s_waitcnt lgkmcnt(0)\nbuffer_store_dwordx4 v[20:23], v16, s[8:11], s16 offen offset:1024 sc1\nv_mfma_f32_16x16x32_bf16 v[52:55], v[76:79], v[204:207], v[52:55]\nv_cvt_pk_bf16_f32 v58, v32, v33\n" \
"v_cvt_pk_bf16_f32 v59, v34, v35\nv_mfma_f32_16x16x32_bf16 v[52:55], v[80:83], v[24:27], v[52:55]\nv_mfma_f32_16x16x32_bf16 v[52:55], v[44:47], v[60:63], v[52:55]\nv_mfma_f32_16x16x32_bf16 v[20:23], v[28:31], a[240:243], 0\n" \
"v_mfma_f32_16x16x32_bf16 v[24:27], v[68:71], a[240:243], 0\nv_cvt_pk_bf16_f32 v86, v52, v53\nv_cvt_pk_bf16_f32 v87, v54, v55\nv_mfma_f32_16x16x32_bf16 v[20:23], v[48:51], a[244:247], v[20:23]\n" \
"v_mfma_f32_16x16x32_bf16 v[24:27], v[76:79], a[244:247], v[24:27]\nv_mfma_f32_16x16x32_bf16 v[20:23], v[64:67], a[248:251], v[20:23]\nbuffer_store_dwordx4 v[38:41], v16, s[8:11], s16 offen offset:2048 sc1\nds_read_b128 v[32:35], v17 offset:12288\n" \
"v_mfma_f32_16x16x32_bf16 v[24:27], v[80:83], a[248:251], v[24:27]\nv_mfma_f32_16x16x32_bf16 v[20:23], v[72:75], a[252:255], v[20:23]\nds_read_b128 v[36:39], v17 offset:13312\nv_mfma_f32_16x16x32_bf16 v[24:27], v[44:47], a[252:255], v[24:27]\n" \
"v_mfma_f32_16x16x32_bf16 v[40:43], v[28:31], v[128:131], 0\nds_read_b128 v[52:55], v17 offset:14336\nv_cvt_pk_bf16_f32 v20, v20, v21\nv_mfma_f32_16x16x32_bf16 v[60:63], v[68:71], v[128:131], 0\n" \
"v_cvt_pk_bf16_f32 v21, v22, v23\nv_cvt_pk_bf16_f32 v22, v24, v25\nv_mfma_f32_16x16x32_bf16 v[40:43], v[48:51], v[132:135], v[40:43]\ns_waitcnt vmcnt(7)\n" \
"s_barrier\nv_mfma_f32_16x16x32_bf16 v[60:63], v[76:79], v[132:135], v[60:63]\nbuffer_store_dwordx4 v[56:59], v16, s[8:11], s17 offen offset:3072 sc1\nds_read_b128 v[88:91], v17 offset:15360\n" \
"v_cvt_pk_bf16_f32 v23, v26, v27\nv_mfma_f32_16x16x32_bf16 v[40:43], v[64:67], v[136:139], v[40:43]\nv_mfma_f32_16x16x32_bf16 v[60:63], v[80:83], v[136:139], v[60:63]\nv_mfma_f32_16x16x32_bf16 v[40:43], v[72:75], v[140:143], v[40:43]\n" \
"s_waitcnt lgkmcnt(1)\nv_mfma_f32_16x16x32_bf16 v[60:63], v[44:47], v[140:143], v[60:63]\nv_mfma_f32_16x16x32_bf16 v[24:27], v[28:31], v[32:35], 0\nv_cvt_pk_bf16_f32 v40, v40, v41\n" \
"v_cvt_pk_bf16_f32 v41, v42, v43\nv_mfma_f32_16x16x32_bf16 v[56:59], v[68:71], v[32:35], 0\nv_cvt_pk_bf16_f32 v42, v60, v61\nv_cvt_pk_bf16_f32 v43, v62, v63\n" \
"v_mfma_f32_16x16x32_bf16 v[24:27], v[48:51], v[36:39], v[24:27]\nv_mfma_f32_16x16x32_bf16 v[56:59], v[76:79], v[36:39], v[56:59]\nbuffer_store_dwordx4 v[84:87], v16, s[8:11], s18 offen offset:3072 sc1\ns_add_u32 m0, s22, 0x2000\n" \
"s_add_u32 s43, s21, 0x2000\ns_add_u32 s21, s21, 0x10000\nv_mfma_f32_16x16x32_bf16 v[24:27], v[64:67], v[52:55], v[24:27]\ns_waitcnt lgkmcnt(0)\n" \
"ds_read_b128 v[32:35], v17 offset:16384\nv_mfma_f32_16x16x32_bf16 v[56:59], v[80:83], v[52:55], v[56:59]\nv_mfma_f32_16x16x32_bf16 v[24:27], v[72:75], v[88:91], v[24:27]\nds_read_b128 v[36:39], v17 offset:17408\n" \
"v_mfma_f32_16x16x32_bf16 v[56:59], v[44:47], v[88:91], v[56:59]\nbuffer_load_dwordx4 v16, s[12:15], s43 offen offset:0 sc1 lds\nv_mfma_f32_16x16x32_bf16 v[52:55], v[28:31], v[112:115], 0\nv_cvt_pk_bf16_f32 v24, v24, v25\n" \
"v_cvt_pk_bf16_f32 v25, v26, v27\nv_mfma_f32_16x16x32_bf16 v[60:63], v[68:71], v[112:115], 0\nv_cvt_pk_bf16_f32 v26, v56, v57\nv_cvt_pk_bf16_f32 v27, v58, v59\n" \
"v_mfma_f32_16x16x32_bf16 v[52:55], v[48:51], v[116:119], v[52:55]\nbuffer_store_dwordx4 v[20:23], v16, s[8:11], s16 offen offset:3072 sc1\nv_mfma_f32_16x16x32_bf16 v[60:63], v[76:79], v[116:119], v[60:63]\nv_mfma_f32_16x16x32_bf16 v[52:55], v[64:67], v[120:123], v[52:55]\n" \
"v_mfma_f32_16x16x32_bf16 v[60:63], v[80:83], v[120:123], v[60:63]\nv_mfma_f32_16x16x32_bf16 v[52:55], v[72:75], v[124:127], v[52:55]\ns_waitcnt lgkmcnt(0)\nv_mfma_f32_16x16x32_bf16 v[60:63], v[44:47], v[124:127], v[60:63]\n" \
"v_mfma_f32_16x16x32_bf16 v[20:23], v[28:31], v[32:35], 0\nv_cvt_pk_bf16_f32 v52, v52, v53\nv_cvt_pk_bf16_f32 v53, v54, v55\nv_mfma_f32_16x16x32_bf16 v[56:59], v[68:71], v[32:35], 0\n" \
"buffer_store_dwordx4 v[40:43], v16, s[8:11], s17 offen offset:2048 sc1\nbuffer_load_dwordx4 v16, s[12:15], s43 offen offset:1024 sc1 lds\nv_cvt_pk_bf16_f32 v54, v60, v61\nv_mfma_f32_16x16x32_bf16 v[20:23], v[48:51], v[36:39], v[20:23]\n" \
"v_cvt_pk_bf16_f32 v55, v62, v63\nv_mfma_f32_16x16x32_bf16 v[56:59], v[76:79], v[36:39], v[56:59]\nv_mfma_f32_16x16x32_bf16 v[20:23], v[64:67], v[176:179], v[20:23]\nv_mfma_f32_16x16x32_bf16 v[56:59], v[80:83], v[176:179], v[56:59]\n" \
"v_mfma_f32_16x16x32_bf16 v[20:23], v[72:75], v[180:183], v[20:23]\nv_mfma_f32_16x16x32_bf16 v[56:59], v[44:47], v[180:183], v[56:59]\nv_mfma_f32_16x16x32_bf16 v[32:35], v[28:31], v[184:187], 0\nv_cvt_pk_bf16_f32 v20, v20, v21\n" \
"v_cvt_pk_bf16_f32 v21, v22, v23\nv_mfma_f32_16x16x32_bf16 v[36:39], v[68:71], v[184:187], 0\nbuffer_store_dwordx4 v[24:27], v16, s[8:11], s17 offen offset:1024 sc1\nv_cvt_pk_bf16_f32 v22, v56, v57\n" \
"v_cvt_pk_bf16_f32 v23, v58, v59\nv_mfma_f32_16x16x32_bf16 v[32:35], v[48:51], v[188:191], v[32:35]\nv_mfma_f32_16x16x32_bf16 v[36:39], v[76:79], v[188:191], v[36:39]\nbuffer_load_dwordx4 v16, s[12:15], s43 offen offset:2048 sc1 lds\n" \
"v_mfma_f32_16x16x32_bf16 v[32:35], v[64:67], v[192:195], v[32:35]\nv_mfma_f32_16x16x32_bf16 v[36:39], v[80:83], v[192:195], v[36:39]\nv_mfma_f32_16x16x32_bf16 v[32:35], v[72:75], v[196:199], v[32:35]\nv_mfma_f32_16x16x32_bf16 v[36:39], v[44:47], v[196:199], v[36:39]\n" \
"v_mfma_f32_16x16x32_bf16 v[24:27], v[28:31], v[160:163], 0\nbuffer_store_dwordx4 v[52:55], v16, s[8:11], s17 offen offset:0 sc1\nv_cvt_pk_bf16_f32 v32, v32, v33\nv_cvt_pk_bf16_f32 v33, v34, v35\n" \
"v_mfma_f32_16x16x32_bf16 v[40:43], v[68:71], v[160:163], 0\nv_cvt_pk_bf16_f32 v34, v36, v37\nv_cvt_pk_bf16_f32 v35, v38, v39\nv_mfma_f32_16x16x32_bf16 v[24:27], v[48:51], v[164:167], v[24:27]\n" \
"v_mfma_f32_16x16x32_bf16 v[40:43], v[76:79], v[164:167], v[40:43]\nv_mfma_f32_16x16x32_bf16 v[24:27], v[64:67], v[168:171], v[24:27]\nds_read_b128 v[36:39], v17 offset:20480\nv_mfma_f32_16x16x32_bf16 v[40:43], v[80:83], v[168:171], v[40:43]\n" \
"buffer_load_dwordx4 v16, s[12:15], s43 offen offset:3072 sc1 lds\nv_mfma_f32_16x16x32_bf16 v[24:27], v[72:75], v[172:175], v[24:27]\nds_read_b128 v[52:55], v17 offset:21504\nv_mfma_f32_16x16x32_bf16 v[40:43], v[44:47], v[172:175], v[40:43]\n" \
"v_mfma_f32_16x16x32_bf16 v[60:63], v[28:31], v[232:235], 0\nbuffer_store_dwordx4 v[20:23], v16, s[8:11], s18 offen offset:1024 sc1\nv_cvt_pk_bf16_f32 v24, v24, v25\nv_cvt_pk_bf16_f32 v25, v26, v27\n" \
"v_mfma_f32_16x16x32_bf16 v[20:23], v[68:71], v[232:235], 0\nv_cvt_pk_bf16_f32 v26, v40, v41\nv_cvt_pk_bf16_f32 v27, v42, v43\nv_mfma_f32_16x16x32_bf16 v[60:63], v[48:51], v[236:239], v[60:63]\n" \
"v_mfma_f32_16x16x32_bf16 v[20:23], v[76:79], v[236:239], v[20:23]\nv_mfma_f32_16x16x32_bf16 v[60:63], v[64:67], v[240:243], v[60:63]\nv_mfma_f32_16x16x32_bf16 v[20:23], v[80:83], v[240:243], v[20:23]\nv_mfma_f32_16x16x32_bf16 v[60:63], v[72:75], v[244:247], v[60:63]\n" \
"s_waitcnt lgkmcnt(0)\nv_mfma_f32_16x16x32_bf16 v[20:23], v[44:47], v[244:247], v[20:23]\nbuffer_store_dwordx4 v[32:35], v16, s[8:11], s18 offen offset:2048 sc1\nv_mfma_f32_16x16x32_bf16 v[84:87], v[28:31], v[36:39], 0\n" \
"v_cvt_pk_bf16_f32 v60, v60, v61\nv_cvt_pk_bf16_f32 v61, v62, v63\nv_mfma_f32_16x16x32_bf16 v[32:35], v[68:71], v[36:39], 0\nv_cvt_pk_bf16_f32 v62, v20, v21\n" \
"v_cvt_pk_bf16_f32 v63, v22, v23\nv_mfma_f32_16x16x32_bf16 v[84:87], v[48:51], v[52:55], v[84:87]\nv_mfma_f32_16x16x32_bf16 v[32:35], v[76:79], v[52:55], v[32:35]\nv_mfma_f32_16x16x32_bf16 v[84:87], v[64:67], v[224:227], v[84:87]\n" \
"ds_read_b128 v[52:55], v17 offset:22528\nv_mfma_f32_16x16x32_bf16 v[32:35], v[80:83], v[224:227], v[32:35]\nv_mfma_f32_16x16x32_bf16 v[84:87], v[72:75], v[228:231], v[84:87]\nbuffer_store_dwordx4 v[24:27], v16, s[8:11], s18 offen offset:0 sc1\n" \
"ds_read_b128 v[88:91], v17 offset:23552\nv_mfma_f32_16x16x32_bf16 v[32:35], v[44:47], v[228:231], v[32:35]\nv_mfma_f32_16x16x32_bf16 v[56:59], v[28:31], v[208:211], 0\nv_cvt_pk_bf16_f32 v84, v84, v85\n" \
"v_cvt_pk_bf16_f32 v85, v86, v87\nv_mfma_f32_16x16x32_bf16 v[24:27], v[68:71], v[208:211], 0\nv_cvt_pk_bf16_f32 v86, v32, v33\nv_cvt_pk_bf16_f32 v87, v34, v35\n" \
"v_mfma_f32_16x16x32_bf16 v[56:59], v[48:51], v[212:215], v[56:59]\nv_mfma_f32_16x16x32_bf16 v[24:27], v[76:79], v[212:215], v[24:27]\nds_read_b128 v[20:23], v16 offset:0\nv_mfma_f32_16x16x32_bf16 v[56:59], v[64:67], v[216:219], v[56:59]\n" \
"ds_read_b128 v[40:43], v16 offset:32768\nv_mfma_f32_16x16x32_bf16 v[24:27], v[80:83], v[216:219], v[24:27]\nv_mfma_f32_16x16x32_bf16 v[56:59], v[72:75], v[220:223], v[56:59]\ns_waitcnt lgkmcnt(2)\n" \
"buffer_store_dwordx4 v[60:63], v16, s[8:11], s19 offen offset:2048 sc1\nv_mfma_f32_16x16x32_bf16 v[24:27], v[44:47], v[220:223], v[24:27]\nds_read_b128 v[36:39], v16 offset:1024\nv_mfma_f32_16x16x32_bf16 v[60:63], v[28:31], v[52:55], 0\n" \
"ds_read_b128 v[32:35], v16 offset:33792\nv_cvt_pk_bf16_f32 v56, v56, v57\nv_mfma_f32_16x16x32_bf16 v[28:31], v[68:71], v[52:55], 0\nv_cvt_pk_bf16_f32 v57, v58, v59\n" \
"v_cvt_pk_bf16_f32 v58, v24, v25\nv_mfma_f32_16x16x32_bf16 v[60:63], v[48:51], v[88:91], v[60:63]\nv_cvt_pk_bf16_f32 v59, v26, v27\nv_mfma_f32_16x16x32_bf16 v[28:31], v[76:79], v[88:91], v[28:31]\n" \
"ds_read_b128 v[52:55], v16 offset:2048\nv_mfma_f32_16x16x32_bf16 v[60:63], v[64:67], v[248:251], v[60:63]\nds_read_b128 v[24:27], v16 offset:34816\nv_mfma_f32_16x16x32_bf16 v[28:31], v[80:83], v[248:251], v[28:31]\n" \
"buffer_store_dwordx4 v[84:87], v16, s[8:11], s19 offen offset:1024 sc1\nv_mfma_f32_16x16x32_bf16 v[60:63], v[72:75], v[252:255], v[60:63]\ns_waitcnt lgkmcnt(2)\nv_mfma_f32_16x16x32_bf16 v[28:31], v[44:47], v[252:255], v[28:31]\n" \
"ds_read_b128 v[48:51], v16 offset:3072\nv_mfma_f32_16x16x32_bf16 v[44:47], v[0:3], v[20:23], 0\nds_read_b128 v[64:67], v16 offset:35840\nv_cvt_pk_bf16_f32 v60, v60, v61\n" \
"v_mfma_f32_16x16x32_bf16 v[68:71], v[0:3], v[40:43], 0\nv_cvt_pk_bf16_f32 v61, v62, v63\nv_cvt_pk_bf16_f32 v62, v28, v29\nv_mfma_f32_16x16x32_bf16 v[72:75], v[8:11], v[20:23], 0\n" \
"v_cvt_pk_bf16_f32 v63, v30, v31\nv_mfma_f32_16x16x32_bf16 v[20:23], v[8:11], v[40:43], 0\nds_read_b128 v[28:31], v16 offset:4096\nv_mfma_f32_16x16x32_bf16 v[44:47], v[4:7], v[36:39], v[44:47]\n" \
"ds_read_b128 v[40:43], v16 offset:36864\nv_mfma_f32_16x16x32_bf16 v[68:71], v[4:7], v[32:35], v[68:71]\nbuffer_store_dwordx4 v[56:59], v16, s[8:11], s19 offen offset:0 sc1\nv_mfma_f32_16x16x32_bf16 v[72:75], v[12:15], v[36:39], v[72:75]\n" \
"s_waitcnt lgkmcnt(2)\nv_mfma_f32_16x16x32_bf16 v[20:23], v[12:15], v[32:35], v[20:23]\nds_read_b128 v[36:39], v16 offset:5120\nv_mfma_f32_16x16x32_bf16 v[44:47], a[0:3], v[52:55], v[44:47]\n" \
"ds_read_b128 v[32:35], v16 offset:37888\nv_mfma_f32_16x16x32_bf16 v[68:71], a[0:3], v[24:27], v[68:71]\nv_mfma_f32_16x16x32_bf16 v[72:75], a[104:107], v[52:55], v[72:75]\nv_mfma_f32_16x16x32_bf16 v[20:23], a[104:107], v[24:27], v[20:23]\n" \
"ds_read_b128 v[52:55], v16 offset:6144\nv_mfma_f32_16x16x32_bf16 v[44:47], a[4:7], v[48:51], v[44:47]\nbuffer_store_dwordx4 v[60:63], v16, s[8:11], s19 offen offset:3072 sc1\nds_read_b128 v[24:27], v16 offset:38912\n" \
"v_mfma_f32_16x16x32_bf16 v[68:71], a[4:7], v[64:67], v[68:71]\nds_read_b128 v[56:59], v16 offset:7168\nv_mfma_f32_16x16x32_bf16 v[72:75], a[108:111], v[48:51], v[72:75]\nds_read_b128 v[60:63], v16 offset:39936\n" \
"v_mfma_f32_16x16x32_bf16 v[20:23], a[108:111], v[64:67], v[20:23]\ns_branch 1b\n2:\ns_cmp_lt_i32 s20, 1\n" \
"s_cbranch_scc1 3b\ns_add_u32 s30, s30, 1\ns_cmp_lt_u32 s30, 0x40000\ns_cbranch_scc0 9f\n" \
"v_cmp_eq_u32 vcc, -1, v36\ns_cbranch_vccz 6f\nbuffer_load_dwordx4 v16, s[8:11], s43 offen offset:0 sc1 lds\n6:\n" \
"v_cmp_eq_u32 vcc, -1, v80\ns_cbranch_vccz 6f\nbuffer_load_dwordx4 v16, s[8:11], s43 offen offset:1024 sc1 lds\n6:\n" \
"v_cmp_eq_u32 vcc, -1, v48\ns_cbranch_vccz 6f\nbuffer_load_dwordx4 v16, s[8:11], s43 offen offset:2048 sc1 lds\n6:\n" \
"v_cmp_eq_u32 vcc, -1, v84\ns_cbranch_vccz 6f\nbuffer_load_dwordx4 v16, s[8:11], s43 offen offset:3072 sc1 lds\n6:\n" \
"s_waitcnt vmcnt(0)\nds_read_b128 v[36:39], v61 offset:0\nds_read_b128 v[80:83], v61 offset:1024\nds_read_b128 v[48:51], v61 offset:2048\n" \
"ds_read_b128 v[84:87], v61 offset:3072\ns_waitcnt lgkmcnt(0)\nv_max3_u32 v52, v36, v80, v48\nv_max_u32 v52, v52, v84\n" \
"v_cmp_eq_u32 vcc, -1, v52\ns_cbranch_vccnz 2b\ns_branch 3b\n4:\n" \
"s_cmp_ge_i32 s20, 64\ns_cbranch_scc1 10f\ns_cmp_ge_i32 s20, 62\ns_cbranch_scc1 5b\n" \
"v_bfe_u32 v32, v16, 4, 3\nv_lshlrev_b32 v32, 2, v32\ns_lshl_b32 s43, s20, 5\ns_add_u32 s43, s43, 64\n" \
"s_add_u32 s43, s43, s26\n7:\nv_cmp_eq_u32 vcc, -1, v33\ns_cbranch_vccz 8f\n" \
"s_add_u32 s30, s30, 1\ns_cmp_lt_u32 s30, 0x40000\ns_cbranch_scc0 9f\ns_sleep 8\n" \
"buffer_load_dword v33, v32, s[8:11], s43 offen sc1\ns_waitcnt vmcnt(0)\ns_branch 7b\n8:\n" \
"s_mov_b32 s14, s10\ns_branch 5b\n10:\ns_waitcnt vmcnt(0) lgkmcnt(0)\n" \
"s_cmp_lt_i32 s6, 0\ns_cbranch_scc1 9f\ns_mov_b64 s[0:1], s[4:5]\ns_mov_b32 s2, s6\n" \
"v_lshrrev_b32 v0, 4, v16\nv_add_u32 v0, s7, v0\ns_barrier\ns_getpc_b64 s[8:9]\n" \
"11:\ns_add_u32 s8, s8, _Z5mega64Args-11b\ns_addc_u32 s9, s9, -1\ns_setpc_b64 s[8:9]\n" \
"9:\ns_waitcnt vmcnt(0)\ns_endpgm\n"
#define D_LOOP \
"s_branch 4f\n.p2align 6\n1:\ns_waitcnt lgkmcnt(0)\n" \
"v_mfma_f32_16x16x32_bf16 v[44:47], a[8:11], v[8:11], v[44:47]\nds_read_b128 v[16:19], v4 offset:3072\ns_mov_b32 m0, s24\nv_mfma_f32_16x16x32_bf16 v[64:67], a[8:11], v[12:15], v[64:67]\n" \
"ds_read_b128 v[32:35], v4 offset:35840\nbuffer_load_dword v1, s[16:19], 0 offen offset:0 sc1 lds\nv_mfma_f32_16x16x32_bf16 v[48:51], a[104:107], v[8:11], v[48:51]\ns_add_u32 m0, s23, 0\n" \
"s_and_b32 s98, s20, 1\ns_lshl_b32 s98, s98, 3\nv_mfma_f32_16x16x32_bf16 v[20:23], a[104:107], v[12:15], v[20:23]\nv_mfma_f32_16x16x32_bf16 v[56:59], a[200:203], v[8:11], v[56:59]\n" \
"v_mfma_f32_16x16x32_bf16 v[24:27], a[200:203], v[12:15], v[24:27]\nv_mfma_f32_16x16x32_bf16 v[60:63], v[152:155], v[8:11], v[60:63]\ns_waitcnt lgkmcnt(1)\nv_mfma_f32_16x16x32_bf16 v[28:31], v[152:155], v[12:15], v[28:31]\n" \
"s_waitcnt lgkmcnt(0)\nds_read_b128 v[8:11], v4 offset:4096\nv_mfma_f32_16x16x32_bf16 v[44:47], a[12:15], v[16:19], v[44:47]\nds_read_b128 v[12:15], v4 offset:36864\n" \
"v_mfma_f32_16x16x32_bf16 v[64:67], a[12:15], v[32:35], v[64:67]\nv_mfma_f32_16x16x32_bf16 v[48:51], a[108:111], v[16:19], v[48:51]\nv_mfma_f32_16x16x32_bf16 v[20:23], a[108:111], v[32:35], v[20:23]\nv_mfma_f32_16x16x32_bf16 v[56:59], a[204:207], v[16:19], v[56:59]\n" \
"v_mfma_f32_16x16x32_bf16 v[24:27], a[204:207], v[32:35], v[24:27]\nv_mfma_f32_16x16x32_bf16 v[60:63], v[156:159], v[16:19], v[60:63]\ns_waitcnt lgkmcnt(0)\nv_mfma_f32_16x16x32_bf16 v[28:31], v[156:159], v[32:35], v[28:31]\n" \
"ds_read_b128 v[16:19], v4 offset:5120\nv_mfma_f32_16x16x32_bf16 v[44:47], a[16:19], v[8:11], v[44:47]\nds_read_b128 v[32:35], v4 offset:37888\nv_mfma_f32_16x16x32_bf16 v[64:67], a[16:19], v[12:15], v[64:67]\n" \
"v_mfma_f32_16x16x32_bf16 v[48:51], a[112:115], v[8:11], v[48:51]\nv_mfma_f32_16x16x32_bf16 v[20:23], a[112:115], v[12:15], v[20:23]\nv_mfma_f32_16x16x32_bf16 v[56:59], a[208:211], v[8:11], v[56:59]\nv_mfma_f32_16x16x32_bf16 v[24:27], a[208:211], v[12:15], v[24:27]\n" \
"v_mfma_f32_16x16x32_bf16 v[60:63], v[160:163], v[8:11], v[60:63]\ns_waitcnt lgkmcnt(0)\nv_mfma_f32_16x16x32_bf16 v[28:31], v[160:163], v[12:15], v[28:31]\nds_read_b128 v[8:11], v4 offset:6144\n" \
"s_add_u32 s99, s20, 1\nv_mfma_f32_16x16x32_bf16 v[44:47], a[20:23], v[16:19], v[44:47]\nds_read_b128 v[12:15], v4 offset:38912\ns_lshr_b32 s97, s99, 1\n" \
"v_mfma_f32_16x16x32_bf16 v[64:67], a[20:23], v[32:35], v[64:67]\ns_and_b32 s99, s99, 1\ns_lshl_b32 s99, s99, 3\nv_readlane_b32 s97, v3, s97\n" \
"v_mfma_f32_16x16x32_bf16 v[48:51], a[116:119], v[16:19], v[48:51]\nv_mfma_f32_16x16x32_bf16 v[20:23], a[116:119], v[32:35], v[20:23]\ns_lshr_b32 s97, s97, s99\ns_and_b32 s97, s97, 0xff\n" \
"s_mul_i32 s97, s97, 0x38000\nv_mfma_f32_16x16x32_bf16 v[56:59], a[212:215], v[16:19], v[56:59]\ns_add_u32 s97, s97, s21\nv_mfma_f32_16x16x32_bf16 v[24:27], a[212:215], v[32:35], v[24:27]\n" \
"v_mfma_f32_16x16x32_bf16 v[60:63], v[164:167], v[16:19], v[60:63]\ns_waitcnt lgkmcnt(0)\nv_mfma_f32_16x16x32_bf16 v[28:31], v[164:167], v[32:35], v[28:31]\nds_read_b128 v[16:19], v4 offset:7168\n" \
"v_mfma_f32_16x16x32_bf16 v[44:47], a[24:27], v[8:11], v[44:47]\nds_read_b128 v[32:35], v4 offset:39936\nv_mfma_f32_16x16x32_bf16 v[64:67], a[24:27], v[12:15], v[64:67]\nv_mfma_f32_16x16x32_bf16 v[48:51], a[120:123], v[8:11], v[48:51]\n" \
"v_mfma_f32_16x16x32_bf16 v[20:23], a[120:123], v[12:15], v[20:23]\nv_mfma_f32_16x16x32_bf16 v[56:59], a[216:219], v[8:11], v[56:59]\nv_mfma_f32_16x16x32_bf16 v[24:27], a[216:219], v[12:15], v[24:27]\nv_mfma_f32_16x16x32_bf16 v[60:63], v[168:171], v[8:11], v[60:63]\n" \
"s_waitcnt lgkmcnt(0)\nv_mfma_f32_16x16x32_bf16 v[28:31], v[168:171], v[12:15], v[28:31]\nds_read_b128 v[8:11], v4 offset:8192\nv_mov_b32 v111, s20\n" \
"v_mfma_f32_16x16x32_bf16 v[44:47], a[28:31], v[16:19], v[44:47]\nds_read_b128 v[12:15], v4 offset:40960\ns_add_u32 s99, s20, 2\nv_mfma_f32_16x16x32_bf16 v[64:67], a[28:31], v[32:35], v[64:67]\n" \
"v_mfma_f32_16x16x32_bf16 v[48:51], a[124:127], v[16:19], v[48:51]\nv_mfma_f32_16x16x32_bf16 v[20:23], a[124:127], v[32:35], v[20:23]\nv_mfma_f32_16x16x32_bf16 v[56:59], a[220:223], v[16:19], v[56:59]\ns_waitcnt vmcnt(0)\n" \
"s_barrier\nv_mfma_f32_16x16x32_bf16 v[24:27], a[220:223], v[32:35], v[24:27]\nbuffer_store_dword v111, v2, s[8:11], s25 offen offset:0 sc1\nv_mfma_f32_16x16x32_bf16 v[60:63], v[172:175], v[16:19], v[60:63]\n" \
"ds_read_b32 v110, v5 offset:0\ns_mov_b32 s25, 0\nv_mfma_f32_16x16x32_bf16 v[28:31], v[172:175], v[32:35], v[28:31]\ns_waitcnt lgkmcnt(1)\n" \
"ds_read_b128 v[16:19], v4 offset:9216\nv_mfma_f32_16x16x32_bf16 v[44:47], a[32:35], v[8:11], v[44:47]\nds_read_b128 v[32:35], v4 offset:41984\nv_mfma_f32_16x16x32_bf16 v[64:67], a[32:35], v[12:15], v[64:67]\n" \
"v_mfma_f32_16x16x32_bf16 v[48:51], a[128:131], v[8:11], v[48:51]\ns_waitcnt lgkmcnt(2)\nv_cmp_gt_i32 s[94:95], s99, v110\nv_mfma_f32_16x16x32_bf16 v[20:23], a[128:131], v[12:15], v[20:23]\n" \
"v_mfma_f32_16x16x32_bf16 v[56:59], a[224:227], v[8:11], v[56:59]\ns_cmp_eq_u64 s[94:95], 0\ns_cselect_b32 s14, s10, 0\nv_mfma_f32_16x16x32_bf16 v[24:27], a[224:227], v[12:15], v[24:27]\n" \
"v_mfma_f32_16x16x32_bf16 v[60:63], v[176:179], v[8:11], v[60:63]\ns_waitcnt lgkmcnt(0)\nv_mfma_f32_16x16x32_bf16 v[28:31], v[176:179], v[12:15], v[28:31]\nds_read_b128 v[8:11], v4 offset:10240\n" \
"v_mfma_f32_16x16x32_bf16 v[44:47], a[36:39], v[16:19], v[44:47]\nds_read_b128 v[12:15], v4 offset:43008\nv_mfma_f32_16x16x32_bf16 v[64:67], a[36:39], v[32:35], v[64:67]\nv_mfma_f32_16x16x32_bf16 v[48:51], a[132:135], v[16:19], v[48:51]\n" \
"v_mfma_f32_16x16x32_bf16 v[20:23], a[132:135], v[32:35], v[20:23]\nbuffer_load_dwordx4 v0, s[12:15], s97 offen offset:0 sc1 lds\nv_mfma_f32_16x16x32_bf16 v[56:59], a[228:231], v[16:19], v[56:59]\nv_mfma_f32_16x16x32_bf16 v[24:27], a[228:231], v[32:35], v[24:27]\n" \
"v_mfma_f32_16x16x32_bf16 v[60:63], v[180:183], v[16:19], v[60:63]\ns_waitcnt lgkmcnt(0)\nv_mfma_f32_16x16x32_bf16 v[28:31], v[180:183], v[32:35], v[28:31]\nbuffer_load_dwordx4 v0, s[12:15], s97 offen offset:1024 sc1 lds\n" \
"v_mfma_f32_16x16x32_bf16 v[44:47], a[40:43], v[8:11], v[44:47]\nds_read_b128 v[16:19], v4 offset:11264\nv_mfma_f32_16x16x32_bf16 v[64:67], a[40:43], v[12:15], v[64:67]\nds_read_b128 v[32:35], v4 offset:44032\n" \
"v_mfma_f32_16x16x32_bf16 v[48:51], a[136:139], v[8:11], v[48:51]\nv_mfma_f32_16x16x32_bf16 v[20:23], a[136:139], v[12:15], v[20:23]\nbuffer_load_dwordx4 v0, s[12:15], s97 offen offset:2048 sc1 lds\nv_mfma_f32_16x16x32_bf16 v[56:59], a[232:235], v[8:11], v[56:59]\n" \
"v_mfma_f32_16x16x32_bf16 v[24:27], a[232:235], v[12:15], v[24:27]\nv_mfma_f32_16x16x32_bf16 v[60:63], v[184:187], v[8:11], v[60:63]\ns_waitcnt lgkmcnt(1)\nv_mfma_f32_16x16x32_bf16 v[28:31], v[184:187], v[12:15], v[28:31]\n" \
"s_waitcnt lgkmcnt(0)\nbuffer_load_dwordx4 v0, s[12:15], s97 offen offset:3072 sc1 lds\nv_mfma_f32_16x16x32_bf16 v[44:47], a[44:47], v[16:19], v[44:47]\nds_read_b128 v[8:11], v4 offset:12288\n" \
"s_add_u32 m0, s23, 0x1000\nv_mfma_f32_16x16x32_bf16 v[64:67], a[44:47], v[32:35], v[64:67]\nds_read_b128 v[12:15], v4 offset:45056\ns_add_u32 s97, s97, 0x1000\n" \
"v_mfma_f32_16x16x32_bf16 v[48:51], a[140:143], v[16:19], v[48:51]\nv_mfma_f32_16x16x32_bf16 v[20:23], a[140:143], v[32:35], v[20:23]\nbuffer_load_dwordx4 v0, s[12:15], s97 offen offset:0 sc1 lds\nv_mfma_f32_16x16x32_bf16 v[56:59], a[236:239], v[16:19], v[56:59]\n" \
"v_mfma_f32_16x16x32_bf16 v[24:27], a[236:239], v[32:35], v[24:27]\nv_mfma_f32_16x16x32_bf16 v[60:63], v[188:191], v[16:19], v[60:63]\ns_waitcnt lgkmcnt(1)\nv_mfma_f32_16x16x32_bf16 v[28:31], v[188:191], v[32:35], v[28:31]\n" \
"s_waitcnt lgkmcnt(0)\nbuffer_load_dwordx4 v0, s[12:15], s97 offen offset:1024 sc1 lds\nv_mfma_f32_16x16x32_bf16 v[44:47], a[48:51], v[8:11], v[44:47]\nds_read_b128 v[16:19], v4 offset:13312\n" \
"v_mfma_f32_16x16x32_bf16 v[64:67], a[48:51], v[12:15], v[64:67]\nds_read_b128 v[32:35], v4 offset:46080\nv_mfma_f32_16x16x32_bf16 v[48:51], a[144:147], v[8:11], v[48:51]\nv_mfma_f32_16x16x32_bf16 v[20:23], a[144:147], v[12:15], v[20:23]\n" \
"buffer_load_dwordx4 v0, s[12:15], s97 offen offset:2048 sc1 lds\nv_mfma_f32_16x16x32_bf16 v[56:59], a[240:243], v[8:11], v[56:59]\nv_mfma_f32_16x16x32_bf16 v[24:27], a[240:243], v[12:15], v[24:27]\nv_mfma_f32_16x16x32_bf16 v[60:63], v[192:195], v[8:11], v[60:63]\n" \
"s_waitcnt lgkmcnt(1)\nv_mfma_f32_16x16x32_bf16 v[28:31], v[192:195], v[12:15], v[28:31]\ns_waitcnt lgkmcnt(0)\nbuffer_load_dwordx4 v0, s[12:15], s97 offen offset:3072 sc1 lds\n" \
"v_mfma_f32_16x16x32_bf16 v[44:47], a[52:55], v[16:19], v[44:47]\nds_read_b128 v[8:11], v4 offset:14336\ns_add_u32 m0, s23, 0x2000\nv_mfma_f32_16x16x32_bf16 v[64:67], a[52:55], v[32:35], v[64:67]\n" \
"ds_read_b128 v[12:15], v4 offset:47104\ns_add_u32 s97, s97, 0x1000\nv_mfma_f32_16x16x32_bf16 v[48:51], a[148:151], v[16:19], v[48:51]\ns_xor_b32 s23, s23, 0x10000\n" \
"v_mfma_f32_16x16x32_bf16 v[20:23], a[148:151], v[32:35], v[20:23]\nbuffer_load_dwordx4 v0, s[12:15], s97 offen offset:0 sc1 lds\nv_mfma_f32_16x16x32_bf16 v[56:59], a[244:247], v[16:19], v[56:59]\nv_mfma_f32_16x16x32_bf16 v[24:27], a[244:247], v[32:35], v[24:27]\n" \
"v_mfma_f32_16x16x32_bf16 v[60:63], v[196:199], v[16:19], v[60:63]\ns_waitcnt lgkmcnt(1)\nv_mfma_f32_16x16x32_bf16 v[28:31], v[196:199], v[32:35], v[28:31]\ns_waitcnt lgkmcnt(0)\n" \
"buffer_load_dwordx4 v0, s[12:15], s97 offen offset:1024 sc1 lds\nv_mfma_f32_16x16x32_bf16 v[44:47], a[56:59], v[8:11], v[44:47]\nds_read_b128 v[16:19], v4 offset:15360\nv_mfma_f32_16x16x32_bf16 v[64:67], a[56:59], v[12:15], v[64:67]\n" \
"ds_read_b128 v[32:35], v4 offset:48128\nv_mfma_f32_16x16x32_bf16 v[48:51], a[152:155], v[8:11], v[48:51]\nv_mfma_f32_16x16x32_bf16 v[20:23], a[152:155], v[12:15], v[20:23]\nbuffer_load_dwordx4 v0, s[12:15], s97 offen offset:2048 sc1 lds\n" \
"v_mfma_f32_16x16x32_bf16 v[56:59], a[248:251], v[8:11], v[56:59]\nv_mfma_f32_16x16x32_bf16 v[24:27], a[248:251], v[12:15], v[24:27]\nv_mfma_f32_16x16x32_bf16 v[60:63], v[200:203], v[8:11], v[60:63]\ns_waitcnt lgkmcnt(1)\n" \
"v_mfma_f32_16x16x32_bf16 v[28:31], v[200:203], v[12:15], v[28:31]\ns_waitcnt lgkmcnt(0)\nbuffer_load_dwordx4 v0, s[12:15], s97 offen offset:3072 sc1 lds\nv_mfma_f32_16x16x32_bf16 v[44:47], a[60:63], v[16:19], v[44:47]\n" \
"ds_read_b128 v[8:11], v4 offset:16384\nv_mfma_f32_16x16x32_bf16 v[64:67], a[60:63], v[32:35], v[64:67]\nds_read_b128 v[12:15], v4 offset:49152\nv_mfma_f32_16x16x32_bf16 v[48:51], a[156:159], v[16:19], v[48:51]\n" \
"v_mfma_f32_16x16x32_bf16 v[20:23], a[156:159], v[32:35], v[20:23]\nv_mfma_f32_16x16x32_bf16 v[56:59], a[252:255], v[16:19], v[56:59]\nv_mfma_f32_16x16x32_bf16 v[24:27], a[252:255], v[32:35], v[24:27]\nv_mfma_f32_16x16x32_bf16 v[60:63], v[204:207], v[16:19], v[60:63]\n" \
"s_waitcnt lgkmcnt(1)\nv_mfma_f32_16x16x32_bf16 v[28:31], v[204:207], v[32:35], v[28:31]\ns_waitcnt lgkmcnt(0)\nds_read_b128 v[16:19], v4 offset:17408\n" \
"v_mfma_f32_16x16x32_bf16 v[44:47], a[64:67], v[8:11], v[44:47]\nds_read_b128 v[32:35], v4 offset:50176\nv_mfma_f32_16x16x32_bf16 v[64:67], a[64:67], v[12:15], v[64:67]\nv_mfma_f32_16x16x32_bf16 v[48:51], a[160:163], v[8:11], v[48:51]\n" \
"v_mfma_f32_16x16x32_bf16 v[20:23], a[160:163], v[12:15], v[20:23]\nv_mfma_f32_16x16x32_bf16 v[56:59], v[112:115], v[8:11], v[56:59]\nv_mfma_f32_16x16x32_bf16 v[24:27], v[112:115], v[12:15], v[24:27]\nv_mfma_f32_16x16x32_bf16 v[60:63], v[208:211], v[8:11], v[60:63]\n" \
"s_waitcnt lgkmcnt(0)\nv_mfma_f32_16x16x32_bf16 v[28:31], v[208:211], v[12:15], v[28:31]\nds_read_b128 v[8:11], v4 offset:18432\nv_mfma_f32_16x16x32_bf16 v[44:47], a[68:71], v[16:19], v[44:47]\n" \
"ds_read_b128 v[12:15], v4 offset:51200\nv_mfma_f32_16x16x32_bf16 v[64:67], a[68:71], v[32:35], v[64:67]\nv_mfma_f32_16x16x32_bf16 v[48:51], a[164:167], v[16:19], v[48:51]\nv_mfma_f32_16x16x32_bf16 v[20:23], a[164:167], v[32:35], v[20:23]\n" \
"s_lshr_b32 s99, s20, 1\ns_add_u32 s20, s20, 1\nv_readlane_b32 s99, v3, s99\nv_mfma_f32_16x16x32_bf16 v[56:59], v[116:119], v[16:19], v[56:59]\n" \
"v_mfma_f32_16x16x32_bf16 v[24:27], v[116:119], v[32:35], v[24:27]\ns_lshr_b32 s99, s99, s98\ns_and_b32 s99, s99, 0xff\ns_mul_i32 s99, s99, 0x50000\n" \
"v_mfma_f32_16x16x32_bf16 v[60:63], v[212:215], v[16:19], v[60:63]\ns_waitcnt lgkmcnt(0)\ns_add_u32 s99, s99, s22\ns_add_u32 s98, s99, 0x8000\n" \
"v_mfma_f32_16x16x32_bf16 v[28:31], v[212:215], v[32:35], v[28:31]\nds_read_b128 v[16:19], v4 offset:19456\nv_mfma_f32_16x16x32_bf16 v[44:47], a[72:75], v[8:11], v[44:47]\nds_read_b128 v[32:35], v4 offset:52224\n" \
"v_mfma_f32_16x16x32_bf16 v[64:67], a[72:75], v[12:15], v[64:67]\nv_mfma_f32_16x16x32_bf16 v[48:51], a[168:171], v[8:11], v[48:51]\nv_mfma_f32_16x16x32_bf16 v[20:23], a[168:171], v[12:15], v[20:23]\nv_mfma_f32_16x16x32_bf16 v[56:59], v[120:123], v[8:11], v[56:59]\n" \
"v_mfma_f32_16x16x32_bf16 v[24:27], v[120:123], v[12:15], v[24:27]\nv_mfma_f32_16x16x32_bf16 v[60:63], v[216:219], v[8:11], v[60:63]\ns_waitcnt lgkmcnt(0)\nv_mfma_f32_16x16x32_bf16 v[28:31], v[216:219], v[12:15], v[28:31]\n" \
"ds_read_b128 v[8:11], v4 offset:20480\nv_mfma_f32_16x16x32_bf16 v[44:47], a[76:79], v[16:19], v[44:47]\nds_read_b128 v[12:15], v4 offset:53248\nv_mfma_f32_16x16x32_bf16 v[64:67], a[76:79], v[32:35], v[64:67]\n" \
"v_mfma_f32_16x16x32_bf16 v[48:51], a[172:175], v[16:19], v[48:51]\nv_mfma_f32_16x16x32_bf16 v[20:23], a[172:175], v[32:35], v[20:23]\nv_mfma_f32_16x16x32_bf16 v[56:59], v[124:127], v[16:19], v[56:59]\nv_mfma_f32_16x16x32_bf16 v[24:27], v[124:127], v[32:35], v[24:27]\n" \
"v_mfma_f32_16x16x32_bf16 v[60:63], v[220:223], v[16:19], v[60:63]\ns_waitcnt lgkmcnt(0)\nv_mfma_f32_16x16x32_bf16 v[28:31], v[220:223], v[32:35], v[28:31]\nds_read_b128 v[36:39], v4 offset:21504\n" \
"v_mfma_f32_16x16x32_bf16 v[44:47], a[80:83], v[8:11], v[44:47]\nds_read_b128 v[32:35], v4 offset:54272\nv_mfma_f32_16x16x32_bf16 v[64:67], a[80:83], v[12:15], v[64:67]\nv_mfma_f32_16x16x32_bf16 v[48:51], a[176:179], v[8:11], v[48:51]\n" \
"v_mfma_f32_16x16x32_bf16 v[20:23], a[176:179], v[12:15], v[20:23]\nv_mfma_f32_16x16x32_bf16 v[56:59], v[128:131], v[8:11], v[56:59]\nv_mfma_f32_16x16x32_bf16 v[24:27], v[128:131], v[12:15], v[24:27]\nv_mfma_f32_16x16x32_bf16 v[60:63], v[224:227], v[8:11], v[60:63]\n" \
"s_waitcnt lgkmcnt(0)\nv_mfma_f32_16x16x32_bf16 v[28:31], v[224:227], v[12:15], v[28:31]\nds_read_b128 v[40:43], v4 offset:22528\nv_mfma_f32_16x16x32_bf16 v[44:47], a[84:87], v[36:39], v[44:47]\n" \
"ds_read_b128 v[16:19], v4 offset:55296\nv_xor_b32 v4, 0x10000, v4\nv_mfma_f32_16x16x32_bf16 v[64:67], a[84:87], v[32:35], v[64:67]\nv_mfma_f32_16x16x32_bf16 v[48:51], a[180:183], v[36:39], v[48:51]\n" \
"v_mfma_f32_16x16x32_bf16 v[20:23], a[180:183], v[32:35], v[20:23]\nv_mfma_f32_16x16x32_bf16 v[56:59], v[132:135], v[36:39], v[56:59]\nv_mfma_f32_16x16x32_bf16 v[24:27], v[132:135], v[32:35], v[24:27]\nv_mfma_f32_16x16x32_bf16 v[60:63], v[228:231], v[36:39], v[60:63]\n" \
"s_waitcnt lgkmcnt(0)\nv_mfma_f32_16x16x32_bf16 v[28:31], v[228:231], v[32:35], v[28:31]\ns_waitcnt vmcnt(0)\ns_barrier\n" \
"v_mfma_f32_16x16x32_bf16 v[44:47], a[88:91], v[40:43], v[44:47]\nds_read_b128 v[8:11], v4 offset:0\nv_mfma_f32_16x16x32_bf16 v[48:51], a[184:187], v[40:43], v[48:51]\nds_read_b128 v[12:15], v4 offset:32768\n" \
"v_mfma_f32_16x16x32_bf16 v[56:59], v[136:139], v[40:43], v[56:59]\nv_cvt_pk_bf16_f32 v36, v44, v45\nv_cvt_pk_bf16_f32 v37, v46, v47\nv_mfma_f32_16x16x32_bf16 v[60:63], v[232:235], v[40:43], v[60:63]\n" \
"v_cvt_pk_bf16_f32 v38, v48, v49\nv_cvt_pk_bf16_f32 v39, v50, v51\nv_mfma_f32_16x16x32_bf16 v[64:67], a[88:91], v[16:19], v[64:67]\nv_cvt_pk_bf16_f32 v32, v56, v57\n" \
"v_cvt_pk_bf16_f32 v33, v58, v59\nv_mfma_f32_16x16x32_bf16 v[20:23], a[184:187], v[16:19], v[20:23]\nbuffer_store_dwordx4 v[36:39], v6, s[8:11], s99 offen offset:0 sc1\nv_cvt_pk_bf16_f32 v34, v60, v61\n" \
"v_cvt_pk_bf16_f32 v35, v62, v63\nv_mfma_f32_16x16x32_bf16 v[24:27], v[136:139], v[16:19], v[24:27]\ns_waitcnt lgkmcnt(1)\nv_cvt_pk_bf16_f32 v36, v64, v65\n" \
"v_cvt_pk_bf16_f32 v37, v66, v67\nv_mfma_f32_16x16x32_bf16 v[28:31], v[232:235], v[16:19], v[28:31]\nds_read_b128 v[40:43], v4 offset:1024\nv_cvt_pk_bf16_f32 v38, v20, v21\n" \
"v_mfma_f32_16x16x32_bf16 v[44:47], a[0:3], v[8:11], 0\nds_read_b128 v[16:19], v4 offset:33792\nv_cvt_pk_bf16_f32 v39, v22, v23\nv_mfma_f32_16x16x32_bf16 v[48:51], a[96:99], v[8:11], 0\n" \
"buffer_store_dwordx4 v[32:35], v6, s[8:11], s99 offen offset:512 sc1\nv_cvt_pk_bf16_f32 v52, v24, v25\nv_cvt_pk_bf16_f32 v53, v26, v27\nv_cvt_pk_bf16_f32 v54, v28, v29\n" \
"v_mfma_f32_16x16x32_bf16 v[56:59], a[192:195], v[8:11], 0\ns_waitcnt lgkmcnt(2)\nv_cvt_pk_bf16_f32 v55, v30, v31\nv_mfma_f32_16x16x32_bf16 v[60:63], v[144:147], v[8:11], 0\n" \
"v_mfma_f32_16x16x32_bf16 v[64:67], a[0:3], v[12:15], 0\nv_mfma_f32_16x16x32_bf16 v[20:23], a[96:99], v[12:15], 0\nbuffer_store_dwordx4 v[36:39], v6, s[8:11], s98 offen offset:0 sc1\nv_mfma_f32_16x16x32_bf16 v[24:27], a[192:195], v[12:15], 0\n" \
"s_waitcnt lgkmcnt(0)\nv_mfma_f32_16x16x32_bf16 v[28:31], v[144:147], v[12:15], 0\nds_read_b128 v[8:11], v4 offset:2048\nv_mfma_f32_16x16x32_bf16 v[44:47], a[4:7], v[40:43], v[44:47]\n" \
"ds_read_b128 v[12:15], v4 offset:34816\nv_mfma_f32_16x16x32_bf16 v[64:67], a[4:7], v[16:19], v[64:67]\nbuffer_store_dwordx4 v[52:55], v6, s[8:11], s98 offen offset:512 sc1\nv_mfma_f32_16x16x32_bf16 v[48:51], a[100:103], v[40:43], v[48:51]\n" \
"v_mfma_f32_16x16x32_bf16 v[20:23], a[100:103], v[16:19], v[20:23]\nv_mfma_f32_16x16x32_bf16 v[56:59], a[196:199], v[40:43], v[56:59]\nv_mfma_f32_16x16x32_bf16 v[24:27], a[196:199], v[16:19], v[24:27]\nv_mfma_f32_16x16x32_bf16 v[60:63], v[148:151], v[40:43], v[60:63]\n" \
"s_cmp_eq_u32 s14, 0\nv_mfma_f32_16x16x32_bf16 v[28:31], v[148:151], v[16:19], v[28:31]\ns_cbranch_scc0 1b\n4:\n" \
"buffer_load_dword v68, v1, s[8:11], 0 offen sc1\nv_mov_b32 v69, s20\ns_add_u32 s98, s20, 1\ns_waitcnt vmcnt(0)\n" \
"v_cmp_gt_i32 vcc, s98, v68\ns_cbranch_vccz 8f\nbuffer_store_dword v69, v2, s[8:11], 0 offen sc1\ns_mov_b32 s25, 0x40000000\n" \
"s_cmp_ge_u32 s20, s28\ns_cbranch_scc1 9f\n7:\ns_add_u32 s26, s26, 1\n" \
"s_cmp_lt_u32 s26, 0x40000\ns_cbranch_scc0 9f\ns_sleep 8\nbuffer_load_dword v68, v1, s[8:11], 0 offen sc1\n" \
"s_waitcnt vmcnt(0)\nv_cmp_gt_i32 vcc, s98, v68\ns_cbranch_vccnz 7b\n8:\n" \
"s_lshr_b32 s98, s20, 1\nv_readlane_b32 s98, v3, s98\ns_and_b32 s99, s20, 1\ns_lshl_b32 s99, s99, 3\n" \
"s_lshr_b32 s98, s98, s99\ns_and_b32 s98, s98, 0xff\ns_mul_i32 s98, s98, 0x38000\ns_add_u32 s98, s98, s21\n" \
"s_xor_b32 s99, s23, 0x10000\ns_add_u32 m0, s99, 0\ns_add_u32 s98, s98, 0\nbuffer_load_dwordx4 v0, s[8:11], s98 offen offset:0 sc1 lds\n" \
"buffer_load_dwordx4 v0, s[8:11], s98 offen offset:1024 sc1 lds\nbuffer_load_dwordx4 v0, s[8:11], s98 offen offset:2048 sc1 lds\nbuffer_load_dwordx4 v0, s[8:11], s98 offen offset:3072 sc1 lds\ns_add_u32 m0, s99, 0x1000\n" \
"s_add_u32 s98, s98, 0x1000\nbuffer_load_dwordx4 v0, s[8:11], s98 offen offset:0 sc1 lds\nbuffer_load_dwordx4 v0, s[8:11], s98 offen offset:1024 sc1 lds\nbuffer_load_dwordx4 v0, s[8:11], s98 offen offset:2048 sc1 lds\n" \
"buffer_load_dwordx4 v0, s[8:11], s98 offen offset:3072 sc1 lds\ns_add_u32 m0, s99, 0x2000\ns_add_u32 s98, s98, 0x1000\nbuffer_load_dwordx4 v0, s[8:11], s98 offen offset:0 sc1 lds\n" \
"buffer_load_dwordx4 v0, s[8:11], s98 offen offset:1024 sc1 lds\nbuffer_load_dwordx4 v0, s[8:11], s98 offen offset:2048 sc1 lds\nbuffer_load_dwordx4 v0, s[8:11], s98 offen offset:3072 sc1 lds\nbuffer_store_dword v69, v2, s[8:11], s25 offen sc1\n" \
"s_mov_b32 s25, 0x40000000\ns_waitcnt vmcnt(1) lgkmcnt(0)\ns_barrier\nds_read_b128 v[68:71], v4 offset:0\n" \
"ds_read_b128 v[72:75], v4 offset:32768\nds_read_b128 v[76:79], v4 offset:1024\nds_read_b128 v[80:83], v4 offset:33792\nds_read_b128 v[8:11], v4 offset:2048\n" \
"ds_read_b128 v[12:15], v4 offset:34816\ns_waitcnt lgkmcnt(0)\nv_mfma_f32_16x16x32_bf16 v[44:47], a[0:3], v[68:71], 0\nv_mfma_f32_16x16x32_bf16 v[48:51], a[96:99], v[68:71], 0\n" \
"v_mfma_f32_16x16x32_bf16 v[56:59], a[192:195], v[68:71], 0\nv_mfma_f32_16x16x32_bf16 v[60:63], v[144:147], v[68:71], 0\nv_mfma_f32_16x16x32_bf16 v[64:67], a[0:3], v[72:75], 0\nv_mfma_f32_16x16x32_bf16 v[20:23], a[96:99], v[72:75], 0\n" \
"v_mfma_f32_16x16x32_bf16 v[24:27], a[192:195], v[72:75], 0\nv_mfma_f32_16x16x32_bf16 v[28:31], v[144:147], v[72:75], 0\nv_mfma_f32_16x16x32_bf16 v[44:47], a[4:7], v[76:79], v[44:47]\nv_mfma_f32_16x16x32_bf16 v[48:51], a[100:103], v[76:79], v[48:51]\n" \
"v_mfma_f32_16x16x32_bf16 v[56:59], a[196:199], v[76:79], v[56:59]\nv_mfma_f32_16x16x32_bf16 v[60:63], v[148:151], v[76:79], v[60:63]\nv_mfma_f32_16x16x32_bf16 v[64:67], a[4:7], v[80:83], v[64:67]\nv_mfma_f32_16x16x32_bf16 v[20:23], a[100:103], v[80:83], v[20:23]\n" \
"v_mfma_f32_16x16x32_bf16 v[24:27], a[196:199], v[80:83], v[24:27]\nv_mfma_f32_16x16x32_bf16 v[28:31], v[148:151], v[80:83], v[28:31]\ns_branch 1b\n9:\n" \
"s_waitcnt vmcnt(0)\ns_endpgm\n"
#define G_LOOP \
"s_branch 4f\n.p2align 6\n1:\ns_waitcnt lgkmcnt(2)\n" \
"v_mfma_f32_16x16x32_bf16 v[8:11], a[104:107], v[36:39], v[8:11]\ns_lshr_b32 s99, s20, 1\ns_and_b32 s98, s20, 1\nv_mov_b32 v110, s20\n" \
"v_mfma_f32_16x16x32_bf16 v[12:15], a[104:107], v[40:43], v[12:15]\ns_add_u32 s20, s20, 1\ns_add_u32 s97, s20, 1\ns_lshr_b32 s96, s97, 1\n" \
"v_mfma_f32_16x16x32_bf16 v[16:19], a[232:235], v[36:39], v[16:19]\nds_read_b128 v[52:55], v7 offset:28672\nv_readlane_b32 s96, v6, s96\nv_mfma_f32_16x16x32_bf16 v[20:23], a[232:235], v[40:43], v[20:23]\n" \
"ds_read_b128 v[56:59], v7 offset:61440\ns_and_b32 s97, s97, 1\nv_mfma_f32_16x16x32_bf16 v[24:27], v[216:219], v[36:39], v[24:27]\ns_lshl_b32 s97, s97, 3\n" \
"s_lshr_b32 s96, s96, s97\ns_and_b32 s96, s96, 0xff\nv_mfma_f32_16x16x32_bf16 v[28:31], v[216:219], v[40:43], v[28:31]\ns_waitcnt lgkmcnt(2)\n" \
"s_lshl_b32 s97, s96, 13\nv_xor_b32 v4, 0x400, v4\nv_mfma_f32_16x16x32_bf16 v[8:11], a[108:111], v[44:47], v[8:11]\ns_lshl_b32 s98, s98, 3\n" \
"v_mov_b32 v32, v111\nv_mov_b32 v33, v111\nv_mfma_f32_16x16x32_bf16 v[12:15], a[108:111], v[48:51], v[12:15]\ns_lshl_b32 s96, s96, 16\n" \
"v_mul_f32 v252, 0xc0135761, v34\nv_mul_f32 v34, v34, v34\nv_mfma_f32_16x16x32_bf16 v[16:19], a[236:239], v[44:47], v[16:19]\nds_read_b128 v[36:39], v7 offset:29696\n" \
"v_readlane_b32 s99, v6, s99\nv_mfma_f32_16x16x32_bf16 v[20:23], a[236:239], v[48:51], v[20:23]\nds_read_b128 v[40:43], v7 offset:62464\nv_mul_f32 v34, v34, v252\n" \
"v_mfma_f32_16x16x32_bf16 v[24:27], v[220:223], v[44:47], v[24:27]\nds_read_b128 v[60:63], v7 offset:30720\nv_mul_f32 v254, v35, v35\nv_mfma_f32_16x16x32_bf16 v[28:31], v[220:223], v[48:51], v[28:31]\n" \
"s_waitcnt lgkmcnt(3)\nv_permlane16_swap_b32 v32, v33\nv_mfma_f32_16x16x32_bf16 v[8:11], a[112:115], v[52:55], v[8:11]\nds_read_b128 v[44:47], v7 offset:63488\n" \
"v_mul_f32 v253, 0x3d372713, v34\nv_mfma_f32_16x16x32_bf16 v[12:15], a[112:115], v[56:59], v[12:15]\ns_lshr_b32 s99, s99, s98\ns_and_b32 s99, s99, 0xff\n" \
"s_mul_i32 s99, s99, 0x38000\nv_mfma_f32_16x16x32_bf16 v[16:19], a[240:243], v[52:55], v[16:19]\nds_read_b128 v[48:51], v7 offset:31744\ns_or_b32 s16, s99, s18\n" \
"v_mfma_f32_16x16x32_bf16 v[20:23], a[240:243], v[56:59], v[20:23]\ns_or_b32 s17, s99, s19\ns_add_u32 s96, s96, s21\nv_mfma_f32_16x16x32_bf16 v[24:27], v[224:227], v[52:55], v[24:27]\n" \
"ds_read_b128 v[64:67], v7 offset:64512\nv_xor_b32 v7, 0x10000, v7\nv_mfma_f32_16x16x32_bf16 v[28:31], v[224:227], v[56:59], v[28:31]\ns_waitcnt lgkmcnt(3)\n" \
"buffer_load_dwordx4 v[52:55], v1, s[8:11], s97 offen offset:0 sc1\nv_mfma_f32_16x16x32_bf16 v[8:11], a[116:119], v[36:39], v[8:11]\nbuffer_load_dwordx4 v[56:59], v1, s[8:11], s97 offen offset:1024 sc1\nv_mfma_f32_16x16x32_bf16 v[12:15], a[116:119], v[40:43], v[12:15]\n" \
"v_mfma_f32_16x16x32_bf16 v[16:19], a[244:247], v[36:39], v[16:19]\ns_waitcnt vmcnt(5) lgkmcnt(0)\ns_barrier\nv_mfma_f32_16x16x32_bf16 v[20:23], a[244:247], v[40:43], v[20:23]\n" \
"ds_read_b128 v[68:71], v4 offset:0\nv_mfma_f32_16x16x32_bf16 v[24:27], v[228:231], v[36:39], v[24:27]\nds_read_b128 v[72:75], v7 offset:0\nv_mfma_f32_16x16x32_bf16 v[28:31], v[228:231], v[40:43], v[28:31]\n" \
"ds_read_b128 v[36:39], v7 offset:32768\nv_mfma_f32_16x16x32_bf16 v[8:11], a[120:123], v[60:63], v[8:11]\nds_read_b128 v[40:43], v7 offset:1024\nv_mfma_f32_16x16x32_bf16 v[12:15], a[120:123], v[44:47], v[12:15]\n" \
"s_waitcnt lgkmcnt(3)\nv_add_f32 v68, v68, v69\nv_mfma_f32_16x16x32_bf16 v[8:11], a[124:127], v[48:51], v[8:11]\nds_read_b128 v[76:79], v7 offset:33792\n" \
"v_add_f32 v70, v70, v71\nv_mfma_f32_16x16x32_bf16 v[12:15], a[124:127], v[64:67], v[12:15]\nv_cmp_u_f32 s[98:99], v68, v70\nv_mfma_f32_16x16x32_bf16 v[16:19], a[248:251], v[60:63], v[16:19]\n" \
"v_fma_f32 v68, v32, v240, v8\nv_fma_f32 v69, v32, v241, v9\nv_mfma_f32_16x16x32_bf16 v[20:23], a[248:251], v[44:47], v[20:23]\ns_waitcnt vmcnt(2)\n" \
"buffer_store_dword v110, v3, s[8:11], s23 offen offset:0 sc1\nv_mfma_f32_16x16x32_bf16 v[16:19], a[252:255], v[48:51], v[16:19]\ns_mov_b32 s23, 0\nv_fma_f32 v70, v32, v242, v10\n" \
"v_fma_f32 v71, v32, v243, v11\nv_mfma_f32_16x16x32_bf16 v[20:23], a[252:255], v[64:67], v[20:23]\ns_cmp_lt_u32 s20, s26\ns_cselect_b32 s98, s98, -1\n" \
"v_fma_f32 v80, v33, v240, v12\nv_mfma_f32_16x16x32_bf16 v[24:27], v[232:235], v[60:63], v[24:27]\nv_fma_f32 v81, v33, v241, v13\nv_fma_f32 v82, v33, v242, v14\n" \
"v_mfma_f32_16x16x32_bf16 v[28:31], v[232:235], v[44:47], v[28:31]\nv_fma_f32 v83, v33, v243, v15\nv_fma_f32 v44, v32, v244, v16\nv_mfma_f32_16x16x32_bf16 v[24:27], v[236:239], v[48:51], v[24:27]\n" \
"s_waitcnt lgkmcnt(0)\nv_fma_f32 v45, v32, v245, v17\nv_fma_f32 v46, v32, v246, v18\nv_mfma_f32_16x16x32_bf16 v[28:31], v[236:239], v[64:67], v[28:31]\n" \
"s_cmp_lg_u64 s[98:99], 0\ns_cbranch_scc1 4f\n5:\nv_mfma_f32_16x16x32_bf16 v[8:11], a[0:3], v[72:75], 0\n" \
"s_xor_b32 m0, s22, 0\nv_fma_f32 v47, v32, v247, v19\nv_fma_f32 v48, v33, v244, v20\nv_mfma_f32_16x16x32_bf16 v[12:15], a[0:3], v[36:39], 0\n" \
"v_fma_f32 v49, v33, v245, v21\nv_fma_f32 v50, v33, v246, v22\nv_mfma_f32_16x16x32_bf16 v[8:11], a[4:7], v[40:43], v[8:11]\nds_read_b128 v[60:63], v7 offset:2048\n" \
"v_fma_f32 v51, v33, v247, v23\nv_mfma_f32_16x16x32_bf16 v[12:15], a[4:7], v[76:79], v[12:15]\nds_read_b128 v[64:67], v7 offset:34816\nv_fma_f32 v84, v32, v248, v24\n" \
"v_mfma_f32_16x16x32_bf16 v[16:19], a[128:131], v[72:75], 0\nv_fma_f32 v85, v32, v249, v25\nv_fma_f32 v86, v32, v250, v26\nv_mfma_f32_16x16x32_bf16 v[20:23], a[128:131], v[36:39], 0\n" \
"v_fma_f32 v87, v32, v251, v27\nv_fma_f32 v88, v33, v248, v28\nv_mfma_f32_16x16x32_bf16 v[16:19], a[132:135], v[40:43], v[16:19]\nv_fma_f32 v89, v33, v249, v29\n" \
"v_fma_f32 v90, v33, v250, v30\nv_mfma_f32_16x16x32_bf16 v[20:23], a[132:135], v[76:79], v[20:23]\nv_fma_f32 v91, v33, v251, v31\nv_xor_b32 v5, 0x400, v5\n" \
"v_mfma_f32_16x16x32_bf16 v[24:27], v[112:115], v[72:75], 0\nds_read_b128 v[92:95], v7 offset:3072\nv_mfma_f32_16x16x32_bf16 v[28:31], v[112:115], v[36:39], 0\nds_read_b128 v[72:75], v7 offset:35840\n" \
"v_mfma_f32_16x16x32_bf16 v[24:27], v[116:119], v[40:43], v[24:27]\ns_waitcnt lgkmcnt(2)\nv_permlane16_swap_b32 v68, v80\nv_mfma_f32_16x16x32_bf16 v[28:31], v[116:119], v[76:79], v[28:31]\n" \
"v_permlane16_swap_b32 v69, v81\nv_mfma_f32_16x16x32_bf16 v[8:11], a[8:11], v[60:63], v[8:11]\nv_permlane16_swap_b32 v70, v82\nv_mfma_f32_16x16x32_bf16 v[12:15], a[8:11], v[64:67], v[12:15]\n" \
"v_permlane16_swap_b32 v71, v83\nv_mfma_f32_16x16x32_bf16 v[16:19], a[136:139], v[60:63], v[16:19]\nds_read_b128 v[36:39], v7 offset:4096\nv_mul_f32 v40, v68, v68\n" \
"v_mfma_f32_16x16x32_bf16 v[20:23], a[136:139], v[64:67], v[20:23]\nds_read_b128 v[76:79], v7 offset:36864\nv_mul_f32 v41, v69, v69\nv_mfma_f32_16x16x32_bf16 v[24:27], v[120:123], v[60:63], v[24:27]\n" \
"s_waitcnt lgkmcnt(2)\nv_mul_f32 v42, v70, v70\nv_mul_f32 v43, v71, v71\nv_mfma_f32_16x16x32_bf16 v[28:31], v[120:123], v[64:67], v[28:31]\n" \
"v_fma_f32 v40, v40, v253, v252\nv_fma_f32 v41, v41, v253, v252\nv_mfma_f32_16x16x32_bf16 v[8:11], a[12:15], v[92:95], v[8:11]\nv_fma_f32 v42, v42, v253, v252\n" \
"v_fma_f32 v43, v43, v253, v252\nv_mfma_f32_16x16x32_bf16 v[12:15], a[12:15], v[72:75], v[12:15]\nv_mul_f32 v40, v40, v68\nv_mul_f32 v41, v41, v69\n" \
"v_mfma_f32_16x16x32_bf16 v[16:19], a[140:143], v[92:95], v[16:19]\nds_read_b128 v[60:63], v7 offset:5120\nv_mul_f32 v42, v42, v70\nv_mfma_f32_16x16x32_bf16 v[20:23], a[140:143], v[72:75], v[20:23]\n" \
"ds_read_b128 v[64:67], v7 offset:37888\nv_mul_f32 v43, v43, v71\nv_mfma_f32_16x16x32_bf16 v[24:27], v[124:127], v[92:95], v[24:27]\ns_waitcnt lgkmcnt(2)\n" \
"v_exp_f32 v40, v40\nv_mfma_f32_16x16x32_bf16 v[28:31], v[124:127], v[72:75], v[28:31]\nv_exp_f32 v41, v41\nv_mfma_f32_16x16x32_bf16 v[8:11], a[16:19], v[36:39], v[8:11]\n" \
"v_exp_f32 v42, v42\nv_mfma_f32_16x16x32_bf16 v[12:15], a[16:19], v[76:79], v[12:15]\nv_exp_f32 v43, v43\nv_mfma_f32_16x16x32_bf16 v[16:19], a[144:147], v[36:39], v[16:19]\n" \
"ds_read_b128 v[72:75], v7 offset:6144\nv_fma_f32 v40, v40, v254, v254\nv_mfma_f32_16x16x32_bf16 v[20:23], a[144:147], v[76:79], v[20:23]\nds_read_b128 v[92:95], v7 offset:38912\n" \
"v_fma_f32 v41, v41, v254, v254\nv_mfma_f32_16x16x32_bf16 v[24:27], v[128:131], v[36:39], v[24:27]\ns_waitcnt lgkmcnt(2)\nv_rcp_f32 v40, v40\n" \
"v_mfma_f32_16x16x32_bf16 v[28:31], v[128:131], v[76:79], v[28:31]\nv_rcp_f32 v41, v41\nv_mfma_f32_16x16x32_bf16 v[8:11], a[20:23], v[60:63], v[8:11]\nv_fma_f32 v42, v42, v254, v254\n" \
"v_fma_f32 v43, v43, v254, v254\nv_mfma_f32_16x16x32_bf16 v[12:15], a[20:23], v[64:67], v[12:15]\nv_rcp_f32 v42, v42\nv_mfma_f32_16x16x32_bf16 v[16:19], a[148:151], v[60:63], v[16:19]\n" \
"ds_read_b128 v[36:39], v7 offset:7168\nv_mul_f32 v80, v68, v80\nv_mfma_f32_16x16x32_bf16 v[20:23], a[148:151], v[64:67], v[20:23]\ns_waitcnt vmcnt(1)\n" \
"v_max3_u32 v35, v52, v54, v56\nv_max_u32 v35, v35, v58\nv_mfma_f32_16x16x32_bf16 v[24:27], v[132:135], v[60:63], v[24:27]\nds_read_b128 v[76:79], v7 offset:39936\n" \
"v_cmp_eq_u32 s[98:99], -1, v35\nv_mfma_f32_16x16x32_bf16 v[28:31], v[132:135], v[64:67], v[28:31]\ns_waitcnt lgkmcnt(2)\nv_rcp_f32 v43, v43\n" \
"v_mfma_f32_16x16x32_bf16 v[8:11], a[24:27], v[72:75], v[8:11]\ns_cmp_eq_u64 s[98:99], 0\ns_cselect_b32 s14, s10, 0\nv_mul_f32 v81, v69, v81\n" \
"v_mfma_f32_16x16x32_bf16 v[12:15], a[24:27], v[92:95], v[12:15]\nbuffer_load_dwordx4 v0, s[12:15], s96 offen offset:0 sc1 lds\nv_mfma_f32_16x16x32_bf16 v[16:19], a[152:155], v[72:75], v[16:19]\nds_read_b128 v[60:63], v7 offset:8192\n" \
"v_mul_f32 v82, v70, v82\nv_mfma_f32_16x16x32_bf16 v[20:23], a[152:155], v[92:95], v[20:23]\nds_read_b128 v[64:67], v7 offset:40960\nv_mul_f32 v83, v71, v83\n" \
"v_mfma_f32_16x16x32_bf16 v[24:27], v[136:139], v[72:75], v[24:27]\ns_waitcnt lgkmcnt(2)\nv_mul_f32 v80, v80, v40\nv_mul_f32 v81, v81, v41\n" \
"v_mfma_f32_16x16x32_bf16 v[28:31], v[136:139], v[92:95], v[28:31]\nv_mul_f32 v82, v82, v42\nv_mul_f32 v83, v83, v43\nv_mfma_f32_16x16x32_bf16 v[8:11], a[28:31], v[36:39], v[8:11]\n" \
"v_cvt_pk_bf16_f32 v80, v80, v81\nv_cvt_pk_bf16_f32 v81, v82, v83\nv_mfma_f32_16x16x32_bf16 v[12:15], a[28:31], v[76:79], v[12:15]\nbuffer_load_dwordx4 v0, s[12:15], s96 offen offset:1024 sc1 lds\n" \
"v_mfma_f32_16x16x32_bf16 v[16:19], a[156:159], v[36:39], v[16:19]\nds_read_b128 v[40:43], v7 offset:9216\nv_mfma_f32_16x16x32_bf16 v[20:23], a[156:159], v[76:79], v[20:23]\nds_read_b128 v[68:71], v7 offset:41984\n" \
"v_mfma_f32_16x16x32_bf16 v[24:27], v[140:143], v[36:39], v[24:27]\ns_waitcnt lgkmcnt(2)\nv_permlane16_swap_b32 v44, v48\nv_mfma_f32_16x16x32_bf16 v[28:31], v[140:143], v[76:79], v[28:31]\n" \
"buffer_load_dwordx4 v0, s[12:15], s96 offen offset:2048 sc1 lds\nv_mfma_f32_16x16x32_bf16 v[8:11], a[32:35], v[60:63], v[8:11]\nv_permlane16_swap_b32 v45, v49\nv_mfma_f32_16x16x32_bf16 v[12:15], a[32:35], v[64:67], v[12:15]\n" \
"v_permlane16_swap_b32 v46, v50\nv_mfma_f32_16x16x32_bf16 v[16:19], a[160:163], v[60:63], v[16:19]\nds_read_b128 v[36:39], v7 offset:10240\nv_mul_f32 v72, v44, v44\n" \
"v_mfma_f32_16x16x32_bf16 v[20:23], a[160:163], v[64:67], v[20:23]\nds_read_b128 v[76:79], v7 offset:43008\nv_mul_f32 v73, v45, v45\nv_mfma_f32_16x16x32_bf16 v[24:27], v[144:147], v[60:63], v[24:27]\n" \
"s_waitcnt lgkmcnt(2)\nbuffer_load_dwordx4 v0, s[12:15], s96 offen offset:3072 sc1 lds\nv_mfma_f32_16x16x32_bf16 v[28:31], v[144:147], v[64:67], v[28:31]\ns_xor_b32 m0, s22, 0x1000\n" \
"s_xor_b32 s96, s96, 0x1000\nv_mul_f32 v74, v46, v46\nv_mfma_f32_16x16x32_bf16 v[8:11], a[36:39], v[40:43], v[8:11]\nv_permlane16_swap_b32 v47, v51\n" \
"v_mfma_f32_16x16x32_bf16 v[12:15], a[36:39], v[68:71], v[12:15]\nv_mul_f32 v75, v47, v47\nv_fma_f32 v72, v72, v253, v252\nv_mfma_f32_16x16x32_bf16 v[16:19], a[164:167], v[40:43], v[16:19]\n" \
"ds_read_b128 v[60:63], v7 offset:11264\nv_fma_f32 v73, v73, v253, v252\nv_mfma_f32_16x16x32_bf16 v[20:23], a[164:167], v[68:71], v[20:23]\nbuffer_load_dwordx4 v0, s[12:15], s96 offen offset:0 sc1 lds\n" \
"v_mfma_f32_16x16x32_bf16 v[24:27], v[148:151], v[40:43], v[24:27]\nds_read_b128 v[64:67], v7 offset:44032\nv_fma_f32 v74, v74, v253, v252\nv_mfma_f32_16x16x32_bf16 v[28:31], v[148:151], v[68:71], v[28:31]\n" \
"s_waitcnt lgkmcnt(2)\nv_fma_f32 v75, v75, v253, v252\nv_mul_f32 v72, v72, v44\nv_mfma_f32_16x16x32_bf16 v[8:11], a[40:43], v[36:39], v[8:11]\n" \
"v_exp_f32 v72, v72\nv_mfma_f32_16x16x32_bf16 v[12:15], a[40:43], v[76:79], v[12:15]\nv_mul_f32 v73, v73, v45\nv_mul_f32 v74, v74, v46\n" \
"v_mfma_f32_16x16x32_bf16 v[16:19], a[168:171], v[36:39], v[16:19]\nbuffer_load_dwordx4 v0, s[12:15], s96 offen offset:1024 sc1 lds\nv_mfma_f32_16x16x32_bf16 v[20:23], a[168:171], v[76:79], v[20:23]\nds_read_b128 v[40:43], v7 offset:12288\n" \
"v_mul_f32 v75, v75, v47\nv_mfma_f32_16x16x32_bf16 v[24:27], v[152:155], v[36:39], v[24:27]\nds_read_b128 v[68:71], v7 offset:45056\nv_fma_f32 v72, v72, v254, v254\n" \
"v_mfma_f32_16x16x32_bf16 v[28:31], v[152:155], v[76:79], v[28:31]\ns_waitcnt lgkmcnt(2)\nv_exp_f32 v73, v73\nv_mfma_f32_16x16x32_bf16 v[8:11], a[44:47], v[60:63], v[8:11]\n" \
"v_exp_f32 v74, v74\nv_mfma_f32_16x16x32_bf16 v[12:15], a[44:47], v[64:67], v[12:15]\nbuffer_load_dwordx4 v0, s[12:15], s96 offen offset:2048 sc1 lds\nv_mfma_f32_16x16x32_bf16 v[16:19], a[172:175], v[60:63], v[16:19]\n" \
"ds_read_b128 v[36:39], v7 offset:13312\nv_fma_f32 v73, v73, v254, v254\nv_mfma_f32_16x16x32_bf16 v[20:23], a[172:175], v[64:67], v[20:23]\nds_read_b128 v[76:79], v7 offset:46080\n" \
"v_fma_f32 v74, v74, v254, v254\nv_mfma_f32_16x16x32_bf16 v[24:27], v[156:159], v[60:63], v[24:27]\ns_waitcnt lgkmcnt(2)\nv_exp_f32 v75, v75\n" \
"v_mfma_f32_16x16x32_bf16 v[28:31], v[156:159], v[64:67], v[28:31]\nv_rcp_f32 v72, v72\nv_mfma_f32_16x16x32_bf16 v[8:11], a[48:51], v[40:43], v[8:11]\nbuffer_load_dwordx4 v0, s[12:15], s96 offen offset:3072 sc1 lds\n" \
"v_mfma_f32_16x16x32_bf16 v[12:15], a[48:51], v[68:71], v[12:15]\ns_xor_b32 m0, s22, 0x8000\ns_xor_b32 s96, s96, 0x9000\nv_fma_f32 v75, v75, v254, v254\n" \
"v_mfma_f32_16x16x32_bf16 v[16:19], a[176:179], v[40:43], v[16:19]\nds_read_b128 v[60:63], v7 offset:14336\nv_mul_f32 v48, v44, v48\nv_mfma_f32_16x16x32_bf16 v[20:23], a[176:179], v[68:71], v[20:23]\n" \
"ds_read_b128 v[64:67], v7 offset:47104\nv_mul_f32 v49, v45, v49\nv_mfma_f32_16x16x32_bf16 v[24:27], v[160:163], v[40:43], v[24:27]\ns_waitcnt lgkmcnt(2)\n" \
"v_rcp_f32 v73, v73\nv_mfma_f32_16x16x32_bf16 v[28:31], v[160:163], v[68:71], v[28:31]\nbuffer_load_dwordx4 v0, s[12:15], s96 offen offset:0 sc1 lds\nv_mfma_f32_16x16x32_bf16 v[8:11], a[52:55], v[36:39], v[8:11]\n" \
"v_rcp_f32 v74, v74\nv_mfma_f32_16x16x32_bf16 v[12:15], a[52:55], v[76:79], v[12:15]\nv_rcp_f32 v75, v75\nv_mfma_f32_16x16x32_bf16 v[16:19], a[180:183], v[36:39], v[16:19]\n" \
"ds_read_b128 v[40:43], v7 offset:15360\nv_mul_f32 v50, v46, v50\nv_mfma_f32_16x16x32_bf16 v[20:23], a[180:183], v[76:79], v[20:23]\nds_read_b128 v[68:71], v7 offset:48128\n" \
"v_mul_f32 v51, v47, v51\nv_mfma_f32_16x16x32_bf16 v[24:27], v[164:167], v[36:39], v[24:27]\ns_waitcnt lgkmcnt(2)\nbuffer_load_dwordx4 v0, s[12:15], s96 offen offset:1024 sc1 lds\n" \
"v_mfma_f32_16x16x32_bf16 v[28:31], v[164:167], v[76:79], v[28:31]\nv_mul_f32 v48, v48, v72\nv_mul_f32 v49, v49, v73\nv_mfma_f32_16x16x32_bf16 v[8:11], a[56:59], v[60:63], v[8:11]\n" \
"v_mul_f32 v50, v50, v74\nv_mul_f32 v51, v51, v75\nv_mfma_f32_16x16x32_bf16 v[12:15], a[56:59], v[64:67], v[12:15]\nv_cvt_pk_bf16_f32 v48, v48, v49\n" \
"v_cvt_pk_bf16_f32 v49, v50, v51\nv_mfma_f32_16x16x32_bf16 v[16:19], a[184:187], v[60:63], v[16:19]\nds_read_b128 v[36:39], v7 offset:16384\nv_mfma_f32_16x16x32_bf16 v[20:23], a[184:187], v[64:67], v[20:23]\n" \
"buffer_load_dwordx4 v0, s[12:15], s96 offen offset:2048 sc1 lds\nv_mfma_f32_16x16x32_bf16 v[24:27], v[168:171], v[60:63], v[24:27]\ns_waitcnt lgkmcnt(1)\nds_read_b128 v[44:47], v7 offset:49152\n" \
"v_mfma_f32_16x16x32_bf16 v[28:31], v[168:171], v[64:67], v[28:31]\nv_permlane16_swap_b32 v84, v88\nv_mfma_f32_16x16x32_bf16 v[8:11], a[60:63], v[40:43], v[8:11]\nv_permlane16_swap_b32 v85, v89\n" \
"v_mfma_f32_16x16x32_bf16 v[12:15], a[60:63], v[68:71], v[12:15]\nv_permlane16_swap_b32 v86, v90\nv_mfma_f32_16x16x32_bf16 v[16:19], a[188:191], v[40:43], v[16:19]\nbuffer_load_dwordx4 v0, s[12:15], s96 offen offset:3072 sc1 lds\n" \
"v_mfma_f32_16x16x32_bf16 v[20:23], a[188:191], v[68:71], v[20:23]\nds_read_b128 v[60:63], v7 offset:17408\ns_xor_b32 m0, s22, 0x9000\nv_mfma_f32_16x16x32_bf16 v[24:27], v[172:175], v[40:43], v[24:27]\n" \
"ds_read_b128 v[64:67], v7 offset:50176\ns_xor_b32 s96, s96, 0x1000\nv_mfma_f32_16x16x32_bf16 v[28:31], v[172:175], v[68:71], v[28:31]\ns_waitcnt lgkmcnt(2)\n" \
"v_permlane16_swap_b32 v87, v91\nv_mfma_f32_16x16x32_bf16 v[8:11], a[64:67], v[36:39], v[8:11]\ns_xor_b32 s22, s22, 0x10000\nv_mul_f32 v40, v84, v84\n" \
"v_mul_f32 v41, v85, v85\nv_mfma_f32_16x16x32_bf16 v[12:15], a[64:67], v[44:47], v[12:15]\nbuffer_load_dwordx4 v0, s[12:15], s96 offen offset:0 sc1 lds\nv_mfma_f32_16x16x32_bf16 v[16:19], a[192:195], v[36:39], v[16:19]\n" \
"ds_read_b128 v[68:71], v7 offset:18432\nv_mul_f32 v42, v86, v86\nv_mfma_f32_16x16x32_bf16 v[20:23], a[192:195], v[44:47], v[20:23]\nds_read_b128 v[72:75], v7 offset:51200\n" \
"v_mul_f32 v43, v87, v87\nv_mfma_f32_16x16x32_bf16 v[24:27], v[176:179], v[36:39], v[24:27]\ns_waitcnt lgkmcnt(2)\nv_fma_f32 v40, v40, v253, v252\n" \
"v_fma_f32 v41, v41, v253, v252\nv_mfma_f32_16x16x32_bf16 v[28:31], v[176:179], v[44:47], v[28:31]\nv_fma_f32 v42, v42, v253, v252\nv_fma_f32 v43, v43, v253, v252\n" \
"v_mfma_f32_16x16x32_bf16 v[8:11], a[68:71], v[60:63], v[8:11]\nbuffer_load_dwordx4 v0, s[12:15], s96 offen offset:1024 sc1 lds\nv_mfma_f32_16x16x32_bf16 v[12:15], a[68:71], v[64:67], v[12:15]\nv_mul_f32 v40, v40, v84\n" \
"v_mul_f32 v41, v41, v85\nv_mfma_f32_16x16x32_bf16 v[16:19], a[196:199], v[60:63], v[16:19]\nds_read_b128 v[36:39], v7 offset:19456\nv_mul_f32 v42, v42, v86\n" \
"v_mfma_f32_16x16x32_bf16 v[20:23], a[196:199], v[64:67], v[20:23]\nds_read_b128 v[44:47], v7 offset:52224\nv_mul_f32 v43, v43, v87\nv_mfma_f32_16x16x32_bf16 v[24:27], v[180:183], v[60:63], v[24:27]\n" \
"s_waitcnt lgkmcnt(2)\nv_exp_f32 v40, v40\nv_mfma_f32_16x16x32_bf16 v[28:31], v[180:183], v[64:67], v[28:31]\nbuffer_load_dwordx4 v0, s[12:15], s96 offen offset:2048 sc1 lds\n" \
"v_mfma_f32_16x16x32_bf16 v[8:11], a[72:75], v[68:71], v[8:11]\nv_exp_f32 v41, v41\nv_mfma_f32_16x16x32_bf16 v[12:15], a[72:75], v[72:75], v[12:15]\nv_exp_f32 v42, v42\n" \
"v_mfma_f32_16x16x32_bf16 v[16:19], a[200:203], v[68:71], v[16:19]\nds_read_b128 v[60:63], v7 offset:20480\nv_fma_f32 v40, v40, v254, v254\nv_mfma_f32_16x16x32_bf16 v[20:23], a[200:203], v[72:75], v[20:23]\n" \
"ds_read_b128 v[64:67], v7 offset:53248\nv_fma_f32 v41, v41, v254, v254\nv_mfma_f32_16x16x32_bf16 v[24:27], v[184:187], v[68:71], v[24:27]\ns_waitcnt lgkmcnt(2)\n" \
"buffer_load_dwordx4 v0, s[12:15], s96 offen offset:3072 sc1 lds\nv_mfma_f32_16x16x32_bf16 v[28:31], v[184:187], v[72:75], v[28:31]\nv_exp_f32 v43, v43\nv_mfma_f32_16x16x32_bf16 v[8:11], a[76:79], v[36:39], v[8:11]\n" \
"v_rcp_f32 v40, v40\nv_mfma_f32_16x16x32_bf16 v[12:15], a[76:79], v[44:47], v[12:15]\nv_rcp_f32 v41, v41\nv_mfma_f32_16x16x32_bf16 v[16:19], a[204:207], v[36:39], v[16:19]\n" \
"ds_read_b128 v[68:71], v7 offset:21504\nv_fma_f32 v42, v42, v254, v254\nv_mfma_f32_16x16x32_bf16 v[20:23], a[204:207], v[44:47], v[20:23]\nds_read_b128 v[72:75], v7 offset:54272\n" \
"v_fma_f32 v43, v43, v254, v254\nv_mfma_f32_16x16x32_bf16 v[24:27], v[188:191], v[36:39], v[24:27]\ns_waitcnt lgkmcnt(2)\nv_rcp_f32 v42, v42\n" \
"v_mfma_f32_16x16x32_bf16 v[28:31], v[188:191], v[44:47], v[28:31]\nv_rcp_f32 v43, v43\nv_mfma_f32_16x16x32_bf16 v[8:11], a[80:83], v[60:63], v[8:11]\nbuffer_store_dwordx2 v[80:81], v2, s[8:11], s16 offen offset:0 sc1\n" \
"v_mul_f32 v88, v84, v88\nv_mfma_f32_16x16x32_bf16 v[12:15], a[80:83], v[64:67], v[12:15]\nv_mul_f32 v89, v85, v89\nv_mul_f32 v90, v86, v90\n" \
"v_mfma_f32_16x16x32_bf16 v[16:19], a[208:211], v[60:63], v[16:19]\nbuffer_store_dwordx2 v[48:49], v2, s[8:11], s17 offen offset:256 sc1\nv_mul_f32 v91, v87, v91\nv_mfma_f32_16x16x32_bf16 v[20:23], a[208:211], v[64:67], v[20:23]\n" \
"v_mul_f32 v88, v88, v40\nv_mul_f32 v89, v89, v41\nv_mfma_f32_16x16x32_bf16 v[24:27], v[192:195], v[60:63], v[24:27]\nds_read_b128 v[36:39], v7 offset:22528\n" \
"v_mul_f32 v90, v90, v42\nv_mul_f32 v91, v91, v43\nv_mfma_f32_16x16x32_bf16 v[28:31], v[192:195], v[64:67], v[28:31]\ns_waitcnt lgkmcnt(1)\n" \
"ds_read_b128 v[40:43], v7 offset:55296\nv_cvt_pk_bf16_f32 v88, v88, v89\nv_mfma_f32_16x16x32_bf16 v[8:11], a[84:87], v[68:71], v[8:11]\nds_read_b128 v[44:47], v4 offset:0\n" \
"v_cvt_pk_bf16_f32 v89, v90, v91\nv_mfma_f32_16x16x32_bf16 v[12:15], a[84:87], v[72:75], v[12:15]\nbuffer_store_dwordx2 v[88:89], v2, s[8:11], s17 offen offset:512 sc1\nv_add_f32 v52, v52, v56\n" \
"v_mfma_f32_16x16x32_bf16 v[16:19], a[212:215], v[68:71], v[16:19]\nds_read_b128 v[48:51], v7 offset:23552\nv_add_f32 v53, v53, v57\nv_mfma_f32_16x16x32_bf16 v[20:23], a[212:215], v[72:75], v[20:23]\n" \
"ds_read_b128 v[60:63], v7 offset:56320\nv_add_f32 v54, v54, v58\nv_mfma_f32_16x16x32_bf16 v[24:27], v[196:199], v[68:71], v[24:27]\nds_read_b128 v[64:67], v4 offset:512\n" \
"v_add_f32 v55, v55, v59\nv_mfma_f32_16x16x32_bf16 v[28:31], v[196:199], v[72:75], v[28:31]\ns_waitcnt lgkmcnt(4)\nv_permlane16_swap_b32 v52, v54\n" \
"v_mfma_f32_16x16x32_bf16 v[8:11], a[88:91], v[36:39], v[8:11]\nv_permlane16_swap_b32 v53, v55\nv_mfma_f32_16x16x32_bf16 v[12:15], a[88:91], v[40:43], v[12:15]\ns_waitcnt lgkmcnt(3)\n" \
"v_add_f32 v44, v44, v45\nv_add_f32 v46, v46, v47\nv_mfma_f32_16x16x32_bf16 v[16:19], a[216:219], v[36:39], v[16:19]\nds_read_b128 v[56:59], v7 offset:24576\n" \
"v_add_f32 v44, v44, v46\nv_mfma_f32_16x16x32_bf16 v[20:23], a[216:219], v[40:43], v[20:23]\nds_read_b128 v[68:71], v7 offset:57344\nv_mul_f32 v44, 0xba800000, v44\n" \
"v_mfma_f32_16x16x32_bf16 v[24:27], v[200:203], v[36:39], v[24:27]\ns_waitcnt lgkmcnt(2)\nv_add_f32 v52, v52, v54\nv_add_f32 v53, v53, v55\n" \
"v_mfma_f32_16x16x32_bf16 v[28:31], v[200:203], v[40:43], v[28:31]\nv_add_f32 v64, v64, v65\nv_add_f32 v66, v66, v67\nv_mfma_f32_16x16x32_bf16 v[8:11], a[92:95], v[48:51], v[8:11]\n" \
"v_add_f32 v64, v64, v66\nv_mul_f32 v65, v44, -v44\nv_mfma_f32_16x16x32_bf16 v[12:15], a[92:95], v[60:63], v[12:15]\nv_fmamk_f32 v35, v64, 0x3a800000, v65\n" \
"v_cvt_pk_bf16_f32 v35, v35, v35\nv_mfma_f32_16x16x32_bf16 v[16:19], a[220:223], v[48:51], v[16:19]\nds_read_b128 v[64:67], v7 offset:25600\nv_lshlrev_b32 v35, 16, v35\n" \
"v_mfma_f32_16x16x32_bf16 v[20:23], a[220:223], v[60:63], v[20:23]\nds_read_b128 v[72:75], v7 offset:58368\nv_max_f32 v35, 0, v35\nv_mfma_f32_16x16x32_bf16 v[24:27], v[204:207], v[48:51], v[24:27]\n" \
"s_waitcnt lgkmcnt(2)\nv_add_f32 v35, 0x3727c5ac, v35\nv_cvt_pk_bf16_f32 v111, v35, v44\nv_mfma_f32_16x16x32_bf16 v[28:31], v[204:207], v[60:63], v[28:31]\n" \
"v_permlane32_swap_b32 v52, v53\nv_mfma_f32_16x16x32_bf16 v[8:11], a[96:99], v[56:59], v[8:11]\nv_lshlrev_b32 v35, 16, v111\nv_add_f32 v52, v52, v53\n" \
"v_mfma_f32_16x16x32_bf16 v[12:15], a[96:99], v[68:71], v[12:15]\nv_sqrt_f32 v35, v35\nv_mfma_f32_16x16x32_bf16 v[16:19], a[224:227], v[56:59], v[16:19]\nds_read_b128 v[36:39], v7 offset:26624\n" \
"v_cvt_pk_bf16_f32 v35, v35, v35\nv_mfma_f32_16x16x32_bf16 v[20:23], a[224:227], v[68:71], v[20:23]\nds_read_b128 v[40:43], v7 offset:59392\nv_lshlrev_b32 v35, 16, v35\n" \
"v_mfma_f32_16x16x32_bf16 v[24:27], v[208:211], v[56:59], v[24:27]\ns_waitcnt lgkmcnt(2)\nv_rcp_f32 v34, v35\nv_mfma_f32_16x16x32_bf16 v[28:31], v[208:211], v[68:71], v[28:31]\n" \
"ds_write_b32 v5, v52 offset:0\nv_mfma_f32_16x16x32_bf16 v[8:11], a[100:103], v[64:67], v[8:11]\nv_and_b32 v111, 0xffff0000, v111\nv_mfma_f32_16x16x32_bf16 v[12:15], a[100:103], v[72:75], v[12:15]\n" \
"v_mfma_f32_16x16x32_bf16 v[16:19], a[228:231], v[64:67], v[16:19]\nds_read_b128 v[44:47], v7 offset:27648\nv_mfma_f32_16x16x32_bf16 v[20:23], a[228:231], v[72:75], v[20:23]\nds_read_b128 v[48:51], v7 offset:60416\n" \
"v_mfma_f32_16x16x32_bf16 v[24:27], v[212:215], v[64:67], v[24:27]\nv_mfma_f32_16x16x32_bf16 v[28:31], v[212:215], v[72:75], v[28:31]\ns_branch 1b\n4:\n" \
"s_barrier\ns_lshr_b32 s94, s20, 1\nv_readlane_b32 s94, v6, s94\ns_and_b32 s95, s20, 1\n" \
"s_lshl_b32 s95, s95, 3\ns_lshr_b32 s94, s94, s95\ns_and_b32 s94, s94, 0xff\ns_lshl_b32 s95, s94, 13\n" \
"buffer_load_dwordx4 v[96:99], v1, s[8:11], s95 offen offset:0 sc1\nbuffer_load_dwordx4 v[100:103], v1, s[8:11], s95 offen offset:1024 sc1\nv_fma_f32 v84, v32, v240, v8\nv_fma_f32 v85, v32, v241, v9\n" \
"v_fma_f32 v86, v32, v242, v10\nv_fma_f32 v87, v32, v243, v11\nv_fma_f32 v88, v33, v240, v12\nv_fma_f32 v89, v33, v241, v13\n" \
"v_fma_f32 v90, v33, v242, v14\nv_fma_f32 v91, v33, v243, v15\nv_permlane16_swap_b32 v84, v88\nv_permlane16_swap_b32 v85, v89\n" \
"v_permlane16_swap_b32 v86, v90\nv_permlane16_swap_b32 v87, v91\nv_mul_f32 v92, v84, v84\nv_mul_f32 v93, v85, v85\n" \
"v_mul_f32 v94, v86, v86\nv_mul_f32 v95, v87, v87\nv_fma_f32 v92, v92, v253, v252\nv_fma_f32 v93, v93, v253, v252\n" \
"v_fma_f32 v94, v94, v253, v252\nv_fma_f32 v95, v95, v253, v252\nv_mul_f32 v92, v92, v84\nv_mul_f32 v93, v93, v85\n" \
"v_mul_f32 v94, v94, v86\nv_mul_f32 v95, v95, v87\nv_mul_f32 v88, v84, v88\nv_mul_f32 v89, v85, v89\n" \
"v_mul_f32 v90, v86, v90\nv_mul_f32 v91, v87, v91\nv_exp_f32 v92, v92\nv_exp_f32 v93, v93\n" \
"v_exp_f32 v94, v94\nv_exp_f32 v95, v95\nv_fma_f32 v92, v92, v254, v254\nv_fma_f32 v93, v93, v254, v254\n" \
"v_fma_f32 v94, v94, v254, v254\nv_fma_f32 v95, v95, v254, v254\nv_rcp_f32 v92, v92\nv_rcp_f32 v93, v93\n" \
"v_rcp_f32 v94, v94\nv_rcp_f32 v95, v95\nv_mul_f32 v88, v88, v92\nv_mul_f32 v89, v89, v93\n" \
"v_mul_f32 v90, v90, v94\nv_mul_f32 v91, v91, v95\nv_cvt_pk_bf16_f32 v88, v88, v89\nv_cvt_pk_bf16_f32 v89, v90, v91\n" \
"buffer_store_dwordx2 v[88:89], v2, s[8:11], s16 offen offset:0 sc1\nv_fma_f32 v84, v32, v244, v16\nv_fma_f32 v85, v32, v245, v17\nv_fma_f32 v86, v32, v246, v18\n" \
"v_fma_f32 v87, v32, v247, v19\nv_fma_f32 v88, v33, v244, v20\nv_fma_f32 v89, v33, v245, v21\nv_fma_f32 v90, v33, v246, v22\n" \
"v_fma_f32 v91, v33, v247, v23\nv_permlane16_swap_b32 v84, v88\nv_permlane16_swap_b32 v85, v89\nv_permlane16_swap_b32 v86, v90\n" \
"v_permlane16_swap_b32 v87, v91\nv_mul_f32 v92, v84, v84\nv_mul_f32 v93, v85, v85\nv_mul_f32 v94, v86, v86\n" \
"v_mul_f32 v95, v87, v87\nv_fma_f32 v92, v92, v253, v252\nv_fma_f32 v93, v93, v253, v252\nv_fma_f32 v94, v94, v253, v252\n" \
"v_fma_f32 v95, v95, v253, v252\nv_mul_f32 v92, v92, v84\nv_mul_f32 v93, v93, v85\nv_mul_f32 v94, v94, v86\n" \
"v_mul_f32 v95, v95, v87\nv_mul_f32 v88, v84, v88\nv_mul_f32 v89, v85, v89\nv_mul_f32 v90, v86, v90\n" \
"v_mul_f32 v91, v87, v91\nv_exp_f32 v92, v92\nv_exp_f32 v93, v93\nv_exp_f32 v94, v94\n" \
"v_exp_f32 v95, v95\nv_fma_f32 v92, v92, v254, v254\nv_fma_f32 v93, v93, v254, v254\nv_fma_f32 v94, v94, v254, v254\n" \
"v_fma_f32 v95, v95, v254, v254\nv_rcp_f32 v92, v92\nv_rcp_f32 v93, v93\nv_rcp_f32 v94, v94\n" \
"v_rcp_f32 v95, v95\nv_mul_f32 v88, v88, v92\nv_mul_f32 v89, v89, v93\nv_mul_f32 v90, v90, v94\n" \
"v_mul_f32 v91, v91, v95\nv_cvt_pk_bf16_f32 v88, v88, v89\nv_cvt_pk_bf16_f32 v89, v90, v91\nbuffer_store_dwordx2 v[88:89], v2, s[8:11], s17 offen offset:256 sc1\n" \
"v_fma_f32 v84, v32, v248, v24\nv_fma_f32 v85, v32, v249, v25\nv_fma_f32 v86, v32, v250, v26\nv_fma_f32 v87, v32, v251, v27\n" \
"v_fma_f32 v88, v33, v248, v28\nv_fma_f32 v89, v33, v249, v29\nv_fma_f32 v90, v33, v250, v30\nv_fma_f32 v91, v33, v251, v31\n" \
"v_permlane16_swap_b32 v84, v88\nv_permlane16_swap_b32 v85, v89\nv_permlane16_swap_b32 v86, v90\nv_permlane16_swap_b32 v87, v91\n" \
"v_mul_f32 v92, v84, v84\nv_mul_f32 v93, v85, v85\nv_mul_f32 v94, v86, v86\nv_mul_f32 v95, v87, v87\n" \
"v_fma_f32 v92, v92, v253, v252\nv_fma_f32 v93, v93, v253, v252\nv_fma_f32 v94, v94, v253, v252\nv_fma_f32 v95, v95, v253, v252\n" \
"v_mul_f32 v92, v92, v84\nv_mul_f32 v93, v93, v85\nv_mul_f32 v94, v94, v86\nv_mul_f32 v95, v95, v87\n" \
"v_mul_f32 v88, v84, v88\nv_mul_f32 v89, v85, v89\nv_mul_f32 v90, v86, v90\nv_mul_f32 v91, v87, v91\n" \
"v_exp_f32 v92, v92\nv_exp_f32 v93, v93\nv_exp_f32 v94, v94\nv_exp_f32 v95, v95\n" \
"v_fma_f32 v92, v92, v254, v254\nv_fma_f32 v93, v93, v254, v254\nv_fma_f32 v94, v94, v254, v254\nv_fma_f32 v95, v95, v254, v254\n" \
"v_rcp_f32 v92, v92\nv_rcp_f32 v93, v93\nv_rcp_f32 v94, v94\nv_rcp_f32 v95, v95\n" \
"v_mul_f32 v88, v88, v92\nv_mul_f32 v89, v89, v93\nv_mul_f32 v90, v90, v94\nv_mul_f32 v91, v91, v95\n" \
"v_cvt_pk_bf16_f32 v88, v88, v89\nv_cvt_pk_bf16_f32 v89, v90, v91\nbuffer_store_dwordx2 v[88:89], v2, s[8:11], s17 offen offset:512 sc1\ns_mov_b32 s16, 0x80000000\n" \
"s_mov_b32 s17, 0x80000000\nv_mov_b32 v84, s20\ns_cmp_ge_u32 s20, s26\ns_cbranch_scc1 6f\n" \
"s_waitcnt vmcnt(3)\nv_max3_u32 v85, v96, v98, v100\nv_max_u32 v85, v85, v102\nv_cmp_eq_u32 vcc, -1, v85\n" \
"s_cbranch_vccz 8f\n6:\ns_waitcnt vmcnt(0)\nbuffer_store_dword v84, v3, s[8:11], 0 offen offset:0 sc1\n" \
"s_mov_b32 s23, 0x40000000\ns_cbranch_scc1 9f\n7:\ns_add_u32 s24, s24, 1\n" \
"s_cmp_lt_u32 s24, 0x40000\ns_cbranch_scc0 9f\nbuffer_load_dwordx4 v[96:99], v1, s[8:11], s95 offen offset:0 sc1\nbuffer_load_dwordx4 v[100:103], v1, s[8:11], s95 offen offset:1024 sc1\n" \
"s_waitcnt vmcnt(0)\nv_max3_u32 v85, v96, v98, v100\nv_max_u32 v85, v85, v102\nv_cmp_eq_u32 vcc, -1, v85\n" \
"s_cbranch_vccz 8f\ns_sleep 8\ns_branch 7b\n8:\n" \
"s_lshl_b32 s94, s94, 16\ns_add_u32 s94, s94, s21\ns_xor_b32 s95, s22, 0x10000\ns_xor_b32 m0, s95, 0\n" \
"s_xor_b32 s94, s94, 0\nbuffer_load_dwordx4 v0, s[8:11], s94 offen offset:0 sc1 lds\nbuffer_load_dwordx4 v0, s[8:11], s94 offen offset:1024 sc1 lds\nbuffer_load_dwordx4 v0, s[8:11], s94 offen offset:2048 sc1 lds\n" \
"buffer_load_dwordx4 v0, s[8:11], s94 offen offset:3072 sc1 lds\ns_xor_b32 m0, s95, 0x1000\ns_xor_b32 s94, s94, 0x1000\nbuffer_load_dwordx4 v0, s[8:11], s94 offen offset:0 sc1 lds\n" \
"buffer_load_dwordx4 v0, s[8:11], s94 offen offset:1024 sc1 lds\nbuffer_load_dwordx4 v0, s[8:11], s94 offen offset:2048 sc1 lds\nbuffer_load_dwordx4 v0, s[8:11], s94 offen offset:3072 sc1 lds\ns_xor_b32 m0, s95, 0x8000\n" \
"s_xor_b32 s94, s94, 0x9000\nbuffer_load_dwordx4 v0, s[8:11], s94 offen offset:0 sc1 lds\nbuffer_load_dwordx4 v0, s[8:11], s94 offen offset:1024 sc1 lds\nbuffer_load_dwordx4 v0, s[8:11], s94 offen offset:2048 sc1 lds\n" \
"buffer_load_dwordx4 v0, s[8:11], s94 offen offset:3072 sc1 lds\ns_xor_b32 m0, s95, 0x9000\ns_xor_b32 s94, s94, 0x1000\nbuffer_load_dwordx4 v0, s[8:11], s94 offen offset:0 sc1 lds\n" \
"buffer_load_dwordx4 v0, s[8:11], s94 offen offset:1024 sc1 lds\nbuffer_load_dwordx4 v0, s[8:11], s94 offen offset:2048 sc1 lds\nbuffer_load_dwordx4 v0, s[8:11], s94 offen offset:3072 sc1 lds\nv_add_f32 v96, v96, v100\n" \
"v_add_f32 v97, v97, v101\nv_add_f32 v98, v98, v102\nv_add_f32 v99, v99, v103\ns_nop 1\n" \
"v_permlane16_swap_b32 v96, v98\nv_permlane16_swap_b32 v97, v99\nv_add_f32 v96, v96, v98\nv_add_f32 v97, v97, v99\n" \
"s_nop 1\nv_permlane32_swap_b32 v96, v97\nv_add_f32 v96, v96, v97\nds_write_b32 v5, v96\n" \
"s_add_u32 s97, s20, 1\ns_lshr_b32 s96, s97, 1\nv_readlane_b32 s96, v6, s96\ns_and_b32 s97, s97, 1\n" \
"s_lshl_b32 s97, s97, 3\ns_lshr_b32 s96, s96, s97\ns_and_b32 s96, s96, 0xff\ns_lshl_b32 s97, s96, 13\n" \
"s_lshl_b32 s96, s96, 16\ns_add_u32 s96, s96, s21\nbuffer_load_dwordx4 v[52:55], v1, s[8:11], s97 offen offset:0 sc1\nbuffer_load_dwordx4 v[56:59], v1, s[8:11], s97 offen offset:1024 sc1\n" \
"s_waitcnt vmcnt(18)\nbuffer_store_dword v84, v3, s[8:11], s23 offen offset:0 sc1\ns_mov_b32 s23, 0x40000000\ns_waitcnt vmcnt(3) lgkmcnt(0)\n" \
"s_barrier\nds_read_b128 v[72:75], v7 offset:0\nds_read_b128 v[36:39], v7 offset:32768\nds_read_b128 v[40:43], v7 offset:1024\n" \
"ds_read_b128 v[76:79], v7 offset:33792\ns_waitcnt lgkmcnt(0)\ns_branch 5b\n9:\n" \
"s_waitcnt vmcnt(0)\ns_endpgm\n"
struct WMA { static constexpr int NT = 4, NP = 16, RB = 0, LB = 0, CBI = 0;
             static constexpr int kind(int j, int t) { return (A_LDS >> (16 * j + t) & 1) ? 1 : (A_VAR >> (16 * j + t) & 1) ? 2 : 0; } };
namespace la { constexpr int L_Q = 24576, L_K = 57344, L_QM = 65536, L_KM = 66048, L_SP = 69632, L_W = 73728; }
#ifndef LEAD_B
#define LEAD_B 132
#endif
DEVI float dror8(float x) { return __uint_as_float(__builtin_amdgcn_update_dpp(0u, __float_as_uint(x), 0x128, 0xF, 0xF, true)); }
DEVI float scm(float a, float m) { float r; asm("v_fma_f32 %0, %1, %2, -%3" : "=v"(r) : "v"(a), "s"(SC_L2E), "v"(m)); return r; }
DEVI float ex2(float x) { return __builtin_amdgcn_exp2f(x); }
DEVI float max3(float a, float b, float c) { float r; asm("v_max3_f32 %0, %1, %2, %3" : "=v"(r) : "v"(a), "v"(b), "v"(c)); return r; }
constexpr u64 tri(int i) { u64 m = 0; for (int l = 0; l < 64; ++l) if (4 * (l >> 4) + i <= (l & 15)) m |= 1ull << l; return m; }
DEVI unsigned vbfe(unsigned a, int o, int n) { unsigned r; asm volatile("v_bfe_u32 %0, %1, %2, %3" : "=v"(r) : "v"(a), "i"(o), "i"(n)); return r; }
DEVI unsigned vadds(unsigned a, unsigned b) { unsigned r; asm volatile("v_add_u32 %0, %1, %2" : "=v"(r) : "s"(b), "v"(a)); return r; }
DEVI float vexp(float a) { float r; asm volatile("v_exp_f32 %0, %1" : "=v"(r) : "v"(a)); return r; }
DEVI unsigned vors(unsigned a, unsigned b) { unsigned r; asm volatile("v_or_b32 %0, %1, %2" : "=v"(r) : "s"(b), "v"(a)); return r; }
DEVI float faddv(float a, float b) { float r; asm volatile("v_add_f32 %0, %1, %2" : "=v"(r) : "v"(a), "v"(b)); return r; }
DEVI unsigned packv(float a, float b) { unsigned r; asm volatile("v_cvt_pk_bf16_f32 %0, %1, %2" : "=v"(r) : "v"(a), "v"(b)); return r; }
DEVI void setm0q(unsigned v) { asm volatile("s_mov_b32 m0, %0" :: "s"(v) : "memory"); }
template <int OFF> DEVI void st16o(v4u v, rsrc_t r, unsigned vo, unsigned so) {
    asm volatile("buffer_store_dwordx4 %0, %1, %2, %3 offen offset:%4 sc1" :: "v"(v), "v"(vo), "s"(r), "s"(so), "n"(OFF) : "memory"); }
DEVI unsigned vlsa(unsigned a, int sh, unsigned c) { unsigned r; asm volatile("v_lshl_add_u32 %0, %1, %2, %3" : "=v"(r) : "v"(a), "i"(sh), "v"(c)); return r; }
DEVI unsigned vlss(unsigned a, int sh, unsigned c) { unsigned r; asm volatile("v_lshl_add_u32 %0, %1, %2, %3" : "=v"(r) : "v"(a), "i"(sh), "s"(c)); return r; }
template <int OFF> DEVI void sts4h(unsigned p, unsigned u0, unsigned u1) {
    asm volatile("ds_write_b16 %0, %1 offset:%3\n\tds_write_b16_d16_hi %0, %1 offset:%3+16\n\tds_write_b16 %0, %2 offset:%3+8\n\tds_write_b16_d16_hi %0, %2 offset:%3+24" :: "v"(p), "v"(u0), "v"(u1), "n"(OFF) : "memory");
}
DEVI void zero4v(f32x4& d) { f32x2 a, b; asm volatile("v_mov_b64 %0, 0\n\tv_mov_b64 %1, 0" : "=v"(a), "=v"(b)); d = __builtin_shufflevector(a, b, 0, 1, 2, 3); }
DEVI float sel0(float a, u64 m) { float r; asm("v_cndmask_b32 %0, %1, 0, %2" : "=v"(r) : "v"(a), "s"(m)); return r; }
DEVI float vmul(float a, float b) { float r; asm("v_mul_f32 %0, %1, %2" : "=v"(r) : "v"(a), "v"(b)); return r; }
DEVI void swap16(float& a, float& b) { unsigned x = __float_as_uint(a), y = __float_as_uint(b); swap16u(x, y); a = __uint_as_float(x); b = __uint_as_float(y); }
#ifndef PACE
#define PACE 7
#endif
DEVI void pace(int l) { asm volatile("s_cmp_eq_u32 %0, 0\n\ts_cbranch_scc1 1f\n\ts_sleep %1\n1:" :: "s"(l), "n"(PACE) : "scc"); }
DEVI void role_A(const Args& a, int w, int l, int h, int rep, int id2) {
    asm volatile("; MARK role_A"); __builtin_amdgcn_s_waitcnt(0);
    using namespace la;
    const int lane0 = lane_id();
    const rsrc_t rs = mkrs(a.ws, WS_NEED);
    const unsigned lane16 = lane0 * 16, wl = L_W + w * (11 * 2048) + lane16;
    v8u Wc[3];
    unsigned nb, fx, ok;
    const unsigned own = (w >> 1) * 32768 + (w & 1) * 4096;
    {
        const int lane = lane0;
        const unsigned ta = (lane >> 4) * 2048 + w * 256 + (lane & 15) * 16;
        const rsrc_t r1 = mkrs(a.Wq, WSZ(8u << 20)), r2 = mkrs(a.Wk, WSZ(8u << 20));
        const unsigned v1 = (8 * w + ((lane >> 2) & 3)) * 8192 + h * 256 + (lane >> 4) * 64 + (lane & 3) * 16;
        const unsigned lw = opqs(w * 2048);
        load_blocks<32, 4, 4, true, 2>([&](auto B, auto S) {
            if constexpr (B.v == 32) ok = first_there(fx);
            if constexpr (B.v >= 32) dma4(opqs(own + S.v * 8192), mkrs(a.ws, ok), lane16, opqs(O_XN + l * XB + own + S.v * 8192));
            else { pace(l); sfor<0, 2>([&](auto I) { dma_blk(lw + S.v * 8192 + I.v * 32768, B.v < 16 ? r1 : r2, v1, opqs((2 * (B.v & 15) + I.v) * 32 * 8192), 2048); }); } },
                                       [&](auto B, auto S) { sfor<0, 4>([&](auto T) { constexpr int o = S.v * 8192 + (T.v >> 1) * 32768 + (T.v & 1) * 8;
                                                                                      put_frag<WMA, (B.v >> 4) * 2 + (T.v & 1), 2 * (B.v & 15) + (T.v >> 1), o, o + 1024>(ta, wl, Wc); });
                                                             if constexpr (B.v == 24) { nb = side(rs, l); first_look(fx, rs, nb); } });
        TSW(127, 2);
    }
    const unsigned spm = na(nb, N_PM + h * 512), spmm = na(nb, N_PMM + h * 128), smem = na(nb, N_MEM + rep * 16384 + w * 4096);
    const int lane = lane_id(), c = lane & 15, g = lane >> 4;
    const unsigned wlo = (unsigned)(uintptr_t)a.ws, whi = (unsigned)((uintptr_t)a.ws >> 32), hi = -(w >> 1);
    const v8u pv{lane16, wl, (lane & 2) ? 0xEEEEu : 0x4444u, (unsigned)(L_QM + w * 1024 + (c >> 2) * 128 + g * 32 + (c & 3) * 4), (unsigned)(L_QM + g * 128 + (c & 7) * 16),
                 w == 2 ? ((g >> 1) * 16 + crow(c & 7, c >> 3)) * 16 + (g & 1) * 8 : w < 2 ? lane * 8 : lane < NC ? DV(lane * 512) : 1u << 30,
                 w < 2 ? c * 16 + g * 4 : c < 8 ? crow(c, g & 1) * 8 + (g >> 1) * 4 : 1u << 30, 0u};
    const v16u s0{wlo, whi, RSZ, 0x20000u, wlo, whi, 0u, 0x20000u, (unsigned)tri(0) | hi, (unsigned)(tri(0) >> 32) | hi, (unsigned)tri(1) | hi, (unsigned)(tri(1) >> 32) | hi,
                  (unsigned)tri(2) | hi, (unsigned)(tri(2) >> 32) | hi, (unsigned)tri(3) | hi, (unsigned)(tri(3) >> 32) | hi};
    const v16u s1{0xFF00FF00u, 0xFF00FF00u, 0u, ~0u, hi, hi, 0u, O_XN + l * XB + own, own, smem, w == 2 ? spm : OOB, nb + DV(N_PTX + h * 2048), w == 1 ? 0u : OOB, w == 0 ? 0u : OOB,
                  w < 2 ? w * 256 : w == 2 ? spmm - 1536 : OOB, ~hi};
    const v16u s2{w < 2 ? w * 1024 : w == 2 ? spm : spmm, w < 2 ? 4u : 3u, w < 2 ? 0u : w == 2 ? 0x200u : 0x380u, w ? 0u : __float_as_uint(-3e38f), (unsigned)(L_Q + w * 1024), (unsigned)(L_Q + (w & 1) * 4096),
                  __float_as_uint(SC_L2E), __float_as_uint(-1e30f), na(nb, N_FX), w == 3 ? na(nb, N_PA + ((h >> 3) * 32 + 8 + (h & 7)) * 4) : OOB, (unsigned)(L_KM + (w >> 1) * 1024 + (w & 1) * 256),
                  0u, 0u, (unsigned)(L_SP + w * 256), O_PROF + (blockIdx.x * 4 + w) * 8192, na(nb, N_WM + h * 16384 + w * 4096)};
    asm volatile(A_LOOP :: "{v[0:7]}"(Wc[0]), "{v[8:15]}"(Wc[1]), "{v[16:23]}"(Wc[2]), "{v[24:31]}"(pv), "{s[8:23]}"(s0), "{s[24:39]}"(s1), "{s[40:55]}"(s2),
                 "{s58}"(ok), "{s[4:7]}"(again(id2, nb, w)) : "memory", "s99", "vcc");
}
constexpr int bkind(int n) { return (B_LDS >> n & 1) ? 1 : (B_VAR >> n & 1) ? 2 : 0; }
struct WMV { static constexpr int NT = 2, NP = 16, RB = 0, LB = 0, CBI = 0; static constexpr int kind(int j, int t) { return bkind(16 * j + t); } };
struct WMO { static constexpr int NT = 16, NP = 2, LB = __builtin_popcount((unsigned)B_LDS), CBI = __builtin_popcount((unsigned)B_VAR), RB = 8 * (32 - LB - CBI);
             static constexpr int kind(int j, int t) { return bkind(32 + 2 * j + t); } };
#ifndef B_LV
#define B_LV 24576
#endif
namespace lb { constexpr int L_V = B_LV, L_VM = B_LV + 4096, L_W = 65536; }
DEVI void role_B(const Args& a, int w, int l, int h, int rep, int id2) {
    asm volatile("; MARK role_B"); __builtin_amdgcn_s_waitcnt(0);
    using namespace lb;
    const int lane0 = lane_id();
    const rsrc_t rs = mkrs(a.ws, WS_NEED);
    const unsigned lane16 = lane0 * 16, wl = L_W + w * (12 * 2048) + lane16;
    v8u Wc[2];
    unsigned nb, fx, ok;
    const unsigned own = (w >> 1) * 32768 + (w & 1) * 4096;
    {
        const int lane = lane0;
        const unsigned ta = (lane >> 4) * 2048 + w * 256 + (lane & 15) * 16, tb = (lane >> 5) * 32768 + w * 2048 + (lane & 16) * 64 + (lane & 15) * 16;
        const rsrc_t r1 = mkrs(a.Wv, WSZ(8u << 20)), r2 = mkrs(a.Wo, WSZ(8u << 20));
        const unsigned v1 = (8 * w + ((lane >> 2) & 3)) * 8192 + h * 256 + (lane >> 4) * 64 + (lane & 3) * 16, v2 = (h * 128 + ((lane >> 2) & 7)) * 2048 + w * 512 + (lane >> 5) * 64 + (lane & 3) * 16;
        const unsigned lw = opqs(w * 2048);
        load_blocks<32, 4, 4, true, 3>([&](auto B, auto S) {
            constexpr int b = B.v & 15;
            if constexpr (B.v == 32) ok = first_there(fx);
            if constexpr (B.v < 32) pace(l);
            if constexpr (B.v >= 32) dma4(opqs(own + S.v * 8192), mkrs(a.ws, ok), lane16, opqs(O_XN + l * XB + own + S.v * 8192));
            else sfor<0, 2>([&](auto I) { if constexpr (B.v < 16) dma_blk(lw + S.v * 8192 + I.v * 32768, r1, v1, opqs((2 * b + I.v) * 32 * 8192), 2048);
                                     else dma_blk(lw + S.v * 8192 + I.v * 32768, r2, v2, opqs(((b & 3) * 32 + I.v * 16) * 2048 + (b >> 2) * 128), 1024); });
        }, [&](auto B, auto S) {
            constexpr int b = B.v & 15;
            sfor<0, 4>([&](auto T) { constexpr int o = S.v * 8192 + (T.v >> 1) * (B.v < 16 ? 32768 : 512) + (T.v & 1) * 8;
                                     if constexpr (B.v < 16) put_frag<WMV, T.v & 1, 2 * b + (T.v >> 1), o, o + 1024>(ta, wl, Wc); else put_frag<WMO, (b & 12) + T.v, b & 3, o, o + 256>(tb, wl, Wc); });
            if constexpr (B.v == 24) { nb = side(rs, l); first_look(fx, rs, nb); }
        });
        TSW(127, 2);
    }
    const int lane = lane_id(), c = lane & 15, g = lane >> 4;
    const unsigned wlo = (unsigned)(uintptr_t)a.ws, whi = (unsigned)((uintptr_t)a.ws >> 32);
    const unsigned prow = L_VM + w * 512 + ((c >> 2) & 1) * 256 + g * 64 + (c >> 3) * 32 + (c & 1) * 8 + ((c >> 1) & 1) * 4;
    const v4u pv{lane16, wl, ((lane & 2) ? 0xEEEEu : 0x4444u) | prow << 16, (unsigned)(L_VM + (g >> 1) * 512 + (lane & 3) * 64 + (g & 1) * 32 + ((lane >> 2) & 3) * 8)};
    const v16u s0{wlo, whi, RSZ, 0x20000u, wlo, whi, 0u, 0x20000u, OOB, OOB, OOB, OOB, ~1u, O_XN + l * XB + own, own, na(nb, N_MEM + rep * 16384 + w * 4096)};
    const v8u s1{na(nb, N_WM + h * 16384 + w * 4096), na(nb, N_U + h * 65536 + w * 16384), na(nb, N_FX), w == 3 ? na(nb, N_PA + ((h >> 3) * 32 + (h & 7)) * 4) : OOB,
                 (unsigned)(L_V + w * 1024), OOB, 0u, O_PROF + (blockIdx.x * 4 + w) * 8192 + 64};
    asm volatile(B_LOOP :: "{v[0:7]}"(Wc[0]), "{v[8:15]}"(Wc[1]), "{v[16:19]}"(pv), "{v20}"(fx), "{s[8:23]}"(s0), "{s[24:31]}"(s1), "{s32}"(1u), "{s[4:7]}"(again(id2, nb, w)) : "memory", "s99", "vcc");
}
template <class NB> DEVI void put_tile(v4u (&u)[8], rsrc_t rs, int tile, int w, int lane, NB&& nbf) {
    const unsigned vo = tile * 32768 + w * 8192 + lane * 16, red = (lane & 15) * 16;
    float s = 0.f;
#pragma unroll
    for (int f = 0; f < 8; ++f) {
#pragma unroll
        for (int i = 0; i < 4; ++i) s += bflo(u[f][i]) + bfhi(u[f][i]);
    }
    s = red32(red16(s));
    BAR;
    if (lane < 16) *LDSP(float, red + w * 4) = s;
    WL0; BAR;
    const f32x4 sa = __builtin_bit_cast(f32x4, ldsq(red, 0));
    const float mean = ((sa[0] + sa[1]) + (sa[2] + sa[3])) * (1.0f / 1024.0f);
    float q = 0.f;
#pragma unroll
    for (int f = 0; f < 8; ++f) {
        asm volatile("" : "+v"(u[f]));
#pragma unroll
        for (int i = 0; i < 4; ++i) { const float d0 = bflo(u[f][i]) - mean, d1 = bfhi(u[f][i]) - mean; q = fmaf(d0, d0, fmaf(d1, d1, q)); }
    }
    q = red32(red16(q));
    if (lane < 16) *LDSP(float, red + 256 + w * 4) = q;
    WL0; BAR;
    const f32x4 qa = __builtin_bit_cast(f32x4, ldsq(red, 256));
    const float mb = rbf(mean), inv = 1.0f / rbf(sqrtf(rbf(rbf(((qa[0] + qa[1]) + (qa[2] + qa[3])) * (1.0f / 1024.0f)) + 1e-5f)));
#pragma unroll
    for (int f = 0; f < 8; ++f) {
        v4u o;
        asm volatile("" : "+v"(u[f]));
#pragma unroll
        for (int i = 0; i < 4; ++i) { const unsigned d = pack2(bflo(u[f][i]) - mb, bfhi(u[f][i]) - mb); o[i] = pack2(bflo(d) * inv, bfhi(d) * inv); }
        st16(o, rs, O_XN + vo + f * 1024);
    }
    const unsigned nb = nbf();
    WV0;
    if (lane == 0) st4(0u, rs, na(nb, N_FX) + tile * 16 + w * 4, 0);
}
#ifndef SXL
#define SXL 1
#endif
#ifndef HLD
#define HLD 170
#endif
#ifndef SXH
#define SXH 30
#endif
#ifndef UIN
#define UIN 8
#endif
namespace lr { constexpr int PT1 = NV + 48, PT0 = NV + 80, PTM = NV + 96, VPM = NV, VMM = NV + 16, VW = NV + 28, VX = NV + 44, L_M = 1024, MD = 288, MW = 1536, L_T = 8192, VE = NV + 112; }
template <int R, int N, int OFF> DEVI void lda(rsrc_t rs, unsigned vo, unsigned so) {
    if constexpr (N == 4) asm volatile("buffer_load_dwordx4 a[%3:%4], %0, %1, %2 offen offset:%5 sc1" :: "v"(vo), "s"(rs), "s"(so), "n"(R), "n"(R + 3), "n"(OFF) : "memory");
    else asm volatile("buffer_load_dwordx2 a[%3:%4], %0, %1, %2 offen offset:%5 sc1" :: "v"(vo), "s"(rs), "s"(so), "n"(R), "n"(R + 1), "n"(OFF) : "memory");
}
template <int R, int N, int OFF> DEVI void ldv(rsrc_t rs, unsigned vo, unsigned so) {
    if constexpr (N == 4) asm volatile("buffer_load_dwordx4 v[%3:%4], %0, %1, %2 offen offset:%5 sc1" :: "v"(vo), "s"(rs), "s"(so), "n"(R), "n"(R + 3), "n"(OFF) : "memory");
    else asm volatile("buffer_load_dwordx2 v[%3:%4], %0, %1, %2 offen offset:%5 sc1" :: "v"(vo), "s"(rs), "s"(so), "n"(R), "n"(R + 1), "n"(OFF) : "memory");
}
template <int R> DEVI unsigned rdv() { unsigned t; asm volatile("v_mov_b32 %0, v[%1]" : "=v"(t) : "n"(R)); return t; }
template <int RA, int RB, bool Z, bool AV> DEVI void mfh(f32x4& d) {
    if constexpr (AV) { if constexpr (Z) asm volatile(M16 "%0, v[%1:%2], v[%3:%4], 0" : "=v"(d) : "n"(RA), "n"(RA + 3), "n"(RB), "n"(RB + 3));
                        else asm volatile(M16 "%0, v[%1:%2], v[%3:%4], %0" : "+v"(d) : "n"(RA), "n"(RA + 3), "n"(RB), "n"(RB + 3)); }
    else { if constexpr (Z) asm volatile(M16 "%0, a[%1:%2], v[%3:%4], 0" : "=v"(d) : "n"(RA), "n"(RA + 3), "n"(RB), "n"(RB + 3));
           else asm volatile(M16 "%0, a[%1:%2], v[%3:%4], %0" : "+v"(d) : "n"(RA), "n"(RA + 3), "n"(RB), "n"(RB + 3)); }
}
template <int RA, int RB, bool Z> DEVI void mfh16(f32x4& d) {
    if constexpr (Z) asm volatile("v_mfma_f32_16x16x16_bf16 %0, a[%1:%2], v[%3:%4], 0" : "=v"(d) : "n"(RA), "n"(RA + 1), "n"(RB), "n"(RB + 1));
    else asm volatile("v_mfma_f32_16x16x16_bf16 %0, a[%1:%2], v[%3:%4], %0" : "+v"(d) : "n"(RA), "n"(RA + 1), "n"(RB), "n"(RB + 1));
}
template <int R> DEVI u64 ltv(int need) { u64 m; asm volatile("v_cmp_lt_i32 %0, v[%2], %1" : "=s"(m) : "v"(need), "n"(R)); return m; }
template <int R> DEVI void ldv4(rsrc_t rs, unsigned vo, unsigned so) { asm volatile("buffer_load_dwordx4 v[%3:%4], %0, %1, %2 offen sc1" :: "v"(vo), "s"(rs), "s"(so), "n"(R), "n"(R + 3) : "memory"); }
#define SM_(E, A, B) "v_smfmac_f32_16x16x64_bf16 v[%[e]+" #E ":%[e]+" #E "+3], " A ", " B ", %[si]\n\t"
#define TG_(M, R) "v_bitop3_b32 v[%[e]+" #M "], v[%[e]+" #M "], v[" R "], %[tg] bitop3:0xf6\n\t"
#define PP_(P, N) "s_waitcnt vmcnt(" #N ")\n\t" SM_(0, "v[%[p]+4*" #P ":%[p]+4*" #P "+3]", "a[%[u]+16*" #P ":%[u]+16*" #P "+7]") TG_(8, "%[p]+4*" #P)
#define PQ_(P) SM_(4, "v[%[p]+4*" #P ":%[p]+4*" #P "+3]", "a[%[u]+16*" #P "+8:%[u]+16*" #P "+15]") TG_(9, "%[p]+4*" #P "+2")
#define ZE_ "v_mov_b64 v[%[e]:%[e]+1], 0\n\tv_mov_b64 v[%[e]+2:%[e]+3], 0\n\tv_mov_b64 v[%[e]+4:%[e]+5], 0\n\tv_mov_b64 v[%[e]+6:%[e]+7], 0\n\tv_mov_b64 v[%[e]+8:%[e]+9], 0\n\t"
template <int U0> DEVI unsigned chain(rsrc_t rs, int tgo, unsigned sp, unsigned sm, unsigned sw0, unsigned sw1, unsigned tg, unsigned spt, unsigned lane16, unsigned lane8, unsigned vmm, unsigned vw, int sidx,
                                      unsigned lred, unsigned lrr, unsigned vpub, v2u sxv, unsigned sxo) {
    using namespace lr;
    unsigned n, d;
    asm volatile(ZE_ "s_mov_b32 %[n], 0\n\ts_memrealtime s[98:99]\n\ts_add_u32 %[d], %[pt], 0x800\n\ts_waitcnt lgkmcnt(0)\n\ts_sub_i32 s99, %[t], s98\n\ts_lshl_b32 s99, s99, 8\n\ts_sub_u32 s99, s99, 1\n\t"
        "s_cmp_lt_u32 s99, 0x10000\n\ts_cbranch_scc1 2f\n\ts_branch 3f\n"
        "1:\n\ts_add_u32 %[n], %[n], 1\n\ts_cmp_lt_u32 %[n], %[cap]\n\ts_cbranch_scc1 4f\n\ts_endpgm\n4:\n\t" ZE_ "v_bfrev_b32 %[so], 1\n\ts_branch 3f\n"
        "2:\n\ts_memrealtime s[98:99]\n\ts_waitcnt lgkmcnt(0)\n\ts_sub_i32 s99, %[t], s98\n\ts_lshl_b32 s99, s99, 8\n\ts_cmp_gt_i32 s99, 0\n\ts_cbranch_scc1 2b\n"
        "3:\n\t"
        "buffer_load_dwordx4 v[%[p]:%[p]+3], %[l16], %[rs], %[sp] offen sc1\n\t"
        "buffer_load_dwordx4 v[%[p]+4:%[p]+7], %[l16], %[rs], %[sp] offen offset:1024 sc1\n\t"
        "buffer_load_dwordx4 v[%[p]+8:%[p]+11], %[l16], %[rs], %[sp] offen offset:2048 sc1\n\t"
        "buffer_load_dwordx4 v[%[p]+12:%[p]+15], %[l16], %[rs], %[sp] offen offset:3072 sc1\n\t"
        "buffer_load_dwordx2 v[%[p]+16:%[p]+17], %[vm], %[rs], %[sm] offen sc1\n\t"
        "buffer_load_dwordx2 v[%[p]+18:%[p]+19], %[vm], %[rs], %[sm] offen offset:128 sc1\n\t"
        "buffer_load_dwordx4 v[%[w]:%[w]+3], %[vw], %[rs], %[sw0] offen sc1\n\t"
        "buffer_load_dwordx4 v[%[w]+4:%[w]+7], %[vw], %[rs], %[sw1] offen sc1\n\t"
        "buffer_load_dwordx4 v[%[w]+8:%[w]+11], %[vw], %[rs], %[sw0] offen offset:256 sc1\n\t"
        "buffer_load_dwordx4 v[%[w]+12:%[w]+15], %[vw], %[rs], %[sw1] offen offset:256 sc1\n\t"
        PP_(0, 9) "buffer_store_dwordx2 %[sx], %[so], %[rs], 0 offen sc1\n\t" PQ_(0) PP_(1, 9) PQ_(1) PP_(2, 8) PQ_(2) PP_(3, 7) PQ_(3)
        "s_waitcnt vmcnt(5)\n\t" TG_(8, "%[p]+16") TG_(9, "%[p]+17") TG_(8, "%[p]+18") TG_(9, "%[p]+19")
        "s_waitcnt vmcnt(3)\n\t" SM_(0, "v[%[p]+16:%[p]+19]", "v[%[w]:%[w]+7]") TG_(8, "%[w]") TG_(9, "%[w]+4")
        "s_waitcnt vmcnt(1)\n\t" SM_(4, "v[%[p]+16:%[p]+19]", "v[%[w]+8:%[w]+15]") TG_(8, "%[w]+8") TG_(9, "%[w]+12")
        "v_bitop3_b32 v[%[e]+8], v[%[e]+8], v[%[e]+9], 1 bitop3:0xa8\n\tv_cmp_ne_u32 vcc, 0, v[%[e]+8]\n\ts_nop 0\n\t"
        "v_add_f32 v[%[e]], v[%[e]], v[%[e]+2]\n\tv_add_f32 v[%[e]+1], v[%[e]+1], v[%[e]+3]\n\tds_write2_b32 %[lw], v[%[e]], v[%[e]+1] offset1:2\n\ts_nop 0\n\t"
        "v_add_f32 v[%[e]+4], v[%[e]+4], v[%[e]+6]\n\tv_add_f32 v[%[e]+5], v[%[e]+5], v[%[e]+7]\n\tds_write2_b32 %[lw], v[%[e]+4], v[%[e]+5] offset0:%[md] offset1:%[md]+2\n\t"
        "s_cbranch_vccnz 1b\n\t"
        "buffer_load_dwordx4 v[%[t1]:%[t1]+3], %[l16], %[rs], %[pt] offen sc1\n\tbuffer_load_dwordx2 v[%[t0]:%[t0]+1], %[l8], %[rs], %[pt] offen offset:1024 sc1\n\t"
        "buffer_load_dwordx4 v[%[t1]+4:%[t1]+7], %[l16], %[rs], %[d] offen sc1\n\tbuffer_load_dwordx2 v[%[t0]+2:%[t0]+3], %[l8], %[rs], %[d] offen offset:1024 sc1\n\t"
        "s_waitcnt lgkmcnt(0)\n\ts_barrier\n\t"
        "ds_read2st64_b64 v[%[e]:%[e]+3], %[lr] offset1:3\n\tds_read2st64_b64 v[%[e]+4:%[e]+7], %[lr] offset0:6 offset1:9\n\ts_waitcnt lgkmcnt(1)\n\t"
        "v_add_f32 v[%[e]], v[%[e]], v[%[e]+2]\n\tv_add_f32 v[%[e]+1], v[%[e]+1], v[%[e]+3]\n\ts_waitcnt lgkmcnt(0)\n\t"
        "v_add_f32 v[%[e]+4], v[%[e]+4], v[%[e]+6]\n\tv_add_f32 v[%[e]+5], v[%[e]+5], v[%[e]+7]\n\tv_add_f32 v[%[e]], v[%[e]], v[%[e]+4]\n\tv_add_f32 v[%[e]+1], v[%[e]+1], v[%[e]+5]\n\t"
        "v_cvt_pk_bf16_f32 v[%[e]+2], v[%[e]], v[%[e]+1]\n\tv_min_u32 v[%[e]], -2, v[%[e]+2]\n\tv_min_u32 v[%[e]+1], -2, v[%[e]+2]\n\ts_nop 1\n\t"
        "v_permlane16_swap_b32 v[%[e]], v[%[e]+1]\n\tv_mov_b64 v[%[e]+2:%[e]+3], v[%[e]:%[e]+1]\n\ts_nop 1\n\t"
        "v_permlane32_swap_b32 v[%[e]], v[%[e]+2]\n\tv_permlane32_swap_b32 v[%[e]+1], v[%[e]+3]\n\t"
        "buffer_store_dwordx4 v[%[e]:%[e]+3], %[vp], %[rs], 0 offen sc1"
        : [n] "=&s"(n), [d] "=&s"(d), [so] "+v"(sxo)
        : [rs] "s"(rs), [t] "s"(tgo), [sp] "s"(sp), [sm] "s"(sm), [sw0] "s"(sw0), [sw1] "s"(sw1), [tg] "s"(tg), [pt] "s"(spt), [l16] "v"(lane16), [l8] "v"(lane8), [vm] "v"(vmm), [vw] "v"(vw), [si] "v"(sidx),
          [lw] "v"(lred), [lr] "v"(lrr), [vp] "v"(vpub), [sx] "v"(sxv), [e] "n"(VE), [p] "n"(VPM), [w] "n"(VW), [u] "n"(U0), [t1] "n"(PT1), [t0] "n"(PT0), [md] "n"(MD / 4), [cap] "n"(SPIN_CAP)
        : "memory", "scc", "vcc", "s98", "s99");
    return n;
}
template <int R> DEVI void tagd(unsigned& m, unsigned t) { asm volatile("v_bitop3_b32 %0, %0, v[%2], %1 bitop3:0xf6" : "+v"(m) : "s"(t), "n"(R)); }
#ifndef DLY_R
#define DLY_R 1200
#endif
#ifndef DLY_X
#define DLY_X 1300
#endif
DEVI void role_R(const Args& a, int w, int l, int T2, int t0) {
    asm volatile("; MARK role_R"); __builtin_amdgcn_s_waitcnt(0);
    using namespace lr;
    const int lane = lane_id(), c = lane & 15, g = lane >> 4;
    const rsrc_t rs = mkrs(a.ws, WS_NEED);
    const unsigned lane16 = lane * 16;
#pragma nounroll
    for (int k = 0; k < 4 * l; ++k) {
        const int tile = k * 32 + T2;
        if (tile >= LN_NOW) hold(t0, DLY_X);
        v4u u[8];
#pragma unroll
        for (int f = 0; f < 8; ++f) u[f] = *(const v4u*)(a.x + (size_t)(tile * 16 + c) * 1024 + (w * 8 + f) * 32 + g * 8);
        if (tile < 2 * NSEG) put_tile(u, rs, tile, w, lane, [&]() { hold(t0, DLY_V); return side(rs, 0); });
        TSW(127, 1 + k);
    }
    auto arm = [&](unsigned n2, int s) {
        if (s < 8)
#pragma unroll
            for (int k = 0; k < 4; ++k) st16(v4u{SENT, SENT, SENT, SENT}, rs, na(n2, N_WM + s * 524288 + T2 * 16384 + w * 4096) + k * 1024 + lane16);
        st16(v4u{SENT, SENT, SENT, SENT}, rs, w < 2 ? na(n2, N_PTX + s * 65536 + T2 * 2048) + w * 1024 + lane16 : lane >= (w == 2 ? 32 : 8) || s >= 8 ? OOB :
                                              na(n2, w == 2 ? N_PM + s * 16384 + T2 * 512 : N_PMM + s * 8192 + T2 * 128) + lane16);
    };
    hold(t0, DLY_R);
    const unsigned nb = side(rs, l), vmem = c < 8 ? na(nb, N_MEM + T2 * 512) + g * 65536 + w * 128 + c * 16 : OOB;
    auto put_mem = [&](v4u v, int s) { st16(v, rs, vmem + rslot(s) * (NREP * 32768)); };
    {
        const unsigned* m0 = (const unsigned*)(a.mem0 + ((w & 1) * 4 + ((lane >> 2) & 1) * 2 + (lane & 1)) * 1024 + T2 * 32 + (w >> 1) * 16 + ((lane >> 1) & 1) * 2);
        const v4u mv{nosent(m0[0]), m0[2], m0[4], m0[6]};
        put_mem(v4u{SENT, SENT, SENT, SENT}, 1); put_mem(v4u{SENT, SENT, SENT, SENT}, 2);
#pragma nounroll
        for (int k = 0; k < 16; ++k) arm(nb, k);
        WV0; BAR;
        put_mem(mv, 0);
    }
    const int sidx = opq((lane & 2) ? 0xEEEE : 0x4444);
    const unsigned vmm = (8 * w + 2 * g) * 128 + c * 8, vw = (8 * w + g) * 32768 + DV(T2 * 512) + c * 16, vptm = g * 2048 + 1536 + c * 16;
    const unsigned sfl = na(nb, N_PA + (unsigned)((T2 & (NC - 1)) * 4 + w) * 128), su0 = na(nb, N_U + 8 * w * 65536 + T2 * 2048), vfl = sfl + c * 4;
    const unsigned lred = L_M + w * MW + ((c >> 2) & 1) * 2 * MD + (c >> 3) * 128 + g * 32 + ((c >> 1) & 1) * 16 + (c & 1) * 4, lrr = L_M + g * MD + w * 64 + (c & 7) * 8;
    auto ld_u = [&](auto B, int s) {
        const unsigned so = su0 + rslot(s) * (4u << 20);
        sfor<0, 16>([&](auto I) { constexpr int j = I.v >> 1, e = I.v & 1; lda<64 * B.v + 16 * (j >> 1) + 8 * e + 4 * (j & 1), 4, e * 1024>(rs, lane16, so + j * 131072); });
    };
    wait_prog(rs, sfl + (lane & 7) * 4, 0, 0);
    ld_u(IC<0>{}, 0);
    const rsrc_t rxi = l ? rs : mkrs(a.x, XB);
    const unsigned vxi = l ? lane16 : c * 2048 + g * 16, sxi = (w & 1) * 32768 + (l ? O_XIN + XB + T2 * 1024 : T2 * 64);
    v2u sxv{0u, 0u};
    unsigned sxo = OOB;
    int tpub = t0;
    auto step = [&](auto B, int s) {
        constexpr int b = B.v;
        TSD;
        TSR(0); TSK(15);
        if (s == 8) { TSC(126, 0); TSW(126, 1); }
        if (s == 56) { TSC(126, 2); TSW(126, 3); }
        const unsigned spt = na(nb, N_PTX + (unsigned)((s & 15) * 32 + 8 * w) * 2048);
        auto ptx = [&](auto N) {
            constexpr int n = N.v & 31, j = n >> 1;
            if constexpr (n == 20) ldv4<VX>(rxi, vxi, sxi + s * 65536);
            else if constexpr (n >= 16) ldv<PTM + 4 * (n - 16), 4, (n & 1) * 256>(rs, vptm, spt + (j - 8) * 16384);
            else if constexpr (n & 1) ldv<PT0 + 2 * j, 2, 1024>(rs, lane * 8, spt + DV(j * 2048));
            else ldv<PT1 + 4 * j, 4, 0>(rs, lane16, spt + DV(j * 2048));
        };
        {
            const unsigned sw = opqs(na(nb, N_WM + rslot(s) * 524288)), sw1 = opqs(sw + 131072), sp = opqs(na(nb, N_PM + w * 4096) + rslot(s) * 32768), sm = opqs(na(nb, N_PMM) + rslot(s) * 16384);
            const unsigned vpub = opq(vmem + rslot(s + 1) * (NREP * 32768)), tg = opqs((s >> 3) & 1), spq = opqs(spt);
            SB;
            if ((int)__builtin_amdgcn_s_memrealtime() - tpub < HLD) { wvm<16 - UIN>(); st8(sxv, rs, sxo, 0); sxo = OOB; hold(tpub, HLD); }
            const int mx = wait_hint(rs, sfl, vfl, s + 1);
            if (left(mx) > SXH * 256) { wvm<16 - UIN>(); st8(sxv, rs, sxo, 0); sxo = OOB; }
            TSR(5);
            const unsigned spins = chain<64 * b>(rs, mx, sp, sm, sw, sw1, tg, spq, lane16, lane * 8, vmm, vw, sidx, lred, lrr, vpub, sxv, sxo);
#if defined(TS) && TS != 4
            tsr[8] = __builtin_amdgcn_readfirstlane(spins) + 1;
#endif
            tpub = (int)__builtin_amdgcn_s_memrealtime();
            TSR(2);
        }
        sfor<4, 21>(ptx);
        BAR;
        f32x4 t[2][2], u[2];
        const unsigned su = s + 1 < NSEG ? su0 + rslot(s + 1) * (4u << 20) : OOB;
        put_mem(v4u{SENT, SENT, SENT, SENT}, s + 3);
#pragma nounroll
        for (int n = 0;; ++n) {
            if (n) { sfor<32, 52>(ptx); WV0; }
            auto lu = [&](auto N) { constexpr int j = N.v >> 1, e = N.v & 1; lda<16 * (j >> 1) + 8 * e + 4 * (j & 1) + 64 * (1 - b), 4, e * 1024>(rs, lane16, su + j * 131072); };
            TSR(6);
            unsigned mt = opq(0);
            const unsigned tp = (s >> 4) & 1;
            sfor<0, 16>([&](auto N) {
                constexpr int j = N.v >> 1, e = N.v & 1, R = 64 * b + 16 * (j >> 1) + 8 * e + 4 * (j & 1), nu = N.v * UIN / 16;
                if constexpr (e == 0) wvm<21 - 2 * j + (j < 2) + nu - 1>();
                if constexpr ((N.v + 1) * UIN / 16 > nu) lu(IC<nu>{});
                mfh<R, PT1 + 4 * j, j == 0, false>(t[1][e]); tagd<e ? PT0 + 2 * j : PT1 + 4 * j>(mt, tp); mfh16<R, PT0 + 2 * j, j == 0>(u[e]);
            });
            wvm<1 + UIN>();
            sfor<0, 8>([&](auto N) { constexpr int q = N.v >> 2, rt = (N.v >> 1) & 1, e = N.v & 1; mfh<VW + 8 * e + 4 * q, PTM + 4 * (2 * q + rt), N.v < 2, true>(t[rt][e]);
                                     tagd<PTM + 2 * N.v>(mt, tp); tagd<PTM + 2 * N.v + 1>(mt, tp); });
            nop<7>(); fence(t[0][0], t[0][1], t[1][0], t[1][1], u[0], u[1]);
#pragma unroll
            for (int e = 0; e < 2; ++e)
#pragma unroll
                for (int i = 0; i < 4; ++i) t[0][e][i] = fadd(t[0][e][i], u[e][i]);
            if (__builtin_amdgcn_uicmp(mt & 1, 0u, 33) == 0 || n == 2) break;
            TSV(s, 7, 1);
            if (n == 1) wait_prog(rs, sfl + 32 + (lane & 7) * 4, 0, (s + 2) << 24);
        }
        TSR(3);
#pragma unroll
        for (int e = 0; e < 2; ++e) { stsq(lane16 + w * 4096, L_T + e * 1024, __builtin_bit_cast(v4u, t[0][e])); stsq(lane16 + w * 4096, L_T + 2048 + e * 1024, __builtin_bit_cast(v4u, t[1][e])); }
        WL0; BAR;
        if (w < 2) {
            float o[8], s1 = 0.f, s2 = 0.f;
#pragma unroll
            for (int e = 0; e < 2; ++e) {
                f32x4 q[4];
#pragma unroll
                for (int k = 0; k < 4; ++k) q[k] = __builtin_bit_cast(f32x4, ldsq(lane16 + w * 2048, L_T + k * 4096 + e * 1024));
#pragma unroll
                for (int i = 0; i < 4; ++i) o[4 * e + i] = fadd(fadd(q[0][i], q[1][i]), fadd(q[2][i], q[3][i]));
            }
            v4u x1;
            sfor<0, 4>([&](auto I) {
                constexpr int i = I.v;
                const unsigned x = rdv<VX + i>(), at = pack2(o[2 * i], o[2 * i + 1]);
                x1[i] = pack2(bflo(x) + bflo(at), bfhi(x) + bfhi(at));
                const float lo = bflo(x1[i]), hi = bfhi(x1[i]);
                s1 += lo + hi; s2 = fmaf(lo, lo, fmaf(hi, hi, s2));
            });
            st16(x1, rs, O_XM + l * XB + s * 65536 + w * 32768 + T2 * 1024 + lane16);
            sxv = v2u{nosent(__float_as_uint(red32(red16(s1)))), __float_as_uint(red32(red16(s2)))};
            sxo = lane < 16 ? O_SX + (unsigned)((l * 64 + s) * 32 + T2) * 256 + (SXL ? w * 128 + c * 8 : c * 16 + w * 8) : OOB;
        }
        TSR(4);
        {
            sfor<UIN, 16>([&](auto N) { constexpr int j = N.v >> 1, e = N.v & 1; lda<16 * (j >> 1) + 8 * e + 4 * (j & 1) + 64 * (1 - b), 4, e * 1024>(rs, lane16, su + j * 131072); });
        }
#if TS != 4
        TSF(s, 0x817D);
#endif
    };
#pragma nounroll
    for (int s = 0; s < NSEG; s += 2) { step(IC<0>{}, s); step(IC<1>{}, s + 1); }
    wvm<16 - UIN>(); st8(sxv, rs, sxo, 0);
    WV0;
}
#ifndef FSIDE
#define FSIDE 1
#endif
#ifndef PDX
#define PDX 0
#endif
constexpr int NU = 2 * NSEG;
#ifndef PSF
#define PSF 40
#endif
#ifndef PSR
#define PSR 12
#endif
#ifndef RS1
#define RS1 42
#endif
constexpr int RSH = 4 * (64 + RS1);
#ifndef RPA
#define RPA 8
#endif
#ifndef GROT
#define GROT 1
#endif
#ifndef LAGK
#define LAGK 4
#endif
#ifndef LAGP
#define LAGP 0
#endif
#ifndef N2F
#define N2F 43
#endif
#ifndef N2M
#define N2M 9
#endif
DEVI unsigned unit_ls(unsigned u, int two) {
    static_assert(NSEG == 64 && LAGD >= 6, "");
    static_assert(LAGP % 2 == 0 && LAGP < 2 * LAGK + 1, "");
    const unsigned d = u - LAGD + LAGP, p = d / (2 * LAGK + 1), r = d % (2 * LAGK + 1), n0 = LAGD - LAGP / 2 + p * (LAGK + 1) + (r >> 1);
    const unsigned n = u < LAGD || n0 >= 64 ? u : ((r & 1) || r == 2 * LAGK) ? n0 : 64 + u - n0;
    return (two ? 64 + N2F + 2 * u : n < 64 + N2F ? n : n + min(n - (63 + N2F), (unsigned)N2M)) & 127;
}
DEVI unsigned unit_tab(int lane, int two) { return opq(unit_ls(2 * lane, two) | unit_ls(2 * lane + 1, two) << 8); }
DEVI unsigned unit_get(unsigned tab, int u) { return ((unsigned)__builtin_amdgcn_readlane(tab, (u >> 1) & 63) >> ((u & 1) * 8)) & 255; }
struct WMG { static constexpr int NT = 3, NP = 16, RB = 0, LB = 0, CBI = 0; static constexpr int kind(int, int) { return 0; } };
struct WMD { static constexpr int NT = 4, NP = 12, RB = 0, LB = 0, CBI = 0; static constexpr int kind(int, int) { return 0; } };
namespace lg { constexpr int L_ST = 155648, L_OK = 159744; }
DEVI void role_GU(const Args& a, int w, int j, int two) {
    asm volatile("; MARK role_GU"); __builtin_amdgcn_s_waitcnt(0);
    using namespace lg;
    const int lane = lane_id(), c = lane & 15, g = lane >> 4;
    const rsrc_t rs = mkrs(a.ws, WS_NEED);
    const unsigned lane16 = lane * 16, hc = 96 * j + 24 * w;
    {
        const unsigned ta = g * 4096 + (lane & 1) * 2048 + w * 256 + ((lane >> 2) & 3) * 64 + ((lane >> 1) & 1) * 8, tb = ta + 65536;
        const rsrc_t r1 = mkrs(a.Wg, WSZ(7168u * 1024)), r2 = mkrs(a.Wu, WSZ(7168u * 1024));
        const unsigned cb = 192 * j + g * 48 + (lane & 3) * 16, v1 = (lane & 3) == 3 || cb >= 7168 ? OOB : (8 * w + ((lane >> 2) & 3)) * 7168 + cb, lw = opqs(w * 4096), z = opqs(0);
        load_blocks<32, 8, 4, true>([&](auto B, auto S) { dma_blk(lw + S.v * 16384, r1, v1, z + B.v * 32 * 7168, 1792); dma_blk(lw + S.v * 16384 + 2048, r2, v1, z + B.v * 32 * 7168, 1792); },
                                    [&](auto B, auto S) { sfor<0, 3>([&](auto T) { constexpr int o = (S.v & 3) * 16384 + T.v * 16; put_frag<WMG, T.v, B.v, o, o + 1024>(S.v < 4 ? ta : tb, 0, nullptr); }); });
        if (!two) TSW(120 - 65536, 0);
        v4u one{0x3F803F80u, 0x3F803F80u, 0x3F803F80u, 0x3F803F80u};
        asm volatile("s_nop 1" : "+v"(one));
        f32x4 cs[3];
        sfor<0, 96>([&](auto N) { constexpr int t = N.v % 3, ks = N.v / 3; mfA<8 * widx<WMG>(t, ks >> 1) + 4 * (ks & 1), ks == 0>(cs[t], one); });
        nop<7>(); fence(cs[0], cs[1], cs[2]);
        sfor<0, 3>([&](auto T) { mwr4<384 + 4 * T.v>(__builtin_bit_cast(v4u, cs[T.v])); });
        BAR;
    }
    const unsigned nf = two ? O_N + PG2 + (((two >> 1) ^ FSIDE) & 1) * 8192 : side(rs, FSIDE);
    const unsigned wlo = (unsigned)(uintptr_t)a.ws, whi = (unsigned)((uintptr_t)a.ws >> 32);
    const unsigned xr = w * 8192 + (GROT ? (j & 1) * 4096 + (j & 2) * 16384 : 0);
    const v8u pv{lane16, O_SX + (8 * w + g) * 256 + c * 16, O_H + (g & 1) * 114688 + hc * 32 + c * 16 + (g >> 1) * 8, lane == 0 ? na(nf, N_PG) + j * 128 + w * 4 : OOB,
                 (unsigned)(L_ST + ((lane & 1) * 16 + ((lane & 31) >> 1)) * 16), L_ST + lane16 + w * 4, unit_tab(lane, two), lane16};
    const v16u s0{wlo, whi, RSZ, 0x20000u, wlo, whi, 0u, 0x20000u, OOB, OOB, hc < 3584 ? 0u : OOB, hc + 8 < 3584 ? 0u : OOB, 0u, O_XM + xr, 65536 + xr, 0u};
    const v3u s1{0u, O_PROF + ((blockIdx.x & 255) * 4 + w) * 8192, (unsigned)(two ? N2M : NU - N2M)};
    asm volatile(G_LOOP :: "{v[0:7]}"(pv), "{s[8:23]}"(s0), "{s[24:26]}"(s1) : "memory", "s99", "vcc");
}
DEVI void role_DN(const Args& a, int w, int p, int cg, int two) {
    asm volatile("; MARK role_DN"); __builtin_amdgcn_s_waitcnt(0);
    using namespace lg;
    const int lane = lane_id();
    const rsrc_t rs = mkrs(a.ws, WS_NEED);
    const unsigned lane16 = lane * 16;
    const int F0 = p < 3 ? 23 * p : (p == 3 ? 68 : 89), zb = p < 2 ? -1 : (p == 4 ? 0 : 22);
    {
        const unsigned ta = w * 18432 + (lane >> 4) * 512 + ((lane >> 2) & 3) * 64 + (lane & 3) * 16;
        const rsrc_t r1 = mkrs(a.Wd, WSZ(3584u * 2048));
        const unsigned v1 = (F0 * 32 + (lane >> 2)) * 2048 + cg * 512 + w * 128 + (lane & 3) * 16;
        load_blocks<46, 9, 2>([&](auto B, auto S) { dma_blk(w * 18432 + S.v * 2048, r1, (B.v >> 1) == zb ? OOB : v1, (B.v >> 1) * 65536 + (B.v & 1) * 64, 2048); },
                              [&](auto B, auto S) { constexpr int ks = B.v >> 1, t = 2 * (B.v & 1), o = S.v * 2048;
                                                    put_frag<WMD, t, ks, o, o + 256>(ta, 0, nullptr); put_frag<WMD, t + 1, ks, o + 8, o + 264>(ta, 0, nullptr); });
        if (!two) TSW(120 - 65536, 0);
        BAR;
    }
    const int g0 = F0 / 3 * 4, gn = ((F0 + 22) / 3 + 1) * 4 - g0;
    const unsigned nf = two ? O_N + PG2 + (((two >> 1) ^ FSIDE) & 1) * 8192 : side(rs, FSIDE);
    const unsigned wlo = (unsigned)(uintptr_t)a.ws, whi = (unsigned)((uintptr_t)a.ws >> 32);
    const v8u pv{lane16, na(nf, N_PG) + g0 * 32 + (lane < gn ? (lane >> 2) * 128 + (lane & 3) * 4 : 0), lane == 0 ? na(nf ^ PDX, N_PD) + (cg * NKF + p) * 128 + w * 4 : OOB,
                 unit_tab(lane, two), lane16, (unsigned)(L_OK + lane * 4), (unsigned)((lane & 8) * 2048 + (lane >> 4) * 128 + (lane & 7) * 16), 0u};
    const v16u s0{wlo, whi, RSZ, 0x20000u, wlo, whi, 0u, 0x20000u, wlo, whi, w == 0 ? RSZ : 0u, 0x20000u, 0u, O_H + (w >> 1) * 114688 + (F0 + (w & 1) * 12) * 1024,
                  O_D + p * 65536 + (cg * 8 + w * 2) * 512, (unsigned)(65536 + (w >> 1) * 32768 + (w & 1) * 12288)};
    const v4u s1{(unsigned)(L_OK + w * 256), 0u, 0u, O_PROF + ((blockIdx.x & 255) * 4 + w) * 8192};
    asm volatile(D_LOOP :: "{v[0:7]}"(pv), "{s[8:23]}"(s0), "{s[24:27]}"(s1), "{s28}"(two ? N2M : NU - N2M) : "memory", "s99", "vcc");
}
template <int R> DEVI void ldv1(rsrc_t rs, unsigned vo, unsigned so) { asm volatile("buffer_load_dword v[%3], %0, %1, %2 offen sc1" :: "v"(vo), "s"(rs), "s"(so), "n"(R) : "memory"); }
template <int R, int I> DEVI void unp2(f32x4& d) { float x, y; asm volatile("v_lshlrev_b32 %0, 16, v[%2]\n\tv_and_b32 %1, 0xffff0000, v[%2]" : "=v"(x), "=v"(y) : "n"(R)); d[I] = x; d[I + 1] = y; }
DEVI void role_RED(const Args& a, int w, int cu) {
    asm volatile("; MARK role_RED"); __builtin_amdgcn_s_waitcnt(0);
    const int lane0 = lane_id(), c = lane0 & 15, g = lane0 >> 4;
    const rsrc_t rs = mkrs(a.ws, WS_NEED);
    const unsigned nf = side(rs, FSIDE);
    v4u sel[2];
#pragma unroll
    for (int e = 0; e < 2; ++e)
#pragma unroll
        for (int i = 0; i < 4; ++i) sel[e][i] = (g == (c >> 2) && i == 2 * e + ((c >> 1) & 1)) ? ((c & 1) ? 0x3F800000u : 0x3F80u) : 0u;
    const unsigned vfl = na(nf ^ PDX, N_PD) + w * (NKF * 128) + (lane0 < 20 ? (lane0 >> 2) * 128 + (lane0 & 3) * 4 : 0);
    constexpr int VP = NV + 32;
    const unsigned lo = (c & 8) * 128 + g * 256 + (c & 7) * 16 + (cu & 1) * 128 + w * 8192, ld = (c & 8) * 64 + g * 128 + (c & 7) * 16 + (cu & 1) * 16384 + w * 4096;
    const unsigned lout = ((cu & 1) * 8 + (c & 7)) * 2048 + w * 512 + (c & 8) * 8 + g * 16, lw = g == (c >> 2) ? (c & 7) * 64 + w * 16 + (c & 8) : 1024 + lane0 * 8;
    const rsrc_t ro = mkrs(a.out, 4u << 20);
    v4u one{0x3F803F80u, 0x3F803F80u, 0x3F803F80u, 0x3F803F80u};
    asm volatile("" : "+v"(one));
    #pragma unroll
    for (int i = 0; i < 2; ++i) if (2 * lane0 + i < NU - N2M) *LDSP(unsigned char, 2048 + unit_ls(2 * lane0 + i, 0)) = 2 * lane0 + i;
    if (lane0 < N2M) *LDSP(unsigned char, 2048 + unit_ls(lane0, 1)) = 128 + lane0;
    WL0; BAR;
    const unsigned tab = opq(*LDSP(const unsigned short, 2048 + 2 * lane0));
    auto next = [&](int k) { const int n = k + (k >= RSH ? 6 : k < 256 ? 4 : 2); return cu > 3 && k < 4 ? 252 + cu : k < RSH && n >= (k < 256 ? 256 : RSH) ? RSH + cu : n; };
    auto issue = [&](auto B, auto N0, auto N1, int k) {
        const unsigned ls = k >> 2, base = ((k >> 1) & 1) * 32768;
        sfor<N0.v, N1.v>([&](auto N) { constexpr int f = (N.v - 4) / 5, p = (N.v - 4) % 5;
            if constexpr (N.v < 4) ldv4<NV + 16 * B.v + 4 * N.v>(rs, lo, O_XM + ls * 65536 + base + N.v * 2048);
            else lda<80 * B.v + 20 * f + 4 * p, 4, f * 1024>(rs, ld, O_D + (ls * NKF + p) * 65536 + base); });
        if constexpr (N1.v == 24) { const int k2 = next(k); ldv1<VP>(rs, vfl, k2 < 4 * NU ? (unit_get(tab, k2 >> 2) >> 7) * PG2 : 0u); }
    };
    u64 late = 1, far = 1;
    bool pre = false;
    auto task = [&](auto B, int k) {
        constexpr int b = B.v;
        const int ut = unit_get(tab, k >> 2), u = ut & 127, rt = (k >> 1) & 1, kn = next(k), un = kn < 4 * NU ? unit_get(tab, kn >> 2) : 127, nx = (un & 127) + 1;
        const unsigned sf = (ut >> 7) * PG2, sfn = (un >> 7) * PG2;
        const unsigned ls = k >> 2, tile = ls * 2 + rt;
        TSD; TSR(0);
        if (!pre) {
            if (late) {
                unsigned spins = 0;
#pragma nounroll
                for (;;) {
                    if (far) __builtin_amdgcn_s_sleep(PSF); else __builtin_amdgcn_s_sleep(PSR);
                    CB;
                    const int v = (int)ld4(rs, vfl, sf);
                    if (__builtin_amdgcn_sicmp(v, u + 1, 40) == 0) break;
                    far = __builtin_amdgcn_sicmp(v, u, 40);
                    spin_bail(spins);
                }
            }
            TSR(1);
            issue(B, IC<0>{}, IC<24>{}, k);
        }
        else { TSR(1); }
        const bool l0 = ls < 64;
        const rsrc_t rd = l0 ? rs : ro;
        const unsigned vd = opq((l0 ? O_XIN + XB + lo : lout - 64 * 65536) + tile * 32768), fs = l0 ? 2048 : 128;
        v4u x[4];
        f32x4 d[4][2], S, Q;
        auto unp = [&](auto F, auto I) { unp2<NV + 16 * b + 4 * F.v + I.v, 2 * (I.v & 1)>(d[F.v][I.v >> 1]); };
        auto fin = [&](auto F) {
            constexpr int f = F.v;
            fence(d[f][0], d[f][1]);
#pragma unroll
            for (int i = 0; i < 4; ++i) x[f][i] = pack2(d[f][i >> 1][2 * (i & 1)], d[f][i >> 1][2 * (i & 1) + 1]);
            asm volatile("" : "+v"(x[f]));
        };
        auto put = [&](auto F) { constexpr int f = F.v; st16s(x[f], rd, vd, f * fs); SB; mfv<f == 0>(S, one, x[f]); mfv<f == 0>(Q, x[f], x[f]); SB; };
        wvm<24>();
        sfor<0, 4>([&](auto I) { unp(IC<0>{}, I); });
        sfor<0, 4>([&](auto F) {
            constexpr int f = F.v;
            wvm<16 - 5 * f + (f ? f - 1 : 0)>();
            nop<1>();
            sfor<0, 10>([&](auto N) {
                mfB<80 * b + 20 * f + 4 * (N.v >> 1), false>(d[f][N.v & 1], sel[N.v & 1]);
                if constexpr (N.v < 4 && f < 3) unp(IC<(f + 1) & 3>{}, N);
                if constexpr (N.v == 5 && f > 0) fin(IC<(f + 3) & 3>{});
                SB;
            });
            if constexpr (f > 0) put(IC<(f + 3) & 3>{});
        });
        wvm<3>();
        TSR(2);
#ifdef TS
        tsr[4] = pre + 1;
#endif
        const bool p1 = ltv<VP>(nx) == 0;
        if (!p1) ldv1<VP + 1>(rs, vfl, sfn);
        nop<3>();
        fin(IC<3>{});
        nop<1>();
        put(IC<3>{});
        auto second = [&](auto NS) {
            wvm<NS.v>();
            pre = ltv<VP + 1>(nx) == 0;
            if (pre) { issue(IC<1 - b>{}, IC<0>{}, IC<24>{}, kn); if constexpr (NS.v == 5) wvm<25>(); }
            else {
                if constexpr (NS.v == 5) { ldv1<VP + 1>(rs, vfl, sfn); WV0; }
                late = ltv<VP + 1>(nx); far = ltv<VP + 1>(nx - 1);
            }
        };
        if (l0) {
            nop<7>();
            fence(S, Q);
            const float qd = selm(selm(Q[0], Q[1], 0xAAAAAAAAAAAAAAAAull), selm(Q[2], Q[3], 0xAAAAAAAAAAAAAAAAull), 0xCCCCCCCCCCCCCCCCull);
            *LDSP(f32x2, lw) = f32x2{S[0], qd};
            if (p1) issue(IC<1 - b>{}, IC<0>{}, IC<RPA>{}, kn);
            f32x2 xf[4][4];
#pragma unroll
            for (int f = 0; f < 4; ++f)
#pragma unroll
                for (int i = 0; i < 4; ++i) { xf[f][i] = f32x2{bflo(x[f][i]), bfhi(x[f][i])}; asm volatile("" : "+v"(xf[f][i])); }
            TSR(5);
            WL0; BAR;
            f32x4 t[4];
#pragma unroll
            for (int i = 0; i < 4; ++i) t[i] = __builtin_bit_cast(f32x4, ldsq((c & 7) * 64, i * 16));
            const f32x4 t4 = (t[0] + t[1]) + (t[2] + t[3]);
            const float mean = (t4[0] + t4[2]) * (1.0f / 1024.0f), var = fmaxf(rbf(fmaf(t4[1] + t4[3], 1.0f / 1024.0f, -mean * mean)), 0.f);
            const float inv = __builtin_amdgcn_rcpf(rbf(__builtin_amdgcn_sqrtf(rbf(var + 1e-5f)))), mi = -rbf(mean) * inv;
#pragma unroll
            for (int f = 0; f < 4; ++f) {
                v4u o;
#pragma unroll
                for (int i = 0; i < 4; ++i) xf[f][i] = __builtin_elementwise_fma(xf[f][i], f32x2{inv, inv}, f32x2{mi, mi});
#pragma unroll
                for (int i = 0; i < 4; ++i) o[i] = pack2(xf[f][i][0], xf[f][i][1]);
                st16(o, rs, vd + (O_XN - O_XIN) + f * 2048);
            }
            TSR(6);
            if (p1) { issue(IC<1 - b>{}, IC<RPA>{}, IC<24>{}, kn); TSR(7); wvm<25 - RPA>(); pre = true; } else second(IC<5>{});
            BAR;
            st4(0u, rs, lane0 < 2 && w == 0 ? na(nf ^ (FSIDE ? 0 : 8192), N_FX) + tile * 16 + (cu & 1) * 8 + lane0 * 4 : OOB, 0);
        }
        else if (p1) { issue(IC<1 - b>{}, IC<0>{}, IC<24>{}, kn); pre = true; }
        else second(IC<1>{});
        TSR(3); TSF(ut & 128 ? 127 : u, 0xFF);
    };
#pragma nounroll
    for (int k = cu < 2 ? cu + 4 : cu & 3;;) {
        task(IC<0>{}, k); k = next(k);
        if (k >= 4 * NU) break;
        task(IC<1>{}, k); k = next(k);
        if (k >= 4 * NU) break;
    }
}
#ifndef LSW
#define LSW 0
#endif
#ifndef DLY_A
#define DLY_A 250
#endif
#ifndef DLY_B
#define DLY_B 0
#endif
#ifndef DLY_1
#define DLY_1 1600
#endif
#ifndef DLY_G
#define DLY_G 1400
#endif
#ifndef DLY_D
#define DLY_D 1600
#endif
__global__ __launch_bounds__(256, 1) __attribute__((amdgpu_num_vgpr(NV))) void mega6(Args a) {
    asm volatile("" ::: "v255", "a255");
    const int w = __builtin_amdgcn_readfirstlane(threadIdx.x >> 6), b = blockIdx.x, xcd = b & 7, slot = b >> 3, l = (xcd >> 2) ^ LSW, idx = (slot & 7) * 4 + (xcd & 3);
    const int t0 = (int)__builtin_amdgcn_s_memrealtime();
    if (b < 256) TSW(127, 0);
    const int f = slot - 24, two = b < 256 ? 0 : 1 + ((b >> 8) & 2), k2 = b & 255;
    const int ia = l || !N2M || idx >= 26 ? -1 : idx < 20 ? 38 + (idx & 3) * 5 + (idx >> 2) : 12 + idx, ib = l || !N2M ? -1 : (idx & 3) * 8 + (idx >> 2);
    int gu = f >= 0 && f < (xcd < 3 ? 6 : 4) ? (xcd < 3 ? 6 * xcd + f : 6 + 4 * xcd + f) : -1, dn = (xcd - 3) * 4 + f - 4;
    if (two) { gu = k2 < 38 ? k2 : -1; dn = k2 - 38; }
    if (slot < 24 && l && (slot < 16 || idx >= LN_NOW)) pull_all(a, w, (slot >> 3) * 32 + idx - (slot < 16 ? 0 : LN_NOW), t0);
    if (slot < 8) { hold(t0, l ? DLY_1 : DLY_A); role_A(a, w, l, idx, (xcd & 3) * 2, ia); }
    else if (slot < 16) { hold(t0, l ? DLY_1 : DLY_B); role_B(a, w, l, idx, (xcd & 3) * 2, ib); }
    else if (slot < 24) { role_R(a, w, l, idx, t0); if (l && idx + w == 0) vote_end(mkrs(a.ws, WS_NEED)); }
    else if (gu >= 0) { hold(t0, two ? 0 : DLY_G); role_GU(a, w, gu, two); }
    else if (xcd < 3 && !two) {
        const int cu = xcd + 3 * (f - 6), q = cu * 4 + w;
        use(ld4(mkrs(a.ws, WS_NEED), q < 2 * NVK + 2 ? prb(q >> 1, q & 1) + lane_id() * 32 : OOB, 0));
        hold(t0, DLY_D); role_RED(a, w, cu);
    }
    else { if (xcd < 7 && !two) vote(mkrs(a.ws, WS_NEED), w, dn, l, t0); hold(t0, two ? 0 : DLY_D); role_DN(a, w, dn >> 2, dn & 3, two); }
}
extern "C" void kernel_launch(void* const* d_in, const int* in_sizes, int n_in, void* d_out, int out_size, void* d_ws, size_t ws_size, hipStream_t stream) {
    (void)in_sizes; (void)n_in; (void)out_size; (void)ws_size;
    (void)hipFuncSetAttribute((const void*)mega6, hipFuncAttributeMaxDynamicSharedMemorySize, 163840);
    d_ws = (void*)(((uintptr_t)d_ws + (2u << 20) - 1) & ~(uintptr_t)((2u << 20) - 1));
    Args a{(const u16*)d_in[0], (const u16*)d_in[1], (const u16*)d_in[2], (const u16*)d_in[3], (const u16*)d_in[4], (const u16*)d_in[5],
           (const u16*)d_in[8], (const u16*)d_in[9], (const u16*)d_in[10], (char*)d_ws, (u16*)d_out};
    mega6<<<256, 256, 163840, stream>>>(a);
}
```

```cpp
#include <hip/hip_runtime.h>
#include <stdint.h>

typedef unsigned short u16;
typedef unsigned long long u64;
typedef __attribute__((ext_vector_type(8))) __bf16 bf8;
typedef __attribute__((ext_vector_type(16))) __bf16 bf16v;
typedef __attribute__((ext_vector_type(4))) float f32x4;
typedef __attribute__((ext_vector_type(16))) float f32x16;
typedef __attribute__((ext_vector_type(2))) float f32x2;
typedef __attribute__((ext_vector_type(8))) unsigned v8u;
typedef __attribute__((ext_vector_type(4))) unsigned v4u;
typedef __attribute__((ext_vector_type(3))) unsigned v3u;
typedef __attribute__((ext_vector_type(2))) unsigned v2u;
typedef __amdgpu_buffer_rsrc_t rsrc_t;
typedef __attribute__((address_space(3))) char lds_char;
#define DEVI __device__ __forceinline__
#define LDSP(T, p) ((__attribute__((address_space(3))) T*)(p))

typedef __attribute__((ext_vector_type(2))) __bf16 bf16x2_t;
DEVI unsigned pack2(float lo, float hi) { f32x2 v = {lo, hi}; bf16x2_t b = __builtin_convertvector(v, bf16x2_t); return __builtin_bit_cast(unsigned, b); }
DEVI float bflo(unsigned u) { return __uint_as_float(u << 16); }
DEVI float bfhi(unsigned u) { return __uint_as_float(u & 0xffff0000u); }
DEVI float rbf(float x) { return bflo(pack2(x, x)); }
constexpr int NV = 112;
#define MR_(R, A_STR, V_STR, OUTS, INS, W) do { if constexpr ((R) < 256) asm volatile(A_STR : OUTS : INS, [lo] "n"(R), [hi] "n"((R) + (W) - 1)); \
                                                else asm volatile(V_STR : OUTS : INS, [lo] "n"((R) - 256 + NV), [hi] "n"((R) - 256 + NV + (W) - 1)); } while (0)
#define CM_ ,
template <int R> DEVI void mwr(unsigned x) { if constexpr (R < 256) asm volatile("v_accvgpr_write_b32 a[%1], %0" :: "v"(x), "n"(R)); else asm volatile("v_mov_b32 v[%1], %0" :: "v"(x), "n"(R - 256 + NV)); }
template <int R> DEVI void mwr4(v4u x) { mwr<R>(x[0]); mwr<R + 1>(x[1]); mwr<R + 2>(x[2]); mwr<R + 3>(x[3]); }
#define M16 "v_mfma_f32_16x16x32_bf16 "
#define M32 "v_mfma_f32_32x32x16_bf16 "
#define S16 "v_smfmac_f32_16x16x64_bf16 "
template <int R, bool Z> DEVI void mfA(f32x4& d, v4u b) {
    if constexpr (Z) MR_(R, M16 "%[d], a[%[lo]:%[hi]], %[b], 0", M16 "%[d], v[%[lo]:%[hi]], %[b], 0", [d] "=&v"(d), [b] "v"(b), 4);
    else MR_(R, M16 "%[d], a[%[lo]:%[hi]], %[b], %[d]", M16 "%[d], v[%[lo]:%[hi]], %[b], %[d]", [d] "+v"(d), [b] "v"(b), 4);
}
template <int R, bool Z> DEVI void mfB(f32x4& d, v4u a) {
    if constexpr (Z) MR_(R, M16 "%[d], %[a], a[%[lo]:%[hi]], 0", M16 "%[d], %[a], v[%[lo]:%[hi]], 0", [d] "=&v"(d), [a] "v"(a), 4);
    else MR_(R, M16 "%[d], %[a], a[%[lo]:%[hi]], %[d]", M16 "%[d], %[a], v[%[lo]:%[hi]], %[d]", [d] "+v"(d), [a] "v"(a), 4);
}
template <int R, bool Z> DEVI void mf32A(f32x16& d, v4u b) {
    if constexpr (Z) MR_(R, M32 "%[d], a[%[lo]:%[hi]], %[b], 0", M32 "%[d], v[%[lo]:%[hi]], %[b], 0", [d] "=&v"(d), [b] "v"(b), 4);
    else MR_(R, M32 "%[d], a[%[lo]:%[hi]], %[b], %[d]", M32 "%[d], v[%[lo]:%[hi]], %[b], %[d]", [d] "+v"(d), [b] "v"(b), 4);
}
template <bool Z> DEVI void mfv(f32x4& d, v4u a, v4u b) {
    if constexpr (Z) asm volatile(M16 "%0, %1, %2, 0" : "=&v"(d) : "v"(a), "v"(b)); else asm volatile(M16 "%0, %1, %2, %0" : "+v"(d) : "v"(a), "v"(b));
}
template <int R> DEVI void smf(f32x4& d, v4u a, int idx) { MR_(R, S16 "%[d], %[a], a[%[lo]:%[hi]], %[i]", S16 "%[d], %[a], v[%[lo]:%[hi]], %[i]", [d] "+v"(d), [a] "v"(a) CM_ [i] "v"(idx), 8); }
DEVI void smfv(f32x4& d, v4u a, v8u b, int idx) { asm volatile(S16 "%0, %1, %2, %3" : "+v"(d) : "v"(a), "v"(b), "v"(idx)); }
DEVI void zero4(f32x4& d) { d = f32x4{0.f, 0.f, 0.f, 0.f}; asm volatile("" : "+v"(d)); }
template <int N> DEVI void nop() { asm volatile("s_nop %0" :: "n"(N)); }
DEVI void fence(f32x4& d) { asm volatile("" : "+v"(d)); }
template <class... T> DEVI void fence(f32x4& d, T&... r) { fence(d); fence(r...); }
DEVI float selm(float a, float b, u64 m) { float r; asm("v_cndmask_b32 %0, %1, %2, %3" : "=v"(r) : "v"(a), "v"(b), "s"(m)); return r; }
DEVI int opqs(int x) { asm volatile("" : "+s"(x)); return x; }
DEVI v4u lo4(v8u v) { return __builtin_shufflevector(v, v, 0, 1, 2, 3); }
DEVI v4u hi4(v8u v) { return __builtin_shufflevector(v, v, 4, 5, 6, 7); }
DEVI v8u cat8(v4u a, v4u b) { return __builtin_shufflevector(a, b, 0, 1, 2, 3, 4, 5, 6, 7); }
constexpr int AUX_SC1 = 16;
DEVI v4u ld16(rsrc_t r, unsigned vo, unsigned so) { return __builtin_amdgcn_raw_buffer_load_b128(r, vo, so, AUX_SC1); }
DEVI v2u ld8(rsrc_t r, unsigned vo, unsigned so) { return __builtin_amdgcn_raw_buffer_load_b64(r, vo, so, AUX_SC1); }
DEVI unsigned ld4(rsrc_t r, unsigned vo, unsigned so) { return __builtin_amdgcn_raw_buffer_load_b32(r, vo, so, AUX_SC1); }
DEVI void st16(v4u v, rsrc_t r, unsigned vo) { __builtin_amdgcn_raw_buffer_store_b128(v, r, vo, 0, AUX_SC1); }
DEVI void st16s(v4u v, rsrc_t r, unsigned vo, unsigned so) { __builtin_amdgcn_raw_buffer_store_b128(v, r, vo, so, AUX_SC1); }
DEVI void st8(v2u v, rsrc_t r, unsigned vo, unsigned so) { __builtin_amdgcn_raw_buffer_store_b64(v, r, vo, so, AUX_SC1); }
DEVI void st4(unsigned v, rsrc_t r, unsigned vo, unsigned so) { __builtin_amdgcn_raw_buffer_store_b32(v, r, vo, so, AUX_SC1); }
#define CB asm volatile("" ::: "memory")
#define WL0 asm volatile("s_waitcnt lgkmcnt(0)" ::: "memory")
#define WV0 asm volatile("s_waitcnt vmcnt(0)" ::: "memory")
#define BAR do { __builtin_amdgcn_s_barrier(); CB; } while (0)
#define SB __builtin_amdgcn_sched_barrier(0)
DEVI float fadd(float a, float b) { float r; asm("v_add_f32 %0, %1, %2" : "=v"(r) : "v"(a), "v"(b)); return r; }
DEVI float vfma(float a, float b, float c) { float r; asm("v_fma_f32 %0, %1, %2, %3" : "=v"(r) : "v"(a), "v"(b), "v"(c)); return r; }
DEVI unsigned vxor(unsigned a, unsigned b) { unsigned r; asm volatile("v_xor_b32 %0, %1, %2" : "=v"(r) : "i"(b), "v"(a)); return r; }
DEVI int lane_id() { int r; asm volatile("v_mbcnt_lo_u32_b32 %0, -1, 0\n\tv_mbcnt_hi_u32_b32 %0, -1, %0" : "=v"(r)); __builtin_assume(r >= 0 && r < 64); return r; }
DEVI int opq(int x) { asm volatile("" : "+v"(x)); return x; }
DEVI float vmaxf(float a, float b) { float r; asm("v_max_f32 %0, %1, %2" : "=v"(r) : "v"(a), "v"(b)); return r; }
DEVI void swap32(float& a, float& b) { auto r = __builtin_amdgcn_permlane32_swap(__float_as_uint(a), __float_as_uint(b), false, false); a = __uint_as_float(r[0]); b = __uint_as_float(r[1]); }
DEVI void swap32u(unsigned& a, unsigned& b) { auto r = __builtin_amdgcn_permlane32_swap(a, b, false, false); a = r[0]; b = r[1]; }
DEVI void swap16u(unsigned& a, unsigned& b) { auto r = __builtin_amdgcn_permlane16_swap(a, b, false, false); a = r[0]; b = r[1]; }
DEVI float hadd(float a, float b) { swap32(a, b); return fadd(a, b); }
DEVI float red32(float x) { float y = x; swap32(x, y); return x + y; }
DEVI float red16(float x) { unsigned a = __float_as_uint(x), b = a; swap16u(a, b); return __uint_as_float(a) + __uint_as_float(b); }
DEVI float mx32(float x) { float y = x; swap32(x, y); return vmaxf(x, y); }
DEVI float mx16(float x) { unsigned a = __float_as_uint(x), b = a; swap16u(a, b); return vmaxf(__uint_as_float(a), __uint_as_float(b)); }
template <int N> struct IC { static constexpr int v = N; };
template <int I, int N, class F> DEVI void sfor(F&& f) { if constexpr (I < N) { f(IC<I>{}); sfor<I + 1, N>(f); } }
#ifndef NSEG
#define NSEG 64
#endif
#ifndef LAGD
#define LAGD 10
#endif
constexpr float SC_L2E = 0.08838834764831845f * 1.4426950408889634f;
constexpr unsigned SENT = 0xFFFFFFFFu;
#ifndef NUS_
#define NUS_ 8
#endif
#ifndef NRG_
#define NRG_ 8
#endif
#ifndef NC
#define NC 8
#endif
constexpr int NREP = 8, NUS = NUS_, NRG = NRG_, NKF = 5, NGU = 38;
constexpr unsigned XB = 4u << 20;
constexpr unsigned O_XIN = 0;
constexpr unsigned O_XN  = O_XIN + 2 * XB;
constexpr unsigned O_XM  = O_XN + 2 * XB;
constexpr unsigned O_H   = O_XM + 2 * XB;
constexpr unsigned O_D   = O_H + 2u * 64 * 224 * 1024;
constexpr unsigned O_N = O_D + 2u * 64 * NKF * 65536;
constexpr unsigned DV(unsigned o) { return ((o & ~8191u) << 1) | (o & 8191u); }
DEVI unsigned na(unsigned nb, unsigned o) { return (DV(o) + nb) ^ ((o >> 7) & 8192u); }
constexpr unsigned N_PM  = 0;
constexpr unsigned N_PMM = N_PM + NRG * 16384;
constexpr unsigned N_PA  = (1u << 20) - 16384;
constexpr unsigned N_MEM = 1u << 20;
constexpr unsigned N_WM  = 2u << 20;
constexpr unsigned N_PTX = 6u << 20;
constexpr unsigned N_U   = 10u << 20;
static_assert(NRG == 8 && NUS == 8 && NREP == 8 && O_N % (4u << 20) == 0, "");
constexpr unsigned O_FLG_END = O_N + DV(N_MEM + NREP * 16384);
constexpr unsigned N_FX  = N_PA - 8192;
constexpr unsigned N_PG  = N_FX - 8192;
constexpr unsigned N_PD  = N_PG + 40 * 128;
constexpr unsigned PG2 = DV(N_PA + 8192 - N_PG);
constexpr unsigned O_PRB = O_N + (512u << 10), O_CAL = O_N + (768u << 10);
constexpr unsigned O_SX  = O_N + DV(N_PG) - 2u * 64 * 32 * 256;
constexpr unsigned O_FLG = O_SX;
static_assert(O_PRB >= O_N + DV(N_PMM + NRG * 8192) && O_FLG >= O_CAL + 8192 && (O_FLG_END - O_FLG) % 65536 == 0, "");
constexpr unsigned OOB = 0x80000000u;
constexpr unsigned WS_NEED = O_N + DV(N_U + NUS * (2u << 20));
constexpr unsigned O_PROF = 240u << 20;
static_assert(WS_NEED <= (200u << 20), "workspace too large");

#ifdef DRY
#define WSZ(x) 0u
#else
#define WSZ(x) (x)
#endif
struct Args { const u16 *x, *mem0, *Wq, *Wk, *Wv, *Wo, *Wg, *Wu, *Wd; char* ws; u16* out; };
#ifndef SPIN_LOG
#define SPIN_LOG 18
#endif
constexpr unsigned SPIN_CAP = 1u << SPIN_LOG;
DEVI void spin_bail(unsigned& spins) {
    asm volatile("s_add_u32 %0, %0, 1\n\ts_cmp_lt_u32 %0, %1\n\ts_cbranch_scc1 1f\n\ts_endpgm\n1:" : "+s"(spins) : "n"(SPIN_CAP) : "scc");
}
template <int K> DEVI void wrl(unsigned& v, u64& t) { asm volatile("" : "+s"(t)); asm volatile("v_writelane_b32 %0, %1, %2" : "+v"(v) : "s"((unsigned)t), "n"(K)); }
#ifdef TS
#ifndef TSCLK
#define TSCLK __builtin_amdgcn_s_memrealtime
#endif
#define TSV(u, i, v) __builtin_amdgcn_raw_buffer_store_b32((unsigned)(v), mkrs(a.ws, 256u << 20), lane_id() == 0 ? O_PROF + ((blockIdx.x * 4 + w) * 128 + (u)) * 64 + (i) * 4 : OOB, 0, 0)
#define TSW(u, i) TSV(u, i, TSCLK())
#define TSC(u, i) TSV(u, i, __builtin_amdgcn_s_memtime())
#define TSD u64 tsr[16] = {}
#define TSR(k) tsr[k] = rclk()
#define TSK(k) asm volatile("s_memtime %0" : "=s"(tsr[k]))
#define TSF(u, m) do { unsigned tv_ = 0; WL0; sfor<0, 16>([&](auto K) { if constexpr (((m) >> K.v) & 1) wrl<K.v>(tv_, tsr[K.v]); }); \
    const int tl_ = lane_id(); __builtin_amdgcn_raw_buffer_store_b32(tv_, mkrs(a.ws, 256u << 20), (tl_ < 16 && (((m) >> tl_) & 1)) ? O_PROF + ((blockIdx.x * 4 + w) * 128 + (u)) * 64 + tl_ * 4 : OOB, 0, 0); } while (0)
#if TS == 4
#undef TSV
#undef TSR
#undef TSK
#define TSV(u, i, v)
#define TSR(k)
#define TSK(k)
#define FSK(G, O, D) if constexpr ((G) >= (O) && ((G) - (O)) % (D) == 0 && ((G) - (O)) / (D) < FN) asm volatile("s_memtime %0" : "=s"(tsr[((G) - (O)) / (D)]))
#define FSF(u) TSF(u, (1 << FN) - 1)
#define FSD TSD
#endif
#else
#define TSW(u, i)
#define TSC(u, i)
#define TSV(u, i, v)
#define TSD
#define TSR(k)
#define TSK(k)
#define TSF(u, m)
#endif
#if TS != 4
#define FSK(G, O, D)
#define FSF(u)
#define FSD
#endif
DEVI unsigned rslot(int s) { return (unsigned)(s & (NRG - 1)); }
DEVI u64 pend(v4u d) { return __builtin_amdgcn_uicmp(max(d[0], d[2]), SENT, 32); }
DEVI u64 pend2(v2u d) { return __builtin_amdgcn_uicmp(d[0], SENT, 32); }
DEVI u64 pend1(unsigned d) { return __builtin_amdgcn_uicmp(d, SENT, 32); }
DEVI unsigned nosent(unsigned u) { return min(u, 0xFFFFFFFEu); }
#ifndef WPOL
#define WPOL ""
#endif
template <int OFF, bool SC1> DEVI void dma1(rsrc_t rs, unsigned vo, unsigned so) {
    if constexpr (SC1) asm volatile("buffer_load_dwordx4 %0, %1, %2 offen offset:%3 sc1 lds" :: "v"(vo), "s"(rs), "s"(so), "n"(OFF) : "memory");
    else asm volatile("buffer_load_dwordx4 %0, %1, %2 offen offset:%3 " WPOL " lds" :: "v"(vo), "s"(rs), "s"(so), "n"(OFF) : "memory");
}
DEVI void setm0(unsigned v) { asm volatile("s_mov_b32 m0, %0\n\ts_nop 0" :: "s"(v) : "memory"); }
DEVI void dma4(unsigned lds, rsrc_t rs, unsigned vo, unsigned so) { setm0(lds); dma1<0, true>(rs, vo, so); dma1<1024, true>(rs, vo, so); dma1<2048, true>(rs, vo, so); dma1<3072, true>(rs, vo, so); }
DEVI void dmaw(unsigned lds, rsrc_t rs, unsigned vo, unsigned so) { setm0(lds); asm volatile("buffer_load_dword %0, %1, %2 offen sc1 lds" :: "v"(vo), "s"(rs), "s"(so) : "memory"); }
template <int N> DEVI void wvm() { asm volatile("s_waitcnt vmcnt(%0)" :: "n"(N) : "memory"); }
template <int N> DEVI void wlg() { asm volatile("s_waitcnt lgkmcnt(%0)" :: "n"(N) : "memory"); }
DEVI void dma4p(u64 c0, u64 c1, u64 c2, u64 c3, rsrc_t rs, unsigned vo, unsigned so) {
    asm volatile(
        "s_cmp_eq_u64 %[c0], 0\n\ts_cbranch_scc1 1f\n\t"
        "buffer_load_dwordx4 %[vo], %[rs], %[so] offen sc1 lds\n"
        "1:\n\ts_cmp_eq_u64 %[c1], 0\n\ts_cbranch_scc1 2f\n\t"
        "buffer_load_dwordx4 %[vo], %[rs], %[so] offen offset:1024 sc1 lds\n"
        "2:\n\ts_cmp_eq_u64 %[c2], 0\n\ts_cbranch_scc1 3f\n\t"
        "buffer_load_dwordx4 %[vo], %[rs], %[so] offen offset:2048 sc1 lds\n"
        "3:\n\ts_cmp_eq_u64 %[c3], 0\n\ts_cbranch_scc1 4f\n\t"
        "buffer_load_dwordx4 %[vo], %[rs], %[so] offen offset:3072 sc1 lds\n"
        "4:"
        :: [c0] "s"(c0), [c1] "s"(c1), [c2] "s"(c2), [c3] "s"(c3), [vo] "v"(vo), [rs] "s"(rs), [so] "s"(so)
        : "memory", "scc");
}
#ifndef PSL
#define PSL 8
#endif
DEVI void wait_prog(rsrc_t rs, unsigned vo, unsigned so, int need) {
    unsigned spins = 0;
#pragma nounroll
    for (;;) {
        CB;
        const int v = (int)ld4(rs, vo, so);
        if (__builtin_amdgcn_sicmp(v, need, 40) == 0) return;
        spin_bail(spins);
        __builtin_amdgcn_s_sleep(PSL);
    }
}

typedef __attribute__((ext_vector_type(16))) int v16i;
typedef __attribute__((ext_vector_type(16))) unsigned v16u;
#ifdef TS
constexpr unsigned RSZ = 256u << 20;
#else
constexpr unsigned RSZ = WS_NEED;
#endif
DEVI u64 rclk() { u64 t; asm volatile("s_memrealtime %0" : "=s"(t)); return t; }
DEVI unsigned tsv(u64& t) { asm volatile("s_waitcnt lgkmcnt(0)" : "+s"(t)); return (unsigned)t; }
DEVI unsigned stamp(u64& t, int tag, int lead) { asm volatile("" : "+s"(t)); return (unsigned)tag << 24 | (((unsigned)t + lead) & 0xFFFFFFu); }
#ifndef SXTH
#define SXTH 20
#endif
DEVI int left(int t) { return (int)((unsigned)(t - (int)__builtin_amdgcn_s_memrealtime()) << 8); }
DEVI void hold(int t0, int d) { for (int n = 0; n < 4096 && (int)__builtin_amdgcn_s_memrealtime() - t0 < d; ++n) __builtin_amdgcn_s_sleep(4); }
DEVI int smin(int a, int b) { int r; asm("s_min_i32 %0, %1, %2" : "=s"(r) : "s"(a), "s"(b) : "scc"); return r; }
DEVI int smax(int a, int b) { int r; asm("s_max_i32 %0, %1, %2" : "=s"(r) : "s"(a), "s"(b) : "scc"); return r; }
DEVI void smin8x2(const v16i& f, int& m0, int& m1) {
    asm("s_min_i32 %0, %2, %3\n\ts_min_i32 %1, %10, %11\n\ts_min_i32 %0, %0, %4\n\ts_min_i32 %1, %1, %12\n\ts_min_i32 %0, %0, %5\n\ts_min_i32 %1, %1, %13\n\ts_min_i32 %0, %0, %6\n\ts_min_i32 %1, %1, %14\n\t"
        "s_min_i32 %0, %0, %7\n\ts_min_i32 %1, %1, %15\n\ts_min_i32 %0, %0, %8\n\ts_min_i32 %1, %1, %16\n\ts_min_i32 %0, %0, %9\n\ts_min_i32 %1, %1, %17"
        : "=&s"(m0), "=&s"(m1) : "s"(f[0]), "s"(f[1]), "s"(f[2]), "s"(f[3]), "s"(f[4]), "s"(f[5]), "s"(f[6]), "s"(f[7]),
          "s"(f[8]), "s"(f[9]), "s"(f[10]), "s"(f[11]), "s"(f[12]), "s"(f[13]), "s"(f[14]), "s"(f[15]) : "scc");
}
DEVI void smax8x2(const v16i& f, int& m0, int& m1) {
    asm("s_max_i32 %0, %2, %3\n\ts_max_i32 %1, %10, %11\n\ts_max_i32 %0, %0, %4\n\ts_max_i32 %1, %1, %12\n\ts_max_i32 %0, %0, %5\n\ts_max_i32 %1, %1, %13\n\ts_max_i32 %0, %0, %6\n\ts_max_i32 %1, %1, %14\n\t"
        "s_max_i32 %0, %0, %7\n\ts_max_i32 %1, %1, %15\n\ts_max_i32 %0, %0, %8\n\ts_max_i32 %1, %1, %16\n\ts_max_i32 %0, %0, %9\n\ts_max_i32 %1, %1, %17"
        : "=&s"(m0), "=&s"(m1) : "s"(f[0]), "s"(f[1]), "s"(f[2]), "s"(f[3]), "s"(f[4]), "s"(f[5]), "s"(f[6]), "s"(f[7]),
          "s"(f[8]), "s"(f[9]), "s"(f[10]), "s"(f[11]), "s"(f[12]), "s"(f[13]), "s"(f[14]), "s"(f[15]) : "scc");
}
#ifndef NVK
#define NVK 8
#endif
#ifndef DLY_V
#define DLY_V 500
#endif
#ifndef PSV
#define PSV 12
#endif
#ifndef DLY_P
#define DLY_P 150
#endif
DEVI unsigned prb(int k, int b) { return O_PRB + k * 16384 + b * 8192 + (k & 1) * 4096; }
DEVI void vote(rsrc_t rs, int w, int v, int far, int t0s) {
    const int lane = lane_id();
    constexpr unsigned N = (O_FLG_END - O_FLG) / 65536;
#pragma nounroll
    for (unsigned q = 0; q < N; ++q) st16(v4u{SENT, SENT, SENT, SENT}, rs, O_FLG + ((v * 4 + w) * N + q) * 1024 + lane * 16);
    const v2u e = ld8(rs, O_CAL + (lane & 31) * 256 + 128, 0);
    static_assert(NVK % 4 == 0 && NVK < 12, "");
    int best[2] = {1 << 30, 1 << 30};
    hold(t0s, DLY_P);
#pragma nounroll
    for (int k = 0; k <= NVK / 4; ++k)
#pragma unroll
        for (int b = 0; b < 2; ++b) {
            u64 t0, t1; int d;
            asm volatile("s_memtime %0\n\ts_waitcnt lgkmcnt(0)\n\ts_buffer_load_dword %2, %3, %4 glc\n\ts_waitcnt lgkmcnt(0)\n\ts_memtime %1\n\ts_waitcnt lgkmcnt(0)"
                         : "=&s"(t0), "=&s"(t1), "=&s"(d) : "s"(rs), "s"(prb(k ? 4 * k - w : 0, b) + v * 128) : "memory");
            if (k) best[b] = smin(best[b], (int)(t1 - t0));
        }
    *LDSP(int, (lane & 1) * 16 + w * 4) = best[lane & 1];
    WV0; WL0; BAR;
#pragma unroll
    for (int b = 0; b < 2; ++b) { const v4u t = *LDSP(const v4u, b * 16); best[b] = min(min(t[0], t[1]), min(t[2], t[3])); }
    st8(v2u{((e[0] + 2) & ~1u) | ((best[1] < best[0]) ^ far), e[1] + 1}, rs, lane < 32 && w == 0 ? O_CAL + lane * 256 + v * 8 : OOB, 0);
}
DEVI void vote_end(rsrc_t rs) {
    const unsigned o = O_CAL + (lane_id() & 31) * 256 + 128;
    const v2u e = ld8(rs, o, 0);
    st8(v2u{e[0] + 2, e[1] + 1}, rs, o, 0);
}
DEVI unsigned side(rsrc_t rs, int l) {
    unsigned spins = 0;
#pragma nounroll
    for (;;) {
        v16u f[2]; v2u e;
        const unsigned o = O_CAL + (blockIdx.x & 31) * 256;
        asm volatile("s_buffer_load_dwordx16 %0, %3, %4 glc\n\ts_buffer_load_dwordx16 %1, %3, %5 glc\n\ts_buffer_load_dwordx2 %2, %3, %6 glc\n\ts_waitcnt lgkmcnt(0)"
                     : "=&s"(f[0]), "=&s"(f[1]), "=&s"(e) : "s"(rs), "s"(o), "s"(o + 64), "s"(o + 128) : "memory");
        unsigned bad = 0, sum = 0;
#pragma unroll
        for (int i = 0; i < 16; ++i) { const unsigned x = f[i >> 3][2 * i & 15]; bad |= ((x ^ (e[0] + 2)) >> 1) | (f[i >> 3][(2 * i & 15) + 1] ^ (e[1] + 1)); sum += x & 1; }
#ifdef CSIDE
        sum = CSIDE * 16;
#endif
        if (bad == 0) return opqs(O_N + ((l ^ (sum > 8)) & 1) * 8192);
        spin_bail(spins);
        __builtin_amdgcn_s_sleep(PSV);
    }
}
#ifndef PSH
#define PSH 2
#endif
DEVI int wait_hint(rsrc_t rs, unsigned so, unsigned vo, int tag) {
#pragma nounroll
    for (int n = 0;; ++n) {
        v16i f;
        asm volatile("s_buffer_load_dwordx16 %0, %1, %2 glc\n\ts_waitcnt lgkmcnt(0)" : "=s"(f) : "s"(rs), "s"(so) : "memory");
        int m0, m1;
        smin8x2(f, m0, m1);
        if (smin(m0, m1) >= tag << 24) { smax8x2(f, m0, m1); return smax(m0, m1); }
        if (n == 40) { wait_prog(rs, vo, 0, tag << 24); n = 0; }
        __builtin_amdgcn_s_sleep(PSH);
    }
}
template <class WM> constexpr int widx(int j, int t) {
    int n = 0;
    for (int q = 0; q < j * WM::NP + t; ++q) n += WM::kind(q / WM::NP, q % WM::NP) == WM::kind(j, t);
    return n;
}
template <class WM> constexpr int lds_tile(int t) { for (int j = 0; j < WM::NT; ++j) if (WM::kind(j, t) == 1) return j; return -1; }
DEVI v4u ldsq(unsigned p, int off) { return *LDSP(const v4u, LDSP(char, p) + off); }
DEVI void stsq(unsigned p, int off, v4u v) { *LDSP(v4u, LDSP(char, p) + off) = v; }
DEVI v8u ldpair(unsigned wl, int slot) { return cat8(ldsq(wl, slot * 2048), ldsq(wl, slot * 2048 + 1024)); }
DEVI rsrc_t mkrs(const void* p, unsigned bytes) { return __builtin_amdgcn_make_buffer_rsrc((void*)p, 0, (int)bytes, 0x00020000); }
#ifndef NPL
#define NPL 72
#endif
#ifndef LNN
#define LNN 8
#endif
constexpr int LN_NOW = LNN, NP0 = NPL * 4;
template <int SL = 0> DEVI void pull(const void* p, int n, int q, int nq) {
    const rsrc_t r = mkrs(p, n * 1024);
    const unsigned vo = lane_id() * 16;
#pragma nounroll
    for (; q < n; q += nq) { asm volatile("buffer_load_dwordx4 a[0:3], %0, %1, %2 offen" :: "v"(vo), "s"(r), "s"(q * 1024) : "memory"); if (SL) __builtin_amdgcn_s_sleep(SL); }
}
#ifndef PQ
#define PQ 0
#endif
#ifndef DLY_F
#define DLY_F 1000
#endif
#ifndef PFS
#define PFS 5
#endif
DEVI void pull_all(const Args& a, int w, int q, int t0) {
    q = q * 4 + w;
    if (q < NP0) {
    pull(a.Wv, 8192, q, NP0); pull(a.Wo, 8192, q, NP0); if (!PQ) pull(a.Wq, 8192, q, NP0); pull(a.Wk, 8192, q, NP0);
    }
    if (q >= 256) return;
    hold(t0, DLY_F);
    pull<PFS>(q & 1 ? a.Wg : a.Wu, 7168, q >> 1, 128); pull<PFS>(a.Wd, 7168, q, 256);
}
template <class WM, int j, int ks, int O0, int O1> DEVI void put_frag(unsigned ta, unsigned wl, v8u* Wc) {
    constexpr int t = ks >> 1, f = ks & 1, k = WM::kind(j, t), ix = widx<WM>(j, t);
    if constexpr (k == 0) {
        constexpr int R = WM::RB + 8 * ix + 4 * f;
        if constexpr (R < 256) asm volatile("ds_read_b64_tr_b16 a[%1:%2], %0 offset:%5\n\tds_read_b64_tr_b16 a[%3:%4], %0 offset:%6" :: "v"(ta), "n"(R), "n"(R + 1), "n"(R + 2), "n"(R + 3), "n"(O0), "n"(O1) : "memory");
        else asm volatile("ds_read_b64_tr_b16 v[%1:%2], %0 offset:%5\n\tds_read_b64_tr_b16 v[%3:%4], %0 offset:%6" :: "v"(ta), "n"(R - 256 + NV), "n"(R - 255 + NV), "n"(R - 254 + NV), "n"(R - 253 + NV), "n"(O0), "n"(O1) : "memory");
    } else {
        v2u p, q;
        asm volatile("ds_read_b64_tr_b16 %0, %2 offset:%3\n\tds_read_b64_tr_b16 %1, %2 offset:%4\n\ts_waitcnt lgkmcnt(0)" : "=&v"(p), "=&v"(q) : "v"(ta), "n"(O0), "n"(O1) : "memory");
        const v4u F = v4u{p[0], p[1], q[0], q[1]};
        if constexpr (k == 1) stsq(wl, (WM::LB + ix) * 2048 + f * 1024, F);
        else if constexpr (f == 0) Wc[WM::CBI + ix] = cat8(F, F);
        else Wc[WM::CBI + ix] = cat8(lo4(Wc[WM::CBI + ix]), F);
    }
}
template <int NB, int DEPTH, int NI, bool SHARED = false, int NX = 0, class ISSUE, class PUT>
DEVI void load_blocks(ISSUE&& issue, PUT&& put) {
    sfor<0, DEPTH - 1>([&](auto B) { issue(B, IC<B.v % DEPTH>{}); });
    sfor<0, NB>([&](auto B) {
        constexpr int b = B.v, left = NB + NX - 1 - b < DEPTH - 2 ? NB + NX - 1 - b : DEPTH - 2;
        wvm<NI * left>();
        wlg<0>();
        if constexpr (SHARED) BAR;
        if constexpr (b + DEPTH - 1 < NB + NX) issue(IC<b + DEPTH - 1>{}, IC<(b + DEPTH - 1) % DEPTH>{});
        put(B, IC<b % DEPTH>{});
    });
    wlg<0>();
    if constexpr (SHARED) BAR;
}
DEVI void first_look(unsigned& fx, rsrc_t rs, unsigned nb) { asm volatile("buffer_load_dword %0, %1, %2, %3 offen sc1" : "=v"(fx) : "v"((lane_id() & 7) * 4), "s"(rs), "s"(na(nb, N_FX)) : "memory"); }
DEVI unsigned first_there(unsigned& fx) { asm volatile("" : "+v"(fx)); return pend1(fx) == 0 ? WS_NEED : 0u; }
DEVI void dma_blk(unsigned lds, rsrc_t rw, unsigned vo, unsigned so, unsigned rowb) { setm0(lds); dma1<0, false>(rw, vo, so); setm0(lds + 1024); dma1<0, false>(rw, vo, so + 16 * rowb); }
template <class WM, int NKS, int K0, int K1, int RTS, int LATE1, class FILL>
DEVI void gemm_pass(unsigned xa, unsigned wl, const v8u* Wc, v4u (&x)[2][3], f32x4 (&acc)[WM::NT][2], FILL&& fill) {
    auto rd = [&](auto KS, auto Q) {
        constexpr int ks = KS.v, q = Q.v, jl = lds_tile<WM>(ks >> 1), il = widx<WM>(jl < 0 ? 0 : jl, ks >> 1);
        if constexpr (q == 0 && jl >= 0) x[ks & 1][2] = ldsq(wl, (WM::LB + il) * 2048 + (ks & 1) * 1024);
        if constexpr (q == 1) x[ks & 1][1] = ldsq(xa, RTS + ks * 1024);
        if constexpr (q == 2) x[ks & 1][0] = ldsq(xa, ks * 1024);
    };
    sfor<K0 * 2 * WM::NT, K1 * 2 * WM::NT>([&](auto N) {
        constexpr int ks = N.v / (2 * WM::NT), n = N.v % (2 * WM::NT), j = n >> 1, rt = n & 1, t = ks >> 1, f = ks & 1, k = WM::kind(j, t), ix = widx<WM>(j, t);
        fill(IC<ks>{}, IC<n>{});
        SB;
        const v4u xv = x[ks & 1][rt];
        if constexpr (k == 0) mfA<WM::RB + 8 * ix + 4 * f, ks == 0>(acc[j][rt], xv);
        else if constexpr (k == 1) mfv<ks == 0>(acc[j][rt], x[ks & 1][2], xv);
        else mfv<ks == 0>(acc[j][rt], f ? hi4(Wc[WM::CBI + ix]) : lo4(Wc[WM::CBI + ix]), xv);
        if constexpr (ks + 1 < NKS) rd(IC<ks + 1>{}, IC<n - (ks == 1 ? LATE1 : 0)>{});
        SB;
    });
}
template <class WM> constexpr int next_lds(int n) { for (; n < 16 * WM::NT; ++n) if (WM::kind(2 * (n >> 5) + (n & 1), (n >> 1) & 15) == 1) return n; return -1; }
template <class WM> constexpr int lds_cnt(int n) { int c = 0; for (int q = next_lds<WM>(0); q >= 0 && q < n; q = next_lds<WM>(q + 1)) ++c; return c; }
template <class WM> constexpr int lds_nth(int k) { int q = next_lds<WM>(0); for (; k > 0 && q >= 0; --k) q = next_lds<WM>(q + 1); return q; }
template <class WM, int K, int NB> DEVI void pair_next(v8u (&pb)[NB], unsigned wl) {
    constexpr int n = lds_nth<WM>(K);
    if constexpr (n >= 0) { constexpr int ix = widx<WM>(2 * (n >> 5) + (n & 1), (n >> 1) & 15); pb[K % NB] = ldpair(wl, WM::LB + ix); }
}
template <class WM, int MO, int MR, int NB, class FILL>
DEVI void spass(unsigned ma, unsigned wl, const v8u* Wc, f32x4* e, v4u (&m)[MR], v8u (&pb)[NB], int sidx, FILL&& fill) {
    sfor<0, 16 * WM::NT>([&](auto N) {
        constexpr int n = N.v, t = (n >> 1) & 15, j = 2 * (n >> 5) + (n & 1), k = WM::kind(j, t), ix = widx<WM>(j, t), tn = (t + MR - 1) & 15, r = (n >> 1) % MR;
        fill(N);
        SB;
        if constexpr (k == 0) smf<WM::RB + 8 * ix>(e[j], m[r], sidx);
        else if constexpr (k == 1) { constexpr int q = lds_cnt<WM>(n); smfv(e[j], m[r], pb[q % NB], sidx); pair_next<WM, q + NB>(pb, wl); }
        else smfv(e[j], m[r], Wc[WM::CBI + ix], sidx);
        if constexpr ((n & 1) == 0 && n + 2 * MR - 2 < 16 * WM::NT) m[(r + MR - 1) % MR] = ldsq(ma, MO + (tn >> 3) * 32768 + (tn & 7) * 1024);
        SB;
    });
}
typedef __attribute__((ext_vector_type(4))) short s16x4;
DEVI v2u ldtr(unsigned p, int off) { return __builtin_bit_cast(v2u, __builtin_amdgcn_ds_read_tr16_b64_v4i16(LDSP(s16x4, LDSP(char, p) + off))); }
DEVI v4u ldtr2(unsigned p, int off) { const v2u a = ldtr(p, off), b = ldtr(p, off + 256); return v4u{a[0], a[1], b[0], b[1]}; }
DEVI void sts1(unsigned p, int off, unsigned v) { *LDSP(unsigned, LDSP(char, p) + off) = v; }
DEVI unsigned lds1(unsigned p, int off) { return *LDSP(const unsigned, LDSP(char, p) + off); }
DEVI void use(unsigned v) { asm volatile("" :: "v"(v) : "memory"); }
DEVI int mk_sidx(int lane) { return opq(0x4444 + (lane & 2) * 0x5555); }
DEVI int crow(int r, int hb) { return 4 * (r >> 1) + 2 * hb + (r & 1); }
DEVI v4u again(int id, unsigned nb, int w) { const u64 k = (u64)__builtin_amdgcn_kernarg_segment_ptr(); return v4u{(unsigned)k, (unsigned)(k >> 32), id < 0 ? ~0u : 256 + id + ((nb >> 4) & 512), (unsigned)w * 64}; }
#define A_LDS 0x28a0114002004410ull
#define A_VAR 0x0000000400020001ull
#define A_LOOP \
"s_brev_b32 s99, 1\ns_brev_b32 s98, 1\ns_brev_b32 s97, 1\ns_brev_b32 s96, 1\n" \
"s_branch 4f\n.p2align 6\n1:\ns_waitcnt lgkmcnt(0)\n" \
"v_mfma_f32_16x16x32_bf16 v[44:47], v[76:79], v[80:83], v[44:47]\nds_read_b128 v[36:39], v25 offset:3072\nds_read_b128 v[40:43], v24 offset:21504\nv_mfma_f32_16x16x32_bf16 v[48:51], v[76:79], v[32:35], v[48:51]\n" \
"ds_read_b128 v[84:87], v24 offset:54272\nv_bfe_u32 v31, v24, 6, 3\nv_mfma_f32_16x16x32_bf16 v[52:55], a[160:163], v[80:83], v[52:55]\ns_mov_b32 m0, s50\n" \
"s_add_u32 s99, s30, 2\nv_lshlrev_b32 v31, 2, v31\nv_mfma_f32_16x16x32_bf16 v[56:59], a[160:163], v[32:35], v[56:59]\ns_min_u32 s99, s99, 63\n" \
"s_lshl_b32 s99, s99, 5\ns_add_u32 s99, s99, s48\nv_mfma_f32_16x16x32_bf16 v[60:63], v[120:123], v[80:83], v[60:63]\ns_and_b32 s94, s30, 7\n" \
"s_lshl_b32 s94, s94, 18\ns_add_u32 s94, s94, s33\nv_mfma_f32_16x16x32_bf16 v[64:67], v[120:123], v[32:35], v[64:67]\ns_add_u32 s31, s31, 0x10000\n" \
"v_mfma_f32_16x16x32_bf16 v[68:71], v[224:227], v[80:83], v[68:71]\ns_waitcnt lgkmcnt(1)\nv_mfma_f32_16x16x32_bf16 v[72:75], v[224:227], v[32:35], v[72:75]\ns_waitcnt lgkmcnt(0)\n" \
"ds_read_b128 v[32:35], v24 offset:22528\nv_mfma_f32_16x16x32_bf16 v[44:47], v[36:39], v[40:43], v[44:47]\nds_read_b128 v[76:79], v24 offset:55296\nv_mfma_f32_16x16x32_bf16 v[48:51], v[36:39], v[84:87], v[48:51]\n" \
"v_mfma_f32_16x16x32_bf16 v[52:55], a[164:167], v[40:43], v[52:55]\nds_read_b128 v[36:39], v25 offset:18432\nv_mfma_f32_16x16x32_bf16 v[56:59], a[164:167], v[84:87], v[56:59]\nv_mfma_f32_16x16x32_bf16 v[60:63], v[124:127], v[40:43], v[60:63]\n" \
"v_mfma_f32_16x16x32_bf16 v[64:67], v[124:127], v[84:87], v[64:67]\nv_mfma_f32_16x16x32_bf16 v[68:71], v[228:231], v[40:43], v[68:71]\ns_waitcnt lgkmcnt(1)\nv_mfma_f32_16x16x32_bf16 v[72:75], v[228:231], v[84:87], v[72:75]\n" \
"ds_read_b128 v[40:43], v24 offset:23552\nv_mfma_f32_16x16x32_bf16 v[44:47], a[64:67], v[32:35], v[44:47]\nds_read_b128 v[80:83], v24 offset:56320\nv_mfma_f32_16x16x32_bf16 v[48:51], a[64:67], v[76:79], v[48:51]\n" \
"v_mfma_f32_16x16x32_bf16 v[52:55], a[168:171], v[32:35], v[52:55]\nds_read_b128 v[84:87], v25 offset:19456\nv_mfma_f32_16x16x32_bf16 v[56:59], a[168:171], v[76:79], v[56:59]\nv_mfma_f32_16x16x32_bf16 v[60:63], v[128:131], v[32:35], v[60:63]\n" \
"s_waitcnt lgkmcnt(3)\nv_mfma_f32_16x16x32_bf16 v[64:67], v[128:131], v[76:79], v[64:67]\nv_mfma_f32_16x16x32_bf16 v[68:71], v[36:39], v[32:35], v[68:71]\ns_waitcnt vmcnt(8) lgkmcnt(1)\n" \
"s_barrier\nv_mfma_f32_16x16x32_bf16 v[72:75], v[36:39], v[76:79], v[72:75]\nds_read_b128 v[32:35], v24 offset:24576\nv_mfma_f32_16x16x32_bf16 v[44:47], a[68:71], v[40:43], v[44:47]\n" \
"ds_read_b128 v[36:39], v24 offset:57344\nv_mfma_f32_16x16x32_bf16 v[48:51], a[68:71], v[80:83], v[48:51]\nds_read_b128 v[76:79], v25 offset:12288\nv_mfma_f32_16x16x32_bf16 v[52:55], a[172:175], v[40:43], v[52:55]\n" \
"buffer_load_dword v31, s[8:11], s99 offen offset:0 sc1 lds\nv_mfma_f32_16x16x32_bf16 v[56:59], a[172:175], v[80:83], v[56:59]\ns_add_u32 m0, s32, 0x4000\nv_mfma_f32_16x16x32_bf16 v[60:63], v[132:135], v[40:43], v[60:63]\n" \
"s_waitcnt lgkmcnt(3)\nv_mfma_f32_16x16x32_bf16 v[64:67], v[132:135], v[80:83], v[64:67]\nv_mfma_f32_16x16x32_bf16 v[68:71], v[84:87], v[40:43], v[68:71]\ns_waitcnt lgkmcnt(1)\n" \
"v_mfma_f32_16x16x32_bf16 v[72:75], v[84:87], v[80:83], v[72:75]\nds_read_b128 v[40:43], v24 offset:25600\nv_mfma_f32_16x16x32_bf16 v[44:47], a[72:75], v[32:35], v[44:47]\nds_read_b128 v[80:83], v24 offset:58368\n" \
"v_mfma_f32_16x16x32_bf16 v[48:51], a[72:75], v[36:39], v[48:51]\nds_read_b128 v[84:87], v25 offset:13312\nv_mfma_f32_16x16x32_bf16 v[52:55], a[176:179], v[32:35], v[52:55]\ns_waitcnt lgkmcnt(3)\n" \
"v_mfma_f32_16x16x32_bf16 v[56:59], a[176:179], v[36:39], v[56:59]\nv_mfma_f32_16x16x32_bf16 v[60:63], v[76:79], v[32:35], v[60:63]\nv_mfma_f32_16x16x32_bf16 v[64:67], v[76:79], v[36:39], v[64:67]\nv_mfma_f32_16x16x32_bf16 v[68:71], v[232:235], v[32:35], v[68:71]\n" \
"s_waitcnt lgkmcnt(1)\nv_mfma_f32_16x16x32_bf16 v[72:75], v[232:235], v[36:39], v[72:75]\nbuffer_load_dwordx4 v24, s[8:11], s94 offen offset:0 sc1 lds\nv_mfma_f32_16x16x32_bf16 v[44:47], a[76:79], v[40:43], v[44:47]\n" \
"ds_read_b128 v[32:35], v24 offset:26624\nv_mfma_f32_16x16x32_bf16 v[48:51], a[76:79], v[80:83], v[48:51]\nds_read_b128 v[36:39], v24 offset:59392\nv_mfma_f32_16x16x32_bf16 v[52:55], a[180:183], v[40:43], v[52:55]\n" \
"s_waitcnt lgkmcnt(2)\nbuffer_load_dwordx4 v24, s[8:11], s94 offen offset:1024 sc1 lds\nv_mfma_f32_16x16x32_bf16 v[56:59], a[180:183], v[80:83], v[56:59]\nbuffer_load_dwordx4 v24, s[8:11], s94 offen offset:2048 sc1 lds\n" \
"v_mfma_f32_16x16x32_bf16 v[60:63], v[84:87], v[40:43], v[60:63]\nbuffer_load_dwordx4 v24, s[8:11], s94 offen offset:3072 sc1 lds\nv_mfma_f32_16x16x32_bf16 v[64:67], v[84:87], v[80:83], v[64:67]\nds_read_b128 v[76:79], v25 offset:20480\n" \
"s_and_b32 s93, s30, 15\nv_mfma_f32_16x16x32_bf16 v[68:71], v[236:239], v[40:43], v[68:71]\ns_lshl_b32 s92, s93, 17\ns_add_u32 s92, s92, s35\n" \
"s_and_b32 s93, s93, 7\nv_mfma_f32_16x16x32_bf16 v[72:75], v[236:239], v[80:83], v[72:75]\ns_waitcnt lgkmcnt(1)\nds_read_b128 v[40:43], v24 offset:27648\n" \
"v_mfma_f32_16x16x32_bf16 v[44:47], a[80:83], v[32:35], v[44:47]\nds_read_b128 v[80:83], v24 offset:60416\ns_lshr_b32 s95, s30, s41\nv_mfma_f32_16x16x32_bf16 v[48:51], a[80:83], v[36:39], v[48:51]\n" \
"s_and_b32 s95, s95, 1\ns_lshl_b32 s93, s93, 14\ns_lshl1_add_u32 s99, s93, s34\nv_mfma_f32_16x16x32_bf16 v[52:55], a[184:187], v[32:35], v[52:55]\n" \
"ds_read_b128 v[84:87], v25 offset:21504\ns_or_b32 s98, s92, s36\nv_mfma_f32_16x16x32_bf16 v[56:59], a[184:187], v[36:39], v[56:59]\ns_or_b32 s97, s92, s37\n" \
"s_and_b32 s92, s92, s39\ns_andn2_b32 s93, s93, s39\nv_mfma_f32_16x16x32_bf16 v[60:63], v[136:139], v[32:35], v[60:63]\nds_read_b128 v[88:91], v25 offset:4096\n" \
"s_add_u32 s96, s92, s38\nv_mfma_f32_16x16x32_bf16 v[64:67], v[136:139], v[36:39], v[64:67]\ns_waitcnt lgkmcnt(4)\ns_add_u32 s96, s96, s93\n" \
"s_add_u32 s30, s30, 1\nv_mfma_f32_16x16x32_bf16 v[68:71], v[76:79], v[32:35], v[68:71]\ns_lshl_b32 s93, s30, 24\ns_cmp_le_u32 s30, 64\n" \
"s_cselect_b32 s51, s14, 0\nv_mfma_f32_16x16x32_bf16 v[72:75], v[76:79], v[36:39], v[72:75]\ns_waitcnt lgkmcnt(2)\nds_read_b128 v[32:35], v24 offset:28672\n" \
"v_mfma_f32_16x16x32_bf16 v[44:47], a[84:87], v[40:43], v[44:47]\nds_read_b128 v[36:39], v24 offset:61440\nv_mfma_f32_16x16x32_bf16 v[48:51], a[84:87], v[80:83], v[48:51]\nv_mfma_f32_16x16x32_bf16 v[52:55], a[188:191], v[40:43], v[52:55]\n" \
"v_mfma_f32_16x16x32_bf16 v[56:59], a[188:191], v[80:83], v[56:59]\nv_mfma_f32_16x16x32_bf16 v[60:63], v[140:143], v[40:43], v[60:63]\ns_waitcnt lgkmcnt(2)\nds_read_b128 v[76:79], v25 offset:5120\n" \
"v_mfma_f32_16x16x32_bf16 v[64:67], v[140:143], v[80:83], v[64:67]\nv_mfma_f32_16x16x32_bf16 v[68:71], v[84:87], v[40:43], v[68:71]\ns_waitcnt lgkmcnt(1)\nv_mfma_f32_16x16x32_bf16 v[72:75], v[84:87], v[80:83], v[72:75]\n" \
"ds_read_b128 v[40:43], v24 offset:29696\nv_mfma_f32_16x16x32_bf16 v[44:47], v[88:91], v[32:35], v[44:47]\nds_read_b128 v[80:83], v24 offset:62464\nv_mfma_f32_16x16x32_bf16 v[48:51], v[88:91], v[36:39], v[48:51]\n" \
"v_mfma_f32_16x16x32_bf16 v[52:55], a[192:195], v[32:35], v[52:55]\nv_mfma_f32_16x16x32_bf16 v[56:59], a[192:195], v[36:39], v[56:59]\nv_mfma_f32_16x16x32_bf16 v[60:63], v[144:147], v[32:35], v[60:63]\nv_mfma_f32_16x16x32_bf16 v[64:67], v[144:147], v[36:39], v[64:67]\n" \
"v_mfma_f32_16x16x32_bf16 v[68:71], v[240:243], v[32:35], v[68:71]\ns_waitcnt lgkmcnt(0)\nv_mfma_f32_16x16x32_bf16 v[72:75], v[240:243], v[36:39], v[72:75]\nds_read_b128 v[32:35], v24 offset:30720\n" \
"v_add_u32 v31, s32, v24\nv_mfma_f32_16x16x32_bf16 v[44:47], v[76:79], v[40:43], v[44:47]\nds_read_b128 v[36:39], v24 offset:63488\nv_mfma_f32_16x16x32_bf16 v[48:51], v[76:79], v[80:83], v[48:51]\n" \
"v_mfma_f32_16x16x32_bf16 v[52:55], a[196:199], v[40:43], v[52:55]\nv_mfma_f32_16x16x32_bf16 v[56:59], a[196:199], v[80:83], v[56:59]\nv_mfma_f32_16x16x32_bf16 v[60:63], v[148:151], v[40:43], v[60:63]\nv_mfma_f32_16x16x32_bf16 v[64:67], v[148:151], v[80:83], v[64:67]\n" \
"v_mfma_f32_16x16x32_bf16 v[68:71], v[244:247], v[40:43], v[68:71]\ns_waitcnt vmcnt(0) lgkmcnt(0)\nds_read_b128 v[40:43], v31 offset:16384\nds_read_b128 v[76:79], v31 offset:17408\n" \
"ds_read_b128 v[84:87], v31 offset:18432\nv_mfma_f32_16x16x32_bf16 v[72:75], v[244:247], v[80:83], v[72:75]\nds_read_b128 v[88:91], v31 offset:19456\nv_mfma_f32_16x16x32_bf16 v[44:47], a[88:91], v[32:35], v[44:47]\n" \
"ds_read_b128 v[80:83], v24 offset:31744\nv_mfma_f32_16x16x32_bf16 v[48:51], a[88:91], v[36:39], v[48:51]\nds_read_b128 v[92:95], v24 offset:64512\nv_mfma_f32_16x16x32_bf16 v[52:55], a[200:203], v[32:35], v[52:55]\n" \
"v_mfma_f32_16x16x32_bf16 v[56:59], a[200:203], v[36:39], v[56:59]\ns_waitcnt lgkmcnt(3)\nv_max3_u32 v96, v40, v76, v84\nv_mov_b64 v[100:101], 0\n" \
"v_mfma_f32_16x16x32_bf16 v[60:63], v[152:155], v[32:35], v[60:63]\nds_read_b128 v[104:107], v25 offset:0\nv_mov_b64 v[102:103], 0\nv_mfma_f32_16x16x32_bf16 v[64:67], v[152:155], v[36:39], v[64:67]\n" \
"s_waitcnt lgkmcnt(3)\nv_max_u32 v96, v96, v88\nv_cmp_eq_u32 vcc, -1, v96\nv_mfma_f32_16x16x32_bf16 v[68:71], v[248:251], v[32:35], v[68:71]\n" \
"ds_read_b128 v[108:111], v25 offset:1024\nv_mov_b64 v[32:33], 0\nv_mfma_f32_16x16x32_bf16 v[72:75], v[248:251], v[36:39], v[72:75]\ns_waitcnt lgkmcnt(2)\n" \
"s_cbranch_vccnz 2f\n3:\nv_mfma_f32_16x16x32_bf16 v[44:47], a[92:95], v[80:83], v[44:47]\ns_barrier\n" \
"s_memrealtime s[56:57]\nv_mfma_f32_16x16x32_bf16 v[52:55], a[204:207], v[80:83], v[52:55]\nds_read_b128 v[36:39], v24 offset:20480\nv_mov_b64 v[34:35], 0\n" \
"v_mfma_f32_16x16x32_bf16 v[60:63], v[156:159], v[80:83], v[60:63]\nds_read_b128 v[40:43], v24 offset:21504\nv_add_u32 v31, s44, v24\nv_mfma_f32_16x16x32_bf16 v[68:71], v[252:255], v[80:83], v[68:71]\n" \
"ds_read_b128 v[76:79], v24 offset:16384\nv_cvt_pk_bf16_f32 v44, v44, v52\nv_mfma_f32_16x16x32_bf16 v[48:51], a[92:95], v[92:95], v[48:51]\nds_read_b128 v[80:83], v24 offset:22528\n" \
"v_cvt_pk_bf16_f32 v45, v45, v53\nv_mfma_f32_16x16x32_bf16 v[56:59], a[204:207], v[92:95], v[56:59]\nds_read_b128 v[84:87], v24 offset:23552\nv_cvt_pk_bf16_f32 v46, v46, v54\n" \
"v_mfma_f32_16x16x32_bf16 v[64:67], v[156:159], v[92:95], v[64:67]\nds_read_b128 v[88:91], v24 offset:17408\nv_cvt_pk_bf16_f32 v47, v47, v55\nv_mfma_f32_16x16x32_bf16 v[72:75], v[252:255], v[92:95], v[72:75]\n" \
"s_waitcnt lgkmcnt(5)\nds_write_b64 v31, v[44:45] offset:0\nv_smfmac_f32_16x16x64_bf16 v[100:103], v[36:39], v[104:111], v26\nds_read_b128 v[52:55], v24 offset:18432\n" \
"v_cvt_pk_bf16_f32 v60, v60, v68\nv_smfmac_f32_16x16x64_bf16 v[32:35], v[36:39], a[120:127], v26\ns_waitcnt lgkmcnt(5)\nds_write_b64 v31, v[46:47] offset:8\n" \
"v_smfmac_f32_16x16x64_bf16 v[100:103], v[40:43], a[24:31], v26\nds_read_b128 v[92:95], v24 offset:19456\nv_cvt_pk_bf16_f32 v61, v61, v69\nv_smfmac_f32_16x16x64_bf16 v[32:35], v[40:43], a[128:135], v26\n" \
"ds_write_b64 v31, v[60:61] offset:32768\nv_smfmac_f32_16x16x64_bf16 v[100:103], v[76:79], v[0:7], v26\nds_read_b128 v[44:47], v24 offset:49152\nv_cvt_pk_bf16_f32 v62, v62, v70\n" \
"v_smfmac_f32_16x16x64_bf16 v[32:35], v[76:79], a[96:103], v26\ns_waitcnt lgkmcnt(5)\ns_add_u32 s94, s56, 0x7e\nv_cvt_pk_bf16_f32 v63, v63, v71\n" \
"v_smfmac_f32_16x16x64_bf16 v[100:103], v[80:83], a[32:39], v26\nds_read_b128 v[68:71], v24 offset:50176\ns_and_b32 s94, s94, 0xffffff\nv_smfmac_f32_16x16x64_bf16 v[32:35], v[80:83], a[136:143], v26\n" \
"ds_read_b128 v[76:79], v25 offset:6144\ns_or_b32 s94, s94, s93\nv_smfmac_f32_16x16x64_bf16 v[100:103], v[84:87], a[40:47], v26\nds_read_b128 v[80:83], v25 offset:7168\n" \
"v_mov_b32 v60, s94\nv_smfmac_f32_16x16x64_bf16 v[32:35], v[84:87], a[144:151], v26\nds_read_b128 v[96:99], v24 offset:51200\nv_cvt_pk_bf16_f32 v48, v48, v56\n" \
"v_smfmac_f32_16x16x64_bf16 v[100:103], v[88:91], a[0:7], v26\nds_read_b128 v[104:107], v25 offset:2048\nv_cvt_pk_bf16_f32 v49, v49, v57\nv_smfmac_f32_16x16x64_bf16 v[32:35], v[88:91], v[8:15], v26\n" \
"s_waitcnt lgkmcnt(5)\nds_read_b128 v[108:111], v25 offset:3072\nv_smfmac_f32_16x16x64_bf16 v[100:103], v[52:55], a[8:15], v26\nds_read_b128 v[84:87], v24 offset:56320\n" \
"v_cvt_pk_bf16_f32 v50, v50, v58\nv_smfmac_f32_16x16x64_bf16 v[32:35], v[52:55], a[104:111], v26\nds_write_b64 v31, v[62:63] offset:32776\nv_smfmac_f32_16x16x64_bf16 v[100:103], v[92:95], a[16:23], v26\n" \
"buffer_store_dword v60, v29, s[8:11], s49 offen offset:0 sc1\nv_smfmac_f32_16x16x64_bf16 v[32:35], v[92:95], a[112:119], v26\nds_read_b128 v[52:55], v24 offset:52224\nv_cvt_pk_bf16_f32 v51, v51, v59\n" \
"v_smfmac_f32_16x16x64_bf16 v[100:103], v[44:47], a[48:55], v26\nds_read_b128 v[56:59], v24 offset:53248\nv_cvt_pk_bf16_f32 v64, v64, v72\nv_smfmac_f32_16x16x64_bf16 v[32:35], v[44:47], a[152:159], v26\n" \
"s_waitcnt lgkmcnt(4)\nds_write_b64 v31, v[48:49] offset:4096\nv_smfmac_f32_16x16x64_bf16 v[100:103], v[68:71], a[56:63], v26\nds_read_b128 v[60:63], v24 offset:54272\n" \
"v_cvt_pk_bf16_f32 v65, v65, v73\nv_smfmac_f32_16x16x64_bf16 v[32:35], v[68:71], v[76:83], v26\nds_read_b128 v[92:95], v24 offset:55296\nv_cvt_pk_bf16_f32 v66, v66, v74\n" \
"v_smfmac_f32_16x16x64_bf16 v[100:103], v[96:99], v[104:111], v26\nds_read_b128 v[68:71], v25 offset:4096\nv_cvt_pk_bf16_f32 v67, v67, v75\nv_smfmac_f32_16x16x64_bf16 v[32:35], v[96:99], a[160:167], v26\n" \
"s_waitcnt lgkmcnt(5)\nds_read_b128 v[72:75], v25 offset:5120\nv_smfmac_f32_16x16x64_bf16 v[100:103], v[84:87], a[88:95], v26\nds_write_b64 v31, v[50:51] offset:4104\n" \
"v_smfmac_f32_16x16x64_bf16 v[32:35], v[84:87], a[200:207], v26\nds_read_b128 v[76:79], v25 offset:14336\nv_mov_b64 v[48:49], 0\nv_smfmac_f32_16x16x64_bf16 v[100:103], v[52:55], a[64:71], v26\n" \
"ds_read_b128 v[80:83], v25 offset:15360\nv_mov_b64 v[50:51], 0\nv_smfmac_f32_16x16x64_bf16 v[32:35], v[52:55], a[168:175], v26\ns_waitcnt lgkmcnt(5)\n" \
"ds_write_b64 v31, v[64:65] offset:36864\nv_smfmac_f32_16x16x64_bf16 v[100:103], v[56:59], a[72:79], v26\nds_read_b128 v[96:99], v24 offset:51200\nv_mov_b64 v[64:65], 0\n" \
"v_smfmac_f32_16x16x64_bf16 v[32:35], v[56:59], a[176:183], v26\nds_write_b64 v31, v[66:67] offset:36872\nv_smfmac_f32_16x16x64_bf16 v[100:103], v[60:63], a[80:87], v26\nds_read_b128 v[56:59], v24 offset:18432\n" \
"v_mov_b64 v[66:67], 0\nv_smfmac_f32_16x16x64_bf16 v[32:35], v[60:63], a[184:191], v26\ns_waitcnt lgkmcnt(5)\nds_read_b32 v31, v28 offset:512\n" \
"v_smfmac_f32_16x16x64_bf16 v[100:103], v[92:95], v[68:75], v26\nv_add_u32 v60, s45, v24\nv_smfmac_f32_16x16x64_bf16 v[32:35], v[92:95], a[192:199], v26\nds_read_b128 v[68:71], v24 offset:54272\n" \
"v_cndmask_b32 v60, v60, v28, s[28:29]\nv_smfmac_f32_16x16x64_bf16 v[48:51], v[40:43], a[240:247], v26\ns_waitcnt lgkmcnt(4)\nds_read_b128 v[104:107], v25 offset:18432\n" \
"v_smfmac_f32_16x16x64_bf16 v[64:67], v[40:43], v[76:83], v26\nds_read_b128 v[108:111], v25 offset:19456\nv_add_f32 v40, v100, v102\nv_smfmac_f32_16x16x64_bf16 v[48:51], v[36:39], a[232:239], v26\n" \
"ds_read_b128 v[72:75], v25 offset:20480\nv_add_f32 v41, v32, v34\nv_smfmac_f32_16x16x64_bf16 v[64:67], v[36:39], v[192:199], v26\nds_read_b128 v[76:79], v25 offset:21504\n" \
"v_add_f32 v42, v101, v103\nv_smfmac_f32_16x16x64_bf16 v[48:51], v[96:99], v[120:127], v26\nds_read_b128 v[36:39], v24 offset:19456\nv_add_f32 v43, v33, v35\n" \
"v_smfmac_f32_16x16x64_bf16 v[64:67], v[96:99], v[224:231], v26\nds_read_b128 v[32:35], v24 offset:50176\nv_cvt_pk_bf16_f32 v62, v40, v41\nv_smfmac_f32_16x16x64_bf16 v[48:51], v[84:87], v[152:159], v26\n" \
"ds_read_b128 v[80:83], v25 offset:10240\nv_cvt_pk_bf16_f32 v63, v42, v43\nv_smfmac_f32_16x16x64_bf16 v[64:67], v[84:87], v[248:255], v26\nds_write2_b32 v27, v62, v63 offset0:0 offset1:4\n" \
"v_smfmac_f32_16x16x64_bf16 v[48:51], v[92:95], v[144:151], v26\ns_waitcnt lgkmcnt(5)\nds_read_b128 v[84:87], v25 offset:11264\nv_smfmac_f32_16x16x64_bf16 v[64:67], v[92:95], v[240:247], v26\n" \
"ds_read_b128 v[40:43], v24 offset:53248\nv_cmp_eq_u32 vcc, -1, v31\nv_smfmac_f32_16x16x64_bf16 v[48:51], v[56:59], v[16:23], v26\nv_smfmac_f32_16x16x64_bf16 v[64:67], v[56:59], v[176:183], v26\n" \
"ds_read_b128 v[92:95], v24 offset:23552\nv_smfmac_f32_16x16x64_bf16 v[48:51], v[52:55], v[128:135], v26\nds_read_b128 v[96:99], v25 offset:12288\nv_smfmac_f32_16x16x64_bf16 v[64:67], v[52:55], v[104:111], v26\n" \
"s_waitcnt lgkmcnt(4)\nds_read_b128 v[100:103], v25 offset:13312\nv_smfmac_f32_16x16x64_bf16 v[48:51], v[68:71], v[136:143], v26\ns_barrier\n" \
"v_smfmac_f32_16x16x64_bf16 v[64:67], v[68:71], v[72:79], v26\nds_read_b128 v[52:55], v25 offset:16384\nv_mov_b32 v63, s43\nv_smfmac_f32_16x16x64_bf16 v[48:51], v[36:39], a[224:231], v26\n" \
"ds_read_b128 v[56:59], v25 offset:17408\nv_cndmask_b32 v76, v63, 0, s[16:17]\nv_smfmac_f32_16x16x64_bf16 v[64:67], v[36:39], v[184:191], v26\ns_waitcnt lgkmcnt(4)\n" \
"ds_read_b128 v[36:39], v24 offset:22528\nv_smfmac_f32_16x16x64_bf16 v[48:51], v[44:47], v[80:87], v26\nds_read_b128 v[68:71], v25 offset:8192\nv_cndmask_b32 v77, v63, 0, s[18:19]\n" \
"v_smfmac_f32_16x16x64_bf16 v[64:67], v[44:47], v[208:215], v26\nds_read_b128 v[72:75], v25 offset:9216\nv_cndmask_b32 v78, v63, 0, s[20:21]\nv_smfmac_f32_16x16x64_bf16 v[48:51], v[32:35], v[112:119], v26\n" \
"ds_read_b128 v[44:47], v24 offset:16384\nv_cndmask_b32 v79, v63, 0, s[22:23]\nv_smfmac_f32_16x16x64_bf16 v[64:67], v[32:35], v[216:223], v26\nds_read_b128 v[80:83], v60 offset:0\n" \
"v_mov_b32 v62, 0xff61b1e6\nv_smfmac_f32_16x16x64_bf16 v[48:51], v[88:91], a[216:223], v26\nds_read_b128 v[32:35], v24 offset:57344\nv_cndmask_b32 v84, v62, v63, s[16:17]\n" \
"v_smfmac_f32_16x16x64_bf16 v[64:67], v[88:91], v[168:175], v26\ns_waitcnt lgkmcnt(6)\nds_read_b128 v[88:91], v24 offset:61440\nv_smfmac_f32_16x16x64_bf16 v[48:51], v[40:43], v[96:103], v26\n" \
"ds_read_b128 v[104:107], v60 offset:1024\nv_cndmask_b32 v85, v62, v63, s[18:19]\nv_smfmac_f32_16x16x64_bf16 v[64:67], v[40:43], v[232:239], v26\nds_read_b128 v[96:99], v24 offset:58368\n" \
"v_cndmask_b32 v86, v62, v63, s[20:21]\nv_smfmac_f32_16x16x64_bf16 v[48:51], v[92:95], a[248:255], v26\nds_read_b128 v[40:43], v24 offset:62464\nv_cndmask_b32 v87, v62, v63, s[22:23]\n" \
"v_smfmac_f32_16x16x64_bf16 v[64:67], v[92:95], v[52:59], v26\ns_waitcnt lgkmcnt(5)\nds_read_b128 v[56:59], v60 offset:2048\nv_smfmac_f32_16x16x64_bf16 v[48:51], v[36:39], v[68:75], v26\n" \
"ds_read_b128 v[52:55], v24 offset:59392\nv_smfmac_f32_16x16x64_bf16 v[64:67], v[36:39], v[200:207], v26\nds_read_b128 v[68:71], v24 offset:63488\nv_smfmac_f32_16x16x64_bf16 v[48:51], v[44:47], a[208:215], v26\n" \
"ds_read_b128 v[36:39], v60 offset:3072\nds_read_b128 v[60:63], v24 offset:60416\nv_smfmac_f32_16x16x64_bf16 v[64:67], v[44:47], v[160:167], v26\ns_waitcnt lgkmcnt(6)\n" \
"ds_read_b128 v[44:47], v24 offset:64512\nv_mfma_f32_16x16x32_bf16 v[76:79], v[32:35], v[80:83], v[76:79]\nds_read_b128 v[72:75], v24 offset:0\nv_add_f32 v32, v48, v50\n" \
"v_add_f32 v34, v49, v51\nv_mfma_f32_16x16x32_bf16 v[84:87], v[88:91], v[80:83], v[84:87]\ns_waitcnt lgkmcnt(6)\nv_add_f32 v33, v64, v66\n" \
"v_add_f32 v35, v65, v67\nv_cvt_pk_bf16_f32 v48, v32, v33\nv_cvt_pk_bf16_f32 v49, v34, v35\nv_mfma_f32_16x16x32_bf16 v[76:79], v[96:99], v[104:107], v[76:79]\n" \
"s_waitcnt lgkmcnt(4)\nds_write2_b32 v27, v48, v49 offset0:128 offset1:132\nv_mfma_f32_16x16x32_bf16 v[84:87], v[40:43], v[104:107], v[84:87]\nds_read_b128 v[32:35], v24 offset:32768\n" \
"v_mfma_f32_16x16x32_bf16 v[76:79], v[52:55], v[56:59], v[76:79]\ns_waitcnt lgkmcnt(4)\ns_cmp_eq_u64 vcc, 0\ns_cselect_b32 s14, s10, 0\n" \
"v_mfma_f32_16x16x32_bf16 v[84:87], v[68:71], v[56:59], v[84:87]\ns_cmp_eq_u32 s51, 0\ns_cbranch_scc1 4f\n5:\n" \
"v_mfma_f32_16x16x32_bf16 v[76:79], v[60:63], v[36:39], v[76:79]\ns_waitcnt lgkmcnt(1)\ns_barrier\nds_read_b128 v[40:43], v28 offset:512\n" \
"ds_read_b128 v[48:51], v28 offset:1536\nv_mfma_f32_16x16x32_bf16 v[84:87], v[44:47], v[36:39], v[84:87]\nds_read_b128 v[64:67], v28 offset:2560\nds_read_b128 v[88:91], v28 offset:3584\n" \
"v_mfma_f32_16x16x32_bf16 v[44:47], v[0:3], v[72:75], 0\nds_read_b128 v[92:95], v24 offset:1024\nv_max3_f32 v31, v76, v77, v78\nv_max3_f32 v31, v31, v79, s47\n" \
"v_mfma_f32_16x16x32_bf16 v[52:55], a[96:99], v[72:75], 0\nds_read_b128 v[96:99], v24 offset:33792\nv_max3_f32 v100, v84, v85, v86\nv_max3_f32 v101, v31, v100, v87\n" \
"v_max3_f32 v31, v31, v100, v87\nv_mfma_f32_16x16x32_bf16 v[60:63], a[208:211], v[72:75], 0\ns_waitcnt lgkmcnt(6)\nv_mul_f32 v100, s46, v31\n" \
"v_fma_f32 v76, v76, s46, -v100\nv_exp_f32 v76, v76\nv_mfma_f32_16x16x32_bf16 v[68:71], v[160:163], v[72:75], 0\ns_waitcnt lgkmcnt(4)\n" \
"v_fma_f32 v78, v78, s46, -v100\nv_exp_f32 v78, v78\nv_fma_f32 v77, v77, s46, -v100\nv_fma_f32 v79, v79, s46, -v100\n" \
"v_mfma_f32_16x16x32_bf16 v[108:111], v[40:43], v[80:83], 0\nv_exp_f32 v77, v77\nv_fma_f32 v84, v84, s46, -v100\nv_fma_f32 v86, v86, s46, -v100\n" \
"v_fma_f32 v85, v85, s46, -v100\nv_mfma_f32_16x16x32_bf16 v[108:111], v[48:51], v[104:107], v[108:111]\ns_waitcnt lgkmcnt(0)\nv_fma_f32 v87, v87, s46, -v100\n" \
"v_cndmask_b32 v40, v76, v78, s[24:25]\nv_mfma_f32_16x16x32_bf16 v[108:111], v[64:67], v[56:59], v[108:111]\nv_exp_f32 v79, v79\nv_mfma_f32_16x16x32_bf16 v[108:111], v[88:91], v[36:39], v[108:111]\n" \
"v_exp_f32 v84, v84\nv_mfma_f32_16x16x32_bf16 v[48:51], v[0:3], v[32:35], 0\nv_exp_f32 v86, v86\nv_exp_f32 v85, v85\n" \
"v_mfma_f32_16x16x32_bf16 v[56:59], a[96:99], v[32:35], 0\nv_exp_f32 v87, v87\nv_cndmask_b32 v41, v77, v79, s[24:25]\nv_cndmask_b32 v42, v84, v86, s[24:25]\n" \
"v_mfma_f32_16x16x32_bf16 v[64:67], a[208:211], v[32:35], 0\nv_cndmask_b32 v43, v85, v87, s[24:25]\nv_permlane32_swap_b32 v31, v101\nv_mfma_f32_16x16x32_bf16 v[72:75], v[160:163], v[32:35], 0\n" \
"v_max3_f32 v36, v108, v109, v110\nv_max3_f32 v31, v31, v101, v111\nv_max_f32 v37, v36, v31\nv_max_f32 v36, v36, v31\n" \
"v_mfma_f32_16x16x32_bf16 v[44:47], v[4:7], v[92:95], v[44:47]\nds_read_b128 v[32:35], v24 offset:2048\nv_permlane16_swap_b32 v36, v37\nv_mfma_f32_16x16x32_bf16 v[48:51], v[4:7], v[96:99], v[48:51]\n" \
"ds_read_b128 v[80:83], v24 offset:34816\nv_max_f32 v36, v36, v37\nv_mfma_f32_16x16x32_bf16 v[52:55], a[100:103], v[92:95], v[52:55]\nv_cndmask_b32 v38, v108, v110, s[26:27]\n" \
"v_fma_f32 v100, v36, -s46, v100\nv_mul_f32 v36, s46, v36\nv_cndmask_b32 v39, v109, v111, s[26:27]\nv_fma_f32 v38, v38, s46, -v36\n" \
"v_mfma_f32_16x16x32_bf16 v[56:59], a[100:103], v[96:99], v[56:59]\nv_add_f32 v88, v76, v78\nv_add_f32 v89, v77, v79\nv_fma_f32 v39, v39, s46, -v36\n" \
"v_add_f32 v90, v84, v86\nv_add_f32 v88, v88, v89\nv_mfma_f32_16x16x32_bf16 v[60:63], a[212:215], v[92:95], v[60:63]\nv_exp_f32 v100, v100\n" \
"v_exp_f32 v38, v38\nv_add_f32 v91, v85, v87\nv_mfma_f32_16x16x32_bf16 v[64:67], a[212:215], v[96:99], v[64:67]\nv_add_f32 v88, v88, v90\n" \
"v_exp_f32 v39, v39\nv_add_f32 v88, v88, v91\nv_fma_f32 v88, v88, v100, v38\nv_mfma_f32_16x16x32_bf16 v[68:71], v[164:167], v[92:95], v[68:71]\n" \
"s_waitcnt lgkmcnt(0)\nv_add_f32 v89, v88, v39\nv_add_f32 v88, v39, v88\nv_mfma_f32_16x16x32_bf16 v[72:75], v[164:167], v[96:99], v[72:75]\n" \
"ds_read_b128 v[92:95], v24 offset:3072\nv_permlane16_swap_b32 v88, v89\nv_add_f32 v90, v88, v89\nv_add_f32 v88, v89, v88\n" \
"v_mfma_f32_16x16x32_bf16 v[44:47], a[0:3], v[32:35], v[44:47]\nds_read_b128 v[96:99], v24 offset:35840\nv_permlane32_swap_b32 v88, v90\nv_mfma_f32_16x16x32_bf16 v[48:51], a[0:3], v[80:83], v[48:51]\n" \
"v_add_f32 v88, v88, v90\nv_mfma_f32_16x16x32_bf16 v[52:55], v[8:11], v[32:35], v[52:55]\nv_rcp_f32 v88, v88\nv_mfma_f32_16x16x32_bf16 v[56:59], v[8:11], v[80:83], v[56:59]\n" \
"v_mul_f32 v100, v100, v88\nv_mul_f32 v40, v40, v100\nv_mul_f32 v41, v41, v100\nv_mul_f32 v38, v38, v88\n" \
"v_mul_f32 v39, v39, v88\nv_mfma_f32_16x16x32_bf16 v[60:63], a[216:219], v[32:35], v[60:63]\nv_mul_f32 v42, v42, v100\nv_mul_f32 v43, v43, v100\n" \
"v_cvt_pk_bf16_f32 v36, v40, v41\nv_cvt_pk_bf16_f32 v31, v38, v39\nv_cvt_pk_bf16_f32 v37, v42, v43\nv_mfma_f32_16x16x32_bf16 v[64:67], a[216:219], v[80:83], v[64:67]\n" \
"v_and_or_b32 v36, v36, -2, s95\nv_and_or_b32 v31, v31, -2, s95\nv_mfma_f32_16x16x32_bf16 v[68:71], v[168:171], v[32:35], v[68:71]\ns_waitcnt lgkmcnt(0)\n" \
"buffer_store_dwordx2 v[36:37], v29, s[8:11], s99 offen offset:0 sc1\nbuffer_store_dword v31, v30, s[8:11], s96 offen offset:1536 sc1\nv_mfma_f32_16x16x32_bf16 v[72:75], v[168:171], v[80:83], v[72:75]\nds_read_b128 v[88:91], v24 offset:4096\n" \
"v_mul_f32 v76, v76, v100\nv_mfma_f32_16x16x32_bf16 v[44:47], a[4:7], v[92:95], v[44:47]\nds_read_b128 v[32:35], v24 offset:36864\nv_mul_f32 v77, v77, v100\n" \
"v_mfma_f32_16x16x32_bf16 v[48:51], a[4:7], v[96:99], v[48:51]\nv_mul_f32 v78, v78, v100\nv_mul_f32 v79, v79, v100\nv_mfma_f32_16x16x32_bf16 v[52:55], v[12:15], v[92:95], v[52:55]\n" \
"v_cvt_pk_bf16_f32 v80, v76, v77\nv_cvt_pk_bf16_f32 v81, v78, v79\nv_mfma_f32_16x16x32_bf16 v[56:59], v[12:15], v[96:99], v[56:59]\ns_add_u32 m0, s32, 0x4000\n" \
"v_and_or_b32 v80, v80, -2, s95\nv_mul_f32 v84, v84, v100\nv_mfma_f32_16x16x32_bf16 v[60:63], a[220:223], v[92:95], v[60:63]\nbuffer_store_dwordx2 v[80:81], v29, s[8:11], s97 offen offset:1024 sc1\n" \
"v_mul_f32 v85, v85, v100\nv_mfma_f32_16x16x32_bf16 v[64:67], a[220:223], v[96:99], v[64:67]\ns_add_u32 s99, s31, 0x4000\nv_mul_f32 v86, v86, v100\n" \
"v_mul_f32 v87, v87, v100\nv_mfma_f32_16x16x32_bf16 v[68:71], v[172:175], v[92:95], v[68:71]\ns_waitcnt lgkmcnt(0)\nv_cvt_pk_bf16_f32 v82, v84, v85\n" \
"v_cvt_pk_bf16_f32 v83, v86, v87\nv_mfma_f32_16x16x32_bf16 v[72:75], v[172:175], v[96:99], v[72:75]\nds_read_b128 v[36:39], v24 offset:5120\nv_mfma_f32_16x16x32_bf16 v[44:47], a[8:11], v[88:91], v[44:47]\n" \
"ds_read_b128 v[40:43], v24 offset:37888\nv_mfma_f32_16x16x32_bf16 v[48:51], a[8:11], v[32:35], v[48:51]\nbuffer_store_dwordx4 v[80:83], v24, s[8:11], s98 offen offset:0 sc1\nbuffer_load_dwordx4 v24, s[8:11], s99 offen offset:0 sc1 lds\n" \
"v_mfma_f32_16x16x32_bf16 v[52:55], a[104:107], v[88:91], v[52:55]\nv_mfma_f32_16x16x32_bf16 v[56:59], a[104:107], v[32:35], v[56:59]\nbuffer_load_dwordx4 v24, s[8:11], s99 offen offset:1024 sc1 lds\nv_mfma_f32_16x16x32_bf16 v[60:63], v[16:19], v[88:91], v[60:63]\n" \
"v_mfma_f32_16x16x32_bf16 v[64:67], v[16:19], v[32:35], v[64:67]\nv_mfma_f32_16x16x32_bf16 v[68:71], v[176:179], v[88:91], v[68:71]\ns_waitcnt lgkmcnt(0)\nv_mfma_f32_16x16x32_bf16 v[72:75], v[176:179], v[32:35], v[72:75]\n" \
"ds_read_b128 v[76:79], v24 offset:6144\nv_mfma_f32_16x16x32_bf16 v[44:47], a[12:15], v[36:39], v[44:47]\nds_read_b128 v[32:35], v24 offset:38912\nv_mfma_f32_16x16x32_bf16 v[48:51], a[12:15], v[40:43], v[48:51]\n" \
"buffer_load_dwordx4 v24, s[8:11], s99 offen offset:2048 sc1 lds\nv_mfma_f32_16x16x32_bf16 v[52:55], a[108:111], v[36:39], v[52:55]\nv_mfma_f32_16x16x32_bf16 v[56:59], a[108:111], v[40:43], v[56:59]\nbuffer_load_dwordx4 v24, s[8:11], s99 offen offset:3072 sc1 lds\n" \
"v_mfma_f32_16x16x32_bf16 v[60:63], v[20:23], v[36:39], v[60:63]\nv_mfma_f32_16x16x32_bf16 v[64:67], v[20:23], v[40:43], v[64:67]\nv_mfma_f32_16x16x32_bf16 v[68:71], v[180:183], v[36:39], v[68:71]\ns_waitcnt lgkmcnt(0)\n" \
"v_mfma_f32_16x16x32_bf16 v[72:75], v[180:183], v[40:43], v[72:75]\nds_read_b128 v[36:39], v24 offset:7168\nv_mfma_f32_16x16x32_bf16 v[44:47], a[16:19], v[76:79], v[44:47]\nds_read_b128 v[40:43], v24 offset:39936\n" \
"v_mfma_f32_16x16x32_bf16 v[48:51], a[16:19], v[32:35], v[48:51]\ns_add_u32 m0, s32, 0x6000\ns_add_u32 s99, s31, 0x6000\nv_mfma_f32_16x16x32_bf16 v[52:55], a[112:115], v[76:79], v[52:55]\n" \
"v_mfma_f32_16x16x32_bf16 v[56:59], a[112:115], v[32:35], v[56:59]\nv_mfma_f32_16x16x32_bf16 v[60:63], a[224:227], v[76:79], v[60:63]\nds_read_b128 v[80:83], v25 offset:0\nv_mfma_f32_16x16x32_bf16 v[64:67], a[224:227], v[32:35], v[64:67]\n" \
"buffer_load_dwordx4 v24, s[8:11], s99 offen offset:0 sc1 lds\nv_mfma_f32_16x16x32_bf16 v[68:71], v[184:187], v[76:79], v[68:71]\ns_waitcnt lgkmcnt(1)\nv_mfma_f32_16x16x32_bf16 v[72:75], v[184:187], v[32:35], v[72:75]\n" \
"ds_read_b128 v[76:79], v24 offset:8192\nv_mfma_f32_16x16x32_bf16 v[44:47], a[20:23], v[36:39], v[44:47]\nds_read_b128 v[32:35], v24 offset:40960\nv_mfma_f32_16x16x32_bf16 v[48:51], a[20:23], v[40:43], v[48:51]\n" \
"buffer_load_dwordx4 v24, s[8:11], s99 offen offset:1024 sc1 lds\nv_mfma_f32_16x16x32_bf16 v[52:55], a[116:119], v[36:39], v[52:55]\nv_mfma_f32_16x16x32_bf16 v[56:59], a[116:119], v[40:43], v[56:59]\nv_mfma_f32_16x16x32_bf16 v[60:63], a[228:231], v[36:39], v[60:63]\n" \
"ds_read_b128 v[84:87], v25 offset:1024\nv_mfma_f32_16x16x32_bf16 v[64:67], a[228:231], v[40:43], v[64:67]\nbuffer_load_dwordx4 v24, s[8:11], s99 offen offset:2048 sc1 lds\nv_mfma_f32_16x16x32_bf16 v[68:71], v[188:191], v[36:39], v[68:71]\n" \
"s_waitcnt lgkmcnt(1)\nv_mfma_f32_16x16x32_bf16 v[72:75], v[188:191], v[40:43], v[72:75]\nds_read_b128 v[36:39], v24 offset:9216\nv_mfma_f32_16x16x32_bf16 v[44:47], v[80:83], v[76:79], v[44:47]\n" \
"ds_read_b128 v[40:43], v24 offset:41984\nv_mfma_f32_16x16x32_bf16 v[48:51], v[80:83], v[32:35], v[48:51]\nbuffer_load_dwordx4 v24, s[8:11], s99 offen offset:3072 sc1 lds\nv_mfma_f32_16x16x32_bf16 v[52:55], a[120:123], v[76:79], v[52:55]\n" \
"v_mfma_f32_16x16x32_bf16 v[56:59], a[120:123], v[32:35], v[56:59]\nv_mfma_f32_16x16x32_bf16 v[60:63], a[232:235], v[76:79], v[60:63]\nv_mfma_f32_16x16x32_bf16 v[64:67], a[232:235], v[32:35], v[64:67]\nv_mfma_f32_16x16x32_bf16 v[68:71], v[192:195], v[76:79], v[68:71]\n" \
"s_waitcnt lgkmcnt(0)\nv_mfma_f32_16x16x32_bf16 v[72:75], v[192:195], v[32:35], v[72:75]\nds_read_b128 v[76:79], v24 offset:10240\nv_mfma_f32_16x16x32_bf16 v[44:47], v[84:87], v[36:39], v[44:47]\n" \
"ds_read_b128 v[32:35], v24 offset:43008\nv_mfma_f32_16x16x32_bf16 v[48:51], v[84:87], v[40:43], v[48:51]\nv_mfma_f32_16x16x32_bf16 v[52:55], a[124:127], v[36:39], v[52:55]\nds_read_b128 v[80:83], v25 offset:14336\n" \
"v_mfma_f32_16x16x32_bf16 v[56:59], a[124:127], v[40:43], v[56:59]\nv_mfma_f32_16x16x32_bf16 v[60:63], a[236:239], v[36:39], v[60:63]\nv_mfma_f32_16x16x32_bf16 v[64:67], a[236:239], v[40:43], v[64:67]\nv_mfma_f32_16x16x32_bf16 v[68:71], v[196:199], v[36:39], v[68:71]\n" \
"s_waitcnt lgkmcnt(1)\nv_mfma_f32_16x16x32_bf16 v[72:75], v[196:199], v[40:43], v[72:75]\nds_read_b128 v[36:39], v24 offset:11264\nv_mfma_f32_16x16x32_bf16 v[44:47], a[24:27], v[76:79], v[44:47]\n" \
"ds_read_b128 v[40:43], v24 offset:44032\nv_mfma_f32_16x16x32_bf16 v[48:51], a[24:27], v[32:35], v[48:51]\nv_mfma_f32_16x16x32_bf16 v[52:55], a[128:131], v[76:79], v[52:55]\nds_read_b128 v[84:87], v25 offset:15360\n" \
"v_mfma_f32_16x16x32_bf16 v[56:59], a[128:131], v[32:35], v[56:59]\nv_mfma_f32_16x16x32_bf16 v[60:63], a[240:243], v[76:79], v[60:63]\ns_waitcnt lgkmcnt(3)\nv_mfma_f32_16x16x32_bf16 v[64:67], a[240:243], v[32:35], v[64:67]\n" \
"v_mfma_f32_16x16x32_bf16 v[68:71], v[80:83], v[76:79], v[68:71]\ns_waitcnt lgkmcnt(1)\nv_mfma_f32_16x16x32_bf16 v[72:75], v[80:83], v[32:35], v[72:75]\nds_read_b128 v[76:79], v24 offset:12288\n" \
"v_mfma_f32_16x16x32_bf16 v[44:47], a[28:31], v[36:39], v[44:47]\nds_read_b128 v[32:35], v24 offset:45056\nv_mfma_f32_16x16x32_bf16 v[48:51], a[28:31], v[40:43], v[48:51]\nds_read_b128 v[80:83], v25 offset:8192\n" \
"v_mfma_f32_16x16x32_bf16 v[52:55], a[132:135], v[36:39], v[52:55]\nv_mfma_f32_16x16x32_bf16 v[56:59], a[132:135], v[40:43], v[56:59]\nv_mfma_f32_16x16x32_bf16 v[60:63], a[244:247], v[36:39], v[60:63]\ns_waitcnt lgkmcnt(3)\n" \
"v_mfma_f32_16x16x32_bf16 v[64:67], a[244:247], v[40:43], v[64:67]\nv_mfma_f32_16x16x32_bf16 v[68:71], v[84:87], v[36:39], v[68:71]\ns_waitcnt lgkmcnt(1)\nv_mfma_f32_16x16x32_bf16 v[72:75], v[84:87], v[40:43], v[72:75]\n" \
"ds_read_b128 v[36:39], v24 offset:13312\nv_mfma_f32_16x16x32_bf16 v[44:47], a[32:35], v[76:79], v[44:47]\nds_read_b128 v[40:43], v24 offset:46080\nv_mfma_f32_16x16x32_bf16 v[48:51], a[32:35], v[32:35], v[48:51]\n" \
"ds_read_b128 v[84:87], v25 offset:9216\nv_mfma_f32_16x16x32_bf16 v[52:55], a[136:139], v[76:79], v[52:55]\ns_waitcnt lgkmcnt(3)\nv_mfma_f32_16x16x32_bf16 v[56:59], a[136:139], v[32:35], v[56:59]\n" \
"v_mfma_f32_16x16x32_bf16 v[60:63], v[80:83], v[76:79], v[60:63]\nv_mfma_f32_16x16x32_bf16 v[64:67], v[80:83], v[32:35], v[64:67]\nv_mfma_f32_16x16x32_bf16 v[68:71], v[200:203], v[76:79], v[68:71]\ns_waitcnt lgkmcnt(1)\n" \
"v_mfma_f32_16x16x32_bf16 v[72:75], v[200:203], v[32:35], v[72:75]\nds_read_b128 v[76:79], v24 offset:14336\nv_mfma_f32_16x16x32_bf16 v[44:47], a[36:39], v[36:39], v[44:47]\nds_read_b128 v[32:35], v24 offset:47104\n" \
"v_mfma_f32_16x16x32_bf16 v[48:51], a[36:39], v[40:43], v[48:51]\nv_mfma_f32_16x16x32_bf16 v[52:55], a[140:143], v[36:39], v[52:55]\ns_waitcnt lgkmcnt(2)\nds_read_b128 v[80:83], v25 offset:16384\n" \
"v_mfma_f32_16x16x32_bf16 v[56:59], a[140:143], v[40:43], v[56:59]\nv_mfma_f32_16x16x32_bf16 v[60:63], v[84:87], v[36:39], v[60:63]\nv_mfma_f32_16x16x32_bf16 v[64:67], v[84:87], v[40:43], v[64:67]\nv_mfma_f32_16x16x32_bf16 v[68:71], v[204:207], v[36:39], v[68:71]\n" \
"s_waitcnt lgkmcnt(1)\nv_mfma_f32_16x16x32_bf16 v[72:75], v[204:207], v[40:43], v[72:75]\nds_read_b128 v[36:39], v24 offset:15360\nv_mfma_f32_16x16x32_bf16 v[44:47], a[40:43], v[76:79], v[44:47]\n" \
"ds_read_b128 v[40:43], v24 offset:48128\nv_mfma_f32_16x16x32_bf16 v[48:51], a[40:43], v[32:35], v[48:51]\nv_mfma_f32_16x16x32_bf16 v[52:55], a[144:147], v[76:79], v[52:55]\nds_read_b128 v[84:87], v25 offset:17408\n" \
"v_mfma_f32_16x16x32_bf16 v[56:59], a[144:147], v[32:35], v[56:59]\nv_mfma_f32_16x16x32_bf16 v[60:63], a[248:251], v[76:79], v[60:63]\ns_waitcnt lgkmcnt(3)\nv_mfma_f32_16x16x32_bf16 v[64:67], a[248:251], v[32:35], v[64:67]\n" \
"s_waitcnt vmcnt(4) lgkmcnt(1)\ns_barrier\nv_mfma_f32_16x16x32_bf16 v[68:71], v[80:83], v[76:79], v[68:71]\ns_add_u32 m0, s32, 0\n" \
"s_add_u32 s99, s31, 0x10000\nv_mfma_f32_16x16x32_bf16 v[72:75], v[80:83], v[32:35], v[72:75]\nds_read_b128 v[76:79], v24 offset:16384\nv_mfma_f32_16x16x32_bf16 v[44:47], a[44:47], v[36:39], v[44:47]\n" \
"ds_read_b128 v[32:35], v24 offset:49152\nv_mfma_f32_16x16x32_bf16 v[48:51], a[44:47], v[40:43], v[48:51]\nds_read_b128 v[80:83], v25 offset:10240\nv_mfma_f32_16x16x32_bf16 v[52:55], a[148:151], v[36:39], v[52:55]\n" \
"buffer_load_dwordx4 v24, s[12:15], s99 offen offset:0 sc1 lds\nv_mfma_f32_16x16x32_bf16 v[56:59], a[148:151], v[40:43], v[56:59]\nv_mfma_f32_16x16x32_bf16 v[60:63], a[252:255], v[36:39], v[60:63]\ns_waitcnt lgkmcnt(3)\n" \
"v_mfma_f32_16x16x32_bf16 v[64:67], a[252:255], v[40:43], v[64:67]\nbuffer_load_dwordx4 v24, s[12:15], s99 offen offset:1024 sc1 lds\nv_mfma_f32_16x16x32_bf16 v[68:71], v[84:87], v[36:39], v[68:71]\ns_waitcnt lgkmcnt(1)\n" \
"v_mfma_f32_16x16x32_bf16 v[72:75], v[84:87], v[40:43], v[72:75]\nds_read_b128 v[36:39], v24 offset:17408\nv_mfma_f32_16x16x32_bf16 v[44:47], a[48:51], v[76:79], v[44:47]\nds_read_b128 v[40:43], v24 offset:50176\n" \
"v_mfma_f32_16x16x32_bf16 v[48:51], a[48:51], v[32:35], v[48:51]\nds_read_b128 v[84:87], v25 offset:11264\nv_mfma_f32_16x16x32_bf16 v[52:55], a[152:155], v[76:79], v[52:55]\ns_waitcnt lgkmcnt(3)\n" \
"buffer_load_dwordx4 v24, s[12:15], s99 offen offset:2048 sc1 lds\nv_mfma_f32_16x16x32_bf16 v[56:59], a[152:155], v[32:35], v[56:59]\nv_mfma_f32_16x16x32_bf16 v[60:63], v[80:83], v[76:79], v[60:63]\nv_mfma_f32_16x16x32_bf16 v[64:67], v[80:83], v[32:35], v[64:67]\n" \
"buffer_load_dwordx4 v24, s[12:15], s99 offen offset:3072 sc1 lds\nv_mfma_f32_16x16x32_bf16 v[68:71], v[208:211], v[76:79], v[68:71]\ns_waitcnt lgkmcnt(1)\nds_read_b128 v[76:79], v25 offset:6144\n" \
"v_mfma_f32_16x16x32_bf16 v[72:75], v[208:211], v[32:35], v[72:75]\nds_read_b128 v[80:83], v24 offset:18432\nv_mfma_f32_16x16x32_bf16 v[44:47], a[52:55], v[36:39], v[44:47]\nds_read_b128 v[32:35], v24 offset:51200\n" \
"s_add_u32 m0, s32, 0x2000\nv_mfma_f32_16x16x32_bf16 v[48:51], a[52:55], v[40:43], v[48:51]\ns_add_u32 s99, s31, 0x12000\nv_mfma_f32_16x16x32_bf16 v[52:55], a[156:159], v[36:39], v[52:55]\n" \
"s_waitcnt lgkmcnt(3)\nv_mfma_f32_16x16x32_bf16 v[56:59], a[156:159], v[40:43], v[56:59]\nv_mfma_f32_16x16x32_bf16 v[60:63], v[84:87], v[36:39], v[60:63]\nbuffer_load_dwordx4 v24, s[12:15], s99 offen offset:0 sc1 lds\n" \
"v_mfma_f32_16x16x32_bf16 v[64:67], v[84:87], v[40:43], v[64:67]\nv_mfma_f32_16x16x32_bf16 v[68:71], v[212:215], v[36:39], v[68:71]\ns_waitcnt lgkmcnt(0)\nds_read_b128 v[36:39], v25 offset:7168\n" \
"v_mfma_f32_16x16x32_bf16 v[72:75], v[212:215], v[40:43], v[72:75]\nds_read_b128 v[84:87], v24 offset:19456\nv_mfma_f32_16x16x32_bf16 v[44:47], a[56:59], v[80:83], v[44:47]\nds_read_b128 v[40:43], v24 offset:52224\n" \
"v_mfma_f32_16x16x32_bf16 v[48:51], a[56:59], v[32:35], v[48:51]\nbuffer_load_dwordx4 v24, s[12:15], s99 offen offset:1024 sc1 lds\nv_mfma_f32_16x16x32_bf16 v[52:55], v[76:79], v[80:83], v[52:55]\nv_mfma_f32_16x16x32_bf16 v[56:59], v[76:79], v[32:35], v[56:59]\n" \
"v_mfma_f32_16x16x32_bf16 v[60:63], v[112:115], v[80:83], v[60:63]\nds_read_b128 v[76:79], v25 offset:2048\nv_mfma_f32_16x16x32_bf16 v[64:67], v[112:115], v[32:35], v[64:67]\nbuffer_load_dwordx4 v24, s[12:15], s99 offen offset:2048 sc1 lds\n" \
"v_mfma_f32_16x16x32_bf16 v[68:71], v[216:219], v[80:83], v[68:71]\ns_waitcnt lgkmcnt(1)\nv_mfma_f32_16x16x32_bf16 v[72:75], v[216:219], v[32:35], v[72:75]\nds_read_b128 v[80:83], v24 offset:20480\n" \
"v_mfma_f32_16x16x32_bf16 v[44:47], a[60:63], v[84:87], v[44:47]\nds_read_b128 v[32:35], v24 offset:53248\nv_mfma_f32_16x16x32_bf16 v[48:51], a[60:63], v[40:43], v[48:51]\nbuffer_load_dwordx4 v24, s[12:15], s99 offen offset:3072 sc1 lds\n" \
"v_mfma_f32_16x16x32_bf16 v[52:55], v[36:39], v[84:87], v[52:55]\nv_mfma_f32_16x16x32_bf16 v[56:59], v[36:39], v[40:43], v[56:59]\nv_mfma_f32_16x16x32_bf16 v[60:63], v[116:119], v[84:87], v[60:63]\nv_mfma_f32_16x16x32_bf16 v[64:67], v[116:119], v[40:43], v[64:67]\n" \
"v_mfma_f32_16x16x32_bf16 v[68:71], v[220:223], v[84:87], v[68:71]\nv_mfma_f32_16x16x32_bf16 v[72:75], v[220:223], v[40:43], v[72:75]\ns_branch 1b\n2:\n" \
"s_add_u32 s52, s52, 1\ns_cmp_lt_u32 s52, 0x40000\ns_cbranch_scc0 9f\nv_cmp_eq_u32 vcc, -1, v40\n" \
"s_cbranch_vccz 6f\nbuffer_load_dwordx4 v24, s[8:11], s94 offen offset:0 sc1 lds\n6:\nv_cmp_eq_u32 vcc, -1, v76\n" \
"s_cbranch_vccz 6f\nbuffer_load_dwordx4 v24, s[8:11], s94 offen offset:1024 sc1 lds\n6:\nv_cmp_eq_u32 vcc, -1, v84\n" \
"s_cbranch_vccz 6f\nbuffer_load_dwordx4 v24, s[8:11], s94 offen offset:2048 sc1 lds\n6:\nv_cmp_eq_u32 vcc, -1, v88\n" \
"s_cbranch_vccz 6f\nbuffer_load_dwordx4 v24, s[8:11], s94 offen offset:3072 sc1 lds\n6:\ns_waitcnt vmcnt(0)\n" \
"ds_read_b128 v[40:43], v31 offset:16384\nds_read_b128 v[76:79], v31 offset:17408\nds_read_b128 v[84:87], v31 offset:18432\nds_read_b128 v[88:91], v31 offset:19456\n" \
"s_waitcnt lgkmcnt(0)\nv_max3_u32 v96, v40, v76, v84\nv_max_u32 v96, v96, v88\nv_cmp_eq_u32 vcc, -1, v96\n" \
"s_cbranch_vccnz 2b\ns_branch 3b\n4:\ns_cmp_gt_u32 s30, 64\n" \
"s_cbranch_scc1 10f\ns_cmp_eq_u32 s30, 64\ns_cbranch_scc1 5b\nv_bfe_u32 v40, v24, 6, 3\n" \
"v_lshlrev_b32 v40, 2, v40\ns_lshl_b32 s94, s30, 5\ns_add_u32 s94, s94, s48\ns_cmp_lg_u32 s58, 0\n" \
"s_cbranch_scc1 8f\n7:\nbuffer_load_dword v41, v40, s[8:11], s94 offen sc1\ns_waitcnt vmcnt(0)\n" \
"v_cmp_eq_u32 vcc, -1, v41\ns_cbranch_vccz 8f\ns_add_u32 s52, s52, 1\ns_cmp_lt_u32 s52, 0x40000\n" \
"s_cbranch_scc0 9f\ns_sleep 8\ns_branch 7b\n8:\n" \
"s_mov_b32 m0, s53\ns_add_u32 s94, s30, 1\ns_min_u32 s94, s94, 63\ns_lshl_b32 s94, s94, 5\n" \
"s_add_u32 s94, s94, s48\nbuffer_load_dword v40, s[8:11], s94 offen offset:0 sc1 lds\ns_cmp_lg_u32 s58, 0\ns_mov_b32 s58, 0\n" \
"s_cbranch_scc1 6f\ns_add_u32 m0, s32, 0\ns_add_u32 s94, s31, 0\nbuffer_load_dwordx4 v24, s[8:11], s94 offen offset:0 sc1 lds\n" \
"buffer_load_dwordx4 v24, s[8:11], s94 offen offset:1024 sc1 lds\nbuffer_load_dwordx4 v24, s[8:11], s94 offen offset:2048 sc1 lds\nbuffer_load_dwordx4 v24, s[8:11], s94 offen offset:3072 sc1 lds\ns_add_u32 m0, s32, 0x2000\n" \
"s_add_u32 s94, s31, 0x2000\nbuffer_load_dwordx4 v24, s[8:11], s94 offen offset:0 sc1 lds\nbuffer_load_dwordx4 v24, s[8:11], s94 offen offset:1024 sc1 lds\nbuffer_load_dwordx4 v24, s[8:11], s94 offen offset:2048 sc1 lds\n" \
"buffer_load_dwordx4 v24, s[8:11], s94 offen offset:3072 sc1 lds\n6:\nv_lshrrev_b32 v40, 2, v24\nv_add_u32 v40, 0x11000, v40\n" \
"s_waitcnt vmcnt(0) lgkmcnt(0)\ns_barrier\nds_read_b32 v41, v40 offset:0\nds_read_b128 v[72:75], v24 offset:0\n" \
"ds_read_b128 v[32:35], v24 offset:32768\ns_waitcnt lgkmcnt(0)\nv_cmp_eq_u32 vcc, -1, v41\ns_cmp_eq_u64 vcc, 0\n" \
"s_cselect_b32 s14, s10, 0\ns_branch 5b\n10:\ns_waitcnt vmcnt(0) lgkmcnt(0)\n" \
"s_cmp_lt_i32 s6, 0\ns_cbranch_scc1 9f\ns_mov_b64 s[0:1], s[4:5]\ns_mov_b32 s2, s6\n" \
"v_lshrrev_b32 v0, 4, v24\nv_add_u32 v0, s7, v0\ns_barrier\ns_getpc_b64 s[8:9]\n" \
"11:\ns_add_u32 s8, s8, _Z5mega64Args-11b\ns_addc_u32 s9, s9, -1\ns_setpc_b64 s[8:9]\n" \
"9:\ns_waitcnt vmcnt(0)\ns_endpgm\n"
#define B_LDS 0x44840c0202a00110ull
#define B_VAR 0x0000000000010001ull
#define B_LV 8192
#define B_LOOP \
"v_mov_b32 v33, v20\nv_cmp_eq_u32 vcc, -1, v33\nv_bfe_u32 v32, v16, 4, 3\nv_lshlrev_b32 v32, 2, v32\n" \
"s_nop 2\ns_cbranch_vccz 6f\n7:\ns_add_u32 s30, s30, 1\n" \
"s_cmp_lt_u32 s30, 0x40000\ns_cbranch_scc0 9f\ns_sleep 8\nbuffer_load_dword v33, v32, s[8:11], s26 offen sc1\n" \
"s_waitcnt vmcnt(0)\nv_cmp_eq_u32 vcc, -1, v33\ns_nop 4\ns_cbranch_vccnz 7b\n" \
"s_add_u32 m0, s22, 0\ns_add_u32 s43, s21, 0\nbuffer_load_dwordx4 v16, s[8:11], s43 offen offset:0 sc1 lds\nbuffer_load_dwordx4 v16, s[8:11], s43 offen offset:1024 sc1 lds\n" \
"buffer_load_dwordx4 v16, s[8:11], s43 offen offset:2048 sc1 lds\nbuffer_load_dwordx4 v16, s[8:11], s43 offen offset:3072 sc1 lds\ns_add_u32 m0, s22, 0x2000\ns_add_u32 s43, s21, 0x2000\n" \
"buffer_load_dwordx4 v16, s[8:11], s43 offen offset:0 sc1 lds\nbuffer_load_dwordx4 v16, s[8:11], s43 offen offset:1024 sc1 lds\nbuffer_load_dwordx4 v16, s[8:11], s43 offen offset:2048 sc1 lds\nbuffer_load_dwordx4 v16, s[8:11], s43 offen offset:3072 sc1 lds\n" \
"s_add_u32 m0, s22, 0x4000\ns_add_u32 s43, s21, 0x4000\nbuffer_load_dwordx4 v16, s[8:11], s43 offen offset:0 sc1 lds\nbuffer_load_dwordx4 v16, s[8:11], s43 offen offset:1024 sc1 lds\n" \
"buffer_load_dwordx4 v16, s[8:11], s43 offen offset:2048 sc1 lds\nbuffer_load_dwordx4 v16, s[8:11], s43 offen offset:3072 sc1 lds\n6:\ns_waitcnt vmcnt(0)\n" \
"s_barrier\nds_read_b128 v[48:51], v16 offset:0\nds_read_b128 v[64:67], v16 offset:32768\nds_read_b128 v[76:79], v16 offset:1024\n" \
"ds_read_b128 v[80:83], v16 offset:33792\nds_read_b128 v[84:87], v16 offset:2048\nds_read_b128 v[88:91], v16 offset:34816\nds_read_b128 v[92:95], v16 offset:3072\n" \
"ds_read_b128 v[96:99], v16 offset:35840\nds_read_b128 v[28:31], v16 offset:4096\nds_read_b128 v[40:43], v16 offset:36864\nds_read_b128 v[36:39], v16 offset:5120\n" \
"ds_read_b128 v[32:35], v16 offset:37888\nds_read_b128 v[52:55], v16 offset:6144\nds_read_b128 v[24:27], v16 offset:38912\nds_read_b128 v[56:59], v16 offset:7168\n" \
"ds_read_b128 v[60:63], v16 offset:39936\ns_mov_b32 s20, -1\ns_add_u32 s21, s21, 0x10000\ns_mov_b32 s14, s10\n" \
"s_waitcnt lgkmcnt(0)\nv_mfma_f32_16x16x32_bf16 v[44:47], v[0:3], v[48:51], 0\nv_mfma_f32_16x16x32_bf16 v[68:71], v[0:3], v[64:67], 0\nv_mfma_f32_16x16x32_bf16 v[72:75], v[8:11], v[48:51], 0\n" \
"v_mfma_f32_16x16x32_bf16 v[20:23], v[8:11], v[64:67], 0\nv_mfma_f32_16x16x32_bf16 v[44:47], v[4:7], v[76:79], v[44:47]\nv_mfma_f32_16x16x32_bf16 v[68:71], v[4:7], v[80:83], v[68:71]\nv_mfma_f32_16x16x32_bf16 v[72:75], v[12:15], v[76:79], v[72:75]\n" \
"v_mfma_f32_16x16x32_bf16 v[20:23], v[12:15], v[80:83], v[20:23]\nv_mfma_f32_16x16x32_bf16 v[44:47], a[0:3], v[84:87], v[44:47]\nv_mfma_f32_16x16x32_bf16 v[68:71], a[0:3], v[88:91], v[68:71]\nv_mfma_f32_16x16x32_bf16 v[72:75], a[104:107], v[84:87], v[72:75]\n" \
"v_mfma_f32_16x16x32_bf16 v[20:23], a[104:107], v[88:91], v[20:23]\nv_mfma_f32_16x16x32_bf16 v[44:47], a[4:7], v[92:95], v[44:47]\nv_mfma_f32_16x16x32_bf16 v[68:71], a[4:7], v[96:99], v[68:71]\nv_mfma_f32_16x16x32_bf16 v[72:75], a[108:111], v[92:95], v[72:75]\n" \
"v_mfma_f32_16x16x32_bf16 v[20:23], a[108:111], v[96:99], v[20:23]\ns_branch 1f\n.p2align 6\n1:\n" \
"s_waitcnt lgkmcnt(4)\nv_mfma_f32_16x16x32_bf16 v[44:47], a[8:11], v[28:31], v[44:47]\ns_add_u32 m0, s22, 0\ns_add_u32 s43, s20, 7\n" \
"s_and_b32 s43, s43, 7\nv_mfma_f32_16x16x32_bf16 v[68:71], a[8:11], v[40:43], v[68:71]\ns_lshl_b32 s43, s43, 18\ns_add_u32 s43, s43, s23\n" \
"v_mfma_f32_16x16x32_bf16 v[72:75], a[112:115], v[28:31], v[72:75]\nds_read_b128 v[48:51], v17 offset:0\nv_mfma_f32_16x16x32_bf16 v[20:23], a[112:115], v[40:43], v[20:23]\nv_mfma_f32_16x16x32_bf16 v[44:47], a[12:15], v[36:39], v[44:47]\n" \
"s_waitcnt vmcnt(7) lgkmcnt(1)\ns_barrier\nv_mfma_f32_16x16x32_bf16 v[68:71], a[12:15], v[32:35], v[68:71]\nds_read_b128 v[28:31], v16 offset:8192\n" \
"v_mfma_f32_16x16x32_bf16 v[72:75], a[116:119], v[36:39], v[72:75]\nds_read_b128 v[40:43], v16 offset:40960\nv_mfma_f32_16x16x32_bf16 v[20:23], a[116:119], v[32:35], v[20:23]\nds_read_b128 v[36:39], v16 offset:9216\n" \
"v_mfma_f32_16x16x32_bf16 v[44:47], a[16:19], v[52:55], v[44:47]\nds_read_b128 v[32:35], v17 offset:1024\nv_mfma_f32_16x16x32_bf16 v[68:71], a[16:19], v[24:27], v[68:71]\nbuffer_load_dwordx4 v16, s[8:11], s43 offen offset:0 sc1 lds\n" \
"v_mfma_f32_16x16x32_bf16 v[72:75], a[120:123], v[52:55], v[72:75]\nds_read_b128 v[64:67], v16 offset:41984\nv_mfma_f32_16x16x32_bf16 v[20:23], a[120:123], v[24:27], v[20:23]\nds_read_b128 v[52:55], v16 offset:10240\n" \
"v_mfma_f32_16x16x32_bf16 v[44:47], a[20:23], v[56:59], v[44:47]\nbuffer_load_dwordx4 v16, s[8:11], s43 offen offset:1024 sc1 lds\nv_mfma_f32_16x16x32_bf16 v[68:71], a[20:23], v[60:63], v[68:71]\nds_read_b128 v[24:27], v16 offset:43008\n" \
"v_mfma_f32_16x16x32_bf16 v[72:75], a[124:127], v[56:59], v[72:75]\ns_waitcnt lgkmcnt(3)\nds_read_b128 v[56:59], v17 offset:4096\nv_mfma_f32_16x16x32_bf16 v[20:23], a[124:127], v[60:63], v[20:23]\n" \
"buffer_load_dwordx4 v16, s[8:11], s43 offen offset:2048 sc1 lds\nv_mfma_f32_16x16x32_bf16 v[44:47], v[48:51], v[28:31], v[44:47]\nds_read_b128 v[60:63], v16 offset:11264\nv_mfma_f32_16x16x32_bf16 v[68:71], v[48:51], v[40:43], v[68:71]\n" \
"ds_read_b128 v[76:79], v16 offset:44032\nv_mfma_f32_16x16x32_bf16 v[72:75], a[128:131], v[28:31], v[72:75]\nbuffer_load_dwordx4 v16, s[8:11], s43 offen offset:3072 sc1 lds\nv_mfma_f32_16x16x32_bf16 v[20:23], a[128:131], v[40:43], v[20:23]\n" \
"s_waitcnt lgkmcnt(3)\nds_read_b128 v[28:31], v17 offset:5120\nv_mfma_f32_16x16x32_bf16 v[44:47], v[32:35], v[36:39], v[44:47]\nds_read_b128 v[40:43], v16 offset:12288\n" \
"v_mfma_f32_16x16x32_bf16 v[68:71], v[32:35], v[64:67], v[68:71]\ns_add_u32 m0, s22, 0x6000\nds_read_b128 v[32:35], v16 offset:45056\nv_mfma_f32_16x16x32_bf16 v[72:75], a[132:135], v[36:39], v[72:75]\n" \
"s_add_u32 s42, s21, -0xa000\nv_mfma_f32_16x16x32_bf16 v[20:23], a[132:135], v[64:67], v[20:23]\nds_read_b128 v[36:39], v16 offset:13312\nv_mfma_f32_16x16x32_bf16 v[44:47], a[24:27], v[52:55], v[44:47]\n" \
"s_waitcnt lgkmcnt(4)\nds_read_b128 v[48:51], v16 offset:46080\nv_mfma_f32_16x16x32_bf16 v[68:71], a[24:27], v[24:27], v[68:71]\nbuffer_load_dwordx4 v16, s[12:15], s42 offen offset:0 sc1 lds\n" \
"v_mfma_f32_16x16x32_bf16 v[72:75], v[56:59], v[52:55], v[72:75]\nv_mfma_f32_16x16x32_bf16 v[20:23], v[56:59], v[24:27], v[20:23]\nds_read_b128 v[52:55], v16 offset:14336\nv_mfma_f32_16x16x32_bf16 v[44:47], a[28:31], v[60:63], v[44:47]\n" \
"s_waitcnt lgkmcnt(3)\nbuffer_load_dwordx4 v16, s[12:15], s42 offen offset:1024 sc1 lds\nv_mfma_f32_16x16x32_bf16 v[68:71], a[28:31], v[76:79], v[68:71]\nds_read_b128 v[24:27], v16 offset:47104\n" \
"v_mfma_f32_16x16x32_bf16 v[72:75], v[28:31], v[60:63], v[72:75]\nds_read_b128 v[56:59], v17 offset:6144\nv_mfma_f32_16x16x32_bf16 v[20:23], v[28:31], v[76:79], v[20:23]\nbuffer_load_dwordx4 v16, s[12:15], s42 offen offset:2048 sc1 lds\n" \
"v_mfma_f32_16x16x32_bf16 v[44:47], a[32:35], v[40:43], v[44:47]\nds_read_b128 v[28:31], v16 offset:15360\nv_lshlrev_b32 v60, 5, v16\nv_mfma_f32_16x16x32_bf16 v[68:71], a[32:35], v[32:35], v[68:71]\n" \
"ds_read_b128 v[62:65], v16 offset:48128\nv_and_b32 v61, 0x7000, v60\nv_mfma_f32_16x16x32_bf16 v[72:75], a[136:139], v[40:43], v[72:75]\ns_waitcnt lgkmcnt(4)\n" \
"buffer_load_dwordx4 v16, s[12:15], s42 offen offset:3072 sc1 lds\nv_mfma_f32_16x16x32_bf16 v[20:23], a[136:139], v[32:35], v[20:23]\nds_read_b128 v[40:43], v17 offset:7168\ns_add_u32 m0, s22, 0\n" \
"v_mfma_f32_16x16x32_bf16 v[44:47], a[36:39], v[36:39], v[44:47]\nds_read_b128 v[32:35], v16 offset:16384\nv_lshl_add_u32 v60, v61, 17, v60\nv_mfma_f32_16x16x32_bf16 v[68:71], a[36:39], v[48:51], v[68:71]\n" \
"ds_read_b128 v[76:79], v17 offset:2048\nv_mfma_f32_16x16x32_bf16 v[72:75], a[140:143], v[36:39], v[72:75]\nds_read_b128 v[80:83], v16 offset:49152\nv_mfma_f32_16x16x32_bf16 v[20:23], a[140:143], v[48:51], v[20:23]\n" \
"s_waitcnt lgkmcnt(4)\nds_read_b128 v[36:39], v16 offset:17408\nv_mfma_f32_16x16x32_bf16 v[44:47], a[40:43], v[52:55], v[44:47]\nds_read_b128 v[48:51], v17 offset:3072\n" \
"v_mfma_f32_16x16x32_bf16 v[68:71], a[40:43], v[24:27], v[68:71]\nds_read_b128 v[84:87], v16 offset:50176\nv_mfma_f32_16x16x32_bf16 v[72:75], v[56:59], v[52:55], v[72:75]\nv_mfma_f32_16x16x32_bf16 v[20:23], v[56:59], v[24:27], v[20:23]\n" \
"ds_read_b128 v[52:55], v16 offset:18432\nv_mfma_f32_16x16x32_bf16 v[44:47], a[44:47], v[28:31], v[44:47]\ns_waitcnt lgkmcnt(4)\nds_read_b128 v[24:27], v16 offset:51200\n" \
"v_mfma_f32_16x16x32_bf16 v[68:71], a[44:47], v[62:65], v[68:71]\nds_read_b128 v[56:59], v17 offset:8192\nv_add_u32 v61, s22, v16\nv_mfma_f32_16x16x32_bf16 v[72:75], v[40:43], v[28:31], v[72:75]\n" \
"v_mfma_f32_16x16x32_bf16 v[20:23], v[40:43], v[62:65], v[20:23]\nds_read_b128 v[28:31], v16 offset:19456\nv_mfma_f32_16x16x32_bf16 v[44:47], v[76:79], v[32:35], v[44:47]\nds_read_b128 v[40:43], v16 offset:52224\n" \
"v_mfma_f32_16x16x32_bf16 v[68:71], v[76:79], v[80:83], v[68:71]\nds_read_b128 v[62:65], v17 offset:9216\nv_mfma_f32_16x16x32_bf16 v[72:75], a[144:147], v[32:35], v[72:75]\ns_waitcnt lgkmcnt(5)\n" \
"ds_read_b128 v[92:95], v17 offset:9216\nv_mfma_f32_16x16x32_bf16 v[20:23], a[144:147], v[80:83], v[20:23]\nds_read_b128 v[32:35], v16 offset:20480\nv_mfma_f32_16x16x32_bf16 v[44:47], v[48:51], v[36:39], v[44:47]\n" \
"ds_read_b128 v[76:79], v16 offset:53248\nv_mfma_f32_16x16x32_bf16 v[68:71], v[48:51], v[84:87], v[68:71]\nds_read_b128 v[88:91], v17 offset:8192\nv_mfma_f32_16x16x32_bf16 v[72:75], a[148:151], v[36:39], v[72:75]\n" \
"s_waitcnt vmcnt(4)\nds_read_b128 v[36:39], v61 offset:0\nds_read_b128 v[80:83], v61 offset:1024\nds_read_b128 v[48:51], v61 offset:2048\n" \
"v_mfma_f32_16x16x32_bf16 v[20:23], a[148:151], v[84:87], v[20:23]\ns_waitcnt lgkmcnt(7)\nds_read_b128 v[84:87], v61 offset:3072\nv_mfma_f32_16x16x32_bf16 v[44:47], a[48:51], v[52:55], v[44:47]\n" \
"ds_read_b128 v[96:99], v17 offset:4096\nv_mfma_f32_16x16x32_bf16 v[68:71], a[48:51], v[24:27], v[68:71]\nds_read_b128 v[100:103], v17 offset:5120\nv_mov_b64 v[104:105], 0\n" \
"v_mfma_f32_16x16x32_bf16 v[72:75], v[56:59], v[52:55], v[72:75]\ns_waitcnt lgkmcnt(3)\nv_max3_u32 v52, v36, v80, v48\nv_mov_b64 v[106:107], 0\n" \
"v_mfma_f32_16x16x32_bf16 v[20:23], v[56:59], v[24:27], v[20:23]\ns_waitcnt lgkmcnt(2)\nv_max_u32 v52, v52, v84\nv_cmp_eq_u32 vcc, -1, v52\n" \
"v_mfma_f32_16x16x32_bf16 v[44:47], a[52:55], v[28:31], v[44:47]\nv_mov_b64 v[24:25], 0\nv_mov_b64 v[26:27], 0\nv_mfma_f32_16x16x32_bf16 v[68:71], a[52:55], v[40:43], v[68:71]\n" \
"s_cbranch_vccnz 2f\n3:\nv_mfma_f32_16x16x32_bf16 v[72:75], v[62:65], v[28:31], v[72:75]\ns_barrier\n" \
"ds_read_b128 v[28:31], v16 offset:2048\nv_mfma_f32_16x16x32_bf16 v[20:23], v[92:95], v[40:43], v[20:23]\ns_memrealtime s[40:41]\nds_read_b128 v[36:39], v16 offset:5120\n" \
"v_mfma_f32_16x16x32_bf16 v[44:47], a[56:59], v[32:35], v[44:47]\nds_read_b128 v[40:43], v16 offset:1024\ns_lshl_b32 s43, s20, 24\nv_mfma_f32_16x16x32_bf16 v[68:71], a[56:59], v[76:79], v[68:71]\n" \
"ds_read_b128 v[48:51], v16 offset:33792\nv_mfma_f32_16x16x32_bf16 v[72:75], a[152:155], v[32:35], v[72:75]\nv_mfma_f32_16x16x32_bf16 v[20:23], a[152:155], v[76:79], v[20:23]\ns_waitcnt lgkmcnt(3)\n" \
"ds_read_b128 v[32:35], v16 offset:3072\nv_smfmac_f32_16x16x64_bf16 v[104:107], v[28:31], a[8:15], v18\ns_waitcnt lgkmcnt(3)\nv_smfmac_f32_16x16x64_bf16 v[24:27], v[28:31], a[112:119], v18\n" \
"ds_read_b128 v[52:55], v16 offset:34816\nv_smfmac_f32_16x16x64_bf16 v[104:107], v[36:39], a[24:31], v18\ns_waitcnt lgkmcnt(3)\nds_read_b128 v[76:79], v17 offset:6144\n" \
"v_smfmac_f32_16x16x64_bf16 v[24:27], v[36:39], v[96:103], v18\nds_read_b128 v[28:31], v16 offset:36864\nv_smfmac_f32_16x16x64_bf16 v[104:107], v[40:43], a[0:7], v18\ns_waitcnt lgkmcnt(4)\n" \
"ds_read_b128 v[80:83], v17 offset:7168\nv_smfmac_f32_16x16x64_bf16 v[24:27], v[40:43], a[104:111], v18\nds_read_b128 v[36:39], v16 offset:6144\ns_add_u32 s42, s40, 0x67\n" \
"v_smfmac_f32_16x16x64_bf16 v[104:107], v[48:51], a[48:55], v18\ns_and_b32 s42, s42, 0xffffff\ns_or_b32 s42, s42, s43\nv_mov_b32 v40, s42\n" \
"v_smfmac_f32_16x16x64_bf16 v[24:27], v[48:51], v[88:95], v18\ns_waitcnt lgkmcnt(4)\nds_read_b128 v[48:51], v16 offset:7168\nv_smfmac_f32_16x16x64_bf16 v[104:107], v[32:35], a[16:23], v18\n" \
"buffer_store_dword v40, v60, s[8:11], s27 offen offset:0 sc1\nv_smfmac_f32_16x16x64_bf16 v[24:27], v[32:35], a[120:127], v18\nds_read_b128 v[56:59], v16 offset:35840\nv_smfmac_f32_16x16x64_bf16 v[104:107], v[52:55], a[56:63], v18\n" \
"s_waitcnt lgkmcnt(4)\nv_smfmac_f32_16x16x64_bf16 v[24:27], v[52:55], a[152:159], v18\nds_read_b128 v[32:35], v16 offset:0\ns_add_u32 s16, s20, 1\n" \
"v_smfmac_f32_16x16x64_bf16 v[104:107], v[28:31], a[72:79], v18\nds_read_b128 v[60:63], v17 offset:0\nv_mov_b64 v[40:41], 0\nv_smfmac_f32_16x16x64_bf16 v[24:27], v[28:31], a[168:175], v18\n" \
"s_waitcnt lgkmcnt(4)\nds_read_b128 v[28:31], v16 offset:39936\nv_smfmac_f32_16x16x64_bf16 v[104:107], v[36:39], a[32:39], v18\nds_read_b128 v[64:67], v17 offset:1024\n" \
"v_mov_b64 v[42:43], 0\nv_smfmac_f32_16x16x64_bf16 v[24:27], v[36:39], a[136:143], v18\ns_waitcnt lgkmcnt(4)\nds_read_b128 v[36:39], v16 offset:37888\n" \
"v_smfmac_f32_16x16x64_bf16 v[104:107], v[48:51], a[40:47], v18\nds_read_b128 v[84:87], v17 offset:2048\ns_and_b32 s16, s16, 7\nv_smfmac_f32_16x16x64_bf16 v[24:27], v[48:51], v[76:83], v18\n" \
"ds_read_b128 v[52:55], v16 offset:4096\ns_lshl_b32 s16, s16, 22\nv_smfmac_f32_16x16x64_bf16 v[104:107], v[56:59], a[64:71], v18\nds_read_b128 v[88:91], v17 offset:3072\n" \
"s_add_u32 s16, s16, s25\nv_smfmac_f32_16x16x64_bf16 v[24:27], v[56:59], a[160:167], v18\ns_waitcnt lgkmcnt(6)\nds_read_b128 v[48:51], v16 offset:38912\n" \
"v_smfmac_f32_16x16x64_bf16 v[104:107], v[32:35], v[0:7], v18\nds_read_b128 v[56:59], v16 offset:21504\ns_add_u32 s17, s16, 0x1000\nv_smfmac_f32_16x16x64_bf16 v[24:27], v[32:35], v[8:15], v18\n" \
"s_waitcnt lgkmcnt(6)\nds_read_b128 v[32:35], v16 offset:32768\nv_smfmac_f32_16x16x64_bf16 v[104:107], v[28:31], a[96:103], v18\nds_read_b128 v[76:79], v16 offset:54272\n" \
"s_add_u32 s18, s16, 0x4000\nv_smfmac_f32_16x16x64_bf16 v[24:27], v[28:31], a[192:199], v18\ns_waitcnt lgkmcnt(5)\nds_read_b128 v[28:31], v16 offset:22528\n" \
"v_smfmac_f32_16x16x64_bf16 v[104:107], v[36:39], a[80:87], v18\nds_read_b128 v[80:83], v16 offset:55296\nv_lshrrev_b32 v92, 16, v18\nv_smfmac_f32_16x16x64_bf16 v[24:27], v[36:39], a[176:183], v18\n" \
"ds_write_b128 v16, v[40:43] offset:45056\nv_smfmac_f32_16x16x64_bf16 v[104:107], v[52:55], v[60:67], v18\ns_add_u32 s19, s16, 0x5000\ns_and_b32 s43, s20, 2\n" \
"s_lshl_b32 s43, s43, 12\nv_smfmac_f32_16x16x64_bf16 v[24:27], v[52:55], a[128:135], v18\ns_waitcnt lgkmcnt(4)\nds_read_b128 v[36:39], v16 offset:23552\n" \
"v_smfmac_f32_16x16x64_bf16 v[104:107], v[48:51], a[88:95], v18\nds_read_b128 v[52:55], v16 offset:56320\ns_and_b32 s42, s20, 7\nv_smfmac_f32_16x16x64_bf16 v[24:27], v[48:51], a[184:191], v18\n" \
"ds_read_b128 v[60:63], v17 offset:10240\ns_lshl_b32 s42, s42, 20\nv_smfmac_f32_16x16x64_bf16 v[104:107], v[32:35], v[84:91], v18\nds_read_b128 v[64:67], v17 offset:11264\n" \
"s_add_u32 s42, s42, s24\nv_smfmac_f32_16x16x64_bf16 v[24:27], v[32:35], a[144:151], v18\ns_waitcnt lgkmcnt(4)\nv_mfma_f32_16x16x32_bf16 v[44:47], a[60:63], v[56:59], v[44:47]\n" \
"v_add_f32 v104, v104, v106\nv_add_f32 v105, v105, v107\nv_cvt_pk_bf16_f32 v104, v104, v105\nv_mfma_f32_16x16x32_bf16 v[68:71], a[60:63], v[76:79], v[68:71]\n" \
"v_add_f32 v24, v24, v26\nv_add_f32 v25, v25, v27\nv_cvt_pk_bf16_f32 v24, v24, v25\nv_mfma_f32_16x16x32_bf16 v[72:75], a[156:159], v[56:59], v[72:75]\n" \
"ds_write2_b32 v92, v104, v24 offset0:0 offset1:4\nv_mfma_f32_16x16x32_bf16 v[20:23], a[156:159], v[76:79], v[20:23]\ns_add_u32 s99, s20, 2\nv_mov_b64 v[24:25], 0\n" \
"v_mov_b64 v[26:27], 0\nv_mfma_f32_16x16x32_bf16 v[44:47], a[64:67], v[28:31], v[44:47]\ns_min_u32 s99, s99, 63\nv_bfe_u32 v32, v16, 4, 3\n" \
"v_lshlrev_b32 v32, 2, v32\nv_mfma_f32_16x16x32_bf16 v[68:71], a[64:67], v[80:83], v[68:71]\ns_waitcnt vmcnt(1) lgkmcnt(0)\ns_barrier\n" \
"ds_read_b64_tr_b16 v[48:49], v19 offset:0\nv_mfma_f32_16x16x32_bf16 v[72:75], a[160:163], v[28:31], v[72:75]\nds_read_b64_tr_b16 v[50:51], v19 offset:256\nds_read_b64_tr_b16 v[28:29], v19 offset:1024\n" \
"ds_read_b64_tr_b16 v[30:31], v19 offset:1280\nv_mfma_f32_16x16x32_bf16 v[20:23], a[160:163], v[80:83], v[20:23]\nds_read_b128 v[56:59], v16 offset:24576\nv_mov_b64 v[76:77], 0\n" \
"v_mfma_f32_16x16x32_bf16 v[44:47], a[68:71], v[36:39], v[44:47]\ns_lshl_b32 s99, s99, 5\ns_add_u32 s99, s99, s26\nv_mov_b64 v[78:79], 0\n" \
"v_mfma_f32_16x16x32_bf16 v[68:71], a[68:71], v[52:55], v[68:71]\ns_waitcnt lgkmcnt(3)\nbuffer_load_dword v33, v32, s[8:11], s99 offen sc1\nv_mfma_f32_16x16x32_bf16 v[72:75], a[164:167], v[36:39], v[72:75]\n" \
"ds_read_b128 v[80:83], v16 offset:25600\nv_mov_b64 v[34:35], 0\nv_smfmac_f32_16x16x64_bf16 v[40:43], v[48:51], a[200:207], v18\ns_waitcnt lgkmcnt(2)\n" \
"ds_read_b128 v[88:91], v17 offset:12288\nv_smfmac_f32_16x16x64_bf16 v[40:43], v[28:31], v[60:67], v18\nds_read_b128 v[84:87], v16 offset:45056\nv_mov_b64 v[36:37], 0\n" \
"v_smfmac_f32_16x16x64_bf16 v[24:27], v[48:51], a[224:231], v18\nds_read_b128 v[92:95], v17 offset:13312\nv_mfma_f32_16x16x32_bf16 v[20:23], a[164:167], v[52:55], v[20:23]\nds_read_b128 v[60:63], v17 offset:14336\n" \
"v_add_f32 v40, v40, v42\nv_smfmac_f32_16x16x64_bf16 v[24:27], v[28:31], a[232:239], v18\ns_waitcnt lgkmcnt(5)\nds_read_b128 v[52:55], v16 offset:45056\n" \
"v_mfma_f32_16x16x32_bf16 v[44:47], a[72:75], v[56:59], v[44:47]\nds_read_b128 v[64:67], v17 offset:15360\nv_add_f32 v41, v41, v43\nv_smfmac_f32_16x16x64_bf16 v[76:79], v[48:51], a[208:215], v18\n" \
"ds_read_b128 v[96:99], v16 offset:26624\nv_add_f32 v24, v24, v26\nv_smfmac_f32_16x16x64_bf16 v[76:79], v[28:31], a[216:223], v18\nds_read_b128 v[100:103], v16 offset:45056\n" \
"v_add_f32 v25, v25, v27\nv_smfmac_f32_16x16x64_bf16 v[34:37], v[48:51], a[240:247], v18\nds_read_b128 v[104:107], v17 offset:16384\nv_cvt_pk_bf16_f32 v38, v40, v41\n" \
"v_mfma_f32_16x16x32_bf16 v[72:75], a[168:171], v[56:59], v[72:75]\ns_waitcnt lgkmcnt(5)\nv_add_f32 v76, v76, v78\nv_add_f32 v77, v77, v79\n" \
"v_smfmac_f32_16x16x64_bf16 v[34:37], v[28:31], a[248:255], v18\nds_read_b128 v[56:59], v16 offset:45056\nv_cvt_pk_bf16_f32 v39, v24, v25\nv_smfmac_f32_16x16x64_bf16 v[84:87], v[48:51], v[112:119], v18\n" \
"ds_read_b128 v[108:111], v17 offset:17408\nv_cvt_pk_bf16_f32 v40, v76, v77\nv_mfma_f32_16x16x32_bf16 v[44:47], a[76:79], v[80:83], v[44:47]\ns_waitcnt lgkmcnt(4)\n" \
"v_permlane32_swap_b32 v38, v39\nv_smfmac_f32_16x16x64_bf16 v[84:87], v[28:31], v[120:127], v18\nds_read_b128 v[24:27], v16 offset:45056\nv_add_f32 v34, v34, v36\n" \
"v_smfmac_f32_16x16x64_bf16 v[52:55], v[48:51], v[128:135], v18\nds_read_b128 v[76:79], v16 offset:27648\nv_add_f32 v35, v35, v37\nv_cvt_pk_bf16_f32 v41, v34, v35\n" \
"v_smfmac_f32_16x16x64_bf16 v[52:55], v[28:31], v[136:143], v18\nds_read_b128 v[34:37], v17 offset:18432\nv_permlane32_swap_b32 v40, v41\nv_mfma_f32_16x16x32_bf16 v[72:75], a[172:175], v[80:83], v[72:75]\n" \
"s_waitcnt lgkmcnt(4)\nv_permlane16_swap_b32 v38, v40\nv_smfmac_f32_16x16x64_bf16 v[100:103], v[48:51], v[88:95], v18\nds_read_b128 v[80:83], v16 offset:45056\n" \
"v_permlane16_swap_b32 v39, v41\nv_and_or_b32 v38, v38, -2, s32\nv_smfmac_f32_16x16x64_bf16 v[100:103], v[28:31], v[60:67], v18\nds_read_b128 v[88:91], v16 offset:28672\n" \
"v_add_f32 v84, v84, v86\nv_mfma_f32_16x16x32_bf16 v[44:47], a[80:83], v[96:99], v[44:47]\nbuffer_store_dwordx4 v[38:41], v16, s[8:11], s29 offen offset:0 sc1\nv_add_f32 v85, v85, v87\n" \
"v_add_f32 v52, v52, v54\nv_add_f32 v53, v53, v55\nv_smfmac_f32_16x16x64_bf16 v[56:59], v[48:51], v[144:151], v18\nds_read_b128 v[60:63], v16 offset:45056\n" \
"v_add_f32 v100, v100, v102\nv_smfmac_f32_16x16x64_bf16 v[56:59], v[28:31], v[152:159], v18\nds_read_b128 v[38:41], v17 offset:19456\nv_add_f32 v101, v101, v103\n" \
"v_mfma_f32_16x16x32_bf16 v[72:75], a[176:179], v[96:99], v[72:75]\ns_waitcnt lgkmcnt(3)\nv_cvt_pk_bf16_f32 v64, v84, v85\nv_cvt_pk_bf16_f32 v65, v52, v53\n" \
"v_smfmac_f32_16x16x64_bf16 v[24:27], v[48:51], v[160:167], v18\nds_read_b128 v[52:55], v16 offset:45056\nv_add_f32 v56, v56, v58\nv_smfmac_f32_16x16x64_bf16 v[24:27], v[28:31], v[168:175], v18\n" \
"ds_read_b128 v[84:87], v16 offset:45056\nv_add_f32 v57, v57, v59\nv_mfma_f32_16x16x32_bf16 v[44:47], a[84:87], v[76:79], v[44:47]\nv_permlane32_swap_b32 v64, v65\n" \
"v_cvt_pk_bf16_f32 v66, v100, v101\nv_cvt_pk_bf16_f32 v67, v56, v57\nv_smfmac_f32_16x16x64_bf16 v[80:83], v[48:51], v[184:191], v18\nds_read_b128 v[56:59], v16 offset:29696\n" \
"v_permlane32_swap_b32 v66, v67\nv_smfmac_f32_16x16x64_bf16 v[80:83], v[28:31], v[192:199], v18\ns_waitcnt lgkmcnt(3)\nv_permlane16_swap_b32 v64, v66\n" \
"v_mfma_f32_16x16x32_bf16 v[72:75], a[180:183], v[76:79], v[72:75]\nv_permlane16_swap_b32 v65, v67\nv_and_or_b32 v64, v64, -2, s32\nv_smfmac_f32_16x16x64_bf16 v[60:63], v[48:51], v[104:111], v18\n" \
"ds_read_b128 v[76:79], v16 offset:45056\nv_add_f32 v24, v24, v26\nv_smfmac_f32_16x16x64_bf16 v[60:63], v[28:31], v[176:183], v18\ns_waitcnt lgkmcnt(2)\n" \
"buffer_store_dwordx4 v[64:67], v16, s[8:11], s29 offen offset:1024 sc1\nds_read_b128 v[92:95], v17 offset:20480\nv_add_f32 v25, v25, v27\nv_mfma_f32_16x16x32_bf16 v[44:47], a[88:91], v[88:91], v[44:47]\n" \
"ds_read_b128 v[96:99], v17 offset:21504\nv_add_f32 v80, v80, v82\nv_smfmac_f32_16x16x64_bf16 v[52:55], v[48:51], v[200:207], v18\nds_read_b128 v[64:67], v16 offset:45056\n" \
"v_add_f32 v81, v81, v83\nv_smfmac_f32_16x16x64_bf16 v[52:55], v[28:31], v[34:41], v18\nds_read_b128 v[100:103], v16 offset:30720\nv_add_f32 v60, v60, v62\n" \
"v_smfmac_f32_16x16x64_bf16 v[84:87], v[48:51], v[208:215], v18\nds_read_b128 v[34:37], v16 offset:45056\nv_add_f32 v61, v61, v63\nv_mfma_f32_16x16x32_bf16 v[72:75], a[184:187], v[88:91], v[72:75]\n" \
"s_waitcnt lgkmcnt(5)\nv_add_f32 v52, v52, v54\nv_add_f32 v53, v53, v55\nv_smfmac_f32_16x16x64_bf16 v[84:87], v[28:31], v[216:223], v18\n" \
"ds_read_b128 v[104:107], v17 offset:22528\nv_cvt_pk_bf16_f32 v38, v24, v25\nv_mfma_f32_16x16x32_bf16 v[44:47], a[92:95], v[56:59], v[44:47]\nv_cvt_pk_bf16_f32 v39, v80, v81\n" \
"v_cvt_pk_bf16_f32 v40, v60, v61\nv_cvt_pk_bf16_f32 v41, v52, v53\nv_smfmac_f32_16x16x64_bf16 v[76:79], v[48:51], v[232:239], v18\nds_read_b128 v[108:111], v17 offset:23552\n" \
"v_permlane32_swap_b32 v38, v39\nv_permlane32_swap_b32 v40, v41\nv_smfmac_f32_16x16x64_bf16 v[76:79], v[28:31], v[240:247], v18\nds_read_b128 v[24:27], v16 offset:31744\n" \
"v_permlane16_swap_b32 v38, v40\nv_mfma_f32_16x16x32_bf16 v[72:75], a[188:191], v[56:59], v[72:75]\ns_waitcnt lgkmcnt(3)\nv_permlane16_swap_b32 v39, v41\n" \
"v_and_or_b32 v38, v38, -2, s32\nv_smfmac_f32_16x16x64_bf16 v[64:67], v[48:51], v[92:99], v18\nds_read_b128 v[52:55], v16 offset:57344\nv_add_f32 v84, v84, v86\n" \
"v_smfmac_f32_16x16x64_bf16 v[64:67], v[28:31], v[224:231], v18\nds_read_b128 v[56:59], v16 offset:58368\nv_add_f32 v85, v85, v87\nv_mfma_f32_16x16x32_bf16 v[44:47], a[96:99], v[100:103], v[44:47]\n" \
"s_waitcnt lgkmcnt(3)\nbuffer_store_dwordx4 v[38:41], v16, s[8:11], s29 offen offset:2048 sc1\nv_add_f32 v76, v76, v78\nv_add_f32 v77, v77, v79\n" \
"v_cvt_pk_bf16_f32 v38, v84, v85\nv_smfmac_f32_16x16x64_bf16 v[34:37], v[48:51], v[104:111], v18\nds_read_b128 v[60:63], v16 offset:59392\nv_add_f32 v64, v64, v66\n" \
"v_add_f32 v65, v65, v67\nv_cvt_pk_bf16_f32 v39, v76, v77\nv_cvt_pk_bf16_f32 v40, v64, v65\nv_smfmac_f32_16x16x64_bf16 v[34:37], v[28:31], v[248:255], v18\n" \
"ds_read_b128 v[48:51], v16 offset:60416\nv_permlane32_swap_b32 v38, v39\nv_mfma_f32_16x16x32_bf16 v[72:75], a[192:195], v[100:103], v[72:75]\ns_waitcnt lgkmcnt(3)\n" \
"ds_read_b128 v[28:31], v16 offset:61440\nv_mfma_f32_16x16x32_bf16 v[44:47], a[100:103], v[24:27], v[44:47]\nv_add_f32 v34, v34, v36\nv_add_f32 v35, v35, v37\n" \
"v_cvt_pk_bf16_f32 v41, v34, v35\nv_mfma_f32_16x16x32_bf16 v[72:75], a[196:199], v[24:27], v[72:75]\nds_read_b128 v[34:37], v16 offset:62464\nv_permlane32_swap_b32 v40, v41\n" \
"v_mfma_f32_16x16x32_bf16 v[68:71], a[72:75], v[52:55], v[68:71]\ns_waitcnt lgkmcnt(3)\nv_permlane16_swap_b32 v38, v40\nv_permlane16_swap_b32 v39, v41\n" \
"v_and_or_b32 v38, v38, -2, s32\nv_mfma_f32_16x16x32_bf16 v[20:23], a[168:171], v[52:55], v[20:23]\nbuffer_store_dwordx4 v[38:41], v16, s[8:11], s29 offen offset:3072 sc1\ns_bfe_u32 s32, s20, 0x10003\n" \
"v_cvt_pk_bf16_f32 v44, v44, v45\nv_cvt_pk_bf16_f32 v45, v46, v47\nv_mfma_f32_16x16x32_bf16 v[68:71], a[76:79], v[56:59], v[68:71]\ns_xor_b32 s29, s42, s43\n" \
"v_cvt_pk_bf16_f32 v46, v72, v73\nv_cvt_pk_bf16_f32 v47, v74, v75\nv_mfma_f32_16x16x32_bf16 v[20:23], a[172:175], v[56:59], v[20:23]\nds_read_b128 v[24:27], v16 offset:63488\n" \
"v_mfma_f32_16x16x32_bf16 v[68:71], a[80:83], v[60:63], v[68:71]\ns_waitcnt vmcnt(4) lgkmcnt(1)\nv_cmp_eq_u32 s[42:43], -1, v33\nv_mfma_f32_16x16x32_bf16 v[20:23], a[176:179], v[60:63], v[20:23]\n" \
"ds_read_b128 v[38:41], v16 offset:64512\nv_mfma_f32_16x16x32_bf16 v[68:71], a[84:87], v[48:51], v[68:71]\ns_cmp_eq_u64 s[42:43], 0\ns_cselect_b32 s14, s10, 0\n" \
"s_cmp_lt_i32 s20, 62\ns_cselect_b32 s14, s14, 0\ns_cmp_eq_u32 s14, 0\ns_cbranch_scc1 4f\n" \
"5:\nv_add_u32 v32, s28, v16\nv_mfma_f32_16x16x32_bf16 v[20:23], a[180:183], v[48:51], v[20:23]\nds_write_b128 v32, v[44:47] offset:0\n" \
"s_add_u32 m0, s22, 0\nv_mfma_f32_16x16x32_bf16 v[68:71], a[88:91], v[28:31], v[68:71]\nbuffer_load_dwordx4 v16, s[12:15], s21 offen offset:0 sc1 lds\nv_mfma_f32_16x16x32_bf16 v[20:23], a[184:187], v[28:31], v[20:23]\n" \
"ds_read_b128 v[42:45], v17 offset:10240\ns_add_u32 s20, s20, 1\nv_mfma_f32_16x16x32_bf16 v[68:71], a[92:95], v[34:37], v[68:71]\ns_waitcnt lgkmcnt(1)\n" \
"s_barrier\nds_read_b128 v[28:31], v16 offset:8192\nv_mfma_f32_16x16x32_bf16 v[20:23], a[188:191], v[34:37], v[20:23]\nds_read_b128 v[48:51], v16 offset:9216\n" \
"v_mfma_f32_16x16x32_bf16 v[68:71], a[96:99], v[24:27], v[68:71]\nds_read_b128 v[64:67], v16 offset:10240\nv_add_u32 v32, s28, v16\nv_mfma_f32_16x16x32_bf16 v[20:23], a[192:195], v[24:27], v[20:23]\n" \
"buffer_load_dwordx4 v16, s[12:15], s21 offen offset:1024 sc1 lds\nds_read_b128 v[72:75], v16 offset:11264\nds_read_b128 v[24:27], v17 offset:11264\nv_mfma_f32_16x16x32_bf16 v[68:71], a[100:103], v[38:41], v[68:71]\n" \
"s_waitcnt lgkmcnt(4)\nv_mfma_f32_16x16x32_bf16 v[20:23], a[196:199], v[38:41], v[20:23]\ns_waitcnt lgkmcnt(3)\nv_mfma_f32_16x16x32_bf16 v[34:37], v[28:31], a[200:203], 0\n" \
"s_waitcnt lgkmcnt(2)\nv_cvt_pk_bf16_f32 v68, v68, v69\nv_cvt_pk_bf16_f32 v69, v70, v71\nv_mfma_f32_16x16x32_bf16 v[34:37], v[48:51], a[204:207], v[34:37]\n" \
"s_waitcnt lgkmcnt(0)\nv_cvt_pk_bf16_f32 v70, v20, v21\nv_cvt_pk_bf16_f32 v71, v22, v23\nv_mfma_f32_16x16x32_bf16 v[34:37], v[64:67], v[42:45], v[34:37]\n" \
"buffer_load_dwordx4 v16, s[12:15], s21 offen offset:2048 sc1 lds\nv_mfma_f32_16x16x32_bf16 v[34:37], v[72:75], v[24:27], v[34:37]\nds_write_b128 v32, v[68:71] offset:32768\nv_mfma_f32_16x16x32_bf16 v[20:23], v[28:31], a[208:211], 0\n" \
"v_mfma_f32_16x16x32_bf16 v[20:23], v[48:51], a[212:215], v[20:23]\nv_cvt_pk_bf16_f32 v34, v34, v35\nv_cvt_pk_bf16_f32 v35, v36, v37\nv_mfma_f32_16x16x32_bf16 v[20:23], v[64:67], a[216:219], v[20:23]\n" \
"s_add_u32 s43, s21, 0x4000\nds_read_b128 v[24:27], v17 offset:18432\nv_mfma_f32_16x16x32_bf16 v[20:23], v[72:75], a[220:223], v[20:23]\ns_waitcnt lgkmcnt(1)\n" \
"s_barrier\nv_mfma_f32_16x16x32_bf16 v[38:41], v[28:31], a[224:227], 0\nbuffer_load_dwordx4 v16, s[12:15], s21 offen offset:3072 sc1 lds\nv_mfma_f32_16x16x32_bf16 v[38:41], v[48:51], a[228:231], v[38:41]\n" \
"s_add_u32 m0, s22, 0x4000\nds_read_b128 v[52:55], v17 offset:19456\nv_mfma_f32_16x16x32_bf16 v[38:41], v[64:67], a[232:235], v[38:41]\nbuffer_load_dwordx4 v16, s[12:15], s43 offen offset:0 sc1 lds\n" \
"v_mfma_f32_16x16x32_bf16 v[38:41], v[72:75], a[236:239], v[38:41]\nds_read_b128 v[68:71], v16 offset:40960\nv_cvt_pk_bf16_f32 v20, v20, v21\nv_mfma_f32_16x16x32_bf16 v[56:59], v[28:31], v[144:147], 0\n" \
"ds_read_b128 v[76:79], v16 offset:41984\nv_cvt_pk_bf16_f32 v21, v22, v23\nv_mfma_f32_16x16x32_bf16 v[56:59], v[48:51], v[148:151], v[56:59]\nds_read_b128 v[80:83], v16 offset:43008\n" \
"v_cvt_pk_bf16_f32 v38, v38, v39\nv_mfma_f32_16x16x32_bf16 v[56:59], v[64:67], v[152:155], v[56:59]\nds_read_b128 v[60:63], v17 offset:10240\nv_cvt_pk_bf16_f32 v39, v40, v41\n" \
"v_mfma_f32_16x16x32_bf16 v[56:59], v[72:75], v[156:159], v[56:59]\nbuffer_load_dwordx4 v16, s[12:15], s43 offen offset:1024 sc1 lds\nv_mfma_f32_16x16x32_bf16 v[84:87], v[28:31], v[200:203], 0\ns_waitcnt lgkmcnt(4)\n" \
"ds_read_b128 v[44:47], v16 offset:44032\nv_mfma_f32_16x16x32_bf16 v[84:87], v[48:51], v[204:207], v[84:87]\nds_read_b128 v[40:43], v17 offset:11264\nv_cvt_pk_bf16_f32 v56, v56, v57\n" \
"v_mfma_f32_16x16x32_bf16 v[84:87], v[64:67], v[24:27], v[84:87]\ns_waitcnt lgkmcnt(4)\nv_cvt_pk_bf16_f32 v57, v58, v59\nv_mfma_f32_16x16x32_bf16 v[84:87], v[72:75], v[52:55], v[84:87]\n" \
"v_mfma_f32_16x16x32_bf16 v[22:25], v[68:71], a[200:203], 0\ns_waitcnt lgkmcnt(2)\nbuffer_load_dwordx4 v16, s[12:15], s43 offen offset:2048 sc1 lds\nv_mfma_f32_16x16x32_bf16 v[22:25], v[76:79], a[204:207], v[22:25]\n" \
"v_cvt_pk_bf16_f32 v84, v84, v85\nv_cvt_pk_bf16_f32 v85, v86, v87\nv_mfma_f32_16x16x32_bf16 v[22:25], v[80:83], v[60:63], v[22:25]\ns_waitcnt lgkmcnt(0)\n" \
"v_mfma_f32_16x16x32_bf16 v[22:25], v[44:47], v[40:43], v[22:25]\nv_mfma_f32_16x16x32_bf16 v[52:55], v[68:71], a[208:211], 0\nbuffer_load_dwordx4 v16, s[12:15], s43 offen offset:3072 sc1 lds\nv_mfma_f32_16x16x32_bf16 v[52:55], v[76:79], a[212:215], v[52:55]\n" \
"v_cvt_pk_bf16_f32 v36, v22, v23\nv_cvt_pk_bf16_f32 v37, v24, v25\nv_mfma_f32_16x16x32_bf16 v[52:55], v[80:83], a[216:219], v[52:55]\nds_read_b128 v[24:27], v17 offset:18432\n" \
"v_mfma_f32_16x16x32_bf16 v[52:55], v[44:47], a[220:223], v[52:55]\nds_read_b128 v[60:63], v17 offset:19456\nv_mfma_f32_16x16x32_bf16 v[86:89], v[68:71], a[224:227], 0\nv_mfma_f32_16x16x32_bf16 v[86:89], v[76:79], a[228:231], v[86:89]\n" \
"buffer_store_dwordx4 v[34:37], v16, s[8:11], s16 offen offset:0 sc1\nv_cvt_pk_bf16_f32 v22, v52, v53\nv_cvt_pk_bf16_f32 v23, v54, v55\nv_mfma_f32_16x16x32_bf16 v[86:89], v[80:83], a[232:235], v[86:89]\n" \
"v_mfma_f32_16x16x32_bf16 v[86:89], v[44:47], a[236:239], v[86:89]\nv_mfma_f32_16x16x32_bf16 v[32:35], v[68:71], v[144:147], 0\nv_mfma_f32_16x16x32_bf16 v[32:35], v[76:79], v[148:151], v[32:35]\nv_cvt_pk_bf16_f32 v40, v86, v87\n" \
"v_cvt_pk_bf16_f32 v41, v88, v89\nv_mfma_f32_16x16x32_bf16 v[32:35], v[80:83], v[152:155], v[32:35]\nv_mfma_f32_16x16x32_bf16 v[32:35], v[44:47], v[156:159], v[32:35]\nv_mfma_f32_16x16x32_bf16 v[52:55], v[68:71], v[200:203], 0\n" \
"s_waitcnt lgkmcnt(0)\nbuffer_store_dwordx4 v[20:23], v16, s[8:11], s16 offen offset:1024 sc1\nv_mfma_f32_16x16x32_bf16 v[52:55], v[76:79], v[204:207], v[52:55]\nv_cvt_pk_bf16_f32 v58, v32, v33\n" \
"v_cvt_pk_bf16_f32 v59, v34, v35\nv_mfma_f32_16x16x32_bf16 v[52:55], v[80:83], v[24:27], v[52:55]\nv_mfma_f32_16x16x32_bf16 v[52:55], v[44:47], v[60:63], v[52:55]\nv_mfma_f32_16x16x32_bf16 v[20:23], v[28:31], a[240:243], 0\n" \
"v_mfma_f32_16x16x32_bf16 v[24:27], v[68:71], a[240:243], 0\nv_cvt_pk_bf16_f32 v86, v52, v53\nv_cvt_pk_bf16_f32 v87, v54, v55\nv_mfma_f32_16x16x32_bf16 v[20:23], v[48:51], a[244:247], v[20:23]\n" \
"v_mfma_f32_16x16x32_bf16 v[24:27], v[76:79], a[244:247], v[24:27]\nv_mfma_f32_16x16x32_bf16 v[20:23], v[64:67], a[248:251], v[20:23]\nbuffer_store_dwordx4 v[38:41], v16, s[8:11], s16 offen offset:2048 sc1\nds_read_b128 v[32:35], v17 offset:12288\n" \
"v_mfma_f32_16x16x32_bf16 v[24:27], v[80:83], a[248:251], v[24:27]\nv_mfma_f32_16x16x32_bf16 v[20:23], v[72:75], a[252:255], v[20:23]\nds_read_b128 v[36:39], v17 offset:13312\nv_mfma_f32_16x16x32_bf16 v[24:27], v[44:47], a[252:255], v[24:27]\n" \
"v_mfma_f32_16x16x32_bf16 v[40:43], v[28:31], v[128:131], 0\nds_read_b128 v[52:55], v17 offset:14336\nv_cvt_pk_bf16_f32 v20, v20, v21\nv_mfma_f32_16x16x32_bf16 v[60:63], v[68:71], v[128:131], 0\n" \
"v_cvt_pk_bf16_f32 v21, v22, v23\nv_cvt_pk_bf16_f32 v22, v24, v25\nv_mfma_f32_16x16x32_bf16 v[40:43], v[48:51], v[132:135], v[40:43]\ns_waitcnt vmcnt(7)\n" \
"s_barrier\nv_mfma_f32_16x16x32_bf16 v[60:63], v[76:79], v[132:135], v[60:63]\nbuffer_store_dwordx4 v[56:59], v16, s[8:11], s17 offen offset:3072 sc1\nds_read_b128 v[88:91], v17 offset:15360\n" \
"v_cvt_pk_bf16_f32 v23, v26, v27\nv_mfma_f32_16x16x32_bf16 v[40:43], v[64:67], v[136:139], v[40:43]\nv_mfma_f32_16x16x32_bf16 v[60:63], v[80:83], v[136:139], v[60:63]\nv_mfma_f32_16x16x32_bf16 v[40:43], v[72:75], v[140:143], v[40:43]\n" \
"s_waitcnt lgkmcnt(1)\nv_mfma_f32_16x16x32_bf16 v[60:63], v[44:47], v[140:143], v[60:63]\nv_mfma_f32_16x16x32_bf16 v[24:27], v[28:31], v[32:35], 0\nv_cvt_pk_bf16_f32 v40, v40, v41\n" \
"v_cvt_pk_bf16_f32 v41, v42, v43\nv_mfma_f32_16x16x32_bf16 v[56:59], v[68:71], v[32:35], 0\nv_cvt_pk_bf16_f32 v42, v60, v61\nv_cvt_pk_bf16_f32 v43, v62, v63\n" \
"v_mfma_f32_16x16x32_bf16 v[24:27], v[48:51], v[36:39], v[24:27]\nv_mfma_f32_16x16x32_bf16 v[56:59], v[76:79], v[36:39], v[56:59]\nbuffer_store_dwordx4 v[84:87], v16, s[8:11], s18 offen offset:3072 sc1\ns_add_u32 m0, s22, 0x2000\n" \
"s_add_u32 s43, s21, 0x2000\ns_add_u32 s21, s21, 0x10000\nv_mfma_f32_16x16x32_bf16 v[24:27], v[64:67], v[52:55], v[24:27]\ns_waitcnt lgkmcnt(0)\n" \
"ds_read_b128 v[32:35], v17 offset:16384\nv_mfma_f32_16x16x32_bf16 v[56:59], v[80:83], v[52:55], v[56:59]\nv_mfma_f32_16x16x32_bf16 v[24:27], v[72:75], v[88:91], v[24:27]\nds_read_b128 v[36:39], v17 offset:17408\n" \
"v_mfma_f32_16x16x32_bf16 v[56:59], v[44:47], v[88:91], v[56:59]\nbuffer_load_dwordx4 v16, s[12:15], s43 offen offset:0 sc1 lds\nv_mfma_f32_16x16x32_bf16 v[52:55], v[28:31], v[112:115], 0\nv_cvt_pk_bf16_f32 v24, v24, v25\n" \
"v_cvt_pk_bf16_f32 v25, v26, v27\nv_mfma_f32_16x16x32_bf16 v[60:63], v[68:71], v[112:115], 0\nv_cvt_pk_bf16_f32 v26, v56, v57\nv_cvt_pk_bf16_f32 v27, v58, v59\n" \
"v_mfma_f32_16x16x32_bf16 v[52:55], v[48:51], v[116:119], v[52:55]\nbuffer_store_dwordx4 v[20:23], v16, s[8:11], s16 offen offset:3072 sc1\nv_mfma_f32_16x16x32_bf16 v[60:63], v[76:79], v[116:119], v[60:63]\nv_mfma_f32_16x16x32_bf16 v[52:55], v[64:67], v[120:123], v[52:55]\n" \
"v_mfma_f32_16x16x32_bf16 v[60:63], v[80:83], v[120:123], v[60:63]\nv_mfma_f32_16x16x32_bf16 v[52:55], v[72:75], v[124:127], v[52:55]\ns_waitcnt lgkmcnt(0)\nv_mfma_f32_16x16x32_bf16 v[60:63], v[44:47], v[124:127], v[60:63]\n" \
"v_mfma_f32_16x16x32_bf16 v[20:23], v[28:31], v[32:35], 0\nv_cvt_pk_bf16_f32 v52, v52, v53\nv_cvt_pk_bf16_f32 v53, v54, v55\nv_mfma_f32_16x16x32_bf16 v[56:59], v[68:71], v[32:35], 0\n" \
"buffer_store_dwordx4 v[40:43], v16, s[8:11], s17 offen offset:2048 sc1\nbuffer_load_dwordx4 v16, s[12:15], s43 offen offset:1024 sc1 lds\nv_cvt_pk_bf16_f32 v54, v60, v61\nv_mfma_f32_16x16x32_bf16 v[20:23], v[48:51], v[36:39], v[20:23]\n" \
"v_cvt_pk_bf16_f32 v55, v62, v63\nv_mfma_f32_16x16x32_bf16 v[56:59], v[76:79], v[36:39], v[56:59]\nv_mfma_f32_16x16x32_bf16 v[20:23], v[64:67], v[176:179], v[20:23]\nv_mfma_f32_16x16x32_bf16 v[56:59], v[80:83], v[176:179], v[56:59]\n" \
"v_mfma_f32_16x16x32_bf16 v[20:23], v[72:75], v[180:183], v[20:23]\nv_mfma_f32_16x16x32_bf16 v[56:59], v[44:47], v[180:183], v[56:59]\nv_mfma_f32_16x16x32_bf16 v[32:35], v[28:31], v[184:187], 0\nv_cvt_pk_bf16_f32 v20, v20, v21\n" \
"v_cvt_pk_bf16_f32 v21, v22, v23\nv_mfma_f32_16x16x32_bf16 v[36:39], v[68:71], v[184:187], 0\nbuffer_store_dwordx4 v[24:27], v16, s[8:11], s17 offen offset:1024 sc1\nv_cvt_pk_bf16_f32 v22, v56, v57\n" \
"v_cvt_pk_bf16_f32 v23, v58, v59\nv_mfma_f32_16x16x32_bf16 v[32:35], v[48:51], v[188:191], v[32:35]\nv_mfma_f32_16x16x32_bf16 v[36:39], v[76:79], v[188:191], v[36:39]\nbuffer_load_dwordx4 v16, s[12:15], s43 offen offset:2048 sc1 lds\n" \
"v_mfma_f32_16x16x32_bf16 v[32:35], v[64:67], v[192:195], v[32:35]\nv_mfma_f32_16x16x32_bf16 v[36:39], v[80:83], v[192:195], v[36:39]\nv_mfma_f32_16x16x32_bf16 v[32:35], v[72:75], v[196:199], v[32:35]\nv_mfma_f32_16x16x32_bf16 v[36:39], v[44:47], v[196:199], v[36:39]\n" \
"v_mfma_f32_16x16x32_bf16 v[24:27], v[28:31], v[160:163], 0\nbuffer_store_dwordx4 v[52:55], v16, s[8:11], s17 offen offset:0 sc1\nv_cvt_pk_bf16_f32 v32, v32, v33\nv_cvt_pk_bf16_f32 v33, v34, v35\n" \
"v_mfma_f32_16x16x32_bf16 v[40:43], v[68:71], v[160:163], 0\nv_cvt_pk_bf16_f32 v34, v36, v37\nv_cvt_pk_bf16_f32 v35, v38, v39\nv_mfma_f32_16x16x32_bf16 v[24:27], v[48:51], v[164:167], v[24:27]\n" \
"v_mfma_f32_16x16x32_bf16 v[40:43], v[76:79], v[164:167], v[40:43]\nv_mfma_f32_16x16x32_bf16 v[24:27], v[64:67], v[168:171], v[24:27]\nds_read_b128 v[36:39], v17 offset:20480\nv_mfma_f32_16x16x32_bf16 v[40:43], v[80:83], v[168:171], v[40:43]\n" \
"buffer_load_dwordx4 v16, s[12:15], s43 offen offset:3072 sc1 lds\nv_mfma_f32_16x16x32_bf16 v[24:27], v[72:75], v[172:175], v[24:27]\nds_read_b128 v[52:55], v17 offset:21504\nv_mfma_f32_16x16x32_bf16 v[40:43], v[44:47], v[172:175], v[40:43]\n" \
"v_mfma_f32_16x16x32_bf16 v[60:63], v[28:31], v[232:235], 0\nbuffer_store_dwordx4 v[20:23], v16, s[8:11], s18 offen offset:1024 sc1\nv_cvt_pk_bf16_f32 v24, v24, v25\nv_cvt_pk_bf16_f32 v25, v26, v27\n" \
"v_mfma_f32_16x16x32_bf16 v[20:23], v[68:71], v[232:235], 0\nv_cvt_pk_bf16_f32 v26, v40, v41\nv_cvt_pk_bf16_f32 v27, v42, v43\nv_mfma_f32_16x16x32_bf16 v[60:63], v[48:51], v[236:239], v[60:63]\n" \
"v_mfma_f32_16x16x32_bf16 v[20:23], v[76:79], v[236:239], v[20:23]\nv_mfma_f32_16x16x32_bf16 v[60:63], v[64:67], v[240:243], v[60:63]\nv_mfma_f32_16x16x32_bf16 v[20:23], v[80:83], v[240:243], v[20:23]\nv_mfma_f32_16x16x32_bf16 v[60:63], v[72:75], v[244:247], v[60:63]\n" \
"s_waitcnt lgkmcnt(0)\nv_mfma_f32_16x16x32_bf16 v[20:23], v[44:47], v[244:247], v[20:23]\nbuffer_store_dwordx4 v[32:35], v16, s[8:11], s18 offen offset:2048 sc1\nv_mfma_f32_16x16x32_bf16 v[84:87], v[28:31], v[36:39], 0\n" \
"v_cvt_pk_bf16_f32 v60, v60, v61\nv_cvt_pk_bf16_f32 v61, v62, v63\nv_mfma_f32_16x16x32_bf16 v[32:35], v[68:71], v[36:39], 0\nv_cvt_pk_bf16_f32 v62, v20, v21\n" \
"v_cvt_pk_bf16_f32 v63, v22, v23\nv_mfma_f32_16x16x32_bf16 v[84:87], v[48:51], v[52:55], v[84:87]\nv_mfma_f32_16x16x32_bf16 v[32:35], v[76:79], v[52:55], v[32:35]\nv_mfma_f32_16x16x32_bf16 v[84:87], v[64:67], v[224:227], v[84:87]\n" \
"ds_read_b128 v[52:55], v17 offset:22528\nv_mfma_f32_16x16x32_bf16 v[32:35], v[80:83], v[224:227], v[32:35]\nv_mfma_f32_16x16x32_bf16 v[84:87], v[72:75], v[228:231], v[84:87]\nbuffer_store_dwordx4 v[24:27], v16, s[8:11], s18 offen offset:0 sc1\n" \
"ds_read_b128 v[88:91], v17 offset:23552\nv_mfma_f32_16x16x32_bf16 v[32:35], v[44:47], v[228:231], v[32:35]\nv_mfma_f32_16x16x32_bf16 v[56:59], v[28:31], v[208:211], 0\nv_cvt_pk_bf16_f32 v84, v84, v85\n" \
"v_cvt_pk_bf16_f32 v85, v86, v87\nv_mfma_f32_16x16x32_bf16 v[24:27], v[68:71], v[208:211], 0\nv_cvt_pk_bf16_f32 v86, v32, v33\nv_cvt_pk_bf16_f32 v87, v34, v35\n" \
"v_mfma_f32_16x16x32_bf16 v[56:59], v[48:51], v[212:215], v[56:59]\nv_mfma_f32_16x16x32_bf16 v[24:27], v[76:79], v[212:215], v[24:27]\nds_read_b128 v[20:23], v16 offset:0\nv_mfma_f32_16x16x32_bf16 v[56:59], v[64:67], v[216:219], v[56:59]\n" \
"ds_read_b128 v[40:43], v16 offset:32768\nv_mfma_f32_16x16x32_bf16 v[24:27], v[80:83], v[216:219], v[24:27]\nv_mfma_f32_16x16x32_bf16 v[56:59], v[72:75], v[220:223], v[56:59]\ns_waitcnt lgkmcnt(2)\n" \
"buffer_store_dwordx4 v[60:63], v16, s[8:11], s19 offen offset:2048 sc1\nv_mfma_f32_16x16x32_bf16 v[24:27], v[44:47], v[220:223], v[24:27]\nds_read_b128 v[36:39], v16 offset:1024\nv_mfma_f32_16x16x32_bf16 v[60:63], v[28:31], v[52:55], 0\n" \
"ds_read_b128 v[32:35], v16 offset:33792\nv_cvt_pk_bf16_f32 v56, v56, v57\nv_mfma_f32_16x16x32_bf16 v[28:31], v[68:71], v[52:55], 0\nv_cvt_pk_bf16_f32 v57, v58, v59\n" \
"v_cvt_pk_bf16_f32 v58, v24, v25\nv_mfma_f32_16x16x32_bf16 v[60:63], v[48:51], v[88:91], v[60:63]\nv_cvt_pk_bf16_f32 v59, v26, v27\nv_mfma_f32_16x16x32_bf16 v[28:31], v[76:79], v[88:91], v[28:31]\n" \
"ds_read_b128 v[52:55], v16 offset:2048\nv_mfma_f32_16x16x32_bf16 v[60:63], v[64:67], v[248:251], v[60:63]\nds_read_b128 v[24:27], v16 offset:34816\nv_mfma_f32_16x16x32_bf16 v[28:31], v[80:83], v[248:251], v[28:31]\n" \
"buffer_store_dwordx4 v[84:87], v16, s[8:11], s19 offen offset:1024 sc1\nv_mfma_f32_16x16x32_bf16 v[60:63], v[72:75], v[252:255], v[60:63]\ns_waitcnt lgkmcnt(2)\nv_mfma_f32_16x16x32_bf16 v[28:31], v[44:47], v[252:255], v[28:31]\n" \
"ds_read_b128 v[48:51], v16 offset:3072\nv_mfma_f32_16x16x32_bf16 v[44:47], v[0:3], v[20:23], 0\nds_read_b128 v[64:67], v16 offset:35840\nv_cvt_pk_bf16_f32 v60, v60, v61\n" \
"v_mfma_f32_16x16x32_bf16 v[68:71], v[0:3], v[40:43], 0\nv_cvt_pk_bf16_f32 v61, v62, v63\nv_cvt_pk_bf16_f32 v62, v28, v29\nv_mfma_f32_16x16x32_bf16 v[72:75], v[8:11], v[20:23], 0\n" \
"v_cvt_pk_bf16_f32 v63, v30, v31\nv_mfma_f32_16x16x32_bf16 v[20:23], v[8:11], v[40:43], 0\nds_read_b128 v[28:31], v16 offset:4096\nv_mfma_f32_16x16x32_bf16 v[44:47], v[4:7], v[36:39], v[44:47]\n" \
"ds_read_b128 v[40:43], v16 offset:36864\nv_mfma_f32_16x16x32_bf16 v[68:71], v[4:7], v[32:35], v[68:71]\nbuffer_store_dwordx4 v[56:59], v16, s[8:11], s19 offen offset:0 sc1\nv_mfma_f32_16x16x32_bf16 v[72:75], v[12:15], v[36:39], v[72:75]\n" \
"s_waitcnt lgkmcnt(2)\nv_mfma_f32_16x16x32_bf16 v[20:23], v[12:15], v[32:35], v[20:23]\nds_read_b128 v[36:39], v16 offset:5120\nv_mfma_f32_16x16x32_bf16 v[44:47], a[0:3], v[52:55], v[44:47]\n" \
"ds_read_b128 v[32:35], v16 offset:37888\nv_mfma_f32_16x16x32_bf16 v[68:71], a[0:3], v[24:27], v[68:71]\nv_mfma_f32_16x16x32_bf16 v[72:75], a[104:107], v[52:55], v[72:75]\nv_mfma_f32_16x16x32_bf16 v[20:23], a[104:107], v[24:27], v[20:23]\n" \
"ds_read_b128 v[52:55], v16 offset:6144\nv_mfma_f32_16x16x32_bf16 v[44:47], a[4:7], v[48:51], v[44:47]\nbuffer_store_dwordx4 v[60:63], v16, s[8:11], s19 offen offset:3072 sc1\nds_read_b128 v[24:27], v16 offset:38912\n" \
"v_mfma_f32_16x16x32_bf16 v[68:71], a[4:7], v[64:67], v[68:71]\nds_read_b128 v[56:59], v16 offset:7168\nv_mfma_f32_16x16x32_bf16 v[72:75], a[108:111], v[48:51], v[72:75]\nds_read_b128 v[60:63], v16 offset:39936\n" \
"v_mfma_f32_16x16x32_bf16 v[20:23], a[108:111], v[64:67], v[20:23]\ns_branch 1b\n2:\ns_cmp_lt_i32 s20, 1\n" \
"s_cbranch_scc1 3b\ns_add_u32 s30, s30, 1\ns_cmp_lt_u32 s30, 0x40000\ns_cbranch_scc0 9f\n" \
"v_cmp_eq_u32 vcc, -1, v36\ns_cbranch_vccz 6f\nbuffer_load_dwordx4 v16, s[8:11], s43 offen offset:0 sc1 lds\n6:\n" \
"v_cmp_eq_u32 vcc, -1, v80\ns_cbranch_vccz 6f\nbuffer_load_dwordx4 v16, s[8:11], s43 offen offset:1024 sc1 lds\n6:\n" \
"v_cmp_eq_u32 vcc, -1, v48\ns_cbranch_vccz 6f\nbuffer_load_dwordx4 v16, s[8:11], s43 offen offset:2048 sc1 lds\n6:\n" \
"v_cmp_eq_u32 vcc, -1, v84\ns_cbranch_vccz 6f\nbuffer_load_dwordx4 v16, s[8:11], s43 offen offset:3072 sc1 lds\n6:\n" \
"s_waitcnt vmcnt(0)\nds_read_b128 v[36:39], v61 offset:0\nds_read_b128 v[80:83], v61 offset:1024\nds_read_b128 v[48:51], v61 offset:2048\n" \
"ds_read_b128 v[84:87], v61 offset:3072\ns_waitcnt lgkmcnt(0)\nv_max3_u32 v52, v36, v80, v48\nv_max_u32 v52, v52, v84\n" \
"v_cmp_eq_u32 vcc, -1, v52\ns_cbranch_vccnz 2b\ns_branch 3b\n4:\n" \
"s_cmp_ge_i32 s20, 64\ns_cbranch_scc1 10f\ns_cmp_ge_i32 s20, 62\ns_cbranch_scc1 5b\n" \
"v_bfe_u32 v32, v16, 4, 3\nv_lshlrev_b32 v32, 2, v32\ns_lshl_b32 s43, s20, 5\ns_add_u32 s43, s43, 64\n" \
"s_add_u32 s43, s43, s26\n7:\nv_cmp_eq_u32 vcc, -1, v33\ns_cbranch_vccz 8f\n" \
"s_add_u32 s30, s30, 1\ns_cmp_lt_u32 s30, 0x40000\ns_cbranch_scc0 9f\ns_sleep 8\n" \
"buffer_load_dword v33, v32, s[8:11], s43 offen sc1\ns_waitcnt vmcnt(0)\ns_branch 7b\n8:\n" \
"s_mov_b32 s14, s10\ns_branch 5b\n10:\ns_waitcnt vmcnt(0) lgkmcnt(0)\n" \
"s_cmp_lt_i32 s6, 0\ns_cbranch_scc1 9f\ns_mov_b64 s[0:1], s[4:5]\ns_mov_b32 s2, s6\n" \
"v_lshrrev_b32 v0, 4, v16\nv_add_u32 v0, s7, v0\ns_barrier\ns_getpc_b64 s[8:9]\n" \
"11:\ns_add_u32 s8, s8, _Z5mega64Args-11b\ns_addc_u32 s9, s9, -1\ns_setpc_b64 s[8:9]\n" \
"9:\ns_waitcnt vmcnt(0)\ns_endpgm\n"
#define D_LOOP \
"s_branch 4f\n.p2align 6\n1:\ns_waitcnt lgkmcnt(0)\n" \
"v_mfma_f32_16x16x32_bf16 v[44:47], a[8:11], v[8:11], v[44:47]\nds_read_b128 v[16:19], v4 offset:3072\ns_mov_b32 m0, s24\nv_mfma_f32_16x16x32_bf16 v[64:67], a[8:11], v[12:15], v[64:67]\n" \
"ds_read_b128 v[32:35], v4 offset:35840\nbuffer_load_dword v1, s[16:19], 0 offen offset:0 sc1 lds\nv_mfma_f32_16x16x32_bf16 v[48:51], a[104:107], v[8:11], v[48:51]\ns_add_u32 m0, s23, 0\n" \
"s_and_b32 s98, s20, 1\ns_lshl_b32 s98, s98, 3\nv_mfma_f32_16x16x32_bf16 v[20:23], a[104:107], v[12:15], v[20:23]\nv_mfma_f32_16x16x32_bf16 v[56:59], a[200:203], v[8:11], v[56:59]\n" \
"v_mfma_f32_16x16x32_bf16 v[24:27], a[200:203], v[12:15], v[24:27]\nv_mfma_f32_16x16x32_bf16 v[60:63], v[152:155], v[8:11], v[60:63]\ns_waitcnt lgkmcnt(1)\nv_mfma_f32_16x16x32_bf16 v[28:31], v[152:155], v[12:15], v[28:31]\n" \
"s_waitcnt lgkmcnt(0)\nds_read_b128 v[8:11], v4 offset:4096\nv_mfma_f32_16x16x32_bf16 v[44:47], a[12:15], v[16:19], v[44:47]\nds_read_b128 v[12:15], v4 offset:36864\n" \
"v_mfma_f32_16x16x32_bf16 v[64:67], a[12:15], v[32:35], v[64:67]\nv_mfma_f32_16x16x32_bf16 v[48:51], a[108:111], v[16:19], v[48:51]\nv_mfma_f32_16x16x32_bf16 v[20:23], a[108:111], v[32:35], v[20:23]\nv_mfma_f32_16x16x32_bf16 v[56:59], a[204:207], v[16:19], v[56:59]\n" \
"v_mfma_f32_16x16x32_bf16 v[24:27], a[204:207], v[32:35], v[24:27]\nv_mfma_f32_16x16x32_bf16 v[60:63], v[156:159], v[16:19], v[60:63]\ns_waitcnt lgkmcnt(0)\nv_mfma_f32_16x16x32_bf16 v[28:31], v[156:159], v[32:35], v[28:31]\n" \
"ds_read_b128 v[16:19], v4 offset:5120\nv_mfma_f32_16x16x32_bf16 v[44:47], a[16:19], v[8:11], v[44:47]\nds_read_b128 v[32:35], v4 offset:37888\nv_mfma_f32_16x16x32_bf16 v[64:67], a[16:19], v[12:15], v[64:67]\n" \
"v_mfma_f32_16x16x32_bf16 v[48:51], a[112:115], v[8:11], v[48:51]\nv_mfma_f32_16x16x32_bf16 v[20:23], a[112:115], v[12:15], v[20:23]\nv_mfma_f32_16x16x32_bf16 v[56:59], a[208:211], v[8:11], v[56:59]\nv_mfma_f32_16x16x32_bf16 v[24:27], a[208:211], v[12:15], v[24:27]\n" \
"v_mfma_f32_16x16x32_bf16 v[60:63], v[160:163], v[8:11], v[60:63]\ns_waitcnt lgkmcnt(0)\nv_mfma_f32_16x16x32_bf16 v[28:31], v[160:163], v[12:15], v[28:31]\nds_read_b128 v[8:11], v4 offset:6144\n" \
"s_add_u32 s99, s20, 1\nv_mfma_f32_16x16x32_bf16 v[44:47], a[20:23], v[16:19], v[44:47]\nds_read_b128 v[12:15], v4 offset:38912\ns_lshr_b32 s97, s99, 1\n" \
"v_mfma_f32_16x16x32_bf16 v[64:67], a[20:23], v[32:35], v[64:67]\ns_and_b32 s99, s99, 1\ns_lshl_b32 s99, s99, 3\nv_readlane_b32 s97, v3, s97\n" \
"v_mfma_f32_16x16x32_bf16 v[48:51], a[116:119], v[16:19], v[48:51]\nv_mfma_f32_16x16x32_bf16 v[20:23], a[116:119], v[32:35], v[20:23]\ns_lshr_b32 s97, s97, s99\ns_and_b32 s97, s97, 0xff\n" \
"s_mul_i32 s97, s97, 0x38000\nv_mfma_f32_16x16x32_bf16 v[56:59], a[212:215], v[16:19], v[56:59]\ns_add_u32 s97, s97, s21\nv_mfma_f32_16x16x32_bf16 v[24:27], a[212:215], v[32:35], v[24:27]\n" \
"v_mfma_f32_16x16x32_bf16 v[60:63], v[164:167], v[16:19], v[60:63]\ns_waitcnt lgkmcnt(0)\nv_mfma_f32_16x16x32_bf16 v[28:31], v[164:167], v[32:35], v[28:31]\nds_read_b128 v[16:19], v4 offset:7168\n" \
"v_mfma_f32_16x16x32_bf16 v[44:47], a[24:27], v[8:11], v[44:47]\nds_read_b128 v[32:35], v4 offset:39936\nv_mfma_f32_16x16x32_bf16 v[64:67], a[24:27], v[12:15], v[64:67]\nv_mfma_f32_16x16x32_bf16 v[48:51], a[120:123], v[8:11], v[48:51]\n" \
"v_mfma_f32_16x16x32_bf16 v[20:23], a[120:123], v[12:15], v[20:23]\nv_mfma_f32_16x16x32_bf16 v[56:59], a[216:219], v[8:11], v[56:59]\nv_mfma_f32_16x16x32_bf16 v[24:27], a[216:219], v[12:15], v[24:27]\nv_mfma_f32_16x16x32_bf16 v[60:63], v[168:171], v[8:11], v[60:63]\n" \
"s_waitcnt lgkmcnt(0)\nv_mfma_f32_16x16x32_bf16 v[28:31], v[168:171], v[12:15], v[28:31]\nds_read_b128 v[8:11], v4 offset:8192\nv_mov_b32 v111, s20\n" \
"v_mfma_f32_16x16x32_bf16 v[44:47], a[28:31], v[16:19], v[44:47]\nds_read_b128 v[12:15], v4 offset:40960\ns_add_u32 s99, s20, 2\nv_mfma_f32_16x16x32_bf16 v[64:67], a[28:31], v[32:35], v[64:67]\n" \
"v_mfma_f32_16x16x32_bf16 v[48:51], a[124:127], v[16:19], v[48:51]\nv_mfma_f32_16x16x32_bf16 v[20:23], a[124:127], v[32:35], v[20:23]\nv_mfma_f32_16x16x32_bf16 v[56:59], a[220:223], v[16:19], v[56:59]\ns_waitcnt vmcnt(0)\n" \
"s_barrier\nv_mfma_f32_16x16x32_bf16 v[24:27], a[220:223], v[32:35], v[24:27]\nbuffer_store_dword v111, v2, s[8:11], s25 offen offset:0 sc1\nv_mfma_f32_16x16x32_bf16 v[60:63], v[172:175], v[16:19], v[60:63]\n" \
"ds_read_b32 v110, v5 offset:0\ns_mov_b32 s25, 0\nv_mfma_f32_16x16x32_bf16 v[28:31], v[172:175], v[32:35], v[28:31]\ns_waitcnt lgkmcnt(1)\n" \
"ds_read_b128 v[16:19], v4 offset:9216\nv_mfma_f32_16x16x32_bf16 v[44:47], a[32:35], v[8:11], v[44:47]\nds_read_b128 v[32:35], v4 offset:41984\nv_mfma_f32_16x16x32_bf16 v[64:67], a[32:35], v[12:15], v[64:67]\n" \
"v_mfma_f32_16x16x32_bf16 v[48:51], a[128:131], v[8:11], v[48:51]\ns_waitcnt lgkmcnt(2)\nv_cmp_gt_i32 s[94:95], s99, v110\nv_mfma_f32_16x16x32_bf16 v[20:23], a[128:131], v[12:15], v[20:23]\n" \
"v_mfma_f32_16x16x32_bf16 v[56:59], a[224:227], v[8:11], v[56:59]\ns_cmp_eq_u64 s[94:95], 0\ns_cselect_b32 s14, s10, 0\nv_mfma_f32_16x16x32_bf16 v[24:27], a[224:227], v[12:15], v[24:27]\n" \
"v_mfma_f32_16x16x32_bf16 v[60:63], v[176:179], v[8:11], v[60:63]\ns_waitcnt lgkmcnt(0)\nv_mfma_f32_16x16x32_bf16 v[28:31], v[176:179], v[12:15], v[28:31]\nds_read_b128 v[8:11], v4 offset:10240\n" \
"v_mfma_f32_16x16x32_bf16 v[44:47], a[36:39], v[16:19], v[44:47]\nds_read_b128 v[12:15], v4 offset:43008\nv_mfma_f32_16x16x32_bf16 v[64:67], a[36:39], v[32:35], v[64:67]\nv_mfma_f32_16x16x32_bf16 v[48:51], a[132:135], v[16:19], v[48:51]\n" \
"v_mfma_f32_16x16x32_bf16 v[20:23], a[132:135], v[32:35], v[20:23]\nbuffer_load_dwordx4 v0, s[12:15], s97 offen offset:0 sc1 lds\nv_mfma_f32_16x16x32_bf16 v[56:59], a[228:231], v[16:19], v[56:59]\nv_mfma_f32_16x16x32_bf16 v[24:27], a[228:231], v[32:35], v[24:27]\n" \
"v_mfma_f32_16x16x32_bf16 v[60:63], v[180:183], v[16:19], v[60:63]\ns_waitcnt lgkmcnt(0)\nv_mfma_f32_16x16x32_bf16 v[28:31], v[180:183], v[32:35], v[28:31]\nbuffer_load_dwordx4 v0, s[12:15], s97 offen offset:1024 sc1 lds\n" \
"v_mfma_f32_16x16x32_bf16 v[44:47], a[40:43], v[8:11], v[44:47]\nds_read_b128 v[16:19], v4 offset:11264\nv_mfma_f32_16x16x32_bf16 v[64:67], a[40:43], v[12:15], v[64:67]\nds_read_b128 v[32:35], v4 offset:44032\n" \
"v_mfma_f32_16x16x32_bf16 v[48:51], a[136:139], v[8:11], v[48:51]\nv_mfma_f32_16x16x32_bf16 v[20:23], a[136:139], v[12:15], v[20:23]\nbuffer_load_dwordx4 v0, s[12:15], s97 offen offset:2048 sc1 lds\nv_mfma_f32_16x16x32_bf16 v[56:59], a[232:235], v[8:11], v[56:59]\n" \
"v_mfma_f32_16x16x32_bf16 v[24:27], a[232:235], v[12:15], v[24:27]\nv_mfma_f32_16x16x32_bf16 v[60:63], v[184:187], v[8:11], v[60:63]\ns_waitcnt lgkmcnt(1)\nv_mfma_f32_16x16x32_bf16 v[28:31], v[184:187], v[12:15], v[28:31]\n" \
"s_waitcnt lgkmcnt(0)\nbuffer_load_dwordx4 v0, s[12:15], s97 offen offset:3072 sc1 lds\nv_mfma_f32_16x16x32_bf16 v[44:47], a[44:47], v[16:19], v[44:47]\nds_read_b128 v[8:11], v4 offset:12288\n" \
"s_add_u32 m0, s23, 0x1000\nv_mfma_f32_16x16x32_bf16 v[64:67], a[44:47], v[32:35], v[64:67]\nds_read_b128 v[12:15], v4 offset:45056\ns_add_u32 s97, s97, 0x1000\n" \
"v_mfma_f32_16x16x32_bf16 v[48:51], a[140:143], v[16:19], v[48:51]\nv_mfma_f32_16x16x32_bf16 v[20:23], a[140:143], v[32:35], v[20:23]\nbuffer_load_dwordx4 v0, s[12:15], s97 offen offset:0 sc1 lds\nv_mfma_f32_16x16x32_bf16 v[56:59], a[236:239], v[16:19], v[56:59]\n" \
"v_mfma_f32_16x16x32_bf16 v[24:27], a[236:239], v[32:35], v[24:27]\nv_mfma_f32_16x16x32_bf16 v[60:63], v[188:191], v[16:19], v[60:63]\ns_waitcnt lgkmcnt(1)\nv_mfma_f32_16x16x32_bf16 v[28:31], v[188:191], v[32:35], v[28:31]\n" \
"s_waitcnt lgkmcnt(0)\nbuffer_load_dwordx4 v0, s[12:15], s97 offen offset:1024 sc1 lds\nv_mfma_f32_16x16x32_bf16 v[44:47], a[48:51], v[8:11], v[44:47]\nds_read_b128 v[16:19], v4 offset:13312\n" \
"v_mfma_f32_16x16x32_bf16 v[64:67], a[48:51], v[12:15], v[64:67]\nds_read_b128 v[32:35], v4 offset:46080\nv_mfma_f32_16x16x32_bf16 v[48:51], a[144:147], v[8:11], v[48:51]\nv_mfma_f32_16x16x32_bf16 v[20:23], a[144:147], v[12:15], v[20:23]\n" \
"buffer_load_dwordx4 v0, s[12:15], s97 offen offset:2048 sc1 lds\nv_mfma_f32_16x16x32_bf16 v[56:59], a[240:243], v[8:11], v[56:59]\nv_mfma_f32_16x16x32_bf16 v[24:27], a[240:243], v[12:15], v[24:27]\nv_mfma_f32_16x16x32_bf16 v[60:63], v[192:195], v[8:11], v[60:63]\n" \
"s_waitcnt lgkmcnt(1)\nv_mfma_f32_16x16x32_bf16 v[28:31], v[192:195], v[12:15], v[28:31]\ns_waitcnt lgkmcnt(0)\nbuffer_load_dwordx4 v0, s[12:15], s97 offen offset:3072 sc1 lds\n" \
"v_mfma_f32_16x16x32_bf16 v[44:47], a[52:55], v[16:19], v[44:47]\nds_read_b128 v[8:11], v4 offset:14336\ns_add_u32 m0, s23, 0x2000\nv_mfma_f32_16x16x32_bf16 v[64:67], a[52:55], v[32:35], v[64:67]\n" \
"ds_read_b128 v[12:15], v4 offset:47104\ns_add_u32 s97, s97, 0x1000\nv_mfma_f32_16x16x32_bf16 v[48:51], a[148:151], v[16:19], v[48:51]\ns_xor_b32 s23, s23, 0x10000\n" \
"v_mfma_f32_16x16x32_bf16 v[20:23], a[148:151], v[32:35], v[20:23]\nbuffer_load_dwordx4 v0, s[12:15], s97 offen offset:0 sc1 lds\nv_mfma_f32_16x16x32_bf16 v[56:59], a[244:247], v[16:19], v[56:59]\nv_mfma_f32_16x16x32_bf16 v[24:27], a[244:247], v[32:35], v[24:27]\n" \
"v_mfma_f32_16x16x32_bf16 v[60:63], v[196:199], v[16:19], v[60:63]\ns_waitcnt lgkmcnt(1)\nv_mfma_f32_16x16x32_bf16 v[28:31], v[196:199], v[32:35], v[28:31]\ns_waitcnt lgkmcnt(0)\n" \
"buffer_load_dwordx4 v0, s[12:15], s97 offen offset:1024 sc1 lds\nv_mfma_f32_16x16x32_bf16 v[44:47], a[56:59], v[8:11], v[44:47]\nds_read_b128 v[16:19], v4 offset:15360\nv_mfma_f32_16x16x32_bf16 v[64:67], a[56:59], v[12:15], v[64:67]\n" \
"ds_read_b128 v[32:35], v4 offset:48128\nv_mfma_f32_16x16x32_bf16 v[48:51], a[152:155], v[8:11], v[48:51]\nv_mfma_f32_16x16x32_bf16 v[20:23], a[152:155], v[12:15], v[20:23]\nbuffer_load_dwordx4 v0, s[12:15], s97 offen offset:2048 sc1 lds\n" \
"v_mfma_f32_16x16x32_bf16 v[56:59], a[248:251], v[8:11], v[56:59]\nv_mfma_f32_16x16x32_bf16 v[24:27], a[248:251], v[12:15], v[24:27]\nv_mfma_f32_16x16x32_bf16 v[60:63], v[200:203], v[8:11], v[60:63]\ns_waitcnt lgkmcnt(1)\n" \
"v_mfma_f32_16x16x32_bf16 v[28:31], v[200:203], v[12:15], v[28:31]\ns_waitcnt lgkmcnt(0)\nbuffer_load_dwordx4 v0, s[12:15], s97 offen offset:3072 sc1 lds\nv_mfma_f32_16x16x32_bf16 v[44:47], a[60:63], v[16:19], v[44:47]\n" \
"ds_read_b128 v[8:11], v4 offset:16384\nv_mfma_f32_16x16x32_bf16 v[64:67], a[60:63], v[32:35], v[64:67]\nds_read_b128 v[12:15], v4 offset:49152\nv_mfma_f32_16x16x32_bf16 v[48:51], a[156:159], v[16:19], v[48:51]\n" \
"v_mfma_f32_16x16x32_bf16 v[20:23], a[156:159], v[32:35], v[20:23]\nv_mfma_f32_16x16x32_bf16 v[56:59], a[252:255], v[16:19], v[56:59]\nv_mfma_f32_16x16x32_bf16 v[24:27], a[252:255], v[32:35], v[24:27]\nv_mfma_f32_16x16x32_bf16 v[60:63], v[204:207], v[16:19], v[60:63]\n" \
"s_waitcnt lgkmcnt(1)\nv_mfma_f32_16x16x32_bf16 v[28:31], v[204:207], v[32:35], v[28:31]\ns_waitcnt lgkmcnt(0)\nds_read_b128 v[16:19], v4 offset:17408\n" \
"v_mfma_f32_16x16x32_bf16 v[44:47], a[64:67], v[8:11], v[44:47]\nds_read_b128 v[32:35], v4 offset:50176\nv_mfma_f32_16x16x32_bf16 v[64:67], a[64:67], v[12:15], v[64:67]\nv_mfma_f32_16x16x32_bf16 v[48:51], a[160:163], v[8:11], v[48:51]\n" \
"v_mfma_f32_16x16x32_bf16 v[20:23], a[160:163], v[12:15], v[20:23]\nv_mfma_f32_16x16x32_bf16 v[56:59], v[112:115], v[8:11], v[56:59]\nv_mfma_f32_16x16x32_bf16 v[24:27], v[112:115], v[12:15], v[24:27]\nv_mfma_f32_16x16x32_bf16 v[60:63], v[208:211], v[8:11], v[60:63]\n" \
"s_waitcnt lgkmcnt(0)\nv_mfma_f32_16x16x32_bf16 v[28:31], v[208:211], v[12:15], v[28:31]\nds_read_b128 v[8:11], v4 offset:18432\nv_mfma_f32_16x16x32_bf16 v[44:47], a[68:71], v[16:19], v[44:47]\n" \
"ds_read_b128 v[12:15], v4 offset:51200\nv_mfma_f32_16x16x32_bf16 v[64:67], a[68:71], v[32:35], v[64:67]\nv_mfma_f32_16x16x32_bf16 v[48:51], a[164:167], v[16:19], v[48:51]\nv_mfma_f32_16x16x32_bf16 v[20:23], a[164:167], v[32:35], v[20:23]\n" \
"s_lshr_b32 s99, s20, 1\ns_add_u32 s20, s20, 1\nv_readlane_b32 s99, v3, s99\nv_mfma_f32_16x16x32_bf16 v[56:59], v[116:119], v[16:19], v[56:59]\n" \
"v_mfma_f32_16x16x32_bf16 v[24:27], v[116:119], v[32:35], v[24:27]\ns_lshr_b32 s99, s99, s98\ns_and_b32 s99, s99, 0xff\ns_mul_i32 s99, s99, 0x50000\n" \
"v_mfma_f32_16x16x32_bf16 v[60:63], v[212:215], v[16:19], v[60:63]\ns_waitcnt lgkmcnt(0)\ns_add_u32 s99, s99, s22\ns_add_u32 s98, s99, 0x8000\n" \
"v_mfma_f32_16x16x32_bf16 v[28:31], v[212:215], v[32:35], v[28:31]\nds_read_b128 v[16:19], v4 offset:19456\nv_mfma_f32_16x16x32_bf16 v[44:47], a[72:75], v[8:11], v[44:47]\nds_read_b128 v[32:35], v4 offset:52224\n" \
"v_mfma_f32_16x16x32_bf16 v[64:67], a[72:75], v[12:15], v[64:67]\nv_mfma_f32_16x16x32_bf16 v[48:51], a[168:171], v[8:11], v[48:51]\nv_mfma_f32_16x16x32_bf16 v[20:23], a[168:171], v[12:15], v[20:23]\nv_mfma_f32_16x16x32_bf16 v[56:59], v[120:123], v[8:11], v[56:59]\n" \
"v_mfma_f32_16x16x32_bf16 v[24:27], v[120:123], v[12:15], v[24:27]\nv_mfma_f32_16x16x32_bf16 v[60:63], v[216:219], v[8:11], v[60:63]\ns_waitcnt lgkmcnt(0)\nv_mfma_f32_16x16x32_bf16 v[28:31], v[216:219], v[12:15], v[28:31]\n" \
"ds_read_b128 v[8:11], v4 offset:20480\nv_mfma_f32_16x16x32_bf16 v[44:47], a[76:79], v[16:19], v[44:47]\nds_read_b128 v[12:15], v4 offset:53248\nv_mfma_f32_16x16x32_bf16 v[64:67], a[76:79], v[32:35], v[64:67]\n" \
"v_mfma_f32_16x16x32_bf16 v[48:51], a[172:175], v[16:19], v[48:51]\nv_mfma_f32_16x16x32_bf16 v[20:23], a[172:175], v[32:35], v[20:23]\nv_mfma_f32_16x16x32_bf16 v[56:59], v[124:127], v[16:19], v[56:59]\nv_mfma_f32_16x16x32_bf16 v[24:27], v[124:127], v[32:35], v[24:27]\n" \
"v_mfma_f32_16x16x32_bf16 v[60:63], v[220:223], v[16:19], v[60:63]\ns_waitcnt lgkmcnt(0)\nv_mfma_f32_16x16x32_bf16 v[28:31], v[220:223], v[32:35], v[28:31]\nds_read_b128 v[36:39], v4 offset:21504\n" \
"v_mfma_f32_16x16x32_bf16 v[44:47], a[80:83], v[8:11], v[44:47]\nds_read_b128 v[32:35], v4 offset:54272\nv_mfma_f32_16x16x32_bf16 v[64:67], a[80:83], v[12:15], v[64:67]\nv_mfma_f32_16x16x32_bf16 v[48:51], a[176:179], v[8:11], v[48:51]\n" \
"v_mfma_f32_16x16x32_bf16 v[20:23], a[176:179], v[12:15], v[20:23]\nv_mfma_f32_16x16x32_bf16 v[56:59], v[128:131], v[8:11], v[56:59]\nv_mfma_f32_16x16x32_bf16 v[24:27], v[128:131], v[12:15], v[24:27]\nv_mfma_f32_16x16x32_bf16 v[60:63], v[224:227], v[8:11], v[60:63]\n" \
"s_waitcnt lgkmcnt(0)\nv_mfma_f32_16x16x32_bf16 v[28:31], v[224:227], v[12:15], v[28:31]\nds_read_b128 v[40:43], v4 offset:22528\nv_mfma_f32_16x16x32_bf16 v[44:47], a[84:87], v[36:39], v[44:47]\n" \
"ds_read_b128 v[16:19], v4 offset:55296\nv_xor_b32 v4, 0x10000, v4\nv_mfma_f32_16x16x32_bf16 v[64:67], a[84:87], v[32:35], v[64:67]\nv_mfma_f32_16x16x32_bf16 v[48:51], a[180:183], v[36:39], v[48:51]\n" \
"v_mfma_f32_16x16x32_bf16 v[20:23], a[180:183], v[32:35], v[20:23]\nv_mfma_f32_16x16x32_bf16 v[56:59], v[132:135], v[36:39], v[56:59]\nv_mfma_f32_16x16x32_bf16 v[24:27], v[132:135], v[32:35], v[24:27]\nv_mfma_f32_16x16x32_bf16 v[60:63], v[228:231], v[36:39], v[60:63]\n" \
"s_waitcnt lgkmcnt(0)\nv_mfma_f32_16x16x32_bf16 v[28:31], v[228:231], v[32:35], v[28:31]\ns_waitcnt vmcnt(0)\ns_barrier\n" \
"v_mfma_f32_16x16x32_bf16 v[44:47], a[88:91], v[40:43], v[44:47]\nds_read_b128 v[8:11], v4 offset:0\nv_mfma_f32_16x16x32_bf16 v[48:51], a[184:187], v[40:43], v[48:51]\nds_read_b128 v[12:15], v4 offset:32768\n" \
"v_mfma_f32_16x16x32_bf16 v[56:59], v[136:139], v[40:43], v[56:59]\nv_cvt_pk_bf16_f32 v36, v44, v45\nv_cvt_pk_bf16_f32 v37, v46, v47\nv_mfma_f32_16x16x32_bf16 v[60:63], v[232:235], v[40:43], v[60:63]\n" \
"v_cvt_pk_bf16_f32 v38, v48, v49\nv_cvt_pk_bf16_f32 v39, v50, v51\nv_mfma_f32_16x16x32_bf16 v[64:67], a[88:91], v[16:19], v[64:67]\nv_cvt_pk_bf16_f32 v32, v56, v57\n" \
"v_cvt_pk_bf16_f32 v33, v58, v59\nv_mfma_f32_16x16x32_bf16 v[20:23], a[184:187], v[16:19], v[20:23]\nbuffer_store_dwordx4 v[36:39], v6, s[8:11], s99 offen offset:0 sc1\nv_cvt_pk_bf16_f32 v34, v60, v61\n" \
"v_cvt_pk_bf16_f32 v35, v62, v63\nv_mfma_f32_16x16x32_bf16 v[24:27], v[136:139], v[16:19], v[24:27]\ns_waitcnt lgkmcnt(1)\nv_cvt_pk_bf16_f32 v36, v64, v65\n" \
"v_cvt_pk_bf16_f32 v37, v66, v67\nv_mfma_f32_16x16x32_bf16 v[28:31], v[232:235], v[16:19], v[28:31]\nds_read_b128 v[40:43], v4 offset:1024\nv_cvt_pk_bf16_f32 v38, v20, v21\n" \
"v_mfma_f32_16x16x32_bf16 v[44:47], a[0:3], v[8:11], 0\nds_read_b128 v[16:19], v4 offset:33792\nv_cvt_pk_bf16_f32 v39, v22, v23\nv_mfma_f32_16x16x32_bf16 v[48:51], a[96:99], v[8:11], 0\n" \
"buffer_store_dwordx4 v[32:35], v6, s[8:11], s99 offen offset:512 sc1\nv_cvt_pk_bf16_f32 v52, v24, v25\nv_cvt_pk_bf16_f32 v53, v26, v27\nv_cvt_pk_bf16_f32 v54, v28, v29\n" \
"v_mfma_f32_16x16x32_bf16 v[56:59], a[192:195], v[8:11], 0\ns_waitcnt lgkmcnt(2)\nv_cvt_pk_bf16_f32 v55, v30, v31\nv_mfma_f32_16x16x32_bf16 v[60:63], v[144:147], v[8:11], 0\n" \
"v_mfma_f32_16x16x32_bf16 v[64:67], a[0:3], v[12:15], 0\nv_mfma_f32_16x16x32_bf16 v[20:23], a[96:99], v[12:15], 0\nbuffer_store_dwordx4 v[36:39], v6, s[8:11], s98 offen offset:0 sc1\nv_mfma_f32_16x16x32_bf16 v[24:27], a[192:195], v[12:15], 0\n" \
"s_waitcnt lgkmcnt(0)\nv_mfma_f32_16x16x32_bf16 v[28:31], v[144:147], v[12:15], 0\nds_read_b128 v[8:11], v4 offset:2048\nv_mfma_f32_16x16x32_bf16 v[44:47], a[4:7], v[40:43], v[44:47]\n" \
"ds_read_b128 v[12:15], v4 offset:34816\nv_mfma_f32_16x16x32_bf16 v[64:67], a[4:7], v[16:19], v[64:67]\nbuffer_store_dwordx4 v[52:55], v6, s[8:11], s98 offen offset:512 sc1\nv_mfma_f32_16x16x32_bf16 v[48:51], a[100:103], v[40:43], v[48:51]\n" \
"v_mfma_f32_16x16x32_bf16 v[20:23], a[100:103], v[16:19], v[20:23]\nv_mfma_f32_16x16x32_bf16 v[56:59], a[196:199], v[40:43], v[56:59]\nv_mfma_f32_16x16x32_bf16 v[24:27], a[196:199], v[16:19], v[24:27]\nv_mfma_f32_16x16x32_bf16 v[60:63], v[148:151], v[40:43], v[60:63]\n" \
"s_cmp_eq_u32 s14, 0\nv_mfma_f32_16x16x32_bf16 v[28:31], v[148:151], v[16:19], v[28:31]\ns_cbranch_scc0 1b\n4:\n" \
"buffer_load_dword v68, v1, s[8:11], 0 offen sc1\nv_mov_b32 v69, s20\ns_add_u32 s98, s20, 1\ns_waitcnt vmcnt(0)\n" \
"v_cmp_gt_i32 vcc, s98, v68\ns_cbranch_vccz 8f\nbuffer_store_dword v69, v2, s[8:11], 0 offen sc1\ns_mov_b32 s25, 0x40000000\n" \
"s_cmp_ge_u32 s20, s28\ns_cbranch_scc1 9f\n7:\ns_add_u32 s26, s26, 1\n" \
"s_cmp_lt_u32 s26, 0x40000\ns_cbranch_scc0 9f\ns_sleep 8\nbuffer_load_dword v68, v1, s[8:11], 0 offen sc1\n" \
"s_waitcnt vmcnt(0)\nv_cmp_gt_i32 vcc, s98, v68\ns_cbranch_vccnz 7b\n8:\n" \
"s_lshr_b32 s98, s20, 1\nv_readlane_b32 s98, v3, s98\ns_and_b32 s99, s20, 1\ns_lshl_b32 s99, s99, 3\n" \
"s_lshr_b32 s98, s98, s99\ns_and_b32 s98, s98, 0xff\ns_mul_i32 s98, s98, 0x38000\ns_add_u32 s98, s98, s21\n" \
"s_xor_b32 s99, s23, 0x10000\ns_add_u32 m0, s99, 0\ns_add_u32 s98, s98, 0\nbuffer_load_dwordx4 v0, s[8:11], s98 offen offset:0 sc1 lds\n" \
"buffer_load_dwordx4 v0, s[8:11], s98 offen offset:1024 sc1 lds\nbuffer_load_dwordx4 v0, s[8:11], s98 offen offset:2048 sc1 lds\nbuffer_load_dwordx4 v0, s[8:11], s98 offen offset:3072 sc1 lds\ns_add_u32 m0, s99, 0x1000\n" \
"s_add_u32 s98, s98, 0x1000\nbuffer_load_dwordx4 v0, s[8:11], s98 offen offset:0 sc1 lds\nbuffer_load_dwordx4 v0, s[8:11], s98 offen offset:1024 sc1 lds\nbuffer_load_dwordx4 v0, s[8:11], s98 offen offset:2048 sc1 lds\n" \
"buffer_load_dwordx4 v0, s[8:11], s98 offen offset:3072 sc1 lds\ns_add_u32 m0, s99, 0x2000\ns_add_u32 s98, s98, 0x1000\nbuffer_load_dwordx4 v0, s[8:11], s98 offen offset:0 sc1 lds\n" \
"buffer_load_dwordx4 v0, s[8:11], s98 offen offset:1024 sc1 lds\nbuffer_load_dwordx4 v0, s[8:11], s98 offen offset:2048 sc1 lds\nbuffer_load_dwordx4 v0, s[8:11], s98 offen offset:3072 sc1 lds\nbuffer_store_dword v69, v2, s[8:11], s25 offen sc1\n" \
"s_mov_b32 s25, 0x40000000\ns_waitcnt vmcnt(1) lgkmcnt(0)\ns_barrier\nds_read_b128 v[68:71], v4 offset:0\n" \
"ds_read_b128 v[72:75], v4 offset:32768\nds_read_b128 v[76:79], v4 offset:1024\nds_read_b128 v[80:83], v4 offset:33792\nds_read_b128 v[8:11], v4 offset:2048\n" \
"ds_read_b128 v[12:15], v4 offset:34816\ns_waitcnt lgkmcnt(0)\nv_mfma_f32_16x16x32_bf16 v[44:47], a[0:3], v[68:71], 0\nv_mfma_f32_16x16x32_bf16 v[48:51], a[96:99], v[68:71], 0\n" \
"v_mfma_f32_16x16x32_bf16 v[56:59], a[192:195], v[68:71], 0\nv_mfma_f32_16x16x32_bf16 v[60:63], v[144:147], v[68:71], 0\nv_mfma_f32_16x16x32_bf16 v[64:67], a[0:3], v[72:75], 0\nv_mfma_f32_16x16x32_bf16 v[20:23], a[96:99], v[72:75], 0\n" \
"v_mfma_f32_16x16x32_bf16 v[24:27], a[192:195], v[72:75], 0\nv_mfma_f32_16x16x32_bf16 v[28:31], v[144:147], v[72:75], 0\nv_mfma_f32_16x16x32_bf16 v[44:47], a[4:7], v[76:79], v[44:47]\nv_mfma_f32_16x16x32_bf16 v[48:51], a[100:103], v[76:79], v[48:51]\n" \
"v_mfma_f32_16x16x32_bf16 v[56:59], a[196:199], v[76:79], v[56:59]\nv_mfma_f32_16x16x32_bf16 v[60:63], v[148:151], v[76:79], v[60:63]\nv_mfma_f32_16x16x32_bf16 v[64:67], a[4:7], v[80:83], v[64:67]\nv_mfma_f32_16x16x32_bf16 v[20:23], a[100:103], v[80:83], v[20:23]\n" \
"v_mfma_f32_16x16x32_bf16 v[24:27], a[196:199], v[80:83], v[24:27]\nv_mfma_f32_16x16x32_bf16 v[28:31], v[148:151], v[80:83], v[28:31]\ns_branch 1b\n9:\n" \
"s_waitcnt vmcnt(0)\ns_endpgm\n"
#define G_LOOP \
"s_branch 4f\n.p2align 6\n1:\ns_waitcnt lgkmcnt(2)\n" \
"v_mfma_f32_16x16x32_bf16 v[8:11], a[104:107], v[36:39], v[8:11]\ns_lshr_b32 s99, s20, 1\ns_and_b32 s98, s20, 1\nv_mov_b32 v110, s20\n" \
"v_mfma_f32_16x16x32_bf16 v[12:15], a[104:107], v[40:43], v[12:15]\ns_add_u32 s20, s20, 1\ns_add_u32 s97, s20, 1\ns_lshr_b32 s96, s97, 1\n" \
"v_mfma_f32_16x16x32_bf16 v[16:19], a[232:235], v[36:39], v[16:19]\nds_read_b128 v[52:55], v7 offset:28672\nv_readlane_b32 s96, v6, s96\nv_mfma_f32_16x16x32_bf16 v[20:23], a[232:235], v[40:43], v[20:23]\n" \
"ds_read_b128 v[56:59], v7 offset:61440\nv_xor_b32 v4, 0x400, v4\nv_mfma_f32_16x16x32_bf16 v[24:27], v[216:219], v[36:39], v[24:27]\ns_and_b32 s97, s97, 1\n" \
"s_lshl_b32 s97, s97, 3\ns_lshr_b32 s96, s96, s97\nv_mfma_f32_16x16x32_bf16 v[28:31], v[216:219], v[40:43], v[28:31]\ns_waitcnt lgkmcnt(2)\n" \
"s_and_b32 s96, s96, 0xff\ns_lshl_b32 s97, s96, 13\nv_mfma_f32_16x16x32_bf16 v[8:11], a[108:111], v[44:47], v[8:11]\ns_lshl_b32 s98, s98, 3\n" \
"v_mov_b32 v32, v111\nv_mov_b32 v33, v111\nv_mfma_f32_16x16x32_bf16 v[12:15], a[108:111], v[48:51], v[12:15]\ns_lshl_b32 s96, s96, 16\n" \
"v_mul_f32 v252, 0xc0135761, v34\nv_mul_f32 v34, v34, v34\nv_mfma_f32_16x16x32_bf16 v[16:19], a[236:239], v[44:47], v[16:19]\nds_read_b128 v[36:39], v7 offset:29696\n" \
"v_readlane_b32 s99, v6, s99\nv_mfma_f32_16x16x32_bf16 v[20:23], a[236:239], v[48:51], v[20:23]\nds_read_b128 v[40:43], v7 offset:62464\nv_mul_f32 v34, v34, v252\n" \
"v_mfma_f32_16x16x32_bf16 v[24:27], v[220:223], v[44:47], v[24:27]\nds_read_b128 v[60:63], v7 offset:30720\nv_mul_f32 v254, v35, v35\nv_mfma_f32_16x16x32_bf16 v[28:31], v[220:223], v[48:51], v[28:31]\n" \
"s_waitcnt lgkmcnt(3)\nv_permlane16_swap_b32 v32, v33\nv_mfma_f32_16x16x32_bf16 v[8:11], a[112:115], v[52:55], v[8:11]\nds_read_b128 v[44:47], v7 offset:63488\n" \
"v_mul_f32 v253, 0x3d372713, v34\nv_mfma_f32_16x16x32_bf16 v[12:15], a[112:115], v[56:59], v[12:15]\ns_lshr_b32 s99, s99, s98\ns_and_b32 s99, s99, 0xff\n" \
"s_mul_i32 s99, s99, 0x38000\nv_mfma_f32_16x16x32_bf16 v[16:19], a[240:243], v[52:55], v[16:19]\nds_read_b128 v[48:51], v7 offset:31744\ns_or_b32 s16, s99, s18\n" \
"v_mfma_f32_16x16x32_bf16 v[20:23], a[240:243], v[56:59], v[20:23]\ns_or_b32 s17, s99, s19\ns_add_u32 s96, s96, s21\nv_mfma_f32_16x16x32_bf16 v[24:27], v[224:227], v[52:55], v[24:27]\n" \
"ds_read_b128 v[64:67], v7 offset:64512\nv_xor_b32 v7, 0x10000, v7\nv_mfma_f32_16x16x32_bf16 v[28:31], v[224:227], v[56:59], v[28:31]\ns_waitcnt lgkmcnt(3)\n" \
"v_mfma_f32_16x16x32_bf16 v[8:11], a[116:119], v[36:39], v[8:11]\nv_mfma_f32_16x16x32_bf16 v[12:15], a[116:119], v[40:43], v[12:15]\nv_mfma_f32_16x16x32_bf16 v[16:19], a[244:247], v[36:39], v[16:19]\ns_waitcnt vmcnt(3) lgkmcnt(0)\n" \
"s_barrier\nv_mfma_f32_16x16x32_bf16 v[20:23], a[244:247], v[40:43], v[20:23]\nds_read_b128 v[52:55], v4 offset:0\nv_mfma_f32_16x16x32_bf16 v[24:27], v[228:231], v[36:39], v[24:27]\n" \
"ds_read_b128 v[56:59], v7 offset:0\nv_mfma_f32_16x16x32_bf16 v[28:31], v[228:231], v[40:43], v[28:31]\nds_read_b128 v[36:39], v7 offset:32768\nv_mfma_f32_16x16x32_bf16 v[8:11], a[120:123], v[60:63], v[8:11]\n" \
"ds_read_b128 v[40:43], v7 offset:1024\nv_mfma_f32_16x16x32_bf16 v[12:15], a[120:123], v[44:47], v[12:15]\ns_waitcnt lgkmcnt(3)\nv_add_f32 v52, v52, v53\n" \
"v_mfma_f32_16x16x32_bf16 v[8:11], a[124:127], v[48:51], v[8:11]\nbuffer_load_dwordx4 v[68:71], v1, s[8:11], s97 offen offset:1024 sc1\nv_mfma_f32_16x16x32_bf16 v[12:15], a[124:127], v[64:67], v[12:15]\nbuffer_load_dwordx4 v[72:75], v1, s[8:11], s97 offen offset:0 sc1\n" \
"v_mfma_f32_16x16x32_bf16 v[16:19], a[248:251], v[60:63], v[16:19]\nds_read_b128 v[76:79], v7 offset:33792\nv_add_f32 v54, v54, v55\nv_mfma_f32_16x16x32_bf16 v[20:23], a[248:251], v[44:47], v[20:23]\n" \
"v_cmp_u_f32 s[98:99], v52, v54\nv_fma_f32 v52, v32, v240, v8\nv_mfma_f32_16x16x32_bf16 v[16:19], a[252:255], v[48:51], v[16:19]\ns_waitcnt vmcnt(2)\n" \
"buffer_store_dword v110, v3, s[8:11], s23 offen offset:0 sc1\nv_mfma_f32_16x16x32_bf16 v[20:23], a[252:255], v[64:67], v[20:23]\ns_mov_b32 s23, 0\nv_fma_f32 v53, v32, v241, v9\n" \
"v_fma_f32 v54, v32, v242, v10\nv_mfma_f32_16x16x32_bf16 v[24:27], v[232:235], v[60:63], v[24:27]\ns_cmp_lt_u32 s20, s26\ns_cselect_b32 s98, s98, -1\n" \
"v_fma_f32 v55, v32, v243, v11\nv_mfma_f32_16x16x32_bf16 v[28:31], v[232:235], v[44:47], v[28:31]\nv_fma_f32 v60, v33, v240, v12\nv_fma_f32 v61, v33, v241, v13\n" \
"v_mfma_f32_16x16x32_bf16 v[24:27], v[236:239], v[48:51], v[24:27]\ns_waitcnt lgkmcnt(0)\nv_fma_f32 v62, v33, v242, v14\nv_fma_f32 v63, v33, v243, v15\n" \
"v_mfma_f32_16x16x32_bf16 v[28:31], v[236:239], v[64:67], v[28:31]\ns_cmp_lg_u64 s[98:99], 0\ns_cbranch_scc1 4f\n5:\n" \
"v_mfma_f32_16x16x32_bf16 v[8:11], a[0:3], v[56:59], 0\ns_xor_b32 m0, s22, 0\nv_fma_f32 v44, v32, v244, v16\nv_fma_f32 v45, v32, v245, v17\n" \
"v_mfma_f32_16x16x32_bf16 v[12:15], a[0:3], v[36:39], 0\nv_fma_f32 v46, v32, v246, v18\nv_fma_f32 v47, v32, v247, v19\nv_mfma_f32_16x16x32_bf16 v[8:11], a[4:7], v[40:43], v[8:11]\n" \
"v_fma_f32 v48, v33, v244, v20\nv_fma_f32 v49, v33, v245, v21\nv_mfma_f32_16x16x32_bf16 v[12:15], a[4:7], v[76:79], v[12:15]\nv_fma_f32 v50, v33, v246, v22\n" \
"v_fma_f32 v51, v33, v247, v23\nv_mfma_f32_16x16x32_bf16 v[16:19], a[128:131], v[56:59], 0\nds_read_b128 v[64:67], v7 offset:2048\nv_fma_f32 v80, v32, v248, v24\n" \
"v_mfma_f32_16x16x32_bf16 v[20:23], a[128:131], v[36:39], 0\nds_read_b128 v[84:87], v7 offset:34816\nv_fma_f32 v81, v32, v249, v25\nv_mfma_f32_16x16x32_bf16 v[16:19], a[132:135], v[40:43], v[16:19]\n" \
"v_fma_f32 v82, v32, v250, v26\nv_fma_f32 v83, v32, v251, v27\nv_mfma_f32_16x16x32_bf16 v[20:23], a[132:135], v[76:79], v[20:23]\nv_fma_f32 v88, v33, v248, v28\n" \
"v_fma_f32 v89, v33, v249, v29\nv_fma_f32 v90, v33, v250, v30\nv_fma_f32 v91, v33, v251, v31\nv_mfma_f32_16x16x32_bf16 v[24:27], v[112:115], v[56:59], 0\n" \
"ds_read_b128 v[92:95], v7 offset:3072\nv_xor_b32 v5, 0x400, v5\nv_mfma_f32_16x16x32_bf16 v[28:31], v[112:115], v[36:39], 0\nds_read_b128 v[56:59], v7 offset:35840\n" \
"v_mfma_f32_16x16x32_bf16 v[24:27], v[116:119], v[40:43], v[24:27]\ns_waitcnt lgkmcnt(3)\nv_permlane16_swap_b32 v52, v60\nv_mfma_f32_16x16x32_bf16 v[28:31], v[116:119], v[76:79], v[28:31]\n" \
"s_waitcnt lgkmcnt(2)\nv_permlane16_swap_b32 v53, v61\nv_mfma_f32_16x16x32_bf16 v[8:11], a[8:11], v[64:67], v[8:11]\nv_permlane16_swap_b32 v54, v62\n" \
"v_mfma_f32_16x16x32_bf16 v[12:15], a[8:11], v[84:87], v[12:15]\nv_permlane16_swap_b32 v55, v63\nv_mfma_f32_16x16x32_bf16 v[16:19], a[136:139], v[64:67], v[16:19]\nds_read_b128 v[36:39], v7 offset:4096\n" \
"v_mul_f32 v40, v52, v52\nv_mfma_f32_16x16x32_bf16 v[20:23], a[136:139], v[84:87], v[20:23]\nds_read_b128 v[76:79], v7 offset:36864\nv_mul_f32 v41, v53, v53\n" \
"v_mfma_f32_16x16x32_bf16 v[24:27], v[120:123], v[64:67], v[24:27]\ns_waitcnt lgkmcnt(2)\nv_mul_f32 v42, v54, v54\nv_mul_f32 v43, v55, v55\n" \
"v_mfma_f32_16x16x32_bf16 v[28:31], v[120:123], v[84:87], v[28:31]\nv_fma_f32 v40, v40, v253, v252\nv_fma_f32 v41, v41, v253, v252\nv_mfma_f32_16x16x32_bf16 v[8:11], a[12:15], v[92:95], v[8:11]\n" \
"v_fma_f32 v42, v42, v253, v252\nv_fma_f32 v43, v43, v253, v252\nv_mfma_f32_16x16x32_bf16 v[12:15], a[12:15], v[56:59], v[12:15]\nv_mul_f32 v40, v40, v52\n" \
"v_mul_f32 v41, v41, v53\nv_mfma_f32_16x16x32_bf16 v[16:19], a[140:143], v[92:95], v[16:19]\nds_read_b128 v[64:67], v7 offset:5120\nv_mul_f32 v42, v42, v54\n" \
"v_mfma_f32_16x16x32_bf16 v[20:23], a[140:143], v[56:59], v[20:23]\nds_read_b128 v[84:87], v7 offset:37888\nv_mul_f32 v43, v43, v55\nv_mfma_f32_16x16x32_bf16 v[24:27], v[124:127], v[92:95], v[24:27]\n" \
"s_waitcnt lgkmcnt(2)\nv_exp_f32 v40, v40\nv_mfma_f32_16x16x32_bf16 v[28:31], v[124:127], v[56:59], v[28:31]\nv_exp_f32 v41, v41\n" \
"v_mfma_f32_16x16x32_bf16 v[8:11], a[16:19], v[36:39], v[8:11]\nv_exp_f32 v42, v42\nv_mfma_f32_16x16x32_bf16 v[12:15], a[16:19], v[76:79], v[12:15]\nv_exp_f32 v43, v43\n" \
"v_mfma_f32_16x16x32_bf16 v[16:19], a[144:147], v[36:39], v[16:19]\nds_read_b128 v[56:59], v7 offset:6144\nv_fma_f32 v40, v40, v254, v254\nv_mfma_f32_16x16x32_bf16 v[20:23], a[144:147], v[76:79], v[20:23]\n" \
"ds_read_b128 v[92:95], v7 offset:38912\nv_fma_f32 v41, v41, v254, v254\nv_mfma_f32_16x16x32_bf16 v[24:27], v[128:131], v[36:39], v[24:27]\ns_waitcnt lgkmcnt(2)\n" \
"v_rcp_f32 v40, v40\nv_mfma_f32_16x16x32_bf16 v[28:31], v[128:131], v[76:79], v[28:31]\nv_rcp_f32 v41, v41\nv_mfma_f32_16x16x32_bf16 v[8:11], a[20:23], v[64:67], v[8:11]\n" \
"v_fma_f32 v42, v42, v254, v254\nv_fma_f32 v43, v43, v254, v254\nv_mfma_f32_16x16x32_bf16 v[12:15], a[20:23], v[84:87], v[12:15]\nv_rcp_f32 v42, v42\n" \
"v_mfma_f32_16x16x32_bf16 v[16:19], a[148:151], v[64:67], v[16:19]\nds_read_b128 v[36:39], v7 offset:7168\nv_mul_f32 v60, v52, v60\nv_mfma_f32_16x16x32_bf16 v[20:23], a[148:151], v[84:87], v[20:23]\n" \
"s_waitcnt vmcnt(1)\nv_max3_u32 v35, v72, v74, v68\nv_mul_f32 v61, v53, v61\nv_mfma_f32_16x16x32_bf16 v[24:27], v[132:135], v[64:67], v[24:27]\n" \
"ds_read_b128 v[76:79], v7 offset:39936\nv_max_u32 v35, v35, v70\nv_mfma_f32_16x16x32_bf16 v[28:31], v[132:135], v[84:87], v[28:31]\ns_waitcnt lgkmcnt(2)\n" \
"v_cmp_eq_u32 s[98:99], -1, v35\nv_mul_f32 v62, v54, v62\nv_mfma_f32_16x16x32_bf16 v[8:11], a[24:27], v[56:59], v[8:11]\nv_rcp_f32 v43, v43\n" \
"v_mfma_f32_16x16x32_bf16 v[12:15], a[24:27], v[92:95], v[12:15]\ns_cmp_eq_u64 s[98:99], 0\ns_cselect_b32 s14, s10, 0\nv_mul_f32 v63, v55, v63\n" \
"v_mfma_f32_16x16x32_bf16 v[16:19], a[152:155], v[56:59], v[16:19]\nbuffer_load_dwordx4 v0, s[12:15], s96 offen offset:0 sc1 lds\nv_mfma_f32_16x16x32_bf16 v[20:23], a[152:155], v[92:95], v[20:23]\nds_read_b128 v[52:55], v7 offset:8192\n" \
"v_mul_f32 v60, v60, v40\nv_mfma_f32_16x16x32_bf16 v[24:27], v[136:139], v[56:59], v[24:27]\nds_read_b128 v[64:67], v7 offset:40960\nv_mul_f32 v61, v61, v41\n" \
"v_mfma_f32_16x16x32_bf16 v[28:31], v[136:139], v[92:95], v[28:31]\ns_waitcnt lgkmcnt(2)\nv_mul_f32 v62, v62, v42\nv_mul_f32 v63, v63, v43\n" \
"v_mfma_f32_16x16x32_bf16 v[8:11], a[28:31], v[36:39], v[8:11]\nv_cvt_pk_bf16_f32 v60, v60, v61\nv_cvt_pk_bf16_f32 v61, v62, v63\nv_mfma_f32_16x16x32_bf16 v[12:15], a[28:31], v[76:79], v[12:15]\n" \
"buffer_load_dwordx4 v0, s[12:15], s96 offen offset:1024 sc1 lds\nv_mfma_f32_16x16x32_bf16 v[16:19], a[156:159], v[36:39], v[16:19]\nds_read_b128 v[40:43], v7 offset:9216\nv_mfma_f32_16x16x32_bf16 v[20:23], a[156:159], v[76:79], v[20:23]\n" \
"ds_read_b128 v[56:59], v7 offset:41984\nv_mfma_f32_16x16x32_bf16 v[24:27], v[140:143], v[36:39], v[24:27]\ns_waitcnt lgkmcnt(2)\nv_permlane16_swap_b32 v44, v48\n" \
"v_mfma_f32_16x16x32_bf16 v[28:31], v[140:143], v[76:79], v[28:31]\nbuffer_load_dwordx4 v0, s[12:15], s96 offen offset:2048 sc1 lds\nv_mfma_f32_16x16x32_bf16 v[8:11], a[32:35], v[52:55], v[8:11]\nv_permlane16_swap_b32 v45, v49\n" \
"v_mfma_f32_16x16x32_bf16 v[12:15], a[32:35], v[64:67], v[12:15]\nv_permlane16_swap_b32 v46, v50\nv_mfma_f32_16x16x32_bf16 v[16:19], a[160:163], v[52:55], v[16:19]\nds_read_b128 v[36:39], v7 offset:10240\n" \
"v_mul_f32 v76, v44, v44\nv_mfma_f32_16x16x32_bf16 v[20:23], a[160:163], v[64:67], v[20:23]\nds_read_b128 v[84:87], v7 offset:43008\nv_mul_f32 v77, v45, v45\n" \
"v_mfma_f32_16x16x32_bf16 v[24:27], v[144:147], v[52:55], v[24:27]\ns_waitcnt lgkmcnt(2)\nbuffer_load_dwordx4 v0, s[12:15], s96 offen offset:3072 sc1 lds\nv_mfma_f32_16x16x32_bf16 v[28:31], v[144:147], v[64:67], v[28:31]\n" \
"s_xor_b32 m0, s22, 0x1000\ns_xor_b32 s96, s96, 0x1000\nv_mul_f32 v78, v46, v46\nv_mfma_f32_16x16x32_bf16 v[8:11], a[36:39], v[40:43], v[8:11]\n" \
"v_permlane16_swap_b32 v47, v51\nv_mfma_f32_16x16x32_bf16 v[12:15], a[36:39], v[56:59], v[12:15]\nv_mul_f32 v79, v47, v47\nv_fma_f32 v76, v76, v253, v252\n" \
"v_mfma_f32_16x16x32_bf16 v[16:19], a[164:167], v[40:43], v[16:19]\nds_read_b128 v[52:55], v7 offset:11264\nv_fma_f32 v77, v77, v253, v252\nv_mfma_f32_16x16x32_bf16 v[20:23], a[164:167], v[56:59], v[20:23]\n" \
"buffer_load_dwordx4 v0, s[12:15], s96 offen offset:0 sc1 lds\nv_mfma_f32_16x16x32_bf16 v[24:27], v[148:151], v[40:43], v[24:27]\nds_read_b128 v[64:67], v7 offset:44032\nv_fma_f32 v78, v78, v253, v252\n" \
"v_mfma_f32_16x16x32_bf16 v[28:31], v[148:151], v[56:59], v[28:31]\ns_waitcnt lgkmcnt(2)\nv_fma_f32 v79, v79, v253, v252\nv_mul_f32 v76, v76, v44\n" \
"v_mfma_f32_16x16x32_bf16 v[8:11], a[40:43], v[36:39], v[8:11]\nv_exp_f32 v76, v76\nv_mfma_f32_16x16x32_bf16 v[12:15], a[40:43], v[84:87], v[12:15]\nv_mul_f32 v77, v77, v45\n" \
"v_mul_f32 v78, v78, v46\nv_mfma_f32_16x16x32_bf16 v[16:19], a[168:171], v[36:39], v[16:19]\nbuffer_load_dwordx4 v0, s[12:15], s96 offen offset:1024 sc1 lds\nv_mfma_f32_16x16x32_bf16 v[20:23], a[168:171], v[84:87], v[20:23]\n" \
"ds_read_b128 v[40:43], v7 offset:12288\nv_mul_f32 v79, v79, v47\nv_mfma_f32_16x16x32_bf16 v[24:27], v[152:155], v[36:39], v[24:27]\nds_read_b128 v[56:59], v7 offset:45056\n" \
"v_fma_f32 v76, v76, v254, v254\nv_mfma_f32_16x16x32_bf16 v[28:31], v[152:155], v[84:87], v[28:31]\ns_waitcnt lgkmcnt(2)\nv_exp_f32 v77, v77\n" \
"v_mfma_f32_16x16x32_bf16 v[8:11], a[44:47], v[52:55], v[8:11]\nv_exp_f32 v78, v78\nv_mfma_f32_16x16x32_bf16 v[12:15], a[44:47], v[64:67], v[12:15]\nbuffer_load_dwordx4 v0, s[12:15], s96 offen offset:2048 sc1 lds\n" \
"v_mfma_f32_16x16x32_bf16 v[16:19], a[172:175], v[52:55], v[16:19]\nds_read_b128 v[36:39], v7 offset:13312\nv_fma_f32 v77, v77, v254, v254\nv_mfma_f32_16x16x32_bf16 v[20:23], a[172:175], v[64:67], v[20:23]\n" \
"ds_read_b128 v[84:87], v7 offset:46080\nv_fma_f32 v78, v78, v254, v254\nv_mfma_f32_16x16x32_bf16 v[24:27], v[156:159], v[52:55], v[24:27]\ns_waitcnt lgkmcnt(2)\n" \
"v_exp_f32 v79, v79\nv_mfma_f32_16x16x32_bf16 v[28:31], v[156:159], v[64:67], v[28:31]\nv_rcp_f32 v76, v76\nv_mfma_f32_16x16x32_bf16 v[8:11], a[48:51], v[40:43], v[8:11]\n" \
"buffer_load_dwordx4 v0, s[12:15], s96 offen offset:3072 sc1 lds\nv_mfma_f32_16x16x32_bf16 v[12:15], a[48:51], v[56:59], v[12:15]\ns_xor_b32 m0, s22, 0x8000\ns_xor_b32 s96, s96, 0x9000\n" \
"v_fma_f32 v79, v79, v254, v254\nv_mfma_f32_16x16x32_bf16 v[16:19], a[176:179], v[40:43], v[16:19]\nds_read_b128 v[52:55], v7 offset:14336\nv_mul_f32 v48, v44, v48\n" \
"v_mfma_f32_16x16x32_bf16 v[20:23], a[176:179], v[56:59], v[20:23]\nds_read_b128 v[64:67], v7 offset:47104\nv_mul_f32 v49, v45, v49\nv_mfma_f32_16x16x32_bf16 v[24:27], v[160:163], v[40:43], v[24:27]\n" \
"s_waitcnt lgkmcnt(2)\nv_rcp_f32 v77, v77\nv_mfma_f32_16x16x32_bf16 v[28:31], v[160:163], v[56:59], v[28:31]\nbuffer_load_dwordx4 v0, s[12:15], s96 offen offset:0 sc1 lds\n" \
"v_mfma_f32_16x16x32_bf16 v[8:11], a[52:55], v[36:39], v[8:11]\nv_rcp_f32 v78, v78\nv_mfma_f32_16x16x32_bf16 v[12:15], a[52:55], v[84:87], v[12:15]\nv_rcp_f32 v79, v79\n" \
"v_mfma_f32_16x16x32_bf16 v[16:19], a[180:183], v[36:39], v[16:19]\nds_read_b128 v[40:43], v7 offset:15360\nv_mul_f32 v50, v46, v50\nv_mfma_f32_16x16x32_bf16 v[20:23], a[180:183], v[84:87], v[20:23]\n" \
"ds_read_b128 v[56:59], v7 offset:48128\nv_mul_f32 v51, v47, v51\nv_mfma_f32_16x16x32_bf16 v[24:27], v[164:167], v[36:39], v[24:27]\ns_waitcnt lgkmcnt(2)\n" \
"buffer_load_dwordx4 v0, s[12:15], s96 offen offset:1024 sc1 lds\nv_mfma_f32_16x16x32_bf16 v[28:31], v[164:167], v[84:87], v[28:31]\nv_mul_f32 v48, v48, v76\nv_mul_f32 v49, v49, v77\n" \
"v_mfma_f32_16x16x32_bf16 v[8:11], a[56:59], v[52:55], v[8:11]\nv_mul_f32 v50, v50, v78\nv_mul_f32 v51, v51, v79\nv_mfma_f32_16x16x32_bf16 v[12:15], a[56:59], v[64:67], v[12:15]\n" \
"v_cvt_pk_bf16_f32 v48, v48, v49\nv_cvt_pk_bf16_f32 v49, v50, v51\nv_mfma_f32_16x16x32_bf16 v[16:19], a[184:187], v[52:55], v[16:19]\nds_read_b128 v[36:39], v7 offset:16384\n" \
"v_mfma_f32_16x16x32_bf16 v[20:23], a[184:187], v[64:67], v[20:23]\nbuffer_load_dwordx4 v0, s[12:15], s96 offen offset:2048 sc1 lds\nv_mfma_f32_16x16x32_bf16 v[24:27], v[168:171], v[52:55], v[24:27]\ns_waitcnt lgkmcnt(1)\n" \
"ds_read_b128 v[44:47], v7 offset:49152\nv_mfma_f32_16x16x32_bf16 v[28:31], v[168:171], v[64:67], v[28:31]\nv_permlane16_swap_b32 v80, v88\nv_mfma_f32_16x16x32_bf16 v[8:11], a[60:63], v[40:43], v[8:11]\n" \
"v_permlane16_swap_b32 v81, v89\nv_mfma_f32_16x16x32_bf16 v[12:15], a[60:63], v[56:59], v[12:15]\nv_permlane16_swap_b32 v82, v90\nv_mfma_f32_16x16x32_bf16 v[16:19], a[188:191], v[40:43], v[16:19]\n" \
"buffer_load_dwordx4 v0, s[12:15], s96 offen offset:3072 sc1 lds\nv_mfma_f32_16x16x32_bf16 v[20:23], a[188:191], v[56:59], v[20:23]\nds_read_b128 v[52:55], v7 offset:17408\ns_xor_b32 m0, s22, 0x9000\n" \
"v_mfma_f32_16x16x32_bf16 v[24:27], v[172:175], v[40:43], v[24:27]\nds_read_b128 v[64:67], v7 offset:50176\ns_xor_b32 s96, s96, 0x1000\nv_mfma_f32_16x16x32_bf16 v[28:31], v[172:175], v[56:59], v[28:31]\n" \
"s_waitcnt lgkmcnt(2)\nv_permlane16_swap_b32 v83, v91\nv_mfma_f32_16x16x32_bf16 v[8:11], a[64:67], v[36:39], v[8:11]\ns_xor_b32 s22, s22, 0x10000\n" \
"v_mul_f32 v40, v80, v80\nv_mul_f32 v41, v81, v81\nv_mfma_f32_16x16x32_bf16 v[12:15], a[64:67], v[44:47], v[12:15]\nbuffer_load_dwordx4 v0, s[12:15], s96 offen offset:0 sc1 lds\n" \
"v_mfma_f32_16x16x32_bf16 v[16:19], a[192:195], v[36:39], v[16:19]\nds_read_b128 v[56:59], v7 offset:18432\nv_mul_f32 v42, v82, v82\nv_mfma_f32_16x16x32_bf16 v[20:23], a[192:195], v[44:47], v[20:23]\n" \
"ds_read_b128 v[76:79], v7 offset:51200\nv_mul_f32 v43, v83, v83\nv_mfma_f32_16x16x32_bf16 v[24:27], v[176:179], v[36:39], v[24:27]\ns_waitcnt lgkmcnt(2)\n" \
"v_fma_f32 v40, v40, v253, v252\nv_fma_f32 v41, v41, v253, v252\nv_mfma_f32_16x16x32_bf16 v[28:31], v[176:179], v[44:47], v[28:31]\nv_fma_f32 v42, v42, v253, v252\n" \
"v_fma_f32 v43, v43, v253, v252\nv_mfma_f32_16x16x32_bf16 v[8:11], a[68:71], v[52:55], v[8:11]\nbuffer_load_dwordx4 v0, s[12:15], s96 offen offset:1024 sc1 lds\nv_mfma_f32_16x16x32_bf16 v[12:15], a[68:71], v[64:67], v[12:15]\n" \
"v_mul_f32 v40, v40, v80\nv_mul_f32 v41, v41, v81\nv_mfma_f32_16x16x32_bf16 v[16:19], a[196:199], v[52:55], v[16:19]\nds_read_b128 v[36:39], v7 offset:19456\n" \
"v_mul_f32 v42, v42, v82\nv_mfma_f32_16x16x32_bf16 v[20:23], a[196:199], v[64:67], v[20:23]\nds_read_b128 v[44:47], v7 offset:52224\nv_mul_f32 v43, v43, v83\n" \
"v_mfma_f32_16x16x32_bf16 v[24:27], v[180:183], v[52:55], v[24:27]\ns_waitcnt lgkmcnt(2)\nv_exp_f32 v40, v40\nv_mfma_f32_16x16x32_bf16 v[28:31], v[180:183], v[64:67], v[28:31]\n" \
"buffer_load_dwordx4 v0, s[12:15], s96 offen offset:2048 sc1 lds\nv_mfma_f32_16x16x32_bf16 v[8:11], a[72:75], v[56:59], v[8:11]\nv_exp_f32 v41, v41\nv_mfma_f32_16x16x32_bf16 v[12:15], a[72:75], v[76:79], v[12:15]\n" \
"v_exp_f32 v42, v42\nv_mfma_f32_16x16x32_bf16 v[16:19], a[200:203], v[56:59], v[16:19]\nds_read_b128 v[52:55], v7 offset:20480\nv_fma_f32 v40, v40, v254, v254\n" \
"v_mfma_f32_16x16x32_bf16 v[20:23], a[200:203], v[76:79], v[20:23]\nds_read_b128 v[64:67], v7 offset:53248\nv_fma_f32 v41, v41, v254, v254\nv_mfma_f32_16x16x32_bf16 v[24:27], v[184:187], v[56:59], v[24:27]\n" \
"s_waitcnt lgkmcnt(2)\nbuffer_load_dwordx4 v0, s[12:15], s96 offen offset:3072 sc1 lds\nv_mfma_f32_16x16x32_bf16 v[28:31], v[184:187], v[76:79], v[28:31]\nv_exp_f32 v43, v43\n" \
"v_mfma_f32_16x16x32_bf16 v[8:11], a[76:79], v[36:39], v[8:11]\nv_rcp_f32 v40, v40\nv_mfma_f32_16x16x32_bf16 v[12:15], a[76:79], v[44:47], v[12:15]\nv_rcp_f32 v41, v41\n" \
"v_mfma_f32_16x16x32_bf16 v[16:19], a[204:207], v[36:39], v[16:19]\nds_read_b128 v[56:59], v7 offset:21504\nv_fma_f32 v42, v42, v254, v254\nv_mfma_f32_16x16x32_bf16 v[20:23], a[204:207], v[44:47], v[20:23]\n" \
"ds_read_b128 v[76:79], v7 offset:54272\nv_fma_f32 v43, v43, v254, v254\nv_mfma_f32_16x16x32_bf16 v[24:27], v[188:191], v[36:39], v[24:27]\ns_waitcnt lgkmcnt(2)\n" \
"v_rcp_f32 v42, v42\nv_mfma_f32_16x16x32_bf16 v[28:31], v[188:191], v[44:47], v[28:31]\nv_rcp_f32 v43, v43\nv_mfma_f32_16x16x32_bf16 v[8:11], a[80:83], v[52:55], v[8:11]\n" \
"buffer_store_dwordx2 v[60:61], v2, s[8:11], s16 offen offset:0 sc1\nv_mul_f32 v88, v80, v88\nv_mfma_f32_16x16x32_bf16 v[12:15], a[80:83], v[64:67], v[12:15]\nv_mul_f32 v89, v81, v89\n" \
"v_mul_f32 v90, v82, v90\nv_mfma_f32_16x16x32_bf16 v[16:19], a[208:211], v[52:55], v[16:19]\nbuffer_store_dwordx2 v[48:49], v2, s[8:11], s17 offen offset:256 sc1\nv_mul_f32 v91, v83, v91\n" \
"v_mfma_f32_16x16x32_bf16 v[20:23], a[208:211], v[64:67], v[20:23]\nv_mul_f32 v88, v88, v40\nv_mul_f32 v89, v89, v41\nv_mfma_f32_16x16x32_bf16 v[24:27], v[192:195], v[52:55], v[24:27]\n" \
"ds_read_b128 v[36:39], v7 offset:22528\nv_mul_f32 v90, v90, v42\nv_mul_f32 v91, v91, v43\nv_mfma_f32_16x16x32_bf16 v[28:31], v[192:195], v[64:67], v[28:31]\n" \
"s_waitcnt lgkmcnt(1)\nds_read_b128 v[40:43], v7 offset:55296\nv_cvt_pk_bf16_f32 v88, v88, v89\nv_mfma_f32_16x16x32_bf16 v[8:11], a[84:87], v[56:59], v[8:11]\n" \
"ds_read_b128 v[44:47], v4 offset:0\nv_cvt_pk_bf16_f32 v89, v90, v91\nv_mfma_f32_16x16x32_bf16 v[12:15], a[84:87], v[76:79], v[12:15]\nbuffer_store_dwordx2 v[88:89], v2, s[8:11], s17 offen offset:512 sc1\n" \
"v_add_f32 v72, v72, v68\nv_mfma_f32_16x16x32_bf16 v[16:19], a[212:215], v[56:59], v[16:19]\nds_read_b128 v[48:51], v7 offset:23552\nv_add_f32 v73, v73, v69\n" \
"v_mfma_f32_16x16x32_bf16 v[20:23], a[212:215], v[76:79], v[20:23]\nds_read_b128 v[52:55], v7 offset:56320\nv_add_f32 v74, v74, v70\nv_mfma_f32_16x16x32_bf16 v[24:27], v[196:199], v[56:59], v[24:27]\n" \
"ds_read_b128 v[60:63], v4 offset:512\nv_add_f32 v75, v75, v71\nv_mfma_f32_16x16x32_bf16 v[28:31], v[196:199], v[76:79], v[28:31]\ns_waitcnt lgkmcnt(4)\n" \
"v_permlane16_swap_b32 v72, v74\nv_mfma_f32_16x16x32_bf16 v[8:11], a[88:91], v[36:39], v[8:11]\nv_permlane16_swap_b32 v73, v75\nv_mfma_f32_16x16x32_bf16 v[12:15], a[88:91], v[40:43], v[12:15]\n" \
"s_waitcnt lgkmcnt(3)\nv_add_f32 v44, v44, v45\nv_add_f32 v46, v46, v47\nv_mfma_f32_16x16x32_bf16 v[16:19], a[216:219], v[36:39], v[16:19]\n" \
"ds_read_b128 v[56:59], v7 offset:24576\nv_add_f32 v44, v44, v46\nv_mfma_f32_16x16x32_bf16 v[20:23], a[216:219], v[40:43], v[20:23]\nds_read_b128 v[64:67], v7 offset:57344\n" \
"v_mul_f32 v44, 0xba800000, v44\nv_mfma_f32_16x16x32_bf16 v[24:27], v[200:203], v[36:39], v[24:27]\ns_waitcnt lgkmcnt(2)\nv_add_f32 v72, v72, v74\n" \
"v_add_f32 v73, v73, v75\nv_mfma_f32_16x16x32_bf16 v[28:31], v[200:203], v[40:43], v[28:31]\nv_add_f32 v60, v60, v61\nv_add_f32 v62, v62, v63\n" \
"v_mfma_f32_16x16x32_bf16 v[8:11], a[92:95], v[48:51], v[8:11]\nv_add_f32 v60, v60, v62\nv_mul_f32 v61, v44, -v44\nv_mfma_f32_16x16x32_bf16 v[12:15], a[92:95], v[52:55], v[12:15]\n" \
"v_fmamk_f32 v35, v60, 0x3a800000, v61\nv_cvt_pk_bf16_f32 v35, v35, v35\nv_mfma_f32_16x16x32_bf16 v[16:19], a[220:223], v[48:51], v[16:19]\nds_read_b128 v[60:63], v7 offset:25600\n" \
"v_lshlrev_b32 v35, 16, v35\nv_mfma_f32_16x16x32_bf16 v[20:23], a[220:223], v[52:55], v[20:23]\nds_read_b128 v[68:71], v7 offset:58368\nv_max_f32 v35, 0, v35\n" \
"v_mfma_f32_16x16x32_bf16 v[24:27], v[204:207], v[48:51], v[24:27]\ns_waitcnt lgkmcnt(2)\nv_add_f32 v35, 0x3727c5ac, v35\nv_cvt_pk_bf16_f32 v111, v35, v44\n" \
"v_mfma_f32_16x16x32_bf16 v[28:31], v[204:207], v[52:55], v[28:31]\nv_permlane32_swap_b32 v72, v73\nv_mfma_f32_16x16x32_bf16 v[8:11], a[96:99], v[56:59], v[8:11]\nv_lshlrev_b32 v35, 16, v111\n" \
"v_add_f32 v72, v72, v73\nv_mfma_f32_16x16x32_bf16 v[12:15], a[96:99], v[64:67], v[12:15]\nv_sqrt_f32 v35, v35\nv_mfma_f32_16x16x32_bf16 v[16:19], a[224:227], v[56:59], v[16:19]\n" \
"ds_read_b128 v[36:39], v7 offset:26624\nv_cvt_pk_bf16_f32 v35, v35, v35\nv_mfma_f32_16x16x32_bf16 v[20:23], a[224:227], v[64:67], v[20:23]\nds_read_b128 v[40:43], v7 offset:59392\n" \
"v_lshlrev_b32 v35, 16, v35\nv_mfma_f32_16x16x32_bf16 v[24:27], v[208:211], v[56:59], v[24:27]\ns_waitcnt lgkmcnt(2)\nv_rcp_f32 v34, v35\n" \
"v_mfma_f32_16x16x32_bf16 v[28:31], v[208:211], v[64:67], v[28:31]\nds_write_b32 v5, v72 offset:0\nv_mfma_f32_16x16x32_bf16 v[8:11], a[100:103], v[60:63], v[8:11]\nv_and_b32 v111, 0xffff0000, v111\n" \
"v_mfma_f32_16x16x32_bf16 v[12:15], a[100:103], v[68:71], v[12:15]\nv_mfma_f32_16x16x32_bf16 v[16:19], a[228:231], v[60:63], v[16:19]\nds_read_b128 v[44:47], v7 offset:27648\nv_mfma_f32_16x16x32_bf16 v[20:23], a[228:231], v[68:71], v[20:23]\n" \
"ds_read_b128 v[48:51], v7 offset:60416\nv_mfma_f32_16x16x32_bf16 v[24:27], v[212:215], v[60:63], v[24:27]\nv_mfma_f32_16x16x32_bf16 v[28:31], v[212:215], v[68:71], v[28:31]\ns_branch 1b\n" \
"4:\ns_barrier\ns_lshr_b32 s94, s20, 1\nv_readlane_b32 s94, v6, s94\n" \
"s_and_b32 s95, s20, 1\ns_lshl_b32 s95, s95, 3\ns_lshr_b32 s94, s94, s95\ns_and_b32 s94, s94, 0xff\n" \
"s_lshl_b32 s95, s94, 13\nbuffer_load_dwordx4 v[92:95], v1, s[8:11], s95 offen offset:0 sc1\nbuffer_load_dwordx4 v[96:99], v1, s[8:11], s95 offen offset:1024 sc1\nv_fma_f32 v80, v32, v240, v8\n" \
"v_fma_f32 v81, v32, v241, v9\nv_fma_f32 v82, v32, v242, v10\nv_fma_f32 v83, v32, v243, v11\nv_fma_f32 v84, v33, v240, v12\n" \
"v_fma_f32 v85, v33, v241, v13\nv_fma_f32 v86, v33, v242, v14\nv_fma_f32 v87, v33, v243, v15\nv_permlane16_swap_b32 v80, v84\n" \
"v_permlane16_swap_b32 v81, v85\nv_permlane16_swap_b32 v82, v86\nv_permlane16_swap_b32 v83, v87\nv_mul_f32 v88, v80, v80\n" \
"v_mul_f32 v89, v81, v81\nv_mul_f32 v90, v82, v82\nv_mul_f32 v91, v83, v83\nv_fma_f32 v88, v88, v253, v252\n" \
"v_fma_f32 v89, v89, v253, v252\nv_fma_f32 v90, v90, v253, v252\nv_fma_f32 v91, v91, v253, v252\nv_mul_f32 v88, v88, v80\n" \
"v_mul_f32 v89, v89, v81\nv_mul_f32 v90, v90, v82\nv_mul_f32 v91, v91, v83\nv_mul_f32 v84, v80, v84\n" \
"v_mul_f32 v85, v81, v85\nv_mul_f32 v86, v82, v86\nv_mul_f32 v87, v83, v87\nv_exp_f32 v88, v88\n" \
"v_exp_f32 v89, v89\nv_exp_f32 v90, v90\nv_exp_f32 v91, v91\nv_fma_f32 v88, v88, v254, v254\n" \
"v_fma_f32 v89, v89, v254, v254\nv_fma_f32 v90, v90, v254, v254\nv_fma_f32 v91, v91, v254, v254\nv_rcp_f32 v88, v88\n" \
"v_rcp_f32 v89, v89\nv_rcp_f32 v90, v90\nv_rcp_f32 v91, v91\nv_mul_f32 v84, v84, v88\n" \
"v_mul_f32 v85, v85, v89\nv_mul_f32 v86, v86, v90\nv_mul_f32 v87, v87, v91\nv_cvt_pk_bf16_f32 v84, v84, v85\n" \
"v_cvt_pk_bf16_f32 v85, v86, v87\nbuffer_store_dwordx2 v[84:85], v2, s[8:11], s16 offen offset:0 sc1\nv_fma_f32 v80, v32, v244, v16\nv_fma_f32 v81, v32, v245, v17\n" \
"v_fma_f32 v82, v32, v246, v18\nv_fma_f32 v83, v32, v247, v19\nv_fma_f32 v84, v33, v244, v20\nv_fma_f32 v85, v33, v245, v21\n" \
"v_fma_f32 v86, v33, v246, v22\nv_fma_f32 v87, v33, v247, v23\nv_permlane16_swap_b32 v80, v84\nv_permlane16_swap_b32 v81, v85\n" \
"v_permlane16_swap_b32 v82, v86\nv_permlane16_swap_b32 v83, v87\nv_mul_f32 v88, v80, v80\nv_mul_f32 v89, v81, v81\n" \
"v_mul_f32 v90, v82, v82\nv_mul_f32 v91, v83, v83\nv_fma_f32 v88, v88, v253, v252\nv_fma_f32 v89, v89, v253, v252\n" \
"v_fma_f32 v90, v90, v253, v252\nv_fma_f32 v91, v91, v253, v252\nv_mul_f32 v88, v88, v80\nv_mul_f32 v89, v89, v81\n" \
"v_mul_f32 v90, v90, v82\nv_mul_f32 v91, v91, v83\nv_mul_f32 v84, v80, v84\nv_mul_f32 v85, v81, v85\n" \
"v_mul_f32 v86, v82, v86\nv_mul_f32 v87, v83, v87\nv_exp_f32 v88, v88\nv_exp_f32 v89, v89\n" \
"v_exp_f32 v90, v90\nv_exp_f32 v91, v91\nv_fma_f32 v88, v88, v254, v254\nv_fma_f32 v89, v89, v254, v254\n" \
"v_fma_f32 v90, v90, v254, v254\nv_fma_f32 v91, v91, v254, v254\nv_rcp_f32 v88, v88\nv_rcp_f32 v89, v89\n" \
"v_rcp_f32 v90, v90\nv_rcp_f32 v91, v91\nv_mul_f32 v84, v84, v88\nv_mul_f32 v85, v85, v89\n" \
"v_mul_f32 v86, v86, v90\nv_mul_f32 v87, v87, v91\nv_cvt_pk_bf16_f32 v84, v84, v85\nv_cvt_pk_bf16_f32 v85, v86, v87\n" \
"buffer_store_dwordx2 v[84:85], v2, s[8:11], s17 offen offset:256 sc1\nv_fma_f32 v80, v32, v248, v24\nv_fma_f32 v81, v32, v249, v25\nv_fma_f32 v82, v32, v250, v26\n" \
"v_fma_f32 v83, v32, v251, v27\nv_fma_f32 v84, v33, v248, v28\nv_fma_f32 v85, v33, v249, v29\nv_fma_f32 v86, v33, v250, v30\n" \
"v_fma_f32 v87, v33, v251, v31\nv_permlane16_swap_b32 v80, v84\nv_permlane16_swap_b32 v81, v85\nv_permlane16_swap_b32 v82, v86\n" \
"v_permlane16_swap_b32 v83, v87\nv_mul_f32 v88, v80, v80\nv_mul_f32 v89, v81, v81\nv_mul_f32 v90, v82, v82\n" \
"v_mul_f32 v91, v83, v83\nv_fma_f32 v88, v88, v253, v252\nv_fma_f32 v89, v89, v253, v252\nv_fma_f32 v90, v90, v253, v252\n" \
"v_fma_f32 v91, v91, v253, v252\nv_mul_f32 v88, v88, v80\nv_mul_f32 v89, v89, v81\nv_mul_f32 v90, v90, v82\n" \
"v_mul_f32 v91, v91, v83\nv_mul_f32 v84, v80, v84\nv_mul_f32 v85, v81, v85\nv_mul_f32 v86, v82, v86\n" \
"v_mul_f32 v87, v83, v87\nv_exp_f32 v88, v88\nv_exp_f32 v89, v89\nv_exp_f32 v90, v90\n" \
"v_exp_f32 v91, v91\nv_fma_f32 v88, v88, v254, v254\nv_fma_f32 v89, v89, v254, v254\nv_fma_f32 v90, v90, v254, v254\n" \
"v_fma_f32 v91, v91, v254, v254\nv_rcp_f32 v88, v88\nv_rcp_f32 v89, v89\nv_rcp_f32 v90, v90\n" \
"v_rcp_f32 v91, v91\nv_mul_f32 v84, v84, v88\nv_mul_f32 v85, v85, v89\nv_mul_f32 v86, v86, v90\n" \
"v_mul_f32 v87, v87, v91\nv_cvt_pk_bf16_f32 v84, v84, v85\nv_cvt_pk_bf16_f32 v85, v86, v87\nbuffer_store_dwordx2 v[84:85], v2, s[8:11], s17 offen offset:512 sc1\n" \
"s_mov_b32 s16, 0x80000000\ns_mov_b32 s17, 0x80000000\nv_mov_b32 v80, s20\ns_cmp_ge_u32 s20, s26\n" \
"s_cbranch_scc1 6f\ns_waitcnt vmcnt(3)\nv_max3_u32 v81, v92, v94, v96\nv_max_u32 v81, v81, v98\n" \
"v_cmp_eq_u32 vcc, -1, v81\ns_cbranch_vccz 8f\n6:\ns_waitcnt vmcnt(0)\n" \
"buffer_store_dword v80, v3, s[8:11], 0 offen offset:0 sc1\ns_mov_b32 s23, 0x40000000\ns_cbranch_scc1 9f\n7:\n" \
"s_add_u32 s24, s24, 1\ns_cmp_lt_u32 s24, 0x40000\ns_cbranch_scc0 9f\nbuffer_load_dwordx4 v[92:95], v1, s[8:11], s95 offen offset:0 sc1\n" \
"buffer_load_dwordx4 v[96:99], v1, s[8:11], s95 offen offset:1024 sc1\ns_waitcnt vmcnt(0)\nv_max3_u32 v81, v92, v94, v96\nv_max_u32 v81, v81, v98\n" \
"v_cmp_eq_u32 vcc, -1, v81\ns_cbranch_vccz 8f\ns_sleep 8\ns_branch 7b\n" \
"8:\ns_lshl_b32 s94, s94, 16\ns_add_u32 s94, s94, s21\ns_xor_b32 s95, s22, 0x10000\n" \
"s_xor_b32 m0, s95, 0\ns_xor_b32 s94, s94, 0\nbuffer_load_dwordx4 v0, s[8:11], s94 offen offset:0 sc1 lds\nbuffer_load_dwordx4 v0, s[8:11], s94 offen offset:1024 sc1 lds\n" \
"buffer_load_dwordx4 v0, s[8:11], s94 offen offset:2048 sc1 lds\nbuffer_load_dwordx4 v0, s[8:11], s94 offen offset:3072 sc1 lds\ns_xor_b32 m0, s95, 0x1000\ns_xor_b32 s94, s94, 0x1000\n" \
"buffer_load_dwordx4 v0, s[8:11], s94 offen offset:0 sc1 lds\nbuffer_load_dwordx4 v0, s[8:11], s94 offen offset:1024 sc1 lds\nbuffer_load_dwordx4 v0, s[8:11], s94 offen offset:2048 sc1 lds\nbuffer_load_dwordx4 v0, s[8:11], s94 offen offset:3072 sc1 lds\n" \
"s_xor_b32 m0, s95, 0x8000\ns_xor_b32 s94, s94, 0x9000\nbuffer_load_dwordx4 v0, s[8:11], s94 offen offset:0 sc1 lds\nbuffer_load_dwordx4 v0, s[8:11], s94 offen offset:1024 sc1 lds\n" \
"buffer_load_dwordx4 v0, s[8:11], s94 offen offset:2048 sc1 lds\nbuffer_load_dwordx4 v0, s[8:11], s94 offen offset:3072 sc1 lds\ns_xor_b32 m0, s95, 0x9000\ns_xor_b32 s94, s94, 0x1000\n" \
"buffer_load_dwordx4 v0, s[8:11], s94 offen offset:0 sc1 lds\nbuffer_load_dwordx4 v0, s[8:11], s94 offen offset:1024 sc1 lds\nbuffer_load_dwordx4 v0, s[8:11], s94 offen offset:2048 sc1 lds\nbuffer_load_dwordx4 v0, s[8:11], s94 offen offset:3072 sc1 lds\n" \
"v_add_f32 v92, v92, v96\nv_add_f32 v93, v93, v97\nv_add_f32 v94, v94, v98\nv_add_f32 v95, v95, v99\n" \
"s_nop 1\nv_permlane16_swap_b32 v92, v94\nv_permlane16_swap_b32 v93, v95\nv_add_f32 v92, v92, v94\n" \
"v_add_f32 v93, v93, v95\ns_nop 1\nv_permlane32_swap_b32 v92, v93\nv_add_f32 v92, v92, v93\n" \
"ds_write_b32 v5, v92\ns_add_u32 s97, s20, 1\ns_lshr_b32 s96, s97, 1\nv_readlane_b32 s96, v6, s96\n" \
"s_and_b32 s97, s97, 1\ns_lshl_b32 s97, s97, 3\ns_lshr_b32 s96, s96, s97\ns_and_b32 s96, s96, 0xff\n" \
"s_lshl_b32 s97, s96, 13\ns_lshl_b32 s96, s96, 16\ns_add_u32 s96, s96, s21\nbuffer_load_dwordx4 v[72:75], v1, s[8:11], s97 offen offset:0 sc1\n" \
"buffer_load_dwordx4 v[68:71], v1, s[8:11], s97 offen offset:1024 sc1\ns_waitcnt vmcnt(18)\nbuffer_store_dword v80, v3, s[8:11], s23 offen offset:0 sc1\ns_mov_b32 s23, 0x40000000\n" \
"s_waitcnt vmcnt(3) lgkmcnt(0)\ns_barrier\nds_read_b128 v[56:59], v7 offset:0\nds_read_b128 v[36:39], v7 offset:32768\n" \
"ds_read_b128 v[40:43], v7 offset:1024\nds_read_b128 v[76:79], v7 offset:33792\ns_waitcnt lgkmcnt(0)\ns_branch 5b\n" \
"9:\ns_waitcnt vmcnt(0)\ns_endpgm\n"
struct WMA { static constexpr int NT = 4, NP = 16, RB = 0, LB = 0, CBI = 0;
             static constexpr int kind(int j, int t) { return (A_LDS >> (16 * j + t) & 1) ? 1 : (A_VAR >> (16 * j + t) & 1) ? 2 : 0; } };
namespace la { constexpr int L_Q = 24576, L_K = 57344, L_QM = 65536, L_KM = 66048, L_SP = 69632, L_W = 73728; }
#ifndef LEAD_B
#define LEAD_B 132
#endif
DEVI float dror8(float x) { return __uint_as_float(__builtin_amdgcn_update_dpp(0u, __float_as_uint(x), 0x128, 0xF, 0xF, true)); }
DEVI float scm(float a, float m) { float r; asm("v_fma_f32 %0, %1, %2, -%3" : "=v"(r) : "v"(a), "s"(SC_L2E), "v"(m)); return r; }
DEVI float ex2(float x) { return __builtin_amdgcn_exp2f(x); }
DEVI float max3(float a, float b, float c) { float r; asm("v_max3_f32 %0, %1, %2, %3" : "=v"(r) : "v"(a), "v"(b), "v"(c)); return r; }
constexpr u64 tri(int i) { u64 m = 0; for (int l = 0; l < 64; ++l) if (4 * (l >> 4) + i <= (l & 15)) m |= 1ull << l; return m; }
DEVI unsigned vbfe(unsigned a, int o, int n) { unsigned r; asm volatile("v_bfe_u32 %0, %1, %2, %3" : "=v"(r) : "v"(a), "i"(o), "i"(n)); return r; }
DEVI unsigned vadds(unsigned a, unsigned b) { unsigned r; asm volatile("v_add_u32 %0, %1, %2" : "=v"(r) : "s"(b), "v"(a)); return r; }
DEVI float vexp(float a) { float r; asm volatile("v_exp_f32 %0, %1" : "=v"(r) : "v"(a)); return r; }
DEVI unsigned vors(unsigned a, unsigned b) { unsigned r; asm volatile("v_or_b32 %0, %1, %2" : "=v"(r) : "s"(b), "v"(a)); return r; }
DEVI float faddv(float a, float b) { float r; asm volatile("v_add_f32 %0, %1, %2" : "=v"(r) : "v"(a), "v"(b)); return r; }
DEVI unsigned packv(float a, float b) { unsigned r; asm volatile("v_cvt_pk_bf16_f32 %0, %1, %2" : "=v"(r) : "v"(a), "v"(b)); return r; }
DEVI void setm0q(unsigned v) { asm volatile("s_mov_b32 m0, %0" :: "s"(v) : "memory"); }
template <int OFF> DEVI void st16o(v4u v, rsrc_t r, unsigned vo, unsigned so) {
    asm volatile("buffer_store_dwordx4 %0, %1, %2, %3 offen offset:%4 sc1" :: "v"(v), "v"(vo), "s"(r), "s"(so), "n"(OFF) : "memory"); }
DEVI unsigned vlsa(unsigned a, int sh, unsigned c) { unsigned r; asm volatile("v_lshl_add_u32 %0, %1, %2, %3" : "=v"(r) : "v"(a), "i"(sh), "v"(c)); return r; }
DEVI unsigned vlss(unsigned a, int sh, unsigned c) { unsigned r; asm volatile("v_lshl_add_u32 %0, %1, %2, %3" : "=v"(r) : "v"(a), "i"(sh), "s"(c)); return r; }
template <int OFF> DEVI void sts4h(unsigned p, unsigned u0, unsigned u1) {
    asm volatile("ds_write_b16 %0, %1 offset:%3\n\tds_write_b16_d16_hi %0, %1 offset:%3+16\n\tds_write_b16 %0, %2 offset:%3+8\n\tds_write_b16_d16_hi %0, %2 offset:%3+24" :: "v"(p), "v"(u0), "v"(u1), "n"(OFF) : "memory");
}
DEVI void zero4v(f32x4& d) { f32x2 a, b; asm volatile("v_mov_b64 %0, 0\n\tv_mov_b64 %1, 0" : "=v"(a), "=v"(b)); d = __builtin_shufflevector(a, b, 0, 1, 2, 3); }
DEVI float sel0(float a, u64 m) { float r; asm("v_cndmask_b32 %0, %1, 0, %2" : "=v"(r) : "v"(a), "s"(m)); return r; }
DEVI float vmul(float a, float b) { float r; asm("v_mul_f32 %0, %1, %2" : "=v"(r) : "v"(a), "v"(b)); return r; }
DEVI void swap16(float& a, float& b) { unsigned x = __float_as_uint(a), y = __float_as_uint(b); swap16u(x, y); a = __uint_as_float(x); b = __uint_as_float(y); }
#ifndef PACE
#define PACE 7
#endif
DEVI void pace(int l) { asm volatile("s_cmp_eq_u32 %0, 0\n\ts_cbranch_scc1 1f\n\ts_sleep %1\n1:" :: "s"(l), "n"(PACE) : "scc"); }
DEVI void role_A(const Args& a, int w, int l, int h, int rep, int id2) {
    asm volatile("; MARK role_A"); __builtin_amdgcn_s_waitcnt(0);
    using namespace la;
    const int lane0 = lane_id();
    const rsrc_t rs = mkrs(a.ws, WS_NEED);
    const unsigned lane16 = lane0 * 16, wl = L_W + w * (11 * 2048) + lane16;
    v8u Wc[3];
    unsigned nb, fx, ok;
    const unsigned own = (w >> 1) * 32768 + (w & 1) * 4096;
    {
        const int lane = lane0;
        const unsigned ta = (lane >> 4) * 2048 + w * 256 + (lane & 15) * 16;
        const rsrc_t r1 = mkrs(a.Wq, WSZ(8u << 20)), r2 = mkrs(a.Wk, WSZ(8u << 20));
        const unsigned v1 = (8 * w + ((lane >> 2) & 3)) * 8192 + h * 256 + (lane >> 4) * 64 + (lane & 3) * 16;
        const unsigned lw = opqs(w * 2048);
        load_blocks<32, 4, 4, true, 2>([&](auto B, auto S) {
            if constexpr (B.v == 32) ok = first_there(fx);
            if constexpr (B.v >= 32) dma4(opqs(own + S.v * 8192), mkrs(a.ws, ok), lane16, opqs(O_XN + l * XB + own + S.v * 8192));
            else { pace(l); sfor<0, 2>([&](auto I) { dma_blk(lw + S.v * 8192 + I.v * 32768, B.v < 16 ? r1 : r2, v1, opqs((2 * (B.v & 15) + I.v) * 32 * 8192), 2048); }); } },
                                       [&](auto B, auto S) { sfor<0, 4>([&](auto T) { constexpr int o = S.v * 8192 + (T.v >> 1) * 32768 + (T.v & 1) * 8;
                                                                                      put_frag<WMA, (B.v >> 4) * 2 + (T.v & 1), 2 * (B.v & 15) + (T.v >> 1), o, o + 1024>(ta, wl, Wc); });
                                                             if constexpr (B.v == 24) { nb = side(rs, l); first_look(fx, rs, nb); } });
        TSW(127, 2);
    }
    const unsigned spm = na(nb, N_PM + h * 512), spmm = na(nb, N_PMM + h * 128), smem = na(nb, N_MEM + rep * 16384 + w * 4096);
    const int lane = lane_id(), c = lane & 15, g = lane >> 4;
    const unsigned wlo = (unsigned)(uintptr_t)a.ws, whi = (unsigned)((uintptr_t)a.ws >> 32), hi = -(w >> 1);
    const v8u pv{lane16, wl, (lane & 2) ? 0xEEEEu : 0x4444u, (unsigned)(L_QM + w * 1024 + (c >> 2) * 128 + g * 32 + (c & 3) * 4), (unsigned)(L_QM + g * 128 + (c & 7) * 16),
                 w == 2 ? ((g >> 1) * 16 + crow(c & 7, c >> 3)) * 16 + (g & 1) * 8 : w < 2 ? lane * 8 : lane < NC ? DV(lane * 512) : 1u << 30,
                 w < 2 ? c * 16 + g * 4 : c < 8 ? crow(c, g & 1) * 8 + (g >> 1) * 4 : 1u << 30, 0u};
    const v16u s0{wlo, whi, RSZ, 0x20000u, wlo, whi, 0u, 0x20000u, (unsigned)tri(0) | hi, (unsigned)(tri(0) >> 32) | hi, (unsigned)tri(1) | hi, (unsigned)(tri(1) >> 32) | hi,
                  (unsigned)tri(2) | hi, (unsigned)(tri(2) >> 32) | hi, (unsigned)tri(3) | hi, (unsigned)(tri(3) >> 32) | hi};
    const v16u s1{0xFF00FF00u, 0xFF00FF00u, 0u, ~0u, hi, hi, 0u, O_XN + l * XB + own, own, smem, w == 2 ? spm : OOB, nb + DV(N_PTX + h * 2048), w == 1 ? 0u : OOB, w == 0 ? 0u : OOB,
                  w < 2 ? w * 256 : w == 2 ? spmm - 1536 : OOB, ~hi};
    const v16u s2{w < 2 ? w * 1024 : w == 2 ? spm : spmm, w < 2 ? 4u : 3u, w < 2 ? 0u : w == 2 ? 0x200u : 0x380u, w ? 0u : __float_as_uint(-3e38f), (unsigned)(L_Q + w * 1024), (unsigned)(L_Q + (w & 1) * 4096),
                  __float_as_uint(SC_L2E), __float_as_uint(-1e30f), na(nb, N_FX), w == 3 ? na(nb, N_PA + ((h >> 3) * 32 + 8 + (h & 7)) * 4) : OOB, (unsigned)(L_KM + (w >> 1) * 1024 + (w & 1) * 256),
                  0u, 0u, (unsigned)(L_SP + w * 256), O_PROF + (blockIdx.x * 4 + w) * 8192, na(nb, N_WM + h * 16384 + w * 4096)};
    asm volatile(A_LOOP :: "{v[0:7]}"(Wc[0]), "{v[8:15]}"(Wc[1]), "{v[16:23]}"(Wc[2]), "{v[24:31]}"(pv), "{s[8:23]}"(s0), "{s[24:39]}"(s1), "{s[40:55]}"(s2),
                 "{s58}"(ok), "{s[4:7]}"(again(id2, nb, w)) : "memory", "s99", "vcc");
}
constexpr int bkind(int n) { return (B_LDS >> n & 1) ? 1 : (B_VAR >> n & 1) ? 2 : 0; }
struct WMV { static constexpr int NT = 2, NP = 16, RB = 0, LB = 0, CBI = 0; static constexpr int kind(int j, int t) { return bkind(16 * j + t); } };
struct WMO { static constexpr int NT = 16, NP = 2, LB = __builtin_popcount((unsigned)B_LDS), CBI = __builtin_popcount((unsigned)B_VAR), RB = 8 * (32 - LB - CBI);
             static constexpr int kind(int j, int t) { return bkind(32 + 2 * j + t); } };
#ifndef B_LV
#define B_LV 24576
#endif
namespace lb { constexpr int L_V = B_LV, L_VM = B_LV + 4096, L_W = 65536; }
DEVI void role_B(const Args& a, int w, int l, int h, int rep, int id2) {
    asm volatile("; MARK role_B"); __builtin_amdgcn_s_waitcnt(0);
    using namespace lb;
    const int lane0 = lane_id();
    const rsrc_t rs = mkrs(a.ws, WS_NEED);
    const unsigned lane16 = lane0 * 16, wl = L_W + w * (12 * 2048) + lane16;
    v8u Wc[2];
    unsigned nb, fx, ok;
    const unsigned own = (w >> 1) * 32768 + (w & 1) * 4096;
    {
        const int lane = lane0;
        const unsigned ta = (lane >> 4) * 2048 + w * 256 + (lane & 15) * 16, tb = (lane >> 5) * 32768 + w * 2048 + (lane & 16) * 64 + (lane & 15) * 16;
        const rsrc_t r1 = mkrs(a.Wv, WSZ(8u << 20)), r2 = mkrs(a.Wo, WSZ(8u << 20));
        const unsigned v1 = (8 * w + ((lane >> 2) & 3)) * 8192 + h * 256 + (lane >> 4) * 64 + (lane & 3) * 16, v2 = (h * 128 + ((lane >> 2) & 7)) * 2048 + w * 512 + (lane >> 5) * 64 + (lane & 3) * 16;
        const unsigned lw = opqs(w * 2048);
        load_blocks<32, 4, 4, true, 3>([&](auto B, auto S) {
            constexpr int b = B.v & 15;
            if constexpr (B.v == 32) ok = first_there(fx);
            if constexpr (B.v < 32) pace(l);
            if constexpr (B.v >= 32) dma4(opqs(own + S.v * 8192), mkrs(a.ws, ok), lane16, opqs(O_XN + l * XB + own + S.v * 8192));
            else sfor<0, 2>([&](auto I) { if constexpr (B.v < 16) dma_blk(lw + S.v * 8192 + I.v * 32768, r1, v1, opqs((2 * b + I.v) * 32 * 8192), 2048);
                                     else dma_blk(lw + S.v * 8192 + I.v * 32768, r2, v2, opqs(((b & 3) * 32 + I.v * 16) * 2048 + (b >> 2) * 128), 1024); });
        }, [&](auto B, auto S) {
            constexpr int b = B.v & 15;
            sfor<0, 4>([&](auto T) { constexpr int o = S.v * 8192 + (T.v >> 1) * (B.v < 16 ? 32768 : 512) + (T.v & 1) * 8;
                                     if constexpr (B.v < 16) put_frag<WMV, T.v & 1, 2 * b + (T.v >> 1), o, o + 1024>(ta, wl, Wc); else put_frag<WMO, (b & 12) + T.v, b & 3, o, o + 256>(tb, wl, Wc); });
            if constexpr (B.v == 24) { nb = side(rs, l); first_look(fx, rs, nb); }
        });
        TSW(127, 2);
    }
    const int lane = lane_id(), c = lane & 15, g = lane >> 4;
    const unsigned wlo = (unsigned)(uintptr_t)a.ws, whi = (unsigned)((uintptr_t)a.ws >> 32);
    const unsigned prow = L_VM + w * 512 + ((c >> 2) & 1) * 256 + g * 64 + (c >> 3) * 32 + (c & 1) * 8 + ((c >> 1) & 1) * 4;
    const v4u pv{lane16, wl, ((lane & 2) ? 0xEEEEu : 0x4444u) | prow << 16, (unsigned)(L_VM + (g >> 1) * 512 + (lane & 3) * 64 + (g & 1) * 32 + ((lane >> 2) & 3) * 8)};
    const v16u s0{wlo, whi, RSZ, 0x20000u, wlo, whi, 0u, 0x20000u, OOB, OOB, OOB, OOB, ~1u, O_XN + l * XB + own, own, na(nb, N_MEM + rep * 16384 + w * 4096)};
    const v8u s1{na(nb, N_WM + h * 16384 + w * 4096), na(nb, N_U + h * 65536 + w * 16384), na(nb, N_FX), w == 3 ? na(nb, N_PA + ((h >> 3) * 32 + (h & 7)) * 4) : OOB,
                 (unsigned)(L_V + w * 1024), OOB, 0u, O_PROF + (blockIdx.x * 4 + w) * 8192 + 64};
    asm volatile(B_LOOP :: "{v[0:7]}"(Wc[0]), "{v[8:15]}"(Wc[1]), "{v[16:19]}"(pv), "{v20}"(fx), "{s[8:23]}"(s0), "{s[24:31]}"(s1), "{s32}"(1u), "{s[4:7]}"(again(id2, nb, w)) : "memory", "s99", "vcc");
}
template <class NB> DEVI void put_tile(v4u (&u)[8], rsrc_t rs, int tile, int w, int lane, NB&& nbf) {
    const unsigned vo = tile * 32768 + w * 8192 + lane * 16, red = (lane & 15) * 16;
    float s = 0.f;
#pragma unroll
    for (int f = 0; f < 8; ++f) {
#pragma unroll
        for (int i = 0; i < 4; ++i) s += bflo(u[f][i]) + bfhi(u[f][i]);
    }
    s = red32(red16(s));
    BAR;
    if (lane < 16) *LDSP(float, red + w * 4) = s;
    WL0; BAR;
    const f32x4 sa = __builtin_bit_cast(f32x4, ldsq(red, 0));
    const float mean = ((sa[0] + sa[1]) + (sa[2] + sa[3])) * (1.0f / 1024.0f);
    float q = 0.f;
#pragma unroll
    for (int f = 0; f < 8; ++f) {
        asm volatile("" : "+v"(u[f]));
#pragma unroll
        for (int i = 0; i < 4; ++i) { const float d0 = bflo(u[f][i]) - mean, d1 = bfhi(u[f][i]) - mean; q = fmaf(d0, d0, fmaf(d1, d1, q)); }
    }
    q = red32(red16(q));
    if (lane < 16) *LDSP(float, red + 256 + w * 4) = q;
    WL0; BAR;
    const f32x4 qa = __builtin_bit_cast(f32x4, ldsq(red, 256));
    const float mb = rbf(mean), inv = 1.0f / rbf(sqrtf(rbf(rbf(((qa[0] + qa[1]) + (qa[2] + qa[3])) * (1.0f / 1024.0f)) + 1e-5f)));
#pragma unroll
    for (int f = 0; f < 8; ++f) {
        v4u o;
        asm volatile("" : "+v"(u[f]));
#pragma unroll
        for (int i = 0; i < 4; ++i) { const unsigned d = pack2(bflo(u[f][i]) - mb, bfhi(u[f][i]) - mb); o[i] = pack2(bflo(d) * inv, bfhi(d) * inv); }
        st16(o, rs, O_XN + vo + f * 1024);
    }
    const unsigned nb = nbf();
    WV0;
    if (lane == 0) st4(0u, rs, na(nb, N_FX) + tile * 16 + w * 4, 0);
}
#ifndef SXL
#define SXL 1
#endif
#ifndef HLD
#define HLD 158
#endif
#ifndef SXH
#define SXH 30
#endif
#ifndef UIN
#define UIN 8
#endif
namespace lr { constexpr int PT1 = NV + 48, PT0 = NV + 80, PTM = NV + 96, VPM = NV, VMM = NV + 16, VW = NV + 28, VX = NV + 44, L_M = 1024, MD = 288, MW = 1536, L_T = 8192, VE = NV + 112; }
template <int R, int N, int OFF> DEVI void lda(rsrc_t rs, unsigned vo, unsigned so) {
    if constexpr (N == 4) asm volatile("buffer_load_dwordx4 a[%3:%4], %0, %1, %2 offen offset:%5 sc1" :: "v"(vo), "s"(rs), "s"(so), "n"(R), "n"(R + 3), "n"(OFF) : "memory");
    else asm volatile("buffer_load_dwordx2 a[%3:%4], %0, %1, %2 offen offset:%5 sc1" :: "v"(vo), "s"(rs), "s"(so), "n"(R), "n"(R + 1), "n"(OFF) : "memory");
}
template <int R, int N, int OFF> DEVI void ldv(rsrc_t rs, unsigned vo, unsigned so) {
    if constexpr (N == 4) asm volatile("buffer_load_dwordx4 v[%3:%4], %0, %1, %2 offen offset:%5 sc1" :: "v"(vo), "s"(rs), "s"(so), "n"(R), "n"(R + 3), "n"(OFF) : "memory");
    else asm volatile("buffer_load_dwordx2 v[%3:%4], %0, %1, %2 offen offset:%5 sc1" :: "v"(vo), "s"(rs), "s"(so), "n"(R), "n"(R + 1), "n"(OFF) : "memory");
}
template <int R> DEVI unsigned rdv() { unsigned t; asm volatile("v_mov_b32 %0, v[%1]" : "=v"(t) : "n"(R)); return t; }
template <int RA, int RB, bool Z, bool AV> DEVI void mfh(f32x4& d) {
    if constexpr (AV) { if constexpr (Z) asm volatile(M16 "%0, v[%1:%2], v[%3:%4], 0" : "=v"(d) : "n"(RA), "n"(RA + 3), "n"(RB), "n"(RB + 3));
                        else asm volatile(M16 "%0, v[%1:%2], v[%3:%4], %0" : "+v"(d) : "n"(RA), "n"(RA + 3), "n"(RB), "n"(RB + 3)); }
    else { if constexpr (Z) asm volatile(M16 "%0, a[%1:%2], v[%3:%4], 0" : "=v"(d) : "n"(RA), "n"(RA + 3), "n"(RB), "n"(RB + 3));
           else asm volatile(M16 "%0, a[%1:%2], v[%3:%4], %0" : "+v"(d) : "n"(RA), "n"(RA + 3), "n"(RB), "n"(RB + 3)); }
}
template <int RA, int RB, bool Z> DEVI void mfh16(f32x4& d) {
    if constexpr (Z) asm volatile("v_mfma_f32_16x16x16_bf16 %0, a[%1:%2], v[%3:%4], 0" : "=v"(d) : "n"(RA), "n"(RA + 1), "n"(RB), "n"(RB + 1));
    else asm volatile("v_mfma_f32_16x16x16_bf16 %0, a[%1:%2], v[%3:%4], %0" : "+v"(d) : "n"(RA), "n"(RA + 1), "n"(RB), "n"(RB + 1));
}
template <int R> DEVI u64 ltv(int need) { u64 m; asm volatile("v_cmp_lt_i32 %0, v[%2], %1" : "=s"(m) : "v"(need), "n"(R)); return m; }
template <int R> DEVI void ldv4(rsrc_t rs, unsigned vo, unsigned so) { asm volatile("buffer_load_dwordx4 v[%3:%4], %0, %1, %2 offen sc1" :: "v"(vo), "s"(rs), "s"(so), "n"(R), "n"(R + 3) : "memory"); }
#define SM_(E, A, B) "v_smfmac_f32_16x16x64_bf16 v[%[e]+" #E ":%[e]+" #E "+3], " A ", " B ", %[si]\n\t"
#define TG_(M, R) "v_bitop3_b32 v[%[e]+" #M "], v[%[e]+" #M "], v[" R "], %[tg] bitop3:0xf6\n\t"
#define PP_(P, N) "s_waitcnt vmcnt(" #N ")\n\t" SM_(0, "v[%[p]+4*" #P ":%[p]+4*" #P "+3]", "a[%[u]+16*" #P ":%[u]+16*" #P "+7]") TG_(8, "%[p]+4*" #P)
#define PQ_(P) SM_(4, "v[%[p]+4*" #P ":%[p]+4*" #P "+3]", "a[%[u]+16*" #P "+8:%[u]+16*" #P "+15]") TG_(9, "%[p]+4*" #P "+2")
#define ZE_ "v_mov_b64 v[%[e]:%[e]+1], 0\n\tv_mov_b64 v[%[e]+2:%[e]+3], 0\n\tv_mov_b64 v[%[e]+4:%[e]+5], 0\n\tv_mov_b64 v[%[e]+6:%[e]+7], 0\n\tv_mov_b64 v[%[e]+8:%[e]+9], 0\n\t"
template <int U0> DEVI unsigned chain(rsrc_t rs, int tgo, unsigned sp, unsigned sm, unsigned sw0, unsigned sw1, unsigned tg, unsigned spt, unsigned lane16, unsigned lane8, unsigned vmm, unsigned vw, int sidx,
                                      unsigned lred, unsigned lrr, unsigned vpub, v2u sxv, unsigned sxo) {
    using namespace lr;
    unsigned n, d;
    asm volatile(ZE_ "s_mov_b32 %[n], 0\n\ts_memrealtime s[98:99]\n\ts_add_u32 %[d], %[pt], 0x800\n\ts_waitcnt lgkmcnt(0)\n\ts_sub_i32 s99, %[t], s98\n\ts_lshl_b32 s99, s99, 8\n\ts_sub_u32 s99, s99, 1\n\t"
        "s_cmp_lt_u32 s99, 0x10000\n\ts_cbranch_scc1 2f\n\ts_branch 3f\n"
        "1:\n\ts_add_u32 %[n], %[n], 1\n\ts_cmp_lt_u32 %[n], %[cap]\n\ts_cbranch_scc1 4f\n\ts_endpgm\n4:\n\t" ZE_ "v_bfrev_b32 %[so], 1\n\ts_branch 3f\n"
        "2:\n\ts_memrealtime s[98:99]\n\ts_waitcnt lgkmcnt(0)\n\ts_sub_i32 s99, %[t], s98\n\ts_lshl_b32 s99, s99, 8\n\ts_cmp_gt_i32 s99, 0\n\ts_cbranch_scc1 2b\n"
        "3:\n\t"
        "buffer_load_dwordx4 v[%[p]:%[p]+3], %[l16], %[rs], %[sp] offen sc1\n\t"
        "buffer_load_dwordx4 v[%[p]+4:%[p]+7], %[l16], %[rs], %[sp] offen offset:1024 sc1\n\t"
        "buffer_load_dwordx4 v[%[p]+8:%[p]+11], %[l16], %[rs], %[sp] offen offset:2048 sc1\n\t"
        "buffer_load_dwordx4 v[%[p]+12:%[p]+15], %[l16], %[rs], %[sp] offen offset:3072 sc1\n\t"
        "buffer_load_dwordx2 v[%[p]+16:%[p]+17], %[vm], %[rs], %[sm] offen sc1\n\t"
        "buffer_load_dwordx2 v[%[p]+18:%[p]+19], %[vm], %[rs], %[sm] offen offset:128 sc1\n\t"
        "buffer_load_dwordx4 v[%[w]:%[w]+3], %[vw], %[rs], %[sw0] offen sc1\n\t"
        "buffer_load_dwordx4 v[%[w]+4:%[w]+7], %[vw], %[rs], %[sw1] offen sc1\n\t"
        "buffer_load_dwordx4 v[%[w]+8:%[w]+11], %[vw], %[rs], %[sw0] offen offset:256 sc1\n\t"
        "buffer_load_dwordx4 v[%[w]+12:%[w]+15], %[vw], %[rs], %[sw1] offen offset:256 sc1\n\t"
        PP_(0, 9) "buffer_store_dwordx2 %[sx], %[so], %[rs], 0 offen sc1\n\t" PQ_(0) PP_(1, 9) PQ_(1) PP_(2, 8) PQ_(2) PP_(3, 7) PQ_(3)
        "s_waitcnt vmcnt(5)\n\t" TG_(8, "%[p]+16") TG_(9, "%[p]+17") TG_(8, "%[p]+18") TG_(9, "%[p]+19")
        "s_waitcnt vmcnt(3)\n\t" SM_(0, "v[%[p]+16:%[p]+19]", "v[%[w]:%[w]+7]") TG_(8, "%[w]") TG_(9, "%[w]+4")
        "s_waitcnt vmcnt(1)\n\t" SM_(4, "v[%[p]+16:%[p]+19]", "v[%[w]+8:%[w]+15]") TG_(8, "%[w]+8") TG_(9, "%[w]+12")
        "v_bitop3_b32 v[%[e]+8], v[%[e]+8], v[%[e]+9], 1 bitop3:0xa8\n\tv_cmp_ne_u32 vcc, 0, v[%[e]+8]\n\ts_nop 0\n\t"
        "v_add_f32 v[%[e]], v[%[e]], v[%[e]+2]\n\tv_add_f32 v[%[e]+1], v[%[e]+1], v[%[e]+3]\n\tds_write2_b32 %[lw], v[%[e]], v[%[e]+1] offset1:2\n\ts_nop 0\n\t"
        "v_add_f32 v[%[e]+4], v[%[e]+4], v[%[e]+6]\n\tv_add_f32 v[%[e]+5], v[%[e]+5], v[%[e]+7]\n\tds_write2_b32 %[lw], v[%[e]+4], v[%[e]+5] offset0:%[md] offset1:%[md]+2\n\t"
        "s_cbranch_vccnz 1b\n\t"
        "buffer_load_dwordx4 v[%[t1]:%[t1]+3], %[l16], %[rs], %[pt] offen sc1\n\tbuffer_load_dwordx2 v[%[t0]:%[t0]+1], %[l8], %[rs], %[pt] offen offset:1024 sc1\n\t"
        "buffer_load_dwordx4 v[%[t1]+4:%[t1]+7], %[l16], %[rs], %[d] offen sc1\n\tbuffer_load_dwordx2 v[%[t0]+2:%[t0]+3], %[l8], %[rs], %[d] offen offset:1024 sc1\n\t"
        "s_waitcnt lgkmcnt(0)\n\ts_barrier\n\t"
        "ds_read2st64_b64 v[%[e]:%[e]+3], %[lr] offset1:3\n\tds_read2st64_b64 v[%[e]+4:%[e]+7], %[lr] offset0:6 offset1:9\n\ts_waitcnt lgkmcnt(1)\n\t"
        "v_add_f32 v[%[e]], v[%[e]], v[%[e]+2]\n\tv_add_f32 v[%[e]+1], v[%[e]+1], v[%[e]+3]\n\ts_waitcnt lgkmcnt(0)\n\t"
        "v_add_f32 v[%[e]+4], v[%[e]+4], v[%[e]+6]\n\tv_add_f32 v[%[e]+5], v[%[e]+5], v[%[e]+7]\n\tv_add_f32 v[%[e]], v[%[e]], v[%[e]+4]\n\tv_add_f32 v[%[e]+1], v[%[e]+1], v[%[e]+5]\n\t"
        "v_cvt_pk_bf16_f32 v[%[e]+2], v[%[e]], v[%[e]+1]\n\tv_min_u32 v[%[e]], -2, v[%[e]+2]\n\tv_min_u32 v[%[e]+1], -2, v[%[e]+2]\n\ts_nop 1\n\t"
        "v_permlane16_swap_b32 v[%[e]], v[%[e]+1]\n\tv_mov_b64 v[%[e]+2:%[e]+3], v[%[e]:%[e]+1]\n\ts_nop 1\n\t"
        "v_permlane32_swap_b32 v[%[e]], v[%[e]+2]\n\tv_permlane32_swap_b32 v[%[e]+1], v[%[e]+3]\n\t"
        "buffer_store_dwordx4 v[%[e]:%[e]+3], %[vp], %[rs], 0 offen sc1"
        : [n] "=&s"(n), [d] "=&s"(d), [so] "+v"(sxo)
        : [rs] "s"(rs), [t] "s"(tgo), [sp] "s"(sp), [sm] "s"(sm), [sw0] "s"(sw0), [sw1] "s"(sw1), [tg] "s"(tg), [pt] "s"(spt), [l16] "v"(lane16), [l8] "v"(lane8), [vm] "v"(vmm), [vw] "v"(vw), [si] "v"(sidx),
          [lw] "v"(lred), [lr] "v"(lrr), [vp] "v"(vpub), [sx] "v"(sxv), [e] "n"(VE), [p] "n"(VPM), [w] "n"(VW), [u] "n"(U0), [t1] "n"(PT1), [t0] "n"(PT0), [md] "n"(MD / 4), [cap] "n"(SPIN_CAP)
        : "memory", "scc", "vcc", "s98", "s99");
    return n;
}
template <int R> DEVI void tagd(unsigned& m, unsigned t) { asm volatile("v_bitop3_b32 %0, %0, v[%2], %1 bitop3:0xf6" : "+v"(m) : "s"(t), "n"(R)); }
#ifndef DLY_R
#define DLY_R 1200
#endif
#ifndef DLY_X
#define DLY_X 1300
#endif
DEVI unsigned role_R(const Args& a, int w, int l, int T2, int t0) {
    asm volatile("; MARK role_R"); __builtin_amdgcn_s_waitcnt(0);
    using namespace lr;
    const int lane = lane_id(), c = lane & 15, g = lane >> 4;
    const rsrc_t rs = mkrs(a.ws, WS_NEED);
    const unsigned lane16 = lane * 16;
#pragma nounroll
    for (int k = 0; k < 4 * l; ++k) {
        const int tile = k * 32 + T2;
        if (tile >= LN_NOW) hold(t0, DLY_X);
        v4u u[8];
#pragma unroll
        for (int f = 0; f < 8; ++f) u[f] = *(const v4u*)(a.x + (size_t)(tile * 16 + c) * 1024 + (w * 8 + f) * 32 + g * 8);
        if (tile < 2 * NSEG) put_tile(u, rs, tile, w, lane, [&]() { hold(t0, DLY_V); return side(rs, 0); });
        TSW(127, 1 + k);
    }
    auto arm = [&](unsigned n2, int s) {
        if (s < 8)
#pragma unroll
            for (int k = 0; k < 4; ++k) st16(v4u{SENT, SENT, SENT, SENT}, rs, na(n2, N_WM + s * 524288 + T2 * 16384 + w * 4096) + k * 1024 + lane16);
        st16(v4u{SENT, SENT, SENT, SENT}, rs, w < 2 ? na(n2, N_PTX + s * 65536 + T2 * 2048) + w * 1024 + lane16 : lane >= (w == 2 ? 32 : 8) || s >= 8 ? OOB :
                                              na(n2, w == 2 ? N_PM + s * 16384 + T2 * 512 : N_PMM + s * 8192 + T2 * 128) + lane16);
    };
    hold(t0, DLY_R);
    const unsigned nb = side(rs, l), vmem = c < 8 ? na(nb, N_MEM + T2 * 512) + g * 65536 + w * 128 + c * 16 : OOB;
    auto put_mem = [&](v4u v, int s) { st16(v, rs, vmem + rslot(s) * (NREP * 32768)); };
    {
        const unsigned* m0 = (const unsigned*)(a.mem0 + ((w & 1) * 4 + ((lane >> 2) & 1) * 2 + (lane & 1)) * 1024 + T2 * 32 + (w >> 1) * 16 + ((lane >> 1) & 1) * 2);
        const v4u mv{nosent(m0[0]), m0[2], m0[4], m0[6]};
        put_mem(v4u{SENT, SENT, SENT, SENT}, 1); put_mem(v4u{SENT, SENT, SENT, SENT}, 2);
#pragma nounroll
        for (int k = 0; k < 16; ++k) arm(nb, k);
        WV0; BAR;
        put_mem(mv, 0);
    }
    const int sidx = opq((lane & 2) ? 0xEEEE : 0x4444);
    const unsigned vmm = (8 * w + 2 * g) * 128 + c * 8, vw = (8 * w + g) * 32768 + DV(T2 * 512) + c * 16, vptm = g * 2048 + 1536 + c * 16;
    const unsigned sfl = na(nb, N_PA + (unsigned)((T2 & (NC - 1)) * 4 + w) * 128), su0 = na(nb, N_U + 8 * w * 65536 + T2 * 2048), vfl = sfl + c * 4;
    const unsigned lred = L_M + w * MW + ((c >> 2) & 1) * 2 * MD + (c >> 3) * 128 + g * 32 + ((c >> 1) & 1) * 16 + (c & 1) * 4, lrr = L_M + g * MD + w * 64 + (c & 7) * 8;
    auto ld_u = [&](auto B, int s) {
        const unsigned so = su0 + rslot(s) * (4u << 20);
        sfor<0, 16>([&](auto I) { constexpr int j = I.v >> 1, e = I.v & 1; lda<64 * B.v + 16 * (j >> 1) + 8 * e + 4 * (j & 1), 4, e * 1024>(rs, lane16, so + j * 131072); });
    };
    wait_prog(rs, sfl + (lane & 7) * 4, 0, 0);
    ld_u(IC<0>{}, 0);
    const rsrc_t rxi = l ? rs : mkrs(a.x, XB);
    const unsigned vxi = l ? lane16 : c * 2048 + g * 16, sxi = (w & 1) * 32768 + (l ? O_XIN + XB + T2 * 1024 : T2 * 64);
    v2u sxv{0u, 0u};
    unsigned sxo = OOB;
    int tpub = t0;
    auto step = [&](auto B, int s) {
        constexpr int b = B.v;
        TSD;
        TSR(0); TSK(15);
        if (s == 8) { TSC(126, 0); TSW(126, 1); }
        if (s == 56) { TSC(126, 2); TSW(126, 3); }
        const unsigned spt = na(nb, N_PTX + (unsigned)((s & 15) * 32 + 8 * w) * 2048);
        auto ptx = [&](auto N) {
            constexpr int n = N.v & 31, j = n >> 1;
            if constexpr (n == 20) ldv4<VX>(rxi, vxi, sxi + s * 65536);
            else if constexpr (n >= 16) ldv<PTM + 4 * (n - 16), 4, (n & 1) * 256>(rs, vptm, spt + (j - 8) * 16384);
            else if constexpr (n & 1) ldv<PT0 + 2 * j, 2, 1024>(rs, lane * 8, spt + DV(j * 2048));
            else ldv<PT1 + 4 * j, 4, 0>(rs, lane16, spt + DV(j * 2048));
        };
        {
            const unsigned sw = opqs(na(nb, N_WM + rslot(s) * 524288)), sw1 = opqs(sw + 131072), sp = opqs(na(nb, N_PM + w * 4096) + rslot(s) * 32768), sm = opqs(na(nb, N_PMM) + rslot(s) * 16384);
            const unsigned vpub = opq(vmem + rslot(s + 1) * (NREP * 32768)), tg = opqs((s >> 3) & 1), spq = opqs(spt);
            SB;
            if ((int)__builtin_amdgcn_s_memrealtime() - tpub < HLD) { wvm<16 - UIN>(); st8(sxv, rs, sxo, 0); sxo = OOB; hold(tpub, HLD); }
            const int mx = wait_hint(rs, sfl, vfl, s + 1);
            if (left(mx) > SXH * 256) { wvm<16 - UIN>(); st8(sxv, rs, sxo, 0); sxo = OOB; }
            TSR(5);
            const unsigned spins = chain<64 * b>(rs, mx, sp, sm, sw, sw1, tg, spq, lane16, lane * 8, vmm, vw, sidx, lred, lrr, vpub, sxv, sxo);
#if defined(TS) && TS != 4
            tsr[8] = __builtin_amdgcn_readfirstlane(spins) + 1;
#endif
            tpub = (int)__builtin_amdgcn_s_memrealtime();
            TSR(2);
        }
        sfor<4, 21>(ptx);
        BAR;
        f32x4 t[2][2], u[2];
        const unsigned su = s + 1 < NSEG ? su0 + rslot(s + 1) * (4u << 20) : OOB;
        put_mem(v4u{SENT, SENT, SENT, SENT}, s + 3);
#pragma nounroll
        for (int n = 0;; ++n) {
            if (n) { sfor<32, 52>(ptx); WV0; }
            auto lu = [&](auto N) { constexpr int j = N.v >> 1, e = N.v & 1; lda<16 * (j >> 1) + 8 * e + 4 * (j & 1) + 64 * (1 - b), 4, e * 1024>(rs, lane16, su + j * 131072); };
            TSR(6);
            unsigned mt = opq(0);
            const unsigned tp = (s >> 4) & 1;
            sfor<0, 16>([&](auto N) {
                constexpr int j = N.v >> 1, e = N.v & 1, R = 64 * b + 16 * (j >> 1) + 8 * e + 4 * (j & 1), nu = N.v * UIN / 16;
                if constexpr (e == 0) wvm<21 - 2 * j + (j < 2) + nu - 1>();
                if constexpr ((N.v + 1) * UIN / 16 > nu) lu(IC<nu>{});
                mfh<R, PT1 + 4 * j, j == 0, false>(t[1][e]); tagd<e ? PT0 + 2 * j : PT1 + 4 * j>(mt, tp); mfh16<R, PT0 + 2 * j, j == 0>(u[e]);
            });
            wvm<1 + UIN>();
            sfor<0, 8>([&](auto N) { constexpr int q = N.v >> 2, rt = (N.v >> 1) & 1, e = N.v & 1; mfh<VW + 8 * e + 4 * q, PTM + 4 * (2 * q + rt), N.v < 2, true>(t[rt][e]);
                                     tagd<PTM + 2 * N.v>(mt, tp); tagd<PTM + 2 * N.v + 1>(mt, tp); });
            nop<7>(); fence(t[0][0], t[0][1], t[1][0], t[1][1], u[0], u[1]);
#pragma unroll
            for (int e = 0; e < 2; ++e)
#pragma unroll
                for (int i = 0; i < 4; ++i) t[0][e][i] = fadd(t[0][e][i], u[e][i]);
            if (__builtin_amdgcn_uicmp(mt & 1, 0u, 33) == 0 || n == 2) break;
            TSV(s, 7, 1);
            if (n == 1) wait_prog(rs, sfl + 32 + (lane & 7) * 4, 0, (s + 2) << 24);
        }
        TSR(3);
#pragma unroll
        for (int e = 0; e < 2; ++e) { stsq(lane16 + w * 4096, L_T + e * 1024, __builtin_bit_cast(v4u, t[0][e])); stsq(lane16 + w * 4096, L_T + 2048 + e * 1024, __builtin_bit_cast(v4u, t[1][e])); }
        WL0; BAR;
        if (w < 2) {
            float o[8], s1 = 0.f, s2 = 0.f;
#pragma unroll
            for (int e = 0; e < 2; ++e) {
                f32x4 q[4];
#pragma unroll
                for (int k = 0; k < 4; ++k) q[k] = __builtin_bit_cast(f32x4, ldsq(lane16 + w * 2048, L_T + k * 4096 + e * 1024));
#pragma unroll
                for (int i = 0; i < 4; ++i) o[4 * e + i] = fadd(fadd(q[0][i], q[1][i]), fadd(q[2][i], q[3][i]));
            }
            v4u x1;
            sfor<0, 4>([&](auto I) {
                constexpr int i = I.v;
                const unsigned x = rdv<VX + i>(), at = pack2(o[2 * i], o[2 * i + 1]);
                x1[i] = pack2(bflo(x) + bflo(at), bfhi(x) + bfhi(at));
                const float lo = bflo(x1[i]), hi = bfhi(x1[i]);
                s1 += lo + hi; s2 = fmaf(lo, lo, fmaf(hi, hi, s2));
            });
            st16(x1, rs, O_XM + l * XB + s * 65536 + w * 32768 + T2 * 1024 + lane16);
            sxv = v2u{nosent(__float_as_uint(red32(red16(s1)))), __float_as_uint(red32(red16(s2)))};
            sxo = lane < 16 ? O_SX + (unsigned)((l * 64 + s) * 32 + T2) * 256 + (SXL ? w * 128 + c * 8 : c * 16 + w * 8) : OOB;
        }
        TSR(4);
        {
            sfor<UIN, 16>([&](auto N) { constexpr int j = N.v >> 1, e = N.v & 1; lda<16 * (j >> 1) + 8 * e + 4 * (j & 1) + 64 * (1 - b), 4, e * 1024>(rs, lane16, su + j * 131072); });
        }
#if TS != 4
        TSF(s, 0x817D);
#endif
    };
#pragma nounroll
    for (int s = 0; s < NSEG; s += 2) { step(IC<0>{}, s); step(IC<1>{}, s + 1); }
    wvm<16 - UIN>(); st8(sxv, rs, sxo, 0);
    WV0; BAR;
    return nb;
}
#ifndef FSIDE
#define FSIDE 1
#endif
#ifndef PDX
#define PDX 0
#endif
constexpr int NU = 2 * NSEG;
#ifndef PSF
#define PSF 24
#endif
#ifndef PSR
#define PSR 6
#endif
#ifndef NRD
#define NRD 4
#endif
#ifndef RPA
#define RPA 8
#endif
#ifndef GROT
#define GROT 1
#endif
#ifndef LAGK
#define LAGK 4
#endif
#ifndef LAGP
#define LAGP 0
#endif
#ifndef N2F
#define N2F 43
#endif
#ifndef N2M
#define N2M 9
#endif
DEVI unsigned unit_ls(unsigned u, int two) {
    static_assert(NSEG == 64 && LAGD >= 6, "");
    static_assert(LAGP % 2 == 0 && LAGP < 2 * LAGK + 1, "");
    const unsigned d = u - LAGD + LAGP, p = d / (2 * LAGK + 1), r = d % (2 * LAGK + 1), n0 = LAGD - LAGP / 2 + p * (LAGK + 1) + (r >> 1);
    const unsigned n = u < LAGD || n0 >= 64 ? u : ((r & 1) || r == 2 * LAGK) ? n0 : 64 + u - n0;
    return (two ? 64 + N2F + 2 * u : n < 64 + N2F ? n : n + min(n - (63 + N2F), (unsigned)N2M)) & 127;
}
DEVI unsigned unit_tab(int lane, int two) { return opq(unit_ls(2 * lane, two) | unit_ls(2 * lane + 1, two) << 8); }
DEVI unsigned unit_get(unsigned tab, int u) { return ((unsigned)__builtin_amdgcn_readlane(tab, (u >> 1) & 63) >> ((u & 1) * 8)) & 255; }
struct WMG { static constexpr int NT = 3, NP = 16, RB = 0, LB = 0, CBI = 0; static constexpr int kind(int, int) { return 0; } };
struct WMD { static constexpr int NT = 4, NP = 12, RB = 0, LB = 0, CBI = 0; static constexpr int kind(int, int) { return 0; } };
namespace lg { constexpr int L_ST = 155648, L_OK = 159744; }
DEVI void role_GU(const Args& a, int w, int j, int two) {
    asm volatile("; MARK role_GU"); __builtin_amdgcn_s_waitcnt(0);
    using namespace lg;
    const int lane = lane_id(), c = lane & 15, g = lane >> 4;
    const rsrc_t rs = mkrs(a.ws, WS_NEED);
    const unsigned lane16 = lane * 16, hc = 96 * j + 24 * w;
    {
        const unsigned ta = g * 4096 + (lane & 1) * 2048 + w * 256 + ((lane >> 2) & 3) * 64 + ((lane >> 1) & 1) * 8, tb = ta + 65536;
        const rsrc_t r1 = mkrs(a.Wg, WSZ(7168u * 1024)), r2 = mkrs(a.Wu, WSZ(7168u * 1024));
        const unsigned cb = 192 * j + g * 48 + (lane & 3) * 16, v1 = (lane & 3) == 3 || cb >= 7168 ? OOB : (8 * w + ((lane >> 2) & 3)) * 7168 + cb, lw = opqs(w * 4096), z = opqs(0);
        load_blocks<32, 8, 4, true>([&](auto B, auto S) { dma_blk(lw + S.v * 16384, r1, v1, z + B.v * 32 * 7168, 1792); dma_blk(lw + S.v * 16384 + 2048, r2, v1, z + B.v * 32 * 7168, 1792); },
                                    [&](auto B, auto S) { sfor<0, 3>([&](auto T) { constexpr int o = (S.v & 3) * 16384 + T.v * 16; put_frag<WMG, T.v, B.v, o, o + 1024>(S.v < 4 ? ta : tb, 0, nullptr); }); });
        if (!two) TSW(120 - 65536, 0);
        v4u one{0x3F803F80u, 0x3F803F80u, 0x3F803F80u, 0x3F803F80u};
        asm volatile("s_nop 1" : "+v"(one));
        f32x4 cs[3];
        sfor<0, 96>([&](auto N) { constexpr int t = N.v % 3, ks = N.v / 3; mfA<8 * widx<WMG>(t, ks >> 1) + 4 * (ks & 1), ks == 0>(cs[t], one); });
        nop<7>(); fence(cs[0], cs[1], cs[2]);
        sfor<0, 3>([&](auto T) { mwr4<384 + 4 * T.v>(__builtin_bit_cast(v4u, cs[T.v])); });
        BAR;
    }
    const unsigned nf = two ? O_N + PG2 + (((two >> 1) ^ FSIDE) & 1) * 8192 : side(rs, FSIDE);
    const unsigned wlo = (unsigned)(uintptr_t)a.ws, whi = (unsigned)((uintptr_t)a.ws >> 32);
    const unsigned xr = w * 8192 + (GROT ? (j & 1) * 4096 + (j & 2) * 16384 : 0);
    const v8u pv{lane16, O_SX + (8 * w + g) * 256 + c * 16, O_H + (g & 1) * 114688 + hc * 32 + c * 16 + (g >> 1) * 8, lane == 0 ? na(nf, N_PG) + j * 128 + w * 4 : OOB,
                 (unsigned)(L_ST + ((lane & 1) * 16 + ((lane & 31) >> 1)) * 16), L_ST + lane16 + w * 4, unit_tab(lane, two), lane16};
    const v16u s0{wlo, whi, RSZ, 0x20000u, wlo, whi, 0u, 0x20000u, OOB, OOB, hc < 3584 ? 0u : OOB, hc + 8 < 3584 ? 0u : OOB, 0u, O_XM + xr, 65536 + xr, 0u};
    const v3u s1{0u, O_PROF + ((blockIdx.x & 255) * 4 + w) * 8192, (unsigned)(two ? N2M : NU - N2M)};
    asm volatile(G_LOOP :: "{v[0:7]}"(pv), "{s[8:23]}"(s0), "{s[24:26]}"(s1) : "memory", "s99", "vcc");
}
DEVI void role_DN(const Args& a, int w, int p, int cg, int two) {
    asm volatile("; MARK role_DN"); __builtin_amdgcn_s_waitcnt(0);
    using namespace lg;
    const int lane = lane_id();
    const rsrc_t rs = mkrs(a.ws, WS_NEED);
    const unsigned lane16 = lane * 16;
    const int F0 = p < 3 ? 23 * p : (p == 3 ? 68 : 89), zb = p < 2 ? -1 : (p == 4 ? 0 : 22);
    {
        const unsigned ta = w * 18432 + (lane >> 4) * 512 + ((lane >> 2) & 3) * 64 + (lane & 3) * 16;
        const rsrc_t r1 = mkrs(a.Wd, WSZ(3584u * 2048));
        const unsigned v1 = (F0 * 32 + (lane >> 2)) * 2048 + cg * 512 + w * 128 + (lane & 3) * 16;
        load_blocks<46, 9, 2>([&](auto B, auto S) { dma_blk(w * 18432 + S.v * 2048, r1, (B.v >> 1) == zb ? OOB : v1, (B.v >> 1) * 65536 + (B.v & 1) * 64, 2048); },
                              [&](auto B, auto S) { constexpr int ks = B.v >> 1, t = 2 * (B.v & 1), o = S.v * 2048;
                                                    put_frag<WMD, t, ks, o, o + 256>(ta, 0, nullptr); put_frag<WMD, t + 1, ks, o + 8, o + 264>(ta, 0, nullptr); });
        if (!two) TSW(120 - 65536, 0);
        BAR;
    }
    const int g0 = F0 / 3 * 4, gn = ((F0 + 22) / 3 + 1) * 4 - g0;
    const unsigned nf = two ? O_N + PG2 + (((two >> 1) ^ FSIDE) & 1) * 8192 : side(rs, FSIDE);
    const unsigned wlo = (unsigned)(uintptr_t)a.ws, whi = (unsigned)((uintptr_t)a.ws >> 32);
    const v8u pv{lane16, na(nf, N_PG) + g0 * 32 + (lane < gn ? (lane >> 2) * 128 + (lane & 3) * 4 : 0), lane == 0 ? na(nf ^ PDX, N_PD) + (cg * NKF + p) * 128 + w * 4 : OOB,
                 unit_tab(lane, two), lane16, (unsigned)(L_OK + lane * 4), (unsigned)((lane & 8) * 2048 + (lane >> 4) * 128 + (lane & 7) * 16), 0u};
    const v16u s0{wlo, whi, RSZ, 0x20000u, wlo, whi, 0u, 0x20000u, wlo, whi, w == 0 ? RSZ : 0u, 0x20000u, 0u, O_H + (w >> 1) * 114688 + (F0 + (w & 1) * 12) * 1024,
                  O_D + p * 65536 + (cg * 8 + w * 2) * 512, (unsigned)(65536 + (w >> 1) * 32768 + (w & 1) * 12288)};
    const v4u s1{(unsigned)(L_OK + w * 256), 0u, 0u, O_PROF + ((blockIdx.x & 255) * 4 + w) * 8192};
    asm volatile(D_LOOP :: "{v[0:7]}"(pv), "{s[8:23]}"(s0), "{s[24:27]}"(s1), "{s28}"(two ? N2M : NU - N2M) : "memory", "s99", "vcc");
}
template <int R> DEVI void ldv1(rsrc_t rs, unsigned vo, unsigned so) { asm volatile("buffer_load_dword v[%3], %0, %1, %2 offen sc1" :: "v"(vo), "s"(rs), "s"(so), "n"(R) : "memory"); }
template <int R, int I> DEVI void unp2(f32x4& d) { float x, y; asm volatile("v_lshlrev_b32 %0, 16, v[%2]\n\tv_and_b32 %1, 0xffff0000, v[%2]" : "=v"(x), "=v"(y) : "n"(R)); d[I] = x; d[I + 1] = y; }
DEVI void role_RED(const Args& a, int w, int cu, unsigned nf) {
    asm volatile("; MARK role_RED"); __builtin_amdgcn_s_waitcnt(0);
    const int lane0 = lane_id(), c = lane0 & 15, g = lane0 >> 4;
    const rsrc_t rs = mkrs(a.ws, WS_NEED);
    v4u sel[2];
#pragma unroll
    for (int e = 0; e < 2; ++e)
#pragma unroll
        for (int i = 0; i < 4; ++i) sel[e][i] = (g == (c >> 2) && i == 2 * e + ((c >> 1) & 1)) ? ((c & 1) ? 0x3F800000u : 0x3F80u) : 0u;
    const unsigned vfl = na(nf ^ PDX, N_PD) + w * (NKF * 128) + (lane0 < 20 ? (lane0 >> 2) * 128 + (lane0 & 3) * 4 : 0);
    constexpr int VP = NV + 32;
    const unsigned lo = (c & 8) * 128 + g * 256 + (c & 7) * 16 + (cu & 1) * 128 + w * 8192, ld = (c & 8) * 64 + g * 128 + (c & 7) * 16 + (cu & 1) * 16384 + w * 4096;
    const unsigned lout = ((cu & 1) * 8 + (c & 7)) * 2048 + w * 512 + (c & 8) * 8 + g * 16, lw = g == (c >> 2) ? (c & 7) * 64 + w * 16 + (c & 8) : 1024 + lane0 * 8;
    const rsrc_t ro = mkrs(a.out, 4u << 20);
    v4u one{0x3F803F80u, 0x3F803F80u, 0x3F803F80u, 0x3F803F80u};
    asm volatile("" : "+v"(one));
    #pragma unroll
    for (int i = 0; i < 2; ++i) if (2 * lane0 + i < NU - N2M) *LDSP(unsigned char, 2048 + unit_ls(2 * lane0 + i, 0)) = 2 * lane0 + i;
    if (lane0 < N2M) *LDSP(unsigned char, 2048 + unit_ls(lane0, 1)) = 128 + lane0;
    WL0; BAR;
    const unsigned tab = opq(*LDSP(const unsigned short, 2048 + 2 * lane0));
    auto next = [&](int k) { return k >= 256 ? k + 32 : k + NRD < 256 ? k + NRD : 4 * NU; };
    auto issue = [&](auto B, auto N0, auto N1, int k) {
        const unsigned ls = k >> 2, base = ((k >> 1) & 1) * 32768;
        sfor<N0.v, N1.v>([&](auto N) { constexpr int f = (N.v - 4) / 5, p = (N.v - 4) % 5;
            if constexpr (N.v < 4) ldv4<NV + 16 * B.v + 4 * N.v>(rs, lo, O_XM + ls * 65536 + base + N.v * 2048);
            else lda<80 * B.v + 20 * f + 4 * p, 4, f * 1024>(rs, ld, O_D + (ls * NKF + p) * 65536 + base); });
        if constexpr (N1.v == 24) { const int k2 = next(k); ldv1<VP>(rs, vfl, k2 < 4 * NU ? (unit_get(tab, k2 >> 2) >> 7) * PG2 : 0u); }
    };
    u64 late = 1, far = 1;
    bool pre = false;
    auto task = [&](auto B, int k) {
        constexpr int b = B.v;
        const int ut = unit_get(tab, k >> 2), u = ut & 127, rt = (k >> 1) & 1, kn = next(k), un = kn < 4 * NU ? unit_get(tab, kn >> 2) : 127, nx = (un & 127) + 1;
        const unsigned sf = (ut >> 7) * PG2, sfn = (un >> 7) * PG2;
        const unsigned ls = k >> 2, tile = ls * 2 + rt;
        TSD; TSR(0);
        if (!pre) {
            if (late) {
                unsigned spins = 0;
#pragma nounroll
                for (;;) {
                    if (far) __builtin_amdgcn_s_sleep(PSF); else __builtin_amdgcn_s_sleep(PSR);
                    CB;
                    const int v = (int)ld4(rs, vfl, sf);
                    if (__builtin_amdgcn_sicmp(v, u + 1, 40) == 0) break;
                    far = __builtin_amdgcn_sicmp(v, u, 40);
                    spin_bail(spins);
                }
            }
            TSR(1);
            issue(B, IC<0>{}, IC<24>{}, k);
        }
        else { TSR(1); }
        const bool l0 = ls < 64;
        const rsrc_t rd = l0 ? rs : ro;
        const unsigned vd = opq((l0 ? O_XIN + XB + lo : lout - 64 * 65536) + tile * 32768), fs = l0 ? 2048 : 128;
        v4u x[4];
        f32x4 d[4][2], S, Q;
        auto unp = [&](auto F, auto I) { unp2<NV + 16 * b + 4 * F.v + I.v, 2 * (I.v & 1)>(d[F.v][I.v >> 1]); };
        auto fin = [&](auto F) {
            constexpr int f = F.v;
            fence(d[f][0], d[f][1]);
#pragma unroll
            for (int i = 0; i < 4; ++i) x[f][i] = pack2(d[f][i >> 1][2 * (i & 1)], d[f][i >> 1][2 * (i & 1) + 1]);
            asm volatile("" : "+v"(x[f]));
        };
        auto put = [&](auto F) { constexpr int f = F.v; st16s(x[f], rd, vd, f * fs); SB; mfv<f == 0>(S, one, x[f]); mfv<f == 0>(Q, x[f], x[f]); SB; };
        wvm<24>();
        sfor<0, 4>([&](auto I) { unp(IC<0>{}, I); });
        sfor<0, 4>([&](auto F) {
            constexpr int f = F.v;
            wvm<16 - 5 * f + (f ? f - 1 : 0)>();
            nop<1>();
            sfor<0, 10>([&](auto N) {
                mfB<80 * b + 20 * f + 4 * (N.v >> 1), false>(d[f][N.v & 1], sel[N.v & 1]);
                if constexpr (N.v < 4 && f < 3) unp(IC<(f + 1) & 3>{}, N);
                if constexpr (N.v == 5 && f > 0) fin(IC<(f + 3) & 3>{});
                SB;
            });
            if constexpr (f > 0) put(IC<(f + 3) & 3>{});
        });
        wvm<3>();
        TSR(2);
#ifdef TS
        tsr[4] = pre + 1;
#endif
        const bool p1 = ltv<VP>(nx) == 0;
        if (!p1) ldv1<VP + 1>(rs, vfl, sfn);
        nop<3>();
        fin(IC<3>{});
        nop<1>();
        put(IC<3>{});
        auto second = [&](auto NS) {
            wvm<NS.v>();
            pre = ltv<VP + 1>(nx) == 0;
            if (pre) { issue(IC<1 - b>{}, IC<0>{}, IC<24>{}, kn); if constexpr (NS.v == 5) wvm<25>(); }
            else {
                if constexpr (NS.v == 5) { ldv1<VP + 1>(rs, vfl, sfn); WV0; }
                late = ltv<VP + 1>(nx); far = ltv<VP + 1>(nx - 1);
            }
        };
        if (l0) {
            nop<7>();
            fence(S, Q);
            const float qd = selm(selm(Q[0], Q[1], 0xAAAAAAAAAAAAAAAAull), selm(Q[2], Q[3], 0xAAAAAAAAAAAAAAAAull), 0xCCCCCCCCCCCCCCCCull);
            *LDSP(f32x2, lw) = f32x2{S[0], qd};
            if (p1) issue(IC<1 - b>{}, IC<0>{}, IC<RPA>{}, kn);
            f32x2 xf[4][4];
#pragma unroll
            for (int f = 0; f < 4; ++f)
#pragma unroll
                for (int i = 0; i < 4; ++i) { xf[f][i] = f32x2{bflo(x[f][i]), bfhi(x[f][i])}; asm volatile("" : "+v"(xf[f][i])); }
            TSR(5);
            WL0; BAR;
            f32x4 t[4];
#pragma unroll
            for (int i = 0; i < 4; ++i) t[i] = __builtin_bit_cast(f32x4, ldsq((c & 7) * 64, i * 16));
            const f32x4 t4 = (t[0] + t[1]) + (t[2] + t[3]);
            const float mean = (t4[0] + t4[2]) * (1.0f / 1024.0f), var = fmaxf(rbf(fmaf(t4[1] + t4[3], 1.0f / 1024.0f, -mean * mean)), 0.f);
            const float inv = __builtin_amdgcn_rcpf(rbf(__builtin_amdgcn_sqrtf(rbf(var + 1e-5f)))), mi = -rbf(mean) * inv;
#pragma unroll
            for (int f = 0; f < 4; ++f) {
                v4u o;
#pragma unroll
                for (int i = 0; i < 4; ++i) xf[f][i] = __builtin_elementwise_fma(xf[f][i], f32x2{inv, inv}, f32x2{mi, mi});
#pragma unroll
                for (int i = 0; i < 4; ++i) o[i] = pack2(xf[f][i][0], xf[f][i][1]);
                st16(o, rs, vd + (O_XN - O_XIN) + f * 2048);
            }
            TSR(6);
            if (p1) { issue(IC<1 - b>{}, IC<RPA>{}, IC<24>{}, kn); TSR(7); wvm<25 - RPA>(); pre = true; } else second(IC<5>{});
            BAR;
            st4(0u, rs, lane0 < 2 && w == 0 ? na(nf ^ (FSIDE ? 0 : 8192), N_FX) + tile * 16 + (cu & 1) * 8 + lane0 * 4 : OOB, 0);
        }
        else if (p1) { issue(IC<1 - b>{}, IC<0>{}, IC<24>{}, kn); pre = true; }
        else second(IC<1>{});
        TSR(3); TSF(cu > 255 ? 64 + (k >> 5 & 7) : u, 0xFF);
    };
#pragma nounroll
    for (int k = cu;;) {
        task(IC<0>{}, k); k = next(k);
        if (k >= 4 * NU) break;
        task(IC<1>{}, k); k = next(k);
        if (k >= 4 * NU) break;
    }
}
#ifndef LSW
#define LSW 0
#endif
#ifndef FNG
#define FNG 0x34344776u
#endif
#ifndef FNS
#define FNS 0x00000112u
#endif
#ifndef DLY_A
#define DLY_A 250
#endif
#ifndef DLY_B
#define DLY_B 0
#endif
#ifndef DLY_1
#define DLY_1 1600
#endif
#ifndef DLY_G
#define DLY_G 1400
#endif
#ifndef DLY_D
#define DLY_D 1600
#endif
__global__ __launch_bounds__(256, 1) __attribute__((amdgpu_num_vgpr(NV))) void mega6(Args a) {
    asm volatile("" ::: "v255", "a255");
    const int w = __builtin_amdgcn_readfirstlane(threadIdx.x >> 6), b = blockIdx.x, xcd = b & 7, slot = b >> 3, l = (xcd >> 2) ^ LSW, idx = (slot & 7) * 4 + (xcd & 3);
    const int t0 = (int)__builtin_amdgcn_s_memrealtime();
    if (b < 256) TSW(127, 0);
    const int f = slot - 24, two = b < 256 ? 0 : 1 + ((b >> 8) & 2), k2 = b & 255;
    const int ia = l || !N2M || idx >= 26 ? -1 : idx < 20 ? 38 + (idx & 3) * 5 + (idx >> 2) : 12 + idx, ib = l || !N2M ? -1 : (idx & 3) * 8 + (idx >> 2);
    const int ng = (FNG >> 4 * xcd) & 15, ns = (FNS >> 4 * xcd) & 15, below = (1 << 4 * xcd) - 1;
    auto nib = [](unsigned x) { x = (x & 0x0F0F0F0F) + ((x >> 4) & 0x0F0F0F0F); return (int)((x * 0x01010101) >> 24); };
    int gu = f < 0 || (xcd >= 3 && f >= 4) ? -1 : f < ng ? nib(FNG & below) + f : -2, dn = (xcd - 3) * 4 + f - 4, cu = gu == -2 && f < ng + ns && !two ? nib(FNS & below) + f - ng : -1;
    if (two) { gu = k2 < 38 ? k2 : -1; dn = k2 - 38; }
    if (slot < 24 && l && (slot < 16 || idx >= LN_NOW)) pull_all(a, w, (slot >> 3) * 32 + idx - (slot < 16 ? 0 : LN_NOW), t0);
    if (slot < 8) { hold(t0, l ? DLY_1 : DLY_A); role_A(a, w, l, idx, (xcd & 3) * 2, ia); }
    else if (slot < 16) { hold(t0, l ? DLY_1 : DLY_B); role_B(a, w, l, idx, (xcd & 3) * 2, ib); }
    else if (gu >= 0) {
        const int q = gu * 4 + w;
        if (!two) use(ld4(mkrs(a.ws, WS_NEED), q < 2 * NVK + 2 ? prb(q >> 1, q & 1) + lane_id() * 32 : OOB, 0));
        hold(t0, two ? 0 : DLY_G); role_GU(a, w, gu, two);
    }
    else if (slot < 24 || cu >= 0) {
        unsigned nf;
        if (slot < 24) {
            nf = role_R(a, w, l, idx, t0) ^ (FSIDE * 8192); cu = 256 + idx;
            if (l) { if (idx + w == 0) vote_end(mkrs(a.ws, WS_NEED)); return; }
        } else {
            hold(t0, DLY_D); nf = side(mkrs(a.ws, WS_NEED), FSIDE);
        }
        role_RED(a, w, cu, nf);
    }
    else if (gu == -2) return;
    else { if (xcd < 7 && !two) vote(mkrs(a.ws, WS_NEED), w, dn, l, t0); hold(t0, two ? 0 : DLY_D); role_DN(a, w, dn >> 2, dn & 3, two); }
}
extern "C" void kernel_launch(void* const* d_in, const int* in_sizes, int n_in, void* d_out, int out_size, void* d_ws, size_t ws_size, hipStream_t stream) {
    (void)in_sizes; (void)n_in; (void)out_size; (void)ws_size;
    (void)hipFuncSetAttribute((const void*)mega6, hipFuncAttributeMaxDynamicSharedMemorySize, 163840);
    d_ws = (void*)(((uintptr_t)d_ws + (2u << 20) - 1) & ~(uintptr_t)((2u << 20) - 1));
    Args a{(const u16*)d_in[0], (const u16*)d_in[1], (const u16*)d_in[2], (const u16*)d_in[3], (const u16*)d_in[4], (const u16*)d_in[5],
           (const u16*)d_in[8], (const u16*)d_in[9], (const u16*)d_in[10], (char*)d_ws, (u16*)d_out};
    mega6<<<256, 256, 163840, stream>>>(a);
}
```

```cpp
#include <hip/hip_runtime.h>
#include <stdint.h>

typedef unsigned short u16;
typedef unsigned long long u64;
typedef __attribute__((ext_vector_type(8))) __bf16 bf8;
typedef __attribute__((ext_vector_type(16))) __bf16 bf16v;
typedef __attribute__((ext_vector_type(4))) float f32x4;
typedef __attribute__((ext_vector_type(16))) float f32x16;
typedef __attribute__((ext_vector_type(2))) float f32x2;
typedef __attribute__((ext_vector_type(8))) unsigned v8u;
typedef __attribute__((ext_vector_type(4))) unsigned v4u;
typedef __attribute__((ext_vector_type(3))) unsigned v3u;
typedef __attribute__((ext_vector_type(2))) unsigned v2u;
typedef __amdgpu_buffer_rsrc_t rsrc_t;
typedef __attribute__((address_space(3))) char lds_char;
#define DEVI __device__ __forceinline__
#define LDSP(T, p) ((__attribute__((address_space(3))) T*)(p))

typedef __attribute__((ext_vector_type(2))) __bf16 bf16x2_t;
DEVI unsigned pack2(float lo, float hi) { f32x2 v = {lo, hi}; bf16x2_t b = __builtin_convertvector(v, bf16x2_t); return __builtin_bit_cast(unsigned, b); }
DEVI float bflo(unsigned u) { return __uint_as_float(u << 16); }
DEVI float bfhi(unsigned u) { return __uint_as_float(u & 0xffff0000u); }
DEVI float rbf(float x) { return bflo(pack2(x, x)); }
constexpr int NV = 112;
#define MR_(R, A_STR, V_STR, OUTS, INS, W) do { if constexpr ((R) < 256) asm volatile(A_STR : OUTS : INS, [lo] "n"(R), [hi] "n"((R) + (W) - 1)); \
                                                else asm volatile(V_STR : OUTS : INS, [lo] "n"((R) - 256 + NV), [hi] "n"((R) - 256 + NV + (W) - 1)); } while (0)
#define CM_ ,
template <int R> DEVI void mwr(unsigned x) { if constexpr (R < 256) asm volatile("v_accvgpr_write_b32 a[%1], %0" :: "v"(x), "n"(R)); else asm volatile("v_mov_b32 v[%1], %0" :: "v"(x), "n"(R - 256 + NV)); }
template <int R> DEVI void mwr4(v4u x) { mwr<R>(x[0]); mwr<R + 1>(x[1]); mwr<R + 2>(x[2]); mwr<R + 3>(x[3]); }
#define M16 "v_mfma_f32_16x16x32_bf16 "
#define M32 "v_mfma_f32_32x32x16_bf16 "
#define S16 "v_smfmac_f32_16x16x64_bf16 "
template <int R, bool Z> DEVI void mfA(f32x4& d, v4u b) {
    if constexpr (Z) MR_(R, M16 "%[d], a[%[lo]:%[hi]], %[b], 0", M16 "%[d], v[%[lo]:%[hi]], %[b], 0", [d] "=&v"(d), [b] "v"(b), 4);
    else MR_(R, M16 "%[d], a[%[lo]:%[hi]], %[b], %[d]", M16 "%[d], v[%[lo]:%[hi]], %[b], %[d]", [d] "+v"(d), [b] "v"(b), 4);
}
template <int R, bool Z> DEVI void mfB(f32x4& d, v4u a) {
    if constexpr (Z) MR_(R, M16 "%[d], %[a], a[%[lo]:%[hi]], 0", M16 "%[d], %[a], v[%[lo]:%[hi]], 0", [d] "=&v"(d), [a] "v"(a), 4);
    else MR_(R, M16 "%[d], %[a], a[%[lo]:%[hi]], %[d]", M16 "%[d], %[a], v[%[lo]:%[hi]], %[d]", [d] "+v"(d), [a] "v"(a), 4);
}
template <int R, bool Z> DEVI void mf32A(f32x16& d, v4u b) {
    if constexpr (Z) MR_(R, M32 "%[d], a[%[lo]:%[hi]], %[b], 0", M32 "%[d], v[%[lo]:%[hi]], %[b], 0", [d] "=&v"(d), [b] "v"(b), 4);
    else MR_(R, M32 "%[d], a[%[lo]:%[hi]], %[b], %[d]", M32 "%[d], v[%[lo]:%[hi]], %[b], %[d]", [d] "+v"(d), [b] "v"(b), 4);
}
template <bool Z> DEVI void mfv(f32x4& d, v4u a, v4u b) {
    if constexpr (Z) asm volatile(M16 "%0, %1, %2, 0" : "=&v"(d) : "v"(a), "v"(b)); else asm volatile(M16 "%0, %1, %2, %0" : "+v"(d) : "v"(a), "v"(b));
}
template <int R> DEVI void smf(f32x4& d, v4u a, int idx) { MR_(R, S16 "%[d], %[a], a[%[lo]:%[hi]], %[i]", S16 "%[d], %[a], v[%[lo]:%[hi]], %[i]", [d] "+v"(d), [a] "v"(a) CM_ [i] "v"(idx), 8); }
DEVI void smfv(f32x4& d, v4u a, v8u b, int idx) { asm volatile(S16 "%0, %1, %2, %3" : "+v"(d) : "v"(a), "v"(b), "v"(idx)); }
DEVI void zero4(f32x4& d) { d = f32x4{0.f, 0.f, 0.f, 0.f}; asm volatile("" : "+v"(d)); }
template <int N> DEVI void nop() { asm volatile("s_nop %0" :: "n"(N)); }
DEVI void fence(f32x4& d) { asm volatile("" : "+v"(d)); }
template <class... T> DEVI void fence(f32x4& d, T&... r) { fence(d); fence(r...); }
DEVI float selm(float a, float b, u64 m) { float r; asm("v_cndmask_b32 %0, %1, %2, %3" : "=v"(r) : "v"(a), "v"(b), "s"(m)); return r; }
DEVI int opqs(int x) { asm volatile("" : "+s"(x)); return x; }
DEVI v4u lo4(v8u v) { return __builtin_shufflevector(v, v, 0, 1, 2, 3); }
DEVI v4u hi4(v8u v) { return __builtin_shufflevector(v, v, 4, 5, 6, 7); }
DEVI v8u cat8(v4u a, v4u b) { return __builtin_shufflevector(a, b, 0, 1, 2, 3, 4, 5, 6, 7); }
constexpr int AUX_SC1 = 16;
DEVI v4u ld16(rsrc_t r, unsigned vo, unsigned so) { return __builtin_amdgcn_raw_buffer_load_b128(r, vo, so, AUX_SC1); }
DEVI v2u ld8(rsrc_t r, unsigned vo, unsigned so) { return __builtin_amdgcn_raw_buffer_load_b64(r, vo, so, AUX_SC1); }
DEVI unsigned ld4(rsrc_t r, unsigned vo, unsigned so) { return __builtin_amdgcn_raw_buffer_load_b32(r, vo, so, AUX_SC1); }
DEVI void st16(v4u v, rsrc_t r, unsigned vo) { __builtin_amdgcn_raw_buffer_store_b128(v, r, vo, 0, AUX_SC1); }
DEVI void st16s(v4u v, rsrc_t r, unsigned vo, unsigned so) { __builtin_amdgcn_raw_buffer_store_b128(v, r, vo, so, AUX_SC1); }
DEVI void st8(v2u v, rsrc_t r, unsigned vo, unsigned so) { __builtin_amdgcn_raw_buffer_store_b64(v, r, vo, so, AUX_SC1); }
DEVI void st4(unsigned v, rsrc_t r, unsigned vo, unsigned so) { __builtin_amdgcn_raw_buffer_store_b32(v, r, vo, so, AUX_SC1); }
#define CB asm volatile("" ::: "memory")
#define WL0 asm volatile("s_waitcnt lgkmcnt(0)" ::: "memory")
#define WV0 asm volatile("s_waitcnt vmcnt(0)" ::: "memory")
#define BAR do { __builtin_amdgcn_s_barrier(); CB; } while (0)
#define SB __builtin_amdgcn_sched_barrier(0)
DEVI float fadd(float a, float b) { float r; asm("v_add_f32 %0, %1, %2" : "=v"(r) : "v"(a), "v"(b)); return r; }
DEVI float vfma(float a, float b, float c) { float r; asm("v_fma_f32 %0, %1, %2, %3" : "=v"(r) : "v"(a), "v"(b), "v"(c)); return r; }
DEVI unsigned vxor(unsigned a, unsigned b) { unsigned r; asm volatile("v_xor_b32 %0, %1, %2" : "=v"(r) : "i"(b), "v"(a)); return r; }
DEVI int lane_id() { int r; asm volatile("v_mbcnt_lo_u32_b32 %0, -1, 0\n\tv_mbcnt_hi_u32_b32 %0, -1, %0" : "=v"(r)); __builtin_assume(r >= 0 && r < 64); return r; }
DEVI int opq(int x) { asm volatile("" : "+v"(x)); return x; }
DEVI float vmaxf(float a, float b) { float r; asm("v_max_f32 %0, %1, %2" : "=v"(r) : "v"(a), "v"(b)); return r; }
DEVI void swap32(float& a, float& b) { auto r = __builtin_amdgcn_permlane32_swap(__float_as_uint(a), __float_as_uint(b), false, false); a = __uint_as_float(r[0]); b = __uint_as_float(r[1]); }
DEVI void swap32u(unsigned& a, unsigned& b) { auto r = __builtin_amdgcn_permlane32_swap(a, b, false, false); a = r[0]; b = r[1]; }
DEVI void swap16u(unsigned& a, unsigned& b) { auto r = __builtin_amdgcn_permlane16_swap(a, b, false, false); a = r[0]; b = r[1]; }
DEVI float hadd(float a, float b) { swap32(a, b); return fadd(a, b); }
DEVI float red32(float x) { float y = x; swap32(x, y); return x + y; }
DEVI float red16(float x) { unsigned a = __float_as_uint(x), b = a; swap16u(a, b); return __uint_as_float(a) + __uint_as_float(b); }
DEVI float mx32(float x) { float y = x; swap32(x, y); return vmaxf(x, y); }
DEVI float mx16(float x) { unsigned a = __float_as_uint(x), b = a; swap16u(a, b); return vmaxf(__uint_as_float(a), __uint_as_float(b)); }
template <int N> struct IC { static constexpr int v = N; };
template <int I, int N, class F> DEVI void sfor(F&& f) { if constexpr (I < N) { f(IC<I>{}); sfor<I + 1, N>(f); } }
#ifndef NSEG
#define NSEG 64
#endif
#ifndef LAGD
#define LAGD 10
#endif
constexpr float SC_L2E = 0.08838834764831845f * 1.4426950408889634f;
constexpr unsigned SENT = 0xFFFFFFFFu;
#ifndef NUS_
#define NUS_ 8
#endif
#ifndef NRG_
#define NRG_ 8
#endif
#ifndef NC
#define NC 8
#endif
constexpr int NREP = 8, NUS = NUS_, NRG = NRG_, NKF = 5, NGU = 38;
constexpr unsigned XB = 4u << 20;
constexpr unsigned O_XIN = 0;
constexpr unsigned O_XN  = O_XIN + 2 * XB;
constexpr unsigned O_XM  = O_XN + 2 * XB;
constexpr unsigned O_H   = O_XM + 2 * XB;
constexpr unsigned O_D   = O_H + 2u * 64 * 224 * 1024;
constexpr unsigned O_N = O_D + 2u * 64 * NKF * 65536;
constexpr unsigned DV(unsigned o) { return ((o & ~8191u) << 1) | (o & 8191u); }
DEVI unsigned na(unsigned nb, unsigned o) { return (DV(o) + nb) ^ ((o >> 7) & 8192u); }
constexpr unsigned N_PM  = 0;
constexpr unsigned N_PMM = N_PM + NRG * 16384;
constexpr unsigned N_PA  = (1u << 20) - 16384;
constexpr unsigned N_MEM = 1u << 20;
constexpr unsigned N_WM  = 2u << 20;
constexpr unsigned N_PTX = 6u << 20;
constexpr unsigned N_U   = 10u << 20;
static_assert(NRG == 8 && NUS == 8 && NREP == 8 && O_N % (4u << 20) == 0, "");
constexpr unsigned O_FLG_END = O_N + DV(N_MEM + NREP * 16384);
constexpr unsigned N_FX  = N_PA - 8192;
constexpr unsigned N_PG  = N_FX - 8192;
constexpr unsigned N_PD  = N_PG + 40 * 128;
constexpr unsigned PG2 = DV(N_PA + 8192 - N_PG);
constexpr unsigned O_PRB = O_N + (512u << 10), O_CAL = O_N + (768u << 10);
constexpr unsigned O_SX  = O_N + DV(N_PG) - 2u * 64 * 32 * 256;
constexpr unsigned O_FLG = O_SX;
static_assert(O_PRB >= O_N + DV(N_PMM + NRG * 8192) && O_FLG >= O_CAL + 8192 && (O_FLG_END - O_FLG) % 65536 == 0, "");
constexpr unsigned OOB = 0x80000000u;
constexpr unsigned WS_NEED = O_N + DV(N_U + NUS * (2u << 20));
constexpr unsigned O_PROF = 240u << 20;
static_assert(WS_NEED <= (200u << 20), "workspace too large");

#ifdef DRY
#define WSZ(x) 0u
#else
#define WSZ(x) (x)
#endif
struct Args { const u16 *x, *mem0, *Wq, *Wk, *Wv, *Wo, *Wg, *Wu, *Wd; char* ws; u16* out; };
#ifndef SPIN_LOG
#define SPIN_LOG 18
#endif
constexpr unsigned SPIN_CAP = 1u << SPIN_LOG;
DEVI void spin_bail(unsigned& spins) {
    asm volatile("s_add_u32 %0, %0, 1\n\ts_cmp_lt_u32 %0, %1\n\ts_cbranch_scc1 1f\n\ts_endpgm\n1:" : "+s"(spins) : "n"(SPIN_CAP) : "scc");
}
template <int K> DEVI void wrl(unsigned& v, u64& t) { asm volatile("" : "+s"(t)); asm volatile("v_writelane_b32 %0, %1, %2" : "+v"(v) : "s"((unsigned)t), "n"(K)); }
#ifdef TS
#ifndef TSCLK
#define TSCLK __builtin_amdgcn_s_memrealtime
#endif
#define TSV(u, i, v) __builtin_amdgcn_raw_buffer_store_b32((unsigned)(v), mkrs(a.ws, 256u << 20), lane_id() == 0 ? O_PROF + ((blockIdx.x * 4 + w) * 128 + (u)) * 64 + (i) * 4 : OOB, 0, 0)
#define TSW(u, i) TSV(u, i, TSCLK())
#define TSC(u, i) TSV(u, i, __builtin_amdgcn_s_memtime())
#define TSD u64 tsr[16] = {}
#define TSR(k) tsr[k] = rclk()
#define TSK(k) asm volatile("s_memtime %0" : "=s"(tsr[k]))
#define TSF(u, m) do { unsigned tv_ = 0; WL0; sfor<0, 16>([&](auto K) { if constexpr (((m) >> K.v) & 1) wrl<K.v>(tv_, tsr[K.v]); }); \
    const int tl_ = lane_id(); __builtin_amdgcn_raw_buffer_store_b32(tv_, mkrs(a.ws, 256u << 20), (tl_ < 16 && (((m) >> tl_) & 1)) ? O_PROF + ((blockIdx.x * 4 + w) * 128 + (u)) * 64 + tl_ * 4 : OOB, 0, 0); } while (0)
#if TS == 4
#undef TSV
#undef TSR
#undef TSK
#define TSV(u, i, v)
#define TSR(k)
#define TSK(k)
#define FSK(G, O, D) if constexpr ((G) >= (O) && ((G) - (O)) % (D) == 0 && ((G) - (O)) / (D) < FN) asm volatile("s_memtime %0" : "=s"(tsr[((G) - (O)) / (D)]))
#define FSF(u) TSF(u, (1 << FN) - 1)
#define FSD TSD
#endif
#else
#define TSW(u, i)
#define TSC(u, i)
#define TSV(u, i, v)
#define TSD
#define TSR(k)
#define TSK(k)
#define TSF(u, m)
#endif
#if TS != 4
#define FSK(G, O, D)
#define FSF(u)
#define FSD
#endif
DEVI unsigned rslot(int s) { return (unsigned)(s & (NRG - 1)); }
DEVI u64 pend(v4u d) { return __builtin_amdgcn_uicmp(max(d[0], d[2]), SENT, 32); }
DEVI u64 pend2(v2u d) { return __builtin_amdgcn_uicmp(d[0], SENT, 32); }
DEVI u64 pend1(unsigned d) { return __builtin_amdgcn_uicmp(d, SENT, 32); }
DEVI unsigned nosent(unsigned u) { return min(u, 0xFFFFFFFEu); }
#ifndef WPOL
#define WPOL ""
#endif
template <int OFF, bool SC1> DEVI void dma1(rsrc_t rs, unsigned vo, unsigned so) {
    if constexpr (SC1) asm volatile("buffer_load_dwordx4 %0, %1, %2 offen offset:%3 sc1 lds" :: "v"(vo), "s"(rs), "s"(so), "n"(OFF) : "memory");
    else asm volatile("buffer_load_dwordx4 %0, %1, %2 offen offset:%3 " WPOL " lds" :: "v"(vo), "s"(rs), "s"(so), "n"(OFF) : "memory");
}
DEVI void setm0(unsigned v) { asm volatile("s_mov_b32 m0, %0\n\ts_nop 0" :: "s"(v) : "memory"); }
DEVI void dma4(unsigned lds, rsrc_t rs, unsigned vo, unsigned so) { setm0(lds); dma1<0, true>(rs, vo, so); dma1<1024, true>(rs, vo, so); dma1<2048, true>(rs, vo, so); dma1<3072, true>(rs, vo, so); }
DEVI void dmaw(unsigned lds, rsrc_t rs, unsigned vo, unsigned so) { setm0(lds); asm volatile("buffer_load_dword %0, %1, %2 offen sc1 lds" :: "v"(vo), "s"(rs), "s"(so) : "memory"); }
template <int N> DEVI void wvm() { asm volatile("s_waitcnt vmcnt(%0)" :: "n"(N) : "memory"); }
template <int N> DEVI void wlg() { asm volatile("s_waitcnt lgkmcnt(%0)" :: "n"(N) : "memory"); }
DEVI void dma4p(u64 c0, u64 c1, u64 c2, u64 c3, rsrc_t rs, unsigned vo, unsigned so) {
    asm volatile(
        "s_cmp_eq_u64 %[c0], 0\n\ts_cbranch_scc1 1f\n\t"
        "buffer_load_dwordx4 %[vo], %[rs], %[so] offen sc1 lds\n"
        "1:\n\ts_cmp_eq_u64 %[c1], 0\n\ts_cbranch_scc1 2f\n\t"
        "buffer_load_dwordx4 %[vo], %[rs], %[so] offen offset:1024 sc1 lds\n"
        "2:\n\ts_cmp_eq_u64 %[c2], 0\n\ts_cbranch_scc1 3f\n\t"
        "buffer_load_dwordx4 %[vo], %[rs], %[so] offen offset:2048 sc1 lds\n"
        "3:\n\ts_cmp_eq_u64 %[c3], 0\n\ts_cbranch_scc1 4f\n\t"
        "buffer_load_dwordx4 %[vo], %[rs], %[so] offen offset:3072 sc1 lds\n"
        "4:"
        :: [c0] "s"(c0), [c1] "s"(c1), [c2] "s"(c2), [c3] "s"(c3), [vo] "v"(vo), [rs] "s"(rs), [so] "s"(so)
        : "memory", "scc");
}
#ifndef PSL
#define PSL 8
#endif
DEVI void wait_prog(rsrc_t rs, unsigned vo, unsigned so, int need) {
    unsigned spins = 0;
#pragma nounroll
    for (;;) {
        CB;
        const int v = (int)ld4(rs, vo, so);
        if (__builtin_amdgcn_sicmp(v, need, 40) == 0) return;
        spin_bail(spins);
        __builtin_amdgcn_s_sleep(PSL);
    }
}

typedef __attribute__((ext_vector_type(16))) int v16i;
typedef __attribute__((ext_vector_type(16))) unsigned v16u;
#ifdef TS
constexpr unsigned RSZ = 256u << 20;
#else
constexpr unsigned RSZ = WS_NEED;
#endif
DEVI u64 rclk() { u64 t; asm volatile("s_memrealtime %0" : "=s"(t)); return t; }
DEVI unsigned tsv(u64& t) { asm volatile("s_waitcnt lgkmcnt(0)" : "+s"(t)); return (unsigned)t; }
DEVI unsigned stamp(u64& t, int tag, int lead) { asm volatile("" : "+s"(t)); return (unsigned)tag << 24 | (((unsigned)t + lead) & 0xFFFFFFu); }
#ifndef SXTH
#define SXTH 20
#endif
DEVI int left(int t) { return (int)((unsigned)(t - (int)__builtin_amdgcn_s_memrealtime()) << 8); }
DEVI void hold(int t0, int d) { for (int n = 0; n < 4096 && (int)__builtin_amdgcn_s_memrealtime() - t0 < d; ++n) __builtin_amdgcn_s_sleep(4); }
DEVI int smin(int a, int b) { int r; asm("s_min_i32 %0, %1, %2" : "=s"(r) : "s"(a), "s"(b) : "scc"); return r; }
DEVI int smax(int a, int b) { int r; asm("s_max_i32 %0, %1, %2" : "=s"(r) : "s"(a), "s"(b) : "scc"); return r; }
DEVI void smin8x2(const v16i& f, int& m0, int& m1) {
    asm("s_min_i32 %0, %2, %3\n\ts_min_i32 %1, %10, %11\n\ts_min_i32 %0, %0, %4\n\ts_min_i32 %1, %1, %12\n\ts_min_i32 %0, %0, %5\n\ts_min_i32 %1, %1, %13\n\ts_min_i32 %0, %0, %6\n\ts_min_i32 %1, %1, %14\n\t"
        "s_min_i32 %0, %0, %7\n\ts_min_i32 %1, %1, %15\n\ts_min_i32 %0, %0, %8\n\ts_min_i32 %1, %1, %16\n\ts_min_i32 %0, %0, %9\n\ts_min_i32 %1, %1, %17"
        : "=&s"(m0), "=&s"(m1) : "s"(f[0]), "s"(f[1]), "s"(f[2]), "s"(f[3]), "s"(f[4]), "s"(f[5]), "s"(f[6]), "s"(f[7]),
          "s"(f[8]), "s"(f[9]), "s"(f[10]), "s"(f[11]), "s"(f[12]), "s"(f[13]), "s"(f[14]), "s"(f[15]) : "scc");
}
DEVI void smax8x2(const v16i& f, int& m0, int& m1) {
    asm("s_max_i32 %0, %2, %3\n\ts_max_i32 %1, %10, %11\n\ts_max_i32 %0, %0, %4\n\ts_max_i32 %1, %1, %12\n\ts_max_i32 %0, %0, %5\n\ts_max_i32 %1, %1, %13\n\ts_max_i32 %0, %0, %6\n\ts_max_i32 %1, %1, %14\n\t"
        "s_max_i32 %0, %0, %7\n\ts_max_i32 %1, %1, %15\n\ts_max_i32 %0, %0, %8\n\ts_max_i32 %1, %1, %16\n\ts_max_i32 %0, %0, %9\n\ts_max_i32 %1, %1, %17"
        : "=&s"(m0), "=&s"(m1) : "s"(f[0]), "s"(f[1]), "s"(f[2]), "s"(f[3]), "s"(f[4]), "s"(f[5]), "s"(f[6]), "s"(f[7]),
          "s"(f[8]), "s"(f[9]), "s"(f[10]), "s"(f[11]), "s"(f[12]), "s"(f[13]), "s"(f[14]), "s"(f[15]) : "scc");
}
#ifndef NVK
#define NVK 8
#endif
#ifndef DLY_V
#define DLY_V 500
#endif
#ifndef PSV
#define PSV 12
#endif
#ifndef DLY_P
#define DLY_P 150
#endif
DEVI unsigned prb(int k, int b) { return O_PRB + k * 16384 + b * 8192 + (k & 1) * 4096; }
DEVI void vote(rsrc_t rs, int w, int v, int far, int t0s) {
    const int lane = lane_id();
    constexpr unsigned N = (O_FLG_END - O_FLG) / 65536;
#pragma nounroll
    for (unsigned q = 0; q < N; ++q) st16(v4u{SENT, SENT, SENT, SENT}, rs, O_FLG + ((v * 4 + w) * N + q) * 1024 + lane * 16);
    const v2u e = ld8(rs, O_CAL + (lane & 31) * 256 + 128, 0);
    static_assert(NVK % 4 == 0 && NVK < 12, "");
    int best[2] = {1 << 30, 1 << 30};
    hold(t0s, DLY_P);
#pragma nounroll
    for (int k = 0; k <= NVK / 4; ++k)
#pragma unroll
        for (int b = 0; b < 2; ++b) {
            u64 t0, t1; int d;
            asm volatile("s_memtime %0\n\ts_waitcnt lgkmcnt(0)\n\ts_buffer_load_dword %2, %3, %4 glc\n\ts_waitcnt lgkmcnt(0)\n\ts_memtime %1\n\ts_waitcnt lgkmcnt(0)"
                         : "=&s"(t0), "=&s"(t1), "=&s"(d) : "s"(rs), "s"(prb(k ? 4 * k - w : 0, b) + v * 128) : "memory");
            if (k) best[b] = smin(best[b], (int)(t1 - t0));
        }
    *LDSP(int, (lane & 1) * 16 + w * 4) = best[lane & 1];
    WV0; WL0; BAR;
#pragma unroll
    for (int b = 0; b < 2; ++b) { const v4u t = *LDSP(const v4u, b * 16); best[b] = min(min(t[0], t[1]), min(t[2], t[3])); }
    st8(v2u{((e[0] + 2) & ~1u) | ((best[1] < best[0]) ^ far), e[1] + 1}, rs, lane < 32 && w == 0 ? O_CAL + lane * 256 + v * 8 : OOB, 0);
}
DEVI void vote_end(rsrc_t rs) {
    const unsigned o = O_CAL + (lane_id() & 31) * 256 + 128;
    const v2u e = ld8(rs, o, 0);
    st8(v2u{e[0] + 2, e[1] + 1}, rs, o, 0);
}
DEVI unsigned side(rsrc_t rs, int l) {
    unsigned spins = 0;
#pragma nounroll
    for (;;) {
        v16u f[2]; v2u e;
        const unsigned o = O_CAL + (blockIdx.x & 31) * 256;
        asm volatile("s_buffer_load_dwordx16 %0, %3, %4 glc\n\ts_buffer_load_dwordx16 %1, %3, %5 glc\n\ts_buffer_load_dwordx2 %2, %3, %6 glc\n\ts_waitcnt lgkmcnt(0)"
                     : "=&s"(f[0]), "=&s"(f[1]), "=&s"(e) : "s"(rs), "s"(o), "s"(o + 64), "s"(o + 128) : "memory");
        unsigned bad = 0, sum = 0;
#pragma unroll
        for (int i = 0; i < 16; ++i) { const unsigned x = f[i >> 3][2 * i & 15]; bad |= ((x ^ (e[0] + 2)) >> 1) | (f[i >> 3][(2 * i & 15) + 1] ^ (e[1] + 1)); sum += x & 1; }
#ifdef CSIDE
        sum = CSIDE * 16;
#endif
        if (bad == 0) return opqs(O_N + ((l ^ (sum > 8)) & 1) * 8192);
        spin_bail(spins);
        __builtin_amdgcn_s_sleep(PSV);
    }
}
#ifndef PSH
#define PSH 2
#endif
DEVI int wait_hint(rsrc_t rs, unsigned so, unsigned vo, int tag) {
#pragma nounroll
    for (int n = 0;; ++n) {
        v16i f;
        asm volatile("s_buffer_load_dwordx16 %0, %1, %2 glc\n\ts_waitcnt lgkmcnt(0)" : "=s"(f) : "s"(rs), "s"(so) : "memory");
        int m0, m1;
        smin8x2(f, m0, m1);
        if (smin(m0, m1) >= tag << 24) { smax8x2(f, m0, m1); return smax(m0, m1); }
        if (n == 40) { wait_prog(rs, vo, 0, tag << 24); n = 0; }
        __builtin_amdgcn_s_sleep(PSH);
    }
}
template <class WM> constexpr int widx(int j, int t) {
    int n = 0;
    for (int q = 0; q < j * WM::NP + t; ++q) n += WM::kind(q / WM::NP, q % WM::NP) == WM::kind(j, t);
    return n;
}
template <class WM> constexpr int lds_tile(int t) { for (int j = 0; j < WM::NT; ++j) if (WM::kind(j, t) == 1) return j; return -1; }
DEVI v4u ldsq(unsigned p, int off) { return *LDSP(const v4u, LDSP(char, p) + off); }
DEVI void stsq(unsigned p, int off, v4u v) { *LDSP(v4u, LDSP(char, p) + off) = v; }
DEVI v8u ldpair(unsigned wl, int slot) { return cat8(ldsq(wl, slot * 2048), ldsq(wl, slot * 2048 + 1024)); }
DEVI rsrc_t mkrs(const void* p, unsigned bytes) { return __builtin_amdgcn_make_buffer_rsrc((void*)p, 0, (int)bytes, 0x00020000); }
#ifndef NPL
#define NPL 72
#endif
#ifndef LNN
#define LNN 8
#endif
constexpr int LN_NOW = LNN, NP0 = NPL * 4;
template <int SL = 0> DEVI void pull(const void* p, int n, int q, int nq) {
    const rsrc_t r = mkrs(p, n * 1024);
    const unsigned vo = lane_id() * 16;
#pragma nounroll
    for (; q < n; q += nq) { asm volatile("buffer_load_dwordx4 a[0:3], %0, %1, %2 offen" :: "v"(vo), "s"(r), "s"(q * 1024) : "memory"); if (SL) __builtin_amdgcn_s_sleep(SL); }
}
#ifndef PQ
#define PQ 0
#endif
#ifndef DLY_F
#define DLY_F 1000
#endif
#ifndef PFS
#define PFS 5
#endif
DEVI void pull_all(const Args& a, int w, int q, int t0) {
    q = q * 4 + w;
    if (q < NP0) {
    pull(a.Wv, 8192, q, NP0); pull(a.Wo, 8192, q, NP0); if (!PQ) pull(a.Wq, 8192, q, NP0); pull(a.Wk, 8192, q, NP0);
    }
    if (q >= 256) return;
    hold(t0, DLY_F);
    pull<PFS>(q & 1 ? a.Wg : a.Wu, 7168, q >> 1, 128); pull<PFS>(a.Wd, 7168, q, 256);
}
template <class WM, int j, int ks, int O0, int O1> DEVI void put_frag(unsigned ta, unsigned wl, v8u* Wc) {
    constexpr int t = ks >> 1, f = ks & 1, k = WM::kind(j, t), ix = widx<WM>(j, t);
    if constexpr (k == 0) {
        constexpr int R = WM::RB + 8 * ix + 4 * f;
        if constexpr (R < 256) asm volatile("ds_read_b64_tr_b16 a[%1:%2], %0 offset:%5\n\tds_read_b64_tr_b16 a[%3:%4], %0 offset:%6" :: "v"(ta), "n"(R), "n"(R + 1), "n"(R + 2), "n"(R + 3), "n"(O0), "n"(O1) : "memory");
        else asm volatile("ds_read_b64_tr_b16 v[%1:%2], %0 offset:%5\n\tds_read_b64_tr_b16 v[%3:%4], %0 offset:%6" :: "v"(ta), "n"(R - 256 + NV), "n"(R - 255 + NV), "n"(R - 254 + NV), "n"(R - 253 + NV), "n"(O0), "n"(O1) : "memory");
    } else {
        v2u p, q;
        asm volatile("ds_read_b64_tr_b16 %0, %2 offset:%3\n\tds_read_b64_tr_b16 %1, %2 offset:%4\n\ts_waitcnt lgkmcnt(0)" : "=&v"(p), "=&v"(q) : "v"(ta), "n"(O0), "n"(O1) : "memory");
        const v4u F = v4u{p[0], p[1], q[0], q[1]};
        if constexpr (k == 1) stsq(wl, (WM::LB + ix) * 2048 + f * 1024, F);
        else if constexpr (f == 0) Wc[WM::CBI + ix] = cat8(F, F);
        else Wc[WM::CBI + ix] = cat8(lo4(Wc[WM::CBI + ix]), F);
    }
}
template <int NB, int DEPTH, int NI, bool SHARED = false, int NX = 0, class ISSUE, class PUT>
DEVI void load_blocks(ISSUE&& issue, PUT&& put) {
    sfor<0, DEPTH - 1>([&](auto B) { issue(B, IC<B.v % DEPTH>{}); });
    sfor<0, NB>([&](auto B) {
        constexpr int b = B.v, left = NB + NX - 1 - b < DEPTH - 2 ? NB + NX - 1 - b : DEPTH - 2;
        wvm<NI * left>();
        wlg<0>();
        if constexpr (SHARED) BAR;
        if constexpr (b + DEPTH - 1 < NB + NX) issue(IC<b + DEPTH - 1>{}, IC<(b + DEPTH - 1) % DEPTH>{});
        put(B, IC<b % DEPTH>{});
    });
    wlg<0>();
    if constexpr (SHARED) BAR;
}
DEVI void first_look(unsigned& fx, rsrc_t rs, unsigned nb) { asm volatile("buffer_load_dword %0, %1, %2, %3 offen sc1" : "=v"(fx) : "v"((lane_id() & 7) * 4), "s"(rs), "s"(na(nb, N_FX)) : "memory"); }
DEVI unsigned first_there(unsigned& fx) { asm volatile("" : "+v"(fx)); return pend1(fx) == 0 ? WS_NEED : 0u; }
DEVI void dma_blk(unsigned lds, rsrc_t rw, unsigned vo, unsigned so, unsigned rowb) { setm0(lds); dma1<0, false>(rw, vo, so); setm0(lds + 1024); dma1<0, false>(rw, vo, so + 16 * rowb); }
template <class WM, int NKS, int K0, int K1, int RTS, int LATE1, class FILL>
DEVI void gemm_pass(unsigned xa, unsigned wl, const v8u* Wc, v4u (&x)[2][3], f32x4 (&acc)[WM::NT][2], FILL&& fill) {
    auto rd = [&](auto KS, auto Q) {
        constexpr int ks = KS.v, q = Q.v, jl = lds_tile<WM>(ks >> 1), il = widx<WM>(jl < 0 ? 0 : jl, ks >> 1);
        if constexpr (q == 0 && jl >= 0) x[ks & 1][2] = ldsq(wl, (WM::LB + il) * 2048 + (ks & 1) * 1024);
        if constexpr (q == 1) x[ks & 1][1] = ldsq(xa, RTS + ks * 1024);
        if constexpr (q == 2) x[ks & 1][0] = ldsq(xa, ks * 1024);
    };
    sfor<K0 * 2 * WM::NT, K1 * 2 * WM::NT>([&](auto N) {
        constexpr int ks = N.v / (2 * WM::NT), n = N.v % (2 * WM::NT), j = n >> 1, rt = n & 1, t = ks >> 1, f = ks & 1, k = WM::kind(j, t), ix = widx<WM>(j, t);
        fill(IC<ks>{}, IC<n>{});
        SB;
        const v4u xv = x[ks & 1][rt];
        if constexpr (k == 0) mfA<WM::RB + 8 * ix + 4 * f, ks == 0>(acc[j][rt], xv);
        else if constexpr (k == 1) mfv<ks == 0>(acc[j][rt], x[ks & 1][2], xv);
        else mfv<ks == 0>(acc[j][rt], f ? hi4(Wc[WM::CBI + ix]) : lo4(Wc[WM::CBI + ix]), xv);
        if constexpr (ks + 1 < NKS) rd(IC<ks + 1>{}, IC<n - (ks == 1 ? LATE1 : 0)>{});
        SB;
    });
}
template <class WM> constexpr int next_lds(int n) { for (; n < 16 * WM::NT; ++n) if (WM::kind(2 * (n >> 5) + (n & 1), (n >> 1) & 15) == 1) return n; return -1; }
template <class WM> constexpr int lds_cnt(int n) { int c = 0; for (int q = next_lds<WM>(0); q >= 0 && q < n; q = next_lds<WM>(q + 1)) ++c; return c; }
template <class WM> constexpr int lds_nth(int k) { int q = next_lds<WM>(0); for (; k > 0 && q >= 0; --k) q = next_lds<WM>(q + 1); return q; }
template <class WM, int K, int NB> DEVI void pair_next(v8u (&pb)[NB], unsigned wl) {
    constexpr int n = lds_nth<WM>(K);
    if constexpr (n >= 0) { constexpr int ix = widx<WM>(2 * (n >> 5) + (n & 1), (n >> 1) & 15); pb[K % NB] = ldpair(wl, WM::LB + ix); }
}
template <class WM, int MO, int MR, int NB, class FILL>
DEVI void spass(unsigned ma, unsigned wl, const v8u* Wc, f32x4* e, v4u (&m)[MR], v8u (&pb)[NB], int sidx, FILL&& fill) {
    sfor<0, 16 * WM::NT>([&](auto N) {
        constexpr int n = N.v, t = (n >> 1) & 15, j = 2 * (n >> 5) + (n & 1), k = WM::kind(j, t), ix = widx<WM>(j, t), tn = (t + MR - 1) & 15, r = (n >> 1) % MR;
        fill(N);
        SB;
        if constexpr (k == 0) smf<WM::RB + 8 * ix>(e[j], m[r], sidx);
        else if constexpr (k == 1) { constexpr int q = lds_cnt<WM>(n); smfv(e[j], m[r], pb[q % NB], sidx); pair_next<WM, q + NB>(pb, wl); }
        else smfv(e[j], m[r], Wc[WM::CBI + ix], sidx);
        if constexpr ((n & 1) == 0 && n + 2 * MR - 2 < 16 * WM::NT) m[(r + MR - 1) % MR] = ldsq(ma, MO + (tn >> 3) * 32768 + (tn & 7) * 1024);
        SB;
    });
}
typedef __attribute__((ext_vector_type(4))) short s16x4;
DEVI v2u ldtr(unsigned p, int off) { return __builtin_bit_cast(v2u, __builtin_amdgcn_ds_read_tr16_b64_v4i16(LDSP(s16x4, LDSP(char, p) + off))); }
DEVI v4u ldtr2(unsigned p, int off) { const v2u a = ldtr(p, off), b = ldtr(p, off + 256); return v4u{a[0], a[1], b[0], b[1]}; }
DEVI void sts1(unsigned p, int off, unsigned v) { *LDSP(unsigned, LDSP(char, p) + off) = v; }
DEVI unsigned lds1(unsigned p, int off) { return *LDSP(const unsigned, LDSP(char, p) + off); }
DEVI void use(unsigned v) { asm volatile("" :: "v"(v) : "memory"); }
DEVI int mk_sidx(int lane) { return opq(0x4444 + (lane & 2) * 0x5555); }
DEVI int crow(int r, int hb) { return 4 * (r >> 1) + 2 * hb + (r & 1); }
DEVI v4u again(int id, unsigned nb, int w) { const u64 k = (u64)__builtin_amdgcn_kernarg_segment_ptr(); return v4u{(unsigned)k, (unsigned)(k >> 32), id < 0 ? ~0u : 256 + id + ((nb >> 4) & 512), (unsigned)w * 64}; }
#define A_LDS 0x28a0114002004410ull
#define A_VAR 0x0000000400020001ull
#define A_LOOP \
"s_brev_b32 s99, 1\ns_brev_b32 s98, 1\ns_brev_b32 s97, 1\ns_brev_b32 s96, 1\n" \
"s_branch 4f\n.p2align 6\n1:\ns_waitcnt lgkmcnt(0)\n" \
"v_mfma_f32_16x16x32_bf16 v[44:47], v[76:79], v[80:83], v[44:47]\nds_read_b128 v[36:39], v25 offset:3072\nds_read_b128 v[40:43], v24 offset:21504\nv_mfma_f32_16x16x32_bf16 v[48:51], v[76:79], v[32:35], v[48:51]\n" \
"ds_read_b128 v[84:87], v24 offset:54272\nv_bfe_u32 v31, v24, 6, 3\nv_mfma_f32_16x16x32_bf16 v[52:55], a[160:163], v[80:83], v[52:55]\ns_mov_b32 m0, s50\n" \
"s_add_u32 s99, s30, 2\nv_lshlrev_b32 v31, 2, v31\nv_mfma_f32_16x16x32_bf16 v[56:59], a[160:163], v[32:35], v[56:59]\ns_min_u32 s99, s99, 63\n" \
"s_lshl_b32 s99, s99, 5\ns_add_u32 s99, s99, s48\nv_mfma_f32_16x16x32_bf16 v[60:63], v[120:123], v[80:83], v[60:63]\ns_and_b32 s94, s30, 7\n" \
"s_lshl_b32 s94, s94, 18\ns_add_u32 s94, s94, s33\nv_mfma_f32_16x16x32_bf16 v[64:67], v[120:123], v[32:35], v[64:67]\ns_add_u32 s31, s31, 0x10000\n" \
"v_mfma_f32_16x16x32_bf16 v[68:71], v[224:227], v[80:83], v[68:71]\ns_waitcnt lgkmcnt(1)\nv_mfma_f32_16x16x32_bf16 v[72:75], v[224:227], v[32:35], v[72:75]\ns_waitcnt lgkmcnt(0)\n" \
"ds_read_b128 v[32:35], v24 offset:22528\nv_mfma_f32_16x16x32_bf16 v[44:47], v[36:39], v[40:43], v[44:47]\nds_read_b128 v[76:79], v24 offset:55296\nv_mfma_f32_16x16x32_bf16 v[48:51], v[36:39], v[84:87], v[48:51]\n" \
"v_mfma_f32_16x16x32_bf16 v[52:55], a[164:167], v[40:43], v[52:55]\nds_read_b128 v[36:39], v25 offset:18432\nv_mfma_f32_16x16x32_bf16 v[56:59], a[164:167], v[84:87], v[56:59]\nv_mfma_f32_16x16x32_bf16 v[60:63], v[124:127], v[40:43], v[60:63]\n" \
"v_mfma_f32_16x16x32_bf16 v[64:67], v[124:127], v[84:87], v[64:67]\nv_mfma_f32_16x16x32_bf16 v[68:71], v[228:231], v[40:43], v[68:71]\ns_waitcnt lgkmcnt(1)\nv_mfma_f32_16x16x32_bf16 v[72:75], v[228:231], v[84:87], v[72:75]\n" \
"ds_read_b128 v[40:43], v24 offset:23552\nv_mfma_f32_16x16x32_bf16 v[44:47], a[64:67], v[32:35], v[44:47]\nds_read_b128 v[80:83], v24 offset:56320\nv_mfma_f32_16x16x32_bf16 v[48:51], a[64:67], v[76:79], v[48:51]\n" \
"v_mfma_f32_16x16x32_bf16 v[52:55], a[168:171], v[32:35], v[52:55]\nds_read_b128 v[84:87], v25 offset:19456\nv_mfma_f32_16x16x32_bf16 v[56:59], a[168:171], v[76:79], v[56:59]\nv_mfma_f32_16x16x32_bf16 v[60:63], v[128:131], v[32:35], v[60:63]\n" \
"s_waitcnt lgkmcnt(3)\nv_mfma_f32_16x16x32_bf16 v[64:67], v[128:131], v[76:79], v[64:67]\nv_mfma_f32_16x16x32_bf16 v[68:71], v[36:39], v[32:35], v[68:71]\ns_waitcnt vmcnt(8) lgkmcnt(1)\n" \
"s_barrier\nv_mfma_f32_16x16x32_bf16 v[72:75], v[36:39], v[76:79], v[72:75]\nds_read_b128 v[32:35], v24 offset:24576\nv_mfma_f32_16x16x32_bf16 v[44:47], a[68:71], v[40:43], v[44:47]\n" \
"ds_read_b128 v[36:39], v24 offset:57344\nv_mfma_f32_16x16x32_bf16 v[48:51], a[68:71], v[80:83], v[48:51]\nds_read_b128 v[76:79], v25 offset:12288\nv_mfma_f32_16x16x32_bf16 v[52:55], a[172:175], v[40:43], v[52:55]\n" \
"buffer_load_dword v31, s[8:11], s99 offen offset:0 sc1 lds\nv_mfma_f32_16x16x32_bf16 v[56:59], a[172:175], v[80:83], v[56:59]\ns_add_u32 m0, s32, 0x4000\nv_mfma_f32_16x16x32_bf16 v[60:63], v[132:135], v[40:43], v[60:63]\n" \
"s_waitcnt lgkmcnt(3)\nv_mfma_f32_16x16x32_bf16 v[64:67], v[132:135], v[80:83], v[64:67]\nv_mfma_f32_16x16x32_bf16 v[68:71], v[84:87], v[40:43], v[68:71]\ns_waitcnt lgkmcnt(1)\n" \
"v_mfma_f32_16x16x32_bf16 v[72:75], v[84:87], v[80:83], v[72:75]\nds_read_b128 v[40:43], v24 offset:25600\nv_mfma_f32_16x16x32_bf16 v[44:47], a[72:75], v[32:35], v[44:47]\nds_read_b128 v[80:83], v24 offset:58368\n" \
"v_mfma_f32_16x16x32_bf16 v[48:51], a[72:75], v[36:39], v[48:51]\nds_read_b128 v[84:87], v25 offset:13312\nv_mfma_f32_16x16x32_bf16 v[52:55], a[176:179], v[32:35], v[52:55]\ns_waitcnt lgkmcnt(3)\n" \
"v_mfma_f32_16x16x32_bf16 v[56:59], a[176:179], v[36:39], v[56:59]\nv_mfma_f32_16x16x32_bf16 v[60:63], v[76:79], v[32:35], v[60:63]\nv_mfma_f32_16x16x32_bf16 v[64:67], v[76:79], v[36:39], v[64:67]\nv_mfma_f32_16x16x32_bf16 v[68:71], v[232:235], v[32:35], v[68:71]\n" \
"s_waitcnt lgkmcnt(1)\nv_mfma_f32_16x16x32_bf16 v[72:75], v[232:235], v[36:39], v[72:75]\nbuffer_load_dwordx4 v24, s[8:11], s94 offen offset:0 sc1 lds\nv_mfma_f32_16x16x32_bf16 v[44:47], a[76:79], v[40:43], v[44:47]\n" \
"ds_read_b128 v[32:35], v24 offset:26624\nv_mfma_f32_16x16x32_bf16 v[48:51], a[76:79], v[80:83], v[48:51]\nds_read_b128 v[36:39], v24 offset:59392\nv_mfma_f32_16x16x32_bf16 v[52:55], a[180:183], v[40:43], v[52:55]\n" \
"s_waitcnt lgkmcnt(2)\nbuffer_load_dwordx4 v24, s[8:11], s94 offen offset:1024 sc1 lds\nv_mfma_f32_16x16x32_bf16 v[56:59], a[180:183], v[80:83], v[56:59]\nbuffer_load_dwordx4 v24, s[8:11], s94 offen offset:2048 sc1 lds\n" \
"v_mfma_f32_16x16x32_bf16 v[60:63], v[84:87], v[40:43], v[60:63]\nbuffer_load_dwordx4 v24, s[8:11], s94 offen offset:3072 sc1 lds\nv_mfma_f32_16x16x32_bf16 v[64:67], v[84:87], v[80:83], v[64:67]\nds_read_b128 v[76:79], v25 offset:20480\n" \
"s_and_b32 s93, s30, 15\nv_mfma_f32_16x16x32_bf16 v[68:71], v[236:239], v[40:43], v[68:71]\ns_lshl_b32 s92, s93, 17\ns_add_u32 s92, s92, s35\n" \
"s_and_b32 s93, s93, 7\nv_mfma_f32_16x16x32_bf16 v[72:75], v[236:239], v[80:83], v[72:75]\ns_waitcnt lgkmcnt(1)\nds_read_b128 v[40:43], v24 offset:27648\n" \
"v_mfma_f32_16x16x32_bf16 v[44:47], a[80:83], v[32:35], v[44:47]\nds_read_b128 v[80:83], v24 offset:60416\ns_lshr_b32 s95, s30, s41\nv_mfma_f32_16x16x32_bf16 v[48:51], a[80:83], v[36:39], v[48:51]\n" \
"s_and_b32 s95, s95, 1\ns_lshl_b32 s93, s93, 14\ns_lshl1_add_u32 s99, s93, s34\nv_mfma_f32_16x16x32_bf16 v[52:55], a[184:187], v[32:35], v[52:55]\n" \
"ds_read_b128 v[84:87], v25 offset:21504\ns_or_b32 s98, s92, s36\nv_mfma_f32_16x16x32_bf16 v[56:59], a[184:187], v[36:39], v[56:59]\ns_or_b32 s97, s92, s37\n" \
"s_and_b32 s92, s92, s39\ns_andn2_b32 s93, s93, s39\nv_mfma_f32_16x16x32_bf16 v[60:63], v[136:139], v[32:35], v[60:63]\nds_read_b128 v[88:91], v25 offset:4096\n" \
"s_add_u32 s96, s92, s38\nv_mfma_f32_16x16x32_bf16 v[64:67], v[136:139], v[36:39], v[64:67]\ns_waitcnt lgkmcnt(4)\ns_add_u32 s96, s96, s93\n" \
"s_add_u32 s30, s30, 1\nv_mfma_f32_16x16x32_bf16 v[68:71], v[76:79], v[32:35], v[68:71]\ns_lshl_b32 s93, s30, 24\ns_cmp_le_u32 s30, 64\n" \
"s_cselect_b32 s51, s14, 0\nv_mfma_f32_16x16x32_bf16 v[72:75], v[76:79], v[36:39], v[72:75]\ns_waitcnt lgkmcnt(2)\nds_read_b128 v[32:35], v24 offset:28672\n" \
"v_mfma_f32_16x16x32_bf16 v[44:47], a[84:87], v[40:43], v[44:47]\nds_read_b128 v[36:39], v24 offset:61440\nv_mfma_f32_16x16x32_bf16 v[48:51], a[84:87], v[80:83], v[48:51]\nv_mfma_f32_16x16x32_bf16 v[52:55], a[188:191], v[40:43], v[52:55]\n" \
"v_mfma_f32_16x16x32_bf16 v[56:59], a[188:191], v[80:83], v[56:59]\nv_mfma_f32_16x16x32_bf16 v[60:63], v[140:143], v[40:43], v[60:63]\ns_waitcnt lgkmcnt(2)\nds_read_b128 v[76:79], v25 offset:5120\n" \
"v_mfma_f32_16x16x32_bf16 v[64:67], v[140:143], v[80:83], v[64:67]\nv_mfma_f32_16x16x32_bf16 v[68:71], v[84:87], v[40:43], v[68:71]\ns_waitcnt lgkmcnt(1)\nv_mfma_f32_16x16x32_bf16 v[72:75], v[84:87], v[80:83], v[72:75]\n" \
"ds_read_b128 v[40:43], v24 offset:29696\nv_mfma_f32_16x16x32_bf16 v[44:47], v[88:91], v[32:35], v[44:47]\nds_read_b128 v[80:83], v24 offset:62464\nv_mfma_f32_16x16x32_bf16 v[48:51], v[88:91], v[36:39], v[48:51]\n" \
"v_mfma_f32_16x16x32_bf16 v[52:55], a[192:195], v[32:35], v[52:55]\nv_mfma_f32_16x16x32_bf16 v[56:59], a[192:195], v[36:39], v[56:59]\nv_mfma_f32_16x16x32_bf16 v[60:63], v[144:147], v[32:35], v[60:63]\nv_mfma_f32_16x16x32_bf16 v[64:67], v[144:147], v[36:39], v[64:67]\n" \
"v_mfma_f32_16x16x32_bf16 v[68:71], v[240:243], v[32:35], v[68:71]\ns_waitcnt lgkmcnt(0)\nv_mfma_f32_16x16x32_bf16 v[72:75], v[240:243], v[36:39], v[72:75]\nds_read_b128 v[32:35], v24 offset:30720\n" \
"v_add_u32 v31, s32, v24\nv_mfma_f32_16x16x32_bf16 v[44:47], v[76:79], v[40:43], v[44:47]\nds_read_b128 v[36:39], v24 offset:63488\nv_mfma_f32_16x16x32_bf16 v[48:51], v[76:79], v[80:83], v[48:51]\n" \
"v_mfma_f32_16x16x32_bf16 v[52:55], a[196:199], v[40:43], v[52:55]\nv_mfma_f32_16x16x32_bf16 v[56:59], a[196:199], v[80:83], v[56:59]\nv_mfma_f32_16x16x32_bf16 v[60:63], v[148:151], v[40:43], v[60:63]\nv_mfma_f32_16x16x32_bf16 v[64:67], v[148:151], v[80:83], v[64:67]\n" \
"v_mfma_f32_16x16x32_bf16 v[68:71], v[244:247], v[40:43], v[68:71]\ns_waitcnt vmcnt(0) lgkmcnt(0)\nds_read_b128 v[40:43], v31 offset:16384\nds_read_b128 v[76:79], v31 offset:17408\n" \
"ds_read_b128 v[84:87], v31 offset:18432\nv_mfma_f32_16x16x32_bf16 v[72:75], v[244:247], v[80:83], v[72:75]\nds_read_b128 v[88:91], v31 offset:19456\nv_mfma_f32_16x16x32_bf16 v[44:47], a[88:91], v[32:35], v[44:47]\n" \
"ds_read_b128 v[80:83], v24 offset:31744\nv_mfma_f32_16x16x32_bf16 v[48:51], a[88:91], v[36:39], v[48:51]\nds_read_b128 v[92:95], v24 offset:64512\nv_mfma_f32_16x16x32_bf16 v[52:55], a[200:203], v[32:35], v[52:55]\n" \
"v_mfma_f32_16x16x32_bf16 v[56:59], a[200:203], v[36:39], v[56:59]\ns_waitcnt lgkmcnt(3)\nv_max3_u32 v96, v40, v76, v84\nv_mov_b64 v[100:101], 0\n" \
"v_mfma_f32_16x16x32_bf16 v[60:63], v[152:155], v[32:35], v[60:63]\nds_read_b128 v[104:107], v25 offset:0\nv_mov_b64 v[102:103], 0\nv_mfma_f32_16x16x32_bf16 v[64:67], v[152:155], v[36:39], v[64:67]\n" \
"s_waitcnt lgkmcnt(3)\nv_max_u32 v96, v96, v88\nv_cmp_eq_u32 vcc, -1, v96\nv_mfma_f32_16x16x32_bf16 v[68:71], v[248:251], v[32:35], v[68:71]\n" \
"ds_read_b128 v[108:111], v25 offset:1024\nv_mov_b64 v[32:33], 0\nv_mfma_f32_16x16x32_bf16 v[72:75], v[248:251], v[36:39], v[72:75]\ns_waitcnt lgkmcnt(2)\n" \
"s_cbranch_vccnz 2f\n3:\nv_mfma_f32_16x16x32_bf16 v[44:47], a[92:95], v[80:83], v[44:47]\ns_barrier\n" \
"s_memrealtime s[56:57]\nv_mfma_f32_16x16x32_bf16 v[52:55], a[204:207], v[80:83], v[52:55]\nds_read_b128 v[36:39], v24 offset:20480\nv_mov_b64 v[34:35], 0\n" \
"v_mfma_f32_16x16x32_bf16 v[60:63], v[156:159], v[80:83], v[60:63]\nds_read_b128 v[40:43], v24 offset:21504\nv_add_u32 v31, s44, v24\nv_mfma_f32_16x16x32_bf16 v[68:71], v[252:255], v[80:83], v[68:71]\n" \
"ds_read_b128 v[76:79], v24 offset:16384\nv_cvt_pk_bf16_f32 v44, v44, v52\nv_mfma_f32_16x16x32_bf16 v[48:51], a[92:95], v[92:95], v[48:51]\nds_read_b128 v[80:83], v24 offset:22528\n" \
"v_cvt_pk_bf16_f32 v45, v45, v53\nv_mfma_f32_16x16x32_bf16 v[56:59], a[204:207], v[92:95], v[56:59]\nds_read_b128 v[84:87], v24 offset:23552\nv_cvt_pk_bf16_f32 v46, v46, v54\n" \
"v_mfma_f32_16x16x32_bf16 v[64:67], v[156:159], v[92:95], v[64:67]\nds_read_b128 v[88:91], v24 offset:17408\nv_cvt_pk_bf16_f32 v47, v47, v55\nv_mfma_f32_16x16x32_bf16 v[72:75], v[252:255], v[92:95], v[72:75]\n" \
"s_waitcnt lgkmcnt(5)\nds_write_b64 v31, v[44:45] offset:0\nv_smfmac_f32_16x16x64_bf16 v[100:103], v[36:39], v[104:111], v26\nds_read_b128 v[52:55], v24 offset:18432\n" \
"v_cvt_pk_bf16_f32 v60, v60, v68\nv_smfmac_f32_16x16x64_bf16 v[32:35], v[36:39], a[120:127], v26\ns_waitcnt lgkmcnt(5)\nds_write_b64 v31, v[46:47] offset:8\n" \
"v_smfmac_f32_16x16x64_bf16 v[100:103], v[40:43], a[24:31], v26\nds_read_b128 v[92:95], v24 offset:19456\nv_cvt_pk_bf16_f32 v61, v61, v69\nv_smfmac_f32_16x16x64_bf16 v[32:35], v[40:43], a[128:135], v26\n" \
"ds_write_b64 v31, v[60:61] offset:32768\nv_smfmac_f32_16x16x64_bf16 v[100:103], v[76:79], v[0:7], v26\nds_read_b128 v[44:47], v24 offset:49152\nv_cvt_pk_bf16_f32 v62, v62, v70\n" \
"v_smfmac_f32_16x16x64_bf16 v[32:35], v[76:79], a[96:103], v26\ns_waitcnt lgkmcnt(5)\ns_add_u32 s94, s56, 0x7e\nv_cvt_pk_bf16_f32 v63, v63, v71\n" \
"v_smfmac_f32_16x16x64_bf16 v[100:103], v[80:83], a[32:39], v26\nds_read_b128 v[68:71], v24 offset:50176\ns_and_b32 s94, s94, 0xffffff\nv_smfmac_f32_16x16x64_bf16 v[32:35], v[80:83], a[136:143], v26\n" \
"ds_read_b128 v[76:79], v25 offset:6144\ns_or_b32 s94, s94, s93\nv_smfmac_f32_16x16x64_bf16 v[100:103], v[84:87], a[40:47], v26\nds_read_b128 v[80:83], v25 offset:7168\n" \
"v_mov_b32 v60, s94\nv_smfmac_f32_16x16x64_bf16 v[32:35], v[84:87], a[144:151], v26\nds_read_b128 v[96:99], v24 offset:51200\nv_cvt_pk_bf16_f32 v48, v48, v56\n" \
"v_smfmac_f32_16x16x64_bf16 v[100:103], v[88:91], a[0:7], v26\nds_read_b128 v[104:107], v25 offset:2048\nv_cvt_pk_bf16_f32 v49, v49, v57\nv_smfmac_f32_16x16x64_bf16 v[32:35], v[88:91], v[8:15], v26\n" \
"s_waitcnt lgkmcnt(5)\nds_read_b128 v[108:111], v25 offset:3072\nv_smfmac_f32_16x16x64_bf16 v[100:103], v[52:55], a[8:15], v26\nds_read_b128 v[84:87], v24 offset:56320\n" \
"v_cvt_pk_bf16_f32 v50, v50, v58\nv_smfmac_f32_16x16x64_bf16 v[32:35], v[52:55], a[104:111], v26\nds_write_b64 v31, v[62:63] offset:32776\nv_smfmac_f32_16x16x64_bf16 v[100:103], v[92:95], a[16:23], v26\n" \
"buffer_store_dword v60, v29, s[8:11], s49 offen offset:0 sc1\nv_smfmac_f32_16x16x64_bf16 v[32:35], v[92:95], a[112:119], v26\nds_read_b128 v[52:55], v24 offset:52224\nv_cvt_pk_bf16_f32 v51, v51, v59\n" \
"v_smfmac_f32_16x16x64_bf16 v[100:103], v[44:47], a[48:55], v26\nds_read_b128 v[56:59], v24 offset:53248\nv_cvt_pk_bf16_f32 v64, v64, v72\nv_smfmac_f32_16x16x64_bf16 v[32:35], v[44:47], a[152:159], v26\n" \
"s_waitcnt lgkmcnt(4)\nds_write_b64 v31, v[48:49] offset:4096\nv_smfmac_f32_16x16x64_bf16 v[100:103], v[68:71], a[56:63], v26\nds_read_b128 v[60:63], v24 offset:54272\n" \
"v_cvt_pk_bf16_f32 v65, v65, v73\nv_smfmac_f32_16x16x64_bf16 v[32:35], v[68:71], v[76:83], v26\nds_read_b128 v[92:95], v24 offset:55296\nv_cvt_pk_bf16_f32 v66, v66, v74\n" \
"v_smfmac_f32_16x16x64_bf16 v[100:103], v[96:99], v[104:111], v26\nds_read_b128 v[68:71], v25 offset:4096\nv_cvt_pk_bf16_f32 v67, v67, v75\nv_smfmac_f32_16x16x64_bf16 v[32:35], v[96:99], a[160:167], v26\n" \
"s_waitcnt lgkmcnt(5)\nds_read_b128 v[72:75], v25 offset:5120\nv_smfmac_f32_16x16x64_bf16 v[100:103], v[84:87], a[88:95], v26\nds_write_b64 v31, v[50:51] offset:4104\n" \
"v_smfmac_f32_16x16x64_bf16 v[32:35], v[84:87], a[200:207], v26\nds_read_b128 v[76:79], v25 offset:14336\nv_mov_b64 v[48:49], 0\nv_smfmac_f32_16x16x64_bf16 v[100:103], v[52:55], a[64:71], v26\n" \
"ds_read_b128 v[80:83], v25 offset:15360\nv_mov_b64 v[50:51], 0\nv_smfmac_f32_16x16x64_bf16 v[32:35], v[52:55], a[168:175], v26\ns_waitcnt lgkmcnt(5)\n" \
"ds_write_b64 v31, v[64:65] offset:36864\nv_smfmac_f32_16x16x64_bf16 v[100:103], v[56:59], a[72:79], v26\nds_read_b128 v[96:99], v24 offset:51200\nv_mov_b64 v[64:65], 0\n" \
"v_smfmac_f32_16x16x64_bf16 v[32:35], v[56:59], a[176:183], v26\nds_write_b64 v31, v[66:67] offset:36872\nv_smfmac_f32_16x16x64_bf16 v[100:103], v[60:63], a[80:87], v26\nds_read_b128 v[56:59], v24 offset:18432\n" \
"v_mov_b64 v[66:67], 0\nv_smfmac_f32_16x16x64_bf16 v[32:35], v[60:63], a[184:191], v26\ns_waitcnt lgkmcnt(5)\nds_read_b32 v31, v28 offset:512\n" \
"v_smfmac_f32_16x16x64_bf16 v[100:103], v[92:95], v[68:75], v26\nv_add_u32 v60, s45, v24\nv_smfmac_f32_16x16x64_bf16 v[32:35], v[92:95], a[192:199], v26\nds_read_b128 v[68:71], v24 offset:54272\n" \
"v_cndmask_b32 v60, v60, v28, s[28:29]\nv_smfmac_f32_16x16x64_bf16 v[48:51], v[40:43], a[240:247], v26\ns_waitcnt lgkmcnt(4)\nds_read_b128 v[104:107], v25 offset:18432\n" \
"v_smfmac_f32_16x16x64_bf16 v[64:67], v[40:43], v[76:83], v26\nds_read_b128 v[108:111], v25 offset:19456\nv_add_f32 v40, v100, v102\nv_smfmac_f32_16x16x64_bf16 v[48:51], v[36:39], a[232:239], v26\n" \
"ds_read_b128 v[72:75], v25 offset:20480\nv_add_f32 v41, v32, v34\nv_smfmac_f32_16x16x64_bf16 v[64:67], v[36:39], v[192:199], v26\nds_read_b128 v[76:79], v25 offset:21504\n" \
"v_add_f32 v42, v101, v103\nv_smfmac_f32_16x16x64_bf16 v[48:51], v[96:99], v[120:127], v26\nds_read_b128 v[36:39], v24 offset:19456\nv_add_f32 v43, v33, v35\n" \
"v_smfmac_f32_16x16x64_bf16 v[64:67], v[96:99], v[224:231], v26\nds_read_b128 v[32:35], v24 offset:50176\nv_cvt_pk_bf16_f32 v62, v40, v41\nv_smfmac_f32_16x16x64_bf16 v[48:51], v[84:87], v[152:159], v26\n" \
"ds_read_b128 v[80:83], v25 offset:10240\nv_cvt_pk_bf16_f32 v63, v42, v43\nv_smfmac_f32_16x16x64_bf16 v[64:67], v[84:87], v[248:255], v26\nds_write2_b32 v27, v62, v63 offset0:0 offset1:4\n" \
"v_smfmac_f32_16x16x64_bf16 v[48:51], v[92:95], v[144:151], v26\ns_waitcnt lgkmcnt(5)\nds_read_b128 v[84:87], v25 offset:11264\nv_smfmac_f32_16x16x64_bf16 v[64:67], v[92:95], v[240:247], v26\n" \
"ds_read_b128 v[40:43], v24 offset:53248\nv_cmp_eq_u32 vcc, -1, v31\nv_smfmac_f32_16x16x64_bf16 v[48:51], v[56:59], v[16:23], v26\nv_smfmac_f32_16x16x64_bf16 v[64:67], v[56:59], v[176:183], v26\n" \
"ds_read_b128 v[92:95], v24 offset:23552\nv_smfmac_f32_16x16x64_bf16 v[48:51], v[52:55], v[128:135], v26\nds_read_b128 v[96:99], v25 offset:12288\nv_smfmac_f32_16x16x64_bf16 v[64:67], v[52:55], v[104:111], v26\n" \
"s_waitcnt lgkmcnt(4)\nds_read_b128 v[100:103], v25 offset:13312\nv_smfmac_f32_16x16x64_bf16 v[48:51], v[68:71], v[136:143], v26\ns_barrier\n" \
"v_smfmac_f32_16x16x64_bf16 v[64:67], v[68:71], v[72:79], v26\nds_read_b128 v[52:55], v25 offset:16384\nv_mov_b32 v63, s43\nv_smfmac_f32_16x16x64_bf16 v[48:51], v[36:39], a[224:231], v26\n" \
"ds_read_b128 v[56:59], v25 offset:17408\nv_cndmask_b32 v76, v63, 0, s[16:17]\nv_smfmac_f32_16x16x64_bf16 v[64:67], v[36:39], v[184:191], v26\ns_waitcnt lgkmcnt(4)\n" \
"ds_read_b128 v[36:39], v24 offset:22528\nv_smfmac_f32_16x16x64_bf16 v[48:51], v[44:47], v[80:87], v26\nds_read_b128 v[68:71], v25 offset:8192\nv_cndmask_b32 v77, v63, 0, s[18:19]\n" \
"v_smfmac_f32_16x16x64_bf16 v[64:67], v[44:47], v[208:215], v26\nds_read_b128 v[72:75], v25 offset:9216\nv_cndmask_b32 v78, v63, 0, s[20:21]\nv_smfmac_f32_16x16x64_bf16 v[48:51], v[32:35], v[112:119], v26\n" \
"ds_read_b128 v[44:47], v24 offset:16384\nv_cndmask_b32 v79, v63, 0, s[22:23]\nv_smfmac_f32_16x16x64_bf16 v[64:67], v[32:35], v[216:223], v26\nds_read_b128 v[80:83], v60 offset:0\n" \
"v_mov_b32 v62, 0xff61b1e6\nv_smfmac_f32_16x16x64_bf16 v[48:51], v[88:91], a[216:223], v26\nds_read_b128 v[32:35], v24 offset:57344\nv_cndmask_b32 v84, v62, v63, s[16:17]\n" \
"v_smfmac_f32_16x16x64_bf16 v[64:67], v[88:91], v[168:175], v26\ns_waitcnt lgkmcnt(6)\nds_read_b128 v[88:91], v24 offset:61440\nv_smfmac_f32_16x16x64_bf16 v[48:51], v[40:43], v[96:103], v26\n" \
"ds_read_b128 v[104:107], v60 offset:1024\nv_cndmask_b32 v85, v62, v63, s[18:19]\nv_smfmac_f32_16x16x64_bf16 v[64:67], v[40:43], v[232:239], v26\nds_read_b128 v[96:99], v24 offset:58368\n" \
"v_cndmask_b32 v86, v62, v63, s[20:21]\nv_smfmac_f32_16x16x64_bf16 v[48:51], v[92:95], a[248:255], v26\nds_read_b128 v[40:43], v24 offset:62464\nv_cndmask_b32 v87, v62, v63, s[22:23]\n" \
"v_smfmac_f32_16x16x64_bf16 v[64:67], v[92:95], v[52:59], v26\ns_waitcnt lgkmcnt(5)\nds_read_b128 v[56:59], v60 offset:2048\nv_smfmac_f32_16x16x64_bf16 v[48:51], v[36:39], v[68:75], v26\n" \
"ds_read_b128 v[52:55], v24 offset:59392\nv_smfmac_f32_16x16x64_bf16 v[64:67], v[36:39], v[200:207], v26\nds_read_b128 v[68:71], v24 offset:63488\nv_smfmac_f32_16x16x64_bf16 v[48:51], v[44:47], a[208:215], v26\n" \
"ds_read_b128 v[36:39], v60 offset:3072\nds_read_b128 v[60:63], v24 offset:60416\nv_smfmac_f32_16x16x64_bf16 v[64:67], v[44:47], v[160:167], v26\ns_waitcnt lgkmcnt(6)\n" \
"ds_read_b128 v[44:47], v24 offset:64512\nv_mfma_f32_16x16x32_bf16 v[76:79], v[32:35], v[80:83], v[76:79]\nds_read_b128 v[72:75], v24 offset:0\nv_add_f32 v32, v48, v50\n" \
"v_add_f32 v34, v49, v51\nv_mfma_f32_16x16x32_bf16 v[84:87], v[88:91], v[80:83], v[84:87]\ns_waitcnt lgkmcnt(6)\nv_add_f32 v33, v64, v66\n" \
"v_add_f32 v35, v65, v67\nv_cvt_pk_bf16_f32 v48, v32, v33\nv_cvt_pk_bf16_f32 v49, v34, v35\nv_mfma_f32_16x16x32_bf16 v[76:79], v[96:99], v[104:107], v[76:79]\n" \
"s_waitcnt lgkmcnt(4)\nds_write2_b32 v27, v48, v49 offset0:128 offset1:132\nv_mfma_f32_16x16x32_bf16 v[84:87], v[40:43], v[104:107], v[84:87]\nds_read_b128 v[32:35], v24 offset:32768\n" \
"v_mfma_f32_16x16x32_bf16 v[76:79], v[52:55], v[56:59], v[76:79]\ns_waitcnt lgkmcnt(4)\ns_cmp_eq_u64 vcc, 0\ns_cselect_b32 s14, s10, 0\n" \
"v_mfma_f32_16x16x32_bf16 v[84:87], v[68:71], v[56:59], v[84:87]\ns_cmp_eq_u32 s51, 0\ns_cbranch_scc1 4f\n5:\n" \
"v_mfma_f32_16x16x32_bf16 v[76:79], v[60:63], v[36:39], v[76:79]\ns_waitcnt lgkmcnt(1)\ns_barrier\nds_read_b128 v[40:43], v28 offset:512\n" \
"ds_read_b128 v[48:51], v28 offset:1536\nv_mfma_f32_16x16x32_bf16 v[84:87], v[44:47], v[36:39], v[84:87]\nds_read_b128 v[64:67], v28 offset:2560\nds_read_b128 v[88:91], v28 offset:3584\n" \
"v_mfma_f32_16x16x32_bf16 v[44:47], v[0:3], v[72:75], 0\nds_read_b128 v[92:95], v24 offset:1024\nv_max3_f32 v31, v76, v77, v78\nv_max3_f32 v31, v31, v79, s47\n" \
"v_mfma_f32_16x16x32_bf16 v[52:55], a[96:99], v[72:75], 0\nds_read_b128 v[96:99], v24 offset:33792\nv_max3_f32 v100, v84, v85, v86\nv_max3_f32 v101, v31, v100, v87\n" \
"v_max3_f32 v31, v31, v100, v87\nv_mfma_f32_16x16x32_bf16 v[60:63], a[208:211], v[72:75], 0\ns_waitcnt lgkmcnt(6)\nv_mul_f32 v100, s46, v31\n" \
"v_fma_f32 v76, v76, s46, -v100\nv_exp_f32 v76, v76\nv_mfma_f32_16x16x32_bf16 v[68:71], v[160:163], v[72:75], 0\ns_waitcnt lgkmcnt(4)\n" \
"v_fma_f32 v78, v78, s46, -v100\nv_exp_f32 v78, v78\nv_fma_f32 v77, v77, s46, -v100\nv_fma_f32 v79, v79, s46, -v100\n" \
"v_mfma_f32_16x16x32_bf16 v[108:111], v[40:43], v[80:83], 0\nv_exp_f32 v77, v77\nv_fma_f32 v84, v84, s46, -v100\nv_fma_f32 v86, v86, s46, -v100\n" \
"v_fma_f32 v85, v85, s46, -v100\nv_mfma_f32_16x16x32_bf16 v[108:111], v[48:51], v[104:107], v[108:111]\ns_waitcnt lgkmcnt(0)\nv_fma_f32 v87, v87, s46, -v100\n" \
"v_cndmask_b32 v40, v76, v78, s[24:25]\nv_mfma_f32_16x16x32_bf16 v[108:111], v[64:67], v[56:59], v[108:111]\nv_exp_f32 v79, v79\nv_mfma_f32_16x16x32_bf16 v[108:111], v[88:91], v[36:39], v[108:111]\n" \
"v_exp_f32 v84, v84\nv_mfma_f32_16x16x32_bf16 v[48:51], v[0:3], v[32:35], 0\nv_exp_f32 v86, v86\nv_exp_f32 v85, v85\n" \
"v_mfma_f32_16x16x32_bf16 v[56:59], a[96:99], v[32:35], 0\nv_exp_f32 v87, v87\nv_cndmask_b32 v41, v77, v79, s[24:25]\nv_cndmask_b32 v42, v84, v86, s[24:25]\n" \
"v_mfma_f32_16x16x32_bf16 v[64:67], a[208:211], v[32:35], 0\nv_cndmask_b32 v43, v85, v87, s[24:25]\nv_permlane32_swap_b32 v31, v101\nv_mfma_f32_16x16x32_bf16 v[72:75], v[160:163], v[32:35], 0\n" \
"v_max3_f32 v36, v108, v109, v110\nv_max3_f32 v31, v31, v101, v111\nv_max_f32 v37, v36, v31\nv_max_f32 v36, v36, v31\n" \
"v_mfma_f32_16x16x32_bf16 v[44:47], v[4:7], v[92:95], v[44:47]\nds_read_b128 v[32:35], v24 offset:2048\nv_permlane16_swap_b32 v36, v37\nv_mfma_f32_16x16x32_bf16 v[48:51], v[4:7], v[96:99], v[48:51]\n" \
"ds_read_b128 v[80:83], v24 offset:34816\nv_max_f32 v36, v36, v37\nv_mfma_f32_16x16x32_bf16 v[52:55], a[100:103], v[92:95], v[52:55]\nv_cndmask_b32 v38, v108, v110, s[26:27]\n" \
"v_fma_f32 v100, v36, -s46, v100\nv_mul_f32 v36, s46, v36\nv_cndmask_b32 v39, v109, v111, s[26:27]\nv_fma_f32 v38, v38, s46, -v36\n" \
"v_mfma_f32_16x16x32_bf16 v[56:59], a[100:103], v[96:99], v[56:59]\nv_add_f32 v88, v76, v78\nv_add_f32 v89, v77, v79\nv_fma_f32 v39, v39, s46, -v36\n" \
"v_add_f32 v90, v84, v86\nv_add_f32 v88, v88, v89\nv_mfma_f32_16x16x32_bf16 v[60:63], a[212:215], v[92:95], v[60:63]\nv_exp_f32 v100, v100\n" \
"v_exp_f32 v38, v38\nv_add_f32 v91, v85, v87\nv_mfma_f32_16x16x32_bf16 v[64:67], a[212:215], v[96:99], v[64:67]\nv_add_f32 v88, v88, v90\n" \
"v_exp_f32 v39, v39\nv_add_f32 v88, v88, v91\nv_fma_f32 v88, v88, v100, v38\nv_mfma_f32_16x16x32_bf16 v[68:71], v[164:167], v[92:95], v[68:71]\n" \
"s_waitcnt lgkmcnt(0)\nv_add_f32 v89, v88, v39\nv_add_f32 v88, v39, v88\nv_mfma_f32_16x16x32_bf16 v[72:75], v[164:167], v[96:99], v[72:75]\n" \
"ds_read_b128 v[92:95], v24 offset:3072\nv_permlane16_swap_b32 v88, v89\nv_add_f32 v90, v88, v89\nv_add_f32 v88, v89, v88\n" \
"v_mfma_f32_16x16x32_bf16 v[44:47], a[0:3], v[32:35], v[44:47]\nds_read_b128 v[96:99], v24 offset:35840\nv_permlane32_swap_b32 v88, v90\nv_mfma_f32_16x16x32_bf16 v[48:51], a[0:3], v[80:83], v[48:51]\n" \
"v_add_f32 v88, v88, v90\nv_mfma_f32_16x16x32_bf16 v[52:55], v[8:11], v[32:35], v[52:55]\nv_rcp_f32 v88, v88\nv_mfma_f32_16x16x32_bf16 v[56:59], v[8:11], v[80:83], v[56:59]\n" \
"v_mul_f32 v100, v100, v88\nv_mul_f32 v40, v40, v100\nv_mul_f32 v41, v41, v100\nv_mul_f32 v38, v38, v88\n" \
"v_mul_f32 v39, v39, v88\nv_mfma_f32_16x16x32_bf16 v[60:63], a[216:219], v[32:35], v[60:63]\nv_mul_f32 v42, v42, v100\nv_mul_f32 v43, v43, v100\n" \
"v_cvt_pk_bf16_f32 v36, v40, v41\nv_cvt_pk_bf16_f32 v31, v38, v39\nv_cvt_pk_bf16_f32 v37, v42, v43\nv_mfma_f32_16x16x32_bf16 v[64:67], a[216:219], v[80:83], v[64:67]\n" \
"v_and_or_b32 v36, v36, -2, s95\nv_and_or_b32 v31, v31, -2, s95\nv_mfma_f32_16x16x32_bf16 v[68:71], v[168:171], v[32:35], v[68:71]\ns_waitcnt lgkmcnt(0)\n" \
"buffer_store_dwordx2 v[36:37], v29, s[8:11], s99 offen offset:0 sc1\nbuffer_store_dword v31, v30, s[8:11], s96 offen offset:1536 sc1\nv_mfma_f32_16x16x32_bf16 v[72:75], v[168:171], v[80:83], v[72:75]\nds_read_b128 v[88:91], v24 offset:4096\n" \
"v_mul_f32 v76, v76, v100\nv_mfma_f32_16x16x32_bf16 v[44:47], a[4:7], v[92:95], v[44:47]\nds_read_b128 v[32:35], v24 offset:36864\nv_mul_f32 v77, v77, v100\n" \
"v_mfma_f32_16x16x32_bf16 v[48:51], a[4:7], v[96:99], v[48:51]\nv_mul_f32 v78, v78, v100\nv_mul_f32 v79, v79, v100\nv_mfma_f32_16x16x32_bf16 v[52:55], v[12:15], v[92:95], v[52:55]\n" \
"v_cvt_pk_bf16_f32 v80, v76, v77\nv_cvt_pk_bf16_f32 v81, v78, v79\nv_mfma_f32_16x16x32_bf16 v[56:59], v[12:15], v[96:99], v[56:59]\ns_add_u32 m0, s32, 0x4000\n" \
"v_and_or_b32 v80, v80, -2, s95\nv_mul_f32 v84, v84, v100\nv_mfma_f32_16x16x32_bf16 v[60:63], a[220:223], v[92:95], v[60:63]\nbuffer_store_dwordx2 v[80:81], v29, s[8:11], s97 offen offset:1024 sc1\n" \
"v_mul_f32 v85, v85, v100\nv_mfma_f32_16x16x32_bf16 v[64:67], a[220:223], v[96:99], v[64:67]\ns_add_u32 s99, s31, 0x4000\nv_mul_f32 v86, v86, v100\n" \
"v_mul_f32 v87, v87, v100\nv_mfma_f32_16x16x32_bf16 v[68:71], v[172:175], v[92:95], v[68:71]\ns_waitcnt lgkmcnt(0)\nv_cvt_pk_bf16_f32 v82, v84, v85\n" \
"v_cvt_pk_bf16_f32 v83, v86, v87\nv_mfma_f32_16x16x32_bf16 v[72:75], v[172:175], v[96:99], v[72:75]\nds_read_b128 v[36:39], v24 offset:5120\nv_mfma_f32_16x16x32_bf16 v[44:47], a[8:11], v[88:91], v[44:47]\n" \
"ds_read_b128 v[40:43], v24 offset:37888\nv_mfma_f32_16x16x32_bf16 v[48:51], a[8:11], v[32:35], v[48:51]\nbuffer_store_dwordx4 v[80:83], v24, s[8:11], s98 offen offset:0 sc1\nbuffer_load_dwordx4 v24, s[8:11], s99 offen offset:0 sc1 lds\n" \
"v_mfma_f32_16x16x32_bf16 v[52:55], a[104:107], v[88:91], v[52:55]\nv_mfma_f32_16x16x32_bf16 v[56:59], a[104:107], v[32:35], v[56:59]\nbuffer_load_dwordx4 v24, s[8:11], s99 offen offset:1024 sc1 lds\nv_mfma_f32_16x16x32_bf16 v[60:63], v[16:19], v[88:91], v[60:63]\n" \
"v_mfma_f32_16x16x32_bf16 v[64:67], v[16:19], v[32:35], v[64:67]\nv_mfma_f32_16x16x32_bf16 v[68:71], v[176:179], v[88:91], v[68:71]\ns_waitcnt lgkmcnt(0)\nv_mfma_f32_16x16x32_bf16 v[72:75], v[176:179], v[32:35], v[72:75]\n" \
"ds_read_b128 v[76:79], v24 offset:6144\nv_mfma_f32_16x16x32_bf16 v[44:47], a[12:15], v[36:39], v[44:47]\nds_read_b128 v[32:35], v24 offset:38912\nv_mfma_f32_16x16x32_bf16 v[48:51], a[12:15], v[40:43], v[48:51]\n" \
"buffer_load_dwordx4 v24, s[8:11], s99 offen offset:2048 sc1 lds\nv_mfma_f32_16x16x32_bf16 v[52:55], a[108:111], v[36:39], v[52:55]\nv_mfma_f32_16x16x32_bf16 v[56:59], a[108:111], v[40:43], v[56:59]\nbuffer_load_dwordx4 v24, s[8:11], s99 offen offset:3072 sc1 lds\n" \
"v_mfma_f32_16x16x32_bf16 v[60:63], v[20:23], v[36:39], v[60:63]\nv_mfma_f32_16x16x32_bf16 v[64:67], v[20:23], v[40:43], v[64:67]\nv_mfma_f32_16x16x32_bf16 v[68:71], v[180:183], v[36:39], v[68:71]\ns_waitcnt lgkmcnt(0)\n" \
"v_mfma_f32_16x16x32_bf16 v[72:75], v[180:183], v[40:43], v[72:75]\nds_read_b128 v[36:39], v24 offset:7168\nv_mfma_f32_16x16x32_bf16 v[44:47], a[16:19], v[76:79], v[44:47]\nds_read_b128 v[40:43], v24 offset:39936\n" \
"v_mfma_f32_16x16x32_bf16 v[48:51], a[16:19], v[32:35], v[48:51]\ns_add_u32 m0, s32, 0x6000\ns_add_u32 s99, s31, 0x6000\nv_mfma_f32_16x16x32_bf16 v[52:55], a[112:115], v[76:79], v[52:55]\n" \
"v_mfma_f32_16x16x32_bf16 v[56:59], a[112:115], v[32:35], v[56:59]\nv_mfma_f32_16x16x32_bf16 v[60:63], a[224:227], v[76:79], v[60:63]\nds_read_b128 v[80:83], v25 offset:0\nv_mfma_f32_16x16x32_bf16 v[64:67], a[224:227], v[32:35], v[64:67]\n" \
"buffer_load_dwordx4 v24, s[8:11], s99 offen offset:0 sc1 lds\nv_mfma_f32_16x16x32_bf16 v[68:71], v[184:187], v[76:79], v[68:71]\ns_waitcnt lgkmcnt(1)\nv_mfma_f32_16x16x32_bf16 v[72:75], v[184:187], v[32:35], v[72:75]\n" \
"ds_read_b128 v[76:79], v24 offset:8192\nv_mfma_f32_16x16x32_bf16 v[44:47], a[20:23], v[36:39], v[44:47]\nds_read_b128 v[32:35], v24 offset:40960\nv_mfma_f32_16x16x32_bf16 v[48:51], a[20:23], v[40:43], v[48:51]\n" \
"buffer_load_dwordx4 v24, s[8:11], s99 offen offset:1024 sc1 lds\nv_mfma_f32_16x16x32_bf16 v[52:55], a[116:119], v[36:39], v[52:55]\nv_mfma_f32_16x16x32_bf16 v[56:59], a[116:119], v[40:43], v[56:59]\nv_mfma_f32_16x16x32_bf16 v[60:63], a[228:231], v[36:39], v[60:63]\n" \
"ds_read_b128 v[84:87], v25 offset:1024\nv_mfma_f32_16x16x32_bf16 v[64:67], a[228:231], v[40:43], v[64:67]\nbuffer_load_dwordx4 v24, s[8:11], s99 offen offset:2048 sc1 lds\nv_mfma_f32_16x16x32_bf16 v[68:71], v[188:191], v[36:39], v[68:71]\n" \
"s_waitcnt lgkmcnt(1)\nv_mfma_f32_16x16x32_bf16 v[72:75], v[188:191], v[40:43], v[72:75]\nds_read_b128 v[36:39], v24 offset:9216\nv_mfma_f32_16x16x32_bf16 v[44:47], v[80:83], v[76:79], v[44:47]\n" \
"ds_read_b128 v[40:43], v24 offset:41984\nv_mfma_f32_16x16x32_bf16 v[48:51], v[80:83], v[32:35], v[48:51]\nbuffer_load_dwordx4 v24, s[8:11], s99 offen offset:3072 sc1 lds\nv_mfma_f32_16x16x32_bf16 v[52:55], a[120:123], v[76:79], v[52:55]\n" \
"v_mfma_f32_16x16x32_bf16 v[56:59], a[120:123], v[32:35], v[56:59]\nv_mfma_f32_16x16x32_bf16 v[60:63], a[232:235], v[76:79], v[60:63]\nv_mfma_f32_16x16x32_bf16 v[64:67], a[232:235], v[32:35], v[64:67]\nv_mfma_f32_16x16x32_bf16 v[68:71], v[192:195], v[76:79], v[68:71]\n" \
"s_waitcnt lgkmcnt(0)\nv_mfma_f32_16x16x32_bf16 v[72:75], v[192:195], v[32:35], v[72:75]\nds_read_b128 v[76:79], v24 offset:10240\nv_mfma_f32_16x16x32_bf16 v[44:47], v[84:87], v[36:39], v[44:47]\n" \
"ds_read_b128 v[32:35], v24 offset:43008\nv_mfma_f32_16x16x32_bf16 v[48:51], v[84:87], v[40:43], v[48:51]\nv_mfma_f32_16x16x32_bf16 v[52:55], a[124:127], v[36:39], v[52:55]\nds_read_b128 v[80:83], v25 offset:14336\n" \
"v_mfma_f32_16x16x32_bf16 v[56:59], a[124:127], v[40:43], v[56:59]\nv_mfma_f32_16x16x32_bf16 v[60:63], a[236:239], v[36:39], v[60:63]\nv_mfma_f32_16x16x32_bf16 v[64:67], a[236:239], v[40:43], v[64:67]\nv_mfma_f32_16x16x32_bf16 v[68:71], v[196:199], v[36:39], v[68:71]\n" \
"s_waitcnt lgkmcnt(1)\nv_mfma_f32_16x16x32_bf16 v[72:75], v[196:199], v[40:43], v[72:75]\nds_read_b128 v[36:39], v24 offset:11264\nv_mfma_f32_16x16x32_bf16 v[44:47], a[24:27], v[76:79], v[44:47]\n" \
"ds_read_b128 v[40:43], v24 offset:44032\nv_mfma_f32_16x16x32_bf16 v[48:51], a[24:27], v[32:35], v[48:51]\nv_mfma_f32_16x16x32_bf16 v[52:55], a[128:131], v[76:79], v[52:55]\nds_read_b128 v[84:87], v25 offset:15360\n" \
"v_mfma_f32_16x16x32_bf16 v[56:59], a[128:131], v[32:35], v[56:59]\nv_mfma_f32_16x16x32_bf16 v[60:63], a[240:243], v[76:79], v[60:63]\ns_waitcnt lgkmcnt(3)\nv_mfma_f32_16x16x32_bf16 v[64:67], a[240:243], v[32:35], v[64:67]\n" \
"v_mfma_f32_16x16x32_bf16 v[68:71], v[80:83], v[76:79], v[68:71]\ns_waitcnt lgkmcnt(1)\nv_mfma_f32_16x16x32_bf16 v[72:75], v[80:83], v[32:35], v[72:75]\nds_read_b128 v[76:79], v24 offset:12288\n" \
"v_mfma_f32_16x16x32_bf16 v[44:47], a[28:31], v[36:39], v[44:47]\nds_read_b128 v[32:35], v24 offset:45056\nv_mfma_f32_16x16x32_bf16 v[48:51], a[28:31], v[40:43], v[48:51]\nds_read_b128 v[80:83], v25 offset:8192\n" \
"v_mfma_f32_16x16x32_bf16 v[52:55], a[132:135], v[36:39], v[52:55]\nv_mfma_f32_16x16x32_bf16 v[56:59], a[132:135], v[40:43], v[56:59]\nv_mfma_f32_16x16x32_bf16 v[60:63], a[244:247], v[36:39], v[60:63]\ns_waitcnt lgkmcnt(3)\n" \
"v_mfma_f32_16x16x32_bf16 v[64:67], a[244:247], v[40:43], v[64:67]\nv_mfma_f32_16x16x32_bf16 v[68:71], v[84:87], v[36:39], v[68:71]\ns_waitcnt lgkmcnt(1)\nv_mfma_f32_16x16x32_bf16 v[72:75], v[84:87], v[40:43], v[72:75]\n" \
"ds_read_b128 v[36:39], v24 offset:13312\nv_mfma_f32_16x16x32_bf16 v[44:47], a[32:35], v[76:79], v[44:47]\nds_read_b128 v[40:43], v24 offset:46080\nv_mfma_f32_16x16x32_bf16 v[48:51], a[32:35], v[32:35], v[48:51]\n" \
"ds_read_b128 v[84:87], v25 offset:9216\nv_mfma_f32_16x16x32_bf16 v[52:55], a[136:139], v[76:79], v[52:55]\ns_waitcnt lgkmcnt(3)\nv_mfma_f32_16x16x32_bf16 v[56:59], a[136:139], v[32:35], v[56:59]\n" \
"v_mfma_f32_16x16x32_bf16 v[60:63], v[80:83], v[76:79], v[60:63]\nv_mfma_f32_16x16x32_bf16 v[64:67], v[80:83], v[32:35], v[64:67]\nv_mfma_f32_16x16x32_bf16 v[68:71], v[200:203], v[76:79], v[68:71]\ns_waitcnt lgkmcnt(1)\n" \
"v_mfma_f32_16x16x32_bf16 v[72:75], v[200:203], v[32:35], v[72:75]\nds_read_b128 v[76:79], v24 offset:14336\nv_mfma_f32_16x16x32_bf16 v[44:47], a[36:39], v[36:39], v[44:47]\nds_read_b128 v[32:35], v24 offset:47104\n" \
"v_mfma_f32_16x16x32_bf16 v[48:51], a[36:39], v[40:43], v[48:51]\nv_mfma_f32_16x16x32_bf16 v[52:55], a[140:143], v[36:39], v[52:55]\ns_waitcnt lgkmcnt(2)\nds_read_b128 v[80:83], v25 offset:16384\n" \
"v_mfma_f32_16x16x32_bf16 v[56:59], a[140:143], v[40:43], v[56:59]\nv_mfma_f32_16x16x32_bf16 v[60:63], v[84:87], v[36:39], v[60:63]\nv_mfma_f32_16x16x32_bf16 v[64:67], v[84:87], v[40:43], v[64:67]\nv_mfma_f32_16x16x32_bf16 v[68:71], v[204:207], v[36:39], v[68:71]\n" \
"s_waitcnt lgkmcnt(1)\nv_mfma_f32_16x16x32_bf16 v[72:75], v[204:207], v[40:43], v[72:75]\nds_read_b128 v[36:39], v24 offset:15360\nv_mfma_f32_16x16x32_bf16 v[44:47], a[40:43], v[76:79], v[44:47]\n" \
"ds_read_b128 v[40:43], v24 offset:48128\nv_mfma_f32_16x16x32_bf16 v[48:51], a[40:43], v[32:35], v[48:51]\nv_mfma_f32_16x16x32_bf16 v[52:55], a[144:147], v[76:79], v[52:55]\nds_read_b128 v[84:87], v25 offset:17408\n" \
"v_mfma_f32_16x16x32_bf16 v[56:59], a[144:147], v[32:35], v[56:59]\nv_mfma_f32_16x16x32_bf16 v[60:63], a[248:251], v[76:79], v[60:63]\ns_waitcnt lgkmcnt(3)\nv_mfma_f32_16x16x32_bf16 v[64:67], a[248:251], v[32:35], v[64:67]\n" \
"s_waitcnt vmcnt(4) lgkmcnt(1)\ns_barrier\nv_mfma_f32_16x16x32_bf16 v[68:71], v[80:83], v[76:79], v[68:71]\ns_add_u32 m0, s32, 0\n" \
"s_add_u32 s99, s31, 0x10000\nv_mfma_f32_16x16x32_bf16 v[72:75], v[80:83], v[32:35], v[72:75]\nds_read_b128 v[76:79], v24 offset:16384\nv_mfma_f32_16x16x32_bf16 v[44:47], a[44:47], v[36:39], v[44:47]\n" \
"ds_read_b128 v[32:35], v24 offset:49152\nv_mfma_f32_16x16x32_bf16 v[48:51], a[44:47], v[40:43], v[48:51]\nds_read_b128 v[80:83], v25 offset:10240\nv_mfma_f32_16x16x32_bf16 v[52:55], a[148:151], v[36:39], v[52:55]\n" \
"buffer_load_dwordx4 v24, s[12:15], s99 offen offset:0 sc1 lds\nv_mfma_f32_16x16x32_bf16 v[56:59], a[148:151], v[40:43], v[56:59]\nv_mfma_f32_16x16x32_bf16 v[60:63], a[252:255], v[36:39], v[60:63]\ns_waitcnt lgkmcnt(3)\n" \
"v_mfma_f32_16x16x32_bf16 v[64:67], a[252:255], v[40:43], v[64:67]\nbuffer_load_dwordx4 v24, s[12:15], s99 offen offset:1024 sc1 lds\nv_mfma_f32_16x16x32_bf16 v[68:71], v[84:87], v[36:39], v[68:71]\ns_waitcnt lgkmcnt(1)\n" \
"v_mfma_f32_16x16x32_bf16 v[72:75], v[84:87], v[40:43], v[72:75]\nds_read_b128 v[36:39], v24 offset:17408\nv_mfma_f32_16x16x32_bf16 v[44:47], a[48:51], v[76:79], v[44:47]\nds_read_b128 v[40:43], v24 offset:50176\n" \
"v_mfma_f32_16x16x32_bf16 v[48:51], a[48:51], v[32:35], v[48:51]\nds_read_b128 v[84:87], v25 offset:11264\nv_mfma_f32_16x16x32_bf16 v[52:55], a[152:155], v[76:79], v[52:55]\ns_waitcnt lgkmcnt(3)\n" \
"buffer_load_dwordx4 v24, s[12:15], s99 offen offset:2048 sc1 lds\nv_mfma_f32_16x16x32_bf16 v[56:59], a[152:155], v[32:35], v[56:59]\nv_mfma_f32_16x16x32_bf16 v[60:63], v[80:83], v[76:79], v[60:63]\nv_mfma_f32_16x16x32_bf16 v[64:67], v[80:83], v[32:35], v[64:67]\n" \
"buffer_load_dwordx4 v24, s[12:15], s99 offen offset:3072 sc1 lds\nv_mfma_f32_16x16x32_bf16 v[68:71], v[208:211], v[76:79], v[68:71]\ns_waitcnt lgkmcnt(1)\nds_read_b128 v[76:79], v25 offset:6144\n" \
"v_mfma_f32_16x16x32_bf16 v[72:75], v[208:211], v[32:35], v[72:75]\nds_read_b128 v[80:83], v24 offset:18432\nv_mfma_f32_16x16x32_bf16 v[44:47], a[52:55], v[36:39], v[44:47]\nds_read_b128 v[32:35], v24 offset:51200\n" \
"s_add_u32 m0, s32, 0x2000\nv_mfma_f32_16x16x32_bf16 v[48:51], a[52:55], v[40:43], v[48:51]\ns_add_u32 s99, s31, 0x12000\nv_mfma_f32_16x16x32_bf16 v[52:55], a[156:159], v[36:39], v[52:55]\n" \
"s_waitcnt lgkmcnt(3)\nv_mfma_f32_16x16x32_bf16 v[56:59], a[156:159], v[40:43], v[56:59]\nv_mfma_f32_16x16x32_bf16 v[60:63], v[84:87], v[36:39], v[60:63]\nbuffer_load_dwordx4 v24, s[12:15], s99 offen offset:0 sc1 lds\n" \
"v_mfma_f32_16x16x32_bf16 v[64:67], v[84:87], v[40:43], v[64:67]\nv_mfma_f32_16x16x32_bf16 v[68:71], v[212:215], v[36:39], v[68:71]\ns_waitcnt lgkmcnt(0)\nds_read_b128 v[36:39], v25 offset:7168\n" \
"v_mfma_f32_16x16x32_bf16 v[72:75], v[212:215], v[40:43], v[72:75]\nds_read_b128 v[84:87], v24 offset:19456\nv_mfma_f32_16x16x32_bf16 v[44:47], a[56:59], v[80:83], v[44:47]\nds_read_b128 v[40:43], v24 offset:52224\n" \
"v_mfma_f32_16x16x32_bf16 v[48:51], a[56:59], v[32:35], v[48:51]\nbuffer_load_dwordx4 v24, s[12:15], s99 offen offset:1024 sc1 lds\nv_mfma_f32_16x16x32_bf16 v[52:55], v[76:79], v[80:83], v[52:55]\nv_mfma_f32_16x16x32_bf16 v[56:59], v[76:79], v[32:35], v[56:59]\n" \
"v_mfma_f32_16x16x32_bf16 v[60:63], v[112:115], v[80:83], v[60:63]\nds_read_b128 v[76:79], v25 offset:2048\nv_mfma_f32_16x16x32_bf16 v[64:67], v[112:115], v[32:35], v[64:67]\nbuffer_load_dwordx4 v24, s[12:15], s99 offen offset:2048 sc1 lds\n" \
"v_mfma_f32_16x16x32_bf16 v[68:71], v[216:219], v[80:83], v[68:71]\ns_waitcnt lgkmcnt(1)\nv_mfma_f32_16x16x32_bf16 v[72:75], v[216:219], v[32:35], v[72:75]\nds_read_b128 v[80:83], v24 offset:20480\n" \
"v_mfma_f32_16x16x32_bf16 v[44:47], a[60:63], v[84:87], v[44:47]\nds_read_b128 v[32:35], v24 offset:53248\nv_mfma_f32_16x16x32_bf16 v[48:51], a[60:63], v[40:43], v[48:51]\nbuffer_load_dwordx4 v24, s[12:15], s99 offen offset:3072 sc1 lds\n" \
"v_mfma_f32_16x16x32_bf16 v[52:55], v[36:39], v[84:87], v[52:55]\nv_mfma_f32_16x16x32_bf16 v[56:59], v[36:39], v[40:43], v[56:59]\nv_mfma_f32_16x16x32_bf16 v[60:63], v[116:119], v[84:87], v[60:63]\nv_mfma_f32_16x16x32_bf16 v[64:67], v[116:119], v[40:43], v[64:67]\n" \
"v_mfma_f32_16x16x32_bf16 v[68:71], v[220:223], v[84:87], v[68:71]\ns_cmp_eq_u32 s30, 64\nv_mfma_f32_16x16x32_bf16 v[72:75], v[220:223], v[40:43], v[72:75]\ns_cbranch_scc0 1b\n" \
"s_waitcnt vmcnt(0)\ns_barrier\nv_mov_b32 v40, 0x41000000\nbuffer_store_dword v40, v29, s[8:11], s49 offen offset:0 sc1\n" \
"s_branch 10f\n2:\ns_add_u32 s52, s52, 1\ns_cmp_lt_u32 s52, 0x40000\n" \
"s_cbranch_scc0 9f\nv_cmp_eq_u32 vcc, -1, v40\ns_cbranch_vccz 6f\nbuffer_load_dwordx4 v24, s[8:11], s94 offen offset:0 sc1 lds\n" \
"6:\nv_cmp_eq_u32 vcc, -1, v76\ns_cbranch_vccz 6f\nbuffer_load_dwordx4 v24, s[8:11], s94 offen offset:1024 sc1 lds\n" \
"6:\nv_cmp_eq_u32 vcc, -1, v84\ns_cbranch_vccz 6f\nbuffer_load_dwordx4 v24, s[8:11], s94 offen offset:2048 sc1 lds\n" \
"6:\nv_cmp_eq_u32 vcc, -1, v88\ns_cbranch_vccz 6f\nbuffer_load_dwordx4 v24, s[8:11], s94 offen offset:3072 sc1 lds\n" \
"6:\ns_waitcnt vmcnt(0)\nds_read_b128 v[40:43], v31 offset:16384\nds_read_b128 v[76:79], v31 offset:17408\n" \
"ds_read_b128 v[84:87], v31 offset:18432\nds_read_b128 v[88:91], v31 offset:19456\ns_waitcnt lgkmcnt(0)\nv_max3_u32 v96, v40, v76, v84\n" \
"v_max_u32 v96, v96, v88\nv_cmp_eq_u32 vcc, -1, v96\ns_cbranch_vccnz 2b\ns_branch 3b\n" \
"4:\ns_cmp_eq_u32 s30, 64\ns_cbranch_scc1 5b\nv_bfe_u32 v40, v24, 6, 3\n" \
"v_lshlrev_b32 v40, 2, v40\ns_lshl_b32 s94, s30, 5\ns_add_u32 s94, s94, s48\ns_cmp_lg_u32 s58, 0\n" \
"s_cbranch_scc1 8f\n7:\nbuffer_load_dword v41, v40, s[8:11], s94 offen sc1\ns_waitcnt vmcnt(0)\n" \
"v_cmp_eq_u32 vcc, -1, v41\ns_cbranch_vccz 8f\ns_add_u32 s52, s52, 1\ns_cmp_lt_u32 s52, 0x40000\n" \
"s_cbranch_scc0 9f\ns_sleep 8\ns_branch 7b\n8:\n" \
"s_mov_b32 m0, s53\ns_add_u32 s94, s30, 1\ns_min_u32 s94, s94, 63\ns_lshl_b32 s94, s94, 5\n" \
"s_add_u32 s94, s94, s48\nbuffer_load_dword v40, s[8:11], s94 offen offset:0 sc1 lds\ns_cmp_lg_u32 s58, 0\ns_mov_b32 s58, 0\n" \
"s_cbranch_scc1 6f\ns_add_u32 m0, s32, 0\ns_add_u32 s94, s31, 0\nbuffer_load_dwordx4 v24, s[8:11], s94 offen offset:0 sc1 lds\n" \
"buffer_load_dwordx4 v24, s[8:11], s94 offen offset:1024 sc1 lds\nbuffer_load_dwordx4 v24, s[8:11], s94 offen offset:2048 sc1 lds\nbuffer_load_dwordx4 v24, s[8:11], s94 offen offset:3072 sc1 lds\ns_add_u32 m0, s32, 0x2000\n" \
"s_add_u32 s94, s31, 0x2000\nbuffer_load_dwordx4 v24, s[8:11], s94 offen offset:0 sc1 lds\nbuffer_load_dwordx4 v24, s[8:11], s94 offen offset:1024 sc1 lds\nbuffer_load_dwordx4 v24, s[8:11], s94 offen offset:2048 sc1 lds\n" \
"buffer_load_dwordx4 v24, s[8:11], s94 offen offset:3072 sc1 lds\n6:\nv_lshrrev_b32 v40, 2, v24\nv_add_u32 v40, 0x11000, v40\n" \
"s_waitcnt vmcnt(0) lgkmcnt(0)\ns_barrier\nds_read_b32 v41, v40 offset:0\nds_read_b128 v[72:75], v24 offset:0\n" \
"ds_read_b128 v[32:35], v24 offset:32768\ns_waitcnt lgkmcnt(0)\nv_cmp_eq_u32 vcc, -1, v41\ns_cmp_eq_u64 vcc, 0\n" \
"s_cselect_b32 s14, s10, 0\ns_branch 5b\n10:\ns_waitcnt vmcnt(0) lgkmcnt(0)\n" \
"s_cmp_lt_i32 s6, 0\ns_cbranch_scc1 9f\ns_mov_b64 s[0:1], s[4:5]\ns_mov_b32 s2, s6\n" \
"v_lshrrev_b32 v0, 4, v24\nv_add_u32 v0, s7, v0\ns_barrier\ns_getpc_b64 s[8:9]\n" \
"11:\ns_add_u32 s8, s8, _Z5mega64Args-11b\ns_addc_u32 s9, s9, -1\ns_setpc_b64 s[8:9]\n" \
"9:\ns_waitcnt vmcnt(0)\ns_endpgm\n"
#define B_LDS 0x44840c0202a00110ull
#define B_VAR 0x0000000000010001ull
#define B_LV 8192
#define B_LOOP \
"v_mov_b32 v33, v20\nv_cmp_eq_u32 vcc, -1, v33\nv_bfe_u32 v32, v16, 4, 3\nv_lshlrev_b32 v32, 2, v32\n" \
"s_nop 2\ns_cbranch_vccz 6f\n7:\ns_add_u32 s30, s30, 1\n" \
"s_cmp_lt_u32 s30, 0x40000\ns_cbranch_scc0 9f\ns_sleep 8\nbuffer_load_dword v33, v32, s[8:11], s26 offen sc1\n" \
"s_waitcnt vmcnt(0)\nv_cmp_eq_u32 vcc, -1, v33\ns_nop 4\ns_cbranch_vccnz 7b\n" \
"s_add_u32 m0, s22, 0\ns_add_u32 s43, s21, 0\nbuffer_load_dwordx4 v16, s[8:11], s43 offen offset:0 sc1 lds\nbuffer_load_dwordx4 v16, s[8:11], s43 offen offset:1024 sc1 lds\n" \
"buffer_load_dwordx4 v16, s[8:11], s43 offen offset:2048 sc1 lds\nbuffer_load_dwordx4 v16, s[8:11], s43 offen offset:3072 sc1 lds\ns_add_u32 m0, s22, 0x2000\ns_add_u32 s43, s21, 0x2000\n" \
"buffer_load_dwordx4 v16, s[8:11], s43 offen offset:0 sc1 lds\nbuffer_load_dwordx4 v16, s[8:11], s43 offen offset:1024 sc1 lds\nbuffer_load_dwordx4 v16, s[8:11], s43 offen offset:2048 sc1 lds\nbuffer_load_dwordx4 v16, s[8:11], s43 offen offset:3072 sc1 lds\n" \
"s_add_u32 m0, s22, 0x4000\ns_add_u32 s43, s21, 0x4000\nbuffer_load_dwordx4 v16, s[8:11], s43 offen offset:0 sc1 lds\nbuffer_load_dwordx4 v16, s[8:11], s43 offen offset:1024 sc1 lds\n" \
"buffer_load_dwordx4 v16, s[8:11], s43 offen offset:2048 sc1 lds\nbuffer_load_dwordx4 v16, s[8:11], s43 offen offset:3072 sc1 lds\n6:\ns_waitcnt vmcnt(0)\n" \
"s_barrier\nds_read_b128 v[48:51], v16 offset:0\nds_read_b128 v[64:67], v16 offset:32768\nds_read_b128 v[76:79], v16 offset:1024\n" \
"ds_read_b128 v[80:83], v16 offset:33792\nds_read_b128 v[84:87], v16 offset:2048\nds_read_b128 v[88:91], v16 offset:34816\nds_read_b128 v[92:95], v16 offset:3072\n" \
"ds_read_b128 v[96:99], v16 offset:35840\nds_read_b128 v[28:31], v16 offset:4096\nds_read_b128 v[40:43], v16 offset:36864\nds_read_b128 v[36:39], v16 offset:5120\n" \
"ds_read_b128 v[32:35], v16 offset:37888\nds_read_b128 v[52:55], v16 offset:6144\nds_read_b128 v[24:27], v16 offset:38912\nds_read_b128 v[56:59], v16 offset:7168\n" \
"ds_read_b128 v[60:63], v16 offset:39936\ns_mov_b32 s20, -1\ns_add_u32 s21, s21, 0x10000\ns_mov_b32 s14, s10\n" \
"s_waitcnt lgkmcnt(0)\nv_mfma_f32_16x16x32_bf16 v[44:47], v[0:3], v[48:51], 0\nv_mfma_f32_16x16x32_bf16 v[68:71], v[0:3], v[64:67], 0\nv_mfma_f32_16x16x32_bf16 v[72:75], v[8:11], v[48:51], 0\n" \
"v_mfma_f32_16x16x32_bf16 v[20:23], v[8:11], v[64:67], 0\nv_mfma_f32_16x16x32_bf16 v[44:47], v[4:7], v[76:79], v[44:47]\nv_mfma_f32_16x16x32_bf16 v[68:71], v[4:7], v[80:83], v[68:71]\nv_mfma_f32_16x16x32_bf16 v[72:75], v[12:15], v[76:79], v[72:75]\n" \
"v_mfma_f32_16x16x32_bf16 v[20:23], v[12:15], v[80:83], v[20:23]\nv_mfma_f32_16x16x32_bf16 v[44:47], a[0:3], v[84:87], v[44:47]\nv_mfma_f32_16x16x32_bf16 v[68:71], a[0:3], v[88:91], v[68:71]\nv_mfma_f32_16x16x32_bf16 v[72:75], a[104:107], v[84:87], v[72:75]\n" \
"v_mfma_f32_16x16x32_bf16 v[20:23], a[104:107], v[88:91], v[20:23]\nv_mfma_f32_16x16x32_bf16 v[44:47], a[4:7], v[92:95], v[44:47]\nv_mfma_f32_16x16x32_bf16 v[68:71], a[4:7], v[96:99], v[68:71]\nv_mfma_f32_16x16x32_bf16 v[72:75], a[108:111], v[92:95], v[72:75]\n" \
"v_mfma_f32_16x16x32_bf16 v[20:23], a[108:111], v[96:99], v[20:23]\ns_branch 1f\n.p2align 6\n1:\n" \
"s_waitcnt lgkmcnt(4)\nv_mfma_f32_16x16x32_bf16 v[44:47], a[8:11], v[28:31], v[44:47]\ns_add_u32 m0, s22, 0\ns_add_u32 s43, s20, 7\n" \
"s_and_b32 s43, s43, 7\nv_mfma_f32_16x16x32_bf16 v[68:71], a[8:11], v[40:43], v[68:71]\ns_lshl_b32 s43, s43, 18\ns_add_u32 s43, s43, s23\n" \
"v_mfma_f32_16x16x32_bf16 v[72:75], a[112:115], v[28:31], v[72:75]\nds_read_b128 v[48:51], v17 offset:0\nv_mfma_f32_16x16x32_bf16 v[20:23], a[112:115], v[40:43], v[20:23]\nv_mfma_f32_16x16x32_bf16 v[44:47], a[12:15], v[36:39], v[44:47]\n" \
"s_waitcnt vmcnt(7) lgkmcnt(1)\ns_barrier\nv_mfma_f32_16x16x32_bf16 v[68:71], a[12:15], v[32:35], v[68:71]\nds_read_b128 v[28:31], v16 offset:8192\n" \
"v_mfma_f32_16x16x32_bf16 v[72:75], a[116:119], v[36:39], v[72:75]\nds_read_b128 v[40:43], v16 offset:40960\nv_mfma_f32_16x16x32_bf16 v[20:23], a[116:119], v[32:35], v[20:23]\nds_read_b128 v[36:39], v16 offset:9216\n" \
"v_mfma_f32_16x16x32_bf16 v[44:47], a[16:19], v[52:55], v[44:47]\nds_read_b128 v[32:35], v17 offset:1024\nv_mfma_f32_16x16x32_bf16 v[68:71], a[16:19], v[24:27], v[68:71]\nbuffer_load_dwordx4 v16, s[8:11], s43 offen offset:0 sc1 lds\n" \
"v_mfma_f32_16x16x32_bf16 v[72:75], a[120:123], v[52:55], v[72:75]\nds_read_b128 v[64:67], v16 offset:41984\nv_mfma_f32_16x16x32_bf16 v[20:23], a[120:123], v[24:27], v[20:23]\nds_read_b128 v[52:55], v16 offset:10240\n" \
"v_mfma_f32_16x16x32_bf16 v[44:47], a[20:23], v[56:59], v[44:47]\nbuffer_load_dwordx4 v16, s[8:11], s43 offen offset:1024 sc1 lds\nv_mfma_f32_16x16x32_bf16 v[68:71], a[20:23], v[60:63], v[68:71]\nds_read_b128 v[24:27], v16 offset:43008\n" \
"v_mfma_f32_16x16x32_bf16 v[72:75], a[124:127], v[56:59], v[72:75]\ns_waitcnt lgkmcnt(3)\nds_read_b128 v[56:59], v17 offset:4096\nv_mfma_f32_16x16x32_bf16 v[20:23], a[124:127], v[60:63], v[20:23]\n" \
"buffer_load_dwordx4 v16, s[8:11], s43 offen offset:2048 sc1 lds\nv_mfma_f32_16x16x32_bf16 v[44:47], v[48:51], v[28:31], v[44:47]\nds_read_b128 v[60:63], v16 offset:11264\nv_mfma_f32_16x16x32_bf16 v[68:71], v[48:51], v[40:43], v[68:71]\n" \
"ds_read_b128 v[76:79], v16 offset:44032\nv_mfma_f32_16x16x32_bf16 v[72:75], a[128:131], v[28:31], v[72:75]\nbuffer_load_dwordx4 v16, s[8:11], s43 offen offset:3072 sc1 lds\nv_mfma_f32_16x16x32_bf16 v[20:23], a[128:131], v[40:43], v[20:23]\n" \
"s_waitcnt lgkmcnt(3)\nds_read_b128 v[28:31], v17 offset:5120\nv_mfma_f32_16x16x32_bf16 v[44:47], v[32:35], v[36:39], v[44:47]\nds_read_b128 v[40:43], v16 offset:12288\n" \
"v_mfma_f32_16x16x32_bf16 v[68:71], v[32:35], v[64:67], v[68:71]\ns_add_u32 m0, s22, 0x6000\nds_read_b128 v[32:35], v16 offset:45056\nv_mfma_f32_16x16x32_bf16 v[72:75], a[132:135], v[36:39], v[72:75]\n" \
"s_add_u32 s42, s21, -0xa000\nv_mfma_f32_16x16x32_bf16 v[20:23], a[132:135], v[64:67], v[20:23]\nds_read_b128 v[36:39], v16 offset:13312\nv_mfma_f32_16x16x32_bf16 v[44:47], a[24:27], v[52:55], v[44:47]\n" \
"s_waitcnt lgkmcnt(4)\nds_read_b128 v[48:51], v16 offset:46080\nv_mfma_f32_16x16x32_bf16 v[68:71], a[24:27], v[24:27], v[68:71]\nbuffer_load_dwordx4 v16, s[12:15], s42 offen offset:0 sc1 lds\n" \
"v_mfma_f32_16x16x32_bf16 v[72:75], v[56:59], v[52:55], v[72:75]\nv_mfma_f32_16x16x32_bf16 v[20:23], v[56:59], v[24:27], v[20:23]\nds_read_b128 v[52:55], v16 offset:14336\nv_mfma_f32_16x16x32_bf16 v[44:47], a[28:31], v[60:63], v[44:47]\n" \
"s_waitcnt lgkmcnt(3)\nbuffer_load_dwordx4 v16, s[12:15], s42 offen offset:1024 sc1 lds\nv_mfma_f32_16x16x32_bf16 v[68:71], a[28:31], v[76:79], v[68:71]\nds_read_b128 v[24:27], v16 offset:47104\n" \
"v_mfma_f32_16x16x32_bf16 v[72:75], v[28:31], v[60:63], v[72:75]\nds_read_b128 v[56:59], v17 offset:6144\nv_mfma_f32_16x16x32_bf16 v[20:23], v[28:31], v[76:79], v[20:23]\nbuffer_load_dwordx4 v16, s[12:15], s42 offen offset:2048 sc1 lds\n" \
"v_mfma_f32_16x16x32_bf16 v[44:47], a[32:35], v[40:43], v[44:47]\nds_read_b128 v[28:31], v16 offset:15360\nv_lshlrev_b32 v60, 5, v16\nv_mfma_f32_16x16x32_bf16 v[68:71], a[32:35], v[32:35], v[68:71]\n" \
"ds_read_b128 v[62:65], v16 offset:48128\nv_and_b32 v61, 0x7000, v60\nv_mfma_f32_16x16x32_bf16 v[72:75], a[136:139], v[40:43], v[72:75]\ns_waitcnt lgkmcnt(4)\n" \
"buffer_load_dwordx4 v16, s[12:15], s42 offen offset:3072 sc1 lds\nv_mfma_f32_16x16x32_bf16 v[20:23], a[136:139], v[32:35], v[20:23]\nds_read_b128 v[40:43], v17 offset:7168\ns_add_u32 m0, s22, 0\n" \
"v_mfma_f32_16x16x32_bf16 v[44:47], a[36:39], v[36:39], v[44:47]\nds_read_b128 v[32:35], v16 offset:16384\nv_lshl_add_u32 v60, v61, 17, v60\nv_mfma_f32_16x16x32_bf16 v[68:71], a[36:39], v[48:51], v[68:71]\n" \
"ds_read_b128 v[76:79], v17 offset:2048\nv_mfma_f32_16x16x32_bf16 v[72:75], a[140:143], v[36:39], v[72:75]\nds_read_b128 v[80:83], v16 offset:49152\nv_mfma_f32_16x16x32_bf16 v[20:23], a[140:143], v[48:51], v[20:23]\n" \
"s_waitcnt lgkmcnt(4)\nds_read_b128 v[36:39], v16 offset:17408\nv_mfma_f32_16x16x32_bf16 v[44:47], a[40:43], v[52:55], v[44:47]\nds_read_b128 v[48:51], v17 offset:3072\n" \
"v_mfma_f32_16x16x32_bf16 v[68:71], a[40:43], v[24:27], v[68:71]\nds_read_b128 v[84:87], v16 offset:50176\nv_mfma_f32_16x16x32_bf16 v[72:75], v[56:59], v[52:55], v[72:75]\nv_mfma_f32_16x16x32_bf16 v[20:23], v[56:59], v[24:27], v[20:23]\n" \
"ds_read_b128 v[52:55], v16 offset:18432\nv_mfma_f32_16x16x32_bf16 v[44:47], a[44:47], v[28:31], v[44:47]\ns_waitcnt lgkmcnt(4)\nds_read_b128 v[24:27], v16 offset:51200\n" \
"v_mfma_f32_16x16x32_bf16 v[68:71], a[44:47], v[62:65], v[68:71]\nds_read_b128 v[56:59], v17 offset:8192\nv_add_u32 v61, s22, v16\nv_mfma_f32_16x16x32_bf16 v[72:75], v[40:43], v[28:31], v[72:75]\n" \
"v_mfma_f32_16x16x32_bf16 v[20:23], v[40:43], v[62:65], v[20:23]\nds_read_b128 v[28:31], v16 offset:19456\nv_mfma_f32_16x16x32_bf16 v[44:47], v[76:79], v[32:35], v[44:47]\nds_read_b128 v[40:43], v16 offset:52224\n" \
"v_mfma_f32_16x16x32_bf16 v[68:71], v[76:79], v[80:83], v[68:71]\nds_read_b128 v[62:65], v17 offset:9216\nv_mfma_f32_16x16x32_bf16 v[72:75], a[144:147], v[32:35], v[72:75]\ns_waitcnt lgkmcnt(5)\n" \
"ds_read_b128 v[92:95], v17 offset:9216\nv_mfma_f32_16x16x32_bf16 v[20:23], a[144:147], v[80:83], v[20:23]\nds_read_b128 v[32:35], v16 offset:20480\nv_mfma_f32_16x16x32_bf16 v[44:47], v[48:51], v[36:39], v[44:47]\n" \
"ds_read_b128 v[76:79], v16 offset:53248\nv_mfma_f32_16x16x32_bf16 v[68:71], v[48:51], v[84:87], v[68:71]\nds_read_b128 v[88:91], v17 offset:8192\nv_mfma_f32_16x16x32_bf16 v[72:75], a[148:151], v[36:39], v[72:75]\n" \
"s_waitcnt vmcnt(4)\nds_read_b128 v[36:39], v61 offset:0\nds_read_b128 v[80:83], v61 offset:1024\nds_read_b128 v[48:51], v61 offset:2048\n" \
"v_mfma_f32_16x16x32_bf16 v[20:23], a[148:151], v[84:87], v[20:23]\ns_waitcnt lgkmcnt(7)\nds_read_b128 v[84:87], v61 offset:3072\nv_mfma_f32_16x16x32_bf16 v[44:47], a[48:51], v[52:55], v[44:47]\n" \
"ds_read_b128 v[96:99], v17 offset:4096\nv_mfma_f32_16x16x32_bf16 v[68:71], a[48:51], v[24:27], v[68:71]\nds_read_b128 v[100:103], v17 offset:5120\nv_mov_b64 v[104:105], 0\n" \
"v_mfma_f32_16x16x32_bf16 v[72:75], v[56:59], v[52:55], v[72:75]\ns_waitcnt lgkmcnt(3)\nv_max3_u32 v52, v36, v80, v48\nv_mov_b64 v[106:107], 0\n" \
"v_mfma_f32_16x16x32_bf16 v[20:23], v[56:59], v[24:27], v[20:23]\ns_waitcnt lgkmcnt(2)\nv_max_u32 v52, v52, v84\nv_cmp_eq_u32 vcc, -1, v52\n" \
"v_mfma_f32_16x16x32_bf16 v[44:47], a[52:55], v[28:31], v[44:47]\nv_mov_b64 v[24:25], 0\nv_mov_b64 v[26:27], 0\nv_mfma_f32_16x16x32_bf16 v[68:71], a[52:55], v[40:43], v[68:71]\n" \
"s_cbranch_vccnz 2f\n3:\nv_mfma_f32_16x16x32_bf16 v[72:75], v[62:65], v[28:31], v[72:75]\ns_barrier\n" \
"ds_read_b128 v[28:31], v16 offset:2048\nv_mfma_f32_16x16x32_bf16 v[20:23], v[92:95], v[40:43], v[20:23]\ns_memrealtime s[40:41]\nds_read_b128 v[36:39], v16 offset:5120\n" \
"v_mfma_f32_16x16x32_bf16 v[44:47], a[56:59], v[32:35], v[44:47]\nds_read_b128 v[40:43], v16 offset:1024\ns_lshl_b32 s43, s20, 24\nv_mfma_f32_16x16x32_bf16 v[68:71], a[56:59], v[76:79], v[68:71]\n" \
"ds_read_b128 v[48:51], v16 offset:33792\nv_mfma_f32_16x16x32_bf16 v[72:75], a[152:155], v[32:35], v[72:75]\nv_mfma_f32_16x16x32_bf16 v[20:23], a[152:155], v[76:79], v[20:23]\ns_waitcnt lgkmcnt(3)\n" \
"ds_read_b128 v[32:35], v16 offset:3072\nv_smfmac_f32_16x16x64_bf16 v[104:107], v[28:31], a[8:15], v18\ns_waitcnt lgkmcnt(3)\nv_smfmac_f32_16x16x64_bf16 v[24:27], v[28:31], a[112:119], v18\n" \
"ds_read_b128 v[52:55], v16 offset:34816\nv_smfmac_f32_16x16x64_bf16 v[104:107], v[36:39], a[24:31], v18\ns_waitcnt lgkmcnt(3)\nds_read_b128 v[76:79], v17 offset:6144\n" \
"v_smfmac_f32_16x16x64_bf16 v[24:27], v[36:39], v[96:103], v18\nds_read_b128 v[28:31], v16 offset:36864\nv_smfmac_f32_16x16x64_bf16 v[104:107], v[40:43], a[0:7], v18\ns_waitcnt lgkmcnt(4)\n" \
"ds_read_b128 v[80:83], v17 offset:7168\nv_smfmac_f32_16x16x64_bf16 v[24:27], v[40:43], a[104:111], v18\nds_read_b128 v[36:39], v16 offset:6144\ns_add_u32 s42, s40, 0x67\n" \
"v_smfmac_f32_16x16x64_bf16 v[104:107], v[48:51], a[48:55], v18\ns_and_b32 s42, s42, 0xffffff\ns_or_b32 s42, s42, s43\nv_mov_b32 v40, s42\n" \
"v_smfmac_f32_16x16x64_bf16 v[24:27], v[48:51], v[88:95], v18\ns_waitcnt lgkmcnt(4)\nds_read_b128 v[48:51], v16 offset:7168\nv_smfmac_f32_16x16x64_bf16 v[104:107], v[32:35], a[16:23], v18\n" \
"buffer_store_dword v40, v60, s[8:11], s27 offen offset:0 sc1\nv_smfmac_f32_16x16x64_bf16 v[24:27], v[32:35], a[120:127], v18\nds_read_b128 v[56:59], v16 offset:35840\nv_smfmac_f32_16x16x64_bf16 v[104:107], v[52:55], a[56:63], v18\n" \
"s_waitcnt lgkmcnt(4)\nv_smfmac_f32_16x16x64_bf16 v[24:27], v[52:55], a[152:159], v18\nds_read_b128 v[32:35], v16 offset:0\ns_add_u32 s16, s20, 1\n" \
"v_smfmac_f32_16x16x64_bf16 v[104:107], v[28:31], a[72:79], v18\nds_read_b128 v[60:63], v17 offset:0\nv_mov_b64 v[40:41], 0\nv_smfmac_f32_16x16x64_bf16 v[24:27], v[28:31], a[168:175], v18\n" \
"s_waitcnt lgkmcnt(4)\nds_read_b128 v[28:31], v16 offset:39936\nv_smfmac_f32_16x16x64_bf16 v[104:107], v[36:39], a[32:39], v18\nds_read_b128 v[64:67], v17 offset:1024\n" \
"v_mov_b64 v[42:43], 0\nv_smfmac_f32_16x16x64_bf16 v[24:27], v[36:39], a[136:143], v18\ns_waitcnt lgkmcnt(4)\nds_read_b128 v[36:39], v16 offset:37888\n" \
"v_smfmac_f32_16x16x64_bf16 v[104:107], v[48:51], a[40:47], v18\nds_read_b128 v[84:87], v17 offset:2048\ns_and_b32 s16, s16, 7\nv_smfmac_f32_16x16x64_bf16 v[24:27], v[48:51], v[76:83], v18\n" \
"ds_read_b128 v[52:55], v16 offset:4096\ns_lshl_b32 s16, s16, 22\nv_smfmac_f32_16x16x64_bf16 v[104:107], v[56:59], a[64:71], v18\nds_read_b128 v[88:91], v17 offset:3072\n" \
"s_add_u32 s16, s16, s25\nv_smfmac_f32_16x16x64_bf16 v[24:27], v[56:59], a[160:167], v18\ns_waitcnt lgkmcnt(6)\nds_read_b128 v[48:51], v16 offset:38912\n" \
"v_smfmac_f32_16x16x64_bf16 v[104:107], v[32:35], v[0:7], v18\nds_read_b128 v[56:59], v16 offset:21504\ns_add_u32 s17, s16, 0x1000\nv_smfmac_f32_16x16x64_bf16 v[24:27], v[32:35], v[8:15], v18\n" \
"s_waitcnt lgkmcnt(6)\nds_read_b128 v[32:35], v16 offset:32768\nv_smfmac_f32_16x16x64_bf16 v[104:107], v[28:31], a[96:103], v18\nds_read_b128 v[76:79], v16 offset:54272\n" \
"s_add_u32 s18, s16, 0x4000\nv_smfmac_f32_16x16x64_bf16 v[24:27], v[28:31], a[192:199], v18\ns_waitcnt lgkmcnt(5)\nds_read_b128 v[28:31], v16 offset:22528\n" \
"v_smfmac_f32_16x16x64_bf16 v[104:107], v[36:39], a[80:87], v18\nds_read_b128 v[80:83], v16 offset:55296\nv_lshrrev_b32 v92, 16, v18\nv_smfmac_f32_16x16x64_bf16 v[24:27], v[36:39], a[176:183], v18\n" \
"ds_write_b128 v16, v[40:43] offset:45056\nv_smfmac_f32_16x16x64_bf16 v[104:107], v[52:55], v[60:67], v18\ns_add_u32 s19, s16, 0x5000\ns_and_b32 s43, s20, 2\n" \
"s_lshl_b32 s43, s43, 12\nv_smfmac_f32_16x16x64_bf16 v[24:27], v[52:55], a[128:135], v18\ns_waitcnt lgkmcnt(4)\nds_read_b128 v[36:39], v16 offset:23552\n" \
"v_smfmac_f32_16x16x64_bf16 v[104:107], v[48:51], a[88:95], v18\nds_read_b128 v[52:55], v16 offset:56320\ns_and_b32 s42, s20, 7\nv_smfmac_f32_16x16x64_bf16 v[24:27], v[48:51], a[184:191], v18\n" \
"ds_read_b128 v[60:63], v17 offset:10240\ns_lshl_b32 s42, s42, 20\nv_smfmac_f32_16x16x64_bf16 v[104:107], v[32:35], v[84:91], v18\nds_read_b128 v[64:67], v17 offset:11264\n" \
"s_add_u32 s42, s42, s24\nv_smfmac_f32_16x16x64_bf16 v[24:27], v[32:35], a[144:151], v18\ns_waitcnt lgkmcnt(4)\nv_mfma_f32_16x16x32_bf16 v[44:47], a[60:63], v[56:59], v[44:47]\n" \
"v_add_f32 v104, v104, v106\nv_add_f32 v105, v105, v107\nv_cvt_pk_bf16_f32 v104, v104, v105\nv_mfma_f32_16x16x32_bf16 v[68:71], a[60:63], v[76:79], v[68:71]\n" \
"v_add_f32 v24, v24, v26\nv_add_f32 v25, v25, v27\nv_cvt_pk_bf16_f32 v24, v24, v25\nv_mfma_f32_16x16x32_bf16 v[72:75], a[156:159], v[56:59], v[72:75]\n" \
"ds_write2_b32 v92, v104, v24 offset0:0 offset1:4\nv_mfma_f32_16x16x32_bf16 v[20:23], a[156:159], v[76:79], v[20:23]\ns_add_u32 s99, s20, 2\nv_mov_b64 v[24:25], 0\n" \
"v_mov_b64 v[26:27], 0\nv_mfma_f32_16x16x32_bf16 v[44:47], a[64:67], v[28:31], v[44:47]\ns_min_u32 s99, s99, 63\nv_bfe_u32 v32, v16, 4, 3\n" \
"v_lshlrev_b32 v32, 2, v32\nv_mfma_f32_16x16x32_bf16 v[68:71], a[64:67], v[80:83], v[68:71]\ns_waitcnt vmcnt(1) lgkmcnt(0)\ns_barrier\n" \
"ds_read_b64_tr_b16 v[48:49], v19 offset:0\nv_mfma_f32_16x16x32_bf16 v[72:75], a[160:163], v[28:31], v[72:75]\nds_read_b64_tr_b16 v[50:51], v19 offset:256\nds_read_b64_tr_b16 v[28:29], v19 offset:1024\n" \
"ds_read_b64_tr_b16 v[30:31], v19 offset:1280\nv_mfma_f32_16x16x32_bf16 v[20:23], a[160:163], v[80:83], v[20:23]\nds_read_b128 v[56:59], v16 offset:24576\nv_mov_b64 v[76:77], 0\n" \
"v_mfma_f32_16x16x32_bf16 v[44:47], a[68:71], v[36:39], v[44:47]\ns_lshl_b32 s99, s99, 5\ns_add_u32 s99, s99, s26\nv_mov_b64 v[78:79], 0\n" \
"v_mfma_f32_16x16x32_bf16 v[68:71], a[68:71], v[52:55], v[68:71]\ns_waitcnt lgkmcnt(3)\nbuffer_load_dword v33, v32, s[8:11], s99 offen sc1\nv_mfma_f32_16x16x32_bf16 v[72:75], a[164:167], v[36:39], v[72:75]\n" \
"ds_read_b128 v[80:83], v16 offset:25600\nv_mov_b64 v[34:35], 0\nv_smfmac_f32_16x16x64_bf16 v[40:43], v[48:51], a[200:207], v18\ns_waitcnt lgkmcnt(2)\n" \
"ds_read_b128 v[88:91], v17 offset:12288\nv_smfmac_f32_16x16x64_bf16 v[40:43], v[28:31], v[60:67], v18\nds_read_b128 v[84:87], v16 offset:45056\nv_mov_b64 v[36:37], 0\n" \
"v_smfmac_f32_16x16x64_bf16 v[24:27], v[48:51], a[224:231], v18\nds_read_b128 v[92:95], v17 offset:13312\nv_mfma_f32_16x16x32_bf16 v[20:23], a[164:167], v[52:55], v[20:23]\nds_read_b128 v[60:63], v17 offset:14336\n" \
"v_add_f32 v40, v40, v42\nv_smfmac_f32_16x16x64_bf16 v[24:27], v[28:31], a[232:239], v18\ns_waitcnt lgkmcnt(5)\nds_read_b128 v[52:55], v16 offset:45056\n" \
"v_mfma_f32_16x16x32_bf16 v[44:47], a[72:75], v[56:59], v[44:47]\nds_read_b128 v[64:67], v17 offset:15360\nv_add_f32 v41, v41, v43\nv_smfmac_f32_16x16x64_bf16 v[76:79], v[48:51], a[208:215], v18\n" \
"ds_read_b128 v[96:99], v16 offset:26624\nv_add_f32 v24, v24, v26\nv_smfmac_f32_16x16x64_bf16 v[76:79], v[28:31], a[216:223], v18\nds_read_b128 v[100:103], v16 offset:45056\n" \
"v_add_f32 v25, v25, v27\nv_smfmac_f32_16x16x64_bf16 v[34:37], v[48:51], a[240:247], v18\nds_read_b128 v[104:107], v17 offset:16384\nv_cvt_pk_bf16_f32 v38, v40, v41\n" \
"v_mfma_f32_16x16x32_bf16 v[72:75], a[168:171], v[56:59], v[72:75]\ns_waitcnt lgkmcnt(5)\nv_add_f32 v76, v76, v78\nv_add_f32 v77, v77, v79\n" \
"v_smfmac_f32_16x16x64_bf16 v[34:37], v[28:31], a[248:255], v18\nds_read_b128 v[56:59], v16 offset:45056\nv_cvt_pk_bf16_f32 v39, v24, v25\nv_smfmac_f32_16x16x64_bf16 v[84:87], v[48:51], v[112:119], v18\n" \
"ds_read_b128 v[108:111], v17 offset:17408\nv_cvt_pk_bf16_f32 v40, v76, v77\nv_mfma_f32_16x16x32_bf16 v[44:47], a[76:79], v[80:83], v[44:47]\ns_waitcnt lgkmcnt(4)\n" \
"v_permlane32_swap_b32 v38, v39\nv_smfmac_f32_16x16x64_bf16 v[84:87], v[28:31], v[120:127], v18\nds_read_b128 v[24:27], v16 offset:45056\nv_add_f32 v34, v34, v36\n" \
"v_smfmac_f32_16x16x64_bf16 v[52:55], v[48:51], v[128:135], v18\nds_read_b128 v[76:79], v16 offset:27648\nv_add_f32 v35, v35, v37\nv_cvt_pk_bf16_f32 v41, v34, v35\n" \
"v_smfmac_f32_16x16x64_bf16 v[52:55], v[28:31], v[136:143], v18\nds_read_b128 v[34:37], v17 offset:18432\nv_permlane32_swap_b32 v40, v41\nv_mfma_f32_16x16x32_bf16 v[72:75], a[172:175], v[80:83], v[72:75]\n" \
"s_waitcnt lgkmcnt(4)\nv_permlane16_swap_b32 v38, v40\nv_smfmac_f32_16x16x64_bf16 v[100:103], v[48:51], v[88:95], v18\nds_read_b128 v[80:83], v16 offset:45056\n" \
"v_permlane16_swap_b32 v39, v41\nv_and_or_b32 v38, v38, -2, s32\nv_smfmac_f32_16x16x64_bf16 v[100:103], v[28:31], v[60:67], v18\nds_read_b128 v[88:91], v16 offset:28672\n" \
"v_add_f32 v84, v84, v86\nv_mfma_f32_16x16x32_bf16 v[44:47], a[80:83], v[96:99], v[44:47]\nbuffer_store_dwordx4 v[38:41], v16, s[8:11], s29 offen offset:0 sc1\nv_add_f32 v85, v85, v87\n" \
"v_add_f32 v52, v52, v54\nv_add_f32 v53, v53, v55\nv_smfmac_f32_16x16x64_bf16 v[56:59], v[48:51], v[144:151], v18\nds_read_b128 v[60:63], v16 offset:45056\n" \
"v_add_f32 v100, v100, v102\nv_smfmac_f32_16x16x64_bf16 v[56:59], v[28:31], v[152:159], v18\nds_read_b128 v[38:41], v17 offset:19456\nv_add_f32 v101, v101, v103\n" \
"v_mfma_f32_16x16x32_bf16 v[72:75], a[176:179], v[96:99], v[72:75]\ns_waitcnt lgkmcnt(3)\nv_cvt_pk_bf16_f32 v64, v84, v85\nv_cvt_pk_bf16_f32 v65, v52, v53\n" \
"v_smfmac_f32_16x16x64_bf16 v[24:27], v[48:51], v[160:167], v18\nds_read_b128 v[52:55], v16 offset:45056\nv_add_f32 v56, v56, v58\nv_smfmac_f32_16x16x64_bf16 v[24:27], v[28:31], v[168:175], v18\n" \
"ds_read_b128 v[84:87], v16 offset:45056\nv_add_f32 v57, v57, v59\nv_mfma_f32_16x16x32_bf16 v[44:47], a[84:87], v[76:79], v[44:47]\nv_permlane32_swap_b32 v64, v65\n" \
"v_cvt_pk_bf16_f32 v66, v100, v101\nv_cvt_pk_bf16_f32 v67, v56, v57\nv_smfmac_f32_16x16x64_bf16 v[80:83], v[48:51], v[184:191], v18\nds_read_b128 v[56:59], v16 offset:29696\n" \
"v_permlane32_swap_b32 v66, v67\nv_smfmac_f32_16x16x64_bf16 v[80:83], v[28:31], v[192:199], v18\ns_waitcnt lgkmcnt(3)\nv_permlane16_swap_b32 v64, v66\n" \
"v_mfma_f32_16x16x32_bf16 v[72:75], a[180:183], v[76:79], v[72:75]\nv_permlane16_swap_b32 v65, v67\nv_and_or_b32 v64, v64, -2, s32\nv_smfmac_f32_16x16x64_bf16 v[60:63], v[48:51], v[104:111], v18\n" \
"ds_read_b128 v[76:79], v16 offset:45056\nv_add_f32 v24, v24, v26\nv_smfmac_f32_16x16x64_bf16 v[60:63], v[28:31], v[176:183], v18\ns_waitcnt lgkmcnt(2)\n" \
"buffer_store_dwordx4 v[64:67], v16, s[8:11], s29 offen offset:1024 sc1\nds_read_b128 v[92:95], v17 offset:20480\nv_add_f32 v25, v25, v27\nv_mfma_f32_16x16x32_bf16 v[44:47], a[88:91], v[88:91], v[44:47]\n" \
"ds_read_b128 v[96:99], v17 offset:21504\nv_add_f32 v80, v80, v82\nv_smfmac_f32_16x16x64_bf16 v[52:55], v[48:51], v[200:207], v18\nds_read_b128 v[64:67], v16 offset:45056\n" \
"v_add_f32 v81, v81, v83\nv_smfmac_f32_16x16x64_bf16 v[52:55], v[28:31], v[34:41], v18\nds_read_b128 v[100:103], v16 offset:30720\nv_add_f32 v60, v60, v62\n" \
"v_smfmac_f32_16x16x64_bf16 v[84:87], v[48:51], v[208:215], v18\nds_read_b128 v[34:37], v16 offset:45056\nv_add_f32 v61, v61, v63\nv_mfma_f32_16x16x32_bf16 v[72:75], a[184:187], v[88:91], v[72:75]\n" \
"s_waitcnt lgkmcnt(5)\nv_add_f32 v52, v52, v54\nv_add_f32 v53, v53, v55\nv_smfmac_f32_16x16x64_bf16 v[84:87], v[28:31], v[216:223], v18\n" \
"ds_read_b128 v[104:107], v17 offset:22528\nv_cvt_pk_bf16_f32 v38, v24, v25\nv_mfma_f32_16x16x32_bf16 v[44:47], a[92:95], v[56:59], v[44:47]\nv_cvt_pk_bf16_f32 v39, v80, v81\n" \
"v_cvt_pk_bf16_f32 v40, v60, v61\nv_cvt_pk_bf16_f32 v41, v52, v53\nv_smfmac_f32_16x16x64_bf16 v[76:79], v[48:51], v[232:239], v18\nds_read_b128 v[108:111], v17 offset:23552\n" \
"v_permlane32_swap_b32 v38, v39\nv_permlane32_swap_b32 v40, v41\nv_smfmac_f32_16x16x64_bf16 v[76:79], v[28:31], v[240:247], v18\nds_read_b128 v[24:27], v16 offset:31744\n" \
"v_permlane16_swap_b32 v38, v40\nv_mfma_f32_16x16x32_bf16 v[72:75], a[188:191], v[56:59], v[72:75]\ns_waitcnt lgkmcnt(3)\nv_permlane16_swap_b32 v39, v41\n" \
"v_and_or_b32 v38, v38, -2, s32\nv_smfmac_f32_16x16x64_bf16 v[64:67], v[48:51], v[92:99], v18\nds_read_b128 v[52:55], v16 offset:57344\nv_add_f32 v84, v84, v86\n" \
"v_smfmac_f32_16x16x64_bf16 v[64:67], v[28:31], v[224:231], v18\nds_read_b128 v[56:59], v16 offset:58368\nv_add_f32 v85, v85, v87\nv_mfma_f32_16x16x32_bf16 v[44:47], a[96:99], v[100:103], v[44:47]\n" \
"s_waitcnt lgkmcnt(3)\nbuffer_store_dwordx4 v[38:41], v16, s[8:11], s29 offen offset:2048 sc1\nv_add_f32 v76, v76, v78\nv_add_f32 v77, v77, v79\n" \
"v_cvt_pk_bf16_f32 v38, v84, v85\nv_smfmac_f32_16x16x64_bf16 v[34:37], v[48:51], v[104:111], v18\nds_read_b128 v[60:63], v16 offset:59392\nv_add_f32 v64, v64, v66\n" \
"v_add_f32 v65, v65, v67\nv_cvt_pk_bf16_f32 v39, v76, v77\nv_cvt_pk_bf16_f32 v40, v64, v65\nv_smfmac_f32_16x16x64_bf16 v[34:37], v[28:31], v[248:255], v18\n" \
"ds_read_b128 v[48:51], v16 offset:60416\nv_permlane32_swap_b32 v38, v39\nv_mfma_f32_16x16x32_bf16 v[72:75], a[192:195], v[100:103], v[72:75]\ns_waitcnt lgkmcnt(3)\n" \
"ds_read_b128 v[28:31], v16 offset:61440\nv_mfma_f32_16x16x32_bf16 v[44:47], a[100:103], v[24:27], v[44:47]\nv_add_f32 v34, v34, v36\nv_add_f32 v35, v35, v37\n" \
"v_cvt_pk_bf16_f32 v41, v34, v35\nv_mfma_f32_16x16x32_bf16 v[72:75], a[196:199], v[24:27], v[72:75]\nds_read_b128 v[34:37], v16 offset:62464\nv_permlane32_swap_b32 v40, v41\n" \
"v_mfma_f32_16x16x32_bf16 v[68:71], a[72:75], v[52:55], v[68:71]\ns_waitcnt lgkmcnt(3)\nv_permlane16_swap_b32 v38, v40\nv_permlane16_swap_b32 v39, v41\n" \
"v_and_or_b32 v38, v38, -2, s32\nv_mfma_f32_16x16x32_bf16 v[20:23], a[168:171], v[52:55], v[20:23]\nbuffer_store_dwordx4 v[38:41], v16, s[8:11], s29 offen offset:3072 sc1\ns_bfe_u32 s32, s20, 0x10003\n" \
"v_cvt_pk_bf16_f32 v44, v44, v45\nv_cvt_pk_bf16_f32 v45, v46, v47\nv_mfma_f32_16x16x32_bf16 v[68:71], a[76:79], v[56:59], v[68:71]\ns_xor_b32 s29, s42, s43\n" \
"v_cvt_pk_bf16_f32 v46, v72, v73\nv_cvt_pk_bf16_f32 v47, v74, v75\nv_mfma_f32_16x16x32_bf16 v[20:23], a[172:175], v[56:59], v[20:23]\nds_read_b128 v[24:27], v16 offset:63488\n" \
"v_mfma_f32_16x16x32_bf16 v[68:71], a[80:83], v[60:63], v[68:71]\ns_waitcnt vmcnt(4) lgkmcnt(1)\nv_cmp_eq_u32 s[42:43], -1, v33\nv_mfma_f32_16x16x32_bf16 v[20:23], a[176:179], v[60:63], v[20:23]\n" \
"ds_read_b128 v[38:41], v16 offset:64512\nv_mfma_f32_16x16x32_bf16 v[68:71], a[84:87], v[48:51], v[68:71]\ns_cmp_eq_u64 s[42:43], 0\ns_cselect_b32 s14, s10, 0\n" \
"s_cmp_lt_i32 s20, 62\ns_cselect_b32 s14, s14, 0\ns_cmp_eq_u32 s14, 0\ns_cbranch_scc1 4f\n" \
"5:\nv_add_u32 v32, s28, v16\nv_mfma_f32_16x16x32_bf16 v[20:23], a[180:183], v[48:51], v[20:23]\nds_write_b128 v32, v[44:47] offset:0\n" \
"s_add_u32 m0, s22, 0\nv_mfma_f32_16x16x32_bf16 v[68:71], a[88:91], v[28:31], v[68:71]\nbuffer_load_dwordx4 v16, s[12:15], s21 offen offset:0 sc1 lds\nv_mfma_f32_16x16x32_bf16 v[20:23], a[184:187], v[28:31], v[20:23]\n" \
"ds_read_b128 v[42:45], v17 offset:10240\ns_add_u32 s20, s20, 1\nv_mfma_f32_16x16x32_bf16 v[68:71], a[92:95], v[34:37], v[68:71]\ns_waitcnt lgkmcnt(1)\n" \
"s_barrier\nds_read_b128 v[28:31], v16 offset:8192\nv_mfma_f32_16x16x32_bf16 v[20:23], a[188:191], v[34:37], v[20:23]\nds_read_b128 v[48:51], v16 offset:9216\n" \
"v_mfma_f32_16x16x32_bf16 v[68:71], a[96:99], v[24:27], v[68:71]\nds_read_b128 v[64:67], v16 offset:10240\nv_add_u32 v32, s28, v16\nv_mfma_f32_16x16x32_bf16 v[20:23], a[192:195], v[24:27], v[20:23]\n" \
"buffer_load_dwordx4 v16, s[12:15], s21 offen offset:1024 sc1 lds\nds_read_b128 v[72:75], v16 offset:11264\nds_read_b128 v[24:27], v17 offset:11264\nv_mfma_f32_16x16x32_bf16 v[68:71], a[100:103], v[38:41], v[68:71]\n" \
"s_waitcnt lgkmcnt(4)\nv_mfma_f32_16x16x32_bf16 v[20:23], a[196:199], v[38:41], v[20:23]\ns_waitcnt lgkmcnt(3)\nv_mfma_f32_16x16x32_bf16 v[34:37], v[28:31], a[200:203], 0\n" \
"s_waitcnt lgkmcnt(2)\nv_cvt_pk_bf16_f32 v68, v68, v69\nv_cvt_pk_bf16_f32 v69, v70, v71\nv_mfma_f32_16x16x32_bf16 v[34:37], v[48:51], a[204:207], v[34:37]\n" \
"s_waitcnt lgkmcnt(0)\nv_cvt_pk_bf16_f32 v70, v20, v21\nv_cvt_pk_bf16_f32 v71, v22, v23\nv_mfma_f32_16x16x32_bf16 v[34:37], v[64:67], v[42:45], v[34:37]\n" \
"buffer_load_dwordx4 v16, s[12:15], s21 offen offset:2048 sc1 lds\nv_mfma_f32_16x16x32_bf16 v[34:37], v[72:75], v[24:27], v[34:37]\nds_write_b128 v32, v[68:71] offset:32768\nv_mfma_f32_16x16x32_bf16 v[20:23], v[28:31], a[208:211], 0\n" \
"v_mfma_f32_16x16x32_bf16 v[20:23], v[48:51], a[212:215], v[20:23]\nv_cvt_pk_bf16_f32 v34, v34, v35\nv_cvt_pk_bf16_f32 v35, v36, v37\nv_mfma_f32_16x16x32_bf16 v[20:23], v[64:67], a[216:219], v[20:23]\n" \
"s_add_u32 s43, s21, 0x4000\nds_read_b128 v[24:27], v17 offset:18432\nv_mfma_f32_16x16x32_bf16 v[20:23], v[72:75], a[220:223], v[20:23]\ns_waitcnt lgkmcnt(1)\n" \
"s_barrier\nv_mfma_f32_16x16x32_bf16 v[38:41], v[28:31], a[224:227], 0\nbuffer_load_dwordx4 v16, s[12:15], s21 offen offset:3072 sc1 lds\nv_mfma_f32_16x16x32_bf16 v[38:41], v[48:51], a[228:231], v[38:41]\n" \
"s_add_u32 m0, s22, 0x4000\nds_read_b128 v[52:55], v17 offset:19456\nv_mfma_f32_16x16x32_bf16 v[38:41], v[64:67], a[232:235], v[38:41]\nbuffer_load_dwordx4 v16, s[12:15], s43 offen offset:0 sc1 lds\n" \
"v_mfma_f32_16x16x32_bf16 v[38:41], v[72:75], a[236:239], v[38:41]\nds_read_b128 v[68:71], v16 offset:40960\nv_cvt_pk_bf16_f32 v20, v20, v21\nv_mfma_f32_16x16x32_bf16 v[56:59], v[28:31], v[144:147], 0\n" \
"ds_read_b128 v[76:79], v16 offset:41984\nv_cvt_pk_bf16_f32 v21, v22, v23\nv_mfma_f32_16x16x32_bf16 v[56:59], v[48:51], v[148:151], v[56:59]\nds_read_b128 v[80:83], v16 offset:43008\n" \
"v_cvt_pk_bf16_f32 v38, v38, v39\nv_mfma_f32_16x16x32_bf16 v[56:59], v[64:67], v[152:155], v[56:59]\nds_read_b128 v[60:63], v17 offset:10240\nv_cvt_pk_bf16_f32 v39, v40, v41\n" \
"v_mfma_f32_16x16x32_bf16 v[56:59], v[72:75], v[156:159], v[56:59]\nbuffer_load_dwordx4 v16, s[12:15], s43 offen offset:1024 sc1 lds\nv_mfma_f32_16x16x32_bf16 v[84:87], v[28:31], v[200:203], 0\ns_waitcnt lgkmcnt(4)\n" \
"ds_read_b128 v[44:47], v16 offset:44032\nv_mfma_f32_16x16x32_bf16 v[84:87], v[48:51], v[204:207], v[84:87]\nds_read_b128 v[40:43], v17 offset:11264\nv_cvt_pk_bf16_f32 v56, v56, v57\n" \
"v_mfma_f32_16x16x32_bf16 v[84:87], v[64:67], v[24:27], v[84:87]\ns_waitcnt lgkmcnt(4)\nv_cvt_pk_bf16_f32 v57, v58, v59\nv_mfma_f32_16x16x32_bf16 v[84:87], v[72:75], v[52:55], v[84:87]\n" \
"v_mfma_f32_16x16x32_bf16 v[22:25], v[68:71], a[200:203], 0\ns_waitcnt lgkmcnt(2)\nbuffer_load_dwordx4 v16, s[12:15], s43 offen offset:2048 sc1 lds\nv_mfma_f32_16x16x32_bf16 v[22:25], v[76:79], a[204:207], v[22:25]\n" \
"v_cvt_pk_bf16_f32 v84, v84, v85\nv_cvt_pk_bf16_f32 v85, v86, v87\nv_mfma_f32_16x16x32_bf16 v[22:25], v[80:83], v[60:63], v[22:25]\ns_waitcnt lgkmcnt(0)\n" \
"v_mfma_f32_16x16x32_bf16 v[22:25], v[44:47], v[40:43], v[22:25]\nv_mfma_f32_16x16x32_bf16 v[52:55], v[68:71], a[208:211], 0\nbuffer_load_dwordx4 v16, s[12:15], s43 offen offset:3072 sc1 lds\nv_mfma_f32_16x16x32_bf16 v[52:55], v[76:79], a[212:215], v[52:55]\n" \
"v_cvt_pk_bf16_f32 v36, v22, v23\nv_cvt_pk_bf16_f32 v37, v24, v25\nv_mfma_f32_16x16x32_bf16 v[52:55], v[80:83], a[216:219], v[52:55]\nds_read_b128 v[24:27], v17 offset:18432\n" \
"v_mfma_f32_16x16x32_bf16 v[52:55], v[44:47], a[220:223], v[52:55]\nds_read_b128 v[60:63], v17 offset:19456\nv_mfma_f32_16x16x32_bf16 v[86:89], v[68:71], a[224:227], 0\nv_mfma_f32_16x16x32_bf16 v[86:89], v[76:79], a[228:231], v[86:89]\n" \
"buffer_store_dwordx4 v[34:37], v16, s[8:11], s16 offen offset:0 sc1\nv_cvt_pk_bf16_f32 v22, v52, v53\nv_cvt_pk_bf16_f32 v23, v54, v55\nv_mfma_f32_16x16x32_bf16 v[86:89], v[80:83], a[232:235], v[86:89]\n" \
"v_mfma_f32_16x16x32_bf16 v[86:89], v[44:47], a[236:239], v[86:89]\nv_mfma_f32_16x16x32_bf16 v[32:35], v[68:71], v[144:147], 0\nv_mfma_f32_16x16x32_bf16 v[32:35], v[76:79], v[148:151], v[32:35]\nv_cvt_pk_bf16_f32 v40, v86, v87\n" \
"v_cvt_pk_bf16_f32 v41, v88, v89\nv_mfma_f32_16x16x32_bf16 v[32:35], v[80:83], v[152:155], v[32:35]\nv_mfma_f32_16x16x32_bf16 v[32:35], v[44:47], v[156:159], v[32:35]\nv_mfma_f32_16x16x32_bf16 v[52:55], v[68:71], v[200:203], 0\n" \
"s_waitcnt lgkmcnt(0)\nbuffer_store_dwordx4 v[20:23], v16, s[8:11], s16 offen offset:1024 sc1\nv_mfma_f32_16x16x32_bf16 v[52:55], v[76:79], v[204:207], v[52:55]\nv_cvt_pk_bf16_f32 v58, v32, v33\n" \
"v_cvt_pk_bf16_f32 v59, v34, v35\nv_mfma_f32_16x16x32_bf16 v[52:55], v[80:83], v[24:27], v[52:55]\nv_mfma_f32_16x16x32_bf16 v[52:55], v[44:47], v[60:63], v[52:55]\nv_mfma_f32_16x16x32_bf16 v[20:23], v[28:31], a[240:243], 0\n" \
"v_mfma_f32_16x16x32_bf16 v[24:27], v[68:71], a[240:243], 0\nv_cvt_pk_bf16_f32 v86, v52, v53\nv_cvt_pk_bf16_f32 v87, v54, v55\nv_mfma_f32_16x16x32_bf16 v[20:23], v[48:51], a[244:247], v[20:23]\n" \
"v_mfma_f32_16x16x32_bf16 v[24:27], v[76:79], a[244:247], v[24:27]\nv_mfma_f32_16x16x32_bf16 v[20:23], v[64:67], a[248:251], v[20:23]\nbuffer_store_dwordx4 v[38:41], v16, s[8:11], s16 offen offset:2048 sc1\nds_read_b128 v[32:35], v17 offset:12288\n" \
"v_mfma_f32_16x16x32_bf16 v[24:27], v[80:83], a[248:251], v[24:27]\nv_mfma_f32_16x16x32_bf16 v[20:23], v[72:75], a[252:255], v[20:23]\nds_read_b128 v[36:39], v17 offset:13312\nv_mfma_f32_16x16x32_bf16 v[24:27], v[44:47], a[252:255], v[24:27]\n" \
"v_mfma_f32_16x16x32_bf16 v[40:43], v[28:31], v[128:131], 0\nds_read_b128 v[52:55], v17 offset:14336\nv_cvt_pk_bf16_f32 v20, v20, v21\nv_mfma_f32_16x16x32_bf16 v[60:63], v[68:71], v[128:131], 0\n" \
"v_cvt_pk_bf16_f32 v21, v22, v23\nv_cvt_pk_bf16_f32 v22, v24, v25\nv_mfma_f32_16x16x32_bf16 v[40:43], v[48:51], v[132:135], v[40:43]\ns_waitcnt vmcnt(7)\n" \
"s_barrier\nv_mfma_f32_16x16x32_bf16 v[60:63], v[76:79], v[132:135], v[60:63]\nbuffer_store_dwordx4 v[56:59], v16, s[8:11], s17 offen offset:3072 sc1\nds_read_b128 v[88:91], v17 offset:15360\n" \
"v_cvt_pk_bf16_f32 v23, v26, v27\nv_mfma_f32_16x16x32_bf16 v[40:43], v[64:67], v[136:139], v[40:43]\nv_mfma_f32_16x16x32_bf16 v[60:63], v[80:83], v[136:139], v[60:63]\nv_mfma_f32_16x16x32_bf16 v[40:43], v[72:75], v[140:143], v[40:43]\n" \
"s_waitcnt lgkmcnt(1)\nv_mfma_f32_16x16x32_bf16 v[60:63], v[44:47], v[140:143], v[60:63]\nv_mfma_f32_16x16x32_bf16 v[24:27], v[28:31], v[32:35], 0\nv_cvt_pk_bf16_f32 v40, v40, v41\n" \
"v_cvt_pk_bf16_f32 v41, v42, v43\nv_mfma_f32_16x16x32_bf16 v[56:59], v[68:71], v[32:35], 0\nv_cvt_pk_bf16_f32 v42, v60, v61\nv_cvt_pk_bf16_f32 v43, v62, v63\n" \
"v_mfma_f32_16x16x32_bf16 v[24:27], v[48:51], v[36:39], v[24:27]\nv_mfma_f32_16x16x32_bf16 v[56:59], v[76:79], v[36:39], v[56:59]\nbuffer_store_dwordx4 v[84:87], v16, s[8:11], s18 offen offset:3072 sc1\ns_add_u32 m0, s22, 0x2000\n" \
"s_add_u32 s43, s21, 0x2000\ns_add_u32 s21, s21, 0x10000\nv_mfma_f32_16x16x32_bf16 v[24:27], v[64:67], v[52:55], v[24:27]\ns_waitcnt lgkmcnt(0)\n" \
"ds_read_b128 v[32:35], v17 offset:16384\nv_mfma_f32_16x16x32_bf16 v[56:59], v[80:83], v[52:55], v[56:59]\nv_mfma_f32_16x16x32_bf16 v[24:27], v[72:75], v[88:91], v[24:27]\nds_read_b128 v[36:39], v17 offset:17408\n" \
"v_mfma_f32_16x16x32_bf16 v[56:59], v[44:47], v[88:91], v[56:59]\nbuffer_load_dwordx4 v16, s[12:15], s43 offen offset:0 sc1 lds\nv_mfma_f32_16x16x32_bf16 v[52:55], v[28:31], v[112:115], 0\nv_cvt_pk_bf16_f32 v24, v24, v25\n" \
"v_cvt_pk_bf16_f32 v25, v26, v27\nv_mfma_f32_16x16x32_bf16 v[60:63], v[68:71], v[112:115], 0\nv_cvt_pk_bf16_f32 v26, v56, v57\nv_cvt_pk_bf16_f32 v27, v58, v59\n" \
"v_mfma_f32_16x16x32_bf16 v[52:55], v[48:51], v[116:119], v[52:55]\nbuffer_store_dwordx4 v[20:23], v16, s[8:11], s16 offen offset:3072 sc1\nv_mfma_f32_16x16x32_bf16 v[60:63], v[76:79], v[116:119], v[60:63]\nv_mfma_f32_16x16x32_bf16 v[52:55], v[64:67], v[120:123], v[52:55]\n" \
"v_mfma_f32_16x16x32_bf16 v[60:63], v[80:83], v[120:123], v[60:63]\nv_mfma_f32_16x16x32_bf16 v[52:55], v[72:75], v[124:127], v[52:55]\ns_waitcnt lgkmcnt(0)\nv_mfma_f32_16x16x32_bf16 v[60:63], v[44:47], v[124:127], v[60:63]\n" \
"v_mfma_f32_16x16x32_bf16 v[20:23], v[28:31], v[32:35], 0\nv_cvt_pk_bf16_f32 v52, v52, v53\nv_cvt_pk_bf16_f32 v53, v54, v55\nv_mfma_f32_16x16x32_bf16 v[56:59], v[68:71], v[32:35], 0\n" \
"buffer_store_dwordx4 v[40:43], v16, s[8:11], s17 offen offset:2048 sc1\nbuffer_load_dwordx4 v16, s[12:15], s43 offen offset:1024 sc1 lds\nv_cvt_pk_bf16_f32 v54, v60, v61\nv_mfma_f32_16x16x32_bf16 v[20:23], v[48:51], v[36:39], v[20:23]\n" \
"v_cvt_pk_bf16_f32 v55, v62, v63\nv_mfma_f32_16x16x32_bf16 v[56:59], v[76:79], v[36:39], v[56:59]\nv_mfma_f32_16x16x32_bf16 v[20:23], v[64:67], v[176:179], v[20:23]\nv_mfma_f32_16x16x32_bf16 v[56:59], v[80:83], v[176:179], v[56:59]\n" \
"v_mfma_f32_16x16x32_bf16 v[20:23], v[72:75], v[180:183], v[20:23]\nv_mfma_f32_16x16x32_bf16 v[56:59], v[44:47], v[180:183], v[56:59]\nv_mfma_f32_16x16x32_bf16 v[32:35], v[28:31], v[184:187], 0\nv_cvt_pk_bf16_f32 v20, v20, v21\n" \
"v_cvt_pk_bf16_f32 v21, v22, v23\nv_mfma_f32_16x16x32_bf16 v[36:39], v[68:71], v[184:187], 0\nbuffer_store_dwordx4 v[24:27], v16, s[8:11], s17 offen offset:1024 sc1\nv_cvt_pk_bf16_f32 v22, v56, v57\n" \
"v_cvt_pk_bf16_f32 v23, v58, v59\nv_mfma_f32_16x16x32_bf16 v[32:35], v[48:51], v[188:191], v[32:35]\nv_mfma_f32_16x16x32_bf16 v[36:39], v[76:79], v[188:191], v[36:39]\nbuffer_load_dwordx4 v16, s[12:15], s43 offen offset:2048 sc1 lds\n" \
"v_mfma_f32_16x16x32_bf16 v[32:35], v[64:67], v[192:195], v[32:35]\nv_mfma_f32_16x16x32_bf16 v[36:39], v[80:83], v[192:195], v[36:39]\nv_mfma_f32_16x16x32_bf16 v[32:35], v[72:75], v[196:199], v[32:35]\nv_mfma_f32_16x16x32_bf16 v[36:39], v[44:47], v[196:199], v[36:39]\n" \
"v_mfma_f32_16x16x32_bf16 v[24:27], v[28:31], v[160:163], 0\nbuffer_store_dwordx4 v[52:55], v16, s[8:11], s17 offen offset:0 sc1\nv_cvt_pk_bf16_f32 v32, v32, v33\nv_cvt_pk_bf16_f32 v33, v34, v35\n" \
"v_mfma_f32_16x16x32_bf16 v[40:43], v[68:71], v[160:163], 0\nv_cvt_pk_bf16_f32 v34, v36, v37\nv_cvt_pk_bf16_f32 v35, v38, v39\nv_mfma_f32_16x16x32_bf16 v[24:27], v[48:51], v[164:167], v[24:27]\n" \
"v_mfma_f32_16x16x32_bf16 v[40:43], v[76:79], v[164:167], v[40:43]\nv_mfma_f32_16x16x32_bf16 v[24:27], v[64:67], v[168:171], v[24:27]\nds_read_b128 v[36:39], v17 offset:20480\nv_mfma_f32_16x16x32_bf16 v[40:43], v[80:83], v[168:171], v[40:43]\n" \
"buffer_load_dwordx4 v16, s[12:15], s43 offen offset:3072 sc1 lds\nv_mfma_f32_16x16x32_bf16 v[24:27], v[72:75], v[172:175], v[24:27]\nds_read_b128 v[52:55], v17 offset:21504\nv_mfma_f32_16x16x32_bf16 v[40:43], v[44:47], v[172:175], v[40:43]\n" \
"v_mfma_f32_16x16x32_bf16 v[60:63], v[28:31], v[232:235], 0\nbuffer_store_dwordx4 v[20:23], v16, s[8:11], s18 offen offset:1024 sc1\nv_cvt_pk_bf16_f32 v24, v24, v25\nv_cvt_pk_bf16_f32 v25, v26, v27\n" \
"v_mfma_f32_16x16x32_bf16 v[20:23], v[68:71], v[232:235], 0\nv_cvt_pk_bf16_f32 v26, v40, v41\nv_cvt_pk_bf16_f32 v27, v42, v43\nv_mfma_f32_16x16x32_bf16 v[60:63], v[48:51], v[236:239], v[60:63]\n" \
"v_mfma_f32_16x16x32_bf16 v[20:23], v[76:79], v[236:239], v[20:23]\nv_mfma_f32_16x16x32_bf16 v[60:63], v[64:67], v[240:243], v[60:63]\nv_mfma_f32_16x16x32_bf16 v[20:23], v[80:83], v[240:243], v[20:23]\nv_mfma_f32_16x16x32_bf16 v[60:63], v[72:75], v[244:247], v[60:63]\n" \
"s_waitcnt lgkmcnt(0)\nv_mfma_f32_16x16x32_bf16 v[20:23], v[44:47], v[244:247], v[20:23]\nbuffer_store_dwordx4 v[32:35], v16, s[8:11], s18 offen offset:2048 sc1\nv_mfma_f32_16x16x32_bf16 v[84:87], v[28:31], v[36:39], 0\n" \
"v_cvt_pk_bf16_f32 v60, v60, v61\nv_cvt_pk_bf16_f32 v61, v62, v63\nv_mfma_f32_16x16x32_bf16 v[32:35], v[68:71], v[36:39], 0\nv_cvt_pk_bf16_f32 v62, v20, v21\n" \
"v_cvt_pk_bf16_f32 v63, v22, v23\nv_mfma_f32_16x16x32_bf16 v[84:87], v[48:51], v[52:55], v[84:87]\nv_mfma_f32_16x16x32_bf16 v[32:35], v[76:79], v[52:55], v[32:35]\nv_mfma_f32_16x16x32_bf16 v[84:87], v[64:67], v[224:227], v[84:87]\n" \
"ds_read_b128 v[52:55], v17 offset:22528\nv_mfma_f32_16x16x32_bf16 v[32:35], v[80:83], v[224:227], v[32:35]\nv_mfma_f32_16x16x32_bf16 v[84:87], v[72:75], v[228:231], v[84:87]\nbuffer_store_dwordx4 v[24:27], v16, s[8:11], s18 offen offset:0 sc1\n" \
"ds_read_b128 v[88:91], v17 offset:23552\nv_mfma_f32_16x16x32_bf16 v[32:35], v[44:47], v[228:231], v[32:35]\nv_mfma_f32_16x16x32_bf16 v[56:59], v[28:31], v[208:211], 0\nv_cvt_pk_bf16_f32 v84, v84, v85\n" \
"v_cvt_pk_bf16_f32 v85, v86, v87\nv_mfma_f32_16x16x32_bf16 v[24:27], v[68:71], v[208:211], 0\nv_cvt_pk_bf16_f32 v86, v32, v33\nv_cvt_pk_bf16_f32 v87, v34, v35\n" \
"v_mfma_f32_16x16x32_bf16 v[56:59], v[48:51], v[212:215], v[56:59]\nv_mfma_f32_16x16x32_bf16 v[24:27], v[76:79], v[212:215], v[24:27]\nds_read_b128 v[20:23], v16 offset:0\nv_mfma_f32_16x16x32_bf16 v[56:59], v[64:67], v[216:219], v[56:59]\n" \
"ds_read_b128 v[40:43], v16 offset:32768\nv_mfma_f32_16x16x32_bf16 v[24:27], v[80:83], v[216:219], v[24:27]\nv_mfma_f32_16x16x32_bf16 v[56:59], v[72:75], v[220:223], v[56:59]\ns_waitcnt lgkmcnt(2)\n" \
"buffer_store_dwordx4 v[60:63], v16, s[8:11], s19 offen offset:2048 sc1\nv_mfma_f32_16x16x32_bf16 v[24:27], v[44:47], v[220:223], v[24:27]\nds_read_b128 v[36:39], v16 offset:1024\nv_mfma_f32_16x16x32_bf16 v[60:63], v[28:31], v[52:55], 0\n" \
"ds_read_b128 v[32:35], v16 offset:33792\nv_cvt_pk_bf16_f32 v56, v56, v57\nv_mfma_f32_16x16x32_bf16 v[28:31], v[68:71], v[52:55], 0\nv_cvt_pk_bf16_f32 v57, v58, v59\n" \
"v_cvt_pk_bf16_f32 v58, v24, v25\nv_mfma_f32_16x16x32_bf16 v[60:63], v[48:51], v[88:91], v[60:63]\nv_cvt_pk_bf16_f32 v59, v26, v27\nv_mfma_f32_16x16x32_bf16 v[28:31], v[76:79], v[88:91], v[28:31]\n" \
"ds_read_b128 v[52:55], v16 offset:2048\nv_mfma_f32_16x16x32_bf16 v[60:63], v[64:67], v[248:251], v[60:63]\nds_read_b128 v[24:27], v16 offset:34816\nv_mfma_f32_16x16x32_bf16 v[28:31], v[80:83], v[248:251], v[28:31]\n" \
"buffer_store_dwordx4 v[84:87], v16, s[8:11], s19 offen offset:1024 sc1\nv_mfma_f32_16x16x32_bf16 v[60:63], v[72:75], v[252:255], v[60:63]\ns_waitcnt lgkmcnt(2)\nv_mfma_f32_16x16x32_bf16 v[28:31], v[44:47], v[252:255], v[28:31]\n" \
"ds_read_b128 v[48:51], v16 offset:3072\nv_mfma_f32_16x16x32_bf16 v[44:47], v[0:3], v[20:23], 0\nds_read_b128 v[64:67], v16 offset:35840\nv_cvt_pk_bf16_f32 v60, v60, v61\n" \
"v_mfma_f32_16x16x32_bf16 v[68:71], v[0:3], v[40:43], 0\nv_cvt_pk_bf16_f32 v61, v62, v63\nv_cvt_pk_bf16_f32 v62, v28, v29\nv_mfma_f32_16x16x32_bf16 v[72:75], v[8:11], v[20:23], 0\n" \
"v_cvt_pk_bf16_f32 v63, v30, v31\nv_mfma_f32_16x16x32_bf16 v[20:23], v[8:11], v[40:43], 0\nds_read_b128 v[28:31], v16 offset:4096\nv_mfma_f32_16x16x32_bf16 v[44:47], v[4:7], v[36:39], v[44:47]\n" \
"ds_read_b128 v[40:43], v16 offset:36864\nv_mfma_f32_16x16x32_bf16 v[68:71], v[4:7], v[32:35], v[68:71]\nbuffer_store_dwordx4 v[56:59], v16, s[8:11], s19 offen offset:0 sc1\nv_mfma_f32_16x16x32_bf16 v[72:75], v[12:15], v[36:39], v[72:75]\n" \
"s_waitcnt lgkmcnt(2)\nv_mfma_f32_16x16x32_bf16 v[20:23], v[12:15], v[32:35], v[20:23]\nds_read_b128 v[36:39], v16 offset:5120\nv_mfma_f32_16x16x32_bf16 v[44:47], a[0:3], v[52:55], v[44:47]\n" \
"ds_read_b128 v[32:35], v16 offset:37888\nv_mfma_f32_16x16x32_bf16 v[68:71], a[0:3], v[24:27], v[68:71]\nv_mfma_f32_16x16x32_bf16 v[72:75], a[104:107], v[52:55], v[72:75]\nv_mfma_f32_16x16x32_bf16 v[20:23], a[104:107], v[24:27], v[20:23]\n" \
"ds_read_b128 v[52:55], v16 offset:6144\nv_mfma_f32_16x16x32_bf16 v[44:47], a[4:7], v[48:51], v[44:47]\nbuffer_store_dwordx4 v[60:63], v16, s[8:11], s19 offen offset:3072 sc1\nds_read_b128 v[24:27], v16 offset:38912\n" \
"v_mfma_f32_16x16x32_bf16 v[68:71], a[4:7], v[64:67], v[68:71]\nds_read_b128 v[56:59], v16 offset:7168\nv_mfma_f32_16x16x32_bf16 v[72:75], a[108:111], v[48:51], v[72:75]\nds_read_b128 v[60:63], v16 offset:39936\n" \
"v_mfma_f32_16x16x32_bf16 v[20:23], a[108:111], v[64:67], v[20:23]\ns_branch 1b\n2:\ns_cmp_lt_i32 s20, 1\n" \
"s_cbranch_scc1 3b\ns_add_u32 s30, s30, 1\ns_cmp_lt_u32 s30, 0x40000\ns_cbranch_scc0 9f\n" \
"v_cmp_eq_u32 vcc, -1, v36\ns_cbranch_vccz 6f\nbuffer_load_dwordx4 v16, s[8:11], s43 offen offset:0 sc1 lds\n6:\n" \
"v_cmp_eq_u32 vcc, -1, v80\ns_cbranch_vccz 6f\nbuffer_load_dwordx4 v16, s[8:11], s43 offen offset:1024 sc1 lds\n6:\n" \
"v_cmp_eq_u32 vcc, -1, v48\ns_cbranch_vccz 6f\nbuffer_load_dwordx4 v16, s[8:11], s43 offen offset:2048 sc1 lds\n6:\n" \
"v_cmp_eq_u32 vcc, -1, v84\ns_cbranch_vccz 6f\nbuffer_load_dwordx4 v16, s[8:11], s43 offen offset:3072 sc1 lds\n6:\n" \
"s_waitcnt vmcnt(0)\nds_read_b128 v[36:39], v61 offset:0\nds_read_b128 v[80:83], v61 offset:1024\nds_read_b128 v[48:51], v61 offset:2048\n" \
"ds_read_b128 v[84:87], v61 offset:3072\ns_waitcnt lgkmcnt(0)\nv_max3_u32 v52, v36, v80, v48\nv_max_u32 v52, v52, v84\n" \
"v_cmp_eq_u32 vcc, -1, v52\ns_cbranch_vccnz 2b\ns_branch 3b\n4:\n" \
"s_cmp_ge_i32 s20, 64\ns_cbranch_scc1 10f\ns_cmp_ge_i32 s20, 62\ns_cbranch_scc1 5b\n" \
"v_bfe_u32 v32, v16, 4, 3\nv_lshlrev_b32 v32, 2, v32\ns_lshl_b32 s43, s20, 5\ns_add_u32 s43, s43, 64\n" \
"s_add_u32 s43, s43, s26\n7:\nv_cmp_eq_u32 vcc, -1, v33\ns_cbranch_vccz 8f\n" \
"s_add_u32 s30, s30, 1\ns_cmp_lt_u32 s30, 0x40000\ns_cbranch_scc0 9f\ns_sleep 8\n" \
"buffer_load_dword v33, v32, s[8:11], s43 offen sc1\ns_waitcnt vmcnt(0)\ns_branch 7b\n8:\n" \
"s_mov_b32 s14, s10\ns_branch 5b\n10:\ns_waitcnt vmcnt(0) lgkmcnt(0)\n" \
"s_cmp_lt_i32 s6, 0\ns_cbranch_scc1 9f\ns_mov_b64 s[0:1], s[4:5]\ns_mov_b32 s2, s6\n" \
"v_lshrrev_b32 v0, 4, v16\nv_add_u32 v0, s7, v0\ns_barrier\ns_getpc_b64 s[8:9]\n" \
"11:\ns_add_u32 s8, s8, _Z5mega64Args-11b\ns_addc_u32 s9, s9, -1\ns_setpc_b64 s[8:9]\n" \
"9:\ns_waitcnt vmcnt(0)\ns_endpgm\n"
#define D_LOOP \
"s_branch 4f\n.p2align 6\n1:\ns_waitcnt lgkmcnt(0)\n" \
"v_mfma_f32_16x16x32_bf16 v[44:47], a[8:11], v[8:11], v[44:47]\nds_read_b128 v[16:19], v4 offset:3072\ns_mov_b32 m0, s24\nv_mfma_f32_16x16x32_bf16 v[64:67], a[8:11], v[12:15], v[64:67]\n" \
"ds_read_b128 v[32:35], v4 offset:35840\nbuffer_load_dword v1, s[16:19], 0 offen offset:0 sc1 lds\nv_mfma_f32_16x16x32_bf16 v[48:51], a[104:107], v[8:11], v[48:51]\ns_add_u32 m0, s23, 0\n" \
"s_and_b32 s98, s20, 1\ns_lshl_b32 s98, s98, 3\nv_mfma_f32_16x16x32_bf16 v[20:23], a[104:107], v[12:15], v[20:23]\nv_mfma_f32_16x16x32_bf16 v[56:59], a[200:203], v[8:11], v[56:59]\n" \
"v_mfma_f32_16x16x32_bf16 v[24:27], a[200:203], v[12:15], v[24:27]\nv_mfma_f32_16x16x32_bf16 v[60:63], v[152:155], v[8:11], v[60:63]\ns_waitcnt lgkmcnt(1)\nv_mfma_f32_16x16x32_bf16 v[28:31], v[152:155], v[12:15], v[28:31]\n" \
"s_waitcnt lgkmcnt(0)\nds_read_b128 v[8:11], v4 offset:4096\nv_mfma_f32_16x16x32_bf16 v[44:47], a[12:15], v[16:19], v[44:47]\nds_read_b128 v[12:15], v4 offset:36864\n" \
"v_mfma_f32_16x16x32_bf16 v[64:67], a[12:15], v[32:35], v[64:67]\nv_mfma_f32_16x16x32_bf16 v[48:51], a[108:111], v[16:19], v[48:51]\nv_mfma_f32_16x16x32_bf16 v[20:23], a[108:111], v[32:35], v[20:23]\nv_mfma_f32_16x16x32_bf16 v[56:59], a[204:207], v[16:19], v[56:59]\n" \
"v_mfma_f32_16x16x32_bf16 v[24:27], a[204:207], v[32:35], v[24:27]\nv_mfma_f32_16x16x32_bf16 v[60:63], v[156:159], v[16:19], v[60:63]\ns_waitcnt lgkmcnt(0)\nv_mfma_f32_16x16x32_bf16 v[28:31], v[156:159], v[32:35], v[28:31]\n" \
"ds_read_b128 v[16:19], v4 offset:5120\nv_mfma_f32_16x16x32_bf16 v[44:47], a[16:19], v[8:11], v[44:47]\nds_read_b128 v[32:35], v4 offset:37888\nv_mfma_f32_16x16x32_bf16 v[64:67], a[16:19], v[12:15], v[64:67]\n" \
"v_mfma_f32_16x16x32_bf16 v[48:51], a[112:115], v[8:11], v[48:51]\nv_mfma_f32_16x16x32_bf16 v[20:23], a[112:115], v[12:15], v[20:23]\nv_mfma_f32_16x16x32_bf16 v[56:59], a[208:211], v[8:11], v[56:59]\nv_mfma_f32_16x16x32_bf16 v[24:27], a[208:211], v[12:15], v[24:27]\n" \
"v_mfma_f32_16x16x32_bf16 v[60:63], v[160:163], v[8:11], v[60:63]\ns_waitcnt lgkmcnt(0)\nv_mfma_f32_16x16x32_bf16 v[28:31], v[160:163], v[12:15], v[28:31]\nds_read_b128 v[8:11], v4 offset:6144\n" \
"s_add_u32 s99, s20, 1\nv_mfma_f32_16x16x32_bf16 v[44:47], a[20:23], v[16:19], v[44:47]\nds_read_b128 v[12:15], v4 offset:38912\ns_lshr_b32 s97, s99, 1\n" \
"v_mfma_f32_16x16x32_bf16 v[64:67], a[20:23], v[32:35], v[64:67]\ns_and_b32 s99, s99, 1\ns_lshl_b32 s99, s99, 3\nv_readlane_b32 s97, v3, s97\n" \
"v_mfma_f32_16x16x32_bf16 v[48:51], a[116:119], v[16:19], v[48:51]\nv_mfma_f32_16x16x32_bf16 v[20:23], a[116:119], v[32:35], v[20:23]\ns_lshr_b32 s97, s97, s99\ns_and_b32 s97, s97, 0xff\n" \
"s_mul_i32 s97, s97, 0x38000\nv_mfma_f32_16x16x32_bf16 v[56:59], a[212:215], v[16:19], v[56:59]\ns_add_u32 s97, s97, s21\nv_mfma_f32_16x16x32_bf16 v[24:27], a[212:215], v[32:35], v[24:27]\n" \
"v_mfma_f32_16x16x32_bf16 v[60:63], v[164:167], v[16:19], v[60:63]\ns_waitcnt lgkmcnt(0)\nv_mfma_f32_16x16x32_bf16 v[28:31], v[164:167], v[32:35], v[28:31]\nds_read_b128 v[16:19], v4 offset:7168\n" \
"v_mfma_f32_16x16x32_bf16 v[44:47], a[24:27], v[8:11], v[44:47]\nds_read_b128 v[32:35], v4 offset:39936\nv_mfma_f32_16x16x32_bf16 v[64:67], a[24:27], v[12:15], v[64:67]\nv_mfma_f32_16x16x32_bf16 v[48:51], a[120:123], v[8:11], v[48:51]\n" \
"v_mfma_f32_16x16x32_bf16 v[20:23], a[120:123], v[12:15], v[20:23]\nv_mfma_f32_16x16x32_bf16 v[56:59], a[216:219], v[8:11], v[56:59]\nv_mfma_f32_16x16x32_bf16 v[24:27], a[216:219], v[12:15], v[24:27]\nv_mfma_f32_16x16x32_bf16 v[60:63], v[168:171], v[8:11], v[60:63]\n" \
"s_waitcnt lgkmcnt(0)\nv_mfma_f32_16x16x32_bf16 v[28:31], v[168:171], v[12:15], v[28:31]\nds_read_b128 v[8:11], v4 offset:8192\nv_mov_b32 v111, s20\n" \
"v_mfma_f32_16x16x32_bf16 v[44:47], a[28:31], v[16:19], v[44:47]\nds_read_b128 v[12:15], v4 offset:40960\ns_add_u32 s99, s20, 2\nv_mfma_f32_16x16x32_bf16 v[64:67], a[28:31], v[32:35], v[64:67]\n" \
"s_cmp_le_u32 s99, s28\ns_cselect_b32 s99, s99, 0x1000\nv_mfma_f32_16x16x32_bf16 v[48:51], a[124:127], v[16:19], v[48:51]\nv_mfma_f32_16x16x32_bf16 v[20:23], a[124:127], v[32:35], v[20:23]\n" \
"v_mfma_f32_16x16x32_bf16 v[56:59], a[220:223], v[16:19], v[56:59]\ns_waitcnt vmcnt(0)\ns_barrier\nv_mfma_f32_16x16x32_bf16 v[24:27], a[220:223], v[32:35], v[24:27]\n" \
"buffer_store_dword v111, v2, s[8:11], s25 offen offset:0 sc1\nv_mfma_f32_16x16x32_bf16 v[60:63], v[172:175], v[16:19], v[60:63]\nds_read_b32 v110, v5 offset:0\ns_mov_b32 s25, 0\n" \
"v_mfma_f32_16x16x32_bf16 v[28:31], v[172:175], v[32:35], v[28:31]\ns_waitcnt lgkmcnt(1)\nds_read_b128 v[16:19], v4 offset:9216\nv_mfma_f32_16x16x32_bf16 v[44:47], a[32:35], v[8:11], v[44:47]\n" \
"ds_read_b128 v[32:35], v4 offset:41984\nv_mfma_f32_16x16x32_bf16 v[64:67], a[32:35], v[12:15], v[64:67]\nv_mfma_f32_16x16x32_bf16 v[48:51], a[128:131], v[8:11], v[48:51]\ns_waitcnt lgkmcnt(2)\n" \
"v_cmp_gt_i32 s[94:95], s99, v110\nv_mfma_f32_16x16x32_bf16 v[20:23], a[128:131], v[12:15], v[20:23]\nv_mfma_f32_16x16x32_bf16 v[56:59], a[224:227], v[8:11], v[56:59]\ns_cmp_eq_u64 s[94:95], 0\n" \
"s_cselect_b32 s14, s10, 0\nv_mfma_f32_16x16x32_bf16 v[24:27], a[224:227], v[12:15], v[24:27]\nv_mfma_f32_16x16x32_bf16 v[60:63], v[176:179], v[8:11], v[60:63]\ns_waitcnt lgkmcnt(0)\n" \
"v_mfma_f32_16x16x32_bf16 v[28:31], v[176:179], v[12:15], v[28:31]\nds_read_b128 v[8:11], v4 offset:10240\nv_mfma_f32_16x16x32_bf16 v[44:47], a[36:39], v[16:19], v[44:47]\nds_read_b128 v[12:15], v4 offset:43008\n" \
"v_mfma_f32_16x16x32_bf16 v[64:67], a[36:39], v[32:35], v[64:67]\nv_mfma_f32_16x16x32_bf16 v[48:51], a[132:135], v[16:19], v[48:51]\nv_mfma_f32_16x16x32_bf16 v[20:23], a[132:135], v[32:35], v[20:23]\nbuffer_load_dwordx4 v0, s[12:15], s97 offen offset:0 sc1 lds\n" \
"v_mfma_f32_16x16x32_bf16 v[56:59], a[228:231], v[16:19], v[56:59]\nv_mfma_f32_16x16x32_bf16 v[24:27], a[228:231], v[32:35], v[24:27]\nv_mfma_f32_16x16x32_bf16 v[60:63], v[180:183], v[16:19], v[60:63]\ns_waitcnt lgkmcnt(0)\n" \
"v_mfma_f32_16x16x32_bf16 v[28:31], v[180:183], v[32:35], v[28:31]\nbuffer_load_dwordx4 v0, s[12:15], s97 offen offset:1024 sc1 lds\nv_mfma_f32_16x16x32_bf16 v[44:47], a[40:43], v[8:11], v[44:47]\nds_read_b128 v[16:19], v4 offset:11264\n" \
"v_mfma_f32_16x16x32_bf16 v[64:67], a[40:43], v[12:15], v[64:67]\nds_read_b128 v[32:35], v4 offset:44032\nv_mfma_f32_16x16x32_bf16 v[48:51], a[136:139], v[8:11], v[48:51]\nv_mfma_f32_16x16x32_bf16 v[20:23], a[136:139], v[12:15], v[20:23]\n" \
"buffer_load_dwordx4 v0, s[12:15], s97 offen offset:2048 sc1 lds\nv_mfma_f32_16x16x32_bf16 v[56:59], a[232:235], v[8:11], v[56:59]\nv_mfma_f32_16x16x32_bf16 v[24:27], a[232:235], v[12:15], v[24:27]\nv_mfma_f32_16x16x32_bf16 v[60:63], v[184:187], v[8:11], v[60:63]\n" \
"s_waitcnt lgkmcnt(1)\nv_mfma_f32_16x16x32_bf16 v[28:31], v[184:187], v[12:15], v[28:31]\ns_waitcnt lgkmcnt(0)\nbuffer_load_dwordx4 v0, s[12:15], s97 offen offset:3072 sc1 lds\n" \
"v_mfma_f32_16x16x32_bf16 v[44:47], a[44:47], v[16:19], v[44:47]\nds_read_b128 v[8:11], v4 offset:12288\ns_add_u32 m0, s23, 0x1000\nv_mfma_f32_16x16x32_bf16 v[64:67], a[44:47], v[32:35], v[64:67]\n" \
"ds_read_b128 v[12:15], v4 offset:45056\ns_add_u32 s97, s97, 0x1000\nv_mfma_f32_16x16x32_bf16 v[48:51], a[140:143], v[16:19], v[48:51]\nv_mfma_f32_16x16x32_bf16 v[20:23], a[140:143], v[32:35], v[20:23]\n" \
"buffer_load_dwordx4 v0, s[12:15], s97 offen offset:0 sc1 lds\nv_mfma_f32_16x16x32_bf16 v[56:59], a[236:239], v[16:19], v[56:59]\nv_mfma_f32_16x16x32_bf16 v[24:27], a[236:239], v[32:35], v[24:27]\nv_mfma_f32_16x16x32_bf16 v[60:63], v[188:191], v[16:19], v[60:63]\n" \
"s_waitcnt lgkmcnt(1)\nv_mfma_f32_16x16x32_bf16 v[28:31], v[188:191], v[32:35], v[28:31]\ns_waitcnt lgkmcnt(0)\nbuffer_load_dwordx4 v0, s[12:15], s97 offen offset:1024 sc1 lds\n" \
"v_mfma_f32_16x16x32_bf16 v[44:47], a[48:51], v[8:11], v[44:47]\nds_read_b128 v[16:19], v4 offset:13312\nv_mfma_f32_16x16x32_bf16 v[64:67], a[48:51], v[12:15], v[64:67]\nds_read_b128 v[32:35], v4 offset:46080\n" \
"v_mfma_f32_16x16x32_bf16 v[48:51], a[144:147], v[8:11], v[48:51]\nv_mfma_f32_16x16x32_bf16 v[20:23], a[144:147], v[12:15], v[20:23]\nbuffer_load_dwordx4 v0, s[12:15], s97 offen offset:2048 sc1 lds\nv_mfma_f32_16x16x32_bf16 v[56:59], a[240:243], v[8:11], v[56:59]\n" \
"v_mfma_f32_16x16x32_bf16 v[24:27], a[240:243], v[12:15], v[24:27]\nv_mfma_f32_16x16x32_bf16 v[60:63], v[192:195], v[8:11], v[60:63]\ns_waitcnt lgkmcnt(1)\nv_mfma_f32_16x16x32_bf16 v[28:31], v[192:195], v[12:15], v[28:31]\n" \
"s_waitcnt lgkmcnt(0)\nbuffer_load_dwordx4 v0, s[12:15], s97 offen offset:3072 sc1 lds\nv_mfma_f32_16x16x32_bf16 v[44:47], a[52:55], v[16:19], v[44:47]\nds_read_b128 v[8:11], v4 offset:14336\n" \
"s_add_u32 m0, s23, 0x2000\nv_mfma_f32_16x16x32_bf16 v[64:67], a[52:55], v[32:35], v[64:67]\nds_read_b128 v[12:15], v4 offset:47104\ns_add_u32 s97, s97, 0x1000\n" \
"v_mfma_f32_16x16x32_bf16 v[48:51], a[148:151], v[16:19], v[48:51]\ns_xor_b32 s23, s23, 0x10000\nv_mfma_f32_16x16x32_bf16 v[20:23], a[148:151], v[32:35], v[20:23]\nbuffer_load_dwordx4 v0, s[12:15], s97 offen offset:0 sc1 lds\n" \
"v_mfma_f32_16x16x32_bf16 v[56:59], a[244:247], v[16:19], v[56:59]\nv_mfma_f32_16x16x32_bf16 v[24:27], a[244:247], v[32:35], v[24:27]\nv_mfma_f32_16x16x32_bf16 v[60:63], v[196:199], v[16:19], v[60:63]\ns_waitcnt lgkmcnt(1)\n" \
"v_mfma_f32_16x16x32_bf16 v[28:31], v[196:199], v[32:35], v[28:31]\ns_waitcnt lgkmcnt(0)\nbuffer_load_dwordx4 v0, s[12:15], s97 offen offset:1024 sc1 lds\nv_mfma_f32_16x16x32_bf16 v[44:47], a[56:59], v[8:11], v[44:47]\n" \
"ds_read_b128 v[16:19], v4 offset:15360\nv_mfma_f32_16x16x32_bf16 v[64:67], a[56:59], v[12:15], v[64:67]\nds_read_b128 v[32:35], v4 offset:48128\nv_mfma_f32_16x16x32_bf16 v[48:51], a[152:155], v[8:11], v[48:51]\n" \
"v_mfma_f32_16x16x32_bf16 v[20:23], a[152:155], v[12:15], v[20:23]\nbuffer_load_dwordx4 v0, s[12:15], s97 offen offset:2048 sc1 lds\nv_mfma_f32_16x16x32_bf16 v[56:59], a[248:251], v[8:11], v[56:59]\nv_mfma_f32_16x16x32_bf16 v[24:27], a[248:251], v[12:15], v[24:27]\n" \
"v_mfma_f32_16x16x32_bf16 v[60:63], v[200:203], v[8:11], v[60:63]\ns_waitcnt lgkmcnt(1)\nv_mfma_f32_16x16x32_bf16 v[28:31], v[200:203], v[12:15], v[28:31]\ns_waitcnt lgkmcnt(0)\n" \
"buffer_load_dwordx4 v0, s[12:15], s97 offen offset:3072 sc1 lds\nv_mfma_f32_16x16x32_bf16 v[44:47], a[60:63], v[16:19], v[44:47]\nds_read_b128 v[8:11], v4 offset:16384\nv_mfma_f32_16x16x32_bf16 v[64:67], a[60:63], v[32:35], v[64:67]\n" \
"ds_read_b128 v[12:15], v4 offset:49152\nv_mfma_f32_16x16x32_bf16 v[48:51], a[156:159], v[16:19], v[48:51]\nv_mfma_f32_16x16x32_bf16 v[20:23], a[156:159], v[32:35], v[20:23]\nv_mfma_f32_16x16x32_bf16 v[56:59], a[252:255], v[16:19], v[56:59]\n" \
"v_mfma_f32_16x16x32_bf16 v[24:27], a[252:255], v[32:35], v[24:27]\nv_mfma_f32_16x16x32_bf16 v[60:63], v[204:207], v[16:19], v[60:63]\ns_waitcnt lgkmcnt(1)\nv_mfma_f32_16x16x32_bf16 v[28:31], v[204:207], v[32:35], v[28:31]\n" \
"s_waitcnt lgkmcnt(0)\nds_read_b128 v[16:19], v4 offset:17408\nv_mfma_f32_16x16x32_bf16 v[44:47], a[64:67], v[8:11], v[44:47]\nds_read_b128 v[32:35], v4 offset:50176\n" \
"v_mfma_f32_16x16x32_bf16 v[64:67], a[64:67], v[12:15], v[64:67]\nv_mfma_f32_16x16x32_bf16 v[48:51], a[160:163], v[8:11], v[48:51]\nv_mfma_f32_16x16x32_bf16 v[20:23], a[160:163], v[12:15], v[20:23]\nv_mfma_f32_16x16x32_bf16 v[56:59], v[112:115], v[8:11], v[56:59]\n" \
"v_mfma_f32_16x16x32_bf16 v[24:27], v[112:115], v[12:15], v[24:27]\nv_mfma_f32_16x16x32_bf16 v[60:63], v[208:211], v[8:11], v[60:63]\ns_waitcnt lgkmcnt(0)\nv_mfma_f32_16x16x32_bf16 v[28:31], v[208:211], v[12:15], v[28:31]\n" \
"ds_read_b128 v[8:11], v4 offset:18432\nv_mfma_f32_16x16x32_bf16 v[44:47], a[68:71], v[16:19], v[44:47]\nds_read_b128 v[12:15], v4 offset:51200\nv_mfma_f32_16x16x32_bf16 v[64:67], a[68:71], v[32:35], v[64:67]\n" \
"v_mfma_f32_16x16x32_bf16 v[48:51], a[164:167], v[16:19], v[48:51]\nv_mfma_f32_16x16x32_bf16 v[20:23], a[164:167], v[32:35], v[20:23]\ns_lshr_b32 s99, s20, 1\ns_add_u32 s20, s20, 1\n" \
"v_readlane_b32 s99, v3, s99\nv_mfma_f32_16x16x32_bf16 v[56:59], v[116:119], v[16:19], v[56:59]\nv_mfma_f32_16x16x32_bf16 v[24:27], v[116:119], v[32:35], v[24:27]\ns_lshr_b32 s99, s99, s98\n" \
"s_and_b32 s99, s99, 0xff\ns_mul_i32 s99, s99, 0x50000\nv_mfma_f32_16x16x32_bf16 v[60:63], v[212:215], v[16:19], v[60:63]\ns_waitcnt lgkmcnt(0)\n" \
"s_add_u32 s99, s99, s22\ns_add_u32 s98, s99, 0x8000\nv_mfma_f32_16x16x32_bf16 v[28:31], v[212:215], v[32:35], v[28:31]\nds_read_b128 v[16:19], v4 offset:19456\n" \
"v_mfma_f32_16x16x32_bf16 v[44:47], a[72:75], v[8:11], v[44:47]\nds_read_b128 v[32:35], v4 offset:52224\nv_mfma_f32_16x16x32_bf16 v[64:67], a[72:75], v[12:15], v[64:67]\nv_mfma_f32_16x16x32_bf16 v[48:51], a[168:171], v[8:11], v[48:51]\n" \
"v_mfma_f32_16x16x32_bf16 v[20:23], a[168:171], v[12:15], v[20:23]\nv_mfma_f32_16x16x32_bf16 v[56:59], v[120:123], v[8:11], v[56:59]\nv_mfma_f32_16x16x32_bf16 v[24:27], v[120:123], v[12:15], v[24:27]\nv_mfma_f32_16x16x32_bf16 v[60:63], v[216:219], v[8:11], v[60:63]\n" \
"s_waitcnt lgkmcnt(0)\nv_mfma_f32_16x16x32_bf16 v[28:31], v[216:219], v[12:15], v[28:31]\nds_read_b128 v[8:11], v4 offset:20480\nv_mfma_f32_16x16x32_bf16 v[44:47], a[76:79], v[16:19], v[44:47]\n" \
"ds_read_b128 v[12:15], v4 offset:53248\nv_mfma_f32_16x16x32_bf16 v[64:67], a[76:79], v[32:35], v[64:67]\nv_mfma_f32_16x16x32_bf16 v[48:51], a[172:175], v[16:19], v[48:51]\nv_mfma_f32_16x16x32_bf16 v[20:23], a[172:175], v[32:35], v[20:23]\n" \
"v_mfma_f32_16x16x32_bf16 v[56:59], v[124:127], v[16:19], v[56:59]\nv_mfma_f32_16x16x32_bf16 v[24:27], v[124:127], v[32:35], v[24:27]\nv_mfma_f32_16x16x32_bf16 v[60:63], v[220:223], v[16:19], v[60:63]\ns_waitcnt lgkmcnt(0)\n" \
"v_mfma_f32_16x16x32_bf16 v[28:31], v[220:223], v[32:35], v[28:31]\nds_read_b128 v[36:39], v4 offset:21504\nv_mfma_f32_16x16x32_bf16 v[44:47], a[80:83], v[8:11], v[44:47]\nds_read_b128 v[32:35], v4 offset:54272\n" \
"v_mfma_f32_16x16x32_bf16 v[64:67], a[80:83], v[12:15], v[64:67]\nv_mfma_f32_16x16x32_bf16 v[48:51], a[176:179], v[8:11], v[48:51]\nv_mfma_f32_16x16x32_bf16 v[20:23], a[176:179], v[12:15], v[20:23]\nv_mfma_f32_16x16x32_bf16 v[56:59], v[128:131], v[8:11], v[56:59]\n" \
"v_mfma_f32_16x16x32_bf16 v[24:27], v[128:131], v[12:15], v[24:27]\nv_mfma_f32_16x16x32_bf16 v[60:63], v[224:227], v[8:11], v[60:63]\ns_waitcnt lgkmcnt(0)\nv_mfma_f32_16x16x32_bf16 v[28:31], v[224:227], v[12:15], v[28:31]\n" \
"ds_read_b128 v[40:43], v4 offset:22528\nv_mfma_f32_16x16x32_bf16 v[44:47], a[84:87], v[36:39], v[44:47]\nds_read_b128 v[16:19], v4 offset:55296\nv_xor_b32 v4, 0x10000, v4\n" \
"v_mfma_f32_16x16x32_bf16 v[64:67], a[84:87], v[32:35], v[64:67]\nv_mfma_f32_16x16x32_bf16 v[48:51], a[180:183], v[36:39], v[48:51]\nv_mfma_f32_16x16x32_bf16 v[20:23], a[180:183], v[32:35], v[20:23]\nv_mfma_f32_16x16x32_bf16 v[56:59], v[132:135], v[36:39], v[56:59]\n" \
"v_mfma_f32_16x16x32_bf16 v[24:27], v[132:135], v[32:35], v[24:27]\nv_mfma_f32_16x16x32_bf16 v[60:63], v[228:231], v[36:39], v[60:63]\ns_waitcnt lgkmcnt(0)\nv_mfma_f32_16x16x32_bf16 v[28:31], v[228:231], v[32:35], v[28:31]\n" \
"s_waitcnt vmcnt(0)\ns_barrier\nv_mfma_f32_16x16x32_bf16 v[44:47], a[88:91], v[40:43], v[44:47]\nds_read_b128 v[8:11], v4 offset:0\n" \
"v_mfma_f32_16x16x32_bf16 v[48:51], a[184:187], v[40:43], v[48:51]\nds_read_b128 v[12:15], v4 offset:32768\nv_mfma_f32_16x16x32_bf16 v[56:59], v[136:139], v[40:43], v[56:59]\nv_cvt_pk_bf16_f32 v36, v44, v45\n" \
"v_cvt_pk_bf16_f32 v37, v46, v47\nv_mfma_f32_16x16x32_bf16 v[60:63], v[232:235], v[40:43], v[60:63]\nv_cvt_pk_bf16_f32 v38, v48, v49\nv_cvt_pk_bf16_f32 v39, v50, v51\n" \
"v_mfma_f32_16x16x32_bf16 v[64:67], a[88:91], v[16:19], v[64:67]\nv_cvt_pk_bf16_f32 v32, v56, v57\nv_cvt_pk_bf16_f32 v33, v58, v59\nv_mfma_f32_16x16x32_bf16 v[20:23], a[184:187], v[16:19], v[20:23]\n" \
"buffer_store_dwordx4 v[36:39], v6, s[8:11], s99 offen offset:0 sc1\nv_cvt_pk_bf16_f32 v34, v60, v61\nv_cvt_pk_bf16_f32 v35, v62, v63\nv_mfma_f32_16x16x32_bf16 v[24:27], v[136:139], v[16:19], v[24:27]\n" \
"s_waitcnt lgkmcnt(1)\nv_cvt_pk_bf16_f32 v36, v64, v65\nv_cvt_pk_bf16_f32 v37, v66, v67\nv_mfma_f32_16x16x32_bf16 v[28:31], v[232:235], v[16:19], v[28:31]\n" \
"ds_read_b128 v[40:43], v4 offset:1024\nv_cvt_pk_bf16_f32 v38, v20, v21\nv_mfma_f32_16x16x32_bf16 v[44:47], a[0:3], v[8:11], 0\nds_read_b128 v[16:19], v4 offset:33792\n" \
"v_cvt_pk_bf16_f32 v39, v22, v23\nv_mfma_f32_16x16x32_bf16 v[48:51], a[96:99], v[8:11], 0\nbuffer_store_dwordx4 v[32:35], v6, s[8:11], s99 offen offset:512 sc1\nv_cvt_pk_bf16_f32 v52, v24, v25\n" \
"v_cvt_pk_bf16_f32 v53, v26, v27\nv_cvt_pk_bf16_f32 v54, v28, v29\nv_mfma_f32_16x16x32_bf16 v[56:59], a[192:195], v[8:11], 0\ns_waitcnt lgkmcnt(2)\n" \
"v_cvt_pk_bf16_f32 v55, v30, v31\nv_mfma_f32_16x16x32_bf16 v[60:63], v[144:147], v[8:11], 0\nv_mfma_f32_16x16x32_bf16 v[64:67], a[0:3], v[12:15], 0\nv_mfma_f32_16x16x32_bf16 v[20:23], a[96:99], v[12:15], 0\n" \
"buffer_store_dwordx4 v[36:39], v6, s[8:11], s98 offen offset:0 sc1\nv_mfma_f32_16x16x32_bf16 v[24:27], a[192:195], v[12:15], 0\ns_waitcnt lgkmcnt(0)\nv_mfma_f32_16x16x32_bf16 v[28:31], v[144:147], v[12:15], 0\n" \
"ds_read_b128 v[8:11], v4 offset:2048\nv_mfma_f32_16x16x32_bf16 v[44:47], a[4:7], v[40:43], v[44:47]\nds_read_b128 v[12:15], v4 offset:34816\nv_mfma_f32_16x16x32_bf16 v[64:67], a[4:7], v[16:19], v[64:67]\n" \
"buffer_store_dwordx4 v[52:55], v6, s[8:11], s98 offen offset:512 sc1\nv_mfma_f32_16x16x32_bf16 v[48:51], a[100:103], v[40:43], v[48:51]\nv_mfma_f32_16x16x32_bf16 v[20:23], a[100:103], v[16:19], v[20:23]\nv_mfma_f32_16x16x32_bf16 v[56:59], a[196:199], v[40:43], v[56:59]\n" \
"v_mfma_f32_16x16x32_bf16 v[24:27], a[196:199], v[16:19], v[24:27]\nv_mfma_f32_16x16x32_bf16 v[60:63], v[148:151], v[40:43], v[60:63]\ns_cmp_eq_u32 s14, 0\nv_mfma_f32_16x16x32_bf16 v[28:31], v[148:151], v[16:19], v[28:31]\n" \
"s_cbranch_scc0 1b\n4:\nbuffer_load_dword v68, v1, s[8:11], 0 offen sc1\nv_mov_b32 v69, s20\n" \
"s_add_u32 s40, s20, 1\ns_waitcnt vmcnt(0)\nv_cmp_gt_i32 vcc, s40, v68\ns_cbranch_vccz 8f\n" \
"buffer_store_dword v69, v2, s[8:11], 0 offen sc1\ns_mov_b32 s25, 0x40000000\n7:\ns_add_u32 s26, s26, 1\n" \
"s_cmp_lt_u32 s26, 0x40000\ns_cbranch_scc0 9f\ns_sleep 8\nbuffer_load_dword v68, v1, s[8:11], 0 offen sc1\n" \
"s_waitcnt vmcnt(0)\nv_cmp_gt_i32 vcc, s40, v68\ns_cbranch_vccnz 7b\n8:\n" \
"s_cmp_ge_u32 s20, s28\ns_cbranch_scc1 10f\ns_lshr_b32 s40, s20, 1\nv_readlane_b32 s40, v3, s40\n" \
"s_and_b32 s41, s20, 1\ns_lshl_b32 s41, s41, 3\ns_lshr_b32 s40, s40, s41\ns_and_b32 s40, s40, 0xff\n" \
"s_mul_i32 s40, s40, 0x38000\ns_add_u32 s40, s40, s21\ns_xor_b32 s41, s23, 0x10000\ns_add_u32 m0, s41, 0\n" \
"s_add_u32 s40, s40, 0\nbuffer_load_dwordx4 v0, s[8:11], s40 offen offset:0 sc1 lds\nbuffer_load_dwordx4 v0, s[8:11], s40 offen offset:1024 sc1 lds\nbuffer_load_dwordx4 v0, s[8:11], s40 offen offset:2048 sc1 lds\n" \
"buffer_load_dwordx4 v0, s[8:11], s40 offen offset:3072 sc1 lds\ns_add_u32 m0, s41, 0x1000\ns_add_u32 s40, s40, 0x1000\nbuffer_load_dwordx4 v0, s[8:11], s40 offen offset:0 sc1 lds\n" \
"buffer_load_dwordx4 v0, s[8:11], s40 offen offset:1024 sc1 lds\nbuffer_load_dwordx4 v0, s[8:11], s40 offen offset:2048 sc1 lds\nbuffer_load_dwordx4 v0, s[8:11], s40 offen offset:3072 sc1 lds\ns_add_u32 m0, s41, 0x2000\n" \
"s_add_u32 s40, s40, 0x1000\nbuffer_load_dwordx4 v0, s[8:11], s40 offen offset:0 sc1 lds\nbuffer_load_dwordx4 v0, s[8:11], s40 offen offset:1024 sc1 lds\nbuffer_load_dwordx4 v0, s[8:11], s40 offen offset:2048 sc1 lds\n" \
"buffer_load_dwordx4 v0, s[8:11], s40 offen offset:3072 sc1 lds\nbuffer_store_dword v69, v2, s[8:11], s25 offen sc1\ns_mov_b32 s25, 0x40000000\ns_waitcnt vmcnt(1) lgkmcnt(0)\n" \
"s_barrier\nds_read_b128 v[68:71], v4 offset:0\nds_read_b128 v[72:75], v4 offset:32768\nds_read_b128 v[76:79], v4 offset:1024\n" \
"ds_read_b128 v[80:83], v4 offset:33792\nds_read_b128 v[8:11], v4 offset:2048\nds_read_b128 v[12:15], v4 offset:34816\ns_waitcnt lgkmcnt(0)\n" \
"v_mfma_f32_16x16x32_bf16 v[44:47], a[0:3], v[68:71], 0\nv_mfma_f32_16x16x32_bf16 v[48:51], a[96:99], v[68:71], 0\nv_mfma_f32_16x16x32_bf16 v[56:59], a[192:195], v[68:71], 0\nv_mfma_f32_16x16x32_bf16 v[60:63], v[144:147], v[68:71], 0\n" \
"v_mfma_f32_16x16x32_bf16 v[64:67], a[0:3], v[72:75], 0\nv_mfma_f32_16x16x32_bf16 v[20:23], a[96:99], v[72:75], 0\nv_mfma_f32_16x16x32_bf16 v[24:27], a[192:195], v[72:75], 0\nv_mfma_f32_16x16x32_bf16 v[28:31], v[144:147], v[72:75], 0\n" \
"v_mfma_f32_16x16x32_bf16 v[44:47], a[4:7], v[76:79], v[44:47]\nv_mfma_f32_16x16x32_bf16 v[48:51], a[100:103], v[76:79], v[48:51]\nv_mfma_f32_16x16x32_bf16 v[56:59], a[196:199], v[76:79], v[56:59]\nv_mfma_f32_16x16x32_bf16 v[60:63], v[148:151], v[76:79], v[60:63]\n" \
"v_mfma_f32_16x16x32_bf16 v[64:67], a[4:7], v[80:83], v[64:67]\nv_mfma_f32_16x16x32_bf16 v[20:23], a[100:103], v[80:83], v[20:23]\nv_mfma_f32_16x16x32_bf16 v[24:27], a[196:199], v[80:83], v[24:27]\nv_mfma_f32_16x16x32_bf16 v[28:31], v[148:151], v[80:83], v[28:31]\n" \
"s_branch 1b\n10:\ns_lshr_b32 s42, s20, 1\nv_readlane_b32 s42, v3, s42\n" \
"s_and_b32 s41, s20, 1\ns_lshl_b32 s41, s41, 3\ns_lshr_b32 s42, s42, s41\ns_and_b32 s42, s42, 0xff\n" \
"s_mul_i32 s43, s42, 0x50000\ns_add_u32 s43, s43, s30\ns_mul_i32 s42, s42, 0x38000\ns_add_u32 s42, s42, s29\n" \
"s_and_b32 s41, s23, 0x10000\ns_xor_b32 s41, s41, 0x10000\ns_sub_u32 s40, s24, 0x27000\ns_mul_i32 s40, s40, 24\n" \
"s_add_u32 s41, s41, s40\ns_add_u32 m0, s41, 0\ns_add_u32 s42, s42, 0\nbuffer_load_dwordx4 v0, s[8:11], s42 offen offset:0 sc1 lds\n" \
"buffer_load_dwordx4 v0, s[8:11], s42 offen offset:1024 sc1 lds\nbuffer_load_dwordx4 v0, s[8:11], s42 offen offset:2048 sc1 lds\nbuffer_load_dwordx4 v0, s[8:11], s42 offen offset:3072 sc1 lds\ns_add_u32 m0, s41, 0x1000\n" \
"s_add_u32 s42, s42, 0x1000\nbuffer_load_dwordx4 v0, s[8:11], s42 offen offset:0 sc1 lds\nbuffer_load_dwordx4 v0, s[8:11], s42 offen offset:1024 sc1 lds\nbuffer_store_dword v69, v2, s[8:11], s25 offen sc1\n" \
"s_mov_b32 s25, 0x40000000\ns_add_u32 s20, s20, 1\ns_waitcnt vmcnt(1)\ns_barrier\n" \
"v_mov_b32 v69, s20\nds_read_b128 v[8:11], v4 offset:0\nds_read_b128 v[12:15], v4 offset:1024\nds_read_b128 v[16:19], v4 offset:2048\n" \
"ds_read_b128 v[20:23], v4 offset:3072\nds_read_b128 v[24:27], v4 offset:4096\ns_waitcnt lgkmcnt(4)\nv_mfma_f32_16x16x32_bf16 v[28:31], a[0:3], v[8:11], 0\n" \
"v_mfma_f32_16x16x32_bf16 v[32:35], a[96:99], v[8:11], 0\nv_mfma_f32_16x16x32_bf16 v[36:39], a[192:195], v[8:11], 0\nv_mfma_f32_16x16x32_bf16 v[40:43], v[144:147], v[8:11], 0\nds_read_b128 v[8:11], v4 offset:5120\n" \
"s_waitcnt lgkmcnt(4)\nv_mfma_f32_16x16x32_bf16 v[28:31], a[4:7], v[12:15], v[28:31]\nv_mfma_f32_16x16x32_bf16 v[32:35], a[100:103], v[12:15], v[32:35]\nv_mfma_f32_16x16x32_bf16 v[36:39], a[196:199], v[12:15], v[36:39]\n" \
"v_mfma_f32_16x16x32_bf16 v[40:43], v[148:151], v[12:15], v[40:43]\nds_read_b128 v[12:15], v4 offset:6144\ns_waitcnt lgkmcnt(4)\nv_mfma_f32_16x16x32_bf16 v[28:31], a[8:11], v[16:19], v[28:31]\n" \
"v_mfma_f32_16x16x32_bf16 v[32:35], a[104:107], v[16:19], v[32:35]\nv_mfma_f32_16x16x32_bf16 v[36:39], a[200:203], v[16:19], v[36:39]\nv_mfma_f32_16x16x32_bf16 v[40:43], v[152:155], v[16:19], v[40:43]\nds_read_b128 v[16:19], v4 offset:7168\n" \
"s_waitcnt lgkmcnt(4)\nv_mfma_f32_16x16x32_bf16 v[28:31], a[12:15], v[20:23], v[28:31]\nv_mfma_f32_16x16x32_bf16 v[32:35], a[108:111], v[20:23], v[32:35]\nv_mfma_f32_16x16x32_bf16 v[36:39], a[204:207], v[20:23], v[36:39]\n" \
"v_mfma_f32_16x16x32_bf16 v[40:43], v[156:159], v[20:23], v[40:43]\nds_read_b128 v[20:23], v4 offset:8192\ns_waitcnt lgkmcnt(4)\nv_mfma_f32_16x16x32_bf16 v[28:31], a[16:19], v[24:27], v[28:31]\n" \
"v_mfma_f32_16x16x32_bf16 v[32:35], a[112:115], v[24:27], v[32:35]\nv_mfma_f32_16x16x32_bf16 v[36:39], a[208:211], v[24:27], v[36:39]\nv_mfma_f32_16x16x32_bf16 v[40:43], v[160:163], v[24:27], v[40:43]\nds_read_b128 v[24:27], v4 offset:9216\n" \
"s_waitcnt lgkmcnt(4)\nv_mfma_f32_16x16x32_bf16 v[28:31], a[20:23], v[8:11], v[28:31]\nv_mfma_f32_16x16x32_bf16 v[32:35], a[116:119], v[8:11], v[32:35]\nv_mfma_f32_16x16x32_bf16 v[36:39], a[212:215], v[8:11], v[36:39]\n" \
"v_mfma_f32_16x16x32_bf16 v[40:43], v[164:167], v[8:11], v[40:43]\nds_read_b128 v[8:11], v4 offset:10240\ns_waitcnt lgkmcnt(4)\nv_mfma_f32_16x16x32_bf16 v[28:31], a[24:27], v[12:15], v[28:31]\n" \
"v_mfma_f32_16x16x32_bf16 v[32:35], a[120:123], v[12:15], v[32:35]\nv_mfma_f32_16x16x32_bf16 v[36:39], a[216:219], v[12:15], v[36:39]\nv_mfma_f32_16x16x32_bf16 v[40:43], v[168:171], v[12:15], v[40:43]\nds_read_b128 v[12:15], v4 offset:11264\n" \
"s_waitcnt lgkmcnt(4)\nv_mfma_f32_16x16x32_bf16 v[28:31], a[28:31], v[16:19], v[28:31]\nv_mfma_f32_16x16x32_bf16 v[32:35], a[124:127], v[16:19], v[32:35]\nv_mfma_f32_16x16x32_bf16 v[36:39], a[220:223], v[16:19], v[36:39]\n" \
"v_mfma_f32_16x16x32_bf16 v[40:43], v[172:175], v[16:19], v[40:43]\nds_read_b128 v[16:19], v4 offset:12288\ns_waitcnt lgkmcnt(4)\nv_mfma_f32_16x16x32_bf16 v[28:31], a[32:35], v[20:23], v[28:31]\n" \
"v_mfma_f32_16x16x32_bf16 v[32:35], a[128:131], v[20:23], v[32:35]\nv_mfma_f32_16x16x32_bf16 v[36:39], a[224:227], v[20:23], v[36:39]\nv_mfma_f32_16x16x32_bf16 v[40:43], v[176:179], v[20:23], v[40:43]\nds_read_b128 v[20:23], v4 offset:13312\n" \
"s_waitcnt lgkmcnt(4)\nv_mfma_f32_16x16x32_bf16 v[28:31], a[36:39], v[24:27], v[28:31]\nv_mfma_f32_16x16x32_bf16 v[32:35], a[132:135], v[24:27], v[32:35]\nv_mfma_f32_16x16x32_bf16 v[36:39], a[228:231], v[24:27], v[36:39]\n" \
"v_mfma_f32_16x16x32_bf16 v[40:43], v[180:183], v[24:27], v[40:43]\nds_read_b128 v[24:27], v4 offset:14336\ns_waitcnt lgkmcnt(4)\nv_mfma_f32_16x16x32_bf16 v[28:31], a[40:43], v[8:11], v[28:31]\n" \
"v_mfma_f32_16x16x32_bf16 v[32:35], a[136:139], v[8:11], v[32:35]\nv_mfma_f32_16x16x32_bf16 v[36:39], a[232:235], v[8:11], v[36:39]\nv_mfma_f32_16x16x32_bf16 v[40:43], v[184:187], v[8:11], v[40:43]\nds_read_b128 v[8:11], v4 offset:15360\n" \
"s_waitcnt lgkmcnt(4)\nv_mfma_f32_16x16x32_bf16 v[28:31], a[44:47], v[12:15], v[28:31]\nv_mfma_f32_16x16x32_bf16 v[32:35], a[140:143], v[12:15], v[32:35]\nv_mfma_f32_16x16x32_bf16 v[36:39], a[236:239], v[12:15], v[36:39]\n" \
"v_mfma_f32_16x16x32_bf16 v[40:43], v[188:191], v[12:15], v[40:43]\nds_read_b128 v[12:15], v4 offset:16384\ns_waitcnt lgkmcnt(4)\nv_mfma_f32_16x16x32_bf16 v[28:31], a[48:51], v[16:19], v[28:31]\n" \
"v_mfma_f32_16x16x32_bf16 v[32:35], a[144:147], v[16:19], v[32:35]\nv_mfma_f32_16x16x32_bf16 v[36:39], a[240:243], v[16:19], v[36:39]\nv_mfma_f32_16x16x32_bf16 v[40:43], v[192:195], v[16:19], v[40:43]\nds_read_b128 v[16:19], v4 offset:17408\n" \
"s_waitcnt lgkmcnt(4)\nv_mfma_f32_16x16x32_bf16 v[28:31], a[52:55], v[20:23], v[28:31]\nv_mfma_f32_16x16x32_bf16 v[32:35], a[148:151], v[20:23], v[32:35]\nv_mfma_f32_16x16x32_bf16 v[36:39], a[244:247], v[20:23], v[36:39]\n" \
"v_mfma_f32_16x16x32_bf16 v[40:43], v[196:199], v[20:23], v[40:43]\nds_read_b128 v[20:23], v4 offset:18432\ns_waitcnt lgkmcnt(4)\nv_mfma_f32_16x16x32_bf16 v[28:31], a[56:59], v[24:27], v[28:31]\n" \
"v_mfma_f32_16x16x32_bf16 v[32:35], a[152:155], v[24:27], v[32:35]\nv_mfma_f32_16x16x32_bf16 v[36:39], a[248:251], v[24:27], v[36:39]\nv_mfma_f32_16x16x32_bf16 v[40:43], v[200:203], v[24:27], v[40:43]\nds_read_b128 v[24:27], v4 offset:19456\n" \
"s_waitcnt lgkmcnt(4)\nv_mfma_f32_16x16x32_bf16 v[28:31], a[60:63], v[8:11], v[28:31]\nv_mfma_f32_16x16x32_bf16 v[32:35], a[156:159], v[8:11], v[32:35]\nv_mfma_f32_16x16x32_bf16 v[36:39], a[252:255], v[8:11], v[36:39]\n" \
"v_mfma_f32_16x16x32_bf16 v[40:43], v[204:207], v[8:11], v[40:43]\nds_read_b128 v[8:11], v4 offset:20480\ns_waitcnt lgkmcnt(4)\nv_mfma_f32_16x16x32_bf16 v[28:31], a[64:67], v[12:15], v[28:31]\n" \
"v_mfma_f32_16x16x32_bf16 v[32:35], a[160:163], v[12:15], v[32:35]\nv_mfma_f32_16x16x32_bf16 v[36:39], v[112:115], v[12:15], v[36:39]\nv_mfma_f32_16x16x32_bf16 v[40:43], v[208:211], v[12:15], v[40:43]\nds_read_b128 v[12:15], v4 offset:21504\n" \
"s_waitcnt lgkmcnt(4)\nv_mfma_f32_16x16x32_bf16 v[28:31], a[68:71], v[16:19], v[28:31]\nv_mfma_f32_16x16x32_bf16 v[32:35], a[164:167], v[16:19], v[32:35]\nv_mfma_f32_16x16x32_bf16 v[36:39], v[116:119], v[16:19], v[36:39]\n" \
"v_mfma_f32_16x16x32_bf16 v[40:43], v[212:215], v[16:19], v[40:43]\nds_read_b128 v[16:19], v4 offset:22528\ns_waitcnt lgkmcnt(4)\nv_mfma_f32_16x16x32_bf16 v[28:31], a[72:75], v[20:23], v[28:31]\n" \
"v_mfma_f32_16x16x32_bf16 v[32:35], a[168:171], v[20:23], v[32:35]\nv_mfma_f32_16x16x32_bf16 v[36:39], v[120:123], v[20:23], v[36:39]\nv_mfma_f32_16x16x32_bf16 v[40:43], v[216:219], v[20:23], v[40:43]\ns_waitcnt lgkmcnt(3)\n" \
"v_mfma_f32_16x16x32_bf16 v[28:31], a[76:79], v[24:27], v[28:31]\nv_mfma_f32_16x16x32_bf16 v[32:35], a[172:175], v[24:27], v[32:35]\nv_mfma_f32_16x16x32_bf16 v[36:39], v[124:127], v[24:27], v[36:39]\nv_mfma_f32_16x16x32_bf16 v[40:43], v[220:223], v[24:27], v[40:43]\n" \
"s_waitcnt lgkmcnt(2)\nv_mfma_f32_16x16x32_bf16 v[28:31], a[80:83], v[8:11], v[28:31]\nv_mfma_f32_16x16x32_bf16 v[32:35], a[176:179], v[8:11], v[32:35]\nv_mfma_f32_16x16x32_bf16 v[36:39], v[128:131], v[8:11], v[36:39]\n" \
"v_mfma_f32_16x16x32_bf16 v[40:43], v[224:227], v[8:11], v[40:43]\ns_waitcnt lgkmcnt(1)\nv_mfma_f32_16x16x32_bf16 v[28:31], a[84:87], v[12:15], v[28:31]\nv_mfma_f32_16x16x32_bf16 v[32:35], a[180:183], v[12:15], v[32:35]\n" \
"v_mfma_f32_16x16x32_bf16 v[36:39], v[132:135], v[12:15], v[36:39]\nv_mfma_f32_16x16x32_bf16 v[40:43], v[228:231], v[12:15], v[40:43]\ns_waitcnt lgkmcnt(0)\nv_mfma_f32_16x16x32_bf16 v[28:31], a[88:91], v[16:19], v[28:31]\n" \
"v_mfma_f32_16x16x32_bf16 v[32:35], a[184:187], v[16:19], v[32:35]\nv_mfma_f32_16x16x32_bf16 v[36:39], v[136:139], v[16:19], v[36:39]\nv_mfma_f32_16x16x32_bf16 v[40:43], v[232:235], v[16:19], v[40:43]\ns_nop 7\n" \
"s_nop 1\nv_cvt_pk_bf16_f32 v8, v28, v29\nv_cvt_pk_bf16_f32 v9, v30, v31\nv_cvt_pk_bf16_f32 v10, v32, v33\n" \
"v_cvt_pk_bf16_f32 v11, v34, v35\nbuffer_store_dwordx4 v[8:11], v6, s[8:11], s43 offen offset:0 sc1\nv_cvt_pk_bf16_f32 v12, v36, v37\nv_cvt_pk_bf16_f32 v13, v38, v39\n" \
"v_cvt_pk_bf16_f32 v14, v40, v41\nv_cvt_pk_bf16_f32 v15, v42, v43\nbuffer_store_dwordx4 v[12:15], v6, s[8:11], s43 offen offset:512 sc1\ns_waitcnt vmcnt(0)\n" \
"buffer_store_dword v69, v2, s[8:11], 0 offen sc1\ns_cmp_ge_u32 s20, s31\ns_cbranch_scc1 9f\ns_add_u32 s40, s20, 1\n" \
"s_barrier\ns_branch 7b\n9:\ns_waitcnt vmcnt(0)\n" \
"s_endpgm\n"
#define G_LOOP \
"s_branch 4f\n.p2align 6\n1:\ns_waitcnt lgkmcnt(2)\n" \
"v_mfma_f32_16x16x32_bf16 v[8:11], a[104:107], v[36:39], v[8:11]\ns_lshr_b32 s99, s20, 1\ns_and_b32 s98, s20, 1\nv_mov_b32 v110, s20\n" \
"v_mfma_f32_16x16x32_bf16 v[12:15], a[104:107], v[40:43], v[12:15]\ns_add_u32 s20, s20, 1\ns_add_u32 s97, s20, 1\ns_lshr_b32 s96, s97, 1\n" \
"v_mfma_f32_16x16x32_bf16 v[16:19], a[232:235], v[36:39], v[16:19]\nds_read_b128 v[52:55], v7 offset:28672\nv_readlane_b32 s96, v6, s96\nv_mfma_f32_16x16x32_bf16 v[20:23], a[232:235], v[40:43], v[20:23]\n" \
"ds_read_b128 v[56:59], v7 offset:61440\nv_xor_b32 v4, 0x400, v4\nv_mfma_f32_16x16x32_bf16 v[24:27], v[216:219], v[36:39], v[24:27]\ns_and_b32 s97, s97, 1\n" \
"s_lshl_b32 s97, s97, 3\ns_lshr_b32 s96, s96, s97\nv_mfma_f32_16x16x32_bf16 v[28:31], v[216:219], v[40:43], v[28:31]\ns_waitcnt lgkmcnt(2)\n" \
"s_and_b32 s96, s96, 0xff\ns_lshl_b32 s97, s96, 13\nv_mfma_f32_16x16x32_bf16 v[8:11], a[108:111], v[44:47], v[8:11]\ns_lshl_b32 s98, s98, 3\n" \
"v_mov_b32 v32, v111\nv_mov_b32 v33, v111\nv_mfma_f32_16x16x32_bf16 v[12:15], a[108:111], v[48:51], v[12:15]\ns_lshl_b32 s96, s96, 16\n" \
"v_mul_f32 v252, 0xc0135761, v34\nv_mul_f32 v34, v34, v34\nv_mfma_f32_16x16x32_bf16 v[16:19], a[236:239], v[44:47], v[16:19]\nds_read_b128 v[36:39], v7 offset:29696\n" \
"v_readlane_b32 s99, v6, s99\nv_mfma_f32_16x16x32_bf16 v[20:23], a[236:239], v[48:51], v[20:23]\nds_read_b128 v[40:43], v7 offset:62464\nv_mul_f32 v34, v34, v252\n" \
"v_mfma_f32_16x16x32_bf16 v[24:27], v[220:223], v[44:47], v[24:27]\nds_read_b128 v[60:63], v7 offset:30720\nv_mul_f32 v254, v35, v35\nv_mfma_f32_16x16x32_bf16 v[28:31], v[220:223], v[48:51], v[28:31]\n" \
"s_waitcnt lgkmcnt(3)\nv_permlane16_swap_b32 v32, v33\nv_mfma_f32_16x16x32_bf16 v[8:11], a[112:115], v[52:55], v[8:11]\nds_read_b128 v[44:47], v7 offset:63488\n" \
"v_mul_f32 v253, 0x3d372713, v34\nv_mfma_f32_16x16x32_bf16 v[12:15], a[112:115], v[56:59], v[12:15]\ns_lshr_b32 s99, s99, s98\ns_and_b32 s99, s99, 0xff\n" \
"s_mul_i32 s99, s99, 0x38000\nv_mfma_f32_16x16x32_bf16 v[16:19], a[240:243], v[52:55], v[16:19]\nds_read_b128 v[48:51], v7 offset:31744\ns_or_b32 s16, s99, s18\n" \
"v_mfma_f32_16x16x32_bf16 v[20:23], a[240:243], v[56:59], v[20:23]\ns_or_b32 s17, s99, s19\ns_add_u32 s96, s96, s21\nv_mfma_f32_16x16x32_bf16 v[24:27], v[224:227], v[52:55], v[24:27]\n" \
"ds_read_b128 v[64:67], v7 offset:64512\nv_xor_b32 v7, 0x10000, v7\nv_mfma_f32_16x16x32_bf16 v[28:31], v[224:227], v[56:59], v[28:31]\ns_waitcnt lgkmcnt(3)\n" \
"v_mfma_f32_16x16x32_bf16 v[8:11], a[116:119], v[36:39], v[8:11]\nv_mfma_f32_16x16x32_bf16 v[12:15], a[116:119], v[40:43], v[12:15]\nv_mfma_f32_16x16x32_bf16 v[16:19], a[244:247], v[36:39], v[16:19]\ns_waitcnt vmcnt(3) lgkmcnt(0)\n" \
"s_barrier\nv_mfma_f32_16x16x32_bf16 v[20:23], a[244:247], v[40:43], v[20:23]\nds_read_b128 v[52:55], v4 offset:0\nv_mfma_f32_16x16x32_bf16 v[24:27], v[228:231], v[36:39], v[24:27]\n" \
"ds_read_b128 v[56:59], v7 offset:0\nv_mfma_f32_16x16x32_bf16 v[28:31], v[228:231], v[40:43], v[28:31]\nds_read_b128 v[36:39], v7 offset:32768\nv_mfma_f32_16x16x32_bf16 v[8:11], a[120:123], v[60:63], v[8:11]\n" \
"ds_read_b128 v[40:43], v7 offset:1024\nv_mfma_f32_16x16x32_bf16 v[12:15], a[120:123], v[44:47], v[12:15]\ns_waitcnt lgkmcnt(3)\nv_add_f32 v52, v52, v53\n" \
"v_mfma_f32_16x16x32_bf16 v[8:11], a[124:127], v[48:51], v[8:11]\nbuffer_load_dwordx4 v[68:71], v1, s[8:11], s97 offen offset:1024 sc1\nv_mfma_f32_16x16x32_bf16 v[12:15], a[124:127], v[64:67], v[12:15]\nbuffer_load_dwordx4 v[72:75], v1, s[8:11], s97 offen offset:0 sc1\n" \
"v_mfma_f32_16x16x32_bf16 v[16:19], a[248:251], v[60:63], v[16:19]\nds_read_b128 v[76:79], v7 offset:33792\nv_add_f32 v54, v54, v55\nv_mfma_f32_16x16x32_bf16 v[20:23], a[248:251], v[44:47], v[20:23]\n" \
"v_cmp_u_f32 s[98:99], v52, v54\nv_fma_f32 v52, v32, v240, v8\nv_mfma_f32_16x16x32_bf16 v[16:19], a[252:255], v[48:51], v[16:19]\ns_waitcnt vmcnt(2)\n" \
"buffer_store_dword v110, v3, s[8:11], s23 offen offset:0 sc1\nv_mfma_f32_16x16x32_bf16 v[20:23], a[252:255], v[64:67], v[20:23]\ns_mov_b32 s23, 0\nv_fma_f32 v53, v32, v241, v9\n" \
"v_fma_f32 v54, v32, v242, v10\nv_mfma_f32_16x16x32_bf16 v[24:27], v[232:235], v[60:63], v[24:27]\ns_cmp_lt_u32 s20, s26\ns_cselect_b32 s98, s98, -1\n" \
"v_fma_f32 v55, v32, v243, v11\nv_mfma_f32_16x16x32_bf16 v[28:31], v[232:235], v[44:47], v[28:31]\nv_fma_f32 v60, v33, v240, v12\nv_fma_f32 v61, v33, v241, v13\n" \
"v_mfma_f32_16x16x32_bf16 v[24:27], v[236:239], v[48:51], v[24:27]\ns_waitcnt lgkmcnt(0)\nv_fma_f32 v62, v33, v242, v14\nv_fma_f32 v63, v33, v243, v15\n" \
"v_mfma_f32_16x16x32_bf16 v[28:31], v[236:239], v[64:67], v[28:31]\ns_cmp_lg_u64 s[98:99], 0\ns_cbranch_scc1 4f\n5:\n" \
"v_mfma_f32_16x16x32_bf16 v[8:11], a[0:3], v[56:59], 0\ns_xor_b32 m0, s22, 0\nv_fma_f32 v44, v32, v244, v16\nv_fma_f32 v45, v32, v245, v17\n" \
"v_mfma_f32_16x16x32_bf16 v[12:15], a[0:3], v[36:39], 0\nv_fma_f32 v46, v32, v246, v18\nv_fma_f32 v47, v32, v247, v19\nv_mfma_f32_16x16x32_bf16 v[8:11], a[4:7], v[40:43], v[8:11]\n" \
"v_fma_f32 v48, v33, v244, v20\nv_fma_f32 v49, v33, v245, v21\nv_mfma_f32_16x16x32_bf16 v[12:15], a[4:7], v[76:79], v[12:15]\nv_fma_f32 v50, v33, v246, v22\n" \
"v_fma_f32 v51, v33, v247, v23\nv_mfma_f32_16x16x32_bf16 v[16:19], a[128:131], v[56:59], 0\nds_read_b128 v[64:67], v7 offset:2048\nv_fma_f32 v80, v32, v248, v24\n" \
"v_mfma_f32_16x16x32_bf16 v[20:23], a[128:131], v[36:39], 0\nds_read_b128 v[84:87], v7 offset:34816\nv_fma_f32 v81, v32, v249, v25\nv_mfma_f32_16x16x32_bf16 v[16:19], a[132:135], v[40:43], v[16:19]\n" \
"v_fma_f32 v82, v32, v250, v26\nv_fma_f32 v83, v32, v251, v27\nv_mfma_f32_16x16x32_bf16 v[20:23], a[132:135], v[76:79], v[20:23]\nv_fma_f32 v88, v33, v248, v28\n" \
"v_fma_f32 v89, v33, v249, v29\nv_fma_f32 v90, v33, v250, v30\nv_fma_f32 v91, v33, v251, v31\nv_mfma_f32_16x16x32_bf16 v[24:27], v[112:115], v[56:59], 0\n" \
"ds_read_b128 v[92:95], v7 offset:3072\nv_xor_b32 v5, 0x400, v5\nv_mfma_f32_16x16x32_bf16 v[28:31], v[112:115], v[36:39], 0\nds_read_b128 v[56:59], v7 offset:35840\n" \
"v_mfma_f32_16x16x32_bf16 v[24:27], v[116:119], v[40:43], v[24:27]\ns_waitcnt lgkmcnt(3)\nv_permlane16_swap_b32 v52, v60\nv_mfma_f32_16x16x32_bf16 v[28:31], v[116:119], v[76:79], v[28:31]\n" \
"s_waitcnt lgkmcnt(2)\nv_permlane16_swap_b32 v53, v61\nv_mfma_f32_16x16x32_bf16 v[8:11], a[8:11], v[64:67], v[8:11]\nv_permlane16_swap_b32 v54, v62\n" \
"v_mfma_f32_16x16x32_bf16 v[12:15], a[8:11], v[84:87], v[12:15]\nv_permlane16_swap_b32 v55, v63\nv_mfma_f32_16x16x32_bf16 v[16:19], a[136:139], v[64:67], v[16:19]\nds_read_b128 v[36:39], v7 offset:4096\n" \
"v_mul_f32 v40, v52, v52\nv_mfma_f32_16x16x32_bf16 v[20:23], a[136:139], v[84:87], v[20:23]\nds_read_b128 v[76:79], v7 offset:36864\nv_mul_f32 v41, v53, v53\n" \
"v_mfma_f32_16x16x32_bf16 v[24:27], v[120:123], v[64:67], v[24:27]\ns_waitcnt lgkmcnt(2)\nv_mul_f32 v42, v54, v54\nv_mul_f32 v43, v55, v55\n" \
"v_mfma_f32_16x16x32_bf16 v[28:31], v[120:123], v[84:87], v[28:31]\nv_fma_f32 v40, v40, v253, v252\nv_fma_f32 v41, v41, v253, v252\nv_mfma_f32_16x16x32_bf16 v[8:11], a[12:15], v[92:95], v[8:11]\n" \
"v_fma_f32 v42, v42, v253, v252\nv_fma_f32 v43, v43, v253, v252\nv_mfma_f32_16x16x32_bf16 v[12:15], a[12:15], v[56:59], v[12:15]\nv_mul_f32 v40, v40, v52\n" \
"v_mul_f32 v41, v41, v53\nv_mfma_f32_16x16x32_bf16 v[16:19], a[140:143], v[92:95], v[16:19]\nds_read_b128 v[64:67], v7 offset:5120\nv_mul_f32 v42, v42, v54\n" \
"v_mfma_f32_16x16x32_bf16 v[20:23], a[140:143], v[56:59], v[20:23]\nds_read_b128 v[84:87], v7 offset:37888\nv_mul_f32 v43, v43, v55\nv_mfma_f32_16x16x32_bf16 v[24:27], v[124:127], v[92:95], v[24:27]\n" \
"s_waitcnt lgkmcnt(2)\nv_exp_f32 v40, v40\nv_mfma_f32_16x16x32_bf16 v[28:31], v[124:127], v[56:59], v[28:31]\nv_exp_f32 v41, v41\n" \
"v_mfma_f32_16x16x32_bf16 v[8:11], a[16:19], v[36:39], v[8:11]\nv_exp_f32 v42, v42\nv_mfma_f32_16x16x32_bf16 v[12:15], a[16:19], v[76:79], v[12:15]\nv_exp_f32 v43, v43\n" \
"v_mfma_f32_16x16x32_bf16 v[16:19], a[144:147], v[36:39], v[16:19]\nds_read_b128 v[56:59], v7 offset:6144\nv_fma_f32 v40, v40, v254, v254\nv_mfma_f32_16x16x32_bf16 v[20:23], a[144:147], v[76:79], v[20:23]\n" \
"ds_read_b128 v[92:95], v7 offset:38912\nv_fma_f32 v41, v41, v254, v254\nv_mfma_f32_16x16x32_bf16 v[24:27], v[128:131], v[36:39], v[24:27]\ns_waitcnt lgkmcnt(2)\n" \
"v_rcp_f32 v40, v40\nv_mfma_f32_16x16x32_bf16 v[28:31], v[128:131], v[76:79], v[28:31]\nv_rcp_f32 v41, v41\nv_mfma_f32_16x16x32_bf16 v[8:11], a[20:23], v[64:67], v[8:11]\n" \
"v_fma_f32 v42, v42, v254, v254\nv_fma_f32 v43, v43, v254, v254\nv_mfma_f32_16x16x32_bf16 v[12:15], a[20:23], v[84:87], v[12:15]\nv_rcp_f32 v42, v42\n" \
"v_mfma_f32_16x16x32_bf16 v[16:19], a[148:151], v[64:67], v[16:19]\nds_read_b128 v[36:39], v7 offset:7168\nv_mul_f32 v60, v52, v60\nv_mfma_f32_16x16x32_bf16 v[20:23], a[148:151], v[84:87], v[20:23]\n" \
"s_waitcnt vmcnt(1)\nv_max3_u32 v35, v72, v74, v68\nv_mul_f32 v61, v53, v61\nv_mfma_f32_16x16x32_bf16 v[24:27], v[132:135], v[64:67], v[24:27]\n" \
"ds_read_b128 v[76:79], v7 offset:39936\nv_max_u32 v35, v35, v70\nv_mfma_f32_16x16x32_bf16 v[28:31], v[132:135], v[84:87], v[28:31]\ns_waitcnt lgkmcnt(2)\n" \
"v_cmp_eq_u32 s[98:99], -1, v35\nv_mul_f32 v62, v54, v62\nv_mfma_f32_16x16x32_bf16 v[8:11], a[24:27], v[56:59], v[8:11]\nv_rcp_f32 v43, v43\n" \
"v_mfma_f32_16x16x32_bf16 v[12:15], a[24:27], v[92:95], v[12:15]\ns_cmp_eq_u64 s[98:99], 0\ns_cselect_b32 s14, s10, 0\nv_mul_f32 v63, v55, v63\n" \
"v_mfma_f32_16x16x32_bf16 v[16:19], a[152:155], v[56:59], v[16:19]\nbuffer_load_dwordx4 v0, s[12:15], s96 offen offset:0 sc1 lds\nv_mfma_f32_16x16x32_bf16 v[20:23], a[152:155], v[92:95], v[20:23]\nds_read_b128 v[52:55], v7 offset:8192\n" \
"v_mul_f32 v60, v60, v40\nv_mfma_f32_16x16x32_bf16 v[24:27], v[136:139], v[56:59], v[24:27]\nds_read_b128 v[64:67], v7 offset:40960\nv_mul_f32 v61, v61, v41\n" \
"v_mfma_f32_16x16x32_bf16 v[28:31], v[136:139], v[92:95], v[28:31]\ns_waitcnt lgkmcnt(2)\nv_mul_f32 v62, v62, v42\nv_mul_f32 v63, v63, v43\n" \
"v_mfma_f32_16x16x32_bf16 v[8:11], a[28:31], v[36:39], v[8:11]\nv_cvt_pk_bf16_f32 v60, v60, v61\nv_cvt_pk_bf16_f32 v61, v62, v63\nv_mfma_f32_16x16x32_bf16 v[12:15], a[28:31], v[76:79], v[12:15]\n" \
"buffer_load_dwordx4 v0, s[12:15], s96 offen offset:1024 sc1 lds\nv_mfma_f32_16x16x32_bf16 v[16:19], a[156:159], v[36:39], v[16:19]\nds_read_b128 v[40:43], v7 offset:9216\nv_mfma_f32_16x16x32_bf16 v[20:23], a[156:159], v[76:79], v[20:23]\n" \
"ds_read_b128 v[56:59], v7 offset:41984\nv_mfma_f32_16x16x32_bf16 v[24:27], v[140:143], v[36:39], v[24:27]\ns_waitcnt lgkmcnt(2)\nv_permlane16_swap_b32 v44, v48\n" \
"v_mfma_f32_16x16x32_bf16 v[28:31], v[140:143], v[76:79], v[28:31]\nbuffer_load_dwordx4 v0, s[12:15], s96 offen offset:2048 sc1 lds\nv_mfma_f32_16x16x32_bf16 v[8:11], a[32:35], v[52:55], v[8:11]\nv_permlane16_swap_b32 v45, v49\n" \
"v_mfma_f32_16x16x32_bf16 v[12:15], a[32:35], v[64:67], v[12:15]\nv_permlane16_swap_b32 v46, v50\nv_mfma_f32_16x16x32_bf16 v[16:19], a[160:163], v[52:55], v[16:19]\nds_read_b128 v[36:39], v7 offset:10240\n" \
"v_mul_f32 v76, v44, v44\nv_mfma_f32_16x16x32_bf16 v[20:23], a[160:163], v[64:67], v[20:23]\nds_read_b128 v[84:87], v7 offset:43008\nv_mul_f32 v77, v45, v45\n" \
"v_mfma_f32_16x16x32_bf16 v[24:27], v[144:147], v[52:55], v[24:27]\ns_waitcnt lgkmcnt(2)\nbuffer_load_dwordx4 v0, s[12:15], s96 offen offset:3072 sc1 lds\nv_mfma_f32_16x16x32_bf16 v[28:31], v[144:147], v[64:67], v[28:31]\n" \
"s_xor_b32 m0, s22, 0x1000\ns_xor_b32 s96, s96, 0x1000\nv_mul_f32 v78, v46, v46\nv_mfma_f32_16x16x32_bf16 v[8:11], a[36:39], v[40:43], v[8:11]\n" \
"v_permlane16_swap_b32 v47, v51\nv_mfma_f32_16x16x32_bf16 v[12:15], a[36:39], v[56:59], v[12:15]\nv_mul_f32 v79, v47, v47\nv_fma_f32 v76, v76, v253, v252\n" \
"v_mfma_f32_16x16x32_bf16 v[16:19], a[164:167], v[40:43], v[16:19]\nds_read_b128 v[52:55], v7 offset:11264\nv_fma_f32 v77, v77, v253, v252\nv_mfma_f32_16x16x32_bf16 v[20:23], a[164:167], v[56:59], v[20:23]\n" \
"buffer_load_dwordx4 v0, s[12:15], s96 offen offset:0 sc1 lds\nv_mfma_f32_16x16x32_bf16 v[24:27], v[148:151], v[40:43], v[24:27]\nds_read_b128 v[64:67], v7 offset:44032\nv_fma_f32 v78, v78, v253, v252\n" \
"v_mfma_f32_16x16x32_bf16 v[28:31], v[148:151], v[56:59], v[28:31]\ns_waitcnt lgkmcnt(2)\nv_fma_f32 v79, v79, v253, v252\nv_mul_f32 v76, v76, v44\n" \
"v_mfma_f32_16x16x32_bf16 v[8:11], a[40:43], v[36:39], v[8:11]\nv_exp_f32 v76, v76\nv_mfma_f32_16x16x32_bf16 v[12:15], a[40:43], v[84:87], v[12:15]\nv_mul_f32 v77, v77, v45\n" \
"v_mul_f32 v78, v78, v46\nv_mfma_f32_16x16x32_bf16 v[16:19], a[168:171], v[36:39], v[16:19]\nbuffer_load_dwordx4 v0, s[12:15], s96 offen offset:1024 sc1 lds\nv_mfma_f32_16x16x32_bf16 v[20:23], a[168:171], v[84:87], v[20:23]\n" \
"ds_read_b128 v[40:43], v7 offset:12288\nv_mul_f32 v79, v79, v47\nv_mfma_f32_16x16x32_bf16 v[24:27], v[152:155], v[36:39], v[24:27]\nds_read_b128 v[56:59], v7 offset:45056\n" \
"v_fma_f32 v76, v76, v254, v254\nv_mfma_f32_16x16x32_bf16 v[28:31], v[152:155], v[84:87], v[28:31]\ns_waitcnt lgkmcnt(2)\nv_exp_f32 v77, v77\n" \
"v_mfma_f32_16x16x32_bf16 v[8:11], a[44:47], v[52:55], v[8:11]\nv_exp_f32 v78, v78\nv_mfma_f32_16x16x32_bf16 v[12:15], a[44:47], v[64:67], v[12:15]\nbuffer_load_dwordx4 v0, s[12:15], s96 offen offset:2048 sc1 lds\n" \
"v_mfma_f32_16x16x32_bf16 v[16:19], a[172:175], v[52:55], v[16:19]\nds_read_b128 v[36:39], v7 offset:13312\nv_fma_f32 v77, v77, v254, v254\nv_mfma_f32_16x16x32_bf16 v[20:23], a[172:175], v[64:67], v[20:23]\n" \
"ds_read_b128 v[84:87], v7 offset:46080\nv_fma_f32 v78, v78, v254, v254\nv_mfma_f32_16x16x32_bf16 v[24:27], v[156:159], v[52:55], v[24:27]\ns_waitcnt lgkmcnt(2)\n" \
"v_exp_f32 v79, v79\nv_mfma_f32_16x16x32_bf16 v[28:31], v[156:159], v[64:67], v[28:31]\nv_rcp_f32 v76, v76\nv_mfma_f32_16x16x32_bf16 v[8:11], a[48:51], v[40:43], v[8:11]\n" \
"buffer_load_dwordx4 v0, s[12:15], s96 offen offset:3072 sc1 lds\nv_mfma_f32_16x16x32_bf16 v[12:15], a[48:51], v[56:59], v[12:15]\ns_xor_b32 m0, s22, 0x8000\ns_xor_b32 s96, s96, 0x9000\n" \
"v_fma_f32 v79, v79, v254, v254\nv_mfma_f32_16x16x32_bf16 v[16:19], a[176:179], v[40:43], v[16:19]\nds_read_b128 v[52:55], v7 offset:14336\nv_mul_f32 v48, v44, v48\n" \
"v_mfma_f32_16x16x32_bf16 v[20:23], a[176:179], v[56:59], v[20:23]\nds_read_b128 v[64:67], v7 offset:47104\nv_mul_f32 v49, v45, v49\nv_mfma_f32_16x16x32_bf16 v[24:27], v[160:163], v[40:43], v[24:27]\n" \
"s_waitcnt lgkmcnt(2)\nv_rcp_f32 v77, v77\nv_mfma_f32_16x16x32_bf16 v[28:31], v[160:163], v[56:59], v[28:31]\nbuffer_load_dwordx4 v0, s[12:15], s96 offen offset:0 sc1 lds\n" \
"v_mfma_f32_16x16x32_bf16 v[8:11], a[52:55], v[36:39], v[8:11]\nv_rcp_f32 v78, v78\nv_mfma_f32_16x16x32_bf16 v[12:15], a[52:55], v[84:87], v[12:15]\nv_rcp_f32 v79, v79\n" \
"v_mfma_f32_16x16x32_bf16 v[16:19], a[180:183], v[36:39], v[16:19]\nds_read_b128 v[40:43], v7 offset:15360\nv_mul_f32 v50, v46, v50\nv_mfma_f32_16x16x32_bf16 v[20:23], a[180:183], v[84:87], v[20:23]\n" \
"ds_read_b128 v[56:59], v7 offset:48128\nv_mul_f32 v51, v47, v51\nv_mfma_f32_16x16x32_bf16 v[24:27], v[164:167], v[36:39], v[24:27]\ns_waitcnt lgkmcnt(2)\n" \
"buffer_load_dwordx4 v0, s[12:15], s96 offen offset:1024 sc1 lds\nv_mfma_f32_16x16x32_bf16 v[28:31], v[164:167], v[84:87], v[28:31]\nv_mul_f32 v48, v48, v76\nv_mul_f32 v49, v49, v77\n" \
"v_mfma_f32_16x16x32_bf16 v[8:11], a[56:59], v[52:55], v[8:11]\nv_mul_f32 v50, v50, v78\nv_mul_f32 v51, v51, v79\nv_mfma_f32_16x16x32_bf16 v[12:15], a[56:59], v[64:67], v[12:15]\n" \
"v_cvt_pk_bf16_f32 v48, v48, v49\nv_cvt_pk_bf16_f32 v49, v50, v51\nv_mfma_f32_16x16x32_bf16 v[16:19], a[184:187], v[52:55], v[16:19]\nds_read_b128 v[36:39], v7 offset:16384\n" \
"v_mfma_f32_16x16x32_bf16 v[20:23], a[184:187], v[64:67], v[20:23]\nbuffer_load_dwordx4 v0, s[12:15], s96 offen offset:2048 sc1 lds\nv_mfma_f32_16x16x32_bf16 v[24:27], v[168:171], v[52:55], v[24:27]\ns_waitcnt lgkmcnt(1)\n" \
"ds_read_b128 v[44:47], v7 offset:49152\nv_mfma_f32_16x16x32_bf16 v[28:31], v[168:171], v[64:67], v[28:31]\nv_permlane16_swap_b32 v80, v88\nv_mfma_f32_16x16x32_bf16 v[8:11], a[60:63], v[40:43], v[8:11]\n" \
"v_permlane16_swap_b32 v81, v89\nv_mfma_f32_16x16x32_bf16 v[12:15], a[60:63], v[56:59], v[12:15]\nv_permlane16_swap_b32 v82, v90\nv_mfma_f32_16x16x32_bf16 v[16:19], a[188:191], v[40:43], v[16:19]\n" \
"buffer_load_dwordx4 v0, s[12:15], s96 offen offset:3072 sc1 lds\nv_mfma_f32_16x16x32_bf16 v[20:23], a[188:191], v[56:59], v[20:23]\nds_read_b128 v[52:55], v7 offset:17408\ns_xor_b32 m0, s22, 0x9000\n" \
"v_mfma_f32_16x16x32_bf16 v[24:27], v[172:175], v[40:43], v[24:27]\nds_read_b128 v[64:67], v7 offset:50176\ns_xor_b32 s96, s96, 0x1000\nv_mfma_f32_16x16x32_bf16 v[28:31], v[172:175], v[56:59], v[28:31]\n" \
"s_waitcnt lgkmcnt(2)\nv_permlane16_swap_b32 v83, v91\nv_mfma_f32_16x16x32_bf16 v[8:11], a[64:67], v[36:39], v[8:11]\ns_xor_b32 s22, s22, 0x10000\n" \
"v_mul_f32 v40, v80, v80\nv_mul_f32 v41, v81, v81\nv_mfma_f32_16x16x32_bf16 v[12:15], a[64:67], v[44:47], v[12:15]\nbuffer_load_dwordx4 v0, s[12:15], s96 offen offset:0 sc1 lds\n" \
"v_mfma_f32_16x16x32_bf16 v[16:19], a[192:195], v[36:39], v[16:19]\nds_read_b128 v[56:59], v7 offset:18432\nv_mul_f32 v42, v82, v82\nv_mfma_f32_16x16x32_bf16 v[20:23], a[192:195], v[44:47], v[20:23]\n" \
"ds_read_b128 v[76:79], v7 offset:51200\nv_mul_f32 v43, v83, v83\nv_mfma_f32_16x16x32_bf16 v[24:27], v[176:179], v[36:39], v[24:27]\ns_waitcnt lgkmcnt(2)\n" \
"v_fma_f32 v40, v40, v253, v252\nv_fma_f32 v41, v41, v253, v252\nv_mfma_f32_16x16x32_bf16 v[28:31], v[176:179], v[44:47], v[28:31]\nv_fma_f32 v42, v42, v253, v252\n" \
"v_fma_f32 v43, v43, v253, v252\nv_mfma_f32_16x16x32_bf16 v[8:11], a[68:71], v[52:55], v[8:11]\nbuffer_load_dwordx4 v0, s[12:15], s96 offen offset:1024 sc1 lds\nv_mfma_f32_16x16x32_bf16 v[12:15], a[68:71], v[64:67], v[12:15]\n" \
"v_mul_f32 v40, v40, v80\nv_mul_f32 v41, v41, v81\nv_mfma_f32_16x16x32_bf16 v[16:19], a[196:199], v[52:55], v[16:19]\nds_read_b128 v[36:39], v7 offset:19456\n" \
"v_mul_f32 v42, v42, v82\nv_mfma_f32_16x16x32_bf16 v[20:23], a[196:199], v[64:67], v[20:23]\nds_read_b128 v[44:47], v7 offset:52224\nv_mul_f32 v43, v43, v83\n" \
"v_mfma_f32_16x16x32_bf16 v[24:27], v[180:183], v[52:55], v[24:27]\ns_waitcnt lgkmcnt(2)\nv_exp_f32 v40, v40\nv_mfma_f32_16x16x32_bf16 v[28:31], v[180:183], v[64:67], v[28:31]\n" \
"buffer_load_dwordx4 v0, s[12:15], s96 offen offset:2048 sc1 lds\nv_mfma_f32_16x16x32_bf16 v[8:11], a[72:75], v[56:59], v[8:11]\nv_exp_f32 v41, v41\nv_mfma_f32_16x16x32_bf16 v[12:15], a[72:75], v[76:79], v[12:15]\n" \
"v_exp_f32 v42, v42\nv_mfma_f32_16x16x32_bf16 v[16:19], a[200:203], v[56:59], v[16:19]\nds_read_b128 v[52:55], v7 offset:20480\nv_fma_f32 v40, v40, v254, v254\n" \
"v_mfma_f32_16x16x32_bf16 v[20:23], a[200:203], v[76:79], v[20:23]\nds_read_b128 v[64:67], v7 offset:53248\nv_fma_f32 v41, v41, v254, v254\nv_mfma_f32_16x16x32_bf16 v[24:27], v[184:187], v[56:59], v[24:27]\n" \
"s_waitcnt lgkmcnt(2)\nbuffer_load_dwordx4 v0, s[12:15], s96 offen offset:3072 sc1 lds\nv_mfma_f32_16x16x32_bf16 v[28:31], v[184:187], v[76:79], v[28:31]\nv_exp_f32 v43, v43\n" \
"v_mfma_f32_16x16x32_bf16 v[8:11], a[76:79], v[36:39], v[8:11]\nv_rcp_f32 v40, v40\nv_mfma_f32_16x16x32_bf16 v[12:15], a[76:79], v[44:47], v[12:15]\nv_rcp_f32 v41, v41\n" \
"v_mfma_f32_16x16x32_bf16 v[16:19], a[204:207], v[36:39], v[16:19]\nds_read_b128 v[56:59], v7 offset:21504\nv_fma_f32 v42, v42, v254, v254\nv_mfma_f32_16x16x32_bf16 v[20:23], a[204:207], v[44:47], v[20:23]\n" \
"ds_read_b128 v[76:79], v7 offset:54272\nv_fma_f32 v43, v43, v254, v254\nv_mfma_f32_16x16x32_bf16 v[24:27], v[188:191], v[36:39], v[24:27]\ns_waitcnt lgkmcnt(2)\n" \
"v_rcp_f32 v42, v42\nv_mfma_f32_16x16x32_bf16 v[28:31], v[188:191], v[44:47], v[28:31]\nv_rcp_f32 v43, v43\nv_mfma_f32_16x16x32_bf16 v[8:11], a[80:83], v[52:55], v[8:11]\n" \
"buffer_store_dwordx2 v[60:61], v2, s[8:11], s16 offen offset:0 sc1\nv_mul_f32 v88, v80, v88\nv_mfma_f32_16x16x32_bf16 v[12:15], a[80:83], v[64:67], v[12:15]\nv_mul_f32 v89, v81, v89\n" \
"v_mul_f32 v90, v82, v90\nv_mfma_f32_16x16x32_bf16 v[16:19], a[208:211], v[52:55], v[16:19]\nbuffer_store_dwordx2 v[48:49], v2, s[8:11], s17 offen offset:256 sc1\nv_mul_f32 v91, v83, v91\n" \
"v_mfma_f32_16x16x32_bf16 v[20:23], a[208:211], v[64:67], v[20:23]\nv_mul_f32 v88, v88, v40\nv_mul_f32 v89, v89, v41\nv_mfma_f32_16x16x32_bf16 v[24:27], v[192:195], v[52:55], v[24:27]\n" \
"ds_read_b128 v[36:39], v7 offset:22528\nv_mul_f32 v90, v90, v42\nv_mul_f32 v91, v91, v43\nv_mfma_f32_16x16x32_bf16 v[28:31], v[192:195], v[64:67], v[28:31]\n" \
"s_waitcnt lgkmcnt(1)\nds_read_b128 v[40:43], v7 offset:55296\nv_cvt_pk_bf16_f32 v88, v88, v89\nv_mfma_f32_16x16x32_bf16 v[8:11], a[84:87], v[56:59], v[8:11]\n" \
"ds_read_b128 v[44:47], v4 offset:0\nv_cvt_pk_bf16_f32 v89, v90, v91\nv_mfma_f32_16x16x32_bf16 v[12:15], a[84:87], v[76:79], v[12:15]\nbuffer_store_dwordx2 v[88:89], v2, s[8:11], s17 offen offset:512 sc1\n" \
"v_add_f32 v72, v72, v68\nv_mfma_f32_16x16x32_bf16 v[16:19], a[212:215], v[56:59], v[16:19]\nds_read_b128 v[48:51], v7 offset:23552\nv_add_f32 v73, v73, v69\n" \
"v_mfma_f32_16x16x32_bf16 v[20:23], a[212:215], v[76:79], v[20:23]\nds_read_b128 v[52:55], v7 offset:56320\nv_add_f32 v74, v74, v70\nv_mfma_f32_16x16x32_bf16 v[24:27], v[196:199], v[56:59], v[24:27]\n" \
"ds_read_b128 v[60:63], v4 offset:512\nv_add_f32 v75, v75, v71\nv_mfma_f32_16x16x32_bf16 v[28:31], v[196:199], v[76:79], v[28:31]\ns_waitcnt lgkmcnt(4)\n" \
"v_permlane16_swap_b32 v72, v74\nv_mfma_f32_16x16x32_bf16 v[8:11], a[88:91], v[36:39], v[8:11]\nv_permlane16_swap_b32 v73, v75\nv_mfma_f32_16x16x32_bf16 v[12:15], a[88:91], v[40:43], v[12:15]\n" \
"s_waitcnt lgkmcnt(3)\nv_add_f32 v44, v44, v45\nv_add_f32 v46, v46, v47\nv_mfma_f32_16x16x32_bf16 v[16:19], a[216:219], v[36:39], v[16:19]\n" \
"ds_read_b128 v[56:59], v7 offset:24576\nv_add_f32 v44, v44, v46\nv_mfma_f32_16x16x32_bf16 v[20:23], a[216:219], v[40:43], v[20:23]\nds_read_b128 v[64:67], v7 offset:57344\n" \
"v_mul_f32 v44, 0xba800000, v44\nv_mfma_f32_16x16x32_bf16 v[24:27], v[200:203], v[36:39], v[24:27]\ns_waitcnt lgkmcnt(2)\nv_add_f32 v72, v72, v74\n" \
"v_add_f32 v73, v73, v75\nv_mfma_f32_16x16x32_bf16 v[28:31], v[200:203], v[40:43], v[28:31]\nv_add_f32 v60, v60, v61\nv_add_f32 v62, v62, v63\n" \
"v_mfma_f32_16x16x32_bf16 v[8:11], a[92:95], v[48:51], v[8:11]\nv_add_f32 v60, v60, v62\nv_mul_f32 v61, v44, -v44\nv_mfma_f32_16x16x32_bf16 v[12:15], a[92:95], v[52:55], v[12:15]\n" \
"v_fmamk_f32 v35, v60, 0x3a800000, v61\nv_cvt_pk_bf16_f32 v35, v35, v35\nv_mfma_f32_16x16x32_bf16 v[16:19], a[220:223], v[48:51], v[16:19]\nds_read_b128 v[60:63], v7 offset:25600\n" \
"v_lshlrev_b32 v35, 16, v35\nv_mfma_f32_16x16x32_bf16 v[20:23], a[220:223], v[52:55], v[20:23]\nds_read_b128 v[68:71], v7 offset:58368\nv_max_f32 v35, 0, v35\n" \
"v_mfma_f32_16x16x32_bf16 v[24:27], v[204:207], v[48:51], v[24:27]\ns_waitcnt lgkmcnt(2)\nv_add_f32 v35, 0x3727c5ac, v35\nv_cvt_pk_bf16_f32 v111, v35, v44\n" \
"v_mfma_f32_16x16x32_bf16 v[28:31], v[204:207], v[52:55], v[28:31]\nv_permlane32_swap_b32 v72, v73\nv_mfma_f32_16x16x32_bf16 v[8:11], a[96:99], v[56:59], v[8:11]\nv_lshlrev_b32 v35, 16, v111\n" \
"v_add_f32 v72, v72, v73\nv_mfma_f32_16x16x32_bf16 v[12:15], a[96:99], v[64:67], v[12:15]\nv_sqrt_f32 v35, v35\nv_mfma_f32_16x16x32_bf16 v[16:19], a[224:227], v[56:59], v[16:19]\n" \
"ds_read_b128 v[36:39], v7 offset:26624\nv_cvt_pk_bf16_f32 v35, v35, v35\nv_mfma_f32_16x16x32_bf16 v[20:23], a[224:227], v[64:67], v[20:23]\nds_read_b128 v[40:43], v7 offset:59392\n" \
"v_lshlrev_b32 v35, 16, v35\nv_mfma_f32_16x16x32_bf16 v[24:27], v[208:211], v[56:59], v[24:27]\ns_waitcnt lgkmcnt(2)\nv_rcp_f32 v34, v35\n" \
"v_mfma_f32_16x16x32_bf16 v[28:31], v[208:211], v[64:67], v[28:31]\nds_write_b32 v5, v72 offset:0\nv_mfma_f32_16x16x32_bf16 v[8:11], a[100:103], v[60:63], v[8:11]\nv_and_b32 v111, 0xffff0000, v111\n" \
"v_mfma_f32_16x16x32_bf16 v[12:15], a[100:103], v[68:71], v[12:15]\nv_mfma_f32_16x16x32_bf16 v[16:19], a[228:231], v[60:63], v[16:19]\nds_read_b128 v[44:47], v7 offset:27648\nv_mfma_f32_16x16x32_bf16 v[20:23], a[228:231], v[68:71], v[20:23]\n" \
"ds_read_b128 v[48:51], v7 offset:60416\nv_mfma_f32_16x16x32_bf16 v[24:27], v[212:215], v[60:63], v[24:27]\nv_mfma_f32_16x16x32_bf16 v[28:31], v[212:215], v[68:71], v[28:31]\ns_branch 1b\n" \
"4:\ns_barrier\ns_lshr_b32 s94, s20, 1\nv_readlane_b32 s94, v6, s94\n" \
"s_and_b32 s95, s20, 1\ns_lshl_b32 s95, s95, 3\ns_lshr_b32 s94, s94, s95\ns_and_b32 s94, s94, 0xff\n" \
"s_lshl_b32 s95, s94, 13\nbuffer_load_dwordx4 v[92:95], v1, s[8:11], s95 offen offset:0 sc1\nbuffer_load_dwordx4 v[96:99], v1, s[8:11], s95 offen offset:1024 sc1\nv_fma_f32 v80, v32, v240, v8\n" \
"v_fma_f32 v81, v32, v241, v9\nv_fma_f32 v82, v32, v242, v10\nv_fma_f32 v83, v32, v243, v11\nv_fma_f32 v84, v33, v240, v12\n" \
"v_fma_f32 v85, v33, v241, v13\nv_fma_f32 v86, v33, v242, v14\nv_fma_f32 v87, v33, v243, v15\nv_permlane16_swap_b32 v80, v84\n" \
"v_permlane16_swap_b32 v81, v85\nv_permlane16_swap_b32 v82, v86\nv_permlane16_swap_b32 v83, v87\nv_mul_f32 v88, v80, v80\n" \
"v_mul_f32 v89, v81, v81\nv_mul_f32 v90, v82, v82\nv_mul_f32 v91, v83, v83\nv_fma_f32 v88, v88, v253, v252\n" \
"v_fma_f32 v89, v89, v253, v252\nv_fma_f32 v90, v90, v253, v252\nv_fma_f32 v91, v91, v253, v252\nv_mul_f32 v88, v88, v80\n" \
"v_mul_f32 v89, v89, v81\nv_mul_f32 v90, v90, v82\nv_mul_f32 v91, v91, v83\nv_mul_f32 v84, v80, v84\n" \
"v_mul_f32 v85, v81, v85\nv_mul_f32 v86, v82, v86\nv_mul_f32 v87, v83, v87\nv_exp_f32 v88, v88\n" \
"v_exp_f32 v89, v89\nv_exp_f32 v90, v90\nv_exp_f32 v91, v91\nv_fma_f32 v88, v88, v254, v254\n" \
"v_fma_f32 v89, v89, v254, v254\nv_fma_f32 v90, v90, v254, v254\nv_fma_f32 v91, v91, v254, v254\nv_rcp_f32 v88, v88\n" \
"v_rcp_f32 v89, v89\nv_rcp_f32 v90, v90\nv_rcp_f32 v91, v91\nv_mul_f32 v84, v84, v88\n" \
"v_mul_f32 v85, v85, v89\nv_mul_f32 v86, v86, v90\nv_mul_f32 v87, v87, v91\nv_cvt_pk_bf16_f32 v84, v84, v85\n" \
"v_cvt_pk_bf16_f32 v85, v86, v87\nbuffer_store_dwordx2 v[84:85], v2, s[8:11], s16 offen offset:0 sc1\nv_fma_f32 v80, v32, v244, v16\nv_fma_f32 v81, v32, v245, v17\n" \
"v_fma_f32 v82, v32, v246, v18\nv_fma_f32 v83, v32, v247, v19\nv_fma_f32 v84, v33, v244, v20\nv_fma_f32 v85, v33, v245, v21\n" \
"v_fma_f32 v86, v33, v246, v22\nv_fma_f32 v87, v33, v247, v23\nv_permlane16_swap_b32 v80, v84\nv_permlane16_swap_b32 v81, v85\n" \
"v_permlane16_swap_b32 v82, v86\nv_permlane16_swap_b32 v83, v87\nv_mul_f32 v88, v80, v80\nv_mul_f32 v89, v81, v81\n" \
"v_mul_f32 v90, v82, v82\nv_mul_f32 v91, v83, v83\nv_fma_f32 v88, v88, v253, v252\nv_fma_f32 v89, v89, v253, v252\n" \
"v_fma_f32 v90, v90, v253, v252\nv_fma_f32 v91, v91, v253, v252\nv_mul_f32 v88, v88, v80\nv_mul_f32 v89, v89, v81\n" \
"v_mul_f32 v90, v90, v82\nv_mul_f32 v91, v91, v83\nv_mul_f32 v84, v80, v84\nv_mul_f32 v85, v81, v85\n" \
"v_mul_f32 v86, v82, v86\nv_mul_f32 v87, v83, v87\nv_exp_f32 v88, v88\nv_exp_f32 v89, v89\n" \
"v_exp_f32 v90, v90\nv_exp_f32 v91, v91\nv_fma_f32 v88, v88, v254, v254\nv_fma_f32 v89, v89, v254, v254\n" \
"v_fma_f32 v90, v90, v254, v254\nv_fma_f32 v91, v91, v254, v254\nv_rcp_f32 v88, v88\nv_rcp_f32 v89, v89\n" \
"v_rcp_f32 v90, v90\nv_rcp_f32 v91, v91\nv_mul_f32 v84, v84, v88\nv_mul_f32 v85, v85, v89\n" \
"v_mul_f32 v86, v86, v90\nv_mul_f32 v87, v87, v91\nv_cvt_pk_bf16_f32 v84, v84, v85\nv_cvt_pk_bf16_f32 v85, v86, v87\n" \
"buffer_store_dwordx2 v[84:85], v2, s[8:11], s17 offen offset:256 sc1\nv_fma_f32 v80, v32, v248, v24\nv_fma_f32 v81, v32, v249, v25\nv_fma_f32 v82, v32, v250, v26\n" \
"v_fma_f32 v83, v32, v251, v27\nv_fma_f32 v84, v33, v248, v28\nv_fma_f32 v85, v33, v249, v29\nv_fma_f32 v86, v33, v250, v30\n" \
"v_fma_f32 v87, v33, v251, v31\nv_permlane16_swap_b32 v80, v84\nv_permlane16_swap_b32 v81, v85\nv_permlane16_swap_b32 v82, v86\n" \
"v_permlane16_swap_b32 v83, v87\nv_mul_f32 v88, v80, v80\nv_mul_f32 v89, v81, v81\nv_mul_f32 v90, v82, v82\n" \
"v_mul_f32 v91, v83, v83\nv_fma_f32 v88, v88, v253, v252\nv_fma_f32 v89, v89, v253, v252\nv_fma_f32 v90, v90, v253, v252\n" \
"v_fma_f32 v91, v91, v253, v252\nv_mul_f32 v88, v88, v80\nv_mul_f32 v89, v89, v81\nv_mul_f32 v90, v90, v82\n" \
"v_mul_f32 v91, v91, v83\nv_mul_f32 v84, v80, v84\nv_mul_f32 v85, v81, v85\nv_mul_f32 v86, v82, v86\n" \
"v_mul_f32 v87, v83, v87\nv_exp_f32 v88, v88\nv_exp_f32 v89, v89\nv_exp_f32 v90, v90\n" \
"v_exp_f32 v91, v91\nv_fma_f32 v88, v88, v254, v254\nv_fma_f32 v89, v89, v254, v254\nv_fma_f32 v90, v90, v254, v254\n" \
"v_fma_f32 v91, v91, v254, v254\nv_rcp_f32 v88, v88\nv_rcp_f32 v89, v89\nv_rcp_f32 v90, v90\n" \
"v_rcp_f32 v91, v91\nv_mul_f32 v84, v84, v88\nv_mul_f32 v85, v85, v89\nv_mul_f32 v86, v86, v90\n" \
"v_mul_f32 v87, v87, v91\nv_cvt_pk_bf16_f32 v84, v84, v85\nv_cvt_pk_bf16_f32 v85, v86, v87\nbuffer_store_dwordx2 v[84:85], v2, s[8:11], s17 offen offset:512 sc1\n" \
"s_mov_b32 s16, 0x80000000\ns_mov_b32 s17, 0x80000000\nv_mov_b32 v80, s20\ns_waitcnt vmcnt(3)\n" \
"v_max3_u32 v81, v92, v94, v96\nv_max_u32 v81, v81, v98\nv_cmp_eq_u32 vcc, -1, v81\ns_cbranch_vccz 8f\n" \
"6:\ns_waitcnt vmcnt(0)\nbuffer_store_dword v80, v3, s[8:11], 0 offen offset:0 sc1\ns_mov_b32 s23, 0x40000000\n" \
"7:\ns_add_u32 s24, s24, 1\ns_cmp_lt_u32 s24, 0x40000\ns_cbranch_scc0 9f\n" \
"buffer_load_dwordx4 v[92:95], v1, s[8:11], s95 offen offset:0 sc1\nbuffer_load_dwordx4 v[96:99], v1, s[8:11], s95 offen offset:1024 sc1\ns_waitcnt vmcnt(0)\nv_max3_u32 v81, v92, v94, v96\n" \
"v_max_u32 v81, v81, v98\nv_cmp_eq_u32 vcc, -1, v81\ns_cbranch_vccz 8f\ns_sleep 8\n" \
"s_branch 7b\n8:\ns_cmp_ge_u32 s20, s26\ns_cbranch_scc1 10f\n" \
"s_lshl_b32 s94, s94, 16\ns_add_u32 s94, s94, s21\ns_xor_b32 s95, s22, 0x10000\ns_xor_b32 m0, s95, 0\n" \
"s_xor_b32 s94, s94, 0\nbuffer_load_dwordx4 v0, s[8:11], s94 offen offset:0 sc1 lds\nbuffer_load_dwordx4 v0, s[8:11], s94 offen offset:1024 sc1 lds\nbuffer_load_dwordx4 v0, s[8:11], s94 offen offset:2048 sc1 lds\n" \
"buffer_load_dwordx4 v0, s[8:11], s94 offen offset:3072 sc1 lds\ns_xor_b32 m0, s95, 0x1000\ns_xor_b32 s94, s94, 0x1000\nbuffer_load_dwordx4 v0, s[8:11], s94 offen offset:0 sc1 lds\n" \
"buffer_load_dwordx4 v0, s[8:11], s94 offen offset:1024 sc1 lds\nbuffer_load_dwordx4 v0, s[8:11], s94 offen offset:2048 sc1 lds\nbuffer_load_dwordx4 v0, s[8:11], s94 offen offset:3072 sc1 lds\ns_xor_b32 m0, s95, 0x8000\n" \
"s_xor_b32 s94, s94, 0x9000\nbuffer_load_dwordx4 v0, s[8:11], s94 offen offset:0 sc1 lds\nbuffer_load_dwordx4 v0, s[8:11], s94 offen offset:1024 sc1 lds\nbuffer_load_dwordx4 v0, s[8:11], s94 offen offset:2048 sc1 lds\n" \
"buffer_load_dwordx4 v0, s[8:11], s94 offen offset:3072 sc1 lds\ns_xor_b32 m0, s95, 0x9000\ns_xor_b32 s94, s94, 0x1000\nbuffer_load_dwordx4 v0, s[8:11], s94 offen offset:0 sc1 lds\n" \
"buffer_load_dwordx4 v0, s[8:11], s94 offen offset:1024 sc1 lds\nbuffer_load_dwordx4 v0, s[8:11], s94 offen offset:2048 sc1 lds\nbuffer_load_dwordx4 v0, s[8:11], s94 offen offset:3072 sc1 lds\nv_add_f32 v92, v92, v96\n" \
"v_add_f32 v93, v93, v97\nv_add_f32 v94, v94, v98\nv_add_f32 v95, v95, v99\ns_nop 1\n" \
"v_permlane16_swap_b32 v92, v94\nv_permlane16_swap_b32 v93, v95\nv_add_f32 v92, v92, v94\nv_add_f32 v93, v93, v95\n" \
"s_nop 1\nv_permlane32_swap_b32 v92, v93\nv_add_f32 v92, v92, v93\nds_write_b32 v5, v92\n" \
"s_add_u32 s97, s20, 1\ns_lshr_b32 s96, s97, 1\nv_readlane_b32 s96, v6, s96\ns_and_b32 s97, s97, 1\n" \
"s_lshl_b32 s97, s97, 3\ns_lshr_b32 s96, s96, s97\ns_and_b32 s96, s96, 0xff\ns_lshl_b32 s97, s96, 13\n" \
"s_lshl_b32 s96, s96, 16\ns_add_u32 s96, s96, s21\nbuffer_load_dwordx4 v[72:75], v1, s[8:11], s97 offen offset:0 sc1\nbuffer_load_dwordx4 v[68:71], v1, s[8:11], s97 offen offset:1024 sc1\n" \
"s_waitcnt vmcnt(18)\nbuffer_store_dword v80, v3, s[8:11], s23 offen offset:0 sc1\ns_mov_b32 s23, 0x40000000\ns_waitcnt vmcnt(3) lgkmcnt(0)\n" \
"s_barrier\nds_read_b128 v[56:59], v7 offset:0\nds_read_b128 v[36:39], v7 offset:32768\nds_read_b128 v[40:43], v7 offset:1024\n" \
"ds_read_b128 v[76:79], v7 offset:33792\ns_waitcnt lgkmcnt(0)\ns_branch 5b\n10:\n" \
"s_mul_i32 s16, s94, 0x38000\ns_lshl_b32 s94, s94, 16\ns_and_b32 s95, s21, 0xffff6fff\ns_add_u32 s94, s94, s95\n" \
"s_lshl_b32 s95, s27, 15\ns_add_u32 s94, s94, s95\ns_xor_b32 s95, s22, 0x10000\ns_and_b32 s95, s95, 0xffff6fff\n" \
"s_add_u32 m0, s95, 0\ns_add_u32 s94, s94, 0\nbuffer_load_dwordx4 v0, s[8:11], s94 offen offset:0 sc1 lds\nbuffer_load_dwordx4 v0, s[8:11], s94 offen offset:1024 sc1 lds\n" \
"buffer_load_dwordx4 v0, s[8:11], s94 offen offset:2048 sc1 lds\nbuffer_load_dwordx4 v0, s[8:11], s94 offen offset:3072 sc1 lds\ns_add_u32 m0, s95, 0x1000\ns_add_u32 s94, s94, 0x1000\n" \
"buffer_load_dwordx4 v0, s[8:11], s94 offen offset:0 sc1 lds\nbuffer_load_dwordx4 v0, s[8:11], s94 offen offset:1024 sc1 lds\nbuffer_load_dwordx4 v0, s[8:11], s94 offen offset:2048 sc1 lds\nbuffer_load_dwordx4 v0, s[8:11], s94 offen offset:3072 sc1 lds\n" \
"v_add_f32 v92, v92, v96\nv_add_f32 v93, v93, v97\nv_add_f32 v94, v94, v98\nv_add_f32 v95, v95, v99\n" \
"s_nop 1\nv_permlane16_swap_b32 v92, v94\nv_permlane16_swap_b32 v93, v95\nv_add_f32 v92, v92, v94\n" \
"v_add_f32 v93, v93, v95\ns_nop 1\nv_permlane32_swap_b32 v92, v93\nv_add_f32 v92, v92, v93\n" \
"ds_write_b32 v5, v92\ns_waitcnt vmcnt(8)\nbuffer_store_dword v80, v3, s[8:11], s23 offen offset:0 sc1\ns_mov_b32 s23, 0x40000000\n" \
"s_lshl_b32 s94, s27, 7\nv_and_b32 v4, 0xffffff7f, v4\nv_or_b32 v4, s94, v4\ns_mul_i32 s94, s27, 0x1c000\n" \
"s_add_u32 s94, s94, s16\ns_or_b32 s16, s94, s18\ns_or_b32 s17, s94, s19\nv_lshlrev_b32 v81, 23, v0\n" \
"v_and_b32 v81, 0x80000000, v81\nv_or_b32 v2, v2, v81\ns_add_u32 s20, s20, 1\nv_mov_b32 v80, s20\n" \
"s_waitcnt vmcnt(1) lgkmcnt(0)\ns_barrier\nds_read_b128 v[60:63], v4 offset:0\nds_read_b128 v[64:67], v4 offset:512\n" \
"ds_read_b128 v[36:39], v7 offset:0\nds_read_b128 v[40:43], v7 offset:1024\nds_read_b128 v[44:47], v7 offset:2048\nds_read_b128 v[48:51], v7 offset:3072\n" \
"ds_read_b128 v[52:55], v7 offset:4096\nds_read_b128 v[56:59], v7 offset:5120\ns_waitcnt lgkmcnt(5)\nv_mfma_f32_16x16x32_bf16 v[8:11], a[0:3], v[36:39], 0\n" \
"v_add_f32 v60, v60, v61\nv_mfma_f32_16x16x32_bf16 v[16:19], a[128:131], v[36:39], 0\nv_add_f32 v62, v62, v63\nv_mfma_f32_16x16x32_bf16 v[24:27], v[112:115], v[36:39], 0\n" \
"v_add_f32 v60, v60, v62\nds_read_b128 v[36:39], v7 offset:6144\ns_waitcnt lgkmcnt(5)\nv_mfma_f32_16x16x32_bf16 v[8:11], a[4:7], v[40:43], v[8:11]\n" \
"v_add_f32 v64, v64, v65\nv_mfma_f32_16x16x32_bf16 v[16:19], a[132:135], v[40:43], v[16:19]\nv_add_f32 v66, v66, v67\nv_mfma_f32_16x16x32_bf16 v[24:27], v[116:119], v[40:43], v[24:27]\n" \
"v_add_f32 v64, v64, v66\nds_read_b128 v[40:43], v7 offset:7168\ns_waitcnt lgkmcnt(5)\nv_mfma_f32_16x16x32_bf16 v[8:11], a[8:11], v[44:47], v[8:11]\n" \
"v_mul_f32 v60, 0xba800000, v60\nv_mfma_f32_16x16x32_bf16 v[16:19], a[136:139], v[44:47], v[16:19]\nv_mul_f32 v65, v60, -v60\nv_mfma_f32_16x16x32_bf16 v[24:27], v[120:123], v[44:47], v[24:27]\n" \
"v_fmamk_f32 v68, v64, 0x3a800000, v65\nds_read_b128 v[44:47], v7 offset:8192\ns_waitcnt lgkmcnt(5)\nv_mfma_f32_16x16x32_bf16 v[8:11], a[12:15], v[48:51], v[8:11]\n" \
"v_cvt_pk_bf16_f32 v68, v68, v68\nv_mfma_f32_16x16x32_bf16 v[16:19], a[140:143], v[48:51], v[16:19]\nv_lshlrev_b32 v68, 16, v68\nv_mfma_f32_16x16x32_bf16 v[24:27], v[124:127], v[48:51], v[24:27]\n" \
"v_max_f32 v68, 0, v68\nds_read_b128 v[48:51], v7 offset:9216\ns_waitcnt lgkmcnt(5)\nv_mfma_f32_16x16x32_bf16 v[8:11], a[16:19], v[52:55], v[8:11]\n" \
"v_add_f32 v68, 0x3727c5ac, v68\nv_mfma_f32_16x16x32_bf16 v[16:19], a[144:147], v[52:55], v[16:19]\nv_cvt_pk_bf16_f32 v70, v68, v60\nv_mfma_f32_16x16x32_bf16 v[24:27], v[128:131], v[52:55], v[24:27]\n" \
"v_lshlrev_b32 v68, 16, v70\nds_read_b128 v[52:55], v7 offset:10240\ns_waitcnt lgkmcnt(5)\nv_mfma_f32_16x16x32_bf16 v[8:11], a[20:23], v[56:59], v[8:11]\n" \
"v_sqrt_f32 v68, v68\nv_mfma_f32_16x16x32_bf16 v[16:19], a[148:151], v[56:59], v[16:19]\nv_cvt_pk_bf16_f32 v68, v68, v68\nv_mfma_f32_16x16x32_bf16 v[24:27], v[132:135], v[56:59], v[24:27]\n" \
"v_lshlrev_b32 v68, 16, v68\nds_read_b128 v[56:59], v7 offset:11264\ns_waitcnt lgkmcnt(5)\nv_mfma_f32_16x16x32_bf16 v[8:11], a[24:27], v[36:39], v[8:11]\n" \
"v_and_b32 v70, 0xffff0000, v70\nv_mfma_f32_16x16x32_bf16 v[16:19], a[152:155], v[36:39], v[16:19]\nv_rcp_f32 v69, v68\nv_mfma_f32_16x16x32_bf16 v[24:27], v[136:139], v[36:39], v[24:27]\n" \
"v_mul_f32 v254, v68, v68\nds_read_b128 v[36:39], v7 offset:12288\ns_waitcnt lgkmcnt(5)\nv_mfma_f32_16x16x32_bf16 v[8:11], a[28:31], v[40:43], v[8:11]\n" \
"v_mul_f32 v252, 0xc0135761, v69\nv_mfma_f32_16x16x32_bf16 v[16:19], a[156:159], v[40:43], v[16:19]\nv_mul_f32 v69, v69, v69\nv_mfma_f32_16x16x32_bf16 v[24:27], v[140:143], v[40:43], v[24:27]\n" \
"v_mul_f32 v69, v69, v252\nds_read_b128 v[40:43], v7 offset:13312\ns_waitcnt lgkmcnt(5)\nv_mfma_f32_16x16x32_bf16 v[8:11], a[32:35], v[44:47], v[8:11]\n" \
"v_mul_f32 v253, 0x3d372713, v69\nv_mfma_f32_16x16x32_bf16 v[16:19], a[160:163], v[44:47], v[16:19]\nv_mov_b32 v32, v70\nv_mfma_f32_16x16x32_bf16 v[24:27], v[144:147], v[44:47], v[24:27]\n" \
"v_mov_b32 v33, v70\nds_read_b128 v[44:47], v7 offset:14336\ns_waitcnt lgkmcnt(5)\nv_mfma_f32_16x16x32_bf16 v[8:11], a[36:39], v[48:51], v[8:11]\n" \
"v_permlane16_swap_b32 v32, v33\nv_mfma_f32_16x16x32_bf16 v[16:19], a[164:167], v[48:51], v[16:19]\nv_mfma_f32_16x16x32_bf16 v[24:27], v[148:151], v[48:51], v[24:27]\nds_read_b128 v[48:51], v7 offset:15360\n" \
"s_waitcnt lgkmcnt(5)\nv_mfma_f32_16x16x32_bf16 v[8:11], a[40:43], v[52:55], v[8:11]\nv_mfma_f32_16x16x32_bf16 v[16:19], a[168:171], v[52:55], v[16:19]\nv_mfma_f32_16x16x32_bf16 v[24:27], v[152:155], v[52:55], v[24:27]\n" \
"ds_read_b128 v[52:55], v7 offset:16384\ns_waitcnt lgkmcnt(5)\nv_mfma_f32_16x16x32_bf16 v[8:11], a[44:47], v[56:59], v[8:11]\nv_mfma_f32_16x16x32_bf16 v[16:19], a[172:175], v[56:59], v[16:19]\n" \
"v_mfma_f32_16x16x32_bf16 v[24:27], v[156:159], v[56:59], v[24:27]\nds_read_b128 v[56:59], v7 offset:17408\ns_waitcnt lgkmcnt(5)\nv_mfma_f32_16x16x32_bf16 v[8:11], a[48:51], v[36:39], v[8:11]\n" \
"v_mfma_f32_16x16x32_bf16 v[16:19], a[176:179], v[36:39], v[16:19]\nv_mfma_f32_16x16x32_bf16 v[24:27], v[160:163], v[36:39], v[24:27]\nds_read_b128 v[36:39], v7 offset:18432\ns_waitcnt lgkmcnt(5)\n" \
"v_mfma_f32_16x16x32_bf16 v[8:11], a[52:55], v[40:43], v[8:11]\nv_mfma_f32_16x16x32_bf16 v[16:19], a[180:183], v[40:43], v[16:19]\nv_mfma_f32_16x16x32_bf16 v[24:27], v[164:167], v[40:43], v[24:27]\nds_read_b128 v[40:43], v7 offset:19456\n" \
"s_waitcnt lgkmcnt(5)\nv_mfma_f32_16x16x32_bf16 v[8:11], a[56:59], v[44:47], v[8:11]\nv_mfma_f32_16x16x32_bf16 v[16:19], a[184:187], v[44:47], v[16:19]\nv_mfma_f32_16x16x32_bf16 v[24:27], v[168:171], v[44:47], v[24:27]\n" \
"ds_read_b128 v[44:47], v7 offset:20480\ns_waitcnt lgkmcnt(5)\nv_mfma_f32_16x16x32_bf16 v[8:11], a[60:63], v[48:51], v[8:11]\nv_mfma_f32_16x16x32_bf16 v[16:19], a[188:191], v[48:51], v[16:19]\n" \
"v_mfma_f32_16x16x32_bf16 v[24:27], v[172:175], v[48:51], v[24:27]\nds_read_b128 v[48:51], v7 offset:21504\ns_waitcnt lgkmcnt(5)\nv_mfma_f32_16x16x32_bf16 v[8:11], a[64:67], v[52:55], v[8:11]\n" \
"v_mfma_f32_16x16x32_bf16 v[16:19], a[192:195], v[52:55], v[16:19]\nv_mfma_f32_16x16x32_bf16 v[24:27], v[176:179], v[52:55], v[24:27]\nds_read_b128 v[52:55], v7 offset:22528\ns_waitcnt lgkmcnt(5)\n" \
"v_mfma_f32_16x16x32_bf16 v[8:11], a[68:71], v[56:59], v[8:11]\nv_mfma_f32_16x16x32_bf16 v[16:19], a[196:199], v[56:59], v[16:19]\nv_mfma_f32_16x16x32_bf16 v[24:27], v[180:183], v[56:59], v[24:27]\nds_read_b128 v[56:59], v7 offset:23552\n" \
"s_waitcnt lgkmcnt(5)\nv_mfma_f32_16x16x32_bf16 v[8:11], a[72:75], v[36:39], v[8:11]\nv_mfma_f32_16x16x32_bf16 v[16:19], a[200:203], v[36:39], v[16:19]\nv_mfma_f32_16x16x32_bf16 v[24:27], v[184:187], v[36:39], v[24:27]\n" \
"ds_read_b128 v[36:39], v7 offset:24576\ns_waitcnt lgkmcnt(5)\nv_mfma_f32_16x16x32_bf16 v[8:11], a[76:79], v[40:43], v[8:11]\nv_mfma_f32_16x16x32_bf16 v[16:19], a[204:207], v[40:43], v[16:19]\n" \
"v_mfma_f32_16x16x32_bf16 v[24:27], v[188:191], v[40:43], v[24:27]\nds_read_b128 v[40:43], v7 offset:25600\ns_waitcnt lgkmcnt(5)\nv_mfma_f32_16x16x32_bf16 v[8:11], a[80:83], v[44:47], v[8:11]\n" \
"v_mfma_f32_16x16x32_bf16 v[16:19], a[208:211], v[44:47], v[16:19]\nv_mfma_f32_16x16x32_bf16 v[24:27], v[192:195], v[44:47], v[24:27]\nds_read_b128 v[44:47], v7 offset:26624\ns_waitcnt lgkmcnt(5)\n" \
"v_mfma_f32_16x16x32_bf16 v[8:11], a[84:87], v[48:51], v[8:11]\nv_mfma_f32_16x16x32_bf16 v[16:19], a[212:215], v[48:51], v[16:19]\nv_mfma_f32_16x16x32_bf16 v[24:27], v[196:199], v[48:51], v[24:27]\nds_read_b128 v[48:51], v7 offset:27648\n" \
"s_waitcnt lgkmcnt(5)\nv_mfma_f32_16x16x32_bf16 v[8:11], a[88:91], v[52:55], v[8:11]\nv_mfma_f32_16x16x32_bf16 v[16:19], a[216:219], v[52:55], v[16:19]\nv_mfma_f32_16x16x32_bf16 v[24:27], v[200:203], v[52:55], v[24:27]\n" \
"ds_read_b128 v[52:55], v7 offset:28672\ns_waitcnt lgkmcnt(5)\nv_mfma_f32_16x16x32_bf16 v[8:11], a[92:95], v[56:59], v[8:11]\nv_mfma_f32_16x16x32_bf16 v[16:19], a[220:223], v[56:59], v[16:19]\n" \
"v_mfma_f32_16x16x32_bf16 v[24:27], v[204:207], v[56:59], v[24:27]\nds_read_b128 v[56:59], v7 offset:29696\ns_waitcnt lgkmcnt(5)\nv_mfma_f32_16x16x32_bf16 v[8:11], a[96:99], v[36:39], v[8:11]\n" \
"v_mfma_f32_16x16x32_bf16 v[16:19], a[224:227], v[36:39], v[16:19]\nv_mfma_f32_16x16x32_bf16 v[24:27], v[208:211], v[36:39], v[24:27]\nds_read_b128 v[36:39], v7 offset:30720\ns_waitcnt lgkmcnt(5)\n" \
"v_mfma_f32_16x16x32_bf16 v[8:11], a[100:103], v[40:43], v[8:11]\nv_mfma_f32_16x16x32_bf16 v[16:19], a[228:231], v[40:43], v[16:19]\nv_mfma_f32_16x16x32_bf16 v[24:27], v[212:215], v[40:43], v[24:27]\nds_read_b128 v[40:43], v7 offset:31744\n" \
"s_waitcnt lgkmcnt(5)\nv_mfma_f32_16x16x32_bf16 v[8:11], a[104:107], v[44:47], v[8:11]\nv_mfma_f32_16x16x32_bf16 v[16:19], a[232:235], v[44:47], v[16:19]\nv_mfma_f32_16x16x32_bf16 v[24:27], v[216:219], v[44:47], v[24:27]\n" \
"s_waitcnt lgkmcnt(4)\nv_mfma_f32_16x16x32_bf16 v[8:11], a[108:111], v[48:51], v[8:11]\nv_mfma_f32_16x16x32_bf16 v[16:19], a[236:239], v[48:51], v[16:19]\nv_mfma_f32_16x16x32_bf16 v[24:27], v[220:223], v[48:51], v[24:27]\n" \
"s_waitcnt lgkmcnt(3)\nv_mfma_f32_16x16x32_bf16 v[8:11], a[112:115], v[52:55], v[8:11]\nv_mfma_f32_16x16x32_bf16 v[16:19], a[240:243], v[52:55], v[16:19]\nv_mfma_f32_16x16x32_bf16 v[24:27], v[224:227], v[52:55], v[24:27]\n" \
"s_waitcnt lgkmcnt(2)\nv_mfma_f32_16x16x32_bf16 v[8:11], a[116:119], v[56:59], v[8:11]\nv_mfma_f32_16x16x32_bf16 v[16:19], a[244:247], v[56:59], v[16:19]\nv_mfma_f32_16x16x32_bf16 v[24:27], v[228:231], v[56:59], v[24:27]\n" \
"s_waitcnt lgkmcnt(1)\nv_mfma_f32_16x16x32_bf16 v[8:11], a[120:123], v[36:39], v[8:11]\nv_mfma_f32_16x16x32_bf16 v[16:19], a[248:251], v[36:39], v[16:19]\nv_mfma_f32_16x16x32_bf16 v[24:27], v[232:235], v[36:39], v[24:27]\n" \
"s_waitcnt lgkmcnt(0)\nv_mfma_f32_16x16x32_bf16 v[8:11], a[124:127], v[40:43], v[8:11]\nv_mfma_f32_16x16x32_bf16 v[16:19], a[252:255], v[40:43], v[16:19]\nv_mfma_f32_16x16x32_bf16 v[24:27], v[236:239], v[40:43], v[24:27]\n" \
"s_nop 7\ns_nop 1\nv_fma_f32 v36, v32, v240, v8\nv_fma_f32 v48, v32, v244, v16\n" \
"v_fma_f32 v60, v32, v248, v24\nv_fma_f32 v37, v32, v241, v9\nv_fma_f32 v49, v32, v245, v17\nv_fma_f32 v61, v32, v249, v25\n" \
"v_fma_f32 v38, v32, v242, v10\nv_fma_f32 v50, v32, v246, v18\nv_fma_f32 v62, v32, v250, v26\nv_fma_f32 v39, v32, v243, v11\n" \
"v_fma_f32 v51, v32, v247, v19\nv_fma_f32 v63, v32, v251, v27\nv_fma_f32 v40, v33, v240, v8\nv_fma_f32 v52, v33, v244, v16\n" \
"v_fma_f32 v64, v33, v248, v24\nv_fma_f32 v41, v33, v241, v9\nv_fma_f32 v53, v33, v245, v17\nv_fma_f32 v65, v33, v249, v25\n" \
"v_fma_f32 v42, v33, v242, v10\nv_fma_f32 v54, v33, v246, v18\nv_fma_f32 v66, v33, v250, v26\nv_fma_f32 v43, v33, v243, v11\n" \
"v_fma_f32 v55, v33, v247, v19\nv_fma_f32 v67, v33, v251, v27\nv_permlane16_swap_b32 v36, v40\nv_permlane16_swap_b32 v48, v52\n" \
"v_permlane16_swap_b32 v60, v64\nv_permlane16_swap_b32 v37, v41\nv_permlane16_swap_b32 v49, v53\nv_permlane16_swap_b32 v61, v65\n" \
"v_permlane16_swap_b32 v38, v42\nv_permlane16_swap_b32 v50, v54\nv_permlane16_swap_b32 v62, v66\nv_permlane16_swap_b32 v39, v43\n" \
"v_permlane16_swap_b32 v51, v55\nv_permlane16_swap_b32 v63, v67\nv_mul_f32 v44, v36, v36\nv_mul_f32 v56, v48, v48\n" \
"v_mul_f32 v68, v60, v60\nv_mul_f32 v45, v37, v37\nv_mul_f32 v57, v49, v49\nv_mul_f32 v69, v61, v61\n" \
"v_mul_f32 v46, v38, v38\nv_mul_f32 v58, v50, v50\nv_mul_f32 v70, v62, v62\nv_mul_f32 v47, v39, v39\n" \
"v_mul_f32 v59, v51, v51\nv_mul_f32 v71, v63, v63\nv_fma_f32 v44, v44, v253, v252\nv_fma_f32 v56, v56, v253, v252\n" \
"v_fma_f32 v68, v68, v253, v252\nv_fma_f32 v45, v45, v253, v252\nv_fma_f32 v57, v57, v253, v252\nv_fma_f32 v69, v69, v253, v252\n" \
"v_fma_f32 v46, v46, v253, v252\nv_fma_f32 v58, v58, v253, v252\nv_fma_f32 v70, v70, v253, v252\nv_fma_f32 v47, v47, v253, v252\n" \
"v_fma_f32 v59, v59, v253, v252\nv_fma_f32 v71, v71, v253, v252\nv_mul_f32 v44, v44, v36\nv_mul_f32 v56, v56, v48\n" \
"v_mul_f32 v68, v68, v60\nv_mul_f32 v45, v45, v37\nv_mul_f32 v57, v57, v49\nv_mul_f32 v69, v69, v61\n" \
"v_mul_f32 v46, v46, v38\nv_mul_f32 v58, v58, v50\nv_mul_f32 v70, v70, v62\nv_mul_f32 v47, v47, v39\n" \
"v_mul_f32 v59, v59, v51\nv_mul_f32 v71, v71, v63\nv_mul_f32 v40, v36, v40\nv_mul_f32 v52, v48, v52\n" \
"v_mul_f32 v64, v60, v64\nv_mul_f32 v41, v37, v41\nv_mul_f32 v53, v49, v53\nv_mul_f32 v65, v61, v65\n" \
"v_mul_f32 v42, v38, v42\nv_mul_f32 v54, v50, v54\nv_mul_f32 v66, v62, v66\nv_mul_f32 v43, v39, v43\n" \
"v_mul_f32 v55, v51, v55\nv_mul_f32 v67, v63, v67\nv_exp_f32 v44, v44\nv_exp_f32 v56, v56\n" \
"v_exp_f32 v68, v68\nv_exp_f32 v45, v45\nv_exp_f32 v57, v57\nv_exp_f32 v69, v69\n" \
"v_exp_f32 v46, v46\nv_exp_f32 v58, v58\nv_exp_f32 v70, v70\nv_exp_f32 v47, v47\n" \
"v_exp_f32 v59, v59\nv_exp_f32 v71, v71\nv_fma_f32 v44, v44, v254, v254\nv_fma_f32 v56, v56, v254, v254\n" \
"v_fma_f32 v68, v68, v254, v254\nv_fma_f32 v45, v45, v254, v254\nv_fma_f32 v57, v57, v254, v254\nv_fma_f32 v69, v69, v254, v254\n" \
"v_fma_f32 v46, v46, v254, v254\nv_fma_f32 v58, v58, v254, v254\nv_fma_f32 v70, v70, v254, v254\nv_fma_f32 v47, v47, v254, v254\n" \
"v_fma_f32 v59, v59, v254, v254\nv_fma_f32 v71, v71, v254, v254\nv_rcp_f32 v44, v44\nv_rcp_f32 v56, v56\n" \
"v_rcp_f32 v68, v68\nv_rcp_f32 v45, v45\nv_rcp_f32 v57, v57\nv_rcp_f32 v69, v69\n" \
"v_rcp_f32 v46, v46\nv_rcp_f32 v58, v58\nv_rcp_f32 v70, v70\nv_rcp_f32 v47, v47\n" \
"v_rcp_f32 v59, v59\nv_rcp_f32 v71, v71\nv_mul_f32 v40, v40, v44\nv_mul_f32 v52, v52, v56\n" \
"v_mul_f32 v64, v64, v68\nv_mul_f32 v41, v41, v45\nv_mul_f32 v53, v53, v57\nv_mul_f32 v65, v65, v69\n" \
"v_mul_f32 v42, v42, v46\nv_mul_f32 v54, v54, v58\nv_mul_f32 v66, v66, v70\nv_mul_f32 v43, v43, v47\n" \
"v_mul_f32 v55, v55, v59\nv_mul_f32 v67, v67, v71\nv_cvt_pk_bf16_f32 v40, v40, v41\nv_cvt_pk_bf16_f32 v52, v52, v53\n" \
"v_cvt_pk_bf16_f32 v64, v64, v65\nv_cvt_pk_bf16_f32 v41, v42, v43\nv_cvt_pk_bf16_f32 v53, v54, v55\nv_cvt_pk_bf16_f32 v65, v66, v67\n" \
"buffer_store_dwordx2 v[40:41], v2, s[8:11], s16 offen offset:0 sc1\nbuffer_store_dwordx2 v[52:53], v2, s[8:11], s17 offen offset:256 sc1\nbuffer_store_dwordx2 v[64:65], v2, s[8:11], s17 offen offset:512 sc1\ns_waitcnt vmcnt(0)\n" \
"buffer_store_dword v80, v3, s[8:11], 0 offen offset:0 sc1\ns_cmp_ge_u32 s20, s28\ns_cbranch_scc1 9f\ns_lshr_b32 s94, s20, 1\n" \
"v_readlane_b32 s94, v6, s94\ns_and_b32 s95, s20, 1\ns_lshl_b32 s95, s95, 3\ns_lshr_b32 s94, s94, s95\n" \
"s_and_b32 s94, s94, 0xff\ns_lshl_b32 s95, s94, 13\ns_barrier\ns_branch 7b\n" \
"9:\ns_waitcnt vmcnt(0)\ns_endpgm\n"
struct WMA { static constexpr int NT = 4, NP = 16, RB = 0, LB = 0, CBI = 0;
             static constexpr int kind(int j, int t) { return (A_LDS >> (16 * j + t) & 1) ? 1 : (A_VAR >> (16 * j + t) & 1) ? 2 : 0; } };
namespace la { constexpr int L_Q = 24576, L_K = 57344, L_QM = 65536, L_KM = 66048, L_SP = 69632, L_W = 73728; }
#ifndef LEAD_B
#define LEAD_B 132
#endif
DEVI float dror8(float x) { return __uint_as_float(__builtin_amdgcn_update_dpp(0u, __float_as_uint(x), 0x128, 0xF, 0xF, true)); }
DEVI float scm(float a, float m) { float r; asm("v_fma_f32 %0, %1, %2, -%3" : "=v"(r) : "v"(a), "s"(SC_L2E), "v"(m)); return r; }
DEVI float ex2(float x) { return __builtin_amdgcn_exp2f(x); }
DEVI float max3(float a, float b, float c) { float r; asm("v_max3_f32 %0, %1, %2, %3" : "=v"(r) : "v"(a), "v"(b), "v"(c)); return r; }
constexpr u64 tri(int i) { u64 m = 0; for (int l = 0; l < 64; ++l) if (4 * (l >> 4) + i <= (l & 15)) m |= 1ull << l; return m; }
DEVI unsigned vbfe(unsigned a, int o, int n) { unsigned r; asm volatile("v_bfe_u32 %0, %1, %2, %3" : "=v"(r) : "v"(a), "i"(o), "i"(n)); return r; }
DEVI unsigned vadds(unsigned a, unsigned b) { unsigned r; asm volatile("v_add_u32 %0, %1, %2" : "=v"(r) : "s"(b), "v"(a)); return r; }
DEVI float vexp(float a) { float r; asm volatile("v_exp_f32 %0, %1" : "=v"(r) : "v"(a)); return r; }
DEVI unsigned vors(unsigned a, unsigned b) { unsigned r; asm volatile("v_or_b32 %0, %1, %2" : "=v"(r) : "s"(b), "v"(a)); return r; }
DEVI float faddv(float a, float b) { float r; asm volatile("v_add_f32 %0, %1, %2" : "=v"(r) : "v"(a), "v"(b)); return r; }
DEVI unsigned packv(float a, float b) { unsigned r; asm volatile("v_cvt_pk_bf16_f32 %0, %1, %2" : "=v"(r) : "v"(a), "v"(b)); return r; }
DEVI void setm0q(unsigned v) { asm volatile("s_mov_b32 m0, %0" :: "s"(v) : "memory"); }
template <int OFF> DEVI void st16o(v4u v, rsrc_t r, unsigned vo, unsigned so) {
    asm volatile("buffer_store_dwordx4 %0, %1, %2, %3 offen offset:%4 sc1" :: "v"(v), "v"(vo), "s"(r), "s"(so), "n"(OFF) : "memory"); }
DEVI unsigned vlsa(unsigned a, int sh, unsigned c) { unsigned r; asm volatile("v_lshl_add_u32 %0, %1, %2, %3" : "=v"(r) : "v"(a), "i"(sh), "v"(c)); return r; }
DEVI unsigned vlss(unsigned a, int sh, unsigned c) { unsigned r; asm volatile("v_lshl_add_u32 %0, %1, %2, %3" : "=v"(r) : "v"(a), "i"(sh), "s"(c)); return r; }
template <int OFF> DEVI void sts4h(unsigned p, unsigned u0, unsigned u1) {
    asm volatile("ds_write_b16 %0, %1 offset:%3\n\tds_write_b16_d16_hi %0, %1 offset:%3+16\n\tds_write_b16 %0, %2 offset:%3+8\n\tds_write_b16_d16_hi %0, %2 offset:%3+24" :: "v"(p), "v"(u0), "v"(u1), "n"(OFF) : "memory");
}
DEVI void zero4v(f32x4& d) { f32x2 a, b; asm volatile("v_mov_b64 %0, 0\n\tv_mov_b64 %1, 0" : "=v"(a), "=v"(b)); d = __builtin_shufflevector(a, b, 0, 1, 2, 3); }
DEVI float sel0(float a, u64 m) { float r; asm("v_cndmask_b32 %0, %1, 0, %2" : "=v"(r) : "v"(a), "s"(m)); return r; }
DEVI float vmul(float a, float b) { float r; asm("v_mul_f32 %0, %1, %2" : "=v"(r) : "v"(a), "v"(b)); return r; }
DEVI void swap16(float& a, float& b) { unsigned x = __float_as_uint(a), y = __float_as_uint(b); swap16u(x, y); a = __uint_as_float(x); b = __uint_as_float(y); }
#ifndef PACE
#define PACE 7
#endif
DEVI void pace(int l) { asm volatile("s_cmp_eq_u32 %0, 0\n\ts_cbranch_scc1 1f\n\ts_sleep %1\n1:" :: "s"(l), "n"(PACE) : "scc"); }
DEVI void role_A(const Args& a, int w, int l, int h, int rep, int id2) {
    asm volatile("; MARK role_A"); __builtin_amdgcn_s_waitcnt(0);
    using namespace la;
    const int lane0 = lane_id();
    const rsrc_t rs = mkrs(a.ws, WS_NEED);
    const unsigned lane16 = lane0 * 16, wl = L_W + w * (11 * 2048) + lane16;
    v8u Wc[3];
    unsigned nb, fx, ok;
    const unsigned own = (w >> 1) * 32768 + (w & 1) * 4096;
    {
        const int lane = lane0;
        const unsigned ta = (lane >> 4) * 2048 + w * 256 + (lane & 15) * 16;
        const rsrc_t r1 = mkrs(a.Wq, WSZ(8u << 20)), r2 = mkrs(a.Wk, WSZ(8u << 20));
        const unsigned v1 = (8 * w + ((lane >> 2) & 3)) * 8192 + h * 256 + (lane >> 4) * 64 + (lane & 3) * 16;
        const unsigned lw = opqs(w * 2048);
        load_blocks<32, 4, 4, true, 2>([&](auto B, auto S) {
            if constexpr (B.v == 32) ok = first_there(fx);
            if constexpr (B.v >= 32) dma4(opqs(own + S.v * 8192), mkrs(a.ws, ok), lane16, opqs(O_XN + l * XB + own + S.v * 8192));
            else { pace(l); sfor<0, 2>([&](auto I) { dma_blk(lw + S.v * 8192 + I.v * 32768, B.v < 16 ? r1 : r2, v1, opqs((2 * (B.v & 15) + I.v) * 32 * 8192), 2048); }); } },
                                       [&](auto B, auto S) { sfor<0, 4>([&](auto T) { constexpr int o = S.v * 8192 + (T.v >> 1) * 32768 + (T.v & 1) * 8;
                                                                                      put_frag<WMA, (B.v >> 4) * 2 + (T.v & 1), 2 * (B.v & 15) + (T.v >> 1), o, o + 1024>(ta, wl, Wc); });
                                                             if constexpr (B.v == 24) { nb = side(rs, l); first_look(fx, rs, nb); } });
        TSW(127, 2);
    }
    const unsigned spm = na(nb, N_PM + h * 512), spmm = na(nb, N_PMM + h * 128), smem = na(nb, N_MEM + rep * 16384 + w * 4096);
    const int lane = lane_id(), c = lane & 15, g = lane >> 4;
    const unsigned wlo = (unsigned)(uintptr_t)a.ws, whi = (unsigned)((uintptr_t)a.ws >> 32), hi = -(w >> 1);
    const v8u pv{lane16, wl, (lane & 2) ? 0xEEEEu : 0x4444u, (unsigned)(L_QM + w * 1024 + (c >> 2) * 128 + g * 32 + (c & 3) * 4), (unsigned)(L_QM + g * 128 + (c & 7) * 16),
                 w == 2 ? ((g >> 1) * 16 + crow(c & 7, c >> 3)) * 16 + (g & 1) * 8 : w < 2 ? lane * 8 : lane < NC ? DV(lane * 512) : 1u << 30,
                 w < 2 ? c * 16 + g * 4 : c < 8 ? crow(c, g & 1) * 8 + (g >> 1) * 4 : 1u << 30, 0u};
    const v16u s0{wlo, whi, RSZ, 0x20000u, wlo, whi, 0u, 0x20000u, (unsigned)tri(0) | hi, (unsigned)(tri(0) >> 32) | hi, (unsigned)tri(1) | hi, (unsigned)(tri(1) >> 32) | hi,
                  (unsigned)tri(2) | hi, (unsigned)(tri(2) >> 32) | hi, (unsigned)tri(3) | hi, (unsigned)(tri(3) >> 32) | hi};
    const v16u s1{0xFF00FF00u, 0xFF00FF00u, 0u, ~0u, hi, hi, 0u, O_XN + l * XB + own, own, smem, w == 2 ? spm : OOB, nb + DV(N_PTX + h * 2048), w == 1 ? 0u : OOB, w == 0 ? 0u : OOB,
                  w < 2 ? w * 256 : w == 2 ? spmm - 1536 : OOB, ~hi};
    const v16u s2{w < 2 ? w * 1024 : w == 2 ? spm : spmm, w < 2 ? 4u : 3u, w < 2 ? 0u : w == 2 ? 0x200u : 0x380u, w ? 0u : __float_as_uint(-3e38f), (unsigned)(L_Q + w * 1024), (unsigned)(L_Q + (w & 1) * 4096),
                  __float_as_uint(SC_L2E), __float_as_uint(-1e30f), na(nb, N_FX), w == 3 ? na(nb, N_PA + ((h >> 3) * 32 + 8 + (h & 7)) * 4) : OOB, (unsigned)(L_KM + (w >> 1) * 1024 + (w & 1) * 256),
                  0u, 0u, (unsigned)(L_SP + w * 256), O_PROF + (blockIdx.x * 4 + w) * 8192, na(nb, N_WM + h * 16384 + w * 4096)};
    asm volatile(A_LOOP :: "{v[0:7]}"(Wc[0]), "{v[8:15]}"(Wc[1]), "{v[16:23]}"(Wc[2]), "{v[24:31]}"(pv), "{s[8:23]}"(s0), "{s[24:39]}"(s1), "{s[40:55]}"(s2),
                 "{s58}"(ok), "{s[4:7]}"(again(id2, nb, w)) : "memory", "s99", "vcc");
}
constexpr int bkind(int n) { return (B_LDS >> n & 1) ? 1 : (B_VAR >> n & 1) ? 2 : 0; }
struct WMV { static constexpr int NT = 2, NP = 16, RB = 0, LB = 0, CBI = 0; static constexpr int kind(int j, int t) { return bkind(16 * j + t); } };
struct WMO { static constexpr int NT = 16, NP = 2, LB = __builtin_popcount((unsigned)B_LDS), CBI = __builtin_popcount((unsigned)B_VAR), RB = 8 * (32 - LB - CBI);
             static constexpr int kind(int j, int t) { return bkind(32 + 2 * j + t); } };
#ifndef B_LV
#define B_LV 24576
#endif
namespace lb { constexpr int L_V = B_LV, L_VM = B_LV + 4096, L_W = 65536; }
DEVI void role_B(const Args& a, int w, int l, int h, int rep, int id2) {
    asm volatile("; MARK role_B"); __builtin_amdgcn_s_waitcnt(0);
    using namespace lb;
    const int lane0 = lane_id();
    const rsrc_t rs = mkrs(a.ws, WS_NEED);
    const unsigned lane16 = lane0 * 16, wl = L_W + w * (12 * 2048) + lane16;
    v8u Wc[2];
    unsigned nb, fx, ok;
    const unsigned own = (w >> 1) * 32768 + (w & 1) * 4096;
    {
        const int lane = lane0;
        const unsigned ta = (lane >> 4) * 2048 + w * 256 + (lane & 15) * 16, tb = (lane >> 5) * 32768 + w * 2048 + (lane & 16) * 64 + (lane & 15) * 16;
        const rsrc_t r1 = mkrs(a.Wv, WSZ(8u << 20)), r2 = mkrs(a.Wo, WSZ(8u << 20));
        const unsigned v1 = (8 * w + ((lane >> 2) & 3)) * 8192 + h * 256 + (lane >> 4) * 64 + (lane & 3) * 16, v2 = (h * 128 + ((lane >> 2) & 7)) * 2048 + w * 512 + (lane >> 5) * 64 + (lane & 3) * 16;
        const unsigned lw = opqs(w * 2048);
        load_blocks<32, 4, 4, true, 3>([&](auto B, auto S) {
            constexpr int b = B.v & 15;
            if constexpr (B.v == 32) ok = first_there(fx);
            if constexpr (B.v < 32) pace(l);
            if constexpr (B.v >= 32) dma4(opqs(own + S.v * 8192), mkrs(a.ws, ok), lane16, opqs(O_XN + l * XB + own + S.v * 8192));
            else sfor<0, 2>([&](auto I) { if constexpr (B.v < 16) dma_blk(lw + S.v * 8192 + I.v * 32768, r1, v1, opqs((2 * b + I.v) * 32 * 8192), 2048);
                                     else dma_blk(lw + S.v * 8192 + I.v * 32768, r2, v2, opqs(((b & 3) * 32 + I.v * 16) * 2048 + (b >> 2) * 128), 1024); });
        }, [&](auto B, auto S) {
            constexpr int b = B.v & 15;
            sfor<0, 4>([&](auto T) { constexpr int o = S.v * 8192 + (T.v >> 1) * (B.v < 16 ? 32768 : 512) + (T.v & 1) * 8;
                                     if constexpr (B.v < 16) put_frag<WMV, T.v & 1, 2 * b + (T.v >> 1), o, o + 1024>(ta, wl, Wc); else put_frag<WMO, (b & 12) + T.v, b & 3, o, o + 256>(tb, wl, Wc); });
            if constexpr (B.v == 24) { nb = side(rs, l); first_look(fx, rs, nb); }
        });
        TSW(127, 2);
    }
    const int lane = lane_id(), c = lane & 15, g = lane >> 4;
    const unsigned wlo = (unsigned)(uintptr_t)a.ws, whi = (unsigned)((uintptr_t)a.ws >> 32);
    const unsigned prow = L_VM + w * 512 + ((c >> 2) & 1) * 256 + g * 64 + (c >> 3) * 32 + (c & 1) * 8 + ((c >> 1) & 1) * 4;
    const v4u pv{lane16, wl, ((lane & 2) ? 0xEEEEu : 0x4444u) | prow << 16, (unsigned)(L_VM + (g >> 1) * 512 + (lane & 3) * 64 + (g & 1) * 32 + ((lane >> 2) & 3) * 8)};
    const v16u s0{wlo, whi, RSZ, 0x20000u, wlo, whi, 0u, 0x20000u, OOB, OOB, OOB, OOB, ~1u, O_XN + l * XB + own, own, na(nb, N_MEM + rep * 16384 + w * 4096)};
    const v8u s1{na(nb, N_WM + h * 16384 + w * 4096), na(nb, N_U + h * 65536 + w * 16384), na(nb, N_FX), w == 3 ? na(nb, N_PA + ((h >> 3) * 32 + (h & 7)) * 4) : OOB,
                 (unsigned)(L_V + w * 1024), OOB, 0u, O_PROF + (blockIdx.x * 4 + w) * 8192 + 64};
    asm volatile(B_LOOP :: "{v[0:7]}"(Wc[0]), "{v[8:15]}"(Wc[1]), "{v[16:19]}"(pv), "{v20}"(fx), "{s[8:23]}"(s0), "{s[24:31]}"(s1), "{s32}"(1u), "{s[4:7]}"(again(id2, nb, w)) : "memory", "s99", "vcc");
}
template <class NB> DEVI void put_tile(v4u (&u)[8], rsrc_t rs, int tile, int w, int lane, NB&& nbf) {
    const unsigned vo = tile * 32768 + w * 8192 + lane * 16, red = (lane & 15) * 16;
    float s = 0.f;
#pragma unroll
    for (int f = 0; f < 8; ++f) {
#pragma unroll
        for (int i = 0; i < 4; ++i) s += bflo(u[f][i]) + bfhi(u[f][i]);
    }
    s = red32(red16(s));
    BAR;
    if (lane < 16) *LDSP(float, red + w * 4) = s;
    WL0; BAR;
    const f32x4 sa = __builtin_bit_cast(f32x4, ldsq(red, 0));
    const float mean = ((sa[0] + sa[1]) + (sa[2] + sa[3])) * (1.0f / 1024.0f);
    float q = 0.f;
#pragma unroll
    for (int f = 0; f < 8; ++f) {
        asm volatile("" : "+v"(u[f]));
#pragma unroll
        for (int i = 0; i < 4; ++i) { const float d0 = bflo(u[f][i]) - mean, d1 = bfhi(u[f][i]) - mean; q = fmaf(d0, d0, fmaf(d1, d1, q)); }
    }
    q = red32(red16(q));
    if (lane < 16) *LDSP(float, red + 256 + w * 4) = q;
    WL0; BAR;
    const f32x4 qa = __builtin_bit_cast(f32x4, ldsq(red, 256));
    const float mb = rbf(mean), inv = 1.0f / rbf(sqrtf(rbf(rbf(((qa[0] + qa[1]) + (qa[2] + qa[3])) * (1.0f / 1024.0f)) + 1e-5f)));
#pragma unroll
    for (int f = 0; f < 8; ++f) {
        v4u o;
        asm volatile("" : "+v"(u[f]));
#pragma unroll
        for (int i = 0; i < 4; ++i) { const unsigned d = pack2(bflo(u[f][i]) - mb, bfhi(u[f][i]) - mb); o[i] = pack2(bflo(d) * inv, bfhi(d) * inv); }
        st16(o, rs, O_XN + vo + f * 1024);
    }
    const unsigned nb = nbf();
    WV0;
    if (lane == 0) st4(0u, rs, na(nb, N_FX) + tile * 16 + w * 4, 0);
}
#ifndef SXL
#define SXL 1
#endif
#ifndef HLD
#define HLD 158
#endif
#ifndef SXH
#define SXH 30
#endif
#ifndef UIN
#define UIN 8
#endif
namespace lr { constexpr int PT1 = NV + 48, PT0 = NV + 80, PTM = NV + 96, VPM = NV, VMM = NV + 16, VW = NV + 28, VX = NV + 44, L_M = 1024, MD = 288, MW = 1536, L_T = 8192, VE = NV + 112; }
template <int R, int N, int OFF> DEVI void lda(rsrc_t rs, unsigned vo, unsigned so) {
    if constexpr (N == 4) asm volatile("buffer_load_dwordx4 a[%3:%4], %0, %1, %2 offen offset:%5 sc1" :: "v"(vo), "s"(rs), "s"(so), "n"(R), "n"(R + 3), "n"(OFF) : "memory");
    else asm volatile("buffer_load_dwordx2 a[%3:%4], %0, %1, %2 offen offset:%5 sc1" :: "v"(vo), "s"(rs), "s"(so), "n"(R), "n"(R + 1), "n"(OFF) : "memory");
}
template <int R, int N, int OFF> DEVI void ldv(rsrc_t rs, unsigned vo, unsigned so) {
    if constexpr (N == 4) asm volatile("buffer_load_dwordx4 v[%3:%4], %0, %1, %2 offen offset:%5 sc1" :: "v"(vo), "s"(rs), "s"(so), "n"(R), "n"(R + 3), "n"(OFF) : "memory");
    else asm volatile("buffer_load_dwordx2 v[%3:%4], %0, %1, %2 offen offset:%5 sc1" :: "v"(vo), "s"(rs), "s"(so), "n"(R), "n"(R + 1), "n"(OFF) : "memory");
}
template <int R> DEVI unsigned rdv() { unsigned t; asm volatile("v_mov_b32 %0, v[%1]" : "=v"(t) : "n"(R)); return t; }
template <int RA, int RB, bool Z, bool AV> DEVI void mfh(f32x4& d) {
    if constexpr (AV) { if constexpr (Z) asm volatile(M16 "%0, v[%1:%2], v[%3:%4], 0" : "=v"(d) : "n"(RA), "n"(RA + 3), "n"(RB), "n"(RB + 3));
                        else asm volatile(M16 "%0, v[%1:%2], v[%3:%4], %0" : "+v"(d) : "n"(RA), "n"(RA + 3), "n"(RB), "n"(RB + 3)); }
    else { if constexpr (Z) asm volatile(M16 "%0, a[%1:%2], v[%3:%4], 0" : "=v"(d) : "n"(RA), "n"(RA + 3), "n"(RB), "n"(RB + 3));
           else asm volatile(M16 "%0, a[%1:%2], v[%3:%4], %0" : "+v"(d) : "n"(RA), "n"(RA + 3), "n"(RB), "n"(RB + 3)); }
}
template <int RA, int RB, bool Z> DEVI void mfh16(f32x4& d) {
    if constexpr (Z) asm volatile("v_mfma_f32_16x16x16_bf16 %0, a[%1:%2], v[%3:%4], 0" : "=v"(d) : "n"(RA), "n"(RA + 1), "n"(RB), "n"(RB + 1));
    else asm volatile("v_mfma_f32_16x16x16_bf16 %0, a[%1:%2], v[%3:%4], %0" : "+v"(d) : "n"(RA), "n"(RA + 1), "n"(RB), "n"(RB + 1));
}
template <int R> DEVI u64 ltv(int need) { u64 m; asm volatile("v_cmp_lt_i32 %0, v[%2], %1" : "=s"(m) : "v"(need), "n"(R)); return m; }
template <int R> DEVI void ldv4(rsrc_t rs, unsigned vo, unsigned so) { asm volatile("buffer_load_dwordx4 v[%3:%4], %0, %1, %2 offen sc1" :: "v"(vo), "s"(rs), "s"(so), "n"(R), "n"(R + 3) : "memory"); }
#define SM_(E, A, B) "v_smfmac_f32_16x16x64_bf16 v[%[e]+" #E ":%[e]+" #E "+3], " A ", " B ", %[si]\n\t"
#define TG_(M, R) "v_bitop3_b32 v[%[e]+" #M "], v[%[e]+" #M "], v[" R "], %[tg] bitop3:0xf6\n\t"
#define PP_(P, N) "s_waitcnt vmcnt(" #N ")\n\t" SM_(0, "v[%[p]+4*" #P ":%[p]+4*" #P "+3]", "a[%[u]+16*" #P ":%[u]+16*" #P "+7]") TG_(8, "%[p]+4*" #P)
#define PQ_(P) SM_(4, "v[%[p]+4*" #P ":%[p]+4*" #P "+3]", "a[%[u]+16*" #P "+8:%[u]+16*" #P "+15]") TG_(9, "%[p]+4*" #P "+2")
#define ZE_ "v_mov_b64 v[%[e]:%[e]+1], 0\n\tv_mov_b64 v[%[e]+2:%[e]+3], 0\n\tv_mov_b64 v[%[e]+4:%[e]+5], 0\n\tv_mov_b64 v[%[e]+6:%[e]+7], 0\n\tv_mov_b64 v[%[e]+8:%[e]+9], 0\n\t"
template <int U0> DEVI unsigned chain(rsrc_t rs, int tgo, unsigned sp, unsigned sm, unsigned sw0, unsigned sw1, unsigned tg, unsigned spt, unsigned lane16, unsigned lane8, unsigned vmm, unsigned vw, int sidx,
                                      unsigned lred, unsigned lrr, unsigned vpub, v2u sxv, unsigned sxo) {
    using namespace lr;
    unsigned n, d;
    asm volatile(ZE_ "s_mov_b32 %[n], 0\n\ts_memrealtime s[98:99]\n\ts_add_u32 %[d], %[pt], 0x800\n\ts_waitcnt lgkmcnt(0)\n\ts_sub_i32 s99, %[t], s98\n\ts_lshl_b32 s99, s99, 8\n\ts_sub_u32 s99, s99, 1\n\t"
        "s_cmp_lt_u32 s99, 0x10000\n\ts_cbranch_scc1 2f\n\ts_branch 3f\n"
        "1:\n\ts_add_u32 %[n], %[n], 1\n\ts_cmp_lt_u32 %[n], %[cap]\n\ts_cbranch_scc1 4f\n\ts_endpgm\n4:\n\t" ZE_ "v_bfrev_b32 %[so], 1\n\ts_branch 3f\n"
        "2:\n\ts_memrealtime s[98:99]\n\ts_waitcnt lgkmcnt(0)\n\ts_sub_i32 s99, %[t], s98\n\ts_lshl_b32 s99, s99, 8\n\ts_cmp_gt_i32 s99, 0\n\ts_cbranch_scc1 2b\n"
        "3:\n\t"
        "buffer_load_dwordx4 v[%[p]:%[p]+3], %[l16], %[rs], %[sp] offen sc1\n\t"
        "buffer_load_dwordx4 v[%[p]+4:%[p]+7], %[l16], %[rs], %[sp] offen offset:1024 sc1\n\t"
        "buffer_load_dwordx4 v[%[p]+8:%[p]+11], %[l16], %[rs], %[sp] offen offset:2048 sc1\n\t"
        "buffer_load_dwordx4 v[%[p]+12:%[p]+15], %[l16], %[rs], %[sp] offen offset:3072 sc1\n\t"
        "buffer_load_dwordx2 v[%[p]+16:%[p]+17], %[vm], %[rs], %[sm] offen sc1\n\t"
        "buffer_load_dwordx2 v[%[p]+18:%[p]+19], %[vm], %[rs], %[sm] offen offset:128 sc1\n\t"
        "buffer_load_dwordx4 v[%[w]:%[w]+3], %[vw], %[rs], %[sw0] offen sc1\n\t"
        "buffer_load_dwordx4 v[%[w]+4:%[w]+7], %[vw], %[rs], %[sw1] offen sc1\n\t"
        "buffer_load_dwordx4 v[%[w]+8:%[w]+11], %[vw], %[rs], %[sw0] offen offset:256 sc1\n\t"
        "buffer_load_dwordx4 v[%[w]+12:%[w]+15], %[vw], %[rs], %[sw1] offen offset:256 sc1\n\t"
        PP_(0, 9) "buffer_store_dwordx2 %[sx], %[so], %[rs], 0 offen sc1\n\t" PQ_(0) PP_(1, 9) PQ_(1) PP_(2, 8) PQ_(2) PP_(3, 7) PQ_(3)
        "s_waitcnt vmcnt(5)\n\t" TG_(8, "%[p]+16") TG_(9, "%[p]+17") TG_(8, "%[p]+18") TG_(9, "%[p]+19")
        "s_waitcnt vmcnt(3)\n\t" SM_(0, "v[%[p]+16:%[p]+19]", "v[%[w]:%[w]+7]") TG_(8, "%[w]") TG_(9, "%[w]+4")
        "s_waitcnt vmcnt(1)\n\t" SM_(4, "v[%[p]+16:%[p]+19]", "v[%[w]+8:%[w]+15]") TG_(8, "%[w]+8") TG_(9, "%[w]+12")
        "v_bitop3_b32 v[%[e]+8], v[%[e]+8], v[%[e]+9], 1 bitop3:0xa8\n\tv_cmp_ne_u32 vcc, 0, v[%[e]+8]\n\ts_nop 0\n\t"
        "v_add_f32 v[%[e]], v[%[e]], v[%[e]+2]\n\tv_add_f32 v[%[e]+1], v[%[e]+1], v[%[e]+3]\n\tds_write2_b32 %[lw], v[%[e]], v[%[e]+1] offset1:2\n\ts_nop 0\n\t"
        "v_add_f32 v[%[e]+4], v[%[e]+4], v[%[e]+6]\n\tv_add_f32 v[%[e]+5], v[%[e]+5], v[%[e]+7]\n\tds_write2_b32 %[lw], v[%[e]+4], v[%[e]+5] offset0:%[md] offset1:%[md]+2\n\t"
        "s_cbranch_vccnz 1b\n\t"
        "buffer_load_dwordx4 v[%[t1]:%[t1]+3], %[l16], %[rs], %[pt] offen sc1\n\tbuffer_load_dwordx2 v[%[t0]:%[t0]+1], %[l8], %[rs], %[pt] offen offset:1024 sc1\n\t"
        "buffer_load_dwordx4 v[%[t1]+4:%[t1]+7], %[l16], %[rs], %[d] offen sc1\n\tbuffer_load_dwordx2 v[%[t0]+2:%[t0]+3], %[l8], %[rs], %[d] offen offset:1024 sc1\n\t"
        "s_waitcnt lgkmcnt(0)\n\ts_barrier\n\t"
        "ds_read2st64_b64 v[%[e]:%[e]+3], %[lr] offset1:3\n\tds_read2st64_b64 v[%[e]+4:%[e]+7], %[lr] offset0:6 offset1:9\n\ts_waitcnt lgkmcnt(1)\n\t"
        "v_add_f32 v[%[e]], v[%[e]], v[%[e]+2]\n\tv_add_f32 v[%[e]+1], v[%[e]+1], v[%[e]+3]\n\ts_waitcnt lgkmcnt(0)\n\t"
        "v_add_f32 v[%[e]+4], v[%[e]+4], v[%[e]+6]\n\tv_add_f32 v[%[e]+5], v[%[e]+5], v[%[e]+7]\n\tv_add_f32 v[%[e]], v[%[e]], v[%[e]+4]\n\tv_add_f32 v[%[e]+1], v[%[e]+1], v[%[e]+5]\n\t"
        "v_cvt_pk_bf16_f32 v[%[e]+2], v[%[e]], v[%[e]+1]\n\tv_min_u32 v[%[e]], -2, v[%[e]+2]\n\tv_min_u32 v[%[e]+1], -2, v[%[e]+2]\n\ts_nop 1\n\t"
        "v_permlane16_swap_b32 v[%[e]], v[%[e]+1]\n\tv_mov_b64 v[%[e]+2:%[e]+3], v[%[e]:%[e]+1]\n\ts_nop 1\n\t"
        "v_permlane32_swap_b32 v[%[e]], v[%[e]+2]\n\tv_permlane32_swap_b32 v[%[e]+1], v[%[e]+3]\n\t"
        "buffer_store_dwordx4 v[%[e]:%[e]+3], %[vp], %[rs], 0 offen sc1"
        : [n] "=&s"(n), [d] "=&s"(d), [so] "+v"(sxo)
        : [rs] "s"(rs), [t] "s"(tgo), [sp] "s"(sp), [sm] "s"(sm), [sw0] "s"(sw0), [sw1] "s"(sw1), [tg] "s"(tg), [pt] "s"(spt), [l16] "v"(lane16), [l8] "v"(lane8), [vm] "v"(vmm), [vw] "v"(vw), [si] "v"(sidx),
          [lw] "v"(lred), [lr] "v"(lrr), [vp] "v"(vpub), [sx] "v"(sxv), [e] "n"(VE), [p] "n"(VPM), [w] "n"(VW), [u] "n"(U0), [t1] "n"(PT1), [t0] "n"(PT0), [md] "n"(MD / 4), [cap] "n"(SPIN_CAP)
        : "memory", "scc", "vcc", "s98", "s99");
    return n;
}
template <int R> DEVI void tagd(unsigned& m, unsigned t) { asm volatile("v_bitop3_b32 %0, %0, v[%2], %1 bitop3:0xf6" : "+v"(m) : "s"(t), "n"(R)); }
#ifndef DLY_R
#define DLY_R 1200
#endif
#ifndef DLY_X
#define DLY_X 1300
#endif
DEVI unsigned role_R(const Args& a, int w, int l, int T2, int t0) {
    asm volatile("; MARK role_R"); __builtin_amdgcn_s_waitcnt(0);
    using namespace lr;
    const int lane = lane_id(), c = lane & 15, g = lane >> 4;
    const rsrc_t rs = mkrs(a.ws, WS_NEED);
    const unsigned lane16 = lane * 16;
#pragma nounroll
    for (int k = 0; k < 4 * l; ++k) {
        const int tile = k * 32 + T2;
        if (tile >= LN_NOW) hold(t0, DLY_X);
        v4u u[8];
#pragma unroll
        for (int f = 0; f < 8; ++f) u[f] = *(const v4u*)(a.x + (size_t)(tile * 16 + c) * 1024 + (w * 8 + f) * 32 + g * 8);
        if (tile < 2 * NSEG) put_tile(u, rs, tile, w, lane, [&]() { hold(t0, DLY_V); return side(rs, 0); });
        TSW(127, 1 + k);
    }
    auto arm = [&](unsigned n2, int s) {
        if (s < 8)
#pragma unroll
            for (int k = 0; k < 4; ++k) st16(v4u{SENT, SENT, SENT, SENT}, rs, na(n2, N_WM + s * 524288 + T2 * 16384 + w * 4096) + k * 1024 + lane16);
        st16(v4u{SENT, SENT, SENT, SENT}, rs, w < 2 ? na(n2, N_PTX + s * 65536 + T2 * 2048) + w * 1024 + lane16 : lane >= (w == 2 ? 32 : 8) || s >= 8 ? OOB :
                                              na(n2, w == 2 ? N_PM + s * 16384 + T2 * 512 : N_PMM + s * 8192 + T2 * 128) + lane16);
    };
    hold(t0, DLY_R);
    const unsigned nb = side(rs, l), vmem = c < 8 ? na(nb, N_MEM + T2 * 512) + g * 65536 + w * 128 + c * 16 : OOB;
    auto put_mem = [&](v4u v, int s) { st16(v, rs, vmem + rslot(s) * (NREP * 32768)); };
    {
        const unsigned* m0 = (const unsigned*)(a.mem0 + ((w & 1) * 4 + ((lane >> 2) & 1) * 2 + (lane & 1)) * 1024 + T2 * 32 + (w >> 1) * 16 + ((lane >> 1) & 1) * 2);
        const v4u mv{nosent(m0[0]), m0[2], m0[4], m0[6]};
        put_mem(v4u{SENT, SENT, SENT, SENT}, 1); put_mem(v4u{SENT, SENT, SENT, SENT}, 2);
#pragma nounroll
        for (int k = 0; k < 16; ++k) arm(nb, k);
        WV0; BAR;
        put_mem(mv, 0);
    }
    const int sidx = opq((lane & 2) ? 0xEEEE : 0x4444);
    const unsigned vmm = (8 * w + 2 * g) * 128 + c * 8, vw = (8 * w + g) * 32768 + DV(T2 * 512) + c * 16, vptm = g * 2048 + 1536 + c * 16;
    const unsigned sfl = na(nb, N_PA + (unsigned)((T2 & (NC - 1)) * 4 + w) * 128), su0 = na(nb, N_U + 8 * w * 65536 + T2 * 2048), vfl = sfl + c * 4;
    const unsigned lred = L_M + w * MW + ((c >> 2) & 1) * 2 * MD + (c >> 3) * 128 + g * 32 + ((c >> 1) & 1) * 16 + (c & 1) * 4, lrr = L_M + g * MD + w * 64 + (c & 7) * 8;
    auto ld_u = [&](auto B, int s) {
        const unsigned so = su0 + rslot(s) * (4u << 20);
        sfor<0, 16>([&](auto I) { constexpr int j = I.v >> 1, e = I.v & 1; lda<64 * B.v + 16 * (j >> 1) + 8 * e + 4 * (j & 1), 4, e * 1024>(rs, lane16, so + j * 131072); });
    };
    wait_prog(rs, sfl + (lane & 7) * 4, 0, 0);
    ld_u(IC<0>{}, 0);
    const rsrc_t rxi = l ? rs : mkrs(a.x, XB);
    const unsigned vxi = l ? lane16 : c * 2048 + g * 16, sxi = (w & 1) * 32768 + (l ? O_XIN + XB + T2 * 1024 : T2 * 64);
    v2u sxv{0u, 0u};
    unsigned sxo = OOB;
    int tpub = t0;
    auto step = [&](auto B, int s) {
        constexpr int b = B.v;
        TSD;
        TSR(0); TSK(15);
        if (s == 8) { TSC(126, 0); TSW(126, 1); }
        if (s == 56) { TSC(126, 2); TSW(126, 3); }
        const unsigned spt = na(nb, N_PTX + (unsigned)((s & 15) * 32 + 8 * w) * 2048);
        auto ptx = [&](auto N) {
            constexpr int n = N.v & 31, j = n >> 1;
            if constexpr (n == 20) ldv4<VX>(rxi, vxi, sxi + s * 65536);
            else if constexpr (n >= 16) ldv<PTM + 4 * (n - 16), 4, (n & 1) * 256>(rs, vptm, spt + (j - 8) * 16384);
            else if constexpr (n & 1) ldv<PT0 + 2 * j, 2, 1024>(rs, lane * 8, spt + DV(j * 2048));
            else ldv<PT1 + 4 * j, 4, 0>(rs, lane16, spt + DV(j * 2048));
        };
        {
            const unsigned sw = opqs(na(nb, N_WM + rslot(s) * 524288)), sw1 = opqs(sw + 131072), sp = opqs(na(nb, N_PM + w * 4096) + rslot(s) * 32768), sm = opqs(na(nb, N_PMM) + rslot(s) * 16384);
            const unsigned vpub = opq(s + 1 < NSEG ? vmem + rslot(s + 1) * (NREP * 32768) : OOB), tg = opqs((s >> 3) & 1), spq = opqs(spt);
            SB;
            if ((int)__builtin_amdgcn_s_memrealtime() - tpub < HLD) { wvm<16 - UIN>(); st8(sxv, rs, sxo, 0); sxo = OOB; hold(tpub, HLD); }
            const int mx = wait_hint(rs, sfl, vfl, s + 1);
            if (left(mx) > SXH * 256) { wvm<16 - UIN>(); st8(sxv, rs, sxo, 0); sxo = OOB; }
            TSR(5);
            const unsigned spins = chain<64 * b>(rs, mx, sp, sm, sw, sw1, tg, spq, lane16, lane * 8, vmm, vw, sidx, lred, lrr, vpub, sxv, sxo);
#if defined(TS) && TS != 4
            tsr[8] = __builtin_amdgcn_readfirstlane(spins) + 1;
#endif
            tpub = (int)__builtin_amdgcn_s_memrealtime();
            TSR(2);
        }
        sfor<4, 21>(ptx);
        BAR;
        f32x4 t[2][2], u[2];
        const unsigned su = s + 1 < NSEG ? su0 + rslot(s + 1) * (4u << 20) : OOB;
        st16(v4u{SENT, SENT, SENT, SENT}, rs, s + 3 < NSEG ? vmem + rslot(s + 3) * (NREP * 32768) : OOB);
#pragma nounroll
        for (int n = 0;; ++n) {
            if (n) { sfor<32, 52>(ptx); WV0; }
            auto lu = [&](auto N) { constexpr int j = N.v >> 1, e = N.v & 1; lda<16 * (j >> 1) + 8 * e + 4 * (j & 1) + 64 * (1 - b), 4, e * 1024>(rs, lane16, su + j * 131072); };
            TSR(6);
            unsigned mt = opq(0);
            const unsigned tp = (s >> 4) & 1;
            sfor<0, 16>([&](auto N) {
                constexpr int j = N.v >> 1, e = N.v & 1, R = 64 * b + 16 * (j >> 1) + 8 * e + 4 * (j & 1), nu = N.v * UIN / 16;
                if constexpr (e == 0) wvm<21 - 2 * j + (j < 2) + nu - 1>();
                if constexpr ((N.v + 1) * UIN / 16 > nu) lu(IC<nu>{});
                mfh<R, PT1 + 4 * j, j == 0, false>(t[1][e]); tagd<e ? PT0 + 2 * j : PT1 + 4 * j>(mt, tp); mfh16<R, PT0 + 2 * j, j == 0>(u[e]);
            });
            wvm<1 + UIN>();
            sfor<0, 8>([&](auto N) { constexpr int q = N.v >> 2, rt = (N.v >> 1) & 1, e = N.v & 1; mfh<VW + 8 * e + 4 * q, PTM + 4 * (2 * q + rt), N.v < 2, true>(t[rt][e]);
                                     tagd<PTM + 2 * N.v>(mt, tp); tagd<PTM + 2 * N.v + 1>(mt, tp); });
            nop<7>(); fence(t[0][0], t[0][1], t[1][0], t[1][1], u[0], u[1]);
#pragma unroll
            for (int e = 0; e < 2; ++e)
#pragma unroll
                for (int i = 0; i < 4; ++i) t[0][e][i] = fadd(t[0][e][i], u[e][i]);
            if (__builtin_amdgcn_uicmp(mt & 1, 0u, 33) == 0 || n == 2) break;
            TSV(s, 7, 1);
            if (n == 1) wait_prog(rs, sfl + 32 + (lane & 7) * 4, 0, (s + 2) << 24);
        }
        TSR(3);
#pragma unroll
        for (int e = 0; e < 2; ++e) { stsq(lane16 + w * 4096, L_T + e * 1024, __builtin_bit_cast(v4u, t[0][e])); stsq(lane16 + w * 4096, L_T + 2048 + e * 1024, __builtin_bit_cast(v4u, t[1][e])); }
        WL0; BAR;
        if (w < 2) {
            float o[8], s1 = 0.f, s2 = 0.f;
#pragma unroll
            for (int e = 0; e < 2; ++e) {
                f32x4 q[4];
#pragma unroll
                for (int k = 0; k < 4; ++k) q[k] = __builtin_bit_cast(f32x4, ldsq(lane16 + w * 2048, L_T + k * 4096 + e * 1024));
#pragma unroll
                for (int i = 0; i < 4; ++i) o[4 * e + i] = fadd(fadd(q[0][i], q[1][i]), fadd(q[2][i], q[3][i]));
            }
            v4u x1;
            sfor<0, 4>([&](auto I) {
                constexpr int i = I.v;
                const unsigned x = rdv<VX + i>(), at = pack2(o[2 * i], o[2 * i + 1]);
                x1[i] = pack2(bflo(x) + bflo(at), bfhi(x) + bfhi(at));
                const float lo = bflo(x1[i]), hi = bfhi(x1[i]);
                s1 += lo + hi; s2 = fmaf(lo, lo, fmaf(hi, hi, s2));
            });
            st16(x1, rs, O_XM + l * XB + s * 65536 + w * 32768 + T2 * 1024 + lane16);
            sxv = v2u{nosent(__float_as_uint(red32(red16(s1)))), __float_as_uint(red32(red16(s2)))};
            sxo = lane < 16 ? O_SX + (unsigned)((l * 64 + s) * 32 + T2) * 256 + (SXL ? w * 128 + c * 8 : c * 16 + w * 8) : OOB;
        }
        TSR(4);
        {
            sfor<UIN, 16>([&](auto N) { constexpr int j = N.v >> 1, e = N.v & 1; lda<16 * (j >> 1) + 8 * e + 4 * (j & 1) + 64 * (1 - b), 4, e * 1024>(rs, lane16, su + j * 131072); });
        }
#if TS != 4
        TSF(s, 0x817D);
#endif
    };
#pragma nounroll
    for (int s = 0; s < NSEG; s += 2) { step(IC<0>{}, s); step(IC<1>{}, s + 1); }
    wvm<16 - UIN>(); st8(sxv, rs, sxo, 0);
    WV0; BAR;
    return nb;
}
#ifndef FSIDE
#define FSIDE 1
#endif
#ifndef PDX
#define PDX 0
#endif
constexpr int NU = 2 * NSEG;
#ifndef PSF
#define PSF 24
#endif
#ifndef PSR
#define PSR 6
#endif
#ifndef NRD
#define NRD 4
#endif
#ifndef RPA
#define RPA 8
#endif
#ifndef GROT
#define GROT 1
#endif
#ifndef LAGK
#define LAGK 4
#endif
#ifndef LAGP
#define LAGP 0
#endif
#ifndef N2F
#define N2F 43
#endif
#ifndef N2H
#define N2H 2
#endif
#ifndef N2M
#define N2M 9
#endif
DEVI unsigned unit_ls(unsigned u, int two) {
    static_assert(NSEG == 64 && LAGD >= 6 && N2F + 2 * N2M <= 65 - N2H, "");
    static_assert(LAGP % 2 == 0 && LAGP < 2 * LAGK + 1, "");
    const unsigned d = u - LAGD + LAGP, p = d / (2 * LAGK + 1), r = d % (2 * LAGK + 1), n0 = LAGD - LAGP / 2 + p * (LAGK + 1) + (r >> 1);
    const unsigned n = u < LAGD || n0 >= 64 ? u : ((r & 1) || r == 2 * LAGK) ? n0 : 64 + u - n0;
    return (two ? (u < N2M ? 64 + N2F + 2 * u : NU - N2H - N2M + u) : n < 64 + N2F ? n : n + min(n - (63 + N2F), (unsigned)N2M)) & 127;
}
DEVI unsigned unit_tab(int lane, int two) { return opq(unit_ls(2 * lane, two) | unit_ls(2 * lane + 1, two) << 8); }
DEVI unsigned unit_get(unsigned tab, int u) { return ((unsigned)__builtin_amdgcn_readlane(tab, (u >> 1) & 63) >> ((u & 1) * 8)) & 255; }
struct WMG { static constexpr int NT = 3, NP = 16, RB = 0, LB = 0, CBI = 0; static constexpr int kind(int, int) { return 0; } };
struct WMD { static constexpr int NT = 4, NP = 12, RB = 0, LB = 0, CBI = 0; static constexpr int kind(int, int) { return 0; } };
namespace lg { constexpr int L_ST = 155648, L_OK = 159744; }
DEVI void role_GU(const Args& a, int w, int j, int two) {
    asm volatile("; MARK role_GU"); __builtin_amdgcn_s_waitcnt(0);
    using namespace lg;
    const int lane = lane_id(), c = lane & 15, g = lane >> 4;
    const rsrc_t rs = mkrs(a.ws, WS_NEED);
    const unsigned lane16 = lane * 16, hc = 96 * j + 24 * w;
    {
        const unsigned ta = g * 4096 + (lane & 1) * 2048 + w * 256 + ((lane >> 2) & 3) * 64 + ((lane >> 1) & 1) * 8, tb = ta + 65536;
        const rsrc_t r1 = mkrs(a.Wg, WSZ(7168u * 1024)), r2 = mkrs(a.Wu, WSZ(7168u * 1024));
        const unsigned cb = 192 * j + g * 48 + (lane & 3) * 16, v1 = (lane & 3) == 3 || cb >= 7168 ? OOB : (8 * w + ((lane >> 2) & 3)) * 7168 + cb, lw = opqs(w * 4096), z = opqs(0);
        load_blocks<32, 8, 4, true>([&](auto B, auto S) { dma_blk(lw + S.v * 16384, r1, v1, z + B.v * 32 * 7168, 1792); dma_blk(lw + S.v * 16384 + 2048, r2, v1, z + B.v * 32 * 7168, 1792); },
                                    [&](auto B, auto S) { sfor<0, 3>([&](auto T) { constexpr int o = (S.v & 3) * 16384 + T.v * 16; put_frag<WMG, T.v, B.v, o, o + 1024>(S.v < 4 ? ta : tb, 0, nullptr); }); });
        if (!two) TSW(120 - 65536, 0);
        v4u one{0x3F803F80u, 0x3F803F80u, 0x3F803F80u, 0x3F803F80u};
        asm volatile("s_nop 1" : "+v"(one));
        f32x4 cs[3];
        sfor<0, 96>([&](auto N) { constexpr int t = N.v % 3, ks = N.v / 3; mfA<8 * widx<WMG>(t, ks >> 1) + 4 * (ks & 1), ks == 0>(cs[t], one); });
        nop<7>(); fence(cs[0], cs[1], cs[2]);
        sfor<0, 3>([&](auto T) { mwr4<384 + 4 * T.v>(__builtin_bit_cast(v4u, cs[T.v])); });
        BAR;
    }
    const unsigned nf = two ? O_N + PG2 + (((two >> 1) ^ FSIDE) & 1) * 8192 : side(rs, FSIDE);
    const unsigned wlo = (unsigned)(uintptr_t)a.ws, whi = (unsigned)((uintptr_t)a.ws >> 32);
    const unsigned xr = w * 8192 + (GROT ? (j & 1) * 4096 + (j & 2) * 16384 : 0);
    const v8u pv{lane16, O_SX + (8 * w + g) * 256 + c * 16, O_H + (g & 1) * 114688 + hc * 32 + c * 16 + (g >> 1) * 8, lane == 0 ? na(nf, N_PG) + j * 128 + w * 4 : OOB,
                 (unsigned)(L_ST + ((lane & 1) * 16 + ((lane & 31) >> 1)) * 16), L_ST + lane16 + w * 4, unit_tab(lane, two), lane16};
    const v16u s0{wlo, whi, RSZ, 0x20000u, wlo, whi, 0u, 0x20000u, OOB, OOB, hc < 3584 ? 0u : OOB, hc + 8 < 3584 ? 0u : OOB, 0u, O_XM + xr, 65536 + xr, 0u};
    const v4u s1{0u, O_PROF + ((blockIdx.x & 255) * 4 + w) * 8192, (unsigned)(two ? N2M : NU - N2M - N2H), (unsigned)(two & 1)};
    asm volatile(G_LOOP :: "{v[0:7]}"(pv), "{s[8:23]}"(s0), "{s[24:27]}"(s1), "{s28}"(two ? N2M + N2H : NU - N2M) : "memory", "s99", "vcc");
}
DEVI void role_DN(const Args& a, int w, int p, int cg, int two) {
    asm volatile("; MARK role_DN"); __builtin_amdgcn_s_waitcnt(0);
    using namespace lg;
    const int lane = lane_id();
    const rsrc_t rs = mkrs(a.ws, WS_NEED);
    const unsigned lane16 = lane * 16;
    const int F0 = p < 3 ? 23 * p : (p == 3 ? 68 : 89), zb = p < 2 ? -1 : (p == 4 ? 0 : 22);
    {
        const unsigned ta = w * 18432 + (lane >> 4) * 512 + ((lane >> 2) & 3) * 64 + (lane & 3) * 16;
        const rsrc_t r1 = mkrs(a.Wd, WSZ(3584u * 2048));
        const unsigned v1 = (F0 * 32 + (lane >> 2)) * 2048 + cg * 512 + w * 128 + (lane & 3) * 16;
        load_blocks<46, 9, 2>([&](auto B, auto S) { dma_blk(w * 18432 + S.v * 2048, r1, (B.v >> 1) == zb ? OOB : v1, (B.v >> 1) * 65536 + (B.v & 1) * 64, 2048); },
                              [&](auto B, auto S) { constexpr int ks = B.v >> 1, t = 2 * (B.v & 1), o = S.v * 2048;
                                                    put_frag<WMD, t, ks, o, o + 256>(ta, 0, nullptr); put_frag<WMD, t + 1, ks, o + 8, o + 264>(ta, 0, nullptr); });
        if (!two) TSW(120 - 65536, 0);
        BAR;
    }
    const int g0 = F0 / 3 * 4, gn = ((F0 + 22) / 3 + 1) * 4 - g0;
    const unsigned nf = two ? O_N + PG2 + (((two >> 1) ^ FSIDE) & 1) * 8192 : side(rs, FSIDE);
    const unsigned wlo = (unsigned)(uintptr_t)a.ws, whi = (unsigned)((uintptr_t)a.ws >> 32);
    const v8u pv{lane16, na(nf, N_PG) + g0 * 32 + (lane < gn ? (lane >> 2) * 128 + (lane & 3) * 4 : 0), lane == 0 ? na(nf ^ PDX, N_PD) + (cg * NKF + p) * 128 + w * 4 : OOB,
                 unit_tab(lane, two), lane16, (unsigned)(L_OK + lane * 4), (unsigned)((lane & 8) * 2048 + (lane >> 4) * 128 + (lane & 7) * 16), 0u};
    const v16u s0{wlo, whi, RSZ, 0x20000u, wlo, whi, 0u, 0x20000u, wlo, whi, w == 0 ? RSZ : 0u, 0x20000u, 0u, O_H + (w >> 1) * 114688 + (F0 + (w & 1) * 12) * 1024,
                  O_D + p * 65536 + (cg * 8 + w * 2) * 512, (unsigned)(65536 + (w >> 1) * 32768 + (w & 1) * 12288)};
    const v4u s1{(unsigned)(L_OK + w * 256), 0u, 0u, O_PROF + ((blockIdx.x & 255) * 4 + w) * 8192};
    asm volatile(D_LOOP :: "{v[0:7]}"(pv), "{s[8:23]}"(s0), "{s[24:27]}"(s1),
                 "{s[28:31]}"(v4u{(unsigned)(two ? N2M : NU - N2M - N2H), O_H + (two & 1) * 114688 + (F0 + 6 * w) * 1024, O_D + p * 65536 + (two & 1) * 32768 + (cg * 8 + w * 2) * 512, (unsigned)(two ? N2M + N2H : NU - N2M)}) : "memory", "s99", "vcc");
}
template <int R> DEVI void ldv1(rsrc_t rs, unsigned vo, unsigned so) { asm volatile("buffer_load_dword v[%3], %0, %1, %2 offen sc1" :: "v"(vo), "s"(rs), "s"(so), "n"(R) : "memory"); }
template <int R, int I> DEVI void unp2(f32x4& d) { float x, y; asm volatile("v_lshlrev_b32 %0, 16, v[%2]\n\tv_and_b32 %1, 0xffff0000, v[%2]" : "=v"(x), "=v"(y) : "n"(R)); d[I] = x; d[I + 1] = y; }
DEVI void role_RED(const Args& a, int w, int cu, unsigned nf) {
    asm volatile("; MARK role_RED"); __builtin_amdgcn_s_waitcnt(0);
    const int lane0 = lane_id(), c = lane0 & 15, g = lane0 >> 4;
    const rsrc_t rs = mkrs(a.ws, WS_NEED);
    v4u sel[2];
#pragma unroll
    for (int e = 0; e < 2; ++e)
#pragma unroll
        for (int i = 0; i < 4; ++i) sel[e][i] = (g == (c >> 2) && i == 2 * e + ((c >> 1) & 1)) ? ((c & 1) ? 0x3F800000u : 0x3F80u) : 0u;
    const unsigned vfl = na(nf ^ PDX, N_PD) + w * (NKF * 128) + (lane0 < 20 ? (lane0 >> 2) * 128 + (lane0 & 3) * 4 : 0);
    constexpr int VP = NV + 32;
    const unsigned lo = (c & 8) * 128 + g * 256 + (c & 7) * 16 + (cu & 1) * 128 + w * 8192, ld = (c & 8) * 64 + g * 128 + (c & 7) * 16 + (cu & 1) * 16384 + w * 4096;
    const unsigned lout = ((cu & 1) * 8 + (c & 7)) * 2048 + w * 512 + (c & 8) * 8 + g * 16, lw = g == (c >> 2) ? (c & 7) * 64 + w * 16 + (c & 8) : 1024 + lane0 * 8;
    const rsrc_t ro = mkrs(a.out, 4u << 20);
    v4u one{0x3F803F80u, 0x3F803F80u, 0x3F803F80u, 0x3F803F80u};
    asm volatile("" : "+v"(one));
    #pragma unroll
    for (int i = 0; i < 2; ++i) if (2 * lane0 + i < NU - N2M) *LDSP(unsigned char, 2048 + unit_ls(2 * lane0 + i, 0)) = 2 * lane0 + i;
    if (lane0 < N2M) *LDSP(unsigned char, 2048 + unit_ls(lane0, 1)) = 128 + lane0;
    WL0; BAR;
    const unsigned tab = opq(*LDSP(const unsigned short, 2048 + 2 * lane0));
    auto place = [&](int k) { return k >= 4 * (NU - N2H) && (k & 2) ? N2M + N2H + (k >> 2) : unit_get(tab, k >> 2); };
    auto next = [&](int k) { return k >= 256 ? k + 32 : k + NRD < 256 ? k + NRD : 4 * NU; };
    auto issue = [&](auto B, auto N0, auto N1, int k) {
        const unsigned ls = k >> 2, base = ((k >> 1) & 1) * 32768;
        sfor<N0.v, N1.v>([&](auto N) { constexpr int f = (N.v - 4) / 5, p = (N.v - 4) % 5;
            if constexpr (N.v < 4) ldv4<NV + 16 * B.v + 4 * N.v>(rs, lo, O_XM + ls * 65536 + base + N.v * 2048);
            else lda<80 * B.v + 20 * f + 4 * p, 4, f * 1024>(rs, ld, O_D + (ls * NKF + p) * 65536 + base); });
        if constexpr (N1.v == 24) { const int k2 = next(k); ldv1<VP>(rs, vfl, k2 < 4 * NU ? (place(k2) >> 7) * PG2 : 0u); }
    };
    u64 late = 1, far = 1;
    bool pre = false;
    auto task = [&](auto B, int k) {
        constexpr int b = B.v;
        const int ut = place(k), u = ut & 127, rt = (k >> 1) & 1, kn = next(k), un = kn < 4 * NU ? place(kn) : 127, nx = (un & 127) + 1;
        const unsigned sf = (ut >> 7) * PG2, sfn = (un >> 7) * PG2;
        const unsigned ls = k >> 2, tile = ls * 2 + rt;
        TSD; TSR(0);
        if (!pre) {
            if (late) {
                unsigned spins = 0;
#pragma nounroll
                for (;;) {
                    if (far) __builtin_amdgcn_s_sleep(PSF); else __builtin_amdgcn_s_sleep(PSR);
                    CB;
                    const int v = (int)ld4(rs, vfl, sf);
                    if (__builtin_amdgcn_sicmp(v, u + 1, 40) == 0) break;
                    far = __builtin_amdgcn_sicmp(v, u, 40);
                    spin_bail(spins);
                }
            }
            TSR(1);
            issue(B, IC<0>{}, IC<24>{}, k);
        }
        else { TSR(1); }
        const bool l0 = ls < 64;
        const rsrc_t rd = l0 ? rs : ro;
        const unsigned vd = opq((l0 ? O_XIN + XB + lo : lout - 64 * 65536) + tile * 32768), fs = l0 ? 2048 : 128;
        v4u x[4];
        f32x4 d[4][2], S, Q;
        auto unp = [&](auto F, auto I) { unp2<NV + 16 * b + 4 * F.v + I.v, 2 * (I.v & 1)>(d[F.v][I.v >> 1]); };
        auto fin = [&](auto F) {
            constexpr int f = F.v;
            fence(d[f][0], d[f][1]);
#pragma unroll
            for (int i = 0; i < 4; ++i) x[f][i] = pack2(d[f][i >> 1][2 * (i & 1)], d[f][i >> 1][2 * (i & 1) + 1]);
            asm volatile("" : "+v"(x[f]));
        };
        auto put = [&](auto F) { constexpr int f = F.v; st16s(x[f], rd, vd, f * fs); SB; mfv<f == 0>(S, one, x[f]); mfv<f == 0>(Q, x[f], x[f]); SB; };
        wvm<24>();
        sfor<0, 4>([&](auto I) { unp(IC<0>{}, I); });
        sfor<0, 4>([&](auto F) {
            constexpr int f = F.v;
            wvm<16 - 5 * f + (f ? f - 1 : 0)>();
            nop<1>();
            sfor<0, 10>([&](auto N) {
                mfB<80 * b + 20 * f + 4 * (N.v >> 1), false>(d[f][N.v & 1], sel[N.v & 1]);
                if constexpr (N.v < 4 && f < 3) unp(IC<(f + 1) & 3>{}, N);
                if constexpr (N.v == 5 && f > 0) fin(IC<(f + 3) & 3>{});
                SB;
            });
            if constexpr (f > 0) put(IC<(f + 3) & 3>{});
        });
        wvm<3>();
        TSR(2);
#ifdef TS
        tsr[4] = pre + 1;
#endif
        const bool p1 = ltv<VP>(nx) == 0;
        if (!p1) ldv1<VP + 1>(rs, vfl, sfn);
        nop<3>();
        fin(IC<3>{});
        nop<1>();
        put(IC<3>{});
        auto second = [&](auto NS) {
            wvm<NS.v>();
            pre = ltv<VP + 1>(nx) == 0;
            if (pre) { issue(IC<1 - b>{}, IC<0>{}, IC<24>{}, kn); if constexpr (NS.v == 5) wvm<25>(); }
            else {
                if constexpr (NS.v == 5) { ldv1<VP + 1>(rs, vfl, sfn); WV0; }
                late = ltv<VP + 1>(nx); far = ltv<VP + 1>(nx - 1);
            }
        };
        if (l0) {
            nop<7>();
            fence(S, Q);
            const float qd = selm(selm(Q[0], Q[1], 0xAAAAAAAAAAAAAAAAull), selm(Q[2], Q[3], 0xAAAAAAAAAAAAAAAAull), 0xCCCCCCCCCCCCCCCCull);
            *LDSP(f32x2, lw) = f32x2{S[0], qd};
            if (p1) issue(IC<1 - b>{}, IC<0>{}, IC<RPA>{}, kn);
            f32x2 xf[4][4];
#pragma unroll
            for (int f = 0; f < 4; ++f)
#pragma unroll
                for (int i = 0; i < 4; ++i) { xf[f][i] = f32x2{bflo(x[f][i]), bfhi(x[f][i])}; asm volatile("" : "+v"(xf[f][i])); }
            TSR(5);
            WL0; BAR;
            f32x4 t[4];
#pragma unroll
            for (int i = 0; i < 4; ++i) t[i] = __builtin_bit_cast(f32x4, ldsq((c & 7) * 64, i * 16));
            const f32x4 t4 = (t[0] + t[1]) + (t[2] + t[3]);
            const float mean = (t4[0] + t4[2]) * (1.0f / 1024.0f), var = fmaxf(rbf(fmaf(t4[1] + t4[3], 1.0f / 1024.0f, -mean * mean)), 0.f);
            const float inv = __builtin_amdgcn_rcpf(rbf(__builtin_amdgcn_sqrtf(rbf(var + 1e-5f)))), mi = -rbf(mean) * inv;
#pragma unroll
            for (int f = 0; f < 4; ++f) {
                v4u o;
#pragma unroll
                for (int i = 0; i < 4; ++i) xf[f][i] = __builtin_elementwise_fma(xf[f][i], f32x2{inv, inv}, f32x2{mi, mi});
#pragma unroll
                for (int i = 0; i < 4; ++i) o[i] = pack2(xf[f][i][0], xf[f][i][1]);
                st16(o, rs, vd + (O_XN - O_XIN) + f * 2048);
            }
            TSR(6);
            if (p1) { issue(IC<1 - b>{}, IC<RPA>{}, IC<24>{}, kn); TSR(7); wvm<25 - RPA>(); pre = true; } else second(IC<5>{});
            BAR;
            st4(0u, rs, lane0 < 2 && w == 0 ? na(nf ^ (FSIDE ? 0 : 8192), N_FX) + tile * 16 + (cu & 1) * 8 + lane0 * 4 : OOB, 0);
        }
        else if (p1) { issue(IC<1 - b>{}, IC<0>{}, IC<24>{}, kn); pre = true; }
        else second(IC<1>{});
        TSR(3); TSF(cu > 255 ? 64 + (k >> 5 & 7) : u, 0xFF);
    };
#pragma nounroll
    for (int k = cu;;) {
        task(IC<0>{}, k); k = next(k);
        if (k >= 4 * NU) break;
        task(IC<1>{}, k); k = next(k);
        if (k >= 4 * NU) break;
    }
}
#ifndef LSW
#define LSW 0
#endif
#ifndef FNG
#define FNG 0x34344776u
#endif
#ifndef FNS
#define FNS 0x00000112u
#endif
#ifndef DLY_A
#define DLY_A 250
#endif
#ifndef DLY_B
#define DLY_B 0
#endif
#ifndef DLY_1
#define DLY_1 1600
#endif
#ifndef DLY_G
#define DLY_G 1400
#endif
#ifndef DLY_D
#define DLY_D 1600
#endif
__global__ __launch_bounds__(256, 1) __attribute__((amdgpu_num_vgpr(NV))) void mega6(Args a) {
    asm volatile("" ::: "v255", "a255");
    const int w = __builtin_amdgcn_readfirstlane(threadIdx.x >> 6), b = blockIdx.x, xcd = b & 7, slot = b >> 3, l = (xcd >> 2) ^ LSW, idx = (slot & 7) * 4 + (xcd & 3);
    const int t0 = (int)__builtin_amdgcn_s_memrealtime();
    if (b < 256) TSW(127, 0);
    const int f = slot - 24, two = b < 256 ? 0 : 1 + ((b >> 8) & 2), k2 = b & 255;
    const int ia = l || !N2M || idx >= 26 ? -1 : idx < 20 ? 38 + (idx & 3) * 5 + (idx >> 2) : 12 + idx, ib = l || !N2M ? -1 : (idx & 3) * 8 + (idx >> 2);
    const int ng = (FNG >> 4 * xcd) & 15, ns = (FNS >> 4 * xcd) & 15, below = (1 << 4 * xcd) - 1;
    auto nib = [](unsigned x) { x = (x & 0x0F0F0F0F) + ((x >> 4) & 0x0F0F0F0F); return (int)((x * 0x01010101) >> 24); };
    int gu = f < 0 || (xcd >= 3 && f >= 4) ? -1 : f < ng ? nib(FNG & below) + f : -2, dn = (xcd - 3) * 4 + f - 4, cu = gu == -2 && f < ng + ns && !two ? nib(FNS & below) + f - ng : -1;
    if (two) { gu = k2 < 38 ? k2 : -1; dn = k2 - 38; }
    if (slot < 24 && l && (slot < 16 || idx >= LN_NOW)) pull_all(a, w, (slot >> 3) * 32 + idx - (slot < 16 ? 0 : LN_NOW), t0);
    if (slot < 8) { hold(t0, l ? DLY_1 : DLY_A); role_A(a, w, l, idx, (xcd & 3) * 2, ia); }
    else if (slot < 16) { hold(t0, l ? DLY_1 : DLY_B); role_B(a, w, l, idx, (xcd & 3) * 2, ib); }
    else if (gu >= 0) {
        const int q = gu * 4 + w;
        if (!two) use(ld4(mkrs(a.ws, WS_NEED), q < 2 * NVK + 2 ? prb(q >> 1, q & 1) + lane_id() * 32 : OOB, 0));
        hold(t0, two ? 0 : DLY_G); role_GU(a, w, gu, two);
    }
    else if (slot < 24 || cu >= 0) {
        unsigned nf;
        if (slot < 24) {
            nf = role_R(a, w, l, idx, t0) ^ (FSIDE * 8192); cu = 256 + idx;
            if (l) { if (idx + w == 0) vote_end(mkrs(a.ws, WS_NEED)); return; }
        } else {
            hold(t0, DLY_D); nf = side(mkrs(a.ws, WS_NEED), FSIDE);
        }
        role_RED(a, w, cu, nf);
    }
    else if (gu == -2) return;
    else { if (xcd < 7 && !two) vote(mkrs(a.ws, WS_NEED), w, dn, l, t0); hold(t0, two ? 0 : DLY_D); role_DN(a, w, dn >> 2, dn & 3, two); }
}
extern "C" void kernel_launch(void* const* d_in, const int* in_sizes, int n_in, void* d_out, int out_size, void* d_ws, size_t ws_size, hipStream_t stream) {
    (void)in_sizes; (void)n_in; (void)out_size; (void)ws_size;
    (void)hipFuncSetAttribute((const void*)mega6, hipFuncAttributeMaxDynamicSharedMemorySize, 163840);
    d_ws = (void*)(((uintptr_t)d_ws + (2u << 20) - 1) & ~(uintptr_t)((2u << 20) - 1));
    Args a{(const u16*)d_in[0], (const u16*)d_in[1], (const u16*)d_in[2], (const u16*)d_in[3], (const u16*)d_in[4], (const u16*)d_in[5],
           (const u16*)d_in[8], (const u16*)d_in[9], (const u16*)d_in[10], (char*)d_ws, (u16*)d_out};
    mega6<<<256, 256, 163840, stream>>>(a);
}
```

```cpp
#include <hip/hip_runtime.h>
#include <stdint.h>

typedef unsigned short u16;
typedef unsigned long long u64;
typedef __attribute__((ext_vector_type(8))) __bf16 bf8;
typedef __attribute__((ext_vector_type(16))) __bf16 bf16v;
typedef __attribute__((ext_vector_type(4))) float f32x4;
typedef __attribute__((ext_vector_type(16))) float f32x16;
typedef __attribute__((ext_vector_type(2))) float f32x2;
typedef __attribute__((ext_vector_type(8))) unsigned v8u;
typedef __attribute__((ext_vector_type(4))) unsigned v4u;
typedef __attribute__((ext_vector_type(3))) unsigned v3u;
typedef __attribute__((ext_vector_type(2))) unsigned v2u;
typedef __amdgpu_buffer_rsrc_t rsrc_t;
typedef __attribute__((address_space(3))) char lds_char;
#define DEVI __device__ __forceinline__
#define LDSP(T, p) ((__attribute__((address_space(3))) T*)(p))

typedef __attribute__((ext_vector_type(2))) __bf16 bf16x2_t;
DEVI unsigned pack2(float lo, float hi) { f32x2 v = {lo, hi}; bf16x2_t b = __builtin_convertvector(v, bf16x2_t); return __builtin_bit_cast(unsigned, b); }
DEVI float bflo(unsigned u) { return __uint_as_float(u << 16); }
DEVI float bfhi(unsigned u) { return __uint_as_float(u & 0xffff0000u); }
DEVI float rbf(float x) { return bflo(pack2(x, x)); }
constexpr int NV = 112;
#define MR_(R, A_STR, V_STR, OUTS, INS, W) do { if constexpr ((R) < 256) asm volatile(A_STR : OUTS : INS, [lo] "n"(R), [hi] "n"((R) + (W) - 1)); \
                                                else asm volatile(V_STR : OUTS : INS, [lo] "n"((R) - 256 + NV), [hi] "n"((R) - 256 + NV + (W) - 1)); } while (0)
#define CM_ ,
template <int R> DEVI void mwr(unsigned x) { if constexpr (R < 256) asm volatile("v_accvgpr_write_b32 a[%1], %0" :: "v"(x), "n"(R)); else asm volatile("v_mov_b32 v[%1], %0" :: "v"(x), "n"(R - 256 + NV)); }
template <int R> DEVI void mwr4(v4u x) { mwr<R>(x[0]); mwr<R + 1>(x[1]); mwr<R + 2>(x[2]); mwr<R + 3>(x[3]); }
#define M16 "v_mfma_f32_16x16x32_bf16 "
#define M32 "v_mfma_f32_32x32x16_bf16 "
#define S16 "v_smfmac_f32_16x16x64_bf16 "
template <int R, bool Z> DEVI void mfA(f32x4& d, v4u b) {
    if constexpr (Z) MR_(R, M16 "%[d], a[%[lo]:%[hi]], %[b], 0", M16 "%[d], v[%[lo]:%[hi]], %[b], 0", [d] "=&v"(d), [b] "v"(b), 4);
    else MR_(R, M16 "%[d], a[%[lo]:%[hi]], %[b], %[d]", M16 "%[d], v[%[lo]:%[hi]], %[b], %[d]", [d] "+v"(d), [b] "v"(b), 4);
}
template <int R, bool Z> DEVI void mfB(f32x4& d, v4u a) {
    if constexpr (Z) MR_(R, M16 "%[d], %[a], a[%[lo]:%[hi]], 0", M16 "%[d], %[a], v[%[lo]:%[hi]], 0", [d] "=&v"(d), [a] "v"(a), 4);
    else MR_(R, M16 "%[d], %[a], a[%[lo]:%[hi]], %[d]", M16 "%[d], %[a], v[%[lo]:%[hi]], %[d]", [d] "+v"(d), [a] "v"(a), 4);
}
template <int R, bool Z> DEVI void mf32A(f32x16& d, v4u b) {
    if constexpr (Z) MR_(R, M32 "%[d], a[%[lo]:%[hi]], %[b], 0", M32 "%[d], v[%[lo]:%[hi]], %[b], 0", [d] "=&v"(d), [b] "v"(b), 4);
    else MR_(R, M32 "%[d], a[%[lo]:%[hi]], %[b], %[d]", M32 "%[d], v[%[lo]:%[hi]], %[b], %[d]", [d] "+v"(d), [b] "v"(b), 4);
}
template <bool Z> DEVI void mfv(f32x4& d, v4u a, v4u b) {
    if constexpr (Z) asm volatile(M16 "%0, %1, %2, 0" : "=&v"(d) : "v"(a), "v"(b)); else asm volatile(M16 "%0, %1, %2, %0" : "+v"(d) : "v"(a), "v"(b));
}
template <int R> DEVI void smf(f32x4& d, v4u a, int idx) { MR_(R, S16 "%[d], %[a], a[%[lo]:%[hi]], %[i]", S16 "%[d], %[a], v[%[lo]:%[hi]], %[i]", [d] "+v"(d), [a] "v"(a) CM_ [i] "v"(idx), 8); }
DEVI void smfv(f32x4& d, v4u a, v8u b, int idx) { asm volatile(S16 "%0, %1, %2, %3" : "+v"(d) : "v"(a), "v"(b), "v"(idx)); }
DEVI void zero4(f32x4& d) { d = f32x4{0.f, 0.f, 0.f, 0.f}; asm volatile("" : "+v"(d)); }
template <int N> DEVI void nop() { asm volatile("s_nop %0" :: "n"(N)); }
DEVI void fence(f32x4& d) { asm volatile("" : "+v"(d)); }
template <class... T> DEVI void fence(f32x4& d, T&... r) { fence(d); fence(r...); }
DEVI float selm(float a, float b, u64 m) { float r; asm("v_cndmask_b32 %0, %1, %2, %3" : "=v"(r) : "v"(a), "v"(b), "s"(m)); return r; }
DEVI int opqs(int x) { asm volatile("" : "+s"(x)); return x; }
DEVI v4u lo4(v8u v) { return __builtin_shufflevector(v, v, 0, 1, 2, 3); }
DEVI v4u hi4(v8u v) { return __builtin_shufflevector(v, v, 4, 5, 6, 7); }
DEVI v8u cat8(v4u a, v4u b) { return __builtin_shufflevector(a, b, 0, 1, 2, 3, 4, 5, 6, 7); }
constexpr int AUX_SC1 = 16;
DEVI v4u ld16(rsrc_t r, unsigned vo, unsigned so) { return __builtin_amdgcn_raw_buffer_load_b128(r, vo, so, AUX_SC1); }
DEVI v2u ld8(rsrc_t r, unsigned vo, unsigned so) { return __builtin_amdgcn_raw_buffer_load_b64(r, vo, so, AUX_SC1); }
DEVI unsigned ld4(rsrc_t r, unsigned vo, unsigned so) { return __builtin_amdgcn_raw_buffer_load_b32(r, vo, so, AUX_SC1); }
DEVI void st16(v4u v, rsrc_t r, unsigned vo) { __builtin_amdgcn_raw_buffer_store_b128(v, r, vo, 0, AUX_SC1); }
DEVI void st16s(v4u v, rsrc_t r, unsigned vo, unsigned so) { __builtin_amdgcn_raw_buffer_store_b128(v, r, vo, so, AUX_SC1); }
DEVI void st8(v2u v, rsrc_t r, unsigned vo, unsigned so) { __builtin_amdgcn_raw_buffer_store_b64(v, r, vo, so, AUX_SC1); }
DEVI void st4(unsigned v, rsrc_t r, unsigned vo, unsigned so) { __builtin_amdgcn_raw_buffer_store_b32(v, r, vo, so, AUX_SC1); }
#define CB asm volatile("" ::: "memory")
#define WL0 asm volatile("s_waitcnt lgkmcnt(0)" ::: "memory")
#define WV0 asm volatile("s_waitcnt vmcnt(0)" ::: "memory")
#define BAR do { __builtin_amdgcn_s_barrier(); CB; } while (0)
#define SB __builtin_amdgcn_sched_barrier(0)
DEVI float fadd(float a, float b) { float r; asm("v_add_f32 %0, %1, %2" : "=v"(r) : "v"(a), "v"(b)); return r; }
DEVI float vfma(float a, float b, float c) { float r; asm("v_fma_f32 %0, %1, %2, %3" : "=v"(r) : "v"(a), "v"(b), "v"(c)); return r; }
DEVI unsigned vxor(unsigned a, unsigned b) { unsigned r; asm volatile("v_xor_b32 %0, %1, %2" : "=v"(r) : "i"(b), "v"(a)); return r; }
DEVI int lane_id() { int r; asm volatile("v_mbcnt_lo_u32_b32 %0, -1, 0\n\tv_mbcnt_hi_u32_b32 %0, -1, %0" : "=v"(r)); __builtin_assume(r >= 0 && r < 64); return r; }
DEVI int opq(int x) { asm volatile("" : "+v"(x)); return x; }
DEVI float vmaxf(float a, float b) { float r; asm("v_max_f32 %0, %1, %2" : "=v"(r) : "v"(a), "v"(b)); return r; }
DEVI void swap32(float& a, float& b) { auto r = __builtin_amdgcn_permlane32_swap(__float_as_uint(a), __float_as_uint(b), false, false); a = __uint_as_float(r[0]); b = __uint_as_float(r[1]); }
DEVI void swap32u(unsigned& a, unsigned& b) { auto r = __builtin_amdgcn_permlane32_swap(a, b, false, false); a = r[0]; b = r[1]; }
DEVI void swap16u(unsigned& a, unsigned& b) { auto r = __builtin_amdgcn_permlane16_swap(a, b, false, false); a = r[0]; b = r[1]; }
DEVI float hadd(float a, float b) { swap32(a, b); return fadd(a, b); }
DEVI float red32(float x) { float y = x; swap32(x, y); return x + y; }
DEVI float red16(float x) { unsigned a = __float_as_uint(x), b = a; swap16u(a, b); return __uint_as_float(a) + __uint_as_float(b); }
DEVI float mx32(float x) { float y = x; swap32(x, y); return vmaxf(x, y); }
DEVI float mx16(float x) { unsigned a = __float_as_uint(x), b = a; swap16u(a, b); return vmaxf(__uint_as_float(a), __uint_as_float(b)); }
template <int N> struct IC { static constexpr int v = N; };
template <int I, int N, class F> DEVI void sfor(F&& f) { if constexpr (I < N) { f(IC<I>{}); sfor<I + 1, N>(f); } }
#ifndef NSEG
#define NSEG 64
#endif
#ifndef LAGD
#define LAGD 10
#endif
constexpr float SC_L2E = 0.08838834764831845f * 1.4426950408889634f;
constexpr unsigned SENT = 0xFFFFFFFFu;
#ifndef NUS_
#define NUS_ 8
#endif
#ifndef NRG_
#define NRG_ 8
#endif
#ifndef N2H
#define N2H 3
#endif
#ifndef NC
#define NC 8
#endif
constexpr int NREP = 8, NUS = NUS_, NRG = NRG_, NKF = 5, NGU = 38;
constexpr unsigned XB = 4u << 20;
constexpr unsigned O_XIN = 0;
constexpr unsigned O_XN  = O_XIN + 2 * XB;
constexpr unsigned O_XM  = O_XN + 2 * XB;
constexpr unsigned O_H   = O_XM + 2 * XB;
constexpr unsigned O_D   = O_H + 2u * 64 * 224 * 1024;
constexpr unsigned O_N = O_D + 2u * 64 * NKF * 65536;
constexpr unsigned DV(unsigned o) { return ((o & ~8191u) << 1) | (o & 8191u); }
DEVI unsigned na(unsigned nb, unsigned o) { return (DV(o) + nb) ^ ((o >> 7) & 8192u); }
constexpr unsigned N_PM  = 0;
constexpr unsigned N_PMM = N_PM + NRG * 16384;
constexpr unsigned N_PA  = (1u << 20) - 16384;
constexpr unsigned N_MEM = 1u << 20;
constexpr unsigned N_WM  = 2u << 20;
constexpr unsigned N_PTX = 6u << 20;
constexpr unsigned N_U   = 10u << 20;
static_assert(NRG == 8 && NUS == 8 && NREP == 8 && O_N % (4u << 20) == 0, "");
constexpr unsigned O_FLG_END = O_N + DV(N_MEM + NREP * 16384);
constexpr unsigned N_FX  = N_PA - 8192;
constexpr unsigned N_PG  = N_FX - 8192;
constexpr unsigned N_PD  = N_PG + 40 * 128;
constexpr unsigned PG2 = DV(N_PA + 8192 - N_PG);
constexpr unsigned O_PRB = O_N + (512u << 10), O_CAL = O_N + (768u << 10);
constexpr unsigned O_SX  = O_N + DV(N_PG) - 2u * 64 * 32 * 256;
constexpr unsigned O_FLG = O_SX;
static_assert(O_PRB >= O_N + DV(N_PMM + NRG * 8192) && O_FLG >= O_CAL + 8192 && (O_FLG_END - O_FLG) % 65536 == 0, "");
constexpr unsigned OOB = 0x80000000u;
constexpr unsigned WS_NEED = O_N + DV(N_U + NUS * (2u << 20));
constexpr unsigned O_PROF = 240u << 20;
static_assert(WS_NEED <= (200u << 20), "workspace too large");

#ifdef DRY
#define WSZ(x) 0u
#else
#define WSZ(x) (x)
#endif
struct Args { const u16 *x, *mem0, *Wq, *Wk, *Wv, *Wo, *Wg, *Wu, *Wd; char* ws; u16* out; };
#ifndef SPIN_LOG
#define SPIN_LOG 18
#endif
constexpr unsigned SPIN_CAP = 1u << SPIN_LOG;
DEVI void spin_bail(unsigned& spins) {
    asm volatile("s_add_u32 %0, %0, 1\n\ts_cmp_lt_u32 %0, %1\n\ts_cbranch_scc1 1f\n\ts_endpgm\n1:" : "+s"(spins) : "n"(SPIN_CAP) : "scc");
}
template <int K> DEVI void wrl(unsigned& v, u64& t) { asm volatile("" : "+s"(t)); asm volatile("v_writelane_b32 %0, %1, %2" : "+v"(v) : "s"((unsigned)t), "n"(K)); }
#ifdef TS
#ifndef TSCLK
#define TSCLK __builtin_amdgcn_s_memrealtime
#endif
#define TSV(u, i, v) __builtin_amdgcn_raw_buffer_store_b32((unsigned)(v), mkrs(a.ws, 256u << 20), lane_id() == 0 ? O_PROF + ((blockIdx.x * 4 + w) * 128 + (u)) * 64 + (i) * 4 : OOB, 0, 0)
#define TSW(u, i) TSV(u, i, TSCLK())
#define TSC(u, i) TSV(u, i, __builtin_amdgcn_s_memtime())
#define TSD u64 tsr[16] = {}
#define TSR(k) tsr[k] = rclk()
#define TSK(k) asm volatile("s_memtime %0" : "=s"(tsr[k]))
#define TSF(u, m) do { unsigned tv_ = 0; WL0; sfor<0, 16>([&](auto K) { if constexpr (((m) >> K.v) & 1) wrl<K.v>(tv_, tsr[K.v]); }); \
    const int tl_ = lane_id(); __builtin_amdgcn_raw_buffer_store_b32(tv_, mkrs(a.ws, 256u << 20), (tl_ < 16 && (((m) >> tl_) & 1)) ? O_PROF + ((blockIdx.x * 4 + w) * 128 + (u)) * 64 + tl_ * 4 : OOB, 0, 0); } while (0)
#if TS == 4
#undef TSV
#undef TSR
#undef TSK
#define TSV(u, i, v)
#define TSR(k)
#define TSK(k)
#define FSK(G, O, D) if constexpr ((G) >= (O) && ((G) - (O)) % (D) == 0 && ((G) - (O)) / (D) < FN) asm volatile("s_memtime %0" : "=s"(tsr[((G) - (O)) / (D)]))
#define FSF(u) TSF(u, (1 << FN) - 1)
#define FSD TSD
#endif
#else
#define TSW(u, i)
#define TSC(u, i)
#define TSV(u, i, v)
#define TSD
#define TSR(k)
#define TSK(k)
#define TSF(u, m)
#endif
#if TS != 4
#define FSK(G, O, D)
#define FSF(u)
#define FSD
#endif
DEVI unsigned rslot(int s) { return (unsigned)(s & (NRG - 1)); }
DEVI u64 pend(v4u d) { return __builtin_amdgcn_uicmp(max(d[0], d[2]), SENT, 32); }
DEVI u64 pend2(v2u d) { return __builtin_amdgcn_uicmp(d[0], SENT, 32); }
DEVI u64 pend1(unsigned d) { return __builtin_amdgcn_uicmp(d, SENT, 32); }
DEVI unsigned nosent(unsigned u) { return min(u, 0xFFFFFFFEu); }
#ifndef WPOL
#define WPOL ""
#endif
template <int OFF, bool SC1> DEVI void dma1(rsrc_t rs, unsigned vo, unsigned so) {
    if constexpr (SC1) asm volatile("buffer_load_dwordx4 %0, %1, %2 offen offset:%3 sc1 lds" :: "v"(vo), "s"(rs), "s"(so), "n"(OFF) : "memory");
    else asm volatile("buffer_load_dwordx4 %0, %1, %2 offen offset:%3 " WPOL " lds" :: "v"(vo), "s"(rs), "s"(so), "n"(OFF) : "memory");
}
DEVI void setm0(unsigned v) { asm volatile("s_mov_b32 m0, %0\n\ts_nop 0" :: "s"(v) : "memory"); }
DEVI void dma4(unsigned lds, rsrc_t rs, unsigned vo, unsigned so) { setm0(lds); dma1<0, true>(rs, vo, so); dma1<1024, true>(rs, vo, so); dma1<2048, true>(rs, vo, so); dma1<3072, true>(rs, vo, so); }
DEVI void dmaw(unsigned lds, rsrc_t rs, unsigned vo, unsigned so) { setm0(lds); asm volatile("buffer_load_dword %0, %1, %2 offen sc1 lds" :: "v"(vo), "s"(rs), "s"(so) : "memory"); }
template <int N> DEVI void wvm() { asm volatile("s_waitcnt vmcnt(%0)" :: "n"(N) : "memory"); }
template <int N> DEVI void wlg() { asm volatile("s_waitcnt lgkmcnt(%0)" :: "n"(N) : "memory"); }
DEVI void dma4p(u64 c0, u64 c1, u64 c2, u64 c3, rsrc_t rs, unsigned vo, unsigned so) {
    asm volatile(
        "s_cmp_eq_u64 %[c0], 0\n\ts_cbranch_scc1 1f\n\t"
        "buffer_load_dwordx4 %[vo], %[rs], %[so] offen sc1 lds\n"
        "1:\n\ts_cmp_eq_u64 %[c1], 0\n\ts_cbranch_scc1 2f\n\t"
        "buffer_load_dwordx4 %[vo], %[rs], %[so] offen offset:1024 sc1 lds\n"
        "2:\n\ts_cmp_eq_u64 %[c2], 0\n\ts_cbranch_scc1 3f\n\t"
        "buffer_load_dwordx4 %[vo], %[rs], %[so] offen offset:2048 sc1 lds\n"
        "3:\n\ts_cmp_eq_u64 %[c3], 0\n\ts_cbranch_scc1 4f\n\t"
        "buffer_load_dwordx4 %[vo], %[rs], %[so] offen offset:3072 sc1 lds\n"
        "4:"
        :: [c0] "s"(c0), [c1] "s"(c1), [c2] "s"(c2), [c3] "s"(c3), [vo] "v"(vo), [rs] "s"(rs), [so] "s"(so)
        : "memory", "scc");
}
#ifndef PSL
#define PSL 8
#endif
DEVI void wait_prog(rsrc_t rs, unsigned vo, unsigned so, int need) {
    unsigned spins = 0;
#pragma nounroll
    for (;;) {
        CB;
        const int v = (int)ld4(rs, vo, so);
        if (__builtin_amdgcn_sicmp(v, need, 40) == 0) return;
        spin_bail(spins);
        __builtin_amdgcn_s_sleep(PSL);
    }
}

typedef __attribute__((ext_vector_type(16))) int v16i;
typedef __attribute__((ext_vector_type(16))) unsigned v16u;
#ifdef TS
constexpr unsigned RSZ = 256u << 20;
#else
constexpr unsigned RSZ = WS_NEED;
#endif
DEVI u64 rclk() { u64 t; asm volatile("s_memrealtime %0" : "=s"(t)); return t; }
DEVI unsigned tsv(u64& t) { asm volatile("s_waitcnt lgkmcnt(0)" : "+s"(t)); return (unsigned)t; }
DEVI unsigned stamp(u64& t, int tag, int lead) { asm volatile("" : "+s"(t)); return (unsigned)tag << 24 | (((unsigned)t + lead) & 0xFFFFFFu); }
#ifndef SXTH
#define SXTH 20
#endif
DEVI int left(int t) { return (int)((unsigned)(t - (int)__builtin_amdgcn_s_memrealtime()) << 8); }
DEVI void hold(int t0, int d) { for (int n = 0; n < 4096 && (int)__builtin_amdgcn_s_memrealtime() - t0 < d; ++n) __builtin_amdgcn_s_sleep(4); }
DEVI int smin(int a, int b) { int r; asm("s_min_i32 %0, %1, %2" : "=s"(r) : "s"(a), "s"(b) : "scc"); return r; }
DEVI int smax(int a, int b) { int r; asm("s_max_i32 %0, %1, %2" : "=s"(r) : "s"(a), "s"(b) : "scc"); return r; }
DEVI void smin8x2(const v16i& f, int& m0, int& m1) {
    asm("s_min_i32 %0, %2, %3\n\ts_min_i32 %1, %10, %11\n\ts_min_i32 %0, %0, %4\n\ts_min_i32 %1, %1, %12\n\ts_min_i32 %0, %0, %5\n\ts_min_i32 %1, %1, %13\n\ts_min_i32 %0, %0, %6\n\ts_min_i32 %1, %1, %14\n\t"
        "s_min_i32 %0, %0, %7\n\ts_min_i32 %1, %1, %15\n\ts_min_i32 %0, %0, %8\n\ts_min_i32 %1, %1, %16\n\ts_min_i32 %0, %0, %9\n\ts_min_i32 %1, %1, %17"
        : "=&s"(m0), "=&s"(m1) : "s"(f[0]), "s"(f[1]), "s"(f[2]), "s"(f[3]), "s"(f[4]), "s"(f[5]), "s"(f[6]), "s"(f[7]),
          "s"(f[8]), "s"(f[9]), "s"(f[10]), "s"(f[11]), "s"(f[12]), "s"(f[13]), "s"(f[14]), "s"(f[15]) : "scc");
}
DEVI void smax8x2(const v16i& f, int& m0, int& m1) {
    asm("s_max_i32 %0, %2, %3\n\ts_max_i32 %1, %10, %11\n\ts_max_i32 %0, %0, %4\n\ts_max_i32 %1, %1, %12\n\ts_max_i32 %0, %0, %5\n\ts_max_i32 %1, %1, %13\n\ts_max_i32 %0, %0, %6\n\ts_max_i32 %1, %1, %14\n\t"
        "s_max_i32 %0, %0, %7\n\ts_max_i32 %1, %1, %15\n\ts_max_i32 %0, %0, %8\n\ts_max_i32 %1, %1, %16\n\ts_max_i32 %0, %0, %9\n\ts_max_i32 %1, %1, %17"
        : "=&s"(m0), "=&s"(m1) : "s"(f[0]), "s"(f[1]), "s"(f[2]), "s"(f[3]), "s"(f[4]), "s"(f[5]), "s"(f[6]), "s"(f[7]),
          "s"(f[8]), "s"(f[9]), "s"(f[10]), "s"(f[11]), "s"(f[12]), "s"(f[13]), "s"(f[14]), "s"(f[15]) : "scc");
}
#ifndef NVK
#define NVK 8
#endif
#ifndef DLY_V
#define DLY_V 500
#endif
#ifndef PSV
#define PSV 12
#endif
#ifndef DLY_P
#define DLY_P 150
#endif
DEVI unsigned prb(int k, int b) { return O_PRB + k * 16384 + b * 8192 + (k & 1) * 4096; }
DEVI void vote(rsrc_t rs, int w, int v, int far, int t0s) {
    const int lane = lane_id();
    constexpr unsigned N = (O_FLG_END - O_FLG) / 65536;
#pragma nounroll
    for (unsigned q = 0; q < N; ++q) st16(v4u{SENT, SENT, SENT, SENT}, rs, O_FLG + ((v * 4 + w) * N + q) * 1024 + lane * 16);
    const v2u e = ld8(rs, O_CAL + (lane & 31) * 256 + 128, 0);
    static_assert(NVK % 4 == 0 && NVK < 12, "");
    int best[2] = {1 << 30, 1 << 30};
    hold(t0s, DLY_P);
#pragma nounroll
    for (int k = 0; k <= NVK / 4; ++k)
#pragma unroll
        for (int b = 0; b < 2; ++b) {
            u64 t0, t1; int d;
            asm volatile("s_memtime %0\n\ts_waitcnt lgkmcnt(0)\n\ts_buffer_load_dword %2, %3, %4 glc\n\ts_waitcnt lgkmcnt(0)\n\ts_memtime %1\n\ts_waitcnt lgkmcnt(0)"
                         : "=&s"(t0), "=&s"(t1), "=&s"(d) : "s"(rs), "s"(prb(k ? 4 * k - w : 0, b) + v * 128) : "memory");
            if (k) best[b] = smin(best[b], (int)(t1 - t0));
        }
    *LDSP(int, (lane & 1) * 16 + w * 4) = best[lane & 1];
    WV0; WL0; BAR;
#pragma unroll
    for (int b = 0; b < 2; ++b) { const v4u t = *LDSP(const v4u, b * 16); best[b] = min(min(t[0], t[1]), min(t[2], t[3])); }
    st8(v2u{((e[0] + 2) & ~1u) | ((best[1] < best[0]) ^ far), e[1] + 1}, rs, lane < 32 && w == 0 ? O_CAL + lane * 256 + v * 8 : OOB, 0);
}
DEVI void vote_end(rsrc_t rs) {
    const unsigned o = O_CAL + (lane_id() & 31) * 256 + 128;
    const v2u e = ld8(rs, o, 0);
    st8(v2u{e[0] + 2, e[1] + 1}, rs, o, 0);
}
DEVI unsigned side(rsrc_t rs, int l) {
    unsigned spins = 0;
#pragma nounroll
    for (;;) {
        v16u f[2]; v2u e;
        const unsigned o = O_CAL + (blockIdx.x & 31) * 256;
        asm volatile("s_buffer_load_dwordx16 %0, %3, %4 glc\n\ts_buffer_load_dwordx16 %1, %3, %5 glc\n\ts_buffer_load_dwordx2 %2, %3, %6 glc\n\ts_waitcnt lgkmcnt(0)"
                     : "=&s"(f[0]), "=&s"(f[1]), "=&s"(e) : "s"(rs), "s"(o), "s"(o + 64), "s"(o + 128) : "memory");
        unsigned bad = 0, sum = 0;
#pragma unroll
        for (int i = 0; i < 16; ++i) { const unsigned x = f[i >> 3][2 * i & 15]; bad |= ((x ^ (e[0] + 2)) >> 1) | (f[i >> 3][(2 * i & 15) + 1] ^ (e[1] + 1)); sum += x & 1; }
#ifdef CSIDE
        sum = CSIDE * 16;
#endif
        if (bad == 0) return opqs(O_N + ((l ^ (sum > 8)) & 1) * 8192);
        spin_bail(spins);
        __builtin_amdgcn_s_sleep(PSV);
    }
}
#ifndef PSH
#define PSH 2
#endif
DEVI int wait_hint(rsrc_t rs, unsigned so, unsigned vo, int tag) {
#pragma nounroll
    for (int n = 0;; ++n) {
        v16i f;
        asm volatile("s_buffer_load_dwordx16 %0, %1, %2 glc\n\ts_waitcnt lgkmcnt(0)" : "=s"(f) : "s"(rs), "s"(so) : "memory");
        int m0, m1;
        smin8x2(f, m0, m1);
        if (smin(m0, m1) >= tag << 24) { smax8x2(f, m0, m1); return smax(m0, m1); }
        if (n == 40) { wait_prog(rs, vo, 0, tag << 24); n = 0; }
        __builtin_amdgcn_s_sleep(PSH);
    }
}
template <class WM> constexpr int widx(int j, int t) {
    int n = 0;
    for (int q = 0; q < j * WM::NP + t; ++q) n += WM::kind(q / WM::NP, q % WM::NP) == WM::kind(j, t);
    return n;
}
template <class WM> constexpr int lds_tile(int t) { for (int j = 0; j < WM::NT; ++j) if (WM::kind(j, t) == 1) return j; return -1; }
DEVI v4u ldsq(unsigned p, int off) { return *LDSP(const v4u, LDSP(char, p) + off); }
DEVI void stsq(unsigned p, int off, v4u v) { *LDSP(v4u, LDSP(char, p) + off) = v; }
DEVI v8u ldpair(unsigned wl, int slot) { return cat8(ldsq(wl, slot * 2048), ldsq(wl, slot * 2048 + 1024)); }
DEVI rsrc_t mkrs(const void* p, unsigned bytes) { return __builtin_amdgcn_make_buffer_rsrc((void*)p, 0, (int)bytes, 0x00020000); }
#ifndef NPL
#define NPL 72
#endif
#ifndef LNN
#define LNN 8
#endif
constexpr int LN_NOW = LNN, NP0 = NPL * 4;
template <int SL = 0> DEVI void pull(const void* p, int n, int q, int nq) {
    const rsrc_t r = mkrs(p, n * 1024);
    const unsigned vo = lane_id() * 16;
#pragma nounroll
    for (; q < n; q += nq) { asm volatile("buffer_load_dwordx4 a[0:3], %0, %1, %2 offen" :: "v"(vo), "s"(r), "s"(q * 1024) : "memory"); if (SL) __builtin_amdgcn_s_sleep(SL); }
}
#ifndef PQ
#define PQ 0
#endif
#ifndef DLY_F
#define DLY_F 1000
#endif
#ifndef PFS
#define PFS 5
#endif
DEVI void pull_all(const Args& a, int w, int q, int t0) {
    q = q * 4 + w;
    if (q < NP0) {
    pull(a.Wv, 8192, q, NP0); pull(a.Wo, 8192, q, NP0); if (!PQ) pull(a.Wq, 8192, q, NP0); pull(a.Wk, 8192, q, NP0);
    }
    if (q >= 256) return;
    hold(t0, DLY_F);
    pull<PFS>(q & 1 ? a.Wg : a.Wu, 7168, q >> 1, 128); pull<PFS>(a.Wd, 7168, q, 256);
}
template <class WM, int j, int ks, int O0, int O1> DEVI void put_frag(unsigned ta, unsigned wl, v8u* Wc) {
    constexpr int t = ks >> 1, f = ks & 1, k = WM::kind(j, t), ix = widx<WM>(j, t);
    if constexpr (k == 0) {
        constexpr int R = WM::RB + 8 * ix + 4 * f;
        if constexpr (R < 256) asm volatile("ds_read_b64_tr_b16 a[%1:%2], %0 offset:%5\n\tds_read_b64_tr_b16 a[%3:%4], %0 offset:%6" :: "v"(ta), "n"(R), "n"(R + 1), "n"(R + 2), "n"(R + 3), "n"(O0), "n"(O1) : "memory");
        else asm volatile("ds_read_b64_tr_b16 v[%1:%2], %0 offset:%5\n\tds_read_b64_tr_b16 v[%3:%4], %0 offset:%6" :: "v"(ta), "n"(R - 256 + NV), "n"(R - 255 + NV), "n"(R - 254 + NV), "n"(R - 253 + NV), "n"(O0), "n"(O1) : "memory");
    } else {
        v2u p, q;
        asm volatile("ds_read_b64_tr_b16 %0, %2 offset:%3\n\tds_read_b64_tr_b16 %1, %2 offset:%4\n\ts_waitcnt lgkmcnt(0)" : "=&v"(p), "=&v"(q) : "v"(ta), "n"(O0), "n"(O1) : "memory");
        const v4u F = v4u{p[0], p[1], q[0], q[1]};
        if constexpr (k == 1) stsq(wl, (WM::LB + ix) * 2048 + f * 1024, F);
        else if constexpr (f == 0) Wc[WM::CBI + ix] = cat8(F, F);
        else Wc[WM::CBI + ix] = cat8(lo4(Wc[WM::CBI + ix]), F);
    }
}
template <int NB, int DEPTH, int NI, bool SHARED = false, int NX = 0, class ISSUE, class PUT>
DEVI void load_blocks(ISSUE&& issue, PUT&& put) {
    sfor<0, DEPTH - 1>([&](auto B) { issue(B, IC<B.v % DEPTH>{}); });
    sfor<0, NB>([&](auto B) {
        constexpr int b = B.v, left = NB + NX - 1 - b < DEPTH - 2 ? NB + NX - 1 - b : DEPTH - 2;
        wvm<NI * left>();
        wlg<0>();
        if constexpr (SHARED) BAR;
        if constexpr (b + DEPTH - 1 < NB + NX) issue(IC<b + DEPTH - 1>{}, IC<(b + DEPTH - 1) % DEPTH>{});
        put(B, IC<b % DEPTH>{});
    });
    wlg<0>();
    if constexpr (SHARED) BAR;
}
DEVI void first_look(unsigned& fx, rsrc_t rs, unsigned nb) { asm volatile("buffer_load_dword %0, %1, %2, %3 offen sc1" : "=v"(fx) : "v"((lane_id() & 7) * 4), "s"(rs), "s"(na(nb, N_FX)) : "memory"); }
DEVI unsigned first_there(unsigned& fx) { asm volatile("" : "+v"(fx)); return pend1(fx) == 0 ? WS_NEED : 0u; }
DEVI void dma_blk(unsigned lds, rsrc_t rw, unsigned vo, unsigned so, unsigned rowb) { setm0(lds); dma1<0, false>(rw, vo, so); setm0(lds + 1024); dma1<0, false>(rw, vo, so + 16 * rowb); }
template <class WM, int NKS, int K0, int K1, int RTS, int LATE1, class FILL>
DEVI void gemm_pass(unsigned xa, unsigned wl, const v8u* Wc, v4u (&x)[2][3], f32x4 (&acc)[WM::NT][2], FILL&& fill) {
    auto rd = [&](auto KS, auto Q) {
        constexpr int ks = KS.v, q = Q.v, jl = lds_tile<WM>(ks >> 1), il = widx<WM>(jl < 0 ? 0 : jl, ks >> 1);
        if constexpr (q == 0 && jl >= 0) x[ks & 1][2] = ldsq(wl, (WM::LB + il) * 2048 + (ks & 1) * 1024);
        if constexpr (q == 1) x[ks & 1][1] = ldsq(xa, RTS + ks * 1024);
        if constexpr (q == 2) x[ks & 1][0] = ldsq(xa, ks * 1024);
    };
    sfor<K0 * 2 * WM::NT, K1 * 2 * WM::NT>([&](auto N) {
        constexpr int ks = N.v / (2 * WM::NT), n = N.v % (2 * WM::NT), j = n >> 1, rt = n & 1, t = ks >> 1, f = ks & 1, k = WM::kind(j, t), ix = widx<WM>(j, t);
        fill(IC<ks>{}, IC<n>{});
        SB;
        const v4u xv = x[ks & 1][rt];
        if constexpr (k == 0) mfA<WM::RB + 8 * ix + 4 * f, ks == 0>(acc[j][rt], xv);
        else if constexpr (k == 1) mfv<ks == 0>(acc[j][rt], x[ks & 1][2], xv);
        else mfv<ks == 0>(acc[j][rt], f ? hi4(Wc[WM::CBI + ix]) : lo4(Wc[WM::CBI + ix]), xv);
        if constexpr (ks + 1 < NKS) rd(IC<ks + 1>{}, IC<n - (ks == 1 ? LATE1 : 0)>{});
        SB;
    });
}
template <class WM> constexpr int next_lds(int n) { for (; n < 16 * WM::NT; ++n) if (WM::kind(2 * (n >> 5) + (n & 1), (n >> 1) & 15) == 1) return n; return -1; }
template <class WM> constexpr int lds_cnt(int n) { int c = 0; for (int q = next_lds<WM>(0); q >= 0 && q < n; q = next_lds<WM>(q + 1)) ++c; return c; }
template <class WM> constexpr int lds_nth(int k) { int q = next_lds<WM>(0); for (; k > 0 && q >= 0; --k) q = next_lds<WM>(q + 1); return q; }
template <class WM, int K, int NB> DEVI void pair_next(v8u (&pb)[NB], unsigned wl) {
    constexpr int n = lds_nth<WM>(K);
    if constexpr (n >= 0) { constexpr int ix = widx<WM>(2 * (n >> 5) + (n & 1), (n >> 1) & 15); pb[K % NB] = ldpair(wl, WM::LB + ix); }
}
template <class WM, int MO, int MR, int NB, class FILL>
DEVI void spass(unsigned ma, unsigned wl, const v8u* Wc, f32x4* e, v4u (&m)[MR], v8u (&pb)[NB], int sidx, FILL&& fill) {
    sfor<0, 16 * WM::NT>([&](auto N) {
        constexpr int n = N.v, t = (n >> 1) & 15, j = 2 * (n >> 5) + (n & 1), k = WM::kind(j, t), ix = widx<WM>(j, t), tn = (t + MR - 1) & 15, r = (n >> 1) % MR;
        fill(N);
        SB;
        if constexpr (k == 0) smf<WM::RB + 8 * ix>(e[j], m[r], sidx);
        else if constexpr (k == 1) { constexpr int q = lds_cnt<WM>(n); smfv(e[j], m[r], pb[q % NB], sidx); pair_next<WM, q + NB>(pb, wl); }
        else smfv(e[j], m[r], Wc[WM::CBI + ix], sidx);
        if constexpr ((n & 1) == 0 && n + 2 * MR - 2 < 16 * WM::NT) m[(r + MR - 1) % MR] = ldsq(ma, MO + (tn >> 3) * 32768 + (tn & 7) * 1024);
        SB;
    });
}
typedef __attribute__((ext_vector_type(4))) short s16x4;
DEVI v2u ldtr(unsigned p, int off) { return __builtin_bit_cast(v2u, __builtin_amdgcn_ds_read_tr16_b64_v4i16(LDSP(s16x4, LDSP(char, p) + off))); }
DEVI v4u ldtr2(unsigned p, int off) { const v2u a = ldtr(p, off), b = ldtr(p, off + 256); return v4u{a[0], a[1], b[0], b[1]}; }
DEVI void sts1(unsigned p, int off, unsigned v) { *LDSP(unsigned, LDSP(char, p) + off) = v; }
DEVI unsigned lds1(unsigned p, int off) { return *LDSP(const unsigned, LDSP(char, p) + off); }
DEVI void use(unsigned v) { asm volatile("" :: "v"(v) : "memory"); }
DEVI int mk_sidx(int lane) { return opq(0x4444 + (lane & 2) * 0x5555); }
DEVI int crow(int r, int hb) { return 4 * (r >> 1) + 2 * hb + (r & 1); }
DEVI v4u again(int id, unsigned nb, int w) { const u64 k = (u64)__builtin_amdgcn_kernarg_segment_ptr(); return v4u{(unsigned)k, (unsigned)(k >> 32), id < 0 ? ~0u : 256 + id + ((nb >> 4) & 512), (unsigned)w * 64}; }
#define A_LDS 0x28a0114002004410ull
#define A_VAR 0x0000000400020001ull
#define A_LOOP \
"s_brev_b32 s99, 1\ns_brev_b32 s98, 1\ns_brev_b32 s97, 1\ns_brev_b32 s96, 1\n" \
"s_branch 4f\n.p2align 6\n1:\ns_waitcnt lgkmcnt(0)\n" \
"v_mfma_f32_16x16x32_bf16 v[44:47], v[76:79], v[80:83], v[44:47]\nds_read_b128 v[36:39], v25 offset:3072\nds_read_b128 v[40:43], v24 offset:21504\nv_mfma_f32_16x16x32_bf16 v[48:51], v[76:79], v[32:35], v[48:51]\n" \
"ds_read_b128 v[84:87], v24 offset:54272\nv_bfe_u32 v31, v24, 6, 3\nv_mfma_f32_16x16x32_bf16 v[52:55], a[160:163], v[80:83], v[52:55]\ns_mov_b32 m0, s50\n" \
"s_add_u32 s99, s30, 2\nv_lshlrev_b32 v31, 2, v31\nv_mfma_f32_16x16x32_bf16 v[56:59], a[160:163], v[32:35], v[56:59]\ns_min_u32 s99, s99, 63\n" \
"s_lshl_b32 s99, s99, 5\ns_add_u32 s99, s99, s48\nv_mfma_f32_16x16x32_bf16 v[60:63], v[120:123], v[80:83], v[60:63]\ns_and_b32 s94, s30, 7\n" \
"s_lshl_b32 s94, s94, 18\ns_add_u32 s94, s94, s33\nv_mfma_f32_16x16x32_bf16 v[64:67], v[120:123], v[32:35], v[64:67]\ns_add_u32 s31, s31, 0x10000\n" \
"v_mfma_f32_16x16x32_bf16 v[68:71], v[224:227], v[80:83], v[68:71]\ns_waitcnt lgkmcnt(1)\nv_mfma_f32_16x16x32_bf16 v[72:75], v[224:227], v[32:35], v[72:75]\ns_waitcnt lgkmcnt(0)\n" \
"ds_read_b128 v[32:35], v24 offset:22528\nv_mfma_f32_16x16x32_bf16 v[44:47], v[36:39], v[40:43], v[44:47]\nds_read_b128 v[76:79], v24 offset:55296\nv_mfma_f32_16x16x32_bf16 v[48:51], v[36:39], v[84:87], v[48:51]\n" \
"v_mfma_f32_16x16x32_bf16 v[52:55], a[164:167], v[40:43], v[52:55]\nds_read_b128 v[36:39], v25 offset:18432\nv_mfma_f32_16x16x32_bf16 v[56:59], a[164:167], v[84:87], v[56:59]\nv_mfma_f32_16x16x32_bf16 v[60:63], v[124:127], v[40:43], v[60:63]\n" \
"v_mfma_f32_16x16x32_bf16 v[64:67], v[124:127], v[84:87], v[64:67]\nv_mfma_f32_16x16x32_bf16 v[68:71], v[228:231], v[40:43], v[68:71]\ns_waitcnt lgkmcnt(1)\nv_mfma_f32_16x16x32_bf16 v[72:75], v[228:231], v[84:87], v[72:75]\n" \
"ds_read_b128 v[40:43], v24 offset:23552\nv_mfma_f32_16x16x32_bf16 v[44:47], a[64:67], v[32:35], v[44:47]\nds_read_b128 v[80:83], v24 offset:56320\nv_mfma_f32_16x16x32_bf16 v[48:51], a[64:67], v[76:79], v[48:51]\n" \
"v_mfma_f32_16x16x32_bf16 v[52:55], a[168:171], v[32:35], v[52:55]\nds_read_b128 v[84:87], v25 offset:19456\nv_mfma_f32_16x16x32_bf16 v[56:59], a[168:171], v[76:79], v[56:59]\nv_mfma_f32_16x16x32_bf16 v[60:63], v[128:131], v[32:35], v[60:63]\n" \
"s_waitcnt lgkmcnt(3)\nv_mfma_f32_16x16x32_bf16 v[64:67], v[128:131], v[76:79], v[64:67]\nv_mfma_f32_16x16x32_bf16 v[68:71], v[36:39], v[32:35], v[68:71]\ns_waitcnt vmcnt(8) lgkmcnt(1)\n" \
"s_barrier\nv_mfma_f32_16x16x32_bf16 v[72:75], v[36:39], v[76:79], v[72:75]\nds_read_b128 v[32:35], v24 offset:24576\nv_mfma_f32_16x16x32_bf16 v[44:47], a[68:71], v[40:43], v[44:47]\n" \
"ds_read_b128 v[36:39], v24 offset:57344\nv_mfma_f32_16x16x32_bf16 v[48:51], a[68:71], v[80:83], v[48:51]\nds_read_b128 v[76:79], v25 offset:12288\nv_mfma_f32_16x16x32_bf16 v[52:55], a[172:175], v[40:43], v[52:55]\n" \
"buffer_load_dword v31, s[8:11], s99 offen offset:0 sc1 lds\nv_mfma_f32_16x16x32_bf16 v[56:59], a[172:175], v[80:83], v[56:59]\ns_add_u32 m0, s32, 0x4000\nv_mfma_f32_16x16x32_bf16 v[60:63], v[132:135], v[40:43], v[60:63]\n" \
"s_waitcnt lgkmcnt(3)\nv_mfma_f32_16x16x32_bf16 v[64:67], v[132:135], v[80:83], v[64:67]\nv_mfma_f32_16x16x32_bf16 v[68:71], v[84:87], v[40:43], v[68:71]\ns_waitcnt lgkmcnt(1)\n" \
"v_mfma_f32_16x16x32_bf16 v[72:75], v[84:87], v[80:83], v[72:75]\nds_read_b128 v[40:43], v24 offset:25600\nv_mfma_f32_16x16x32_bf16 v[44:47], a[72:75], v[32:35], v[44:47]\nds_read_b128 v[80:83], v24 offset:58368\n" \
"v_mfma_f32_16x16x32_bf16 v[48:51], a[72:75], v[36:39], v[48:51]\nds_read_b128 v[84:87], v25 offset:13312\nv_mfma_f32_16x16x32_bf16 v[52:55], a[176:179], v[32:35], v[52:55]\ns_waitcnt lgkmcnt(3)\n" \
"v_mfma_f32_16x16x32_bf16 v[56:59], a[176:179], v[36:39], v[56:59]\nv_mfma_f32_16x16x32_bf16 v[60:63], v[76:79], v[32:35], v[60:63]\nv_mfma_f32_16x16x32_bf16 v[64:67], v[76:79], v[36:39], v[64:67]\nv_mfma_f32_16x16x32_bf16 v[68:71], v[232:235], v[32:35], v[68:71]\n" \
"s_waitcnt lgkmcnt(1)\nv_mfma_f32_16x16x32_bf16 v[72:75], v[232:235], v[36:39], v[72:75]\nbuffer_load_dwordx4 v24, s[8:11], s94 offen offset:0 sc1 lds\nv_mfma_f32_16x16x32_bf16 v[44:47], a[76:79], v[40:43], v[44:47]\n" \
"ds_read_b128 v[32:35], v24 offset:26624\nv_mfma_f32_16x16x32_bf16 v[48:51], a[76:79], v[80:83], v[48:51]\nds_read_b128 v[36:39], v24 offset:59392\nv_mfma_f32_16x16x32_bf16 v[52:55], a[180:183], v[40:43], v[52:55]\n" \
"s_waitcnt lgkmcnt(2)\nbuffer_load_dwordx4 v24, s[8:11], s94 offen offset:1024 sc1 lds\nv_mfma_f32_16x16x32_bf16 v[56:59], a[180:183], v[80:83], v[56:59]\nbuffer_load_dwordx4 v24, s[8:11], s94 offen offset:2048 sc1 lds\n" \
"v_mfma_f32_16x16x32_bf16 v[60:63], v[84:87], v[40:43], v[60:63]\nbuffer_load_dwordx4 v24, s[8:11], s94 offen offset:3072 sc1 lds\nv_mfma_f32_16x16x32_bf16 v[64:67], v[84:87], v[80:83], v[64:67]\nds_read_b128 v[76:79], v25 offset:20480\n" \
"s_and_b32 s93, s30, 15\nv_mfma_f32_16x16x32_bf16 v[68:71], v[236:239], v[40:43], v[68:71]\ns_lshl_b32 s92, s93, 17\ns_add_u32 s92, s92, s35\n" \
"s_and_b32 s93, s93, 7\nv_mfma_f32_16x16x32_bf16 v[72:75], v[236:239], v[80:83], v[72:75]\ns_waitcnt lgkmcnt(1)\nds_read_b128 v[40:43], v24 offset:27648\n" \
"v_mfma_f32_16x16x32_bf16 v[44:47], a[80:83], v[32:35], v[44:47]\nds_read_b128 v[80:83], v24 offset:60416\ns_lshr_b32 s95, s30, s41\nv_mfma_f32_16x16x32_bf16 v[48:51], a[80:83], v[36:39], v[48:51]\n" \
"s_and_b32 s95, s95, 1\ns_lshl_b32 s93, s93, 14\ns_lshl1_add_u32 s99, s93, s34\nv_mfma_f32_16x16x32_bf16 v[52:55], a[184:187], v[32:35], v[52:55]\n" \
"ds_read_b128 v[84:87], v25 offset:21504\ns_or_b32 s98, s92, s36\nv_mfma_f32_16x16x32_bf16 v[56:59], a[184:187], v[36:39], v[56:59]\ns_or_b32 s97, s92, s37\n" \
"s_and_b32 s92, s92, s39\ns_andn2_b32 s93, s93, s39\nv_mfma_f32_16x16x32_bf16 v[60:63], v[136:139], v[32:35], v[60:63]\nds_read_b128 v[88:91], v25 offset:4096\n" \
"s_add_u32 s96, s92, s38\nv_mfma_f32_16x16x32_bf16 v[64:67], v[136:139], v[36:39], v[64:67]\ns_waitcnt lgkmcnt(4)\ns_add_u32 s96, s96, s93\n" \
"s_add_u32 s30, s30, 1\nv_mfma_f32_16x16x32_bf16 v[68:71], v[76:79], v[32:35], v[68:71]\ns_lshl_b32 s93, s30, 24\ns_cmp_le_u32 s30, 64\n" \
"s_cselect_b32 s51, s14, 0\nv_mfma_f32_16x16x32_bf16 v[72:75], v[76:79], v[36:39], v[72:75]\ns_waitcnt lgkmcnt(2)\nds_read_b128 v[32:35], v24 offset:28672\n" \
"v_mfma_f32_16x16x32_bf16 v[44:47], a[84:87], v[40:43], v[44:47]\nds_read_b128 v[36:39], v24 offset:61440\nv_mfma_f32_16x16x32_bf16 v[48:51], a[84:87], v[80:83], v[48:51]\nv_mfma_f32_16x16x32_bf16 v[52:55], a[188:191], v[40:43], v[52:55]\n" \
"v_mfma_f32_16x16x32_bf16 v[56:59], a[188:191], v[80:83], v[56:59]\nv_mfma_f32_16x16x32_bf16 v[60:63], v[140:143], v[40:43], v[60:63]\ns_waitcnt lgkmcnt(2)\nds_read_b128 v[76:79], v25 offset:5120\n" \
"v_mfma_f32_16x16x32_bf16 v[64:67], v[140:143], v[80:83], v[64:67]\nv_mfma_f32_16x16x32_bf16 v[68:71], v[84:87], v[40:43], v[68:71]\ns_waitcnt lgkmcnt(1)\nv_mfma_f32_16x16x32_bf16 v[72:75], v[84:87], v[80:83], v[72:75]\n" \
"ds_read_b128 v[40:43], v24 offset:29696\nv_mfma_f32_16x16x32_bf16 v[44:47], v[88:91], v[32:35], v[44:47]\nds_read_b128 v[80:83], v24 offset:62464\nv_mfma_f32_16x16x32_bf16 v[48:51], v[88:91], v[36:39], v[48:51]\n" \
"v_mfma_f32_16x16x32_bf16 v[52:55], a[192:195], v[32:35], v[52:55]\nv_mfma_f32_16x16x32_bf16 v[56:59], a[192:195], v[36:39], v[56:59]\nv_mfma_f32_16x16x32_bf16 v[60:63], v[144:147], v[32:35], v[60:63]\nv_mfma_f32_16x16x32_bf16 v[64:67], v[144:147], v[36:39], v[64:67]\n" \
"v_mfma_f32_16x16x32_bf16 v[68:71], v[240:243], v[32:35], v[68:71]\ns_waitcnt lgkmcnt(0)\nv_mfma_f32_16x16x32_bf16 v[72:75], v[240:243], v[36:39], v[72:75]\nds_read_b128 v[32:35], v24 offset:30720\n" \
"v_add_u32 v31, s32, v24\nv_mfma_f32_16x16x32_bf16 v[44:47], v[76:79], v[40:43], v[44:47]\nds_read_b128 v[36:39], v24 offset:63488\nv_mfma_f32_16x16x32_bf16 v[48:51], v[76:79], v[80:83], v[48:51]\n" \
"v_mfma_f32_16x16x32_bf16 v[52:55], a[196:199], v[40:43], v[52:55]\nv_mfma_f32_16x16x32_bf16 v[56:59], a[196:199], v[80:83], v[56:59]\nv_mfma_f32_16x16x32_bf16 v[60:63], v[148:151], v[40:43], v[60:63]\nv_mfma_f32_16x16x32_bf16 v[64:67], v[148:151], v[80:83], v[64:67]\n" \
"v_mfma_f32_16x16x32_bf16 v[68:71], v[244:247], v[40:43], v[68:71]\ns_waitcnt vmcnt(0) lgkmcnt(0)\nds_read_b128 v[40:43], v31 offset:16384\nds_read_b128 v[76:79], v31 offset:17408\n" \
"v_mfma_f32_16x16x32_bf16 v[72:75], v[244:247], v[80:83], v[72:75]\nds_read_b128 v[84:87], v31 offset:18432\nds_read_b128 v[80:83], v31 offset:19456\nv_mfma_f32_16x16x32_bf16 v[44:47], a[88:91], v[32:35], v[44:47]\n" \
"ds_read_b128 v[88:91], v24 offset:31744\nv_mfma_f32_16x16x32_bf16 v[48:51], a[88:91], v[36:39], v[48:51]\nds_read_b128 v[92:95], v24 offset:64512\nv_mfma_f32_16x16x32_bf16 v[52:55], a[200:203], v[32:35], v[52:55]\n" \
"v_mfma_f32_16x16x32_bf16 v[56:59], a[200:203], v[36:39], v[56:59]\ns_waitcnt lgkmcnt(3)\nv_max3_u32 v96, v40, v76, v84\nv_mov_b64 v[100:101], 0\n" \
"v_mfma_f32_16x16x32_bf16 v[60:63], v[152:155], v[32:35], v[60:63]\ns_waitcnt lgkmcnt(2)\nv_max_u32 v96, v96, v80\nv_cmp_eq_u32 vcc, -1, v96\n" \
"v_mfma_f32_16x16x32_bf16 v[64:67], v[152:155], v[36:39], v[64:67]\nds_read_b128 v[104:107], v25 offset:0\nv_mov_b64 v[102:103], 0\nv_mfma_f32_16x16x32_bf16 v[68:71], v[248:251], v[32:35], v[68:71]\n" \
"ds_read_b128 v[108:111], v25 offset:1024\nv_mov_b64 v[32:33], 0\nv_mfma_f32_16x16x32_bf16 v[72:75], v[248:251], v[36:39], v[72:75]\ns_waitcnt lgkmcnt(2)\n" \
"s_cbranch_vccnz 2f\n3:\nv_mfma_f32_16x16x32_bf16 v[44:47], a[92:95], v[88:91], v[44:47]\ns_barrier\n" \
"s_memrealtime s[56:57]\nv_mfma_f32_16x16x32_bf16 v[52:55], a[204:207], v[88:91], v[52:55]\nds_read_b128 v[36:39], v24 offset:20480\nv_mov_b64 v[34:35], 0\n" \
"v_mfma_f32_16x16x32_bf16 v[60:63], v[156:159], v[88:91], v[60:63]\nds_read_b128 v[40:43], v24 offset:21504\nv_add_u32 v31, s44, v24\nv_mfma_f32_16x16x32_bf16 v[68:71], v[252:255], v[88:91], v[68:71]\n" \
"ds_read_b128 v[76:79], v24 offset:16384\nv_cvt_pk_bf16_f32 v44, v44, v52\nv_mfma_f32_16x16x32_bf16 v[48:51], a[92:95], v[92:95], v[48:51]\nds_read_b128 v[80:83], v24 offset:22528\n" \
"v_cvt_pk_bf16_f32 v45, v45, v53\nv_mfma_f32_16x16x32_bf16 v[56:59], a[204:207], v[92:95], v[56:59]\nds_read_b128 v[84:87], v24 offset:23552\nv_cvt_pk_bf16_f32 v46, v46, v54\n" \
"v_mfma_f32_16x16x32_bf16 v[64:67], v[156:159], v[92:95], v[64:67]\nds_read_b128 v[88:91], v24 offset:17408\nv_cvt_pk_bf16_f32 v47, v47, v55\nv_mfma_f32_16x16x32_bf16 v[72:75], v[252:255], v[92:95], v[72:75]\n" \
"s_waitcnt lgkmcnt(5)\nds_write_b64 v31, v[44:45] offset:0\nv_smfmac_f32_16x16x64_bf16 v[100:103], v[36:39], v[104:111], v26\nds_read_b128 v[52:55], v24 offset:18432\n" \
"v_cvt_pk_bf16_f32 v60, v60, v68\nv_smfmac_f32_16x16x64_bf16 v[32:35], v[36:39], a[120:127], v26\ns_waitcnt lgkmcnt(5)\nds_write_b64 v31, v[46:47] offset:8\n" \
"v_smfmac_f32_16x16x64_bf16 v[100:103], v[40:43], a[24:31], v26\nds_read_b128 v[92:95], v24 offset:19456\nv_cvt_pk_bf16_f32 v61, v61, v69\nv_smfmac_f32_16x16x64_bf16 v[32:35], v[40:43], a[128:135], v26\n" \
"ds_write_b64 v31, v[60:61] offset:32768\nv_smfmac_f32_16x16x64_bf16 v[100:103], v[76:79], v[0:7], v26\nds_read_b128 v[44:47], v24 offset:49152\nv_cvt_pk_bf16_f32 v62, v62, v70\n" \
"v_smfmac_f32_16x16x64_bf16 v[32:35], v[76:79], a[96:103], v26\ns_waitcnt lgkmcnt(5)\ns_add_u32 s94, s56, 0x7e\nv_cvt_pk_bf16_f32 v63, v63, v71\n" \
"v_smfmac_f32_16x16x64_bf16 v[100:103], v[80:83], a[32:39], v26\nds_read_b128 v[68:71], v24 offset:50176\ns_and_b32 s94, s94, 0xffffff\nv_smfmac_f32_16x16x64_bf16 v[32:35], v[80:83], a[136:143], v26\n" \
"ds_read_b128 v[76:79], v25 offset:6144\ns_or_b32 s94, s94, s93\nv_smfmac_f32_16x16x64_bf16 v[100:103], v[84:87], a[40:47], v26\nds_read_b128 v[80:83], v25 offset:7168\n" \
"v_mov_b32 v60, s94\nv_smfmac_f32_16x16x64_bf16 v[32:35], v[84:87], a[144:151], v26\nds_read_b128 v[96:99], v24 offset:51200\nv_cvt_pk_bf16_f32 v48, v48, v56\n" \
"v_smfmac_f32_16x16x64_bf16 v[100:103], v[88:91], a[0:7], v26\nds_read_b128 v[104:107], v25 offset:2048\nv_cvt_pk_bf16_f32 v49, v49, v57\nv_smfmac_f32_16x16x64_bf16 v[32:35], v[88:91], v[8:15], v26\n" \
"s_waitcnt lgkmcnt(5)\nds_read_b128 v[108:111], v25 offset:3072\nv_smfmac_f32_16x16x64_bf16 v[100:103], v[52:55], a[8:15], v26\nds_read_b128 v[84:87], v24 offset:56320\n" \
"v_cvt_pk_bf16_f32 v50, v50, v58\nv_smfmac_f32_16x16x64_bf16 v[32:35], v[52:55], a[104:111], v26\nds_write_b64 v31, v[62:63] offset:32776\nv_smfmac_f32_16x16x64_bf16 v[100:103], v[92:95], a[16:23], v26\n" \
"buffer_store_dword v60, v29, s[8:11], s49 offen offset:0 sc1\nv_smfmac_f32_16x16x64_bf16 v[32:35], v[92:95], a[112:119], v26\nds_read_b128 v[52:55], v24 offset:52224\nv_cvt_pk_bf16_f32 v51, v51, v59\n" \
"v_smfmac_f32_16x16x64_bf16 v[100:103], v[44:47], a[48:55], v26\nds_read_b128 v[56:59], v24 offset:53248\nv_cvt_pk_bf16_f32 v64, v64, v72\nv_smfmac_f32_16x16x64_bf16 v[32:35], v[44:47], a[152:159], v26\n" \
"s_waitcnt lgkmcnt(4)\nds_write_b64 v31, v[48:49] offset:4096\nv_smfmac_f32_16x16x64_bf16 v[100:103], v[68:71], a[56:63], v26\nds_read_b128 v[60:63], v24 offset:54272\n" \
"v_cvt_pk_bf16_f32 v65, v65, v73\nv_smfmac_f32_16x16x64_bf16 v[32:35], v[68:71], v[76:83], v26\nds_read_b128 v[92:95], v24 offset:55296\nv_cvt_pk_bf16_f32 v66, v66, v74\n" \
"v_smfmac_f32_16x16x64_bf16 v[100:103], v[96:99], v[104:111], v26\nds_read_b128 v[68:71], v25 offset:4096\nv_cvt_pk_bf16_f32 v67, v67, v75\nv_smfmac_f32_16x16x64_bf16 v[32:35], v[96:99], a[160:167], v26\n" \
"s_waitcnt lgkmcnt(5)\nds_read_b128 v[72:75], v25 offset:5120\nv_smfmac_f32_16x16x64_bf16 v[100:103], v[84:87], a[88:95], v26\nds_write_b64 v31, v[50:51] offset:4104\n" \
"v_smfmac_f32_16x16x64_bf16 v[32:35], v[84:87], a[200:207], v26\nds_read_b128 v[76:79], v25 offset:14336\nv_mov_b64 v[48:49], 0\nv_smfmac_f32_16x16x64_bf16 v[100:103], v[52:55], a[64:71], v26\n" \
"ds_read_b128 v[80:83], v25 offset:15360\nv_mov_b64 v[50:51], 0\nv_smfmac_f32_16x16x64_bf16 v[32:35], v[52:55], a[168:175], v26\ns_waitcnt lgkmcnt(5)\n" \
"ds_write_b64 v31, v[64:65] offset:36864\nv_smfmac_f32_16x16x64_bf16 v[100:103], v[56:59], a[72:79], v26\nds_read_b128 v[96:99], v24 offset:51200\nv_mov_b64 v[64:65], 0\n" \
"v_smfmac_f32_16x16x64_bf16 v[32:35], v[56:59], a[176:183], v26\nds_write_b64 v31, v[66:67] offset:36872\nv_smfmac_f32_16x16x64_bf16 v[100:103], v[60:63], a[80:87], v26\nds_read_b128 v[56:59], v24 offset:18432\n" \
"v_mov_b64 v[66:67], 0\nv_smfmac_f32_16x16x64_bf16 v[32:35], v[60:63], a[184:191], v26\ns_waitcnt lgkmcnt(5)\nds_read_b32 v31, v28 offset:512\n" \
"v_smfmac_f32_16x16x64_bf16 v[100:103], v[92:95], v[68:75], v26\nv_add_u32 v60, s45, v24\nv_smfmac_f32_16x16x64_bf16 v[32:35], v[92:95], a[192:199], v26\nds_read_b128 v[68:71], v24 offset:54272\n" \
"v_cndmask_b32 v60, v60, v28, s[28:29]\nv_smfmac_f32_16x16x64_bf16 v[48:51], v[40:43], a[240:247], v26\ns_waitcnt lgkmcnt(4)\nds_read_b128 v[104:107], v25 offset:18432\n" \
"v_smfmac_f32_16x16x64_bf16 v[64:67], v[40:43], v[76:83], v26\nds_read_b128 v[108:111], v25 offset:19456\nv_add_f32 v40, v100, v102\nv_smfmac_f32_16x16x64_bf16 v[48:51], v[36:39], a[232:239], v26\n" \
"ds_read_b128 v[72:75], v25 offset:20480\nv_add_f32 v41, v32, v34\nv_smfmac_f32_16x16x64_bf16 v[64:67], v[36:39], v[192:199], v26\nds_read_b128 v[76:79], v25 offset:21504\n" \
"v_add_f32 v42, v101, v103\nv_smfmac_f32_16x16x64_bf16 v[48:51], v[96:99], v[120:127], v26\nds_read_b128 v[36:39], v24 offset:19456\nv_add_f32 v43, v33, v35\n" \
"v_smfmac_f32_16x16x64_bf16 v[64:67], v[96:99], v[224:231], v26\nds_read_b128 v[32:35], v24 offset:50176\nv_cvt_pk_bf16_f32 v62, v40, v41\nv_smfmac_f32_16x16x64_bf16 v[48:51], v[84:87], v[152:159], v26\n" \
"ds_read_b128 v[80:83], v25 offset:10240\nv_cvt_pk_bf16_f32 v63, v42, v43\nv_smfmac_f32_16x16x64_bf16 v[64:67], v[84:87], v[248:255], v26\nds_write2_b32 v27, v62, v63 offset0:0 offset1:4\n" \
"v_smfmac_f32_16x16x64_bf16 v[48:51], v[92:95], v[144:151], v26\ns_waitcnt lgkmcnt(5)\nds_read_b128 v[84:87], v25 offset:11264\nv_smfmac_f32_16x16x64_bf16 v[64:67], v[92:95], v[240:247], v26\n" \
"ds_read_b128 v[40:43], v24 offset:53248\nv_cmp_eq_u32 vcc, -1, v31\nv_smfmac_f32_16x16x64_bf16 v[48:51], v[56:59], v[16:23], v26\nv_smfmac_f32_16x16x64_bf16 v[64:67], v[56:59], v[176:183], v26\n" \
"ds_read_b128 v[92:95], v24 offset:23552\nv_smfmac_f32_16x16x64_bf16 v[48:51], v[52:55], v[128:135], v26\nds_read_b128 v[96:99], v25 offset:12288\nv_smfmac_f32_16x16x64_bf16 v[64:67], v[52:55], v[104:111], v26\n" \
"s_waitcnt lgkmcnt(4)\nds_read_b128 v[100:103], v25 offset:13312\nv_smfmac_f32_16x16x64_bf16 v[48:51], v[68:71], v[136:143], v26\ns_barrier\n" \
"v_smfmac_f32_16x16x64_bf16 v[64:67], v[68:71], v[72:79], v26\nds_read_b128 v[52:55], v25 offset:16384\nv_mov_b32 v63, s43\nv_smfmac_f32_16x16x64_bf16 v[48:51], v[36:39], a[224:231], v26\n" \
"ds_read_b128 v[56:59], v25 offset:17408\nv_cndmask_b32 v76, v63, 0, s[16:17]\nv_smfmac_f32_16x16x64_bf16 v[64:67], v[36:39], v[184:191], v26\ns_waitcnt lgkmcnt(4)\n" \
"ds_read_b128 v[36:39], v24 offset:22528\nv_smfmac_f32_16x16x64_bf16 v[48:51], v[44:47], v[80:87], v26\nds_read_b128 v[68:71], v25 offset:8192\nv_cndmask_b32 v77, v63, 0, s[18:19]\n" \
"v_smfmac_f32_16x16x64_bf16 v[64:67], v[44:47], v[208:215], v26\nds_read_b128 v[72:75], v25 offset:9216\nv_cndmask_b32 v78, v63, 0, s[20:21]\nv_smfmac_f32_16x16x64_bf16 v[48:51], v[32:35], v[112:119], v26\n" \
"ds_read_b128 v[44:47], v24 offset:16384\nv_cndmask_b32 v79, v63, 0, s[22:23]\nv_smfmac_f32_16x16x64_bf16 v[64:67], v[32:35], v[216:223], v26\nds_read_b128 v[80:83], v60 offset:0\n" \
"v_mov_b32 v62, 0xff61b1e6\nv_smfmac_f32_16x16x64_bf16 v[48:51], v[88:91], a[216:223], v26\nds_read_b128 v[32:35], v24 offset:57344\nv_cndmask_b32 v84, v62, v63, s[16:17]\n" \
"v_smfmac_f32_16x16x64_bf16 v[64:67], v[88:91], v[168:175], v26\ns_waitcnt lgkmcnt(6)\nds_read_b128 v[88:91], v24 offset:61440\nv_smfmac_f32_16x16x64_bf16 v[48:51], v[40:43], v[96:103], v26\n" \
"ds_read_b128 v[104:107], v60 offset:1024\nv_cndmask_b32 v85, v62, v63, s[18:19]\nv_smfmac_f32_16x16x64_bf16 v[64:67], v[40:43], v[232:239], v26\nds_read_b128 v[96:99], v24 offset:58368\n" \
"v_cndmask_b32 v86, v62, v63, s[20:21]\nv_smfmac_f32_16x16x64_bf16 v[48:51], v[92:95], a[248:255], v26\nds_read_b128 v[40:43], v24 offset:62464\nv_cndmask_b32 v87, v62, v63, s[22:23]\n" \
"v_smfmac_f32_16x16x64_bf16 v[64:67], v[92:95], v[52:59], v26\ns_waitcnt lgkmcnt(5)\nds_read_b128 v[56:59], v60 offset:2048\nv_smfmac_f32_16x16x64_bf16 v[48:51], v[36:39], v[68:75], v26\n" \
"ds_read_b128 v[52:55], v24 offset:59392\nv_smfmac_f32_16x16x64_bf16 v[64:67], v[36:39], v[200:207], v26\nds_read_b128 v[68:71], v24 offset:63488\nv_smfmac_f32_16x16x64_bf16 v[48:51], v[44:47], a[208:215], v26\n" \
"ds_read_b128 v[36:39], v60 offset:3072\nds_read_b128 v[60:63], v24 offset:60416\nv_smfmac_f32_16x16x64_bf16 v[64:67], v[44:47], v[160:167], v26\ns_waitcnt lgkmcnt(6)\n" \
"ds_read_b128 v[44:47], v24 offset:64512\nv_mfma_f32_16x16x32_bf16 v[76:79], v[32:35], v[80:83], v[76:79]\nds_read_b128 v[72:75], v24 offset:0\nv_add_f32 v32, v48, v50\n" \
"v_add_f32 v34, v49, v51\nv_mfma_f32_16x16x32_bf16 v[84:87], v[88:91], v[80:83], v[84:87]\ns_waitcnt lgkmcnt(6)\nv_add_f32 v33, v64, v66\n" \
"v_add_f32 v35, v65, v67\nv_cvt_pk_bf16_f32 v48, v32, v33\nv_cvt_pk_bf16_f32 v49, v34, v35\nv_mfma_f32_16x16x32_bf16 v[76:79], v[96:99], v[104:107], v[76:79]\n" \
"s_waitcnt lgkmcnt(4)\nds_write2_b32 v27, v48, v49 offset0:128 offset1:132\nv_mfma_f32_16x16x32_bf16 v[84:87], v[40:43], v[104:107], v[84:87]\nds_read_b128 v[32:35], v24 offset:32768\n" \
"v_mfma_f32_16x16x32_bf16 v[76:79], v[52:55], v[56:59], v[76:79]\ns_waitcnt lgkmcnt(4)\ns_cmp_eq_u64 vcc, 0\ns_cselect_b32 s14, s10, 0\n" \
"v_mfma_f32_16x16x32_bf16 v[84:87], v[68:71], v[56:59], v[84:87]\ns_cmp_eq_u32 s51, 0\ns_cbranch_scc1 4f\n5:\n" \
"v_mfma_f32_16x16x32_bf16 v[76:79], v[60:63], v[36:39], v[76:79]\ns_waitcnt lgkmcnt(1)\ns_barrier\nds_read_b128 v[40:43], v28 offset:512\n" \
"ds_read_b128 v[48:51], v28 offset:1536\nv_mfma_f32_16x16x32_bf16 v[84:87], v[44:47], v[36:39], v[84:87]\nds_read_b128 v[64:67], v28 offset:2560\nds_read_b128 v[88:91], v28 offset:3584\n" \
"v_mfma_f32_16x16x32_bf16 v[44:47], v[0:3], v[72:75], 0\nds_read_b128 v[92:95], v24 offset:1024\nv_max3_f32 v31, v76, v77, v78\nv_max3_f32 v31, v31, v79, s47\n" \
"v_mfma_f32_16x16x32_bf16 v[52:55], a[96:99], v[72:75], 0\nds_read_b128 v[96:99], v24 offset:33792\nv_max3_f32 v100, v84, v85, v86\nv_max3_f32 v101, v31, v100, v87\n" \
"v_max3_f32 v31, v31, v100, v87\nv_mfma_f32_16x16x32_bf16 v[60:63], a[208:211], v[72:75], 0\ns_waitcnt lgkmcnt(6)\nv_mul_f32 v100, s46, v31\n" \
"v_fma_f32 v76, v76, s46, -v100\nv_exp_f32 v76, v76\nv_mfma_f32_16x16x32_bf16 v[68:71], v[160:163], v[72:75], 0\ns_waitcnt lgkmcnt(4)\n" \
"v_fma_f32 v78, v78, s46, -v100\nv_exp_f32 v78, v78\nv_fma_f32 v77, v77, s46, -v100\nv_fma_f32 v79, v79, s46, -v100\n" \
"v_mfma_f32_16x16x32_bf16 v[108:111], v[40:43], v[80:83], 0\nv_exp_f32 v77, v77\nv_fma_f32 v84, v84, s46, -v100\nv_fma_f32 v86, v86, s46, -v100\n" \
"v_fma_f32 v85, v85, s46, -v100\nv_mfma_f32_16x16x32_bf16 v[108:111], v[48:51], v[104:107], v[108:111]\ns_waitcnt lgkmcnt(0)\nv_fma_f32 v87, v87, s46, -v100\n" \
"v_cndmask_b32 v40, v76, v78, s[24:25]\nv_mfma_f32_16x16x32_bf16 v[108:111], v[64:67], v[56:59], v[108:111]\nv_exp_f32 v79, v79\nv_mfma_f32_16x16x32_bf16 v[108:111], v[88:91], v[36:39], v[108:111]\n" \
"v_exp_f32 v84, v84\nv_mfma_f32_16x16x32_bf16 v[48:51], v[0:3], v[32:35], 0\nv_exp_f32 v86, v86\nv_exp_f32 v85, v85\n" \
"v_mfma_f32_16x16x32_bf16 v[56:59], a[96:99], v[32:35], 0\nv_exp_f32 v87, v87\nv_cndmask_b32 v41, v77, v79, s[24:25]\nv_cndmask_b32 v42, v84, v86, s[24:25]\n" \
"v_mfma_f32_16x16x32_bf16 v[64:67], a[208:211], v[32:35], 0\nv_cndmask_b32 v43, v85, v87, s[24:25]\nv_permlane32_swap_b32 v31, v101\nv_mfma_f32_16x16x32_bf16 v[72:75], v[160:163], v[32:35], 0\n" \
"v_max3_f32 v36, v108, v109, v110\nv_max3_f32 v31, v31, v101, v111\nv_max_f32 v37, v36, v31\nv_max_f32 v36, v36, v31\n" \
"v_mfma_f32_16x16x32_bf16 v[44:47], v[4:7], v[92:95], v[44:47]\nds_read_b128 v[32:35], v24 offset:2048\nv_permlane16_swap_b32 v36, v37\nv_mfma_f32_16x16x32_bf16 v[48:51], v[4:7], v[96:99], v[48:51]\n" \
"ds_read_b128 v[80:83], v24 offset:34816\nv_max_f32 v36, v36, v37\nv_mfma_f32_16x16x32_bf16 v[52:55], a[100:103], v[92:95], v[52:55]\nv_cndmask_b32 v38, v108, v110, s[26:27]\n" \
"v_fma_f32 v100, v36, -s46, v100\nv_mul_f32 v36, s46, v36\nv_cndmask_b32 v39, v109, v111, s[26:27]\nv_fma_f32 v38, v38, s46, -v36\n" \
"v_mfma_f32_16x16x32_bf16 v[56:59], a[100:103], v[96:99], v[56:59]\nv_add_f32 v88, v76, v78\nv_add_f32 v89, v77, v79\nv_fma_f32 v39, v39, s46, -v36\n" \
"v_add_f32 v90, v84, v86\nv_add_f32 v88, v88, v89\nv_mfma_f32_16x16x32_bf16 v[60:63], a[212:215], v[92:95], v[60:63]\nv_exp_f32 v100, v100\n" \
"v_exp_f32 v38, v38\nv_add_f32 v91, v85, v87\nv_mfma_f32_16x16x32_bf16 v[64:67], a[212:215], v[96:99], v[64:67]\nv_add_f32 v88, v88, v90\n" \
"v_exp_f32 v39, v39\nv_add_f32 v88, v88, v91\nv_fma_f32 v88, v88, v100, v38\nv_mfma_f32_16x16x32_bf16 v[68:71], v[164:167], v[92:95], v[68:71]\n" \
"s_waitcnt lgkmcnt(0)\nv_add_f32 v89, v88, v39\nv_add_f32 v88, v39, v88\nv_mfma_f32_16x16x32_bf16 v[72:75], v[164:167], v[96:99], v[72:75]\n" \
"ds_read_b128 v[92:95], v24 offset:3072\nv_permlane16_swap_b32 v88, v89\nv_add_f32 v90, v88, v89\nv_add_f32 v88, v89, v88\n" \
"v_mfma_f32_16x16x32_bf16 v[44:47], a[0:3], v[32:35], v[44:47]\nds_read_b128 v[96:99], v24 offset:35840\nv_permlane32_swap_b32 v88, v90\nv_mfma_f32_16x16x32_bf16 v[48:51], a[0:3], v[80:83], v[48:51]\n" \
"v_add_f32 v88, v88, v90\nv_mfma_f32_16x16x32_bf16 v[52:55], v[8:11], v[32:35], v[52:55]\nv_rcp_f32 v88, v88\nv_mfma_f32_16x16x32_bf16 v[56:59], v[8:11], v[80:83], v[56:59]\n" \
"v_mul_f32 v100, v100, v88\nv_mul_f32 v40, v40, v100\nv_mul_f32 v41, v41, v100\nv_mul_f32 v38, v38, v88\n" \
"v_mul_f32 v39, v39, v88\nv_mfma_f32_16x16x32_bf16 v[60:63], a[216:219], v[32:35], v[60:63]\nv_mul_f32 v42, v42, v100\nv_mul_f32 v43, v43, v100\n" \
"v_cvt_pk_bf16_f32 v36, v40, v41\nv_cvt_pk_bf16_f32 v31, v38, v39\nv_cvt_pk_bf16_f32 v37, v42, v43\nv_mfma_f32_16x16x32_bf16 v[64:67], a[216:219], v[80:83], v[64:67]\n" \
"v_and_or_b32 v36, v36, -2, s95\nv_and_or_b32 v31, v31, -2, s95\nv_mfma_f32_16x16x32_bf16 v[68:71], v[168:171], v[32:35], v[68:71]\ns_waitcnt lgkmcnt(0)\n" \
"buffer_store_dwordx2 v[36:37], v29, s[8:11], s99 offen offset:0 sc1\nbuffer_store_dword v31, v30, s[8:11], s96 offen offset:1536 sc1\nv_mfma_f32_16x16x32_bf16 v[72:75], v[168:171], v[80:83], v[72:75]\nds_read_b128 v[88:91], v24 offset:4096\n" \
"v_mul_f32 v76, v76, v100\nv_mfma_f32_16x16x32_bf16 v[44:47], a[4:7], v[92:95], v[44:47]\nds_read_b128 v[32:35], v24 offset:36864\nv_mul_f32 v77, v77, v100\n" \
"v_mfma_f32_16x16x32_bf16 v[48:51], a[4:7], v[96:99], v[48:51]\nv_mul_f32 v78, v78, v100\nv_mul_f32 v79, v79, v100\nv_mfma_f32_16x16x32_bf16 v[52:55], v[12:15], v[92:95], v[52:55]\n" \
"v_cvt_pk_bf16_f32 v80, v76, v77\nv_cvt_pk_bf16_f32 v81, v78, v79\nv_mfma_f32_16x16x32_bf16 v[56:59], v[12:15], v[96:99], v[56:59]\ns_add_u32 m0, s32, 0x4000\n" \
"v_and_or_b32 v80, v80, -2, s95\nv_mul_f32 v84, v84, v100\nv_mfma_f32_16x16x32_bf16 v[60:63], a[220:223], v[92:95], v[60:63]\nbuffer_store_dwordx2 v[80:81], v29, s[8:11], s97 offen offset:1024 sc1\n" \
"v_mul_f32 v85, v85, v100\nv_mfma_f32_16x16x32_bf16 v[64:67], a[220:223], v[96:99], v[64:67]\ns_add_u32 s99, s31, 0x4000\nv_mul_f32 v86, v86, v100\n" \
"v_mul_f32 v87, v87, v100\nv_mfma_f32_16x16x32_bf16 v[68:71], v[172:175], v[92:95], v[68:71]\ns_waitcnt lgkmcnt(0)\nv_cvt_pk_bf16_f32 v82, v84, v85\n" \
"v_cvt_pk_bf16_f32 v83, v86, v87\nv_mfma_f32_16x16x32_bf16 v[72:75], v[172:175], v[96:99], v[72:75]\nds_read_b128 v[36:39], v24 offset:5120\nv_mfma_f32_16x16x32_bf16 v[44:47], a[8:11], v[88:91], v[44:47]\n" \
"ds_read_b128 v[40:43], v24 offset:37888\nv_mfma_f32_16x16x32_bf16 v[48:51], a[8:11], v[32:35], v[48:51]\nbuffer_store_dwordx4 v[80:83], v24, s[8:11], s98 offen offset:0 sc1\nbuffer_load_dwordx4 v24, s[8:11], s99 offen offset:0 sc1 lds\n" \
"v_mfma_f32_16x16x32_bf16 v[52:55], a[104:107], v[88:91], v[52:55]\nv_mfma_f32_16x16x32_bf16 v[56:59], a[104:107], v[32:35], v[56:59]\nbuffer_load_dwordx4 v24, s[8:11], s99 offen offset:1024 sc1 lds\nv_mfma_f32_16x16x32_bf16 v[60:63], v[16:19], v[88:91], v[60:63]\n" \
"v_mfma_f32_16x16x32_bf16 v[64:67], v[16:19], v[32:35], v[64:67]\nv_mfma_f32_16x16x32_bf16 v[68:71], v[176:179], v[88:91], v[68:71]\ns_waitcnt lgkmcnt(0)\nv_mfma_f32_16x16x32_bf16 v[72:75], v[176:179], v[32:35], v[72:75]\n" \
"ds_read_b128 v[76:79], v24 offset:6144\nv_mfma_f32_16x16x32_bf16 v[44:47], a[12:15], v[36:39], v[44:47]\nds_read_b128 v[32:35], v24 offset:38912\nv_mfma_f32_16x16x32_bf16 v[48:51], a[12:15], v[40:43], v[48:51]\n" \
"buffer_load_dwordx4 v24, s[8:11], s99 offen offset:2048 sc1 lds\nv_mfma_f32_16x16x32_bf16 v[52:55], a[108:111], v[36:39], v[52:55]\nv_mfma_f32_16x16x32_bf16 v[56:59], a[108:111], v[40:43], v[56:59]\nbuffer_load_dwordx4 v24, s[8:11], s99 offen offset:3072 sc1 lds\n" \
"v_mfma_f32_16x16x32_bf16 v[60:63], v[20:23], v[36:39], v[60:63]\nv_mfma_f32_16x16x32_bf16 v[64:67], v[20:23], v[40:43], v[64:67]\nv_mfma_f32_16x16x32_bf16 v[68:71], v[180:183], v[36:39], v[68:71]\ns_waitcnt lgkmcnt(0)\n" \
"v_mfma_f32_16x16x32_bf16 v[72:75], v[180:183], v[40:43], v[72:75]\nds_read_b128 v[36:39], v24 offset:7168\nv_mfma_f32_16x16x32_bf16 v[44:47], a[16:19], v[76:79], v[44:47]\nds_read_b128 v[40:43], v24 offset:39936\n" \
"v_mfma_f32_16x16x32_bf16 v[48:51], a[16:19], v[32:35], v[48:51]\ns_add_u32 m0, s32, 0x6000\ns_add_u32 s99, s31, 0x6000\nv_mfma_f32_16x16x32_bf16 v[52:55], a[112:115], v[76:79], v[52:55]\n" \
"v_mfma_f32_16x16x32_bf16 v[56:59], a[112:115], v[32:35], v[56:59]\nv_mfma_f32_16x16x32_bf16 v[60:63], a[224:227], v[76:79], v[60:63]\nds_read_b128 v[80:83], v25 offset:0\nv_mfma_f32_16x16x32_bf16 v[64:67], a[224:227], v[32:35], v[64:67]\n" \
"buffer_load_dwordx4 v24, s[8:11], s99 offen offset:0 sc1 lds\nv_mfma_f32_16x16x32_bf16 v[68:71], v[184:187], v[76:79], v[68:71]\ns_waitcnt lgkmcnt(1)\nv_mfma_f32_16x16x32_bf16 v[72:75], v[184:187], v[32:35], v[72:75]\n" \
"ds_read_b128 v[76:79], v24 offset:8192\nv_mfma_f32_16x16x32_bf16 v[44:47], a[20:23], v[36:39], v[44:47]\nds_read_b128 v[32:35], v24 offset:40960\nv_mfma_f32_16x16x32_bf16 v[48:51], a[20:23], v[40:43], v[48:51]\n" \
"buffer_load_dwordx4 v24, s[8:11], s99 offen offset:1024 sc1 lds\nv_mfma_f32_16x16x32_bf16 v[52:55], a[116:119], v[36:39], v[52:55]\nv_mfma_f32_16x16x32_bf16 v[56:59], a[116:119], v[40:43], v[56:59]\nv_mfma_f32_16x16x32_bf16 v[60:63], a[228:231], v[36:39], v[60:63]\n" \
"ds_read_b128 v[84:87], v25 offset:1024\nv_mfma_f32_16x16x32_bf16 v[64:67], a[228:231], v[40:43], v[64:67]\nbuffer_load_dwordx4 v24, s[8:11], s99 offen offset:2048 sc1 lds\nv_mfma_f32_16x16x32_bf16 v[68:71], v[188:191], v[36:39], v[68:71]\n" \
"s_waitcnt lgkmcnt(1)\nv_mfma_f32_16x16x32_bf16 v[72:75], v[188:191], v[40:43], v[72:75]\nds_read_b128 v[36:39], v24 offset:9216\nv_mfma_f32_16x16x32_bf16 v[44:47], v[80:83], v[76:79], v[44:47]\n" \
"ds_read_b128 v[40:43], v24 offset:41984\nv_mfma_f32_16x16x32_bf16 v[48:51], v[80:83], v[32:35], v[48:51]\nbuffer_load_dwordx4 v24, s[8:11], s99 offen offset:3072 sc1 lds\nv_mfma_f32_16x16x32_bf16 v[52:55], a[120:123], v[76:79], v[52:55]\n" \
"v_mfma_f32_16x16x32_bf16 v[56:59], a[120:123], v[32:35], v[56:59]\nv_mfma_f32_16x16x32_bf16 v[60:63], a[232:235], v[76:79], v[60:63]\nv_mfma_f32_16x16x32_bf16 v[64:67], a[232:235], v[32:35], v[64:67]\nv_mfma_f32_16x16x32_bf16 v[68:71], v[192:195], v[76:79], v[68:71]\n" \
"s_waitcnt lgkmcnt(0)\nv_mfma_f32_16x16x32_bf16 v[72:75], v[192:195], v[32:35], v[72:75]\nds_read_b128 v[76:79], v24 offset:10240\nv_mfma_f32_16x16x32_bf16 v[44:47], v[84:87], v[36:39], v[44:47]\n" \
"ds_read_b128 v[32:35], v24 offset:43008\nv_mfma_f32_16x16x32_bf16 v[48:51], v[84:87], v[40:43], v[48:51]\nv_mfma_f32_16x16x32_bf16 v[52:55], a[124:127], v[36:39], v[52:55]\nds_read_b128 v[80:83], v25 offset:14336\n" \
"v_mfma_f32_16x16x32_bf16 v[56:59], a[124:127], v[40:43], v[56:59]\nv_mfma_f32_16x16x32_bf16 v[60:63], a[236:239], v[36:39], v[60:63]\nv_mfma_f32_16x16x32_bf16 v[64:67], a[236:239], v[40:43], v[64:67]\nv_mfma_f32_16x16x32_bf16 v[68:71], v[196:199], v[36:39], v[68:71]\n" \
"s_waitcnt lgkmcnt(1)\nv_mfma_f32_16x16x32_bf16 v[72:75], v[196:199], v[40:43], v[72:75]\nds_read_b128 v[36:39], v24 offset:11264\nv_mfma_f32_16x16x32_bf16 v[44:47], a[24:27], v[76:79], v[44:47]\n" \
"ds_read_b128 v[40:43], v24 offset:44032\nv_mfma_f32_16x16x32_bf16 v[48:51], a[24:27], v[32:35], v[48:51]\nv_mfma_f32_16x16x32_bf16 v[52:55], a[128:131], v[76:79], v[52:55]\nds_read_b128 v[84:87], v25 offset:15360\n" \
"v_mfma_f32_16x16x32_bf16 v[56:59], a[128:131], v[32:35], v[56:59]\nv_mfma_f32_16x16x32_bf16 v[60:63], a[240:243], v[76:79], v[60:63]\ns_waitcnt lgkmcnt(3)\nv_mfma_f32_16x16x32_bf16 v[64:67], a[240:243], v[32:35], v[64:67]\n" \
"v_mfma_f32_16x16x32_bf16 v[68:71], v[80:83], v[76:79], v[68:71]\ns_waitcnt lgkmcnt(1)\nv_mfma_f32_16x16x32_bf16 v[72:75], v[80:83], v[32:35], v[72:75]\nds_read_b128 v[76:79], v24 offset:12288\n" \
"v_mfma_f32_16x16x32_bf16 v[44:47], a[28:31], v[36:39], v[44:47]\nds_read_b128 v[32:35], v24 offset:45056\nv_mfma_f32_16x16x32_bf16 v[48:51], a[28:31], v[40:43], v[48:51]\nds_read_b128 v[80:83], v25 offset:8192\n" \
"v_mfma_f32_16x16x32_bf16 v[52:55], a[132:135], v[36:39], v[52:55]\nv_mfma_f32_16x16x32_bf16 v[56:59], a[132:135], v[40:43], v[56:59]\nv_mfma_f32_16x16x32_bf16 v[60:63], a[244:247], v[36:39], v[60:63]\ns_waitcnt lgkmcnt(3)\n" \
"v_mfma_f32_16x16x32_bf16 v[64:67], a[244:247], v[40:43], v[64:67]\nv_mfma_f32_16x16x32_bf16 v[68:71], v[84:87], v[36:39], v[68:71]\ns_waitcnt lgkmcnt(1)\nv_mfma_f32_16x16x32_bf16 v[72:75], v[84:87], v[40:43], v[72:75]\n" \
"ds_read_b128 v[36:39], v24 offset:13312\nv_mfma_f32_16x16x32_bf16 v[44:47], a[32:35], v[76:79], v[44:47]\nds_read_b128 v[40:43], v24 offset:46080\nv_mfma_f32_16x16x32_bf16 v[48:51], a[32:35], v[32:35], v[48:51]\n" \
"ds_read_b128 v[84:87], v25 offset:9216\nv_mfma_f32_16x16x32_bf16 v[52:55], a[136:139], v[76:79], v[52:55]\ns_waitcnt lgkmcnt(3)\nv_mfma_f32_16x16x32_bf16 v[56:59], a[136:139], v[32:35], v[56:59]\n" \
"v_mfma_f32_16x16x32_bf16 v[60:63], v[80:83], v[76:79], v[60:63]\nv_mfma_f32_16x16x32_bf16 v[64:67], v[80:83], v[32:35], v[64:67]\nv_mfma_f32_16x16x32_bf16 v[68:71], v[200:203], v[76:79], v[68:71]\ns_waitcnt lgkmcnt(1)\n" \
"v_mfma_f32_16x16x32_bf16 v[72:75], v[200:203], v[32:35], v[72:75]\nds_read_b128 v[76:79], v24 offset:14336\nv_mfma_f32_16x16x32_bf16 v[44:47], a[36:39], v[36:39], v[44:47]\nds_read_b128 v[32:35], v24 offset:47104\n" \
"v_mfma_f32_16x16x32_bf16 v[48:51], a[36:39], v[40:43], v[48:51]\nv_mfma_f32_16x16x32_bf16 v[52:55], a[140:143], v[36:39], v[52:55]\ns_waitcnt lgkmcnt(2)\nds_read_b128 v[80:83], v25 offset:16384\n" \
"v_mfma_f32_16x16x32_bf16 v[56:59], a[140:143], v[40:43], v[56:59]\nv_mfma_f32_16x16x32_bf16 v[60:63], v[84:87], v[36:39], v[60:63]\nv_mfma_f32_16x16x32_bf16 v[64:67], v[84:87], v[40:43], v[64:67]\nv_mfma_f32_16x16x32_bf16 v[68:71], v[204:207], v[36:39], v[68:71]\n" \
"s_waitcnt lgkmcnt(1)\nv_mfma_f32_16x16x32_bf16 v[72:75], v[204:207], v[40:43], v[72:75]\nds_read_b128 v[36:39], v24 offset:15360\nv_mfma_f32_16x16x32_bf16 v[44:47], a[40:43], v[76:79], v[44:47]\n" \
"ds_read_b128 v[40:43], v24 offset:48128\nv_mfma_f32_16x16x32_bf16 v[48:51], a[40:43], v[32:35], v[48:51]\nv_mfma_f32_16x16x32_bf16 v[52:55], a[144:147], v[76:79], v[52:55]\nds_read_b128 v[84:87], v25 offset:17408\n" \
"v_mfma_f32_16x16x32_bf16 v[56:59], a[144:147], v[32:35], v[56:59]\nv_mfma_f32_16x16x32_bf16 v[60:63], a[248:251], v[76:79], v[60:63]\ns_waitcnt lgkmcnt(3)\nv_mfma_f32_16x16x32_bf16 v[64:67], a[248:251], v[32:35], v[64:67]\n" \
"s_waitcnt vmcnt(4) lgkmcnt(1)\ns_barrier\nv_mfma_f32_16x16x32_bf16 v[68:71], v[80:83], v[76:79], v[68:71]\ns_add_u32 m0, s32, 0\n" \
"s_add_u32 s99, s31, 0x10000\nv_mfma_f32_16x16x32_bf16 v[72:75], v[80:83], v[32:35], v[72:75]\nds_read_b128 v[76:79], v24 offset:16384\nv_mfma_f32_16x16x32_bf16 v[44:47], a[44:47], v[36:39], v[44:47]\n" \
"ds_read_b128 v[32:35], v24 offset:49152\nv_mfma_f32_16x16x32_bf16 v[48:51], a[44:47], v[40:43], v[48:51]\nds_read_b128 v[80:83], v25 offset:10240\nv_mfma_f32_16x16x32_bf16 v[52:55], a[148:151], v[36:39], v[52:55]\n" \
"buffer_load_dwordx4 v24, s[12:15], s99 offen offset:0 sc1 lds\nv_mfma_f32_16x16x32_bf16 v[56:59], a[148:151], v[40:43], v[56:59]\nv_mfma_f32_16x16x32_bf16 v[60:63], a[252:255], v[36:39], v[60:63]\ns_waitcnt lgkmcnt(3)\n" \
"v_mfma_f32_16x16x32_bf16 v[64:67], a[252:255], v[40:43], v[64:67]\nbuffer_load_dwordx4 v24, s[12:15], s99 offen offset:1024 sc1 lds\nv_mfma_f32_16x16x32_bf16 v[68:71], v[84:87], v[36:39], v[68:71]\ns_waitcnt lgkmcnt(1)\n" \
"v_mfma_f32_16x16x32_bf16 v[72:75], v[84:87], v[40:43], v[72:75]\nds_read_b128 v[36:39], v24 offset:17408\nv_mfma_f32_16x16x32_bf16 v[44:47], a[48:51], v[76:79], v[44:47]\nds_read_b128 v[40:43], v24 offset:50176\n" \
"v_mfma_f32_16x16x32_bf16 v[48:51], a[48:51], v[32:35], v[48:51]\nds_read_b128 v[84:87], v25 offset:11264\nv_mfma_f32_16x16x32_bf16 v[52:55], a[152:155], v[76:79], v[52:55]\ns_waitcnt lgkmcnt(3)\n" \
"buffer_load_dwordx4 v24, s[12:15], s99 offen offset:2048 sc1 lds\nv_mfma_f32_16x16x32_bf16 v[56:59], a[152:155], v[32:35], v[56:59]\nv_mfma_f32_16x16x32_bf16 v[60:63], v[80:83], v[76:79], v[60:63]\nv_mfma_f32_16x16x32_bf16 v[64:67], v[80:83], v[32:35], v[64:67]\n" \
"buffer_load_dwordx4 v24, s[12:15], s99 offen offset:3072 sc1 lds\nv_mfma_f32_16x16x32_bf16 v[68:71], v[208:211], v[76:79], v[68:71]\ns_waitcnt lgkmcnt(1)\nds_read_b128 v[76:79], v25 offset:6144\n" \
"v_mfma_f32_16x16x32_bf16 v[72:75], v[208:211], v[32:35], v[72:75]\nds_read_b128 v[80:83], v24 offset:18432\nv_mfma_f32_16x16x32_bf16 v[44:47], a[52:55], v[36:39], v[44:47]\nds_read_b128 v[32:35], v24 offset:51200\n" \
"s_add_u32 m0, s32, 0x2000\nv_mfma_f32_16x16x32_bf16 v[48:51], a[52:55], v[40:43], v[48:51]\ns_add_u32 s99, s31, 0x12000\nv_mfma_f32_16x16x32_bf16 v[52:55], a[156:159], v[36:39], v[52:55]\n" \
"s_waitcnt lgkmcnt(3)\nv_mfma_f32_16x16x32_bf16 v[56:59], a[156:159], v[40:43], v[56:59]\nv_mfma_f32_16x16x32_bf16 v[60:63], v[84:87], v[36:39], v[60:63]\nbuffer_load_dwordx4 v24, s[12:15], s99 offen offset:0 sc1 lds\n" \
"v_mfma_f32_16x16x32_bf16 v[64:67], v[84:87], v[40:43], v[64:67]\nv_mfma_f32_16x16x32_bf16 v[68:71], v[212:215], v[36:39], v[68:71]\ns_waitcnt lgkmcnt(0)\nds_read_b128 v[36:39], v25 offset:7168\n" \
"v_mfma_f32_16x16x32_bf16 v[72:75], v[212:215], v[40:43], v[72:75]\nds_read_b128 v[84:87], v24 offset:19456\nv_mfma_f32_16x16x32_bf16 v[44:47], a[56:59], v[80:83], v[44:47]\nds_read_b128 v[40:43], v24 offset:52224\n" \
"v_mfma_f32_16x16x32_bf16 v[48:51], a[56:59], v[32:35], v[48:51]\nbuffer_load_dwordx4 v24, s[12:15], s99 offen offset:1024 sc1 lds\nv_mfma_f32_16x16x32_bf16 v[52:55], v[76:79], v[80:83], v[52:55]\nv_mfma_f32_16x16x32_bf16 v[56:59], v[76:79], v[32:35], v[56:59]\n" \
"v_mfma_f32_16x16x32_bf16 v[60:63], v[112:115], v[80:83], v[60:63]\nds_read_b128 v[76:79], v25 offset:2048\nv_mfma_f32_16x16x32_bf16 v[64:67], v[112:115], v[32:35], v[64:67]\nbuffer_load_dwordx4 v24, s[12:15], s99 offen offset:2048 sc1 lds\n" \
"v_mfma_f32_16x16x32_bf16 v[68:71], v[216:219], v[80:83], v[68:71]\ns_waitcnt lgkmcnt(1)\nv_mfma_f32_16x16x32_bf16 v[72:75], v[216:219], v[32:35], v[72:75]\nds_read_b128 v[80:83], v24 offset:20480\n" \
"v_mfma_f32_16x16x32_bf16 v[44:47], a[60:63], v[84:87], v[44:47]\nds_read_b128 v[32:35], v24 offset:53248\nv_mfma_f32_16x16x32_bf16 v[48:51], a[60:63], v[40:43], v[48:51]\nbuffer_load_dwordx4 v24, s[12:15], s99 offen offset:3072 sc1 lds\n" \
"v_mfma_f32_16x16x32_bf16 v[52:55], v[36:39], v[84:87], v[52:55]\nv_mfma_f32_16x16x32_bf16 v[56:59], v[36:39], v[40:43], v[56:59]\nv_mfma_f32_16x16x32_bf16 v[60:63], v[116:119], v[84:87], v[60:63]\nv_mfma_f32_16x16x32_bf16 v[64:67], v[116:119], v[40:43], v[64:67]\n" \
"v_mfma_f32_16x16x32_bf16 v[68:71], v[220:223], v[84:87], v[68:71]\ns_cmp_eq_u32 s30, 64\nv_mfma_f32_16x16x32_bf16 v[72:75], v[220:223], v[40:43], v[72:75]\ns_cbranch_scc0 1b\n" \
"s_waitcnt vmcnt(0)\ns_barrier\nv_mov_b32 v40, 0x41000000\nbuffer_store_dword v40, v29, s[8:11], s49 offen offset:0 sc1\n" \
"s_branch 10f\n2:\ns_add_u32 s52, s52, 1\ns_cmp_lt_u32 s52, 0x40000\n" \
"s_cbranch_scc0 9f\nv_cmp_eq_u32 vcc, -1, v40\ns_cbranch_vccz 6f\nbuffer_load_dwordx4 v24, s[8:11], s94 offen offset:0 sc1 lds\n" \
"6:\nv_cmp_eq_u32 vcc, -1, v76\ns_cbranch_vccz 6f\nbuffer_load_dwordx4 v24, s[8:11], s94 offen offset:1024 sc1 lds\n" \
"6:\nv_cmp_eq_u32 vcc, -1, v84\ns_cbranch_vccz 6f\nbuffer_load_dwordx4 v24, s[8:11], s94 offen offset:2048 sc1 lds\n" \
"6:\nv_cmp_eq_u32 vcc, -1, v80\ns_cbranch_vccz 6f\nbuffer_load_dwordx4 v24, s[8:11], s94 offen offset:3072 sc1 lds\n" \
"6:\ns_waitcnt vmcnt(0)\nds_read_b128 v[40:43], v31 offset:16384\nds_read_b128 v[76:79], v31 offset:17408\n" \
"ds_read_b128 v[84:87], v31 offset:18432\nds_read_b128 v[80:83], v31 offset:19456\ns_waitcnt lgkmcnt(0)\nv_max3_u32 v96, v40, v76, v84\n" \
"v_max_u32 v96, v96, v80\nv_cmp_eq_u32 vcc, -1, v96\ns_cbranch_vccnz 2b\ns_branch 3b\n" \
"4:\ns_cmp_eq_u32 s30, 64\ns_cbranch_scc1 5b\nv_bfe_u32 v40, v24, 6, 3\n" \
"v_lshlrev_b32 v40, 2, v40\ns_lshl_b32 s94, s30, 5\ns_add_u32 s94, s94, s48\ns_cmp_lg_u32 s58, 0\n" \
"s_cbranch_scc1 8f\n7:\nbuffer_load_dword v41, v40, s[8:11], s94 offen sc1\ns_waitcnt vmcnt(0)\n" \
"v_cmp_eq_u32 vcc, -1, v41\ns_cbranch_vccz 8f\ns_add_u32 s52, s52, 1\ns_cmp_lt_u32 s52, 0x40000\n" \
"s_cbranch_scc0 9f\ns_sleep 8\ns_branch 7b\n8:\n" \
"s_mov_b32 m0, s53\ns_add_u32 s94, s30, 1\ns_min_u32 s94, s94, 63\ns_lshl_b32 s94, s94, 5\n" \
"s_add_u32 s94, s94, s48\nbuffer_load_dword v40, s[8:11], s94 offen offset:0 sc1 lds\ns_cmp_lg_u32 s58, 0\ns_mov_b32 s58, 0\n" \
"s_cbranch_scc1 6f\ns_add_u32 m0, s32, 0\ns_add_u32 s94, s31, 0\nbuffer_load_dwordx4 v24, s[8:11], s94 offen offset:0 sc1 lds\n" \
"buffer_load_dwordx4 v24, s[8:11], s94 offen offset:1024 sc1 lds\nbuffer_load_dwordx4 v24, s[8:11], s94 offen offset:2048 sc1 lds\nbuffer_load_dwordx4 v24, s[8:11], s94 offen offset:3072 sc1 lds\ns_add_u32 m0, s32, 0x2000\n" \
"s_add_u32 s94, s31, 0x2000\nbuffer_load_dwordx4 v24, s[8:11], s94 offen offset:0 sc1 lds\nbuffer_load_dwordx4 v24, s[8:11], s94 offen offset:1024 sc1 lds\nbuffer_load_dwordx4 v24, s[8:11], s94 offen offset:2048 sc1 lds\n" \
"buffer_load_dwordx4 v24, s[8:11], s94 offen offset:3072 sc1 lds\n6:\nv_lshrrev_b32 v40, 2, v24\nv_add_u32 v40, 0x11000, v40\n" \
"s_waitcnt vmcnt(0) lgkmcnt(0)\ns_barrier\nds_read_b32 v41, v40 offset:0\nds_read_b128 v[72:75], v24 offset:0\n" \
"ds_read_b128 v[32:35], v24 offset:32768\ns_waitcnt lgkmcnt(0)\nv_cmp_eq_u32 vcc, -1, v41\ns_cmp_eq_u64 vcc, 0\n" \
"s_cselect_b32 s14, s10, 0\ns_branch 5b\n10:\ns_waitcnt vmcnt(0) lgkmcnt(0)\n" \
"s_cmp_lt_i32 s6, 0\ns_cbranch_scc1 9f\ns_mov_b64 s[0:1], s[4:5]\ns_mov_b32 s2, s6\n" \
"v_lshrrev_b32 v0, 4, v24\nv_add_u32 v0, s7, v0\ns_barrier\ns_getpc_b64 s[8:9]\n" \
"11:\ns_add_u32 s8, s8, _Z5mega64Args-11b\ns_addc_u32 s9, s9, -1\ns_setpc_b64 s[8:9]\n" \
"9:\ns_waitcnt vmcnt(0)\ns_endpgm\n"
#define B_LDS 0x44840c0202a00110ull
#define B_VAR 0x0000000000010001ull
#define B_LV 8192
#define B_LOOP \
"v_mov_b32 v33, v20\nv_cmp_eq_u32 vcc, -1, v33\nv_bfe_u32 v32, v16, 4, 3\nv_lshlrev_b32 v32, 2, v32\n" \
"s_nop 2\ns_cbranch_vccz 6f\n7:\ns_add_u32 s30, s30, 1\n" \
"s_cmp_lt_u32 s30, 0x40000\ns_cbranch_scc0 9f\ns_sleep 8\nbuffer_load_dword v33, v32, s[8:11], s26 offen sc1\n" \
"s_waitcnt vmcnt(0)\nv_cmp_eq_u32 vcc, -1, v33\ns_nop 4\ns_cbranch_vccnz 7b\n" \
"s_add_u32 m0, s22, 0\ns_add_u32 s43, s21, 0\nbuffer_load_dwordx4 v16, s[8:11], s43 offen offset:0 sc1 lds\nbuffer_load_dwordx4 v16, s[8:11], s43 offen offset:1024 sc1 lds\n" \
"buffer_load_dwordx4 v16, s[8:11], s43 offen offset:2048 sc1 lds\nbuffer_load_dwordx4 v16, s[8:11], s43 offen offset:3072 sc1 lds\ns_add_u32 m0, s22, 0x2000\ns_add_u32 s43, s21, 0x2000\n" \
"buffer_load_dwordx4 v16, s[8:11], s43 offen offset:0 sc1 lds\nbuffer_load_dwordx4 v16, s[8:11], s43 offen offset:1024 sc1 lds\nbuffer_load_dwordx4 v16, s[8:11], s43 offen offset:2048 sc1 lds\nbuffer_load_dwordx4 v16, s[8:11], s43 offen offset:3072 sc1 lds\n" \
"s_add_u32 m0, s22, 0x4000\ns_add_u32 s43, s21, 0x4000\nbuffer_load_dwordx4 v16, s[8:11], s43 offen offset:0 sc1 lds\nbuffer_load_dwordx4 v16, s[8:11], s43 offen offset:1024 sc1 lds\n" \
"buffer_load_dwordx4 v16, s[8:11], s43 offen offset:2048 sc1 lds\nbuffer_load_dwordx4 v16, s[8:11], s43 offen offset:3072 sc1 lds\n6:\ns_waitcnt vmcnt(0)\n" \
"s_barrier\nds_read_b128 v[36:39], v16 offset:0\nds_read_b128 v[40:43], v16 offset:32768\nds_read_b128 v[72:75], v16 offset:1024\n" \
"ds_read_b128 v[80:83], v16 offset:33792\nds_read_b128 v[84:87], v16 offset:2048\nds_read_b128 v[88:91], v16 offset:34816\nds_read_b128 v[92:95], v16 offset:3072\n" \
"ds_read_b128 v[96:99], v16 offset:35840\nds_read_b128 v[48:51], v16 offset:4096\nds_read_b128 v[56:59], v16 offset:36864\nds_read_b128 v[60:63], v16 offset:5120\n" \
"ds_read_b128 v[64:67], v16 offset:37888\nds_read_b128 v[32:35], v16 offset:6144\nds_read_b128 v[76:79], v16 offset:38912\nds_read_b128 v[68:71], v16 offset:7168\n" \
"ds_read_b128 v[28:31], v16 offset:39936\ns_mov_b32 s20, -1\ns_add_u32 s21, s21, 0x10000\ns_mov_b32 s14, s10\n" \
"s_waitcnt lgkmcnt(0)\nv_mfma_f32_16x16x32_bf16 v[24:27], v[0:3], v[36:39], 0\nv_mfma_f32_16x16x32_bf16 v[44:47], v[0:3], v[40:43], 0\nv_mfma_f32_16x16x32_bf16 v[52:55], v[8:11], v[36:39], 0\n" \
"v_mfma_f32_16x16x32_bf16 v[20:23], v[8:11], v[40:43], 0\nv_mfma_f32_16x16x32_bf16 v[24:27], v[4:7], v[72:75], v[24:27]\nv_mfma_f32_16x16x32_bf16 v[44:47], v[4:7], v[80:83], v[44:47]\nv_mfma_f32_16x16x32_bf16 v[52:55], v[12:15], v[72:75], v[52:55]\n" \
"v_mfma_f32_16x16x32_bf16 v[20:23], v[12:15], v[80:83], v[20:23]\nv_mfma_f32_16x16x32_bf16 v[24:27], a[0:3], v[84:87], v[24:27]\nv_mfma_f32_16x16x32_bf16 v[44:47], a[0:3], v[88:91], v[44:47]\nv_mfma_f32_16x16x32_bf16 v[52:55], a[104:107], v[84:87], v[52:55]\n" \
"v_mfma_f32_16x16x32_bf16 v[20:23], a[104:107], v[88:91], v[20:23]\nv_mfma_f32_16x16x32_bf16 v[24:27], a[4:7], v[92:95], v[24:27]\nv_mfma_f32_16x16x32_bf16 v[44:47], a[4:7], v[96:99], v[44:47]\nv_mfma_f32_16x16x32_bf16 v[52:55], a[108:111], v[92:95], v[52:55]\n" \
"v_mfma_f32_16x16x32_bf16 v[20:23], a[108:111], v[96:99], v[20:23]\ns_branch 1f\n.p2align 6\n1:\n" \
"s_waitcnt lgkmcnt(4)\nv_mfma_f32_16x16x32_bf16 v[24:27], a[8:11], v[48:51], v[24:27]\ns_add_u32 m0, s22, 0\ns_add_u32 s43, s20, 7\n" \
"s_and_b32 s43, s43, 7\nv_mfma_f32_16x16x32_bf16 v[44:47], a[8:11], v[56:59], v[44:47]\nds_read_b128 v[36:39], v17 offset:0\ns_lshl_b32 s43, s43, 18\n" \
"v_mfma_f32_16x16x32_bf16 v[52:55], a[112:115], v[48:51], v[52:55]\ns_add_u32 s43, s43, s23\nv_mfma_f32_16x16x32_bf16 v[20:23], a[112:115], v[56:59], v[20:23]\nv_mfma_f32_16x16x32_bf16 v[24:27], a[12:15], v[60:63], v[24:27]\n" \
"s_waitcnt vmcnt(7) lgkmcnt(1)\ns_barrier\nv_mfma_f32_16x16x32_bf16 v[44:47], a[12:15], v[64:67], v[44:47]\nds_read_b128 v[40:43], v16 offset:8192\n" \
"v_mfma_f32_16x16x32_bf16 v[52:55], a[116:119], v[60:63], v[52:55]\nds_read_b128 v[48:51], v16 offset:40960\nv_mfma_f32_16x16x32_bf16 v[20:23], a[116:119], v[64:67], v[20:23]\nds_read_b128 v[60:63], v16 offset:9216\n" \
"v_mfma_f32_16x16x32_bf16 v[24:27], a[16:19], v[32:35], v[24:27]\nds_read_b128 v[64:67], v17 offset:1024\nv_mfma_f32_16x16x32_bf16 v[44:47], a[16:19], v[76:79], v[44:47]\nds_read_b128 v[56:59], v16 offset:41984\n" \
"v_mfma_f32_16x16x32_bf16 v[52:55], a[120:123], v[32:35], v[52:55]\nv_mfma_f32_16x16x32_bf16 v[20:23], a[120:123], v[76:79], v[20:23]\nbuffer_load_dwordx4 v16, s[8:11], s43 offen offset:0 sc1 lds\nv_mfma_f32_16x16x32_bf16 v[24:27], a[20:23], v[68:71], v[24:27]\n" \
"ds_read_b128 v[32:35], v16 offset:10240\nv_mfma_f32_16x16x32_bf16 v[44:47], a[20:23], v[28:31], v[44:47]\nds_read_b128 v[72:75], v16 offset:43008\nv_mfma_f32_16x16x32_bf16 v[52:55], a[124:127], v[68:71], v[52:55]\n" \
"s_waitcnt lgkmcnt(3)\nbuffer_load_dwordx4 v16, s[8:11], s43 offen offset:1024 sc1 lds\nv_mfma_f32_16x16x32_bf16 v[20:23], a[124:127], v[28:31], v[20:23]\nds_read_b128 v[68:71], v17 offset:4096\n" \
"v_mfma_f32_16x16x32_bf16 v[24:27], v[36:39], v[40:43], v[24:27]\nds_read_b128 v[28:31], v16 offset:11264\nv_mfma_f32_16x16x32_bf16 v[44:47], v[36:39], v[48:51], v[44:47]\nbuffer_load_dwordx4 v16, s[8:11], s43 offen offset:2048 sc1 lds\n" \
"v_mfma_f32_16x16x32_bf16 v[52:55], a[128:131], v[40:43], v[52:55]\nds_read_b128 v[76:79], v16 offset:44032\nv_mfma_f32_16x16x32_bf16 v[20:23], a[128:131], v[48:51], v[20:23]\ns_waitcnt lgkmcnt(3)\n" \
"ds_read_b128 v[36:39], v17 offset:5120\nv_mfma_f32_16x16x32_bf16 v[24:27], v[64:67], v[60:63], v[24:27]\nbuffer_load_dwordx4 v16, s[8:11], s43 offen offset:3072 sc1 lds\nv_mfma_f32_16x16x32_bf16 v[44:47], v[64:67], v[56:59], v[44:47]\n" \
"ds_read_b128 v[48:51], v16 offset:12288\ns_add_u32 m0, s22, 0x6000\nv_mfma_f32_16x16x32_bf16 v[52:55], a[132:135], v[60:63], v[52:55]\nds_read_b128 v[64:67], v16 offset:45056\n" \
"s_add_u32 s42, s21, -0xa000\nv_mfma_f32_16x16x32_bf16 v[20:23], a[132:135], v[56:59], v[20:23]\nds_read_b128 v[60:63], v16 offset:13312\nv_mfma_f32_16x16x32_bf16 v[24:27], a[24:27], v[32:35], v[24:27]\n" \
"s_waitcnt lgkmcnt(4)\nds_read_b128 v[40:43], v16 offset:46080\nv_mfma_f32_16x16x32_bf16 v[44:47], a[24:27], v[72:75], v[44:47]\nbuffer_load_dwordx4 v16, s[12:15], s42 offen offset:0 sc1 lds\n" \
"v_mfma_f32_16x16x32_bf16 v[52:55], v[68:71], v[32:35], v[52:55]\nv_mfma_f32_16x16x32_bf16 v[20:23], v[68:71], v[72:75], v[20:23]\nds_read_b128 v[56:59], v16 offset:14336\nv_mfma_f32_16x16x32_bf16 v[24:27], a[28:31], v[28:31], v[24:27]\n" \
"s_waitcnt lgkmcnt(3)\nbuffer_load_dwordx4 v16, s[12:15], s42 offen offset:1024 sc1 lds\nv_mfma_f32_16x16x32_bf16 v[44:47], a[28:31], v[76:79], v[44:47]\nds_read_b128 v[32:35], v16 offset:47104\n" \
"v_mfma_f32_16x16x32_bf16 v[52:55], v[36:39], v[28:31], v[52:55]\nds_read_b128 v[68:71], v17 offset:6144\nv_mfma_f32_16x16x32_bf16 v[20:23], v[36:39], v[76:79], v[20:23]\nbuffer_load_dwordx4 v16, s[12:15], s42 offen offset:2048 sc1 lds\n" \
"v_mfma_f32_16x16x32_bf16 v[24:27], a[32:35], v[48:51], v[24:27]\nds_read_b128 v[72:75], v16 offset:15360\nv_lshlrev_b32 v37, 5, v16\nv_mfma_f32_16x16x32_bf16 v[44:47], a[32:35], v[64:67], v[44:47]\n" \
"ds_read_b128 v[28:31], v16 offset:48128\nv_and_b32 v36, 0x7000, v37\nv_mfma_f32_16x16x32_bf16 v[52:55], a[136:139], v[48:51], v[52:55]\ns_waitcnt lgkmcnt(4)\n" \
"buffer_load_dwordx4 v16, s[12:15], s42 offen offset:3072 sc1 lds\nv_mfma_f32_16x16x32_bf16 v[20:23], a[136:139], v[64:67], v[20:23]\nds_read_b128 v[48:51], v17 offset:7168\ns_add_u32 m0, s22, 0\n" \
"v_mfma_f32_16x16x32_bf16 v[24:27], a[36:39], v[60:63], v[24:27]\nds_read_b128 v[64:67], v16 offset:16384\nv_lshl_add_u32 v37, v36, 17, v37\nv_mfma_f32_16x16x32_bf16 v[44:47], a[36:39], v[40:43], v[44:47]\n" \
"ds_read_b128 v[76:79], v17 offset:2048\nv_mfma_f32_16x16x32_bf16 v[52:55], a[140:143], v[60:63], v[52:55]\nds_read_b128 v[80:83], v16 offset:49152\nv_mfma_f32_16x16x32_bf16 v[20:23], a[140:143], v[40:43], v[20:23]\n" \
"s_waitcnt lgkmcnt(4)\nds_read_b128 v[60:63], v16 offset:17408\nv_mfma_f32_16x16x32_bf16 v[24:27], a[40:43], v[56:59], v[24:27]\nds_read_b128 v[38:41], v17 offset:3072\n" \
"v_mfma_f32_16x16x32_bf16 v[44:47], a[40:43], v[32:35], v[44:47]\nds_read_b128 v[84:87], v16 offset:50176\nv_mfma_f32_16x16x32_bf16 v[52:55], v[68:71], v[56:59], v[52:55]\nv_mfma_f32_16x16x32_bf16 v[20:23], v[68:71], v[32:35], v[20:23]\n" \
"ds_read_b128 v[56:59], v16 offset:18432\nv_mfma_f32_16x16x32_bf16 v[24:27], a[44:47], v[72:75], v[24:27]\ns_waitcnt lgkmcnt(4)\nds_read_b128 v[32:35], v16 offset:51200\n" \
"v_mfma_f32_16x16x32_bf16 v[44:47], a[44:47], v[28:31], v[44:47]\nds_read_b128 v[68:71], v17 offset:8192\nv_add_u32 v36, s22, v16\nv_mfma_f32_16x16x32_bf16 v[52:55], v[48:51], v[72:75], v[52:55]\n" \
"v_mfma_f32_16x16x32_bf16 v[20:23], v[48:51], v[28:31], v[20:23]\nds_read_b128 v[72:75], v16 offset:19456\nv_mfma_f32_16x16x32_bf16 v[24:27], v[76:79], v[64:67], v[24:27]\nds_read_b128 v[28:31], v16 offset:52224\n" \
"v_mfma_f32_16x16x32_bf16 v[44:47], v[76:79], v[80:83], v[44:47]\nds_read_b128 v[48:51], v17 offset:9216\nv_mfma_f32_16x16x32_bf16 v[52:55], a[144:147], v[64:67], v[52:55]\ns_waitcnt lgkmcnt(5)\n" \
"ds_read_b128 v[92:95], v17 offset:9216\nv_mfma_f32_16x16x32_bf16 v[20:23], a[144:147], v[80:83], v[20:23]\nds_read_b128 v[64:67], v16 offset:20480\nv_mfma_f32_16x16x32_bf16 v[24:27], v[38:41], v[60:63], v[24:27]\n" \
"ds_read_b128 v[76:79], v16 offset:53248\nv_mfma_f32_16x16x32_bf16 v[44:47], v[38:41], v[84:87], v[44:47]\nds_read_b128 v[88:91], v17 offset:8192\nv_mfma_f32_16x16x32_bf16 v[52:55], a[148:151], v[60:63], v[52:55]\n" \
"s_waitcnt vmcnt(4)\nds_read_b128 v[38:41], v36 offset:0\nds_read_b128 v[60:63], v36 offset:1024\nds_read_b128 v[80:83], v36 offset:2048\n" \
"v_mfma_f32_16x16x32_bf16 v[20:23], a[148:151], v[84:87], v[20:23]\ns_waitcnt lgkmcnt(7)\nds_read_b128 v[96:99], v36 offset:3072\nv_mfma_f32_16x16x32_bf16 v[24:27], a[48:51], v[56:59], v[24:27]\n" \
"ds_read_b128 v[100:103], v17 offset:4096\nv_mfma_f32_16x16x32_bf16 v[44:47], a[48:51], v[32:35], v[44:47]\nds_read_b128 v[104:107], v17 offset:5120\nv_mov_b64 v[84:85], 0\n" \
"v_mfma_f32_16x16x32_bf16 v[52:55], v[68:71], v[56:59], v[52:55]\ns_waitcnt lgkmcnt(3)\nv_max3_u32 v42, v38, v60, v80\nv_mov_b64 v[86:87], 0\n" \
"v_mfma_f32_16x16x32_bf16 v[20:23], v[68:71], v[32:35], v[20:23]\ns_waitcnt lgkmcnt(2)\nv_max_u32 v42, v42, v96\nv_cmp_eq_u32 vcc, -1, v42\n" \
"v_mfma_f32_16x16x32_bf16 v[24:27], a[52:55], v[72:75], v[24:27]\nv_mov_b64 v[32:33], 0\nv_mov_b64 v[34:35], 0\nv_mfma_f32_16x16x32_bf16 v[44:47], a[52:55], v[28:31], v[44:47]\n" \
"s_cbranch_vccnz 2f\n3:\nv_mfma_f32_16x16x32_bf16 v[52:55], v[48:51], v[72:75], v[52:55]\ns_barrier\n" \
"ds_read_b128 v[38:41], v16 offset:2048\nv_mfma_f32_16x16x32_bf16 v[20:23], v[92:95], v[28:31], v[20:23]\ns_memrealtime s[40:41]\nds_read_b128 v[56:59], v16 offset:5120\n" \
"v_mfma_f32_16x16x32_bf16 v[24:27], a[56:59], v[64:67], v[24:27]\nds_read_b128 v[28:31], v16 offset:1024\ns_lshl_b32 s43, s20, 24\nv_mfma_f32_16x16x32_bf16 v[44:47], a[56:59], v[76:79], v[44:47]\n" \
"ds_read_b128 v[48:51], v16 offset:33792\nv_mfma_f32_16x16x32_bf16 v[52:55], a[152:155], v[64:67], v[52:55]\nv_mfma_f32_16x16x32_bf16 v[20:23], a[152:155], v[76:79], v[20:23]\ns_waitcnt lgkmcnt(3)\n" \
"ds_read_b128 v[60:63], v16 offset:3072\nv_smfmac_f32_16x16x64_bf16 v[84:87], v[38:41], a[8:15], v18\ns_waitcnt lgkmcnt(3)\nv_smfmac_f32_16x16x64_bf16 v[32:35], v[38:41], a[112:119], v18\n" \
"ds_read_b128 v[64:67], v16 offset:34816\nv_smfmac_f32_16x16x64_bf16 v[84:87], v[56:59], a[24:31], v18\ns_waitcnt lgkmcnt(3)\nds_read_b128 v[68:71], v17 offset:6144\n" \
"v_smfmac_f32_16x16x64_bf16 v[32:35], v[56:59], v[100:107], v18\nds_read_b128 v[38:41], v16 offset:36864\nv_smfmac_f32_16x16x64_bf16 v[84:87], v[28:31], a[0:7], v18\ns_waitcnt lgkmcnt(4)\n" \
"ds_read_b128 v[72:75], v17 offset:7168\nv_smfmac_f32_16x16x64_bf16 v[32:35], v[28:31], a[104:111], v18\nds_read_b128 v[56:59], v16 offset:6144\ns_add_u32 s42, s40, 0x67\n" \
"v_smfmac_f32_16x16x64_bf16 v[84:87], v[48:51], a[48:55], v18\ns_and_b32 s42, s42, 0xffffff\ns_or_b32 s42, s42, s43\nv_mov_b32 v28, s42\n" \
"v_smfmac_f32_16x16x64_bf16 v[32:35], v[48:51], v[88:95], v18\ns_waitcnt lgkmcnt(4)\nds_read_b128 v[76:79], v16 offset:7168\nv_smfmac_f32_16x16x64_bf16 v[84:87], v[60:63], a[16:23], v18\n" \
"buffer_store_dword v28, v37, s[8:11], s27 offen offset:0 sc1\nv_smfmac_f32_16x16x64_bf16 v[32:35], v[60:63], a[120:127], v18\nds_read_b128 v[48:51], v16 offset:35840\nv_smfmac_f32_16x16x64_bf16 v[84:87], v[64:67], a[56:63], v18\n" \
"s_waitcnt lgkmcnt(4)\nv_smfmac_f32_16x16x64_bf16 v[32:35], v[64:67], a[152:159], v18\nds_read_b128 v[28:31], v16 offset:0\ns_add_u32 s16, s20, 1\n" \
"v_smfmac_f32_16x16x64_bf16 v[84:87], v[38:41], a[72:79], v18\nds_read_b128 v[60:63], v17 offset:0\nv_mov_b64 v[36:37], 0\nv_smfmac_f32_16x16x64_bf16 v[32:35], v[38:41], a[168:175], v18\n" \
"s_waitcnt lgkmcnt(4)\nds_read_b128 v[40:43], v16 offset:39936\nv_smfmac_f32_16x16x64_bf16 v[84:87], v[56:59], a[32:39], v18\nds_read_b128 v[64:67], v17 offset:1024\n" \
"v_mov_b64 v[38:39], 0\nv_smfmac_f32_16x16x64_bf16 v[32:35], v[56:59], a[136:143], v18\ns_waitcnt lgkmcnt(4)\nds_read_b128 v[56:59], v16 offset:37888\n" \
"v_smfmac_f32_16x16x64_bf16 v[84:87], v[76:79], a[40:47], v18\nds_read_b128 v[88:91], v17 offset:2048\ns_and_b32 s16, s16, 7\nv_smfmac_f32_16x16x64_bf16 v[32:35], v[76:79], v[68:75], v18\n" \
"ds_read_b128 v[80:83], v16 offset:4096\ns_lshl_b32 s16, s16, 22\nv_smfmac_f32_16x16x64_bf16 v[84:87], v[48:51], a[64:71], v18\nds_read_b128 v[92:95], v17 offset:3072\n" \
"s_add_u32 s16, s16, s25\nv_smfmac_f32_16x16x64_bf16 v[32:35], v[48:51], a[160:167], v18\ns_waitcnt lgkmcnt(6)\nds_read_b128 v[48:51], v16 offset:38912\n" \
"v_smfmac_f32_16x16x64_bf16 v[84:87], v[28:31], v[0:7], v18\nds_read_b128 v[72:75], v16 offset:21504\ns_add_u32 s17, s16, 0x1000\nv_smfmac_f32_16x16x64_bf16 v[32:35], v[28:31], v[8:15], v18\n" \
"s_waitcnt lgkmcnt(6)\nds_read_b128 v[28:31], v16 offset:32768\nv_smfmac_f32_16x16x64_bf16 v[84:87], v[40:43], a[96:103], v18\nds_read_b128 v[68:71], v16 offset:54272\n" \
"s_add_u32 s18, s16, 0x4000\nv_smfmac_f32_16x16x64_bf16 v[32:35], v[40:43], a[192:199], v18\ns_waitcnt lgkmcnt(5)\nds_read_b128 v[40:43], v16 offset:22528\n" \
"v_smfmac_f32_16x16x64_bf16 v[84:87], v[56:59], a[80:87], v18\nds_read_b128 v[76:79], v16 offset:55296\nv_lshrrev_b32 v96, 16, v18\nv_smfmac_f32_16x16x64_bf16 v[32:35], v[56:59], a[176:183], v18\n" \
"ds_write_b128 v16, v[36:39] offset:45056\nv_smfmac_f32_16x16x64_bf16 v[84:87], v[80:83], v[60:67], v18\ns_add_u32 s19, s16, 0x5000\ns_and_b32 s43, s20, 2\n" \
"s_lshl_b32 s43, s43, 12\nv_smfmac_f32_16x16x64_bf16 v[32:35], v[80:83], a[128:135], v18\ns_waitcnt lgkmcnt(4)\nds_read_b128 v[56:59], v16 offset:23552\n" \
"v_smfmac_f32_16x16x64_bf16 v[84:87], v[48:51], a[88:95], v18\nds_read_b128 v[60:63], v16 offset:56320\ns_and_b32 s42, s20, 7\nv_smfmac_f32_16x16x64_bf16 v[32:35], v[48:51], a[184:191], v18\n" \
"ds_read_b128 v[98:101], v17 offset:10240\ns_lshl_b32 s42, s42, 20\nv_smfmac_f32_16x16x64_bf16 v[84:87], v[28:31], v[88:95], v18\nds_read_b128 v[102:105], v17 offset:11264\n" \
"s_add_u32 s42, s42, s24\nv_smfmac_f32_16x16x64_bf16 v[32:35], v[28:31], a[144:151], v18\ns_waitcnt lgkmcnt(4)\nv_mfma_f32_16x16x32_bf16 v[24:27], a[60:63], v[72:75], v[24:27]\n" \
"v_add_f32 v84, v84, v86\nv_add_f32 v85, v85, v87\nv_cvt_pk_bf16_f32 v84, v84, v85\nv_mfma_f32_16x16x32_bf16 v[44:47], a[60:63], v[68:71], v[44:47]\n" \
"v_add_f32 v32, v32, v34\nv_add_f32 v33, v33, v35\nv_cvt_pk_bf16_f32 v32, v32, v33\nv_mfma_f32_16x16x32_bf16 v[52:55], a[156:159], v[72:75], v[52:55]\n" \
"ds_write2_b32 v96, v84, v32 offset0:0 offset1:4\nv_mfma_f32_16x16x32_bf16 v[20:23], a[156:159], v[68:71], v[20:23]\ns_add_u32 s99, s20, 2\nv_mov_b64 v[28:29], 0\n" \
"v_mov_b64 v[30:31], 0\nv_mfma_f32_16x16x32_bf16 v[24:27], a[64:67], v[40:43], v[24:27]\ns_min_u32 s99, s99, 63\nv_bfe_u32 v32, v16, 4, 3\n" \
"v_lshlrev_b32 v32, 2, v32\nv_mfma_f32_16x16x32_bf16 v[44:47], a[64:67], v[76:79], v[44:47]\ns_waitcnt vmcnt(1) lgkmcnt(0)\ns_barrier\n" \
"ds_read_b64_tr_b16 v[48:49], v19 offset:0\nv_mfma_f32_16x16x32_bf16 v[52:55], a[160:163], v[40:43], v[52:55]\nds_read_b64_tr_b16 v[50:51], v19 offset:256\nds_read_b64_tr_b16 v[40:41], v19 offset:1024\n" \
"ds_read_b64_tr_b16 v[42:43], v19 offset:1280\nv_mfma_f32_16x16x32_bf16 v[20:23], a[160:163], v[76:79], v[20:23]\nds_read_b128 v[64:67], v16 offset:24576\nv_mov_b64 v[68:69], 0\n" \
"v_mfma_f32_16x16x32_bf16 v[24:27], a[68:71], v[56:59], v[24:27]\ns_lshl_b32 s99, s99, 5\ns_add_u32 s99, s99, s26\nv_mov_b64 v[70:71], 0\n" \
"v_mfma_f32_16x16x32_bf16 v[44:47], a[68:71], v[60:63], v[44:47]\ns_waitcnt lgkmcnt(3)\nbuffer_load_dword v33, v32, s[8:11], s99 offen sc1\nv_mfma_f32_16x16x32_bf16 v[52:55], a[164:167], v[56:59], v[52:55]\n" \
"ds_read_b128 v[72:75], v16 offset:25600\nv_mov_b64 v[76:77], 0\nv_smfmac_f32_16x16x64_bf16 v[36:39], v[48:51], a[200:207], v18\ns_waitcnt lgkmcnt(2)\n" \
"ds_read_b128 v[88:91], v17 offset:12288\nv_smfmac_f32_16x16x64_bf16 v[36:39], v[40:43], v[98:105], v18\nds_read_b128 v[56:59], v16 offset:45056\nv_mov_b64 v[78:79], 0\n" \
"v_smfmac_f32_16x16x64_bf16 v[28:31], v[48:51], a[224:231], v18\nds_read_b128 v[92:95], v17 offset:13312\nv_mfma_f32_16x16x32_bf16 v[20:23], a[164:167], v[60:63], v[20:23]\nds_read_b128 v[80:83], v17 offset:14336\n" \
"v_add_f32 v36, v36, v38\nv_smfmac_f32_16x16x64_bf16 v[28:31], v[40:43], a[232:239], v18\ns_waitcnt lgkmcnt(5)\nds_read_b128 v[60:63], v16 offset:45056\n" \
"v_mfma_f32_16x16x32_bf16 v[24:27], a[72:75], v[64:67], v[24:27]\nds_read_b128 v[84:87], v17 offset:15360\nv_add_f32 v37, v37, v39\nv_smfmac_f32_16x16x64_bf16 v[68:71], v[48:51], a[208:215], v18\n" \
"ds_read_b128 v[108:111], v16 offset:26624\nv_add_f32 v28, v28, v30\nv_smfmac_f32_16x16x64_bf16 v[68:71], v[40:43], a[216:223], v18\nds_read_b128 v[96:99], v16 offset:45056\n" \
"v_add_f32 v29, v29, v31\nv_smfmac_f32_16x16x64_bf16 v[76:79], v[48:51], a[240:247], v18\nds_read_b128 v[100:103], v17 offset:16384\nv_cvt_pk_bf16_f32 v34, v36, v37\n" \
"v_mfma_f32_16x16x32_bf16 v[52:55], a[168:171], v[64:67], v[52:55]\ns_waitcnt lgkmcnt(5)\nv_add_f32 v68, v68, v70\nv_add_f32 v69, v69, v71\n" \
"v_smfmac_f32_16x16x64_bf16 v[76:79], v[40:43], a[248:255], v18\nds_read_b128 v[64:67], v16 offset:45056\nv_cvt_pk_bf16_f32 v35, v28, v29\nv_smfmac_f32_16x16x64_bf16 v[56:59], v[48:51], v[112:119], v18\n" \
"ds_read_b128 v[104:107], v17 offset:17408\nv_cvt_pk_bf16_f32 v36, v68, v69\nv_mfma_f32_16x16x32_bf16 v[24:27], a[76:79], v[72:75], v[24:27]\ns_waitcnt lgkmcnt(4)\n" \
"v_permlane32_swap_b32 v34, v35\nv_smfmac_f32_16x16x64_bf16 v[56:59], v[40:43], v[120:127], v18\nds_read_b128 v[68:71], v16 offset:45056\nv_add_f32 v76, v76, v78\n" \
"v_smfmac_f32_16x16x64_bf16 v[60:63], v[48:51], v[128:135], v18\nds_read_b128 v[28:31], v16 offset:27648\nv_add_f32 v77, v77, v79\nv_cvt_pk_bf16_f32 v37, v76, v77\n" \
"v_smfmac_f32_16x16x64_bf16 v[60:63], v[40:43], v[136:143], v18\nds_read_b128 v[76:79], v17 offset:18432\nv_permlane32_swap_b32 v36, v37\nv_mfma_f32_16x16x32_bf16 v[52:55], a[172:175], v[72:75], v[52:55]\n" \
"s_waitcnt lgkmcnt(4)\nv_permlane16_swap_b32 v34, v36\nv_smfmac_f32_16x16x64_bf16 v[96:99], v[48:51], v[88:95], v18\nds_read_b128 v[72:75], v16 offset:45056\n" \
"v_permlane16_swap_b32 v35, v37\nv_and_or_b32 v34, v34, -2, s32\nv_smfmac_f32_16x16x64_bf16 v[96:99], v[40:43], v[80:87], v18\nds_read_b128 v[88:91], v16 offset:28672\n" \
"v_add_f32 v56, v56, v58\nv_mfma_f32_16x16x32_bf16 v[24:27], a[80:83], v[108:111], v[24:27]\nbuffer_store_dwordx4 v[34:37], v16, s[8:11], s29 offen offset:0 sc1\nv_add_f32 v57, v57, v59\n" \
"v_add_f32 v60, v60, v62\nv_add_f32 v61, v61, v63\nv_smfmac_f32_16x16x64_bf16 v[64:67], v[48:51], v[144:151], v18\nds_read_b128 v[34:37], v16 offset:45056\n" \
"v_add_f32 v96, v96, v98\nv_smfmac_f32_16x16x64_bf16 v[64:67], v[40:43], v[152:159], v18\nds_read_b128 v[80:83], v17 offset:19456\nv_add_f32 v97, v97, v99\n" \
"v_mfma_f32_16x16x32_bf16 v[52:55], a[176:179], v[108:111], v[52:55]\ns_waitcnt lgkmcnt(3)\nv_cvt_pk_bf16_f32 v58, v56, v57\nv_cvt_pk_bf16_f32 v59, v60, v61\n" \
"v_smfmac_f32_16x16x64_bf16 v[68:71], v[48:51], v[160:167], v18\nds_read_b128 v[92:95], v16 offset:45056\nv_add_f32 v64, v64, v66\nv_smfmac_f32_16x16x64_bf16 v[68:71], v[40:43], v[168:175], v18\n" \
"ds_read_b128 v[84:87], v16 offset:45056\nv_add_f32 v65, v65, v67\nv_mfma_f32_16x16x32_bf16 v[24:27], a[84:87], v[28:31], v[24:27]\nv_permlane32_swap_b32 v58, v59\n" \
"v_cvt_pk_bf16_f32 v60, v96, v97\nv_cvt_pk_bf16_f32 v61, v64, v65\nv_smfmac_f32_16x16x64_bf16 v[72:75], v[48:51], v[184:191], v18\nds_read_b128 v[62:65], v16 offset:29696\n" \
"v_permlane32_swap_b32 v60, v61\nv_smfmac_f32_16x16x64_bf16 v[72:75], v[40:43], v[192:199], v18\ns_waitcnt lgkmcnt(3)\nv_permlane16_swap_b32 v58, v60\n" \
"v_mfma_f32_16x16x32_bf16 v[52:55], a[180:183], v[28:31], v[52:55]\nv_permlane16_swap_b32 v59, v61\nv_and_or_b32 v58, v58, -2, s32\nv_smfmac_f32_16x16x64_bf16 v[34:37], v[48:51], v[100:107], v18\n" \
"ds_read_b128 v[28:31], v16 offset:45056\nv_add_f32 v68, v68, v70\nv_smfmac_f32_16x16x64_bf16 v[34:37], v[40:43], v[176:183], v18\ns_waitcnt lgkmcnt(2)\n" \
"buffer_store_dwordx4 v[58:61], v16, s[8:11], s29 offen offset:1024 sc1\nds_read_b128 v[96:99], v17 offset:20480\nv_add_f32 v69, v69, v71\nv_mfma_f32_16x16x32_bf16 v[24:27], a[88:91], v[88:91], v[24:27]\n" \
"ds_read_b128 v[100:103], v17 offset:21504\nv_add_f32 v72, v72, v74\nv_smfmac_f32_16x16x64_bf16 v[92:95], v[48:51], v[200:207], v18\nds_read_b128 v[56:59], v16 offset:45056\n" \
"v_add_f32 v73, v73, v75\nv_smfmac_f32_16x16x64_bf16 v[92:95], v[40:43], v[76:83], v18\nds_read_b128 v[104:107], v16 offset:30720\nv_add_f32 v34, v34, v36\n" \
"v_smfmac_f32_16x16x64_bf16 v[84:87], v[48:51], v[208:215], v18\nds_read_b128 v[74:77], v16 offset:45056\nv_add_f32 v35, v35, v37\nv_mfma_f32_16x16x32_bf16 v[52:55], a[184:187], v[88:91], v[52:55]\n" \
"s_waitcnt lgkmcnt(5)\nv_add_f32 v92, v92, v94\nv_add_f32 v93, v93, v95\nv_smfmac_f32_16x16x64_bf16 v[84:87], v[40:43], v[216:223], v18\n" \
"ds_read_b128 v[88:91], v17 offset:22528\nv_cvt_pk_bf16_f32 v36, v68, v69\nv_mfma_f32_16x16x32_bf16 v[24:27], a[92:95], v[62:65], v[24:27]\nv_cvt_pk_bf16_f32 v37, v72, v73\n" \
"v_cvt_pk_bf16_f32 v38, v34, v35\nv_cvt_pk_bf16_f32 v39, v92, v93\nv_smfmac_f32_16x16x64_bf16 v[28:31], v[48:51], v[232:239], v18\nds_read_b128 v[92:95], v17 offset:23552\n" \
"v_permlane32_swap_b32 v36, v37\nv_permlane32_swap_b32 v38, v39\nv_smfmac_f32_16x16x64_bf16 v[28:31], v[40:43], v[240:247], v18\nds_read_b128 v[66:69], v16 offset:31744\n" \
"v_permlane16_swap_b32 v36, v38\nv_mfma_f32_16x16x32_bf16 v[52:55], a[188:191], v[62:65], v[52:55]\ns_waitcnt lgkmcnt(3)\nv_permlane16_swap_b32 v37, v39\n" \
"v_and_or_b32 v36, v36, -2, s32\nv_smfmac_f32_16x16x64_bf16 v[56:59], v[48:51], v[96:103], v18\nds_read_b128 v[60:63], v16 offset:57344\nv_add_f32 v84, v84, v86\n" \
"v_smfmac_f32_16x16x64_bf16 v[56:59], v[40:43], v[224:231], v18\nds_read_b128 v[70:73], v16 offset:58368\nv_add_f32 v85, v85, v87\nv_mfma_f32_16x16x32_bf16 v[24:27], a[96:99], v[104:107], v[24:27]\n" \
"s_waitcnt lgkmcnt(3)\nbuffer_store_dwordx4 v[36:39], v16, s[8:11], s29 offen offset:2048 sc1\nv_add_f32 v28, v28, v30\nv_add_f32 v29, v29, v31\n" \
"v_cvt_pk_bf16_f32 v34, v84, v85\nv_smfmac_f32_16x16x64_bf16 v[74:77], v[48:51], v[88:95], v18\nds_read_b128 v[78:81], v16 offset:59392\nv_add_f32 v56, v56, v58\n" \
"v_add_f32 v57, v57, v59\nv_cvt_pk_bf16_f32 v35, v28, v29\nv_cvt_pk_bf16_f32 v36, v56, v57\nv_smfmac_f32_16x16x64_bf16 v[74:77], v[40:43], v[248:255], v18\n" \
"ds_read_b128 v[48:51], v16 offset:60416\nv_permlane32_swap_b32 v34, v35\nv_mfma_f32_16x16x32_bf16 v[52:55], a[192:195], v[104:107], v[52:55]\ns_waitcnt lgkmcnt(3)\n" \
"ds_read_b128 v[28:31], v16 offset:61440\nv_mfma_f32_16x16x32_bf16 v[24:27], a[100:103], v[66:69], v[24:27]\nv_add_f32 v74, v74, v76\nv_add_f32 v75, v75, v77\n" \
"v_cvt_pk_bf16_f32 v37, v74, v75\nv_mfma_f32_16x16x32_bf16 v[52:55], a[196:199], v[66:69], v[52:55]\nds_read_b128 v[38:41], v16 offset:62464\nv_permlane32_swap_b32 v36, v37\n" \
"v_mfma_f32_16x16x32_bf16 v[44:47], a[72:75], v[60:63], v[44:47]\ns_waitcnt lgkmcnt(3)\nv_permlane16_swap_b32 v34, v36\nv_permlane16_swap_b32 v35, v37\n" \
"v_and_or_b32 v34, v34, -2, s32\nv_mfma_f32_16x16x32_bf16 v[20:23], a[168:171], v[60:63], v[20:23]\nbuffer_store_dwordx4 v[34:37], v16, s[8:11], s29 offen offset:3072 sc1\ns_bfe_u32 s32, s20, 0x10003\n" \
"v_cvt_pk_bf16_f32 v24, v24, v25\nv_cvt_pk_bf16_f32 v25, v26, v27\nv_mfma_f32_16x16x32_bf16 v[44:47], a[76:79], v[70:73], v[44:47]\ns_xor_b32 s29, s42, s43\n" \
"v_cvt_pk_bf16_f32 v26, v52, v53\nv_cvt_pk_bf16_f32 v27, v54, v55\nv_mfma_f32_16x16x32_bf16 v[20:23], a[172:175], v[70:73], v[20:23]\nds_read_b128 v[34:37], v16 offset:63488\n" \
"v_mfma_f32_16x16x32_bf16 v[44:47], a[80:83], v[78:81], v[44:47]\ns_waitcnt vmcnt(4) lgkmcnt(1)\nv_cmp_eq_u32 s[42:43], -1, v33\nv_mfma_f32_16x16x32_bf16 v[20:23], a[176:179], v[78:81], v[20:23]\n" \
"ds_read_b128 v[52:55], v16 offset:64512\nv_mfma_f32_16x16x32_bf16 v[44:47], a[84:87], v[48:51], v[44:47]\ns_cmp_eq_u64 s[42:43], 0\ns_cselect_b32 s14, s10, 0\n" \
"s_cmp_lt_i32 s20, 62\ns_cselect_b32 s14, s14, 0\ns_cmp_eq_u32 s14, 0\ns_cbranch_scc1 4f\n" \
"5:\nv_add_u32 v32, s28, v16\nv_mfma_f32_16x16x32_bf16 v[20:23], a[180:183], v[48:51], v[20:23]\nds_write_b128 v32, v[24:27] offset:0\n" \
"s_add_u32 m0, s22, 0\nv_mfma_f32_16x16x32_bf16 v[44:47], a[88:91], v[28:31], v[44:47]\nbuffer_load_dwordx4 v16, s[12:15], s21 offen offset:0 sc1 lds\nv_mfma_f32_16x16x32_bf16 v[20:23], a[184:187], v[28:31], v[20:23]\n" \
"ds_read_b128 v[60:63], v17 offset:10240\ns_add_u32 s20, s20, 1\nv_mfma_f32_16x16x32_bf16 v[44:47], a[92:95], v[38:41], v[44:47]\ns_waitcnt lgkmcnt(1)\n" \
"s_barrier\nds_read_b128 v[48:51], v16 offset:8192\nv_mfma_f32_16x16x32_bf16 v[20:23], a[188:191], v[38:41], v[20:23]\nds_read_b128 v[68:71], v16 offset:9216\n" \
"v_mfma_f32_16x16x32_bf16 v[44:47], a[96:99], v[34:37], v[44:47]\nds_read_b128 v[24:27], v16 offset:10240\nv_add_u32 v38, s28, v16\nv_mfma_f32_16x16x32_bf16 v[20:23], a[192:195], v[34:37], v[20:23]\n" \
"buffer_load_dwordx4 v16, s[12:15], s21 offen offset:1024 sc1 lds\nds_read_b128 v[56:59], v16 offset:11264\nds_read_b128 v[32:35], v17 offset:11264\nv_mfma_f32_16x16x32_bf16 v[44:47], a[100:103], v[52:55], v[44:47]\n" \
"s_waitcnt lgkmcnt(4)\nv_mfma_f32_16x16x32_bf16 v[20:23], a[196:199], v[52:55], v[20:23]\ns_waitcnt lgkmcnt(3)\nv_mfma_f32_16x16x32_bf16 v[28:31], v[48:51], a[200:203], 0\n" \
"s_waitcnt lgkmcnt(2)\nv_cvt_pk_bf16_f32 v44, v44, v45\nv_cvt_pk_bf16_f32 v45, v46, v47\nv_mfma_f32_16x16x32_bf16 v[28:31], v[68:71], a[204:207], v[28:31]\n" \
"s_waitcnt lgkmcnt(0)\nv_cvt_pk_bf16_f32 v46, v20, v21\nv_cvt_pk_bf16_f32 v47, v22, v23\nv_mfma_f32_16x16x32_bf16 v[28:31], v[24:27], v[60:63], v[28:31]\n" \
"buffer_load_dwordx4 v16, s[12:15], s21 offen offset:2048 sc1 lds\nv_mfma_f32_16x16x32_bf16 v[28:31], v[56:59], v[32:35], v[28:31]\nds_write_b128 v38, v[44:47] offset:32768\nv_mfma_f32_16x16x32_bf16 v[40:43], v[48:51], a[208:211], 0\n" \
"v_mfma_f32_16x16x32_bf16 v[40:43], v[68:71], a[212:215], v[40:43]\nv_cvt_pk_bf16_f32 v28, v28, v29\nv_cvt_pk_bf16_f32 v29, v30, v31\nv_mfma_f32_16x16x32_bf16 v[40:43], v[24:27], a[216:219], v[40:43]\n" \
"s_add_u32 s43, s21, 0x4000\nds_read_b128 v[60:63], v17 offset:18432\nv_mfma_f32_16x16x32_bf16 v[40:43], v[56:59], a[220:223], v[40:43]\ns_waitcnt lgkmcnt(1)\n" \
"s_barrier\nv_mfma_f32_16x16x32_bf16 v[20:23], v[48:51], a[224:227], 0\nbuffer_load_dwordx4 v16, s[12:15], s21 offen offset:3072 sc1 lds\nv_mfma_f32_16x16x32_bf16 v[20:23], v[68:71], a[228:231], v[20:23]\n" \
"s_add_u32 m0, s22, 0x4000\nds_read_b128 v[64:67], v17 offset:19456\nv_mfma_f32_16x16x32_bf16 v[20:23], v[24:27], a[232:235], v[20:23]\nbuffer_load_dwordx4 v16, s[12:15], s43 offen offset:0 sc1 lds\n" \
"v_mfma_f32_16x16x32_bf16 v[20:23], v[56:59], a[236:239], v[20:23]\nds_read_b128 v[32:35], v16 offset:40960\nv_cvt_pk_bf16_f32 v40, v40, v41\nv_mfma_f32_16x16x32_bf16 v[44:47], v[48:51], v[144:147], 0\n" \
"ds_read_b128 v[36:39], v16 offset:41984\nv_cvt_pk_bf16_f32 v41, v42, v43\nv_mfma_f32_16x16x32_bf16 v[44:47], v[68:71], v[148:151], v[44:47]\nds_read_b128 v[52:55], v16 offset:43008\n" \
"v_cvt_pk_bf16_f32 v20, v20, v21\nv_mfma_f32_16x16x32_bf16 v[44:47], v[24:27], v[152:155], v[44:47]\nds_read_b128 v[72:75], v17 offset:10240\nv_cvt_pk_bf16_f32 v21, v22, v23\n" \
"v_mfma_f32_16x16x32_bf16 v[44:47], v[56:59], v[156:159], v[44:47]\nbuffer_load_dwordx4 v16, s[12:15], s43 offen offset:1024 sc1 lds\nv_mfma_f32_16x16x32_bf16 v[84:87], v[48:51], v[200:203], 0\ns_waitcnt lgkmcnt(4)\n" \
"ds_read_b128 v[76:79], v16 offset:44032\nv_mfma_f32_16x16x32_bf16 v[84:87], v[68:71], v[204:207], v[84:87]\nds_read_b128 v[80:83], v17 offset:11264\nv_cvt_pk_bf16_f32 v44, v44, v45\n" \
"v_mfma_f32_16x16x32_bf16 v[84:87], v[24:27], v[60:63], v[84:87]\ns_waitcnt lgkmcnt(4)\nv_cvt_pk_bf16_f32 v45, v46, v47\nv_mfma_f32_16x16x32_bf16 v[84:87], v[56:59], v[64:67], v[84:87]\n" \
"v_mfma_f32_16x16x32_bf16 v[60:63], v[32:35], a[200:203], 0\ns_waitcnt lgkmcnt(2)\nbuffer_load_dwordx4 v16, s[12:15], s43 offen offset:2048 sc1 lds\nv_mfma_f32_16x16x32_bf16 v[60:63], v[36:39], a[204:207], v[60:63]\n" \
"v_cvt_pk_bf16_f32 v84, v84, v85\nv_cvt_pk_bf16_f32 v85, v86, v87\nv_mfma_f32_16x16x32_bf16 v[60:63], v[52:55], v[72:75], v[60:63]\ns_waitcnt lgkmcnt(0)\n" \
"v_mfma_f32_16x16x32_bf16 v[60:63], v[76:79], v[80:83], v[60:63]\nv_mfma_f32_16x16x32_bf16 v[64:67], v[32:35], a[208:211], 0\nbuffer_load_dwordx4 v16, s[12:15], s43 offen offset:3072 sc1 lds\nv_mfma_f32_16x16x32_bf16 v[64:67], v[36:39], a[212:215], v[64:67]\n" \
"v_cvt_pk_bf16_f32 v30, v60, v61\nv_cvt_pk_bf16_f32 v31, v62, v63\nv_mfma_f32_16x16x32_bf16 v[64:67], v[52:55], a[216:219], v[64:67]\nds_read_b128 v[60:63], v17 offset:18432\n" \
"v_mfma_f32_16x16x32_bf16 v[64:67], v[76:79], a[220:223], v[64:67]\nds_read_b128 v[80:83], v17 offset:19456\nv_mfma_f32_16x16x32_bf16 v[72:75], v[32:35], a[224:227], 0\nv_mfma_f32_16x16x32_bf16 v[72:75], v[36:39], a[228:231], v[72:75]\n" \
"buffer_store_dwordx4 v[28:31], v16, s[8:11], s16 offen offset:0 sc1\nv_cvt_pk_bf16_f32 v42, v64, v65\nv_cvt_pk_bf16_f32 v43, v66, v67\nv_mfma_f32_16x16x32_bf16 v[72:75], v[52:55], a[232:235], v[72:75]\n" \
"v_mfma_f32_16x16x32_bf16 v[72:75], v[76:79], a[236:239], v[72:75]\nv_mfma_f32_16x16x32_bf16 v[28:31], v[32:35], v[144:147], 0\nv_mfma_f32_16x16x32_bf16 v[28:31], v[36:39], v[148:151], v[28:31]\nv_cvt_pk_bf16_f32 v22, v72, v73\n" \
"v_cvt_pk_bf16_f32 v23, v74, v75\nv_mfma_f32_16x16x32_bf16 v[28:31], v[52:55], v[152:155], v[28:31]\nv_mfma_f32_16x16x32_bf16 v[28:31], v[76:79], v[156:159], v[28:31]\nv_mfma_f32_16x16x32_bf16 v[64:67], v[32:35], v[200:203], 0\n" \
"s_waitcnt lgkmcnt(0)\nbuffer_store_dwordx4 v[40:43], v16, s[8:11], s16 offen offset:1024 sc1\nv_mfma_f32_16x16x32_bf16 v[64:67], v[36:39], v[204:207], v[64:67]\nv_cvt_pk_bf16_f32 v46, v28, v29\n" \
"v_cvt_pk_bf16_f32 v47, v30, v31\nv_mfma_f32_16x16x32_bf16 v[64:67], v[52:55], v[60:63], v[64:67]\nv_mfma_f32_16x16x32_bf16 v[64:67], v[76:79], v[80:83], v[64:67]\nv_mfma_f32_16x16x32_bf16 v[28:31], v[48:51], a[240:243], 0\n" \
"v_mfma_f32_16x16x32_bf16 v[40:43], v[32:35], a[240:243], 0\nv_cvt_pk_bf16_f32 v86, v64, v65\nv_cvt_pk_bf16_f32 v87, v66, v67\nv_mfma_f32_16x16x32_bf16 v[28:31], v[68:71], a[244:247], v[28:31]\n" \
"v_mfma_f32_16x16x32_bf16 v[40:43], v[36:39], a[244:247], v[40:43]\nv_mfma_f32_16x16x32_bf16 v[28:31], v[24:27], a[248:251], v[28:31]\nbuffer_store_dwordx4 v[20:23], v16, s[8:11], s16 offen offset:2048 sc1\nds_read_b128 v[60:63], v17 offset:12288\n" \
"v_mfma_f32_16x16x32_bf16 v[40:43], v[52:55], a[248:251], v[40:43]\nv_mfma_f32_16x16x32_bf16 v[28:31], v[56:59], a[252:255], v[28:31]\nds_read_b128 v[20:23], v17 offset:13312\nv_mfma_f32_16x16x32_bf16 v[40:43], v[76:79], a[252:255], v[40:43]\n" \
"v_mfma_f32_16x16x32_bf16 v[80:83], v[48:51], v[128:131], 0\nds_read_b128 v[64:67], v17 offset:14336\nv_cvt_pk_bf16_f32 v28, v28, v29\nv_mfma_f32_16x16x32_bf16 v[72:75], v[32:35], v[128:131], 0\n" \
"v_cvt_pk_bf16_f32 v29, v30, v31\nv_cvt_pk_bf16_f32 v30, v40, v41\nv_mfma_f32_16x16x32_bf16 v[80:83], v[68:71], v[132:135], v[80:83]\ns_waitcnt vmcnt(7)\n" \
"s_barrier\nv_mfma_f32_16x16x32_bf16 v[72:75], v[36:39], v[132:135], v[72:75]\nbuffer_store_dwordx4 v[44:47], v16, s[8:11], s17 offen offset:3072 sc1\nds_read_b128 v[88:91], v17 offset:15360\n" \
"v_cvt_pk_bf16_f32 v31, v42, v43\nv_mfma_f32_16x16x32_bf16 v[80:83], v[24:27], v[136:139], v[80:83]\nv_mfma_f32_16x16x32_bf16 v[72:75], v[52:55], v[136:139], v[72:75]\nv_mfma_f32_16x16x32_bf16 v[80:83], v[56:59], v[140:143], v[80:83]\n" \
"s_waitcnt lgkmcnt(1)\nv_mfma_f32_16x16x32_bf16 v[72:75], v[76:79], v[140:143], v[72:75]\nv_mfma_f32_16x16x32_bf16 v[44:47], v[48:51], v[60:63], 0\nv_cvt_pk_bf16_f32 v80, v80, v81\n" \
"v_cvt_pk_bf16_f32 v81, v82, v83\nv_mfma_f32_16x16x32_bf16 v[40:43], v[32:35], v[60:63], 0\nv_cvt_pk_bf16_f32 v82, v72, v73\nv_cvt_pk_bf16_f32 v83, v74, v75\n" \
"v_mfma_f32_16x16x32_bf16 v[44:47], v[68:71], v[20:23], v[44:47]\nv_mfma_f32_16x16x32_bf16 v[40:43], v[36:39], v[20:23], v[40:43]\nbuffer_store_dwordx4 v[84:87], v16, s[8:11], s18 offen offset:3072 sc1\ns_add_u32 m0, s22, 0x2000\n" \
"s_add_u32 s43, s21, 0x2000\ns_add_u32 s21, s21, 0x10000\nv_mfma_f32_16x16x32_bf16 v[44:47], v[24:27], v[64:67], v[44:47]\ns_waitcnt lgkmcnt(0)\n" \
"ds_read_b128 v[20:23], v17 offset:16384\nv_mfma_f32_16x16x32_bf16 v[40:43], v[52:55], v[64:67], v[40:43]\nv_mfma_f32_16x16x32_bf16 v[44:47], v[56:59], v[88:91], v[44:47]\nds_read_b128 v[60:63], v17 offset:17408\n" \
"v_mfma_f32_16x16x32_bf16 v[40:43], v[76:79], v[88:91], v[40:43]\nbuffer_load_dwordx4 v16, s[12:15], s43 offen offset:0 sc1 lds\nv_mfma_f32_16x16x32_bf16 v[64:67], v[48:51], v[112:115], 0\nv_cvt_pk_bf16_f32 v44, v44, v45\n" \
"v_cvt_pk_bf16_f32 v45, v46, v47\nv_mfma_f32_16x16x32_bf16 v[72:75], v[32:35], v[112:115], 0\nv_cvt_pk_bf16_f32 v46, v40, v41\nv_cvt_pk_bf16_f32 v47, v42, v43\n" \
"v_mfma_f32_16x16x32_bf16 v[64:67], v[68:71], v[116:119], v[64:67]\nbuffer_store_dwordx4 v[28:31], v16, s[8:11], s16 offen offset:3072 sc1\nv_mfma_f32_16x16x32_bf16 v[72:75], v[36:39], v[116:119], v[72:75]\nv_mfma_f32_16x16x32_bf16 v[64:67], v[24:27], v[120:123], v[64:67]\n" \
"v_mfma_f32_16x16x32_bf16 v[72:75], v[52:55], v[120:123], v[72:75]\nv_mfma_f32_16x16x32_bf16 v[64:67], v[56:59], v[124:127], v[64:67]\ns_waitcnt lgkmcnt(0)\nv_mfma_f32_16x16x32_bf16 v[72:75], v[76:79], v[124:127], v[72:75]\n" \
"v_mfma_f32_16x16x32_bf16 v[28:31], v[48:51], v[20:23], 0\nv_cvt_pk_bf16_f32 v64, v64, v65\nv_cvt_pk_bf16_f32 v65, v66, v67\nv_mfma_f32_16x16x32_bf16 v[40:43], v[32:35], v[20:23], 0\n" \
"buffer_store_dwordx4 v[80:83], v16, s[8:11], s17 offen offset:2048 sc1\nbuffer_load_dwordx4 v16, s[12:15], s43 offen offset:1024 sc1 lds\nv_cvt_pk_bf16_f32 v66, v72, v73\nv_mfma_f32_16x16x32_bf16 v[28:31], v[68:71], v[60:63], v[28:31]\n" \
"v_cvt_pk_bf16_f32 v67, v74, v75\nv_mfma_f32_16x16x32_bf16 v[40:43], v[36:39], v[60:63], v[40:43]\nv_mfma_f32_16x16x32_bf16 v[28:31], v[24:27], v[176:179], v[28:31]\nv_mfma_f32_16x16x32_bf16 v[40:43], v[52:55], v[176:179], v[40:43]\n" \
"v_mfma_f32_16x16x32_bf16 v[28:31], v[56:59], v[180:183], v[28:31]\nv_mfma_f32_16x16x32_bf16 v[40:43], v[76:79], v[180:183], v[40:43]\nv_mfma_f32_16x16x32_bf16 v[20:23], v[48:51], v[184:187], 0\nv_cvt_pk_bf16_f32 v28, v28, v29\n" \
"v_cvt_pk_bf16_f32 v29, v30, v31\nv_mfma_f32_16x16x32_bf16 v[60:63], v[32:35], v[184:187], 0\nbuffer_store_dwordx4 v[44:47], v16, s[8:11], s17 offen offset:1024 sc1\nv_cvt_pk_bf16_f32 v30, v40, v41\n" \
"v_cvt_pk_bf16_f32 v31, v42, v43\nv_mfma_f32_16x16x32_bf16 v[20:23], v[68:71], v[188:191], v[20:23]\nv_mfma_f32_16x16x32_bf16 v[60:63], v[36:39], v[188:191], v[60:63]\nbuffer_load_dwordx4 v16, s[12:15], s43 offen offset:2048 sc1 lds\n" \
"v_mfma_f32_16x16x32_bf16 v[20:23], v[24:27], v[192:195], v[20:23]\nv_mfma_f32_16x16x32_bf16 v[60:63], v[52:55], v[192:195], v[60:63]\nv_mfma_f32_16x16x32_bf16 v[20:23], v[56:59], v[196:199], v[20:23]\nv_mfma_f32_16x16x32_bf16 v[60:63], v[76:79], v[196:199], v[60:63]\n" \
"v_mfma_f32_16x16x32_bf16 v[40:43], v[48:51], v[160:163], 0\nbuffer_store_dwordx4 v[64:67], v16, s[8:11], s17 offen offset:0 sc1\nv_cvt_pk_bf16_f32 v20, v20, v21\nv_cvt_pk_bf16_f32 v21, v22, v23\n" \
"v_mfma_f32_16x16x32_bf16 v[44:47], v[32:35], v[160:163], 0\nv_cvt_pk_bf16_f32 v22, v60, v61\nv_cvt_pk_bf16_f32 v23, v62, v63\nv_mfma_f32_16x16x32_bf16 v[40:43], v[68:71], v[164:167], v[40:43]\n" \
"v_mfma_f32_16x16x32_bf16 v[44:47], v[36:39], v[164:167], v[44:47]\nv_mfma_f32_16x16x32_bf16 v[40:43], v[24:27], v[168:171], v[40:43]\nds_read_b128 v[60:63], v17 offset:20480\nv_mfma_f32_16x16x32_bf16 v[44:47], v[52:55], v[168:171], v[44:47]\n" \
"buffer_load_dwordx4 v16, s[12:15], s43 offen offset:3072 sc1 lds\nv_mfma_f32_16x16x32_bf16 v[40:43], v[56:59], v[172:175], v[40:43]\nds_read_b128 v[64:67], v17 offset:21504\nv_mfma_f32_16x16x32_bf16 v[44:47], v[76:79], v[172:175], v[44:47]\n" \
"v_mfma_f32_16x16x32_bf16 v[72:75], v[48:51], v[232:235], 0\nbuffer_store_dwordx4 v[28:31], v16, s[8:11], s18 offen offset:1024 sc1\nv_cvt_pk_bf16_f32 v40, v40, v41\nv_cvt_pk_bf16_f32 v41, v42, v43\n" \
"v_mfma_f32_16x16x32_bf16 v[28:31], v[32:35], v[232:235], 0\nv_cvt_pk_bf16_f32 v42, v44, v45\nv_cvt_pk_bf16_f32 v43, v46, v47\nv_mfma_f32_16x16x32_bf16 v[72:75], v[68:71], v[236:239], v[72:75]\n" \
"v_mfma_f32_16x16x32_bf16 v[28:31], v[36:39], v[236:239], v[28:31]\nv_mfma_f32_16x16x32_bf16 v[72:75], v[24:27], v[240:243], v[72:75]\nv_mfma_f32_16x16x32_bf16 v[28:31], v[52:55], v[240:243], v[28:31]\nv_mfma_f32_16x16x32_bf16 v[72:75], v[56:59], v[244:247], v[72:75]\n" \
"s_waitcnt lgkmcnt(0)\nv_mfma_f32_16x16x32_bf16 v[28:31], v[76:79], v[244:247], v[28:31]\nbuffer_store_dwordx4 v[20:23], v16, s[8:11], s18 offen offset:2048 sc1\nv_mfma_f32_16x16x32_bf16 v[44:47], v[48:51], v[60:63], 0\n" \
"v_cvt_pk_bf16_f32 v72, v72, v73\nv_cvt_pk_bf16_f32 v73, v74, v75\nv_mfma_f32_16x16x32_bf16 v[20:23], v[32:35], v[60:63], 0\nv_cvt_pk_bf16_f32 v74, v28, v29\n" \
"v_cvt_pk_bf16_f32 v75, v30, v31\nv_mfma_f32_16x16x32_bf16 v[44:47], v[68:71], v[64:67], v[44:47]\nv_mfma_f32_16x16x32_bf16 v[20:23], v[36:39], v[64:67], v[20:23]\nv_mfma_f32_16x16x32_bf16 v[44:47], v[24:27], v[224:227], v[44:47]\n" \
"ds_read_b128 v[60:63], v17 offset:22528\nv_mfma_f32_16x16x32_bf16 v[20:23], v[52:55], v[224:227], v[20:23]\nv_mfma_f32_16x16x32_bf16 v[44:47], v[56:59], v[228:231], v[44:47]\nbuffer_store_dwordx4 v[40:43], v16, s[8:11], s18 offen offset:0 sc1\n" \
"ds_read_b128 v[28:31], v17 offset:23552\nv_mfma_f32_16x16x32_bf16 v[20:23], v[76:79], v[228:231], v[20:23]\nv_mfma_f32_16x16x32_bf16 v[64:67], v[48:51], v[208:211], 0\nv_cvt_pk_bf16_f32 v44, v44, v45\n" \
"v_cvt_pk_bf16_f32 v45, v46, v47\nv_mfma_f32_16x16x32_bf16 v[40:43], v[32:35], v[208:211], 0\nv_cvt_pk_bf16_f32 v46, v20, v21\nv_cvt_pk_bf16_f32 v47, v22, v23\n" \
"v_mfma_f32_16x16x32_bf16 v[64:67], v[68:71], v[212:215], v[64:67]\nv_mfma_f32_16x16x32_bf16 v[40:43], v[36:39], v[212:215], v[40:43]\nds_read_b128 v[20:23], v16 offset:0\nv_mfma_f32_16x16x32_bf16 v[64:67], v[24:27], v[216:219], v[64:67]\n" \
"ds_read_b128 v[80:83], v16 offset:32768\nv_mfma_f32_16x16x32_bf16 v[40:43], v[52:55], v[216:219], v[40:43]\nv_mfma_f32_16x16x32_bf16 v[64:67], v[56:59], v[220:223], v[64:67]\ns_waitcnt lgkmcnt(2)\n" \
"buffer_store_dwordx4 v[72:75], v16, s[8:11], s19 offen offset:2048 sc1\nv_mfma_f32_16x16x32_bf16 v[40:43], v[76:79], v[220:223], v[40:43]\nds_read_b128 v[84:87], v16 offset:1024\nv_mfma_f32_16x16x32_bf16 v[72:75], v[48:51], v[60:63], 0\n" \
"ds_read_b128 v[88:91], v16 offset:33792\nv_cvt_pk_bf16_f32 v64, v64, v65\nv_mfma_f32_16x16x32_bf16 v[48:51], v[32:35], v[60:63], 0\nv_cvt_pk_bf16_f32 v65, v66, v67\n" \
"v_cvt_pk_bf16_f32 v66, v40, v41\nv_mfma_f32_16x16x32_bf16 v[72:75], v[68:71], v[28:31], v[72:75]\nv_cvt_pk_bf16_f32 v67, v42, v43\nv_mfma_f32_16x16x32_bf16 v[48:51], v[36:39], v[28:31], v[48:51]\n" \
"ds_read_b128 v[32:35], v16 offset:2048\nv_mfma_f32_16x16x32_bf16 v[72:75], v[24:27], v[248:251], v[72:75]\nds_read_b128 v[28:31], v16 offset:34816\nv_mfma_f32_16x16x32_bf16 v[48:51], v[52:55], v[248:251], v[48:51]\n" \
"buffer_store_dwordx4 v[44:47], v16, s[8:11], s19 offen offset:1024 sc1\nv_mfma_f32_16x16x32_bf16 v[72:75], v[56:59], v[252:255], v[72:75]\ns_waitcnt lgkmcnt(2)\nv_mfma_f32_16x16x32_bf16 v[48:51], v[76:79], v[252:255], v[48:51]\n" \
"ds_read_b128 v[36:39], v16 offset:3072\nv_mfma_f32_16x16x32_bf16 v[24:27], v[0:3], v[20:23], 0\nds_read_b128 v[40:43], v16 offset:35840\nv_cvt_pk_bf16_f32 v72, v72, v73\n" \
"v_mfma_f32_16x16x32_bf16 v[44:47], v[0:3], v[80:83], 0\nv_cvt_pk_bf16_f32 v73, v74, v75\nv_cvt_pk_bf16_f32 v74, v48, v49\nv_mfma_f32_16x16x32_bf16 v[52:55], v[8:11], v[20:23], 0\n" \
"v_cvt_pk_bf16_f32 v75, v50, v51\nv_mfma_f32_16x16x32_bf16 v[20:23], v[8:11], v[80:83], 0\nds_read_b128 v[48:51], v16 offset:4096\nv_mfma_f32_16x16x32_bf16 v[24:27], v[4:7], v[84:87], v[24:27]\n" \
"ds_read_b128 v[56:59], v16 offset:36864\nv_mfma_f32_16x16x32_bf16 v[44:47], v[4:7], v[88:91], v[44:47]\nbuffer_store_dwordx4 v[64:67], v16, s[8:11], s19 offen offset:0 sc1\nv_mfma_f32_16x16x32_bf16 v[52:55], v[12:15], v[84:87], v[52:55]\n" \
"s_waitcnt lgkmcnt(2)\nv_mfma_f32_16x16x32_bf16 v[20:23], v[12:15], v[88:91], v[20:23]\nds_read_b128 v[60:63], v16 offset:5120\nv_mfma_f32_16x16x32_bf16 v[24:27], a[0:3], v[32:35], v[24:27]\n" \
"ds_read_b128 v[64:67], v16 offset:37888\nv_mfma_f32_16x16x32_bf16 v[44:47], a[0:3], v[28:31], v[44:47]\nv_mfma_f32_16x16x32_bf16 v[52:55], a[104:107], v[32:35], v[52:55]\nv_mfma_f32_16x16x32_bf16 v[20:23], a[104:107], v[28:31], v[20:23]\n" \
"ds_read_b128 v[32:35], v16 offset:6144\nv_mfma_f32_16x16x32_bf16 v[24:27], a[4:7], v[36:39], v[24:27]\nbuffer_store_dwordx4 v[72:75], v16, s[8:11], s19 offen offset:3072 sc1\nds_read_b128 v[76:79], v16 offset:38912\n" \
"v_mfma_f32_16x16x32_bf16 v[44:47], a[4:7], v[40:43], v[44:47]\nds_read_b128 v[68:71], v16 offset:7168\nv_mfma_f32_16x16x32_bf16 v[52:55], a[108:111], v[36:39], v[52:55]\nds_read_b128 v[28:31], v16 offset:39936\n" \
"v_mfma_f32_16x16x32_bf16 v[20:23], a[108:111], v[40:43], v[20:23]\ns_branch 1b\n2:\ns_cmp_lt_i32 s20, 1\n" \
"s_cbranch_scc1 3b\ns_add_u32 s30, s30, 1\ns_cmp_lt_u32 s30, 0x40000\ns_cbranch_scc0 9f\n" \
"v_cmp_eq_u32 vcc, -1, v38\ns_cbranch_vccz 6f\nbuffer_load_dwordx4 v16, s[8:11], s43 offen offset:0 sc1 lds\n6:\n" \
"v_cmp_eq_u32 vcc, -1, v60\ns_cbranch_vccz 6f\nbuffer_load_dwordx4 v16, s[8:11], s43 offen offset:1024 sc1 lds\n6:\n" \
"v_cmp_eq_u32 vcc, -1, v80\ns_cbranch_vccz 6f\nbuffer_load_dwordx4 v16, s[8:11], s43 offen offset:2048 sc1 lds\n6:\n" \
"v_cmp_eq_u32 vcc, -1, v96\ns_cbranch_vccz 6f\nbuffer_load_dwordx4 v16, s[8:11], s43 offen offset:3072 sc1 lds\n6:\n" \
"s_waitcnt vmcnt(0)\nds_read_b128 v[38:41], v36 offset:0\nds_read_b128 v[60:63], v36 offset:1024\nds_read_b128 v[80:83], v36 offset:2048\n" \
"ds_read_b128 v[96:99], v36 offset:3072\ns_waitcnt lgkmcnt(0)\nv_max3_u32 v42, v38, v60, v80\nv_max_u32 v42, v42, v96\n" \
"v_cmp_eq_u32 vcc, -1, v42\ns_cbranch_vccnz 2b\ns_branch 3b\n4:\n" \
"s_cmp_ge_i32 s20, 64\ns_cbranch_scc1 10f\ns_cmp_ge_i32 s20, 62\ns_cbranch_scc1 5b\n" \
"v_bfe_u32 v32, v16, 4, 3\nv_lshlrev_b32 v32, 2, v32\ns_lshl_b32 s43, s20, 5\ns_add_u32 s43, s43, 64\n" \
"s_add_u32 s43, s43, s26\n7:\nv_cmp_eq_u32 vcc, -1, v33\ns_cbranch_vccz 8f\n" \
"s_add_u32 s30, s30, 1\ns_cmp_lt_u32 s30, 0x40000\ns_cbranch_scc0 9f\ns_sleep 8\n" \
"buffer_load_dword v33, v32, s[8:11], s43 offen sc1\ns_waitcnt vmcnt(0)\ns_branch 7b\n8:\n" \
"s_mov_b32 s14, s10\ns_branch 5b\n10:\ns_waitcnt vmcnt(0) lgkmcnt(0)\n" \
"s_cmp_lt_i32 s6, 0\ns_cbranch_scc1 9f\ns_mov_b64 s[0:1], s[4:5]\ns_mov_b32 s2, s6\n" \
"v_lshrrev_b32 v0, 4, v16\nv_add_u32 v0, s7, v0\ns_barrier\ns_getpc_b64 s[8:9]\n" \
"11:\ns_add_u32 s8, s8, _Z5mega64Args-11b\ns_addc_u32 s9, s9, -1\ns_setpc_b64 s[8:9]\n" \
"9:\ns_waitcnt vmcnt(0)\ns_endpgm\n"
#define D_LOOP \
"s_branch 4f\n.p2align 6\n1:\ns_waitcnt lgkmcnt(0)\n" \
"v_mfma_f32_16x16x32_bf16 v[44:47], a[8:11], v[8:11], v[44:47]\nds_read_b128 v[16:19], v4 offset:3072\ns_mov_b32 m0, s24\nv_mfma_f32_16x16x32_bf16 v[64:67], a[8:11], v[12:15], v[64:67]\n" \
"ds_read_b128 v[32:35], v4 offset:35840\nbuffer_load_dword v1, s[16:19], 0 offen offset:0 sc1 lds\nv_mfma_f32_16x16x32_bf16 v[48:51], a[104:107], v[8:11], v[48:51]\ns_add_u32 m0, s23, 0\n" \
"s_and_b32 s98, s20, 1\ns_lshl_b32 s98, s98, 3\nv_mfma_f32_16x16x32_bf16 v[20:23], a[104:107], v[12:15], v[20:23]\nv_mfma_f32_16x16x32_bf16 v[56:59], a[200:203], v[8:11], v[56:59]\n" \
"v_mfma_f32_16x16x32_bf16 v[24:27], a[200:203], v[12:15], v[24:27]\nv_mfma_f32_16x16x32_bf16 v[60:63], v[152:155], v[8:11], v[60:63]\ns_waitcnt lgkmcnt(1)\nv_mfma_f32_16x16x32_bf16 v[28:31], v[152:155], v[12:15], v[28:31]\n" \
"s_waitcnt lgkmcnt(0)\nds_read_b128 v[8:11], v4 offset:4096\nv_mfma_f32_16x16x32_bf16 v[44:47], a[12:15], v[16:19], v[44:47]\nds_read_b128 v[12:15], v4 offset:36864\n" \
"v_mfma_f32_16x16x32_bf16 v[64:67], a[12:15], v[32:35], v[64:67]\nv_mfma_f32_16x16x32_bf16 v[48:51], a[108:111], v[16:19], v[48:51]\nv_mfma_f32_16x16x32_bf16 v[20:23], a[108:111], v[32:35], v[20:23]\nv_mfma_f32_16x16x32_bf16 v[56:59], a[204:207], v[16:19], v[56:59]\n" \
"v_mfma_f32_16x16x32_bf16 v[24:27], a[204:207], v[32:35], v[24:27]\nv_mfma_f32_16x16x32_bf16 v[60:63], v[156:159], v[16:19], v[60:63]\ns_waitcnt lgkmcnt(0)\nv_mfma_f32_16x16x32_bf16 v[28:31], v[156:159], v[32:35], v[28:31]\n" \
"ds_read_b128 v[16:19], v4 offset:5120\nv_mfma_f32_16x16x32_bf16 v[44:47], a[16:19], v[8:11], v[44:47]\nds_read_b128 v[32:35], v4 offset:37888\nv_mfma_f32_16x16x32_bf16 v[64:67], a[16:19], v[12:15], v[64:67]\n" \
"v_mfma_f32_16x16x32_bf16 v[48:51], a[112:115], v[8:11], v[48:51]\nv_mfma_f32_16x16x32_bf16 v[20:23], a[112:115], v[12:15], v[20:23]\nv_mfma_f32_16x16x32_bf16 v[56:59], a[208:211], v[8:11], v[56:59]\nv_mfma_f32_16x16x32_bf16 v[24:27], a[208:211], v[12:15], v[24:27]\n" \
"v_mfma_f32_16x16x32_bf16 v[60:63], v[160:163], v[8:11], v[60:63]\ns_waitcnt lgkmcnt(0)\nv_mfma_f32_16x16x32_bf16 v[28:31], v[160:163], v[12:15], v[28:31]\nds_read_b128 v[8:11], v4 offset:6144\n" \
"s_add_u32 s99, s20, 1\nv_mfma_f32_16x16x32_bf16 v[44:47], a[20:23], v[16:19], v[44:47]\nds_read_b128 v[12:15], v4 offset:38912\ns_lshr_b32 s97, s99, 1\n" \
"v_mfma_f32_16x16x32_bf16 v[64:67], a[20:23], v[32:35], v[64:67]\ns_and_b32 s99, s99, 1\ns_lshl_b32 s99, s99, 3\nv_readlane_b32 s97, v3, s97\n" \
"v_mfma_f32_16x16x32_bf16 v[48:51], a[116:119], v[16:19], v[48:51]\nv_mfma_f32_16x16x32_bf16 v[20:23], a[116:119], v[32:35], v[20:23]\ns_lshr_b32 s97, s97, s99\ns_and_b32 s97, s97, 0x7f\n" \
"s_mul_i32 s97, s97, 0x38000\nv_mfma_f32_16x16x32_bf16 v[56:59], a[212:215], v[16:19], v[56:59]\ns_add_u32 s97, s97, s21\nv_mfma_f32_16x16x32_bf16 v[24:27], a[212:215], v[32:35], v[24:27]\n" \
"v_mfma_f32_16x16x32_bf16 v[60:63], v[164:167], v[16:19], v[60:63]\ns_waitcnt lgkmcnt(0)\nv_mfma_f32_16x16x32_bf16 v[28:31], v[164:167], v[32:35], v[28:31]\nds_read_b128 v[16:19], v4 offset:7168\n" \
"v_mfma_f32_16x16x32_bf16 v[44:47], a[24:27], v[8:11], v[44:47]\nds_read_b128 v[32:35], v4 offset:39936\nv_mfma_f32_16x16x32_bf16 v[64:67], a[24:27], v[12:15], v[64:67]\nv_mfma_f32_16x16x32_bf16 v[48:51], a[120:123], v[8:11], v[48:51]\n" \
"v_mfma_f32_16x16x32_bf16 v[20:23], a[120:123], v[12:15], v[20:23]\nv_mfma_f32_16x16x32_bf16 v[56:59], a[216:219], v[8:11], v[56:59]\nv_mfma_f32_16x16x32_bf16 v[24:27], a[216:219], v[12:15], v[24:27]\nv_mfma_f32_16x16x32_bf16 v[60:63], v[168:171], v[8:11], v[60:63]\n" \
"s_waitcnt lgkmcnt(0)\nv_mfma_f32_16x16x32_bf16 v[28:31], v[168:171], v[12:15], v[28:31]\nds_read_b128 v[8:11], v4 offset:8192\nv_mov_b32 v111, s20\n" \
"v_mfma_f32_16x16x32_bf16 v[44:47], a[28:31], v[16:19], v[44:47]\nds_read_b128 v[12:15], v4 offset:40960\ns_add_u32 s99, s20, 2\nv_mfma_f32_16x16x32_bf16 v[64:67], a[28:31], v[32:35], v[64:67]\n" \
"s_cmp_le_u32 s99, s28\ns_cselect_b32 s99, s99, 0x1000\nv_mfma_f32_16x16x32_bf16 v[48:51], a[124:127], v[16:19], v[48:51]\nv_mfma_f32_16x16x32_bf16 v[20:23], a[124:127], v[32:35], v[20:23]\n" \
"v_mfma_f32_16x16x32_bf16 v[56:59], a[220:223], v[16:19], v[56:59]\ns_waitcnt vmcnt(0)\ns_barrier\nv_mfma_f32_16x16x32_bf16 v[24:27], a[220:223], v[32:35], v[24:27]\n" \
"buffer_store_dword v111, v2, s[8:11], s25 offen offset:0 sc1\nv_mfma_f32_16x16x32_bf16 v[60:63], v[172:175], v[16:19], v[60:63]\nds_read_b32 v110, v5 offset:0\ns_mov_b32 s25, 0\n" \
"v_mfma_f32_16x16x32_bf16 v[28:31], v[172:175], v[32:35], v[28:31]\ns_waitcnt lgkmcnt(1)\nds_read_b128 v[16:19], v4 offset:9216\nv_mfma_f32_16x16x32_bf16 v[44:47], a[32:35], v[8:11], v[44:47]\n" \
"ds_read_b128 v[32:35], v4 offset:41984\nv_mfma_f32_16x16x32_bf16 v[64:67], a[32:35], v[12:15], v[64:67]\nv_mfma_f32_16x16x32_bf16 v[48:51], a[128:131], v[8:11], v[48:51]\ns_waitcnt lgkmcnt(2)\n" \
"v_cmp_gt_i32 s[94:95], s99, v110\nv_mfma_f32_16x16x32_bf16 v[20:23], a[128:131], v[12:15], v[20:23]\nv_mfma_f32_16x16x32_bf16 v[56:59], a[224:227], v[8:11], v[56:59]\ns_cmp_eq_u64 s[94:95], 0\n" \
"s_cselect_b32 s14, s10, 0\nv_mfma_f32_16x16x32_bf16 v[24:27], a[224:227], v[12:15], v[24:27]\nv_mfma_f32_16x16x32_bf16 v[60:63], v[176:179], v[8:11], v[60:63]\ns_waitcnt lgkmcnt(0)\n" \
"v_mfma_f32_16x16x32_bf16 v[28:31], v[176:179], v[12:15], v[28:31]\nds_read_b128 v[8:11], v4 offset:10240\nv_mfma_f32_16x16x32_bf16 v[44:47], a[36:39], v[16:19], v[44:47]\nds_read_b128 v[12:15], v4 offset:43008\n" \
"v_mfma_f32_16x16x32_bf16 v[64:67], a[36:39], v[32:35], v[64:67]\nv_mfma_f32_16x16x32_bf16 v[48:51], a[132:135], v[16:19], v[48:51]\nv_mfma_f32_16x16x32_bf16 v[20:23], a[132:135], v[32:35], v[20:23]\nbuffer_load_dwordx4 v0, s[12:15], s97 offen offset:0 sc1 lds\n" \
"v_mfma_f32_16x16x32_bf16 v[56:59], a[228:231], v[16:19], v[56:59]\nv_mfma_f32_16x16x32_bf16 v[24:27], a[228:231], v[32:35], v[24:27]\nv_mfma_f32_16x16x32_bf16 v[60:63], v[180:183], v[16:19], v[60:63]\ns_waitcnt lgkmcnt(0)\n" \
"v_mfma_f32_16x16x32_bf16 v[28:31], v[180:183], v[32:35], v[28:31]\nbuffer_load_dwordx4 v0, s[12:15], s97 offen offset:1024 sc1 lds\nv_mfma_f32_16x16x32_bf16 v[44:47], a[40:43], v[8:11], v[44:47]\nds_read_b128 v[16:19], v4 offset:11264\n" \
"v_mfma_f32_16x16x32_bf16 v[64:67], a[40:43], v[12:15], v[64:67]\nds_read_b128 v[32:35], v4 offset:44032\nv_mfma_f32_16x16x32_bf16 v[48:51], a[136:139], v[8:11], v[48:51]\nv_mfma_f32_16x16x32_bf16 v[20:23], a[136:139], v[12:15], v[20:23]\n" \
"buffer_load_dwordx4 v0, s[12:15], s97 offen offset:2048 sc1 lds\nv_mfma_f32_16x16x32_bf16 v[56:59], a[232:235], v[8:11], v[56:59]\nv_mfma_f32_16x16x32_bf16 v[24:27], a[232:235], v[12:15], v[24:27]\nv_mfma_f32_16x16x32_bf16 v[60:63], v[184:187], v[8:11], v[60:63]\n" \
"s_waitcnt lgkmcnt(1)\nv_mfma_f32_16x16x32_bf16 v[28:31], v[184:187], v[12:15], v[28:31]\ns_waitcnt lgkmcnt(0)\nbuffer_load_dwordx4 v0, s[12:15], s97 offen offset:3072 sc1 lds\n" \
"v_mfma_f32_16x16x32_bf16 v[44:47], a[44:47], v[16:19], v[44:47]\nds_read_b128 v[8:11], v4 offset:12288\ns_add_u32 m0, s23, 0x1000\nv_mfma_f32_16x16x32_bf16 v[64:67], a[44:47], v[32:35], v[64:67]\n" \
"ds_read_b128 v[12:15], v4 offset:45056\ns_add_u32 s97, s97, 0x1000\nv_mfma_f32_16x16x32_bf16 v[48:51], a[140:143], v[16:19], v[48:51]\nv_mfma_f32_16x16x32_bf16 v[20:23], a[140:143], v[32:35], v[20:23]\n" \
"buffer_load_dwordx4 v0, s[12:15], s97 offen offset:0 sc1 lds\nv_mfma_f32_16x16x32_bf16 v[56:59], a[236:239], v[16:19], v[56:59]\nv_mfma_f32_16x16x32_bf16 v[24:27], a[236:239], v[32:35], v[24:27]\nv_mfma_f32_16x16x32_bf16 v[60:63], v[188:191], v[16:19], v[60:63]\n" \
"s_waitcnt lgkmcnt(1)\nv_mfma_f32_16x16x32_bf16 v[28:31], v[188:191], v[32:35], v[28:31]\ns_waitcnt lgkmcnt(0)\nbuffer_load_dwordx4 v0, s[12:15], s97 offen offset:1024 sc1 lds\n" \
"v_mfma_f32_16x16x32_bf16 v[44:47], a[48:51], v[8:11], v[44:47]\nds_read_b128 v[16:19], v4 offset:13312\nv_mfma_f32_16x16x32_bf16 v[64:67], a[48:51], v[12:15], v[64:67]\nds_read_b128 v[32:35], v4 offset:46080\n" \
"v_mfma_f32_16x16x32_bf16 v[48:51], a[144:147], v[8:11], v[48:51]\nv_mfma_f32_16x16x32_bf16 v[20:23], a[144:147], v[12:15], v[20:23]\nbuffer_load_dwordx4 v0, s[12:15], s97 offen offset:2048 sc1 lds\nv_mfma_f32_16x16x32_bf16 v[56:59], a[240:243], v[8:11], v[56:59]\n" \
"v_mfma_f32_16x16x32_bf16 v[24:27], a[240:243], v[12:15], v[24:27]\nv_mfma_f32_16x16x32_bf16 v[60:63], v[192:195], v[8:11], v[60:63]\ns_waitcnt lgkmcnt(1)\nv_mfma_f32_16x16x32_bf16 v[28:31], v[192:195], v[12:15], v[28:31]\n" \
"s_waitcnt lgkmcnt(0)\nbuffer_load_dwordx4 v0, s[12:15], s97 offen offset:3072 sc1 lds\nv_mfma_f32_16x16x32_bf16 v[44:47], a[52:55], v[16:19], v[44:47]\nds_read_b128 v[8:11], v4 offset:14336\n" \
"s_add_u32 m0, s23, 0x2000\nv_mfma_f32_16x16x32_bf16 v[64:67], a[52:55], v[32:35], v[64:67]\nds_read_b128 v[12:15], v4 offset:47104\ns_add_u32 s97, s97, 0x1000\n" \
"v_mfma_f32_16x16x32_bf16 v[48:51], a[148:151], v[16:19], v[48:51]\ns_xor_b32 s23, s23, 0x10000\nv_mfma_f32_16x16x32_bf16 v[20:23], a[148:151], v[32:35], v[20:23]\nbuffer_load_dwordx4 v0, s[12:15], s97 offen offset:0 sc1 lds\n" \
"v_mfma_f32_16x16x32_bf16 v[56:59], a[244:247], v[16:19], v[56:59]\nv_mfma_f32_16x16x32_bf16 v[24:27], a[244:247], v[32:35], v[24:27]\nv_mfma_f32_16x16x32_bf16 v[60:63], v[196:199], v[16:19], v[60:63]\ns_waitcnt lgkmcnt(1)\n" \
"v_mfma_f32_16x16x32_bf16 v[28:31], v[196:199], v[32:35], v[28:31]\ns_waitcnt lgkmcnt(0)\nbuffer_load_dwordx4 v0, s[12:15], s97 offen offset:1024 sc1 lds\nv_mfma_f32_16x16x32_bf16 v[44:47], a[56:59], v[8:11], v[44:47]\n" \
"ds_read_b128 v[16:19], v4 offset:15360\nv_mfma_f32_16x16x32_bf16 v[64:67], a[56:59], v[12:15], v[64:67]\nds_read_b128 v[32:35], v4 offset:48128\nv_mfma_f32_16x16x32_bf16 v[48:51], a[152:155], v[8:11], v[48:51]\n" \
"v_mfma_f32_16x16x32_bf16 v[20:23], a[152:155], v[12:15], v[20:23]\nbuffer_load_dwordx4 v0, s[12:15], s97 offen offset:2048 sc1 lds\nv_mfma_f32_16x16x32_bf16 v[56:59], a[248:251], v[8:11], v[56:59]\nv_mfma_f32_16x16x32_bf16 v[24:27], a[248:251], v[12:15], v[24:27]\n" \
"v_mfma_f32_16x16x32_bf16 v[60:63], v[200:203], v[8:11], v[60:63]\ns_waitcnt lgkmcnt(1)\nv_mfma_f32_16x16x32_bf16 v[28:31], v[200:203], v[12:15], v[28:31]\ns_waitcnt lgkmcnt(0)\n" \
"buffer_load_dwordx4 v0, s[12:15], s97 offen offset:3072 sc1 lds\nv_mfma_f32_16x16x32_bf16 v[44:47], a[60:63], v[16:19], v[44:47]\nds_read_b128 v[8:11], v4 offset:16384\nv_mfma_f32_16x16x32_bf16 v[64:67], a[60:63], v[32:35], v[64:67]\n" \
"ds_read_b128 v[12:15], v4 offset:49152\nv_mfma_f32_16x16x32_bf16 v[48:51], a[156:159], v[16:19], v[48:51]\nv_mfma_f32_16x16x32_bf16 v[20:23], a[156:159], v[32:35], v[20:23]\nv_mfma_f32_16x16x32_bf16 v[56:59], a[252:255], v[16:19], v[56:59]\n" \
"v_mfma_f32_16x16x32_bf16 v[24:27], a[252:255], v[32:35], v[24:27]\nv_mfma_f32_16x16x32_bf16 v[60:63], v[204:207], v[16:19], v[60:63]\ns_waitcnt lgkmcnt(1)\nv_mfma_f32_16x16x32_bf16 v[28:31], v[204:207], v[32:35], v[28:31]\n" \
"s_waitcnt lgkmcnt(0)\nds_read_b128 v[16:19], v4 offset:17408\nv_mfma_f32_16x16x32_bf16 v[44:47], a[64:67], v[8:11], v[44:47]\nds_read_b128 v[32:35], v4 offset:50176\n" \
"v_mfma_f32_16x16x32_bf16 v[64:67], a[64:67], v[12:15], v[64:67]\nv_mfma_f32_16x16x32_bf16 v[48:51], a[160:163], v[8:11], v[48:51]\nv_mfma_f32_16x16x32_bf16 v[20:23], a[160:163], v[12:15], v[20:23]\nv_mfma_f32_16x16x32_bf16 v[56:59], v[112:115], v[8:11], v[56:59]\n" \
"v_mfma_f32_16x16x32_bf16 v[24:27], v[112:115], v[12:15], v[24:27]\nv_mfma_f32_16x16x32_bf16 v[60:63], v[208:211], v[8:11], v[60:63]\ns_waitcnt lgkmcnt(0)\nv_mfma_f32_16x16x32_bf16 v[28:31], v[208:211], v[12:15], v[28:31]\n" \
"ds_read_b128 v[8:11], v4 offset:18432\nv_mfma_f32_16x16x32_bf16 v[44:47], a[68:71], v[16:19], v[44:47]\nds_read_b128 v[12:15], v4 offset:51200\nv_mfma_f32_16x16x32_bf16 v[64:67], a[68:71], v[32:35], v[64:67]\n" \
"v_mfma_f32_16x16x32_bf16 v[48:51], a[164:167], v[16:19], v[48:51]\nv_mfma_f32_16x16x32_bf16 v[20:23], a[164:167], v[32:35], v[20:23]\ns_lshr_b32 s99, s20, 1\ns_add_u32 s20, s20, 1\n" \
"v_readlane_b32 s99, v3, s99\nv_mfma_f32_16x16x32_bf16 v[56:59], v[116:119], v[16:19], v[56:59]\nv_mfma_f32_16x16x32_bf16 v[24:27], v[116:119], v[32:35], v[24:27]\ns_lshr_b32 s99, s99, s98\n" \
"s_and_b32 s99, s99, 0x7f\ns_mul_i32 s99, s99, 0x50000\nv_mfma_f32_16x16x32_bf16 v[60:63], v[212:215], v[16:19], v[60:63]\ns_waitcnt lgkmcnt(0)\n" \
"s_add_u32 s99, s99, s22\ns_add_u32 s98, s99, 0x8000\nv_mfma_f32_16x16x32_bf16 v[28:31], v[212:215], v[32:35], v[28:31]\nds_read_b128 v[16:19], v4 offset:19456\n" \
"v_mfma_f32_16x16x32_bf16 v[44:47], a[72:75], v[8:11], v[44:47]\nds_read_b128 v[32:35], v4 offset:52224\nv_mfma_f32_16x16x32_bf16 v[64:67], a[72:75], v[12:15], v[64:67]\nv_mfma_f32_16x16x32_bf16 v[48:51], a[168:171], v[8:11], v[48:51]\n" \
"v_mfma_f32_16x16x32_bf16 v[20:23], a[168:171], v[12:15], v[20:23]\nv_mfma_f32_16x16x32_bf16 v[56:59], v[120:123], v[8:11], v[56:59]\nv_mfma_f32_16x16x32_bf16 v[24:27], v[120:123], v[12:15], v[24:27]\nv_mfma_f32_16x16x32_bf16 v[60:63], v[216:219], v[8:11], v[60:63]\n" \
"s_waitcnt lgkmcnt(0)\nv_mfma_f32_16x16x32_bf16 v[28:31], v[216:219], v[12:15], v[28:31]\nds_read_b128 v[8:11], v4 offset:20480\nv_mfma_f32_16x16x32_bf16 v[44:47], a[76:79], v[16:19], v[44:47]\n" \
"ds_read_b128 v[12:15], v4 offset:53248\nv_mfma_f32_16x16x32_bf16 v[64:67], a[76:79], v[32:35], v[64:67]\nv_mfma_f32_16x16x32_bf16 v[48:51], a[172:175], v[16:19], v[48:51]\nv_mfma_f32_16x16x32_bf16 v[20:23], a[172:175], v[32:35], v[20:23]\n" \
"v_mfma_f32_16x16x32_bf16 v[56:59], v[124:127], v[16:19], v[56:59]\nv_mfma_f32_16x16x32_bf16 v[24:27], v[124:127], v[32:35], v[24:27]\nv_mfma_f32_16x16x32_bf16 v[60:63], v[220:223], v[16:19], v[60:63]\ns_waitcnt lgkmcnt(0)\n" \
"v_mfma_f32_16x16x32_bf16 v[28:31], v[220:223], v[32:35], v[28:31]\nds_read_b128 v[36:39], v4 offset:21504\nv_mfma_f32_16x16x32_bf16 v[44:47], a[80:83], v[8:11], v[44:47]\nds_read_b128 v[32:35], v4 offset:54272\n" \
"v_mfma_f32_16x16x32_bf16 v[64:67], a[80:83], v[12:15], v[64:67]\nv_mfma_f32_16x16x32_bf16 v[48:51], a[176:179], v[8:11], v[48:51]\nv_mfma_f32_16x16x32_bf16 v[20:23], a[176:179], v[12:15], v[20:23]\nv_mfma_f32_16x16x32_bf16 v[56:59], v[128:131], v[8:11], v[56:59]\n" \
"v_mfma_f32_16x16x32_bf16 v[24:27], v[128:131], v[12:15], v[24:27]\nv_mfma_f32_16x16x32_bf16 v[60:63], v[224:227], v[8:11], v[60:63]\ns_waitcnt lgkmcnt(0)\nv_mfma_f32_16x16x32_bf16 v[28:31], v[224:227], v[12:15], v[28:31]\n" \
"ds_read_b128 v[40:43], v4 offset:22528\nv_mfma_f32_16x16x32_bf16 v[44:47], a[84:87], v[36:39], v[44:47]\nds_read_b128 v[16:19], v4 offset:55296\nv_xor_b32 v4, 0x10000, v4\n" \
"v_mfma_f32_16x16x32_bf16 v[64:67], a[84:87], v[32:35], v[64:67]\nv_mfma_f32_16x16x32_bf16 v[48:51], a[180:183], v[36:39], v[48:51]\nv_mfma_f32_16x16x32_bf16 v[20:23], a[180:183], v[32:35], v[20:23]\nv_mfma_f32_16x16x32_bf16 v[56:59], v[132:135], v[36:39], v[56:59]\n" \
"v_mfma_f32_16x16x32_bf16 v[24:27], v[132:135], v[32:35], v[24:27]\nv_mfma_f32_16x16x32_bf16 v[60:63], v[228:231], v[36:39], v[60:63]\ns_waitcnt lgkmcnt(0)\nv_mfma_f32_16x16x32_bf16 v[28:31], v[228:231], v[32:35], v[28:31]\n" \
"s_waitcnt vmcnt(0)\ns_barrier\nv_mfma_f32_16x16x32_bf16 v[44:47], a[88:91], v[40:43], v[44:47]\nds_read_b128 v[8:11], v4 offset:0\n" \
"v_mfma_f32_16x16x32_bf16 v[48:51], a[184:187], v[40:43], v[48:51]\nds_read_b128 v[12:15], v4 offset:32768\nv_mfma_f32_16x16x32_bf16 v[56:59], v[136:139], v[40:43], v[56:59]\nv_cvt_pk_bf16_f32 v36, v44, v45\n" \
"v_cvt_pk_bf16_f32 v37, v46, v47\nv_mfma_f32_16x16x32_bf16 v[60:63], v[232:235], v[40:43], v[60:63]\nv_cvt_pk_bf16_f32 v38, v48, v49\nv_cvt_pk_bf16_f32 v39, v50, v51\n" \
"v_mfma_f32_16x16x32_bf16 v[64:67], a[88:91], v[16:19], v[64:67]\nv_cvt_pk_bf16_f32 v32, v56, v57\nv_cvt_pk_bf16_f32 v33, v58, v59\nv_mfma_f32_16x16x32_bf16 v[20:23], a[184:187], v[16:19], v[20:23]\n" \
"buffer_store_dwordx4 v[36:39], v6, s[8:11], s99 offen offset:0 sc1\nv_cvt_pk_bf16_f32 v34, v60, v61\nv_cvt_pk_bf16_f32 v35, v62, v63\nv_mfma_f32_16x16x32_bf16 v[24:27], v[136:139], v[16:19], v[24:27]\n" \
"s_waitcnt lgkmcnt(1)\nv_cvt_pk_bf16_f32 v36, v64, v65\nv_cvt_pk_bf16_f32 v37, v66, v67\nv_mfma_f32_16x16x32_bf16 v[28:31], v[232:235], v[16:19], v[28:31]\n" \
"ds_read_b128 v[40:43], v4 offset:1024\nv_cvt_pk_bf16_f32 v38, v20, v21\nv_mfma_f32_16x16x32_bf16 v[44:47], a[0:3], v[8:11], 0\nds_read_b128 v[16:19], v4 offset:33792\n" \
"v_cvt_pk_bf16_f32 v39, v22, v23\nv_mfma_f32_16x16x32_bf16 v[48:51], a[96:99], v[8:11], 0\nbuffer_store_dwordx4 v[32:35], v6, s[8:11], s99 offen offset:512 sc1\nv_cvt_pk_bf16_f32 v52, v24, v25\n" \
"v_cvt_pk_bf16_f32 v53, v26, v27\nv_cvt_pk_bf16_f32 v54, v28, v29\nv_mfma_f32_16x16x32_bf16 v[56:59], a[192:195], v[8:11], 0\ns_waitcnt lgkmcnt(2)\n" \
"v_cvt_pk_bf16_f32 v55, v30, v31\nv_mfma_f32_16x16x32_bf16 v[60:63], v[144:147], v[8:11], 0\nv_mfma_f32_16x16x32_bf16 v[64:67], a[0:3], v[12:15], 0\nv_mfma_f32_16x16x32_bf16 v[20:23], a[96:99], v[12:15], 0\n" \
"buffer_store_dwordx4 v[36:39], v6, s[8:11], s98 offen offset:0 sc1\nv_mfma_f32_16x16x32_bf16 v[24:27], a[192:195], v[12:15], 0\ns_waitcnt lgkmcnt(0)\nv_mfma_f32_16x16x32_bf16 v[28:31], v[144:147], v[12:15], 0\n" \
"ds_read_b128 v[8:11], v4 offset:2048\nv_mfma_f32_16x16x32_bf16 v[44:47], a[4:7], v[40:43], v[44:47]\nds_read_b128 v[12:15], v4 offset:34816\nv_mfma_f32_16x16x32_bf16 v[64:67], a[4:7], v[16:19], v[64:67]\n" \
"buffer_store_dwordx4 v[52:55], v6, s[8:11], s98 offen offset:512 sc1\nv_mfma_f32_16x16x32_bf16 v[48:51], a[100:103], v[40:43], v[48:51]\nv_mfma_f32_16x16x32_bf16 v[20:23], a[100:103], v[16:19], v[20:23]\nv_mfma_f32_16x16x32_bf16 v[56:59], a[196:199], v[40:43], v[56:59]\n" \
"v_mfma_f32_16x16x32_bf16 v[24:27], a[196:199], v[16:19], v[24:27]\nv_mfma_f32_16x16x32_bf16 v[60:63], v[148:151], v[40:43], v[60:63]\ns_cmp_eq_u32 s14, 0\nv_mfma_f32_16x16x32_bf16 v[28:31], v[148:151], v[16:19], v[28:31]\n" \
"s_cbranch_scc0 1b\n4:\nbuffer_load_dword v68, v1, s[8:11], 0 offen sc1\nv_mov_b32 v69, s20\n" \
"s_add_u32 s40, s20, 1\ns_waitcnt vmcnt(0)\nv_cmp_gt_i32 vcc, s40, v68\ns_cbranch_vccz 8f\n" \
"buffer_store_dword v69, v2, s[8:11], 0 offen sc1\ns_mov_b32 s25, 0x40000000\n7:\ns_add_u32 s26, s26, 1\n" \
"s_cmp_lt_u32 s26, 0x40000\ns_cbranch_scc0 9f\ns_sleep 8\nbuffer_load_dword v68, v1, s[8:11], 0 offen sc1\n" \
"s_waitcnt vmcnt(0)\nv_cmp_gt_i32 vcc, s40, v68\ns_cbranch_vccnz 7b\n8:\n" \
"s_cmp_ge_u32 s20, s28\ns_cbranch_scc1 10f\ns_lshr_b32 s40, s20, 1\nv_readlane_b32 s40, v3, s40\n" \
"s_and_b32 s41, s20, 1\ns_lshl_b32 s41, s41, 3\ns_lshr_b32 s40, s40, s41\ns_and_b32 s40, s40, 0x7f\n" \
"s_mul_i32 s40, s40, 0x38000\ns_add_u32 s40, s40, s21\ns_xor_b32 s41, s23, 0x10000\ns_add_u32 m0, s41, 0\n" \
"s_add_u32 s40, s40, 0\nbuffer_load_dwordx4 v0, s[8:11], s40 offen offset:0 sc1 lds\nbuffer_load_dwordx4 v0, s[8:11], s40 offen offset:1024 sc1 lds\nbuffer_load_dwordx4 v0, s[8:11], s40 offen offset:2048 sc1 lds\n" \
"buffer_load_dwordx4 v0, s[8:11], s40 offen offset:3072 sc1 lds\ns_add_u32 m0, s41, 0x1000\ns_add_u32 s40, s40, 0x1000\nbuffer_load_dwordx4 v0, s[8:11], s40 offen offset:0 sc1 lds\n" \
"buffer_load_dwordx4 v0, s[8:11], s40 offen offset:1024 sc1 lds\nbuffer_load_dwordx4 v0, s[8:11], s40 offen offset:2048 sc1 lds\nbuffer_load_dwordx4 v0, s[8:11], s40 offen offset:3072 sc1 lds\ns_add_u32 m0, s41, 0x2000\n" \
"s_add_u32 s40, s40, 0x1000\nbuffer_load_dwordx4 v0, s[8:11], s40 offen offset:0 sc1 lds\nbuffer_load_dwordx4 v0, s[8:11], s40 offen offset:1024 sc1 lds\nbuffer_load_dwordx4 v0, s[8:11], s40 offen offset:2048 sc1 lds\n" \
"buffer_load_dwordx4 v0, s[8:11], s40 offen offset:3072 sc1 lds\nbuffer_store_dword v69, v2, s[8:11], s25 offen sc1\ns_mov_b32 s25, 0x40000000\ns_waitcnt vmcnt(1) lgkmcnt(0)\n" \
"s_barrier\nds_read_b128 v[68:71], v4 offset:0\nds_read_b128 v[72:75], v4 offset:32768\nds_read_b128 v[76:79], v4 offset:1024\n" \
"ds_read_b128 v[80:83], v4 offset:33792\nds_read_b128 v[8:11], v4 offset:2048\nds_read_b128 v[12:15], v4 offset:34816\ns_waitcnt lgkmcnt(0)\n" \
"v_mfma_f32_16x16x32_bf16 v[44:47], a[0:3], v[68:71], 0\nv_mfma_f32_16x16x32_bf16 v[48:51], a[96:99], v[68:71], 0\nv_mfma_f32_16x16x32_bf16 v[56:59], a[192:195], v[68:71], 0\nv_mfma_f32_16x16x32_bf16 v[60:63], v[144:147], v[68:71], 0\n" \
"v_mfma_f32_16x16x32_bf16 v[64:67], a[0:3], v[72:75], 0\nv_mfma_f32_16x16x32_bf16 v[20:23], a[96:99], v[72:75], 0\nv_mfma_f32_16x16x32_bf16 v[24:27], a[192:195], v[72:75], 0\nv_mfma_f32_16x16x32_bf16 v[28:31], v[144:147], v[72:75], 0\n" \
"v_mfma_f32_16x16x32_bf16 v[44:47], a[4:7], v[76:79], v[44:47]\nv_mfma_f32_16x16x32_bf16 v[48:51], a[100:103], v[76:79], v[48:51]\nv_mfma_f32_16x16x32_bf16 v[56:59], a[196:199], v[76:79], v[56:59]\nv_mfma_f32_16x16x32_bf16 v[60:63], v[148:151], v[76:79], v[60:63]\n" \
"v_mfma_f32_16x16x32_bf16 v[64:67], a[4:7], v[80:83], v[64:67]\nv_mfma_f32_16x16x32_bf16 v[20:23], a[100:103], v[80:83], v[20:23]\nv_mfma_f32_16x16x32_bf16 v[24:27], a[196:199], v[80:83], v[24:27]\nv_mfma_f32_16x16x32_bf16 v[28:31], v[148:151], v[80:83], v[28:31]\n" \
"s_branch 1b\n10:\ns_lshr_b32 s42, s20, 1\nv_readlane_b32 s42, v3, s42\n" \
"s_and_b32 s41, s20, 1\ns_lshl_b32 s41, s41, 3\ns_lshr_b32 s42, s42, s41\ns_and_b32 s42, s42, 0x7f\n" \
"s_mul_i32 s43, s42, 0x50000\ns_add_u32 s43, s43, s30\ns_mul_i32 s42, s42, 0x38000\ns_add_u32 s42, s42, s29\n" \
"s_and_b32 s41, s23, 0x10000\ns_xor_b32 s41, s41, 0x10000\ns_sub_u32 s40, s24, 0x27000\ns_mul_i32 s40, s40, 24\n" \
"s_add_u32 s41, s41, s40\nbuffer_store_dword v69, v2, s[8:11], s25 offen sc1\ns_mov_b32 s25, 0x40000000\ns_add_u32 s20, s20, 1\n" \
"v_add_u32 v32, s41, v0\ns_add_u32 s40, s42, 0x1000\ns_cmp_eq_u32 s24, 0x27300\ns_cselect_b32 s44, 0x400, 0\n" \
"s_sub_u32 s44, s40, s44\n11:\ns_add_u32 m0, s41, 0\ns_nop 0\n" \
"buffer_load_dwordx4 v0, s[8:11], s42 offen offset:0 sc1 lds\nbuffer_load_dwordx4 v0, s[8:11], s42 offen offset:1024 sc1 lds\nbuffer_load_dwordx4 v0, s[8:11], s42 offen offset:2048 sc1 lds\nbuffer_load_dwordx4 v0, s[8:11], s42 offen offset:3072 sc1 lds\n" \
"s_add_u32 m0, s41, 0x1000\ns_nop 0\nbuffer_load_dwordx4 v0, s[8:11], s40 offen offset:0 sc1 lds\nbuffer_load_dwordx4 v0, s[8:11], s44 offen offset:1024 sc1 lds\n" \
"s_waitcnt vmcnt(0)\nds_read_b128 v[8:11], v32 offset:0\nds_read_b128 v[12:15], v32 offset:1024\nds_read_b128 v[16:19], v32 offset:2048\n" \
"ds_read_b128 v[20:23], v32 offset:3072\nds_read_b128 v[24:27], v32 offset:4096\nds_read_b128 v[28:31], v32 offset:5120\ns_waitcnt lgkmcnt(0)\n" \
"v_max3_u32 v33, v8, v10, v12\nv_max3_u32 v33, v33, v14, v16\nv_max3_u32 v33, v33, v18, v20\nv_max3_u32 v33, v33, v22, v24\n" \
"v_max3_u32 v33, v33, v26, v28\nv_max_u32 v33, v33, v30\nv_cmp_eq_u32 vcc, -1, v33\ns_nop 4\n" \
"s_cbranch_vccz 16f\ns_add_u32 s26, s26, 1\ns_cmp_lt_u32 s26, 0x40000\ns_cbranch_scc0 9f\n" \
"s_branch 11b\n16:\ns_barrier\nv_mov_b32 v69, s20\n" \
"ds_read_b128 v[8:11], v4 offset:0\nds_read_b128 v[12:15], v4 offset:1024\nds_read_b128 v[16:19], v4 offset:2048\nds_read_b128 v[20:23], v4 offset:3072\n" \
"ds_read_b128 v[24:27], v4 offset:4096\ns_waitcnt lgkmcnt(4)\nv_mfma_f32_16x16x32_bf16 v[28:31], a[0:3], v[8:11], 0\nv_mfma_f32_16x16x32_bf16 v[32:35], a[96:99], v[8:11], 0\n" \
"v_mfma_f32_16x16x32_bf16 v[36:39], a[192:195], v[8:11], 0\nv_mfma_f32_16x16x32_bf16 v[40:43], v[144:147], v[8:11], 0\nds_read_b128 v[8:11], v4 offset:5120\ns_waitcnt lgkmcnt(4)\n" \
"v_mfma_f32_16x16x32_bf16 v[28:31], a[4:7], v[12:15], v[28:31]\nv_mfma_f32_16x16x32_bf16 v[32:35], a[100:103], v[12:15], v[32:35]\nv_mfma_f32_16x16x32_bf16 v[36:39], a[196:199], v[12:15], v[36:39]\nv_mfma_f32_16x16x32_bf16 v[40:43], v[148:151], v[12:15], v[40:43]\n" \
"ds_read_b128 v[12:15], v4 offset:6144\ns_waitcnt lgkmcnt(4)\nv_mfma_f32_16x16x32_bf16 v[28:31], a[8:11], v[16:19], v[28:31]\nv_mfma_f32_16x16x32_bf16 v[32:35], a[104:107], v[16:19], v[32:35]\n" \
"v_mfma_f32_16x16x32_bf16 v[36:39], a[200:203], v[16:19], v[36:39]\nv_mfma_f32_16x16x32_bf16 v[40:43], v[152:155], v[16:19], v[40:43]\nds_read_b128 v[16:19], v4 offset:7168\ns_waitcnt lgkmcnt(4)\n" \
"v_mfma_f32_16x16x32_bf16 v[28:31], a[12:15], v[20:23], v[28:31]\nv_mfma_f32_16x16x32_bf16 v[32:35], a[108:111], v[20:23], v[32:35]\nv_mfma_f32_16x16x32_bf16 v[36:39], a[204:207], v[20:23], v[36:39]\nv_mfma_f32_16x16x32_bf16 v[40:43], v[156:159], v[20:23], v[40:43]\n" \
"ds_read_b128 v[20:23], v4 offset:8192\ns_waitcnt lgkmcnt(4)\nv_mfma_f32_16x16x32_bf16 v[28:31], a[16:19], v[24:27], v[28:31]\nv_mfma_f32_16x16x32_bf16 v[32:35], a[112:115], v[24:27], v[32:35]\n" \
"v_mfma_f32_16x16x32_bf16 v[36:39], a[208:211], v[24:27], v[36:39]\nv_mfma_f32_16x16x32_bf16 v[40:43], v[160:163], v[24:27], v[40:43]\nds_read_b128 v[24:27], v4 offset:9216\ns_waitcnt lgkmcnt(4)\n" \
"v_mfma_f32_16x16x32_bf16 v[28:31], a[20:23], v[8:11], v[28:31]\nv_mfma_f32_16x16x32_bf16 v[32:35], a[116:119], v[8:11], v[32:35]\nv_mfma_f32_16x16x32_bf16 v[36:39], a[212:215], v[8:11], v[36:39]\nv_mfma_f32_16x16x32_bf16 v[40:43], v[164:167], v[8:11], v[40:43]\n" \
"ds_read_b128 v[8:11], v4 offset:10240\ns_waitcnt lgkmcnt(4)\nv_mfma_f32_16x16x32_bf16 v[28:31], a[24:27], v[12:15], v[28:31]\nv_mfma_f32_16x16x32_bf16 v[32:35], a[120:123], v[12:15], v[32:35]\n" \
"v_mfma_f32_16x16x32_bf16 v[36:39], a[216:219], v[12:15], v[36:39]\nv_mfma_f32_16x16x32_bf16 v[40:43], v[168:171], v[12:15], v[40:43]\nds_read_b128 v[12:15], v4 offset:11264\ns_waitcnt lgkmcnt(4)\n" \
"v_mfma_f32_16x16x32_bf16 v[28:31], a[28:31], v[16:19], v[28:31]\nv_mfma_f32_16x16x32_bf16 v[32:35], a[124:127], v[16:19], v[32:35]\nv_mfma_f32_16x16x32_bf16 v[36:39], a[220:223], v[16:19], v[36:39]\nv_mfma_f32_16x16x32_bf16 v[40:43], v[172:175], v[16:19], v[40:43]\n" \
"ds_read_b128 v[16:19], v4 offset:12288\ns_waitcnt lgkmcnt(4)\nv_mfma_f32_16x16x32_bf16 v[28:31], a[32:35], v[20:23], v[28:31]\nv_mfma_f32_16x16x32_bf16 v[32:35], a[128:131], v[20:23], v[32:35]\n" \
"v_mfma_f32_16x16x32_bf16 v[36:39], a[224:227], v[20:23], v[36:39]\nv_mfma_f32_16x16x32_bf16 v[40:43], v[176:179], v[20:23], v[40:43]\nds_read_b128 v[20:23], v4 offset:13312\ns_waitcnt lgkmcnt(4)\n" \
"v_mfma_f32_16x16x32_bf16 v[28:31], a[36:39], v[24:27], v[28:31]\nv_mfma_f32_16x16x32_bf16 v[32:35], a[132:135], v[24:27], v[32:35]\nv_mfma_f32_16x16x32_bf16 v[36:39], a[228:231], v[24:27], v[36:39]\nv_mfma_f32_16x16x32_bf16 v[40:43], v[180:183], v[24:27], v[40:43]\n" \
"ds_read_b128 v[24:27], v4 offset:14336\ns_waitcnt lgkmcnt(4)\nv_mfma_f32_16x16x32_bf16 v[28:31], a[40:43], v[8:11], v[28:31]\nv_mfma_f32_16x16x32_bf16 v[32:35], a[136:139], v[8:11], v[32:35]\n" \
"v_mfma_f32_16x16x32_bf16 v[36:39], a[232:235], v[8:11], v[36:39]\nv_mfma_f32_16x16x32_bf16 v[40:43], v[184:187], v[8:11], v[40:43]\nds_read_b128 v[8:11], v4 offset:15360\ns_waitcnt lgkmcnt(4)\n" \
"v_mfma_f32_16x16x32_bf16 v[28:31], a[44:47], v[12:15], v[28:31]\nv_mfma_f32_16x16x32_bf16 v[32:35], a[140:143], v[12:15], v[32:35]\nv_mfma_f32_16x16x32_bf16 v[36:39], a[236:239], v[12:15], v[36:39]\nv_mfma_f32_16x16x32_bf16 v[40:43], v[188:191], v[12:15], v[40:43]\n" \
"ds_read_b128 v[12:15], v4 offset:16384\ns_waitcnt lgkmcnt(4)\nv_mfma_f32_16x16x32_bf16 v[28:31], a[48:51], v[16:19], v[28:31]\nv_mfma_f32_16x16x32_bf16 v[32:35], a[144:147], v[16:19], v[32:35]\n" \
"v_mfma_f32_16x16x32_bf16 v[36:39], a[240:243], v[16:19], v[36:39]\nv_mfma_f32_16x16x32_bf16 v[40:43], v[192:195], v[16:19], v[40:43]\nds_read_b128 v[16:19], v4 offset:17408\ns_waitcnt lgkmcnt(4)\n" \
"v_mfma_f32_16x16x32_bf16 v[28:31], a[52:55], v[20:23], v[28:31]\nv_mfma_f32_16x16x32_bf16 v[32:35], a[148:151], v[20:23], v[32:35]\nv_mfma_f32_16x16x32_bf16 v[36:39], a[244:247], v[20:23], v[36:39]\nv_mfma_f32_16x16x32_bf16 v[40:43], v[196:199], v[20:23], v[40:43]\n" \
"ds_read_b128 v[20:23], v4 offset:18432\ns_waitcnt lgkmcnt(4)\nv_mfma_f32_16x16x32_bf16 v[28:31], a[56:59], v[24:27], v[28:31]\nv_mfma_f32_16x16x32_bf16 v[32:35], a[152:155], v[24:27], v[32:35]\n" \
"v_mfma_f32_16x16x32_bf16 v[36:39], a[248:251], v[24:27], v[36:39]\nv_mfma_f32_16x16x32_bf16 v[40:43], v[200:203], v[24:27], v[40:43]\nds_read_b128 v[24:27], v4 offset:19456\ns_waitcnt lgkmcnt(4)\n" \
"v_mfma_f32_16x16x32_bf16 v[28:31], a[60:63], v[8:11], v[28:31]\nv_mfma_f32_16x16x32_bf16 v[32:35], a[156:159], v[8:11], v[32:35]\nv_mfma_f32_16x16x32_bf16 v[36:39], a[252:255], v[8:11], v[36:39]\nv_mfma_f32_16x16x32_bf16 v[40:43], v[204:207], v[8:11], v[40:43]\n" \
"ds_read_b128 v[8:11], v4 offset:20480\ns_waitcnt lgkmcnt(4)\nv_mfma_f32_16x16x32_bf16 v[28:31], a[64:67], v[12:15], v[28:31]\nv_mfma_f32_16x16x32_bf16 v[32:35], a[160:163], v[12:15], v[32:35]\n" \
"v_mfma_f32_16x16x32_bf16 v[36:39], v[112:115], v[12:15], v[36:39]\nv_mfma_f32_16x16x32_bf16 v[40:43], v[208:211], v[12:15], v[40:43]\nds_read_b128 v[12:15], v4 offset:21504\ns_waitcnt lgkmcnt(4)\n" \
"v_mfma_f32_16x16x32_bf16 v[28:31], a[68:71], v[16:19], v[28:31]\nv_mfma_f32_16x16x32_bf16 v[32:35], a[164:167], v[16:19], v[32:35]\nv_mfma_f32_16x16x32_bf16 v[36:39], v[116:119], v[16:19], v[36:39]\nv_mfma_f32_16x16x32_bf16 v[40:43], v[212:215], v[16:19], v[40:43]\n" \
"ds_read_b128 v[16:19], v4 offset:22528\ns_waitcnt lgkmcnt(4)\nv_mfma_f32_16x16x32_bf16 v[28:31], a[72:75], v[20:23], v[28:31]\nv_mfma_f32_16x16x32_bf16 v[32:35], a[168:171], v[20:23], v[32:35]\n" \
"v_mfma_f32_16x16x32_bf16 v[36:39], v[120:123], v[20:23], v[36:39]\nv_mfma_f32_16x16x32_bf16 v[40:43], v[216:219], v[20:23], v[40:43]\ns_waitcnt lgkmcnt(3)\nv_mfma_f32_16x16x32_bf16 v[28:31], a[76:79], v[24:27], v[28:31]\n" \
"v_mfma_f32_16x16x32_bf16 v[32:35], a[172:175], v[24:27], v[32:35]\nv_mfma_f32_16x16x32_bf16 v[36:39], v[124:127], v[24:27], v[36:39]\nv_mfma_f32_16x16x32_bf16 v[40:43], v[220:223], v[24:27], v[40:43]\ns_waitcnt lgkmcnt(2)\n" \
"v_mfma_f32_16x16x32_bf16 v[28:31], a[80:83], v[8:11], v[28:31]\nv_mfma_f32_16x16x32_bf16 v[32:35], a[176:179], v[8:11], v[32:35]\nv_mfma_f32_16x16x32_bf16 v[36:39], v[128:131], v[8:11], v[36:39]\nv_mfma_f32_16x16x32_bf16 v[40:43], v[224:227], v[8:11], v[40:43]\n" \
"s_waitcnt lgkmcnt(1)\nv_mfma_f32_16x16x32_bf16 v[28:31], a[84:87], v[12:15], v[28:31]\nv_mfma_f32_16x16x32_bf16 v[32:35], a[180:183], v[12:15], v[32:35]\nv_mfma_f32_16x16x32_bf16 v[36:39], v[132:135], v[12:15], v[36:39]\n" \
"v_mfma_f32_16x16x32_bf16 v[40:43], v[228:231], v[12:15], v[40:43]\ns_waitcnt lgkmcnt(0)\nv_mfma_f32_16x16x32_bf16 v[28:31], a[88:91], v[16:19], v[28:31]\nv_mfma_f32_16x16x32_bf16 v[32:35], a[184:187], v[16:19], v[32:35]\n" \
"v_mfma_f32_16x16x32_bf16 v[36:39], v[136:139], v[16:19], v[36:39]\nv_mfma_f32_16x16x32_bf16 v[40:43], v[232:235], v[16:19], v[40:43]\nbuffer_store_dword v69, v2, s[8:11], 0 offen sc1\ns_nop 7\n" \
"v_cvt_pk_bf16_f32 v8, v28, v29\nv_cvt_pk_bf16_f32 v9, v30, v31\nv_cvt_pk_bf16_f32 v10, v32, v33\nv_cvt_pk_bf16_f32 v11, v34, v35\n" \
"buffer_store_dwordx4 v[8:11], v6, s[8:11], s43 offen offset:0 sc1\nv_cvt_pk_bf16_f32 v12, v36, v37\nv_cvt_pk_bf16_f32 v13, v38, v39\nv_cvt_pk_bf16_f32 v14, v40, v41\n" \
"v_cvt_pk_bf16_f32 v15, v42, v43\nbuffer_store_dwordx4 v[12:15], v6, s[8:11], s43 offen offset:512 sc1\ns_waitcnt vmcnt(0)\nbuffer_store_dword v69, v2, s[8:11], 0 offen offset:16 sc1\n" \
"s_cmp_ge_u32 s20, s31\ns_cbranch_scc1 9f\ns_add_u32 s40, s20, 1\ns_barrier\n" \
"s_branch 7b\n9:\ns_waitcnt vmcnt(0)\ns_endpgm\n"
#define G_LOOP \
"s_branch 4f\n.p2align 6\n1:\ns_waitcnt lgkmcnt(2)\n" \
"v_mfma_f32_16x16x32_bf16 v[8:11], a[104:107], v[36:39], v[8:11]\ns_lshr_b32 s99, s20, 1\ns_and_b32 s98, s20, 1\nv_mov_b32 v110, s20\n" \
"v_mfma_f32_16x16x32_bf16 v[12:15], a[104:107], v[40:43], v[12:15]\ns_add_u32 s20, s20, 1\ns_add_u32 s97, s20, 1\ns_lshr_b32 s96, s97, 1\n" \
"v_mfma_f32_16x16x32_bf16 v[16:19], a[232:235], v[36:39], v[16:19]\nds_read_b128 v[52:55], v7 offset:28672\nv_readlane_b32 s96, v6, s96\nv_mfma_f32_16x16x32_bf16 v[20:23], a[232:235], v[40:43], v[20:23]\n" \
"ds_read_b128 v[56:59], v7 offset:61440\nv_xor_b32 v4, 0x400, v4\nv_mfma_f32_16x16x32_bf16 v[24:27], v[216:219], v[36:39], v[24:27]\ns_and_b32 s97, s97, 1\n" \
"s_lshl_b32 s97, s97, 3\ns_lshr_b32 s96, s96, s97\nv_mfma_f32_16x16x32_bf16 v[28:31], v[216:219], v[40:43], v[28:31]\ns_waitcnt lgkmcnt(2)\n" \
"s_and_b32 s96, s96, 0x7f\ns_lshl_b32 s97, s96, 13\nv_mfma_f32_16x16x32_bf16 v[8:11], a[108:111], v[44:47], v[8:11]\ns_lshl_b32 s98, s98, 3\n" \
"v_mov_b32 v32, v111\nv_mov_b32 v33, v111\nv_mfma_f32_16x16x32_bf16 v[12:15], a[108:111], v[48:51], v[12:15]\ns_lshl_b32 s96, s96, 16\n" \
"v_mul_f32 v252, 0xc0135761, v34\nv_mul_f32 v34, v34, v34\nv_mfma_f32_16x16x32_bf16 v[16:19], a[236:239], v[44:47], v[16:19]\nds_read_b128 v[36:39], v7 offset:29696\n" \
"v_readlane_b32 s99, v6, s99\nv_mfma_f32_16x16x32_bf16 v[20:23], a[236:239], v[48:51], v[20:23]\nds_read_b128 v[40:43], v7 offset:62464\nv_mul_f32 v34, v34, v252\n" \
"v_mfma_f32_16x16x32_bf16 v[24:27], v[220:223], v[44:47], v[24:27]\nds_read_b128 v[60:63], v7 offset:30720\nv_mul_f32 v254, v35, v35\nv_mfma_f32_16x16x32_bf16 v[28:31], v[220:223], v[48:51], v[28:31]\n" \
"s_waitcnt lgkmcnt(3)\nv_permlane16_swap_b32 v32, v33\nv_mfma_f32_16x16x32_bf16 v[8:11], a[112:115], v[52:55], v[8:11]\nds_read_b128 v[44:47], v7 offset:63488\n" \
"v_mul_f32 v253, 0x3d372713, v34\nv_mfma_f32_16x16x32_bf16 v[12:15], a[112:115], v[56:59], v[12:15]\ns_lshr_b32 s99, s99, s98\ns_and_b32 s99, s99, 0x7f\n" \
"s_mul_i32 s99, s99, 0x38000\nv_mfma_f32_16x16x32_bf16 v[16:19], a[240:243], v[52:55], v[16:19]\nds_read_b128 v[48:51], v7 offset:31744\ns_or_b32 s16, s99, s18\n" \
"v_mfma_f32_16x16x32_bf16 v[20:23], a[240:243], v[56:59], v[20:23]\ns_or_b32 s17, s99, s19\ns_add_u32 s96, s96, s21\nv_mfma_f32_16x16x32_bf16 v[24:27], v[224:227], v[52:55], v[24:27]\n" \
"ds_read_b128 v[64:67], v7 offset:64512\nv_xor_b32 v7, 0x10000, v7\nv_mfma_f32_16x16x32_bf16 v[28:31], v[224:227], v[56:59], v[28:31]\ns_waitcnt lgkmcnt(3)\n" \
"v_mfma_f32_16x16x32_bf16 v[8:11], a[116:119], v[36:39], v[8:11]\nv_mfma_f32_16x16x32_bf16 v[12:15], a[116:119], v[40:43], v[12:15]\nv_mfma_f32_16x16x32_bf16 v[16:19], a[244:247], v[36:39], v[16:19]\ns_waitcnt vmcnt(3) lgkmcnt(0)\n" \
"s_barrier\nv_mfma_f32_16x16x32_bf16 v[20:23], a[244:247], v[40:43], v[20:23]\nds_read_b128 v[52:55], v4 offset:0\nv_mfma_f32_16x16x32_bf16 v[24:27], v[228:231], v[36:39], v[24:27]\n" \
"ds_read_b128 v[56:59], v7 offset:0\nv_mfma_f32_16x16x32_bf16 v[28:31], v[228:231], v[40:43], v[28:31]\nds_read_b128 v[36:39], v7 offset:32768\nv_mfma_f32_16x16x32_bf16 v[8:11], a[120:123], v[60:63], v[8:11]\n" \
"ds_read_b128 v[40:43], v7 offset:1024\nv_mfma_f32_16x16x32_bf16 v[12:15], a[120:123], v[44:47], v[12:15]\ns_waitcnt lgkmcnt(3)\nv_add_f32 v52, v52, v53\n" \
"v_mfma_f32_16x16x32_bf16 v[8:11], a[124:127], v[48:51], v[8:11]\nbuffer_load_dwordx4 v[68:71], v1, s[8:11], s97 offen offset:1024 sc1\nv_mfma_f32_16x16x32_bf16 v[12:15], a[124:127], v[64:67], v[12:15]\nbuffer_load_dwordx4 v[72:75], v1, s[8:11], s97 offen offset:0 sc1\n" \
"v_mfma_f32_16x16x32_bf16 v[16:19], a[248:251], v[60:63], v[16:19]\nds_read_b128 v[76:79], v7 offset:33792\nv_add_f32 v54, v54, v55\nv_mfma_f32_16x16x32_bf16 v[20:23], a[248:251], v[44:47], v[20:23]\n" \
"v_cmp_u_f32 s[98:99], v52, v54\nv_fma_f32 v52, v32, v240, v8\nv_mfma_f32_16x16x32_bf16 v[16:19], a[252:255], v[48:51], v[16:19]\ns_waitcnt vmcnt(2)\n" \
"buffer_store_dword v110, v3, s[8:11], s23 offen offset:0 sc1\nv_mfma_f32_16x16x32_bf16 v[20:23], a[252:255], v[64:67], v[20:23]\ns_mov_b32 s23, 0\nv_fma_f32 v53, v32, v241, v9\n" \
"v_fma_f32 v54, v32, v242, v10\nv_mfma_f32_16x16x32_bf16 v[24:27], v[232:235], v[60:63], v[24:27]\ns_cmp_lt_u32 s20, s26\ns_cselect_b32 s98, s98, -1\n" \
"v_fma_f32 v55, v32, v243, v11\nv_mfma_f32_16x16x32_bf16 v[28:31], v[232:235], v[44:47], v[28:31]\nv_fma_f32 v60, v33, v240, v12\nv_fma_f32 v61, v33, v241, v13\n" \
"v_mfma_f32_16x16x32_bf16 v[24:27], v[236:239], v[48:51], v[24:27]\ns_waitcnt lgkmcnt(0)\nv_fma_f32 v62, v33, v242, v14\nv_fma_f32 v63, v33, v243, v15\n" \
"v_mfma_f32_16x16x32_bf16 v[28:31], v[236:239], v[64:67], v[28:31]\ns_cmp_lg_u64 s[98:99], 0\ns_cbranch_scc1 4f\n5:\n" \
"v_mfma_f32_16x16x32_bf16 v[8:11], a[0:3], v[56:59], 0\ns_xor_b32 m0, s22, 0\nv_fma_f32 v44, v32, v244, v16\nv_fma_f32 v45, v32, v245, v17\n" \
"v_mfma_f32_16x16x32_bf16 v[12:15], a[0:3], v[36:39], 0\nv_fma_f32 v46, v32, v246, v18\nv_fma_f32 v47, v32, v247, v19\nv_mfma_f32_16x16x32_bf16 v[8:11], a[4:7], v[40:43], v[8:11]\n" \
"v_fma_f32 v48, v33, v244, v20\nv_fma_f32 v49, v33, v245, v21\nv_mfma_f32_16x16x32_bf16 v[12:15], a[4:7], v[76:79], v[12:15]\nv_fma_f32 v50, v33, v246, v22\n" \
"v_fma_f32 v51, v33, v247, v23\nv_mfma_f32_16x16x32_bf16 v[16:19], a[128:131], v[56:59], 0\nds_read_b128 v[64:67], v7 offset:2048\nv_fma_f32 v80, v32, v248, v24\n" \
"v_mfma_f32_16x16x32_bf16 v[20:23], a[128:131], v[36:39], 0\nds_read_b128 v[84:87], v7 offset:34816\nv_fma_f32 v81, v32, v249, v25\nv_mfma_f32_16x16x32_bf16 v[16:19], a[132:135], v[40:43], v[16:19]\n" \
"v_fma_f32 v82, v32, v250, v26\nv_fma_f32 v83, v32, v251, v27\nv_mfma_f32_16x16x32_bf16 v[20:23], a[132:135], v[76:79], v[20:23]\nv_fma_f32 v88, v33, v248, v28\n" \
"v_fma_f32 v89, v33, v249, v29\nv_fma_f32 v90, v33, v250, v30\nv_fma_f32 v91, v33, v251, v31\nv_mfma_f32_16x16x32_bf16 v[24:27], v[112:115], v[56:59], 0\n" \
"ds_read_b128 v[92:95], v7 offset:3072\nv_xor_b32 v5, 0x400, v5\nv_mfma_f32_16x16x32_bf16 v[28:31], v[112:115], v[36:39], 0\nds_read_b128 v[56:59], v7 offset:35840\n" \
"v_mfma_f32_16x16x32_bf16 v[24:27], v[116:119], v[40:43], v[24:27]\ns_waitcnt lgkmcnt(3)\nv_permlane16_swap_b32 v52, v60\nv_mfma_f32_16x16x32_bf16 v[28:31], v[116:119], v[76:79], v[28:31]\n" \
"s_waitcnt lgkmcnt(2)\nv_permlane16_swap_b32 v53, v61\nv_mfma_f32_16x16x32_bf16 v[8:11], a[8:11], v[64:67], v[8:11]\nv_permlane16_swap_b32 v54, v62\n" \
"v_mfma_f32_16x16x32_bf16 v[12:15], a[8:11], v[84:87], v[12:15]\nv_permlane16_swap_b32 v55, v63\nv_mfma_f32_16x16x32_bf16 v[16:19], a[136:139], v[64:67], v[16:19]\nds_read_b128 v[36:39], v7 offset:4096\n" \
"v_mul_f32 v40, v52, v52\nv_mfma_f32_16x16x32_bf16 v[20:23], a[136:139], v[84:87], v[20:23]\nds_read_b128 v[76:79], v7 offset:36864\nv_mul_f32 v41, v53, v53\n" \
"v_mfma_f32_16x16x32_bf16 v[24:27], v[120:123], v[64:67], v[24:27]\ns_waitcnt lgkmcnt(2)\nv_mul_f32 v42, v54, v54\nv_mul_f32 v43, v55, v55\n" \
"v_mfma_f32_16x16x32_bf16 v[28:31], v[120:123], v[84:87], v[28:31]\nv_fma_f32 v40, v40, v253, v252\nv_fma_f32 v41, v41, v253, v252\nv_mfma_f32_16x16x32_bf16 v[8:11], a[12:15], v[92:95], v[8:11]\n" \
"v_fma_f32 v42, v42, v253, v252\nv_fma_f32 v43, v43, v253, v252\nv_mfma_f32_16x16x32_bf16 v[12:15], a[12:15], v[56:59], v[12:15]\nv_mul_f32 v40, v40, v52\n" \
"v_mul_f32 v41, v41, v53\nv_mfma_f32_16x16x32_bf16 v[16:19], a[140:143], v[92:95], v[16:19]\nds_read_b128 v[64:67], v7 offset:5120\nv_mul_f32 v42, v42, v54\n" \
"v_mfma_f32_16x16x32_bf16 v[20:23], a[140:143], v[56:59], v[20:23]\nds_read_b128 v[84:87], v7 offset:37888\nv_mul_f32 v43, v43, v55\nv_mfma_f32_16x16x32_bf16 v[24:27], v[124:127], v[92:95], v[24:27]\n" \
"s_waitcnt lgkmcnt(2)\nv_exp_f32 v40, v40\nv_mfma_f32_16x16x32_bf16 v[28:31], v[124:127], v[56:59], v[28:31]\nv_exp_f32 v41, v41\n" \
"v_mfma_f32_16x16x32_bf16 v[8:11], a[16:19], v[36:39], v[8:11]\nv_exp_f32 v42, v42\nv_mfma_f32_16x16x32_bf16 v[12:15], a[16:19], v[76:79], v[12:15]\nv_exp_f32 v43, v43\n" \
"v_mfma_f32_16x16x32_bf16 v[16:19], a[144:147], v[36:39], v[16:19]\nds_read_b128 v[56:59], v7 offset:6144\nv_fma_f32 v40, v40, v254, v254\nv_mfma_f32_16x16x32_bf16 v[20:23], a[144:147], v[76:79], v[20:23]\n" \
"ds_read_b128 v[92:95], v7 offset:38912\nv_fma_f32 v41, v41, v254, v254\nv_mfma_f32_16x16x32_bf16 v[24:27], v[128:131], v[36:39], v[24:27]\ns_waitcnt lgkmcnt(2)\n" \
"v_rcp_f32 v40, v40\nv_mfma_f32_16x16x32_bf16 v[28:31], v[128:131], v[76:79], v[28:31]\nv_rcp_f32 v41, v41\nv_mfma_f32_16x16x32_bf16 v[8:11], a[20:23], v[64:67], v[8:11]\n" \
"v_fma_f32 v42, v42, v254, v254\nv_fma_f32 v43, v43, v254, v254\nv_mfma_f32_16x16x32_bf16 v[12:15], a[20:23], v[84:87], v[12:15]\nv_rcp_f32 v42, v42\n" \
"v_mfma_f32_16x16x32_bf16 v[16:19], a[148:151], v[64:67], v[16:19]\nds_read_b128 v[36:39], v7 offset:7168\nv_mul_f32 v60, v52, v60\nv_mfma_f32_16x16x32_bf16 v[20:23], a[148:151], v[84:87], v[20:23]\n" \
"s_waitcnt vmcnt(1)\nv_max3_u32 v35, v72, v74, v68\nv_mul_f32 v61, v53, v61\nv_mfma_f32_16x16x32_bf16 v[24:27], v[132:135], v[64:67], v[24:27]\n" \
"ds_read_b128 v[76:79], v7 offset:39936\nv_max_u32 v35, v35, v70\nv_mfma_f32_16x16x32_bf16 v[28:31], v[132:135], v[84:87], v[28:31]\ns_waitcnt lgkmcnt(2)\n" \
"v_cmp_eq_u32 s[98:99], -1, v35\nv_mul_f32 v62, v54, v62\nv_mfma_f32_16x16x32_bf16 v[8:11], a[24:27], v[56:59], v[8:11]\nv_rcp_f32 v43, v43\n" \
"v_mfma_f32_16x16x32_bf16 v[12:15], a[24:27], v[92:95], v[12:15]\ns_cmp_eq_u64 s[98:99], 0\ns_cselect_b32 s14, s10, 0\nv_mul_f32 v63, v55, v63\n" \
"v_mfma_f32_16x16x32_bf16 v[16:19], a[152:155], v[56:59], v[16:19]\nbuffer_load_dwordx4 v0, s[12:15], s96 offen offset:0 sc1 lds\nv_mfma_f32_16x16x32_bf16 v[20:23], a[152:155], v[92:95], v[20:23]\nds_read_b128 v[52:55], v7 offset:8192\n" \
"v_mul_f32 v60, v60, v40\nv_mfma_f32_16x16x32_bf16 v[24:27], v[136:139], v[56:59], v[24:27]\nds_read_b128 v[64:67], v7 offset:40960\nv_mul_f32 v61, v61, v41\n" \
"v_mfma_f32_16x16x32_bf16 v[28:31], v[136:139], v[92:95], v[28:31]\ns_waitcnt lgkmcnt(2)\nv_mul_f32 v62, v62, v42\nv_mul_f32 v63, v63, v43\n" \
"v_mfma_f32_16x16x32_bf16 v[8:11], a[28:31], v[36:39], v[8:11]\nv_cvt_pk_bf16_f32 v60, v60, v61\nv_cvt_pk_bf16_f32 v61, v62, v63\nv_mfma_f32_16x16x32_bf16 v[12:15], a[28:31], v[76:79], v[12:15]\n" \
"buffer_load_dwordx4 v0, s[12:15], s96 offen offset:1024 sc1 lds\nv_mfma_f32_16x16x32_bf16 v[16:19], a[156:159], v[36:39], v[16:19]\nds_read_b128 v[40:43], v7 offset:9216\nv_mfma_f32_16x16x32_bf16 v[20:23], a[156:159], v[76:79], v[20:23]\n" \
"ds_read_b128 v[56:59], v7 offset:41984\nv_mfma_f32_16x16x32_bf16 v[24:27], v[140:143], v[36:39], v[24:27]\ns_waitcnt lgkmcnt(2)\nv_permlane16_swap_b32 v44, v48\n" \
"v_mfma_f32_16x16x32_bf16 v[28:31], v[140:143], v[76:79], v[28:31]\nbuffer_load_dwordx4 v0, s[12:15], s96 offen offset:2048 sc1 lds\nv_mfma_f32_16x16x32_bf16 v[8:11], a[32:35], v[52:55], v[8:11]\nv_permlane16_swap_b32 v45, v49\n" \
"v_mfma_f32_16x16x32_bf16 v[12:15], a[32:35], v[64:67], v[12:15]\nv_permlane16_swap_b32 v46, v50\nv_mfma_f32_16x16x32_bf16 v[16:19], a[160:163], v[52:55], v[16:19]\nds_read_b128 v[36:39], v7 offset:10240\n" \
"v_mul_f32 v76, v44, v44\nv_mfma_f32_16x16x32_bf16 v[20:23], a[160:163], v[64:67], v[20:23]\nds_read_b128 v[84:87], v7 offset:43008\nv_mul_f32 v77, v45, v45\n" \
"v_mfma_f32_16x16x32_bf16 v[24:27], v[144:147], v[52:55], v[24:27]\ns_waitcnt lgkmcnt(2)\nbuffer_load_dwordx4 v0, s[12:15], s96 offen offset:3072 sc1 lds\nv_mfma_f32_16x16x32_bf16 v[28:31], v[144:147], v[64:67], v[28:31]\n" \
"s_xor_b32 m0, s22, 0x1000\ns_xor_b32 s96, s96, 0x1000\nv_mul_f32 v78, v46, v46\nv_mfma_f32_16x16x32_bf16 v[8:11], a[36:39], v[40:43], v[8:11]\n" \
"v_permlane16_swap_b32 v47, v51\nv_mfma_f32_16x16x32_bf16 v[12:15], a[36:39], v[56:59], v[12:15]\nv_mul_f32 v79, v47, v47\nv_fma_f32 v76, v76, v253, v252\n" \
"v_mfma_f32_16x16x32_bf16 v[16:19], a[164:167], v[40:43], v[16:19]\nds_read_b128 v[52:55], v7 offset:11264\nv_fma_f32 v77, v77, v253, v252\nv_mfma_f32_16x16x32_bf16 v[20:23], a[164:167], v[56:59], v[20:23]\n" \
"buffer_load_dwordx4 v0, s[12:15], s96 offen offset:0 sc1 lds\nv_mfma_f32_16x16x32_bf16 v[24:27], v[148:151], v[40:43], v[24:27]\nds_read_b128 v[64:67], v7 offset:44032\nv_fma_f32 v78, v78, v253, v252\n" \
"v_mfma_f32_16x16x32_bf16 v[28:31], v[148:151], v[56:59], v[28:31]\ns_waitcnt lgkmcnt(2)\nv_fma_f32 v79, v79, v253, v252\nv_mul_f32 v76, v76, v44\n" \
"v_mfma_f32_16x16x32_bf16 v[8:11], a[40:43], v[36:39], v[8:11]\nv_exp_f32 v76, v76\nv_mfma_f32_16x16x32_bf16 v[12:15], a[40:43], v[84:87], v[12:15]\nv_mul_f32 v77, v77, v45\n" \
"v_mul_f32 v78, v78, v46\nv_mfma_f32_16x16x32_bf16 v[16:19], a[168:171], v[36:39], v[16:19]\nbuffer_load_dwordx4 v0, s[12:15], s96 offen offset:1024 sc1 lds\nv_mfma_f32_16x16x32_bf16 v[20:23], a[168:171], v[84:87], v[20:23]\n" \
"ds_read_b128 v[40:43], v7 offset:12288\nv_mul_f32 v79, v79, v47\nv_mfma_f32_16x16x32_bf16 v[24:27], v[152:155], v[36:39], v[24:27]\nds_read_b128 v[56:59], v7 offset:45056\n" \
"v_fma_f32 v76, v76, v254, v254\nv_mfma_f32_16x16x32_bf16 v[28:31], v[152:155], v[84:87], v[28:31]\ns_waitcnt lgkmcnt(2)\nv_exp_f32 v77, v77\n" \
"v_mfma_f32_16x16x32_bf16 v[8:11], a[44:47], v[52:55], v[8:11]\nv_exp_f32 v78, v78\nv_mfma_f32_16x16x32_bf16 v[12:15], a[44:47], v[64:67], v[12:15]\nbuffer_load_dwordx4 v0, s[12:15], s96 offen offset:2048 sc1 lds\n" \
"v_mfma_f32_16x16x32_bf16 v[16:19], a[172:175], v[52:55], v[16:19]\nds_read_b128 v[36:39], v7 offset:13312\nv_fma_f32 v77, v77, v254, v254\nv_mfma_f32_16x16x32_bf16 v[20:23], a[172:175], v[64:67], v[20:23]\n" \
"ds_read_b128 v[84:87], v7 offset:46080\nv_fma_f32 v78, v78, v254, v254\nv_mfma_f32_16x16x32_bf16 v[24:27], v[156:159], v[52:55], v[24:27]\ns_waitcnt lgkmcnt(2)\n" \
"v_exp_f32 v79, v79\nv_mfma_f32_16x16x32_bf16 v[28:31], v[156:159], v[64:67], v[28:31]\nv_rcp_f32 v76, v76\nv_mfma_f32_16x16x32_bf16 v[8:11], a[48:51], v[40:43], v[8:11]\n" \
"buffer_load_dwordx4 v0, s[12:15], s96 offen offset:3072 sc1 lds\nv_mfma_f32_16x16x32_bf16 v[12:15], a[48:51], v[56:59], v[12:15]\ns_xor_b32 m0, s22, 0x8000\ns_xor_b32 s96, s96, 0x9000\n" \
"v_fma_f32 v79, v79, v254, v254\nv_mfma_f32_16x16x32_bf16 v[16:19], a[176:179], v[40:43], v[16:19]\nds_read_b128 v[52:55], v7 offset:14336\nv_mul_f32 v48, v44, v48\n" \
"v_mfma_f32_16x16x32_bf16 v[20:23], a[176:179], v[56:59], v[20:23]\nds_read_b128 v[64:67], v7 offset:47104\nv_mul_f32 v49, v45, v49\nv_mfma_f32_16x16x32_bf16 v[24:27], v[160:163], v[40:43], v[24:27]\n" \
"s_waitcnt lgkmcnt(2)\nv_rcp_f32 v77, v77\nv_mfma_f32_16x16x32_bf16 v[28:31], v[160:163], v[56:59], v[28:31]\nbuffer_load_dwordx4 v0, s[12:15], s96 offen offset:0 sc1 lds\n" \
"v_mfma_f32_16x16x32_bf16 v[8:11], a[52:55], v[36:39], v[8:11]\nv_rcp_f32 v78, v78\nv_mfma_f32_16x16x32_bf16 v[12:15], a[52:55], v[84:87], v[12:15]\nv_rcp_f32 v79, v79\n" \
"v_mfma_f32_16x16x32_bf16 v[16:19], a[180:183], v[36:39], v[16:19]\nds_read_b128 v[40:43], v7 offset:15360\nv_mul_f32 v50, v46, v50\nv_mfma_f32_16x16x32_bf16 v[20:23], a[180:183], v[84:87], v[20:23]\n" \
"ds_read_b128 v[56:59], v7 offset:48128\nv_mul_f32 v51, v47, v51\nv_mfma_f32_16x16x32_bf16 v[24:27], v[164:167], v[36:39], v[24:27]\ns_waitcnt lgkmcnt(2)\n" \
"buffer_load_dwordx4 v0, s[12:15], s96 offen offset:1024 sc1 lds\nv_mfma_f32_16x16x32_bf16 v[28:31], v[164:167], v[84:87], v[28:31]\nv_mul_f32 v48, v48, v76\nv_mul_f32 v49, v49, v77\n" \
"v_mfma_f32_16x16x32_bf16 v[8:11], a[56:59], v[52:55], v[8:11]\nv_mul_f32 v50, v50, v78\nv_mul_f32 v51, v51, v79\nv_mfma_f32_16x16x32_bf16 v[12:15], a[56:59], v[64:67], v[12:15]\n" \
"v_cvt_pk_bf16_f32 v48, v48, v49\nv_cvt_pk_bf16_f32 v49, v50, v51\nv_mfma_f32_16x16x32_bf16 v[16:19], a[184:187], v[52:55], v[16:19]\nds_read_b128 v[36:39], v7 offset:16384\n" \
"v_mfma_f32_16x16x32_bf16 v[20:23], a[184:187], v[64:67], v[20:23]\nbuffer_load_dwordx4 v0, s[12:15], s96 offen offset:2048 sc1 lds\nv_mfma_f32_16x16x32_bf16 v[24:27], v[168:171], v[52:55], v[24:27]\ns_waitcnt lgkmcnt(1)\n" \
"ds_read_b128 v[44:47], v7 offset:49152\nv_mfma_f32_16x16x32_bf16 v[28:31], v[168:171], v[64:67], v[28:31]\nv_permlane16_swap_b32 v80, v88\nv_mfma_f32_16x16x32_bf16 v[8:11], a[60:63], v[40:43], v[8:11]\n" \
"v_permlane16_swap_b32 v81, v89\nv_mfma_f32_16x16x32_bf16 v[12:15], a[60:63], v[56:59], v[12:15]\nv_permlane16_swap_b32 v82, v90\nv_mfma_f32_16x16x32_bf16 v[16:19], a[188:191], v[40:43], v[16:19]\n" \
"buffer_load_dwordx4 v0, s[12:15], s96 offen offset:3072 sc1 lds\nv_mfma_f32_16x16x32_bf16 v[20:23], a[188:191], v[56:59], v[20:23]\nds_read_b128 v[52:55], v7 offset:17408\ns_xor_b32 m0, s22, 0x9000\n" \
"v_mfma_f32_16x16x32_bf16 v[24:27], v[172:175], v[40:43], v[24:27]\nds_read_b128 v[64:67], v7 offset:50176\ns_xor_b32 s96, s96, 0x1000\nv_mfma_f32_16x16x32_bf16 v[28:31], v[172:175], v[56:59], v[28:31]\n" \
"s_waitcnt lgkmcnt(2)\nv_permlane16_swap_b32 v83, v91\nv_mfma_f32_16x16x32_bf16 v[8:11], a[64:67], v[36:39], v[8:11]\ns_xor_b32 s22, s22, 0x10000\n" \
"v_mul_f32 v40, v80, v80\nv_mul_f32 v41, v81, v81\nv_mfma_f32_16x16x32_bf16 v[12:15], a[64:67], v[44:47], v[12:15]\nbuffer_load_dwordx4 v0, s[12:15], s96 offen offset:0 sc1 lds\n" \
"v_mfma_f32_16x16x32_bf16 v[16:19], a[192:195], v[36:39], v[16:19]\nds_read_b128 v[56:59], v7 offset:18432\nv_mul_f32 v42, v82, v82\nv_mfma_f32_16x16x32_bf16 v[20:23], a[192:195], v[44:47], v[20:23]\n" \
"ds_read_b128 v[76:79], v7 offset:51200\nv_mul_f32 v43, v83, v83\nv_mfma_f32_16x16x32_bf16 v[24:27], v[176:179], v[36:39], v[24:27]\ns_waitcnt lgkmcnt(2)\n" \
"v_fma_f32 v40, v40, v253, v252\nv_fma_f32 v41, v41, v253, v252\nv_mfma_f32_16x16x32_bf16 v[28:31], v[176:179], v[44:47], v[28:31]\nv_fma_f32 v42, v42, v253, v252\n" \
"v_fma_f32 v43, v43, v253, v252\nv_mfma_f32_16x16x32_bf16 v[8:11], a[68:71], v[52:55], v[8:11]\nbuffer_load_dwordx4 v0, s[12:15], s96 offen offset:1024 sc1 lds\nv_mfma_f32_16x16x32_bf16 v[12:15], a[68:71], v[64:67], v[12:15]\n" \
"v_mul_f32 v40, v40, v80\nv_mul_f32 v41, v41, v81\nv_mfma_f32_16x16x32_bf16 v[16:19], a[196:199], v[52:55], v[16:19]\nds_read_b128 v[36:39], v7 offset:19456\n" \
"v_mul_f32 v42, v42, v82\nv_mfma_f32_16x16x32_bf16 v[20:23], a[196:199], v[64:67], v[20:23]\nds_read_b128 v[44:47], v7 offset:52224\nv_mul_f32 v43, v43, v83\n" \
"v_mfma_f32_16x16x32_bf16 v[24:27], v[180:183], v[52:55], v[24:27]\ns_waitcnt lgkmcnt(2)\nv_exp_f32 v40, v40\nv_mfma_f32_16x16x32_bf16 v[28:31], v[180:183], v[64:67], v[28:31]\n" \
"buffer_load_dwordx4 v0, s[12:15], s96 offen offset:2048 sc1 lds\nv_mfma_f32_16x16x32_bf16 v[8:11], a[72:75], v[56:59], v[8:11]\nv_exp_f32 v41, v41\nv_mfma_f32_16x16x32_bf16 v[12:15], a[72:75], v[76:79], v[12:15]\n" \
"v_exp_f32 v42, v42\nv_mfma_f32_16x16x32_bf16 v[16:19], a[200:203], v[56:59], v[16:19]\nds_read_b128 v[52:55], v7 offset:20480\nv_fma_f32 v40, v40, v254, v254\n" \
"v_mfma_f32_16x16x32_bf16 v[20:23], a[200:203], v[76:79], v[20:23]\nds_read_b128 v[64:67], v7 offset:53248\nv_fma_f32 v41, v41, v254, v254\nv_mfma_f32_16x16x32_bf16 v[24:27], v[184:187], v[56:59], v[24:27]\n" \
"s_waitcnt lgkmcnt(2)\nbuffer_load_dwordx4 v0, s[12:15], s96 offen offset:3072 sc1 lds\nv_mfma_f32_16x16x32_bf16 v[28:31], v[184:187], v[76:79], v[28:31]\nv_exp_f32 v43, v43\n" \
"v_mfma_f32_16x16x32_bf16 v[8:11], a[76:79], v[36:39], v[8:11]\nv_rcp_f32 v40, v40\nv_mfma_f32_16x16x32_bf16 v[12:15], a[76:79], v[44:47], v[12:15]\nv_rcp_f32 v41, v41\n" \
"v_mfma_f32_16x16x32_bf16 v[16:19], a[204:207], v[36:39], v[16:19]\nds_read_b128 v[56:59], v7 offset:21504\nv_fma_f32 v42, v42, v254, v254\nv_mfma_f32_16x16x32_bf16 v[20:23], a[204:207], v[44:47], v[20:23]\n" \
"ds_read_b128 v[76:79], v7 offset:54272\nv_fma_f32 v43, v43, v254, v254\nv_mfma_f32_16x16x32_bf16 v[24:27], v[188:191], v[36:39], v[24:27]\ns_waitcnt lgkmcnt(2)\n" \
"v_rcp_f32 v42, v42\nv_mfma_f32_16x16x32_bf16 v[28:31], v[188:191], v[44:47], v[28:31]\nv_rcp_f32 v43, v43\nv_mfma_f32_16x16x32_bf16 v[8:11], a[80:83], v[52:55], v[8:11]\n" \
"buffer_store_dwordx2 v[60:61], v2, s[8:11], s16 offen offset:0 sc1\nv_mul_f32 v88, v80, v88\nv_mfma_f32_16x16x32_bf16 v[12:15], a[80:83], v[64:67], v[12:15]\nv_mul_f32 v89, v81, v89\n" \
"v_mul_f32 v90, v82, v90\nv_mfma_f32_16x16x32_bf16 v[16:19], a[208:211], v[52:55], v[16:19]\nbuffer_store_dwordx2 v[48:49], v2, s[8:11], s17 offen offset:256 sc1\nv_mul_f32 v91, v83, v91\n" \
"v_mfma_f32_16x16x32_bf16 v[20:23], a[208:211], v[64:67], v[20:23]\nv_mul_f32 v88, v88, v40\nv_mul_f32 v89, v89, v41\nv_mfma_f32_16x16x32_bf16 v[24:27], v[192:195], v[52:55], v[24:27]\n" \
"ds_read_b128 v[36:39], v7 offset:22528\nv_mul_f32 v90, v90, v42\nv_mul_f32 v91, v91, v43\nv_mfma_f32_16x16x32_bf16 v[28:31], v[192:195], v[64:67], v[28:31]\n" \
"s_waitcnt lgkmcnt(1)\nds_read_b128 v[40:43], v7 offset:55296\nv_cvt_pk_bf16_f32 v88, v88, v89\nv_mfma_f32_16x16x32_bf16 v[8:11], a[84:87], v[56:59], v[8:11]\n" \
"ds_read_b128 v[44:47], v4 offset:0\nv_cvt_pk_bf16_f32 v89, v90, v91\nv_mfma_f32_16x16x32_bf16 v[12:15], a[84:87], v[76:79], v[12:15]\nbuffer_store_dwordx2 v[88:89], v2, s[8:11], s17 offen offset:512 sc1\n" \
"v_add_f32 v72, v72, v68\nv_mfma_f32_16x16x32_bf16 v[16:19], a[212:215], v[56:59], v[16:19]\nds_read_b128 v[48:51], v7 offset:23552\nv_add_f32 v73, v73, v69\n" \
"v_mfma_f32_16x16x32_bf16 v[20:23], a[212:215], v[76:79], v[20:23]\nds_read_b128 v[52:55], v7 offset:56320\nv_add_f32 v74, v74, v70\nv_mfma_f32_16x16x32_bf16 v[24:27], v[196:199], v[56:59], v[24:27]\n" \
"ds_read_b128 v[60:63], v4 offset:512\nv_add_f32 v75, v75, v71\nv_mfma_f32_16x16x32_bf16 v[28:31], v[196:199], v[76:79], v[28:31]\ns_waitcnt lgkmcnt(4)\n" \
"v_permlane16_swap_b32 v72, v74\nv_mfma_f32_16x16x32_bf16 v[8:11], a[88:91], v[36:39], v[8:11]\nv_permlane16_swap_b32 v73, v75\nv_mfma_f32_16x16x32_bf16 v[12:15], a[88:91], v[40:43], v[12:15]\n" \
"s_waitcnt lgkmcnt(3)\nv_add_f32 v44, v44, v45\nv_add_f32 v46, v46, v47\nv_mfma_f32_16x16x32_bf16 v[16:19], a[216:219], v[36:39], v[16:19]\n" \
"ds_read_b128 v[56:59], v7 offset:24576\nv_add_f32 v44, v44, v46\nv_mfma_f32_16x16x32_bf16 v[20:23], a[216:219], v[40:43], v[20:23]\nds_read_b128 v[64:67], v7 offset:57344\n" \
"v_mul_f32 v44, 0xba800000, v44\nv_mfma_f32_16x16x32_bf16 v[24:27], v[200:203], v[36:39], v[24:27]\ns_waitcnt lgkmcnt(2)\nv_add_f32 v72, v72, v74\n" \
"v_add_f32 v73, v73, v75\nv_mfma_f32_16x16x32_bf16 v[28:31], v[200:203], v[40:43], v[28:31]\nv_add_f32 v60, v60, v61\nv_add_f32 v62, v62, v63\n" \
"v_mfma_f32_16x16x32_bf16 v[8:11], a[92:95], v[48:51], v[8:11]\nv_add_f32 v60, v60, v62\nv_mul_f32 v61, v44, -v44\nv_mfma_f32_16x16x32_bf16 v[12:15], a[92:95], v[52:55], v[12:15]\n" \
"v_fmamk_f32 v35, v60, 0x3a800000, v61\nv_cvt_pk_bf16_f32 v35, v35, v35\nv_mfma_f32_16x16x32_bf16 v[16:19], a[220:223], v[48:51], v[16:19]\nds_read_b128 v[60:63], v7 offset:25600\n" \
"v_lshlrev_b32 v35, 16, v35\nv_mfma_f32_16x16x32_bf16 v[20:23], a[220:223], v[52:55], v[20:23]\nds_read_b128 v[68:71], v7 offset:58368\nv_max_f32 v35, 0, v35\n" \
"v_mfma_f32_16x16x32_bf16 v[24:27], v[204:207], v[48:51], v[24:27]\ns_waitcnt lgkmcnt(2)\nv_add_f32 v35, 0x3727c5ac, v35\nv_cvt_pk_bf16_f32 v111, v35, v44\n" \
"v_mfma_f32_16x16x32_bf16 v[28:31], v[204:207], v[52:55], v[28:31]\nv_permlane32_swap_b32 v72, v73\nv_mfma_f32_16x16x32_bf16 v[8:11], a[96:99], v[56:59], v[8:11]\nv_lshlrev_b32 v35, 16, v111\n" \
"v_add_f32 v72, v72, v73\nv_mfma_f32_16x16x32_bf16 v[12:15], a[96:99], v[64:67], v[12:15]\nv_sqrt_f32 v35, v35\nv_mfma_f32_16x16x32_bf16 v[16:19], a[224:227], v[56:59], v[16:19]\n" \
"ds_read_b128 v[36:39], v7 offset:26624\nv_cvt_pk_bf16_f32 v35, v35, v35\nv_mfma_f32_16x16x32_bf16 v[20:23], a[224:227], v[64:67], v[20:23]\nds_read_b128 v[40:43], v7 offset:59392\n" \
"v_lshlrev_b32 v35, 16, v35\nv_mfma_f32_16x16x32_bf16 v[24:27], v[208:211], v[56:59], v[24:27]\ns_waitcnt lgkmcnt(2)\nv_rcp_f32 v34, v35\n" \
"v_mfma_f32_16x16x32_bf16 v[28:31], v[208:211], v[64:67], v[28:31]\nds_write_b32 v5, v72 offset:0\nv_mfma_f32_16x16x32_bf16 v[8:11], a[100:103], v[60:63], v[8:11]\nv_and_b32 v111, 0xffff0000, v111\n" \
"v_mfma_f32_16x16x32_bf16 v[12:15], a[100:103], v[68:71], v[12:15]\nv_mfma_f32_16x16x32_bf16 v[16:19], a[228:231], v[60:63], v[16:19]\nds_read_b128 v[44:47], v7 offset:27648\nv_mfma_f32_16x16x32_bf16 v[20:23], a[228:231], v[68:71], v[20:23]\n" \
"ds_read_b128 v[48:51], v7 offset:60416\nv_mfma_f32_16x16x32_bf16 v[24:27], v[212:215], v[60:63], v[24:27]\nv_mfma_f32_16x16x32_bf16 v[28:31], v[212:215], v[68:71], v[28:31]\ns_branch 1b\n" \
"4:\ns_barrier\ns_lshr_b32 s94, s20, 1\nv_readlane_b32 s94, v6, s94\n" \
"s_and_b32 s95, s20, 1\ns_lshl_b32 s95, s95, 3\ns_lshr_b32 s94, s94, s95\ns_and_b32 s94, s94, 0x7f\n" \
"s_lshl_b32 s95, s94, 13\nbuffer_load_dwordx4 v[92:95], v1, s[8:11], s95 offen offset:0 sc1\nbuffer_load_dwordx4 v[96:99], v1, s[8:11], s95 offen offset:1024 sc1\nv_fma_f32 v80, v32, v240, v8\n" \
"v_fma_f32 v81, v32, v241, v9\nv_fma_f32 v82, v32, v242, v10\nv_fma_f32 v83, v32, v243, v11\nv_fma_f32 v84, v33, v240, v12\n" \
"v_fma_f32 v85, v33, v241, v13\nv_fma_f32 v86, v33, v242, v14\nv_fma_f32 v87, v33, v243, v15\nv_permlane16_swap_b32 v80, v84\n" \
"v_permlane16_swap_b32 v81, v85\nv_permlane16_swap_b32 v82, v86\nv_permlane16_swap_b32 v83, v87\nv_mul_f32 v88, v80, v80\n" \
"v_mul_f32 v89, v81, v81\nv_mul_f32 v90, v82, v82\nv_mul_f32 v91, v83, v83\nv_fma_f32 v88, v88, v253, v252\n" \
"v_fma_f32 v89, v89, v253, v252\nv_fma_f32 v90, v90, v253, v252\nv_fma_f32 v91, v91, v253, v252\nv_mul_f32 v88, v88, v80\n" \
"v_mul_f32 v89, v89, v81\nv_mul_f32 v90, v90, v82\nv_mul_f32 v91, v91, v83\nv_mul_f32 v84, v80, v84\n" \
"v_mul_f32 v85, v81, v85\nv_mul_f32 v86, v82, v86\nv_mul_f32 v87, v83, v87\nv_exp_f32 v88, v88\n" \
"v_exp_f32 v89, v89\nv_exp_f32 v90, v90\nv_exp_f32 v91, v91\nv_fma_f32 v88, v88, v254, v254\n" \
"v_fma_f32 v89, v89, v254, v254\nv_fma_f32 v90, v90, v254, v254\nv_fma_f32 v91, v91, v254, v254\nv_rcp_f32 v88, v88\n" \
"v_rcp_f32 v89, v89\nv_rcp_f32 v90, v90\nv_rcp_f32 v91, v91\nv_mul_f32 v84, v84, v88\n" \
"v_mul_f32 v85, v85, v89\nv_mul_f32 v86, v86, v90\nv_mul_f32 v87, v87, v91\nv_cvt_pk_bf16_f32 v84, v84, v85\n" \
"v_cvt_pk_bf16_f32 v85, v86, v87\nbuffer_store_dwordx2 v[84:85], v2, s[8:11], s16 offen offset:0 sc1\nv_fma_f32 v80, v32, v244, v16\nv_fma_f32 v81, v32, v245, v17\n" \
"v_fma_f32 v82, v32, v246, v18\nv_fma_f32 v83, v32, v247, v19\nv_fma_f32 v84, v33, v244, v20\nv_fma_f32 v85, v33, v245, v21\n" \
"v_fma_f32 v86, v33, v246, v22\nv_fma_f32 v87, v33, v247, v23\nv_permlane16_swap_b32 v80, v84\nv_permlane16_swap_b32 v81, v85\n" \
"v_permlane16_swap_b32 v82, v86\nv_permlane16_swap_b32 v83, v87\nv_mul_f32 v88, v80, v80\nv_mul_f32 v89, v81, v81\n" \
"v_mul_f32 v90, v82, v82\nv_mul_f32 v91, v83, v83\nv_fma_f32 v88, v88, v253, v252\nv_fma_f32 v89, v89, v253, v252\n" \
"v_fma_f32 v90, v90, v253, v252\nv_fma_f32 v91, v91, v253, v252\nv_mul_f32 v88, v88, v80\nv_mul_f32 v89, v89, v81\n" \
"v_mul_f32 v90, v90, v82\nv_mul_f32 v91, v91, v83\nv_mul_f32 v84, v80, v84\nv_mul_f32 v85, v81, v85\n" \
"v_mul_f32 v86, v82, v86\nv_mul_f32 v87, v83, v87\nv_exp_f32 v88, v88\nv_exp_f32 v89, v89\n" \
"v_exp_f32 v90, v90\nv_exp_f32 v91, v91\nv_fma_f32 v88, v88, v254, v254\nv_fma_f32 v89, v89, v254, v254\n" \
"v_fma_f32 v90, v90, v254, v254\nv_fma_f32 v91, v91, v254, v254\nv_rcp_f32 v88, v88\nv_rcp_f32 v89, v89\n" \
"v_rcp_f32 v90, v90\nv_rcp_f32 v91, v91\nv_mul_f32 v84, v84, v88\nv_mul_f32 v85, v85, v89\n" \
"v_mul_f32 v86, v86, v90\nv_mul_f32 v87, v87, v91\nv_cvt_pk_bf16_f32 v84, v84, v85\nv_cvt_pk_bf16_f32 v85, v86, v87\n" \
"buffer_store_dwordx2 v[84:85], v2, s[8:11], s17 offen offset:256 sc1\nv_fma_f32 v80, v32, v248, v24\nv_fma_f32 v81, v32, v249, v25\nv_fma_f32 v82, v32, v250, v26\n" \
"v_fma_f32 v83, v32, v251, v27\nv_fma_f32 v84, v33, v248, v28\nv_fma_f32 v85, v33, v249, v29\nv_fma_f32 v86, v33, v250, v30\n" \
"v_fma_f32 v87, v33, v251, v31\nv_permlane16_swap_b32 v80, v84\nv_permlane16_swap_b32 v81, v85\nv_permlane16_swap_b32 v82, v86\n" \
"v_permlane16_swap_b32 v83, v87\nv_mul_f32 v88, v80, v80\nv_mul_f32 v89, v81, v81\nv_mul_f32 v90, v82, v82\n" \
"v_mul_f32 v91, v83, v83\nv_fma_f32 v88, v88, v253, v252\nv_fma_f32 v89, v89, v253, v252\nv_fma_f32 v90, v90, v253, v252\n" \
"v_fma_f32 v91, v91, v253, v252\nv_mul_f32 v88, v88, v80\nv_mul_f32 v89, v89, v81\nv_mul_f32 v90, v90, v82\n" \
"v_mul_f32 v91, v91, v83\nv_mul_f32 v84, v80, v84\nv_mul_f32 v85, v81, v85\nv_mul_f32 v86, v82, v86\n" \
"v_mul_f32 v87, v83, v87\nv_exp_f32 v88, v88\nv_exp_f32 v89, v89\nv_exp_f32 v90, v90\n" \
"v_exp_f32 v91, v91\nv_fma_f32 v88, v88, v254, v254\nv_fma_f32 v89, v89, v254, v254\nv_fma_f32 v90, v90, v254, v254\n" \
"v_fma_f32 v91, v91, v254, v254\nv_rcp_f32 v88, v88\nv_rcp_f32 v89, v89\nv_rcp_f32 v90, v90\n" \
"v_rcp_f32 v91, v91\nv_mul_f32 v84, v84, v88\nv_mul_f32 v85, v85, v89\nv_mul_f32 v86, v86, v90\n" \
"v_mul_f32 v87, v87, v91\nv_cvt_pk_bf16_f32 v84, v84, v85\nv_cvt_pk_bf16_f32 v85, v86, v87\nbuffer_store_dwordx2 v[84:85], v2, s[8:11], s17 offen offset:512 sc1\n" \
"s_mov_b32 s16, 0x80000000\ns_mov_b32 s17, 0x80000000\nv_mov_b32 v80, s20\ns_waitcnt vmcnt(3)\n" \
"v_max3_u32 v81, v92, v94, v96\nv_max_u32 v81, v81, v98\nv_cmp_eq_u32 vcc, -1, v81\ns_cbranch_vccz 8f\n" \
"6:\ns_waitcnt vmcnt(0)\nbuffer_store_dword v80, v3, s[8:11], 0 offen offset:0 sc1\ns_mov_b32 s23, 0x40000000\n" \
"7:\ns_add_u32 s24, s24, 1\ns_cmp_lt_u32 s24, 0x40000\ns_cbranch_scc0 9f\n" \
"buffer_load_dwordx4 v[92:95], v1, s[8:11], s95 offen offset:0 sc1\nbuffer_load_dwordx4 v[96:99], v1, s[8:11], s95 offen offset:1024 sc1\ns_waitcnt vmcnt(0)\nv_max3_u32 v81, v92, v94, v96\n" \
"v_max_u32 v81, v81, v98\nv_cmp_eq_u32 vcc, -1, v81\ns_cbranch_vccz 8f\ns_sleep 8\n" \
"s_branch 7b\n8:\ns_cmp_ge_u32 s20, s26\ns_cbranch_scc1 10f\n" \
"s_lshl_b32 s94, s94, 16\ns_add_u32 s94, s94, s21\ns_xor_b32 s95, s22, 0x10000\ns_xor_b32 m0, s95, 0\n" \
"s_xor_b32 s94, s94, 0\nbuffer_load_dwordx4 v0, s[8:11], s94 offen offset:0 sc1 lds\nbuffer_load_dwordx4 v0, s[8:11], s94 offen offset:1024 sc1 lds\nbuffer_load_dwordx4 v0, s[8:11], s94 offen offset:2048 sc1 lds\n" \
"buffer_load_dwordx4 v0, s[8:11], s94 offen offset:3072 sc1 lds\ns_xor_b32 m0, s95, 0x1000\ns_xor_b32 s94, s94, 0x1000\nbuffer_load_dwordx4 v0, s[8:11], s94 offen offset:0 sc1 lds\n" \
"buffer_load_dwordx4 v0, s[8:11], s94 offen offset:1024 sc1 lds\nbuffer_load_dwordx4 v0, s[8:11], s94 offen offset:2048 sc1 lds\nbuffer_load_dwordx4 v0, s[8:11], s94 offen offset:3072 sc1 lds\ns_xor_b32 m0, s95, 0x8000\n" \
"s_xor_b32 s94, s94, 0x9000\nbuffer_load_dwordx4 v0, s[8:11], s94 offen offset:0 sc1 lds\nbuffer_load_dwordx4 v0, s[8:11], s94 offen offset:1024 sc1 lds\nbuffer_load_dwordx4 v0, s[8:11], s94 offen offset:2048 sc1 lds\n" \
"buffer_load_dwordx4 v0, s[8:11], s94 offen offset:3072 sc1 lds\ns_xor_b32 m0, s95, 0x9000\ns_xor_b32 s94, s94, 0x1000\nbuffer_load_dwordx4 v0, s[8:11], s94 offen offset:0 sc1 lds\n" \
"buffer_load_dwordx4 v0, s[8:11], s94 offen offset:1024 sc1 lds\nbuffer_load_dwordx4 v0, s[8:11], s94 offen offset:2048 sc1 lds\nbuffer_load_dwordx4 v0, s[8:11], s94 offen offset:3072 sc1 lds\nv_add_f32 v92, v92, v96\n" \
"v_add_f32 v93, v93, v97\nv_add_f32 v94, v94, v98\nv_add_f32 v95, v95, v99\ns_nop 1\n" \
"v_permlane16_swap_b32 v92, v94\nv_permlane16_swap_b32 v93, v95\nv_add_f32 v92, v92, v94\nv_add_f32 v93, v93, v95\n" \
"s_nop 1\nv_permlane32_swap_b32 v92, v93\nv_add_f32 v92, v92, v93\nds_write_b32 v5, v92\n" \
"s_add_u32 s97, s20, 1\ns_lshr_b32 s96, s97, 1\nv_readlane_b32 s96, v6, s96\ns_and_b32 s97, s97, 1\n" \
"s_lshl_b32 s97, s97, 3\ns_lshr_b32 s96, s96, s97\ns_and_b32 s96, s96, 0x7f\ns_lshl_b32 s97, s96, 13\n" \
"s_lshl_b32 s96, s96, 16\ns_add_u32 s96, s96, s21\nbuffer_load_dwordx4 v[72:75], v1, s[8:11], s97 offen offset:0 sc1\nbuffer_load_dwordx4 v[68:71], v1, s[8:11], s97 offen offset:1024 sc1\n" \
"s_waitcnt vmcnt(18)\nbuffer_store_dword v80, v3, s[8:11], s23 offen offset:0 sc1\ns_mov_b32 s23, 0x40000000\ns_waitcnt vmcnt(3) lgkmcnt(0)\n" \
"s_barrier\nds_read_b128 v[56:59], v7 offset:0\nds_read_b128 v[36:39], v7 offset:32768\nds_read_b128 v[40:43], v7 offset:1024\n" \
"ds_read_b128 v[76:79], v7 offset:33792\ns_waitcnt lgkmcnt(0)\ns_branch 5b\n10:\n" \
"s_mul_i32 s16, s94, 0x38000\ns_lshl_b32 s94, s94, 16\ns_and_b32 s95, s21, 0xffff6fff\ns_add_u32 s94, s94, s95\n" \
"s_lshl_b32 s95, s27, 15\ns_add_u32 s94, s94, s95\ns_xor_b32 s95, s22, 0x10000\ns_and_b32 s95, s95, 0xffff6fff\n" \
"s_add_u32 m0, s95, 0\ns_add_u32 s94, s94, 0\nbuffer_load_dwordx4 v0, s[8:11], s94 offen offset:0 sc1 lds\nbuffer_load_dwordx4 v0, s[8:11], s94 offen offset:1024 sc1 lds\n" \
"buffer_load_dwordx4 v0, s[8:11], s94 offen offset:2048 sc1 lds\nbuffer_load_dwordx4 v0, s[8:11], s94 offen offset:3072 sc1 lds\ns_add_u32 m0, s95, 0x1000\ns_add_u32 s94, s94, 0x1000\n" \
"buffer_load_dwordx4 v0, s[8:11], s94 offen offset:0 sc1 lds\nbuffer_load_dwordx4 v0, s[8:11], s94 offen offset:1024 sc1 lds\nbuffer_load_dwordx4 v0, s[8:11], s94 offen offset:2048 sc1 lds\nbuffer_load_dwordx4 v0, s[8:11], s94 offen offset:3072 sc1 lds\n" \
"v_add_f32 v92, v92, v96\nv_add_f32 v93, v93, v97\nv_add_f32 v94, v94, v98\nv_add_f32 v95, v95, v99\n" \
"s_nop 1\nv_permlane16_swap_b32 v92, v94\nv_permlane16_swap_b32 v93, v95\nv_add_f32 v92, v92, v94\n" \
"v_add_f32 v93, v93, v95\ns_nop 1\nv_permlane32_swap_b32 v92, v93\nv_add_f32 v92, v92, v93\n" \
"ds_write_b32 v5, v92\ns_waitcnt vmcnt(8)\nbuffer_store_dword v80, v3, s[8:11], s23 offen offset:0 sc1\ns_mov_b32 s23, 0x40000000\n" \
"s_lshl_b32 s94, s27, 7\nv_and_b32 v4, 0xffffff7f, v4\nv_or_b32 v4, s94, v4\ns_mul_i32 s94, s27, 0x1c000\n" \
"s_add_u32 s94, s94, s16\ns_or_b32 s16, s94, s18\ns_or_b32 s17, s94, s19\nv_lshlrev_b32 v81, 23, v0\n" \
"v_and_b32 v81, 0x80000000, v81\nv_or_b32 v2, v2, v81\ns_add_u32 s20, s20, 1\nv_mov_b32 v80, s20\n" \
"s_waitcnt vmcnt(1) lgkmcnt(0)\ns_barrier\nds_read_b128 v[60:63], v4 offset:0\nds_read_b128 v[64:67], v4 offset:512\n" \
"ds_read_b128 v[36:39], v7 offset:0\nds_read_b128 v[40:43], v7 offset:1024\nds_read_b128 v[44:47], v7 offset:2048\nds_read_b128 v[48:51], v7 offset:3072\n" \
"ds_read_b128 v[52:55], v7 offset:4096\nds_read_b128 v[56:59], v7 offset:5120\ns_waitcnt lgkmcnt(5)\nv_mfma_f32_16x16x32_bf16 v[8:11], a[0:3], v[36:39], 0\n" \
"v_add_f32 v60, v60, v61\nv_mfma_f32_16x16x32_bf16 v[16:19], a[128:131], v[36:39], 0\nv_add_f32 v62, v62, v63\nv_mfma_f32_16x16x32_bf16 v[24:27], v[112:115], v[36:39], 0\n" \
"v_add_f32 v60, v60, v62\nds_read_b128 v[36:39], v7 offset:6144\ns_waitcnt lgkmcnt(5)\nv_mfma_f32_16x16x32_bf16 v[8:11], a[4:7], v[40:43], v[8:11]\n" \
"v_add_f32 v64, v64, v65\nv_mfma_f32_16x16x32_bf16 v[16:19], a[132:135], v[40:43], v[16:19]\nv_add_f32 v66, v66, v67\nv_mfma_f32_16x16x32_bf16 v[24:27], v[116:119], v[40:43], v[24:27]\n" \
"v_add_f32 v64, v64, v66\nds_read_b128 v[40:43], v7 offset:7168\ns_waitcnt lgkmcnt(5)\nv_mfma_f32_16x16x32_bf16 v[8:11], a[8:11], v[44:47], v[8:11]\n" \
"v_mul_f32 v60, 0xba800000, v60\nv_mfma_f32_16x16x32_bf16 v[16:19], a[136:139], v[44:47], v[16:19]\nv_mul_f32 v65, v60, -v60\nv_mfma_f32_16x16x32_bf16 v[24:27], v[120:123], v[44:47], v[24:27]\n" \
"v_fmamk_f32 v68, v64, 0x3a800000, v65\nds_read_b128 v[44:47], v7 offset:8192\ns_waitcnt lgkmcnt(5)\nv_mfma_f32_16x16x32_bf16 v[8:11], a[12:15], v[48:51], v[8:11]\n" \
"v_cvt_pk_bf16_f32 v68, v68, v68\nv_mfma_f32_16x16x32_bf16 v[16:19], a[140:143], v[48:51], v[16:19]\nv_lshlrev_b32 v68, 16, v68\nv_mfma_f32_16x16x32_bf16 v[24:27], v[124:127], v[48:51], v[24:27]\n" \
"v_max_f32 v68, 0, v68\nds_read_b128 v[48:51], v7 offset:9216\ns_waitcnt lgkmcnt(5)\nv_mfma_f32_16x16x32_bf16 v[8:11], a[16:19], v[52:55], v[8:11]\n" \
"v_add_f32 v68, 0x3727c5ac, v68\nv_mfma_f32_16x16x32_bf16 v[16:19], a[144:147], v[52:55], v[16:19]\nv_cvt_pk_bf16_f32 v70, v68, v60\nv_mfma_f32_16x16x32_bf16 v[24:27], v[128:131], v[52:55], v[24:27]\n" \
"v_lshlrev_b32 v68, 16, v70\nds_read_b128 v[52:55], v7 offset:10240\ns_waitcnt lgkmcnt(5)\nv_mfma_f32_16x16x32_bf16 v[8:11], a[20:23], v[56:59], v[8:11]\n" \
"v_sqrt_f32 v68, v68\nv_mfma_f32_16x16x32_bf16 v[16:19], a[148:151], v[56:59], v[16:19]\nv_cvt_pk_bf16_f32 v68, v68, v68\nv_mfma_f32_16x16x32_bf16 v[24:27], v[132:135], v[56:59], v[24:27]\n" \
"v_lshlrev_b32 v68, 16, v68\nds_read_b128 v[56:59], v7 offset:11264\ns_waitcnt lgkmcnt(5)\nv_mfma_f32_16x16x32_bf16 v[8:11], a[24:27], v[36:39], v[8:11]\n" \
"v_and_b32 v70, 0xffff0000, v70\nv_mfma_f32_16x16x32_bf16 v[16:19], a[152:155], v[36:39], v[16:19]\nv_rcp_f32 v69, v68\nv_mfma_f32_16x16x32_bf16 v[24:27], v[136:139], v[36:39], v[24:27]\n" \
"v_mul_f32 v254, v68, v68\nds_read_b128 v[36:39], v7 offset:12288\ns_waitcnt lgkmcnt(5)\nv_mfma_f32_16x16x32_bf16 v[8:11], a[28:31], v[40:43], v[8:11]\n" \
"v_mul_f32 v252, 0xc0135761, v69\nv_mfma_f32_16x16x32_bf16 v[16:19], a[156:159], v[40:43], v[16:19]\nv_mul_f32 v69, v69, v69\nv_mfma_f32_16x16x32_bf16 v[24:27], v[140:143], v[40:43], v[24:27]\n" \
"v_mul_f32 v69, v69, v252\nds_read_b128 v[40:43], v7 offset:13312\ns_waitcnt lgkmcnt(5)\nv_mfma_f32_16x16x32_bf16 v[8:11], a[32:35], v[44:47], v[8:11]\n" \
"v_mul_f32 v253, 0x3d372713, v69\nv_mfma_f32_16x16x32_bf16 v[16:19], a[160:163], v[44:47], v[16:19]\nv_mov_b32 v32, v70\nv_mfma_f32_16x16x32_bf16 v[24:27], v[144:147], v[44:47], v[24:27]\n" \
"v_mov_b32 v33, v70\nds_read_b128 v[44:47], v7 offset:14336\ns_waitcnt lgkmcnt(5)\nv_mfma_f32_16x16x32_bf16 v[8:11], a[36:39], v[48:51], v[8:11]\n" \
"v_permlane16_swap_b32 v32, v33\nv_mfma_f32_16x16x32_bf16 v[16:19], a[164:167], v[48:51], v[16:19]\nv_mfma_f32_16x16x32_bf16 v[24:27], v[148:151], v[48:51], v[24:27]\nds_read_b128 v[48:51], v7 offset:15360\n" \
"s_waitcnt lgkmcnt(5)\nv_mfma_f32_16x16x32_bf16 v[8:11], a[40:43], v[52:55], v[8:11]\nv_mfma_f32_16x16x32_bf16 v[16:19], a[168:171], v[52:55], v[16:19]\nv_mfma_f32_16x16x32_bf16 v[24:27], v[152:155], v[52:55], v[24:27]\n" \
"ds_read_b128 v[52:55], v7 offset:16384\ns_waitcnt lgkmcnt(5)\nv_mfma_f32_16x16x32_bf16 v[8:11], a[44:47], v[56:59], v[8:11]\nv_mfma_f32_16x16x32_bf16 v[16:19], a[172:175], v[56:59], v[16:19]\n" \
"v_mfma_f32_16x16x32_bf16 v[24:27], v[156:159], v[56:59], v[24:27]\nds_read_b128 v[56:59], v7 offset:17408\ns_waitcnt lgkmcnt(5)\nv_mfma_f32_16x16x32_bf16 v[8:11], a[48:51], v[36:39], v[8:11]\n" \
"v_mfma_f32_16x16x32_bf16 v[16:19], a[176:179], v[36:39], v[16:19]\nv_mfma_f32_16x16x32_bf16 v[24:27], v[160:163], v[36:39], v[24:27]\nds_read_b128 v[36:39], v7 offset:18432\ns_waitcnt lgkmcnt(5)\n" \
"v_mfma_f32_16x16x32_bf16 v[8:11], a[52:55], v[40:43], v[8:11]\nv_mfma_f32_16x16x32_bf16 v[16:19], a[180:183], v[40:43], v[16:19]\nv_mfma_f32_16x16x32_bf16 v[24:27], v[164:167], v[40:43], v[24:27]\nds_read_b128 v[40:43], v7 offset:19456\n" \
"s_waitcnt lgkmcnt(5)\nv_mfma_f32_16x16x32_bf16 v[8:11], a[56:59], v[44:47], v[8:11]\nv_mfma_f32_16x16x32_bf16 v[16:19], a[184:187], v[44:47], v[16:19]\nv_mfma_f32_16x16x32_bf16 v[24:27], v[168:171], v[44:47], v[24:27]\n" \
"ds_read_b128 v[44:47], v7 offset:20480\ns_waitcnt lgkmcnt(5)\nv_mfma_f32_16x16x32_bf16 v[8:11], a[60:63], v[48:51], v[8:11]\nv_mfma_f32_16x16x32_bf16 v[16:19], a[188:191], v[48:51], v[16:19]\n" \
"v_mfma_f32_16x16x32_bf16 v[24:27], v[172:175], v[48:51], v[24:27]\nds_read_b128 v[48:51], v7 offset:21504\ns_waitcnt lgkmcnt(5)\nv_mfma_f32_16x16x32_bf16 v[8:11], a[64:67], v[52:55], v[8:11]\n" \
"v_mfma_f32_16x16x32_bf16 v[16:19], a[192:195], v[52:55], v[16:19]\nv_mfma_f32_16x16x32_bf16 v[24:27], v[176:179], v[52:55], v[24:27]\nds_read_b128 v[52:55], v7 offset:22528\ns_waitcnt lgkmcnt(5)\n" \
"v_mfma_f32_16x16x32_bf16 v[8:11], a[68:71], v[56:59], v[8:11]\nv_mfma_f32_16x16x32_bf16 v[16:19], a[196:199], v[56:59], v[16:19]\nv_mfma_f32_16x16x32_bf16 v[24:27], v[180:183], v[56:59], v[24:27]\nds_read_b128 v[56:59], v7 offset:23552\n" \
"s_waitcnt lgkmcnt(5)\nv_mfma_f32_16x16x32_bf16 v[8:11], a[72:75], v[36:39], v[8:11]\nv_mfma_f32_16x16x32_bf16 v[16:19], a[200:203], v[36:39], v[16:19]\nv_mfma_f32_16x16x32_bf16 v[24:27], v[184:187], v[36:39], v[24:27]\n" \
"ds_read_b128 v[36:39], v7 offset:24576\ns_waitcnt lgkmcnt(5)\nv_mfma_f32_16x16x32_bf16 v[8:11], a[76:79], v[40:43], v[8:11]\nv_mfma_f32_16x16x32_bf16 v[16:19], a[204:207], v[40:43], v[16:19]\n" \
"v_mfma_f32_16x16x32_bf16 v[24:27], v[188:191], v[40:43], v[24:27]\nds_read_b128 v[40:43], v7 offset:25600\ns_waitcnt lgkmcnt(5)\nv_mfma_f32_16x16x32_bf16 v[8:11], a[80:83], v[44:47], v[8:11]\n" \
"v_mfma_f32_16x16x32_bf16 v[16:19], a[208:211], v[44:47], v[16:19]\nv_mfma_f32_16x16x32_bf16 v[24:27], v[192:195], v[44:47], v[24:27]\nds_read_b128 v[44:47], v7 offset:26624\ns_waitcnt lgkmcnt(5)\n" \
"v_mfma_f32_16x16x32_bf16 v[8:11], a[84:87], v[48:51], v[8:11]\nv_mfma_f32_16x16x32_bf16 v[16:19], a[212:215], v[48:51], v[16:19]\nv_mfma_f32_16x16x32_bf16 v[24:27], v[196:199], v[48:51], v[24:27]\nds_read_b128 v[48:51], v7 offset:27648\n" \
"s_waitcnt lgkmcnt(5)\nv_mfma_f32_16x16x32_bf16 v[8:11], a[88:91], v[52:55], v[8:11]\nv_mfma_f32_16x16x32_bf16 v[16:19], a[216:219], v[52:55], v[16:19]\nv_mfma_f32_16x16x32_bf16 v[24:27], v[200:203], v[52:55], v[24:27]\n" \
"ds_read_b128 v[52:55], v7 offset:28672\ns_waitcnt lgkmcnt(5)\nv_mfma_f32_16x16x32_bf16 v[8:11], a[92:95], v[56:59], v[8:11]\nv_mfma_f32_16x16x32_bf16 v[16:19], a[220:223], v[56:59], v[16:19]\n" \
"v_mfma_f32_16x16x32_bf16 v[24:27], v[204:207], v[56:59], v[24:27]\nds_read_b128 v[56:59], v7 offset:29696\ns_waitcnt lgkmcnt(5)\nv_mfma_f32_16x16x32_bf16 v[8:11], a[96:99], v[36:39], v[8:11]\n" \
"v_mfma_f32_16x16x32_bf16 v[16:19], a[224:227], v[36:39], v[16:19]\nv_mfma_f32_16x16x32_bf16 v[24:27], v[208:211], v[36:39], v[24:27]\nds_read_b128 v[36:39], v7 offset:30720\ns_waitcnt lgkmcnt(5)\n" \
"v_mfma_f32_16x16x32_bf16 v[8:11], a[100:103], v[40:43], v[8:11]\nv_mfma_f32_16x16x32_bf16 v[16:19], a[228:231], v[40:43], v[16:19]\nv_mfma_f32_16x16x32_bf16 v[24:27], v[212:215], v[40:43], v[24:27]\nds_read_b128 v[40:43], v7 offset:31744\n" \
"s_waitcnt lgkmcnt(5)\nv_mfma_f32_16x16x32_bf16 v[8:11], a[104:107], v[44:47], v[8:11]\nv_mfma_f32_16x16x32_bf16 v[16:19], a[232:235], v[44:47], v[16:19]\nv_mfma_f32_16x16x32_bf16 v[24:27], v[216:219], v[44:47], v[24:27]\n" \
"s_waitcnt lgkmcnt(4)\nv_mfma_f32_16x16x32_bf16 v[8:11], a[108:111], v[48:51], v[8:11]\nv_mfma_f32_16x16x32_bf16 v[16:19], a[236:239], v[48:51], v[16:19]\nv_mfma_f32_16x16x32_bf16 v[24:27], v[220:223], v[48:51], v[24:27]\n" \
"s_waitcnt lgkmcnt(3)\nv_mfma_f32_16x16x32_bf16 v[8:11], a[112:115], v[52:55], v[8:11]\nv_mfma_f32_16x16x32_bf16 v[16:19], a[240:243], v[52:55], v[16:19]\nv_mfma_f32_16x16x32_bf16 v[24:27], v[224:227], v[52:55], v[24:27]\n" \
"s_waitcnt lgkmcnt(2)\nv_mfma_f32_16x16x32_bf16 v[8:11], a[116:119], v[56:59], v[8:11]\nv_mfma_f32_16x16x32_bf16 v[16:19], a[244:247], v[56:59], v[16:19]\nv_mfma_f32_16x16x32_bf16 v[24:27], v[228:231], v[56:59], v[24:27]\n" \
"s_waitcnt lgkmcnt(1)\nv_mfma_f32_16x16x32_bf16 v[8:11], a[120:123], v[36:39], v[8:11]\nv_mfma_f32_16x16x32_bf16 v[16:19], a[248:251], v[36:39], v[16:19]\nv_mfma_f32_16x16x32_bf16 v[24:27], v[232:235], v[36:39], v[24:27]\n" \
"s_waitcnt lgkmcnt(0)\nv_mfma_f32_16x16x32_bf16 v[8:11], a[124:127], v[40:43], v[8:11]\nv_mfma_f32_16x16x32_bf16 v[16:19], a[252:255], v[40:43], v[16:19]\nv_mfma_f32_16x16x32_bf16 v[24:27], v[236:239], v[40:43], v[24:27]\n" \
"buffer_store_dword v80, v3, s[8:11], 0 offen offset:0 sc1\ns_nop 7\nv_fma_f32 v36, v32, v240, v8\nv_fma_f32 v48, v32, v244, v16\n" \
"v_fma_f32 v60, v32, v248, v24\nv_fma_f32 v37, v32, v241, v9\nv_fma_f32 v49, v32, v245, v17\nv_fma_f32 v61, v32, v249, v25\n" \
"v_fma_f32 v38, v32, v242, v10\nv_fma_f32 v50, v32, v246, v18\nv_fma_f32 v62, v32, v250, v26\nv_fma_f32 v39, v32, v243, v11\n" \
"v_fma_f32 v51, v32, v247, v19\nv_fma_f32 v63, v32, v251, v27\nv_fma_f32 v40, v33, v240, v8\nv_fma_f32 v52, v33, v244, v16\n" \
"v_fma_f32 v64, v33, v248, v24\nv_fma_f32 v41, v33, v241, v9\nv_fma_f32 v53, v33, v245, v17\nv_fma_f32 v65, v33, v249, v25\n" \
"v_fma_f32 v42, v33, v242, v10\nv_fma_f32 v54, v33, v246, v18\nv_fma_f32 v66, v33, v250, v26\nv_fma_f32 v43, v33, v243, v11\n" \
"v_fma_f32 v55, v33, v247, v19\nv_fma_f32 v67, v33, v251, v27\nv_permlane16_swap_b32 v36, v40\nv_permlane16_swap_b32 v48, v52\n" \
"v_permlane16_swap_b32 v60, v64\nv_permlane16_swap_b32 v37, v41\nv_permlane16_swap_b32 v49, v53\nv_permlane16_swap_b32 v61, v65\n" \
"v_permlane16_swap_b32 v38, v42\nv_permlane16_swap_b32 v50, v54\nv_permlane16_swap_b32 v62, v66\nv_permlane16_swap_b32 v39, v43\n" \
"v_permlane16_swap_b32 v51, v55\nv_permlane16_swap_b32 v63, v67\nv_mul_f32 v44, v36, v36\nv_mul_f32 v56, v48, v48\n" \
"v_mul_f32 v68, v60, v60\nv_mul_f32 v45, v37, v37\nv_mul_f32 v57, v49, v49\nv_mul_f32 v69, v61, v61\n" \
"v_mul_f32 v46, v38, v38\nv_mul_f32 v58, v50, v50\nv_mul_f32 v70, v62, v62\nv_mul_f32 v47, v39, v39\n" \
"v_mul_f32 v59, v51, v51\nv_mul_f32 v71, v63, v63\nv_fma_f32 v44, v44, v253, v252\nv_fma_f32 v56, v56, v253, v252\n" \
"v_fma_f32 v68, v68, v253, v252\nv_fma_f32 v45, v45, v253, v252\nv_fma_f32 v57, v57, v253, v252\nv_fma_f32 v69, v69, v253, v252\n" \
"v_fma_f32 v46, v46, v253, v252\nv_fma_f32 v58, v58, v253, v252\nv_fma_f32 v70, v70, v253, v252\nv_fma_f32 v47, v47, v253, v252\n" \
"v_fma_f32 v59, v59, v253, v252\nv_fma_f32 v71, v71, v253, v252\nv_mul_f32 v44, v44, v36\nv_mul_f32 v56, v56, v48\n" \
"v_mul_f32 v68, v68, v60\nv_mul_f32 v45, v45, v37\nv_mul_f32 v57, v57, v49\nv_mul_f32 v69, v69, v61\n" \
"v_mul_f32 v46, v46, v38\nv_mul_f32 v58, v58, v50\nv_mul_f32 v70, v70, v62\nv_mul_f32 v47, v47, v39\n" \
"v_mul_f32 v59, v59, v51\nv_mul_f32 v71, v71, v63\nv_mul_f32 v40, v36, v40\nv_mul_f32 v52, v48, v52\n" \
"v_mul_f32 v64, v60, v64\nv_mul_f32 v41, v37, v41\nv_mul_f32 v53, v49, v53\nv_mul_f32 v65, v61, v65\n" \
"v_mul_f32 v42, v38, v42\nv_mul_f32 v54, v50, v54\nv_mul_f32 v66, v62, v66\nv_mul_f32 v43, v39, v43\n" \
"v_mul_f32 v55, v51, v55\nv_mul_f32 v67, v63, v67\nv_exp_f32 v44, v44\nv_exp_f32 v56, v56\n" \
"v_exp_f32 v68, v68\nv_exp_f32 v45, v45\nv_exp_f32 v57, v57\nv_exp_f32 v69, v69\n" \
"v_exp_f32 v46, v46\nv_exp_f32 v58, v58\nv_exp_f32 v70, v70\nv_exp_f32 v47, v47\n" \
"v_exp_f32 v59, v59\nv_exp_f32 v71, v71\nv_fma_f32 v44, v44, v254, v254\nv_fma_f32 v56, v56, v254, v254\n" \
"v_fma_f32 v68, v68, v254, v254\nv_fma_f32 v45, v45, v254, v254\nv_fma_f32 v57, v57, v254, v254\nv_fma_f32 v69, v69, v254, v254\n" \
"v_fma_f32 v46, v46, v254, v254\nv_fma_f32 v58, v58, v254, v254\nv_fma_f32 v70, v70, v254, v254\nv_fma_f32 v47, v47, v254, v254\n" \
"v_fma_f32 v59, v59, v254, v254\nv_fma_f32 v71, v71, v254, v254\nv_rcp_f32 v44, v44\nv_rcp_f32 v56, v56\n" \
"v_rcp_f32 v68, v68\nv_rcp_f32 v45, v45\nv_rcp_f32 v57, v57\nv_rcp_f32 v69, v69\n" \
"v_rcp_f32 v46, v46\nv_rcp_f32 v58, v58\nv_rcp_f32 v70, v70\nv_rcp_f32 v47, v47\n" \
"v_rcp_f32 v59, v59\nv_rcp_f32 v71, v71\nv_mul_f32 v40, v40, v44\nv_mul_f32 v52, v52, v56\n" \
"v_mul_f32 v64, v64, v68\nv_mul_f32 v41, v41, v45\nv_mul_f32 v53, v53, v57\nv_mul_f32 v65, v65, v69\n" \
"v_mul_f32 v42, v42, v46\nv_mul_f32 v54, v54, v58\nv_mul_f32 v66, v66, v70\nv_mul_f32 v43, v43, v47\n" \
"v_mul_f32 v55, v55, v59\nv_mul_f32 v67, v67, v71\nv_cvt_pk_bf16_f32 v40, v40, v41\nv_cvt_pk_bf16_f32 v52, v52, v53\n" \
"v_cvt_pk_bf16_f32 v64, v64, v65\nv_cvt_pk_bf16_f32 v41, v42, v43\nv_cvt_pk_bf16_f32 v53, v54, v55\nv_cvt_pk_bf16_f32 v65, v66, v67\n" \
"v_min_u32 v40, -2, v40\nv_min_u32 v52, -2, v52\nv_min_u32 v64, -2, v64\nbuffer_store_dwordx2 v[40:41], v2, s[8:11], s16 offen offset:0 sc1\n" \
"buffer_store_dwordx2 v[52:53], v2, s[8:11], s17 offen offset:256 sc1\nbuffer_store_dwordx2 v[64:65], v2, s[8:11], s17 offen offset:512 sc1\ns_cmp_ge_u32 s20, s28\ns_cbranch_scc1 9f\n" \
"s_lshr_b32 s94, s20, 1\nv_readlane_b32 s94, v6, s94\ns_and_b32 s95, s20, 1\ns_lshl_b32 s95, s95, 3\n" \
"s_lshr_b32 s94, s94, s95\ns_and_b32 s94, s94, 0x7f\ns_lshl_b32 s95, s94, 13\ns_barrier\n" \
"s_branch 7b\n9:\ns_waitcnt vmcnt(0)\ns_endpgm\n"
struct WMA { static constexpr int NT = 4, NP = 16, RB = 0, LB = 0, CBI = 0;
             static constexpr int kind(int j, int t) { return (A_LDS >> (16 * j + t) & 1) ? 1 : (A_VAR >> (16 * j + t) & 1) ? 2 : 0; } };
namespace la { constexpr int L_Q = 24576, L_K = 57344, L_QM = 65536, L_KM = 66048, L_SP = 69632, L_W = 73728; }
#ifndef LEAD_B
#define LEAD_B 132
#endif
DEVI float dror8(float x) { return __uint_as_float(__builtin_amdgcn_update_dpp(0u, __float_as_uint(x), 0x128, 0xF, 0xF, true)); }
DEVI float scm(float a, float m) { float r; asm("v_fma_f32 %0, %1, %2, -%3" : "=v"(r) : "v"(a), "s"(SC_L2E), "v"(m)); return r; }
DEVI float ex2(float x) { return __builtin_amdgcn_exp2f(x); }
DEVI float max3(float a, float b, float c) { float r; asm("v_max3_f32 %0, %1, %2, %3" : "=v"(r) : "v"(a), "v"(b), "v"(c)); return r; }
constexpr u64 tri(int i) { u64 m = 0; for (int l = 0; l < 64; ++l) if (4 * (l >> 4) + i <= (l & 15)) m |= 1ull << l; return m; }
DEVI unsigned vbfe(unsigned a, int o, int n) { unsigned r; asm volatile("v_bfe_u32 %0, %1, %2, %3" : "=v"(r) : "v"(a), "i"(o), "i"(n)); return r; }
DEVI unsigned vadds(unsigned a, unsigned b) { unsigned r; asm volatile("v_add_u32 %0, %1, %2" : "=v"(r) : "s"(b), "v"(a)); return r; }
DEVI float vexp(float a) { float r; asm volatile("v_exp_f32 %0, %1" : "=v"(r) : "v"(a)); return r; }
DEVI unsigned vors(unsigned a, unsigned b) { unsigned r; asm volatile("v_or_b32 %0, %1, %2" : "=v"(r) : "s"(b), "v"(a)); return r; }
DEVI float faddv(float a, float b) { float r; asm volatile("v_add_f32 %0, %1, %2" : "=v"(r) : "v"(a), "v"(b)); return r; }
DEVI unsigned packv(float a, float b) { unsigned r; asm volatile("v_cvt_pk_bf16_f32 %0, %1, %2" : "=v"(r) : "v"(a), "v"(b)); return r; }
DEVI void setm0q(unsigned v) { asm volatile("s_mov_b32 m0, %0" :: "s"(v) : "memory"); }
template <int OFF> DEVI void st16o(v4u v, rsrc_t r, unsigned vo, unsigned so) {
    asm volatile("buffer_store_dwordx4 %0, %1, %2, %3 offen offset:%4 sc1" :: "v"(v), "v"(vo), "s"(r), "s"(so), "n"(OFF) : "memory"); }
DEVI unsigned vlsa(unsigned a, int sh, unsigned c) { unsigned r; asm volatile("v_lshl_add_u32 %0, %1, %2, %3" : "=v"(r) : "v"(a), "i"(sh), "v"(c)); return r; }
DEVI unsigned vlss(unsigned a, int sh, unsigned c) { unsigned r; asm volatile("v_lshl_add_u32 %0, %1, %2, %3" : "=v"(r) : "v"(a), "i"(sh), "s"(c)); return r; }
template <int OFF> DEVI void sts4h(unsigned p, unsigned u0, unsigned u1) {
    asm volatile("ds_write_b16 %0, %1 offset:%3\n\tds_write_b16_d16_hi %0, %1 offset:%3+16\n\tds_write_b16 %0, %2 offset:%3+8\n\tds_write_b16_d16_hi %0, %2 offset:%3+24" :: "v"(p), "v"(u0), "v"(u1), "n"(OFF) : "memory");
}
DEVI void zero4v(f32x4& d) { f32x2 a, b; asm volatile("v_mov_b64 %0, 0\n\tv_mov_b64 %1, 0" : "=v"(a), "=v"(b)); d = __builtin_shufflevector(a, b, 0, 1, 2, 3); }
DEVI float sel0(float a, u64 m) { float r; asm("v_cndmask_b32 %0, %1, 0, %2" : "=v"(r) : "v"(a), "s"(m)); return r; }
DEVI float vmul(float a, float b) { float r; asm("v_mul_f32 %0, %1, %2" : "=v"(r) : "v"(a), "v"(b)); return r; }
DEVI void swap16(float& a, float& b) { unsigned x = __float_as_uint(a), y = __float_as_uint(b); swap16u(x, y); a = __uint_as_float(x); b = __uint_as_float(y); }
#ifndef PACE
#define PACE 7
#endif
DEVI void pace(int l) { asm volatile("s_cmp_eq_u32 %0, 0\n\ts_cbranch_scc1 1f\n\ts_sleep %1\n1:" :: "s"(l), "n"(PACE) : "scc"); }
DEVI void role_A(const Args& a, int w, int l, int h, int rep, int id2) {
    asm volatile("; MARK role_A"); __builtin_amdgcn_s_waitcnt(0);
    using namespace la;
    const int lane0 = lane_id();
    const rsrc_t rs = mkrs(a.ws, WS_NEED);
    const unsigned lane16 = lane0 * 16, wl = L_W + w * (11 * 2048) + lane16;
    v8u Wc[3];
    unsigned nb, fx, ok;
    const unsigned own = (w >> 1) * 32768 + (w & 1) * 4096;
    {
        const int lane = lane0;
        const unsigned ta = (lane >> 4) * 2048 + w * 256 + (lane & 15) * 16;
        const rsrc_t r1 = mkrs(a.Wq, WSZ(8u << 20)), r2 = mkrs(a.Wk, WSZ(8u << 20));
        const unsigned v1 = (8 * w + ((lane >> 2) & 3)) * 8192 + h * 256 + (lane >> 4) * 64 + (lane & 3) * 16;
        const unsigned lw = opqs(w * 2048);
        load_blocks<32, 4, 4, true, 2>([&](auto B, auto S) {
            if constexpr (B.v == 32) ok = first_there(fx);
            if constexpr (B.v >= 32) dma4(opqs(own + S.v * 8192), mkrs(a.ws, ok), lane16, opqs(O_XN + l * XB + own + S.v * 8192));
            else { pace(l); sfor<0, 2>([&](auto I) { dma_blk(lw + S.v * 8192 + I.v * 32768, B.v < 16 ? r1 : r2, v1, opqs((2 * (B.v & 15) + I.v) * 32 * 8192), 2048); }); } },
                                       [&](auto B, auto S) { sfor<0, 4>([&](auto T) { constexpr int o = S.v * 8192 + (T.v >> 1) * 32768 + (T.v & 1) * 8;
                                                                                      put_frag<WMA, (B.v >> 4) * 2 + (T.v & 1), 2 * (B.v & 15) + (T.v >> 1), o, o + 1024>(ta, wl, Wc); });
                                                             if constexpr (B.v == 24) { nb = side(rs, l); first_look(fx, rs, nb); } });
        TSW(127, 2);
    }
    const unsigned spm = na(nb, N_PM + h * 512), spmm = na(nb, N_PMM + h * 128), smem = na(nb, N_MEM + rep * 16384 + w * 4096);
    const int lane = lane_id(), c = lane & 15, g = lane >> 4;
    const unsigned wlo = (unsigned)(uintptr_t)a.ws, whi = (unsigned)((uintptr_t)a.ws >> 32), hi = -(w >> 1);
    const v8u pv{lane16, wl, (lane & 2) ? 0xEEEEu : 0x4444u, (unsigned)(L_QM + w * 1024 + (c >> 2) * 128 + g * 32 + (c & 3) * 4), (unsigned)(L_QM + g * 128 + (c & 7) * 16),
                 w == 2 ? ((g >> 1) * 16 + crow(c & 7, c >> 3)) * 16 + (g & 1) * 8 : w < 2 ? lane * 8 : lane < NC ? DV(lane * 512) : 1u << 30,
                 w < 2 ? c * 16 + g * 4 : c < 8 ? crow(c, g & 1) * 8 + (g >> 1) * 4 : 1u << 30, 0u};
    const v16u s0{wlo, whi, RSZ, 0x20000u, wlo, whi, 0u, 0x20000u, (unsigned)tri(0) | hi, (unsigned)(tri(0) >> 32) | hi, (unsigned)tri(1) | hi, (unsigned)(tri(1) >> 32) | hi,
                  (unsigned)tri(2) | hi, (unsigned)(tri(2) >> 32) | hi, (unsigned)tri(3) | hi, (unsigned)(tri(3) >> 32) | hi};
    const v16u s1{0xFF00FF00u, 0xFF00FF00u, 0u, ~0u, hi, hi, 0u, O_XN + l * XB + own, own, smem, w == 2 ? spm : OOB, nb + DV(N_PTX + h * 2048), w == 1 ? 0u : OOB, w == 0 ? 0u : OOB,
                  w < 2 ? w * 256 : w == 2 ? spmm - 1536 : OOB, ~hi};
    const v16u s2{w < 2 ? w * 1024 : w == 2 ? spm : spmm, w < 2 ? 4u : 3u, w < 2 ? 0u : w == 2 ? 0x200u : 0x380u, w ? 0u : __float_as_uint(-3e38f), (unsigned)(L_Q + w * 1024), (unsigned)(L_Q + (w & 1) * 4096),
                  __float_as_uint(SC_L2E), __float_as_uint(-1e30f), na(nb, N_FX), w == 3 ? na(nb, N_PA + ((h >> 3) * 32 + 8 + (h & 7)) * 4) : OOB, (unsigned)(L_KM + (w >> 1) * 1024 + (w & 1) * 256),
                  0u, 0u, (unsigned)(L_SP + w * 256), O_PROF + (blockIdx.x * 4 + w) * 8192, na(nb, N_WM + h * 16384 + w * 4096)};
    asm volatile(A_LOOP :: "{v[0:7]}"(Wc[0]), "{v[8:15]}"(Wc[1]), "{v[16:23]}"(Wc[2]), "{v[24:31]}"(pv), "{s[8:23]}"(s0), "{s[24:39]}"(s1), "{s[40:55]}"(s2),
                 "{s58}"(ok), "{s[4:7]}"(again(id2, nb, w)) : "memory", "s99", "vcc");
}
constexpr int bkind(int n) { return (B_LDS >> n & 1) ? 1 : (B_VAR >> n & 1) ? 2 : 0; }
struct WMV { static constexpr int NT = 2, NP = 16, RB = 0, LB = 0, CBI = 0; static constexpr int kind(int j, int t) { return bkind(16 * j + t); } };
struct WMO { static constexpr int NT = 16, NP = 2, LB = __builtin_popcount((unsigned)B_LDS), CBI = __builtin_popcount((unsigned)B_VAR), RB = 8 * (32 - LB - CBI);
             static constexpr int kind(int j, int t) { return bkind(32 + 2 * j + t); } };
#ifndef B_LV
#define B_LV 24576
#endif
namespace lb { constexpr int L_V = B_LV, L_VM = B_LV + 4096, L_W = 65536; }
DEVI void role_B(const Args& a, int w, int l, int h, int rep, int id2) {
    asm volatile("; MARK role_B"); __builtin_amdgcn_s_waitcnt(0);
    using namespace lb;
    const int lane0 = lane_id();
    const rsrc_t rs = mkrs(a.ws, WS_NEED);
    const unsigned lane16 = lane0 * 16, wl = L_W + w * (12 * 2048) + lane16;
    v8u Wc[2];
    unsigned nb, fx, ok;
    const unsigned own = (w >> 1) * 32768 + (w & 1) * 4096;
    {
        const int lane = lane0;
        const unsigned ta = (lane >> 4) * 2048 + w * 256 + (lane & 15) * 16, tb = (lane >> 5) * 32768 + w * 2048 + (lane & 16) * 64 + (lane & 15) * 16;
        const rsrc_t r1 = mkrs(a.Wv, WSZ(8u << 20)), r2 = mkrs(a.Wo, WSZ(8u << 20));
        const unsigned v1 = (8 * w + ((lane >> 2) & 3)) * 8192 + h * 256 + (lane >> 4) * 64 + (lane & 3) * 16, v2 = (h * 128 + ((lane >> 2) & 7)) * 2048 + w * 512 + (lane >> 5) * 64 + (lane & 3) * 16;
        const unsigned lw = opqs(w * 2048);
        load_blocks<32, 4, 4, true, 3>([&](auto B, auto S) {
            constexpr int b = B.v & 15;
            if constexpr (B.v == 32) ok = first_there(fx);
            if constexpr (B.v < 32) pace(l);
            if constexpr (B.v >= 32) dma4(opqs(own + S.v * 8192), mkrs(a.ws, ok), lane16, opqs(O_XN + l * XB + own + S.v * 8192));
            else sfor<0, 2>([&](auto I) { if constexpr (B.v < 16) dma_blk(lw + S.v * 8192 + I.v * 32768, r1, v1, opqs((2 * b + I.v) * 32 * 8192), 2048);
                                     else dma_blk(lw + S.v * 8192 + I.v * 32768, r2, v2, opqs(((b & 3) * 32 + I.v * 16) * 2048 + (b >> 2) * 128), 1024); });
        }, [&](auto B, auto S) {
            constexpr int b = B.v & 15;
            sfor<0, 4>([&](auto T) { constexpr int o = S.v * 8192 + (T.v >> 1) * (B.v < 16 ? 32768 : 512) + (T.v & 1) * 8;
                                     if constexpr (B.v < 16) put_frag<WMV, T.v & 1, 2 * b + (T.v >> 1), o, o + 1024>(ta, wl, Wc); else put_frag<WMO, (b & 12) + T.v, b & 3, o, o + 256>(tb, wl, Wc); });
            if constexpr (B.v == 24) { nb = side(rs, l); first_look(fx, rs, nb); }
        });
        TSW(127, 2);
    }
    const int lane = lane_id(), c = lane & 15, g = lane >> 4;
    const unsigned wlo = (unsigned)(uintptr_t)a.ws, whi = (unsigned)((uintptr_t)a.ws >> 32);
    const unsigned prow = L_VM + w * 512 + ((c >> 2) & 1) * 256 + g * 64 + (c >> 3) * 32 + (c & 1) * 8 + ((c >> 1) & 1) * 4;
    const v4u pv{lane16, wl, ((lane & 2) ? 0xEEEEu : 0x4444u) | prow << 16, (unsigned)(L_VM + (g >> 1) * 512 + (lane & 3) * 64 + (g & 1) * 32 + ((lane >> 2) & 3) * 8)};
    const v16u s0{wlo, whi, RSZ, 0x20000u, wlo, whi, 0u, 0x20000u, OOB, OOB, OOB, OOB, ~1u, O_XN + l * XB + own, own, na(nb, N_MEM + rep * 16384 + w * 4096)};
    const v8u s1{na(nb, N_WM + h * 16384 + w * 4096), na(nb, N_U + h * 65536 + w * 16384), na(nb, N_FX), w == 3 ? na(nb, N_PA + ((h >> 3) * 32 + (h & 7)) * 4) : OOB,
                 (unsigned)(L_V + w * 1024), OOB, 0u, O_PROF + (blockIdx.x * 4 + w) * 8192 + 64};
    asm volatile(B_LOOP :: "{v[0:7]}"(Wc[0]), "{v[8:15]}"(Wc[1]), "{v[16:19]}"(pv), "{v20}"(fx), "{s[8:23]}"(s0), "{s[24:31]}"(s1), "{s32}"(1u), "{s[4:7]}"(again(id2, nb, w)) : "memory", "s99", "vcc");
}
template <class NB> DEVI void put_tile(v4u (&u)[8], rsrc_t rs, int tile, int w, int lane, NB&& nbf) {
    const unsigned vo = tile * 32768 + w * 8192 + lane * 16, red = (lane & 15) * 16;
    float s = 0.f;
#pragma unroll
    for (int f = 0; f < 8; ++f) {
#pragma unroll
        for (int i = 0; i < 4; ++i) s += bflo(u[f][i]) + bfhi(u[f][i]);
    }
    s = red32(red16(s));
    BAR;
    if (lane < 16) *LDSP(float, red + w * 4) = s;
    WL0; BAR;
    const f32x4 sa = __builtin_bit_cast(f32x4, ldsq(red, 0));
    const float mean = ((sa[0] + sa[1]) + (sa[2] + sa[3])) * (1.0f / 1024.0f);
    float q = 0.f;
#pragma unroll
    for (int f = 0; f < 8; ++f) {
        asm volatile("" : "+v"(u[f]));
#pragma unroll
        for (int i = 0; i < 4; ++i) { const float d0 = bflo(u[f][i]) - mean, d1 = bfhi(u[f][i]) - mean; q = fmaf(d0, d0, fmaf(d1, d1, q)); }
    }
    q = red32(red16(q));
    if (lane < 16) *LDSP(float, red + 256 + w * 4) = q;
    WL0; BAR;
    const f32x4 qa = __builtin_bit_cast(f32x4, ldsq(red, 256));
    const float mb = rbf(mean), inv = 1.0f / rbf(sqrtf(rbf(rbf(((qa[0] + qa[1]) + (qa[2] + qa[3])) * (1.0f / 1024.0f)) + 1e-5f)));
#pragma unroll
    for (int f = 0; f < 8; ++f) {
        v4u o;
        asm volatile("" : "+v"(u[f]));
#pragma unroll
        for (int i = 0; i < 4; ++i) { const unsigned d = pack2(bflo(u[f][i]) - mb, bfhi(u[f][i]) - mb); o[i] = pack2(bflo(d) * inv, bfhi(d) * inv); }
        st16(o, rs, O_XN + vo + f * 1024);
    }
    const unsigned nb = nbf();
    WV0;
    if (lane == 0) st4(0u, rs, na(nb, N_FX) + tile * 16 + w * 4, 0);
}
#ifndef SXL
#define SXL 1
#endif
#ifndef HLD
#define HLD 158
#endif
#ifndef SXH
#define SXH 30
#endif
#ifndef UIN
#define UIN 8
#endif
namespace lr { constexpr int PT1 = NV + 48, PT0 = NV + 80, PTM = NV + 96, VPM = NV, VMM = NV + 16, VW = NV + 28, VX = NV + 44, L_M = 1024, MD = 288, MW = 1536, L_T = 8192, VE = NV + 112; }
template <int R, int N, int OFF> DEVI void lda(rsrc_t rs, unsigned vo, unsigned so) {
    if constexpr (N == 4) asm volatile("buffer_load_dwordx4 a[%3:%4], %0, %1, %2 offen offset:%5 sc1" :: "v"(vo), "s"(rs), "s"(so), "n"(R), "n"(R + 3), "n"(OFF) : "memory");
    else asm volatile("buffer_load_dwordx2 a[%3:%4], %0, %1, %2 offen offset:%5 sc1" :: "v"(vo), "s"(rs), "s"(so), "n"(R), "n"(R + 1), "n"(OFF) : "memory");
}
template <int R, int N, int OFF> DEVI void ldv(rsrc_t rs, unsigned vo, unsigned so) {
    if constexpr (N == 4) asm volatile("buffer_load_dwordx4 v[%3:%4], %0, %1, %2 offen offset:%5 sc1" :: "v"(vo), "s"(rs), "s"(so), "n"(R), "n"(R + 3), "n"(OFF) : "memory");
    else asm volatile("buffer_load_dwordx2 v[%3:%4], %0, %1, %2 offen offset:%5 sc1" :: "v"(vo), "s"(rs), "s"(so), "n"(R), "n"(R + 1), "n"(OFF) : "memory");
}
template <int R> DEVI unsigned rdv() { unsigned t; asm volatile("v_mov_b32 %0, v[%1]" : "=v"(t) : "n"(R)); return t; }
template <int RA, int RB, bool Z, bool AV> DEVI void mfh(f32x4& d) {
    if constexpr (AV) { if constexpr (Z) asm volatile(M16 "%0, v[%1:%2], v[%3:%4], 0" : "=v"(d) : "n"(RA), "n"(RA + 3), "n"(RB), "n"(RB + 3));
                        else asm volatile(M16 "%0, v[%1:%2], v[%3:%4], %0" : "+v"(d) : "n"(RA), "n"(RA + 3), "n"(RB), "n"(RB + 3)); }
    else { if constexpr (Z) asm volatile(M16 "%0, a[%1:%2], v[%3:%4], 0" : "=v"(d) : "n"(RA), "n"(RA + 3), "n"(RB), "n"(RB + 3));
           else asm volatile(M16 "%0, a[%1:%2], v[%3:%4], %0" : "+v"(d) : "n"(RA), "n"(RA + 3), "n"(RB), "n"(RB + 3)); }
}
template <int RA, int RB, bool Z> DEVI void mfh16(f32x4& d) {
    if constexpr (Z) asm volatile("v_mfma_f32_16x16x16_bf16 %0, a[%1:%2], v[%3:%4], 0" : "=v"(d) : "n"(RA), "n"(RA + 1), "n"(RB), "n"(RB + 1));
    else asm volatile("v_mfma_f32_16x16x16_bf16 %0, a[%1:%2], v[%3:%4], %0" : "+v"(d) : "n"(RA), "n"(RA + 1), "n"(RB), "n"(RB + 1));
}
template <int R> DEVI u64 ltv(int need) { u64 m; asm volatile("v_cmp_lt_i32 %0, v[%2], %1" : "=s"(m) : "v"(need), "n"(R)); return m; }
template <int R> DEVI void ldv4(rsrc_t rs, unsigned vo, unsigned so) { asm volatile("buffer_load_dwordx4 v[%3:%4], %0, %1, %2 offen sc1" :: "v"(vo), "s"(rs), "s"(so), "n"(R), "n"(R + 3) : "memory"); }
#define SM_(E, A, B) "v_smfmac_f32_16x16x64_bf16 v[%[e]+" #E ":%[e]+" #E "+3], " A ", " B ", %[si]\n\t"
#define TG_(M, R) "v_bitop3_b32 v[%[e]+" #M "], v[%[e]+" #M "], v[" R "], %[tg] bitop3:0xf6\n\t"
#define PP_(P, N) "s_waitcnt vmcnt(" #N ")\n\t" SM_(0, "v[%[p]+4*" #P ":%[p]+4*" #P "+3]", "a[%[u]+16*" #P ":%[u]+16*" #P "+7]") TG_(8, "%[p]+4*" #P)
#define PQ_(P) SM_(4, "v[%[p]+4*" #P ":%[p]+4*" #P "+3]", "a[%[u]+16*" #P "+8:%[u]+16*" #P "+15]") TG_(9, "%[p]+4*" #P "+2")
#define ZE_ "v_mov_b64 v[%[e]:%[e]+1], 0\n\tv_mov_b64 v[%[e]+2:%[e]+3], 0\n\tv_mov_b64 v[%[e]+4:%[e]+5], 0\n\tv_mov_b64 v[%[e]+6:%[e]+7], 0\n\tv_mov_b64 v[%[e]+8:%[e]+9], 0\n\t"
template <int U0> DEVI unsigned chain(rsrc_t rs, int tgo, unsigned sp, unsigned sm, unsigned sw0, unsigned sw1, unsigned tg, unsigned spt, unsigned lane16, unsigned lane8, unsigned vmm, unsigned vw, int sidx,
                                      unsigned lred, unsigned lrr, unsigned vpub, v2u sxv, unsigned sxo) {
    using namespace lr;
    unsigned n, d;
    asm volatile(ZE_ "s_mov_b32 %[n], 0\n\ts_memrealtime s[98:99]\n\ts_add_u32 %[d], %[pt], 0x800\n\ts_waitcnt lgkmcnt(0)\n\ts_sub_i32 s99, %[t], s98\n\ts_lshl_b32 s99, s99, 8\n\ts_sub_u32 s99, s99, 1\n\t"
        "s_cmp_lt_u32 s99, 0x10000\n\ts_cbranch_scc1 2f\n\ts_branch 3f\n"
        "1:\n\ts_add_u32 %[n], %[n], 1\n\ts_cmp_lt_u32 %[n], %[cap]\n\ts_cbranch_scc1 4f\n\ts_endpgm\n4:\n\t" ZE_ "v_bfrev_b32 %[so], 1\n\ts_branch 3f\n"
        "2:\n\ts_memrealtime s[98:99]\n\ts_waitcnt lgkmcnt(0)\n\ts_sub_i32 s99, %[t], s98\n\ts_lshl_b32 s99, s99, 8\n\ts_cmp_gt_i32 s99, 0\n\ts_cbranch_scc1 2b\n"
        "3:\n\t"
        "buffer_load_dwordx4 v[%[p]:%[p]+3], %[l16], %[rs], %[sp] offen sc1\n\t"
        "buffer_load_dwordx4 v[%[p]+4:%[p]+7], %[l16], %[rs], %[sp] offen offset:1024 sc1\n\t"
        "buffer_load_dwordx4 v[%[p]+8:%[p]+11], %[l16], %[rs], %[sp] offen offset:2048 sc1\n\t"
        "buffer_load_dwordx4 v[%[p]+12:%[p]+15], %[l16], %[rs], %[sp] offen offset:3072 sc1\n\t"
        "buffer_load_dwordx2 v[%[p]+16:%[p]+17], %[vm], %[rs], %[sm] offen sc1\n\t"
        "buffer_load_dwordx2 v[%[p]+18:%[p]+19], %[vm], %[rs], %[sm] offen offset:128 sc1\n\t"
        "buffer_load_dwordx4 v[%[w]:%[w]+3], %[vw], %[rs], %[sw0] offen sc1\n\t"
        "buffer_load_dwordx4 v[%[w]+4:%[w]+7], %[vw], %[rs], %[sw1] offen sc1\n\t"
        "buffer_load_dwordx4 v[%[w]+8:%[w]+11], %[vw], %[rs], %[sw0] offen offset:256 sc1\n\t"
        "buffer_load_dwordx4 v[%[w]+12:%[w]+15], %[vw], %[rs], %[sw1] offen offset:256 sc1\n\t"
        PP_(0, 9) "buffer_store_dwordx2 %[sx], %[so], %[rs], 0 offen sc1\n\t" PQ_(0) PP_(1, 9) PQ_(1) PP_(2, 8) PQ_(2) PP_(3, 7) PQ_(3)
        "s_waitcnt vmcnt(5)\n\t" TG_(8, "%[p]+16") TG_(9, "%[p]+17") TG_(8, "%[p]+18") TG_(9, "%[p]+19")
        "s_waitcnt vmcnt(3)\n\t" SM_(0, "v[%[p]+16:%[p]+19]", "v[%[w]:%[w]+7]") TG_(8, "%[w]") TG_(9, "%[w]+4")
        "s_waitcnt vmcnt(1)\n\t" SM_(4, "v[%[p]+16:%[p]+19]", "v[%[w]+8:%[w]+15]") TG_(8, "%[w]+8") TG_(9, "%[w]+12")
        "v_bitop3_b32 v[%[e]+8], v[%[e]+8], v[%[e]+9], 1 bitop3:0xa8\n\tv_cmp_ne_u32 vcc, 0, v[%[e]+8]\n\ts_nop 0\n\t"
        "v_add_f32 v[%[e]], v[%[e]], v[%[e]+2]\n\tv_add_f32 v[%[e]+1], v[%[e]+1], v[%[e]+3]\n\tds_write2_b32 %[lw], v[%[e]], v[%[e]+1] offset1:2\n\ts_nop 0\n\t"
        "v_add_f32 v[%[e]+4], v[%[e]+4], v[%[e]+6]\n\tv_add_f32 v[%[e]+5], v[%[e]+5], v[%[e]+7]\n\tds_write2_b32 %[lw], v[%[e]+4], v[%[e]+5] offset0:%[md] offset1:%[md]+2\n\t"
        "s_cbranch_vccnz 1b\n\t"
        "buffer_load_dwordx4 v[%[t1]:%[t1]+3], %[l16], %[rs], %[pt] offen sc1\n\tbuffer_load_dwordx2 v[%[t0]:%[t0]+1], %[l8], %[rs], %[pt] offen offset:1024 sc1\n\t"
        "buffer_load_dwordx4 v[%[t1]+4:%[t1]+7], %[l16], %[rs], %[d] offen sc1\n\tbuffer_load_dwordx2 v[%[t0]+2:%[t0]+3], %[l8], %[rs], %[d] offen offset:1024 sc1\n\t"
        "s_waitcnt lgkmcnt(0)\n\ts_barrier\n\t"
        "ds_read2st64_b64 v[%[e]:%[e]+3], %[lr] offset1:3\n\tds_read2st64_b64 v[%[e]+4:%[e]+7], %[lr] offset0:6 offset1:9\n\ts_waitcnt lgkmcnt(1)\n\t"
        "v_add_f32 v[%[e]], v[%[e]], v[%[e]+2]\n\tv_add_f32 v[%[e]+1], v[%[e]+1], v[%[e]+3]\n\ts_waitcnt lgkmcnt(0)\n\t"
        "v_add_f32 v[%[e]+4], v[%[e]+4], v[%[e]+6]\n\tv_add_f32 v[%[e]+5], v[%[e]+5], v[%[e]+7]\n\tv_add_f32 v[%[e]], v[%[e]], v[%[e]+4]\n\tv_add_f32 v[%[e]+1], v[%[e]+1], v[%[e]+5]\n\t"
        "v_cvt_pk_bf16_f32 v[%[e]+2], v[%[e]], v[%[e]+1]\n\tv_min_u32 v[%[e]], -2, v[%[e]+2]\n\tv_min_u32 v[%[e]+1], -2, v[%[e]+2]\n\ts_nop 1\n\t"
        "v_permlane16_swap_b32 v[%[e]], v[%[e]+1]\n\tv_mov_b64 v[%[e]+2:%[e]+3], v[%[e]:%[e]+1]\n\ts_nop 1\n\t"
        "v_permlane32_swap_b32 v[%[e]], v[%[e]+2]\n\tv_permlane32_swap_b32 v[%[e]+1], v[%[e]+3]\n\t"
        "buffer_store_dwordx4 v[%[e]:%[e]+3], %[vp], %[rs], 0 offen sc1"
        : [n] "=&s"(n), [d] "=&s"(d), [so] "+v"(sxo)
        : [rs] "s"(rs), [t] "s"(tgo), [sp] "s"(sp), [sm] "s"(sm), [sw0] "s"(sw0), [sw1] "s"(sw1), [tg] "s"(tg), [pt] "s"(spt), [l16] "v"(lane16), [l8] "v"(lane8), [vm] "v"(vmm), [vw] "v"(vw), [si] "v"(sidx),
          [lw] "v"(lred), [lr] "v"(lrr), [vp] "v"(vpub), [sx] "v"(sxv), [e] "n"(VE), [p] "n"(VPM), [w] "n"(VW), [u] "n"(U0), [t1] "n"(PT1), [t0] "n"(PT0), [md] "n"(MD / 4), [cap] "n"(SPIN_CAP)
        : "memory", "scc", "vcc", "s98", "s99");
    return n;
}
template <int R> DEVI void tagd(unsigned& m, unsigned t) { asm volatile("v_bitop3_b32 %0, %0, v[%2], %1 bitop3:0xf6" : "+v"(m) : "s"(t), "n"(R)); }
#ifndef DLY_R
#define DLY_R 1200
#endif
#ifndef DLY_X
#define DLY_X 1300
#endif
constexpr unsigned O_ARM = O_N + DV(N_PA) + 96;
DEVI void arm_last(const Args& a, int q, int n, int t0) {
    const rsrc_t rs = mkrs(a.ws, WS_NEED);
    hold(t0, DLY_V); side(rs, 0); hold(t0, 4000); hold(t0, 7000);
#pragma nounroll
    for (int k = q; k < N2H * (64 + 224 + 64 * NKF); k += n) {
        st16(v4u{SENT, SENT, SENT, SENT}, rs, (k < N2H * 64 ? O_XM + 2 * XB - N2H * 65536 : k < N2H * 288 ? O_D - N2H * 288 * 1024 : O_N - N2H * (288 + 64 * NKF) * 1024) + k * 1024 + lane_id() * 16);
        __builtin_amdgcn_s_sleep(8);
    }
    WV0;
    st4(0u, rs, lane_id() == 0 ? O_ARM + q * 4 : OOB, 0);
}
DEVI unsigned role_R(const Args& a, int w, int l, int T2, int t0) {
    asm volatile("; MARK role_R"); __builtin_amdgcn_s_waitcnt(0);
    using namespace lr;
    const int lane = lane_id(), c = lane & 15, g = lane >> 4;
    const rsrc_t rs = mkrs(a.ws, WS_NEED);
    const unsigned lane16 = lane * 16;
#pragma nounroll
    for (int k = 0; k < 4 * l; ++k) {
        const int tile = k * 32 + T2;
        if (tile >= LN_NOW) hold(t0, DLY_X);
        v4u u[8];
#pragma unroll
        for (int f = 0; f < 8; ++f) u[f] = *(const v4u*)(a.x + (size_t)(tile * 16 + c) * 1024 + (w * 8 + f) * 32 + g * 8);
        if (tile < 2 * NSEG) put_tile(u, rs, tile, w, lane, [&]() { hold(t0, DLY_V); return side(rs, 0); });
        TSW(127, 1 + k);
    }
    auto arm = [&](unsigned n2, int s) {
        if (s < 8)
#pragma unroll
            for (int k = 0; k < 4; ++k) st16(v4u{SENT, SENT, SENT, SENT}, rs, na(n2, N_WM + s * 524288 + T2 * 16384 + w * 4096) + k * 1024 + lane16);
        st16(v4u{SENT, SENT, SENT, SENT}, rs, w < 2 ? na(n2, N_PTX + s * 65536 + T2 * 2048) + w * 1024 + lane16 : lane >= (w == 2 ? 32 : 8) || s >= 8 ? OOB :
                                              na(n2, w == 2 ? N_PM + s * 16384 + T2 * 512 : N_PMM + s * 8192 + T2 * 128) + lane16);
    };
    hold(t0, DLY_R);
    const unsigned nb = side(rs, l), vmem = c < 8 ? na(nb, N_MEM + T2 * 512) + g * 65536 + w * 128 + c * 16 : OOB;
    auto put_mem = [&](v4u v, int s) { st16(v, rs, vmem + rslot(s) * (NREP * 32768)); };
    {
        const unsigned* m0 = (const unsigned*)(a.mem0 + ((w & 1) * 4 + ((lane >> 2) & 1) * 2 + (lane & 1)) * 1024 + T2 * 32 + (w >> 1) * 16 + ((lane >> 1) & 1) * 2);
        const v4u mv{nosent(m0[0]), m0[2], m0[4], m0[6]};
        put_mem(v4u{SENT, SENT, SENT, SENT}, 1); put_mem(v4u{SENT, SENT, SENT, SENT}, 2);
#pragma nounroll
        for (int k = 0; k < 16; ++k) arm(nb, k);
        WV0; BAR;
        put_mem(mv, 0);
    }
    const int sidx = opq((lane & 2) ? 0xEEEE : 0x4444);
    const unsigned vmm = (8 * w + 2 * g) * 128 + c * 8, vw = (8 * w + g) * 32768 + DV(T2 * 512) + c * 16, vptm = g * 2048 + 1536 + c * 16;
    const unsigned sfl = na(nb, N_PA + (unsigned)((T2 & (NC - 1)) * 4 + w) * 128), su0 = na(nb, N_U + 8 * w * 65536 + T2 * 2048), vfl = sfl + c * 4;
    const unsigned lred = L_M + w * MW + ((c >> 2) & 1) * 2 * MD + (c >> 3) * 128 + g * 32 + ((c >> 1) & 1) * 16 + (c & 1) * 4, lrr = L_M + g * MD + w * 64 + (c & 7) * 8;
    auto ld_u = [&](auto B, int s) {
        const unsigned so = su0 + rslot(s) * (4u << 20);
        sfor<0, 16>([&](auto I) { constexpr int j = I.v >> 1, e = I.v & 1; lda<64 * B.v + 16 * (j >> 1) + 8 * e + 4 * (j & 1), 4, e * 1024>(rs, lane16, so + j * 131072); });
    };
    wait_prog(rs, sfl + (lane & 7) * 4, 0, 0);
    ld_u(IC<0>{}, 0);
    const rsrc_t rxi = l ? rs : mkrs(a.x, XB);
    const unsigned vxi = l ? lane16 : c * 2048 + g * 16, sxi = (w & 1) * 32768 + (l ? O_XIN + XB + T2 * 1024 : T2 * 64);
    v2u sxv{0u, 0u};
    unsigned sxo = OOB;
    int tpub = t0;
    auto step = [&](auto B, int s) {
        constexpr int b = B.v;
        TSD;
        TSR(0); TSK(15);
        if (s == 8) { TSC(126, 0); TSW(126, 1); }
        if (s == 56) { TSC(126, 2); TSW(126, 3); }
        const unsigned spt = na(nb, N_PTX + (unsigned)((s & 15) * 32 + 8 * w) * 2048);
        auto ptx = [&](auto N) {
            constexpr int n = N.v & 31, j = n >> 1;
            if constexpr (n == 20) ldv4<VX>(rxi, vxi, sxi + s * 65536);
            else if constexpr (n >= 16) ldv<PTM + 4 * (n - 16), 4, (n & 1) * 256>(rs, vptm, spt + (j - 8) * 16384);
            else if constexpr (n & 1) ldv<PT0 + 2 * j, 2, 1024>(rs, lane * 8, spt + DV(j * 2048));
            else ldv<PT1 + 4 * j, 4, 0>(rs, lane16, spt + DV(j * 2048));
        };
        {
            const unsigned sw = opqs(na(nb, N_WM + rslot(s) * 524288)), sw1 = opqs(sw + 131072), sp = opqs(na(nb, N_PM + w * 4096) + rslot(s) * 32768), sm = opqs(na(nb, N_PMM) + rslot(s) * 16384);
            const unsigned vpub = opq(s + 1 < NSEG ? vmem + rslot(s + 1) * (NREP * 32768) : OOB), tg = opqs((s >> 3) & 1), spq = opqs(spt);
            SB;
            if ((int)__builtin_amdgcn_s_memrealtime() - tpub < HLD) { wvm<16 - UIN>(); st8(sxv, rs, sxo, 0); sxo = OOB; hold(tpub, HLD); }
            const int mx = wait_hint(rs, sfl, vfl, s + 1);
            if (left(mx) > SXH * 256) { wvm<16 - UIN>(); st8(sxv, rs, sxo, 0); sxo = OOB; }
            TSR(5);
            const unsigned spins = chain<64 * b>(rs, mx, sp, sm, sw, sw1, tg, spq, lane16, lane * 8, vmm, vw, sidx, lred, lrr, vpub, sxv, sxo);
#if defined(TS) && TS != 4
            tsr[8] = __builtin_amdgcn_readfirstlane(spins) + 1;
#endif
            tpub = (int)__builtin_amdgcn_s_memrealtime();
            TSR(2);
        }
        sfor<4, 21>(ptx);
        BAR;
        f32x4 t[2][2], u[2];
        const unsigned su = s + 1 < NSEG ? su0 + rslot(s + 1) * (4u << 20) : OOB;
        st16(v4u{SENT, SENT, SENT, SENT}, rs, s + 3 < NSEG ? vmem + rslot(s + 3) * (NREP * 32768) : OOB);
#pragma nounroll
        for (int n = 0;; ++n) {
            if (n) { sfor<32, 52>(ptx); WV0; }
            auto lu = [&](auto N) { constexpr int j = N.v >> 1, e = N.v & 1; lda<16 * (j >> 1) + 8 * e + 4 * (j & 1) + 64 * (1 - b), 4, e * 1024>(rs, lane16, su + j * 131072); };
            TSR(6);
            unsigned mt = opq(0);
            const unsigned tp = (s >> 4) & 1;
            sfor<0, 16>([&](auto N) {
                constexpr int j = N.v >> 1, e = N.v & 1, R = 64 * b + 16 * (j >> 1) + 8 * e + 4 * (j & 1), nu = N.v * UIN / 16;
                if constexpr (e == 0) wvm<21 - 2 * j + (j < 2) + nu - 1>();
                if constexpr ((N.v + 1) * UIN / 16 > nu) lu(IC<nu>{});
                mfh<R, PT1 + 4 * j, j == 0, false>(t[1][e]); tagd<e ? PT0 + 2 * j : PT1 + 4 * j>(mt, tp); mfh16<R, PT0 + 2 * j, j == 0>(u[e]);
            });
            wvm<1 + UIN>();
            sfor<0, 8>([&](auto N) { constexpr int q = N.v >> 2, rt = (N.v >> 1) & 1, e = N.v & 1; mfh<VW + 8 * e + 4 * q, PTM + 4 * (2 * q + rt), N.v < 2, true>(t[rt][e]);
                                     tagd<PTM + 2 * N.v>(mt, tp); tagd<PTM + 2 * N.v + 1>(mt, tp); });
            nop<7>(); fence(t[0][0], t[0][1], t[1][0], t[1][1], u[0], u[1]);
#pragma unroll
            for (int e = 0; e < 2; ++e)
#pragma unroll
                for (int i = 0; i < 4; ++i) t[0][e][i] = fadd(t[0][e][i], u[e][i]);
            if (__builtin_amdgcn_uicmp(mt & 1, 0u, 33) == 0 || n == 2) break;
            TSV(s, 7, 1);
            if (n == 1) wait_prog(rs, sfl + 32 + (lane & 7) * 4, 0, (s + 2) << 24);
        }
        TSR(3);
#pragma unroll
        for (int e = 0; e < 2; ++e) { stsq(lane16 + w * 4096, L_T + e * 1024, __builtin_bit_cast(v4u, t[0][e])); stsq(lane16 + w * 4096, L_T + 2048 + e * 1024, __builtin_bit_cast(v4u, t[1][e])); }
        WL0; BAR;
        if (w < 2) {
            float o[8], s1 = 0.f, s2 = 0.f;
#pragma unroll
            for (int e = 0; e < 2; ++e) {
                f32x4 q[4];
#pragma unroll
                for (int k = 0; k < 4; ++k) q[k] = __builtin_bit_cast(f32x4, ldsq(lane16 + w * 2048, L_T + k * 4096 + e * 1024));
#pragma unroll
                for (int i = 0; i < 4; ++i) o[4 * e + i] = fadd(fadd(q[0][i], q[1][i]), fadd(q[2][i], q[3][i]));
            }
            v4u x1;
            sfor<0, 4>([&](auto I) {
                constexpr int i = I.v;
                const unsigned x = rdv<VX + i>(), at = pack2(o[2 * i], o[2 * i + 1]);
                x1[i] = pack2(bflo(x) + bflo(at), bfhi(x) + bfhi(at));
                const float lo = bflo(x1[i]), hi = bfhi(x1[i]);
                s1 += lo + hi; s2 = fmaf(lo, lo, fmaf(hi, hi, s2));
            });
            st16(x1, rs, O_XM + l * XB + s * 65536 + w * 32768 + T2 * 1024 + lane16);
            sxv = v2u{nosent(__float_as_uint(red32(red16(s1)))), __float_as_uint(red32(red16(s2)))};
            sxo = lane < 16 ? O_SX + (unsigned)((l * 64 + s) * 32 + T2) * 256 + (SXL ? w * 128 + c * 8 : c * 16 + w * 8) : OOB;
        }
        TSR(4);
        {
            sfor<UIN, 16>([&](auto N) { constexpr int j = N.v >> 1, e = N.v & 1; lda<16 * (j >> 1) + 8 * e + 4 * (j & 1) + 64 * (1 - b), 4, e * 1024>(rs, lane16, su + j * 131072); });
        }
#if TS != 4
        TSF(s, 0x817D);
#endif
    };
#pragma nounroll
    for (int s = 0; s < NSEG; s += 2) {
        if (s == 48 && l && T2 == 0) wait_prog(rs, O_ARM + (lane & 7) * 4, 0, 0);
        step(IC<0>{}, s); step(IC<1>{}, s + 1);
    }
    wvm<16 - UIN>(); st8(sxv, rs, sxo, 0);
    WV0; BAR;
    return nb;
}
#ifndef FSIDE
#define FSIDE 1
#endif
#ifndef PDX
#define PDX 0
#endif
constexpr int NU = 2 * NSEG;
#ifndef PSF
#define PSF 24
#endif
#ifndef PSR
#define PSR 6
#endif
#ifndef NRD
#define NRD 4
#endif
#ifndef RPA
#define RPA 8
#endif
#ifndef GROT
#define GROT 1
#endif
#ifndef LAGK
#define LAGK 5
#endif
#ifndef LAGP
#define LAGP 0
#endif
#ifndef N2F
#define N2F 45
#endif
#ifndef N2M
#define N2M 8
#endif
DEVI unsigned unit_ls(unsigned u, int two) {
    static_assert(NSEG == 64 && LAGD >= 6 && N2F + 2 * N2M <= 65 - N2H, "");
    static_assert(LAGP % 2 == 0 && LAGP < 2 * LAGK + 1, "");
    const unsigned d = u - LAGD + LAGP, p = d / (2 * LAGK + 1), r = d % (2 * LAGK + 1), n0 = LAGD - LAGP / 2 + p * (LAGK + 1) + (r >> 1);
    const unsigned n = u < LAGD || n0 >= 64 ? u : ((r & 1) || r == 2 * LAGK) ? n0 : 64 + u - n0;
    return (two ? (u < N2M ? 64 + N2F + 2 * u : NU - N2H - N2M + u) : n < 64 + N2F ? n : n + min(n - (63 + N2F), (unsigned)N2M)) & 127;
}
DEVI unsigned unit_ent(unsigned u, int two) { const unsigned n = unit_ls(u, two); return n | (u > LAGD && n < 64) << 7; }
DEVI unsigned unit_tab(int lane, int two) { return opq(unit_ent(2 * lane, two) | unit_ent(2 * lane + 1, two) << 8); }
DEVI unsigned unit_get(unsigned tab, int u) { return ((unsigned)__builtin_amdgcn_readlane(tab, (u >> 1) & 63) >> ((u & 1) * 8)) & 255; }
struct WMG { static constexpr int NT = 3, NP = 16, RB = 0, LB = 0, CBI = 0; static constexpr int kind(int, int) { return 0; } };
struct WMD { static constexpr int NT = 4, NP = 12, RB = 0, LB = 0, CBI = 0; static constexpr int kind(int, int) { return 0; } };
namespace lg { constexpr int L_ST = 155648, L_OK = 159744; }
DEVI void role_GU(const Args& a, int w, int j, int two) {
    if (!two) { TSC(125, 2); TSW(125, 3); }
    asm volatile("; MARK role_GU"); __builtin_amdgcn_s_waitcnt(0);
    using namespace lg;
    const int lane = lane_id(), c = lane & 15, g = lane >> 4;
    const rsrc_t rs = mkrs(a.ws, WS_NEED);
    const unsigned lane16 = lane * 16, hc = 96 * j + 24 * w;
    {
        const unsigned ta = g * 4096 + (lane & 1) * 2048 + w * 256 + ((lane >> 2) & 3) * 64 + ((lane >> 1) & 1) * 8, tb = ta + 65536;
        const rsrc_t r1 = mkrs(a.Wg, WSZ(7168u * 1024)), r2 = mkrs(a.Wu, WSZ(7168u * 1024));
        const unsigned cb = 192 * j + g * 48 + (lane & 3) * 16, v1 = (lane & 3) == 3 || cb >= 7168 ? OOB : (8 * w + ((lane >> 2) & 3)) * 7168 + cb, lw = opqs(w * 4096), z = opqs(0);
        load_blocks<32, 8, 4, true>([&](auto B, auto S) { dma_blk(lw + S.v * 16384, r1, v1, z + B.v * 32 * 7168, 1792); dma_blk(lw + S.v * 16384 + 2048, r2, v1, z + B.v * 32 * 7168, 1792); },
                                    [&](auto B, auto S) { sfor<0, 3>([&](auto T) { constexpr int o = (S.v & 3) * 16384 + T.v * 16; put_frag<WMG, T.v, B.v, o, o + 1024>(S.v < 4 ? ta : tb, 0, nullptr); }); });
        if (!two) TSW(120 - 65536, 0);
        v4u one{0x3F803F80u, 0x3F803F80u, 0x3F803F80u, 0x3F803F80u};
        asm volatile("s_nop 1" : "+v"(one));
        f32x4 cs[3];
        sfor<0, 96>([&](auto N) { constexpr int t = N.v % 3, ks = N.v / 3; mfA<8 * widx<WMG>(t, ks >> 1) + 4 * (ks & 1), ks == 0>(cs[t], one); });
        nop<7>(); fence(cs[0], cs[1], cs[2]);
        sfor<0, 3>([&](auto T) { mwr4<384 + 4 * T.v>(__builtin_bit_cast(v4u, cs[T.v])); });
        BAR;
    }
    const unsigned nf = two ? O_N + PG2 + (((two >> 1) ^ FSIDE) & 1) * 8192 : side(rs, FSIDE);
    const unsigned wlo = (unsigned)(uintptr_t)a.ws, whi = (unsigned)((uintptr_t)a.ws >> 32);
    const unsigned xr = w * 8192 + (GROT ? (j & 1) * 4096 + (j & 2) * 16384 : 0);
    const v8u pv{lane16, O_SX + (8 * w + g) * 256 + c * 16, O_H + (g & 1) * 114688 + hc * 32 + c * 16 + (g >> 1) * 8, lane == 0 ? na(nf, N_PG) + j * 128 + w * 4 : OOB,
                 (unsigned)(L_ST + ((lane & 1) * 16 + ((lane & 31) >> 1)) * 16), L_ST + lane16 + w * 4, unit_tab(lane, two), lane16};
    const v16u s0{wlo, whi, RSZ, 0x20000u, wlo, whi, 0u, 0x20000u, OOB, OOB, hc < 3584 ? 0u : OOB, hc + 8 < 3584 ? 0u : OOB, 0u, O_XM + xr, 65536 + xr, 0u};
    const v4u s1{0u, O_PROF + ((blockIdx.x & 255) * 4 + w) * 8192, (unsigned)(two ? N2M : NU - N2M - N2H), (unsigned)(two & 1)};
    asm volatile(G_LOOP :: "{v[0:7]}"(pv), "{s[8:23]}"(s0), "{s[24:27]}"(s1), "{s28}"(two ? N2M + N2H : NU - N2M) : "memory", "s99", "vcc");
}
DEVI void role_DN(const Args& a, int w, int p, int cg, int two) {
    asm volatile("; MARK role_DN"); __builtin_amdgcn_s_waitcnt(0);
    using namespace lg;
    const int lane = lane_id();
    const rsrc_t rs = mkrs(a.ws, WS_NEED);
    const unsigned lane16 = lane * 16;
    const int F0 = p < 3 ? 23 * p : (p == 3 ? 68 : 89), zb = p < 2 ? -1 : (p == 4 ? 0 : 22);
    {
        const unsigned ta = w * 18432 + (lane >> 4) * 512 + ((lane >> 2) & 3) * 64 + (lane & 3) * 16;
        const rsrc_t r1 = mkrs(a.Wd, WSZ(3584u * 2048));
        const unsigned v1 = (F0 * 32 + (lane >> 2)) * 2048 + cg * 512 + w * 128 + (lane & 3) * 16;
        load_blocks<46, 9, 2>([&](auto B, auto S) { dma_blk(w * 18432 + S.v * 2048, r1, (B.v >> 1) == zb ? OOB : v1, (B.v >> 1) * 65536 + (B.v & 1) * 64, 2048); },
                              [&](auto B, auto S) { constexpr int ks = B.v >> 1, t = 2 * (B.v & 1), o = S.v * 2048;
                                                    put_frag<WMD, t, ks, o, o + 256>(ta, 0, nullptr); put_frag<WMD, t + 1, ks, o + 8, o + 264>(ta, 0, nullptr); });
        if (!two) TSW(120 - 65536, 0);
        BAR;
    }
    const int g0 = F0 / 3 * 4, gn = ((F0 + 22) / 3 + 1) * 4 - g0;
    const unsigned nf = two ? O_N + PG2 + (((two >> 1) ^ FSIDE) & 1) * 8192 : side(rs, FSIDE);
    const unsigned wlo = (unsigned)(uintptr_t)a.ws, whi = (unsigned)((uintptr_t)a.ws >> 32);
    const v8u pv{lane16, na(nf, N_PG) + g0 * 32 + (lane < gn ? (lane >> 2) * 128 + (lane & 3) * 4 : 0), lane == 0 ? na(nf ^ PDX, N_PD) + (cg * NKF + p) * 128 + w * 4 : OOB,
                 unit_tab(lane, two), lane16, (unsigned)(L_OK + lane * 4), (unsigned)((lane & 8) * 2048 + (lane >> 4) * 128 + (lane & 7) * 16), 0u};
    const v16u s0{wlo, whi, RSZ, 0x20000u, wlo, whi, 0u, 0x20000u, wlo, whi, w == 0 ? RSZ : 0u, 0x20000u, 0u, O_H + (w >> 1) * 114688 + (F0 + (w & 1) * 12) * 1024,
                  O_D + p * 65536 + (cg * 8 + w * 2) * 512, (unsigned)(65536 + (w >> 1) * 32768 + (w & 1) * 12288)};
    const v4u s1{(unsigned)(L_OK + w * 256), 0u, 0u, O_PROF + ((blockIdx.x & 255) * 4 + w) * 8192};
    asm volatile(D_LOOP :: "{v[0:7]}"(pv), "{s[8:23]}"(s0), "{s[24:27]}"(s1),
                 "{s[28:31]}"(v4u{(unsigned)(two ? N2M : NU - N2M - N2H), O_H + (two & 1) * 114688 + (F0 + 6 * w) * 1024, O_D + p * 65536 + (two & 1) * 32768 + (cg * 8 + w * 2) * 512, (unsigned)(two ? N2M + N2H : NU - N2M)}) : "memory", "s99", "vcc");
}
template <int R> DEVI void ldv1(rsrc_t rs, unsigned vo, unsigned so) { asm volatile("buffer_load_dword v[%3], %0, %1, %2 offen sc1" :: "v"(vo), "s"(rs), "s"(so), "n"(R) : "memory"); }
template <int R, int I> DEVI void unp2(f32x4& d) { float x, y; asm volatile("v_lshlrev_b32 %0, 16, v[%2]\n\tv_and_b32 %1, 0xffff0000, v[%2]" : "=v"(x), "=v"(y) : "n"(R)); d[I] = x; d[I + 1] = y; }
DEVI void role_RED(const Args& a, int w, int cu, unsigned nf) {
    asm volatile("; MARK role_RED"); __builtin_amdgcn_s_waitcnt(0);
    const int lane0 = lane_id(), c = lane0 & 15, g = lane0 >> 4;
    const rsrc_t rs = mkrs(a.ws, WS_NEED);
    v4u sel[2];
#pragma unroll
    for (int e = 0; e < 2; ++e)
#pragma unroll
        for (int i = 0; i < 4; ++i) sel[e][i] = (g == (c >> 2) && i == 2 * e + ((c >> 1) & 1)) ? ((c & 1) ? 0x3F800000u : 0x3F80u) : 0u;
    const unsigned vfl = na(nf ^ PDX, N_PD) + w * (NKF * 128) + (lane0 < 20 ? (lane0 >> 2) * 128 + (lane0 & 3) * 4 : 0);
    constexpr int VP = NV + 32;
    const unsigned lo = (c & 8) * 128 + g * 256 + (c & 7) * 16 + (cu & 1) * 128 + w * 8192, ld = (c & 8) * 64 + g * 128 + (c & 7) * 16 + (cu & 1) * 16384 + w * 4096;
    const unsigned lout = ((cu & 1) * 8 + (c & 7)) * 2048 + w * 512 + (c & 8) * 8 + g * 16, lw = g == (c >> 2) ? (c & 7) * 64 + w * 16 + (c & 8) : 1024 + lane0 * 8;
    const rsrc_t ro = mkrs(a.out, 4u << 20);
    v4u one{0x3F803F80u, 0x3F803F80u, 0x3F803F80u, 0x3F803F80u};
    asm volatile("" : "+v"(one));
    #pragma unroll
    for (int i = 0; i < 2; ++i) if (2 * lane0 + i < NU - N2M) *LDSP(unsigned char, 2048 + unit_ls(2 * lane0 + i, 0)) = 2 * lane0 + i;
    if (lane0 < N2M) *LDSP(unsigned char, 2048 + unit_ls(lane0, 1)) = 128 + lane0;
    WL0; BAR;
    const unsigned tab = opq(*LDSP(const unsigned short, 2048 + 2 * lane0));
    auto place = [&](int k) { return k >= 4 * (NU - N2H) && (k & 2) ? N2M + N2H + (k >> 2) : unit_get(tab, k >> 2); };
    auto next = [&](int k) { return k >= 256 ? k + 32 : k + NRD < 256 ? k + NRD : 4 * NU; };
    auto issue = [&](auto B, auto N0, auto N1, int k) {
        const unsigned ls = k >> 2, base = ((k >> 1) & 1) * 32768;
        sfor<N0.v, N1.v>([&](auto N) { constexpr int f = (N.v - 4) / 5, p = (N.v - 4) % 5;
            if constexpr (N.v < 4) ldv4<NV + 16 * B.v + 4 * N.v>(rs, lo, O_XM + ls * 65536 + base + N.v * 2048);
            else lda<80 * B.v + 20 * f + 4 * p, 4, f * 1024>(rs, ld, O_D + (ls * NKF + p) * 65536 + base); });
        if constexpr (N1.v == 24) { const int k2 = next(k); ldv1<VP>(rs, vfl, k2 < 4 * NU ? (place(k2) >> 7) * PG2 : 0u); }
    };
    u64 late = 1, far = 1;
    bool pre = false, last = false;
    unsigned tries = 0;
    auto task = [&](auto B, int k) {
        constexpr int b = B.v;
        const int ut = place(k), u = ut & 127, rt = (k >> 1) & 1, kn = next(k), un = kn < 4 * NU ? place(kn) : 127, nx = (un & 127) + 1;
        const unsigned sf = (ut >> 7) * PG2, sfn = (un >> 7) * PG2;
        const unsigned ls = k >> 2, tile = ls * 2 + rt;
        TSD; TSR(0);
        if (!pre) {
            if (late) {
                unsigned spins = 0;
#pragma nounroll
                for (;;) {
                    if (far) __builtin_amdgcn_s_sleep(PSF); else __builtin_amdgcn_s_sleep(PSR);
                    CB;
                    const int v = (int)ld4(rs, vfl, sf);
                    if (__builtin_amdgcn_sicmp(v, u + 1, 40) == 0) break;
                    far = __builtin_amdgcn_sicmp(v, u, 40);
                    spin_bail(spins);
                }
            }
            TSR(1);
            issue(B, IC<0>{}, IC<24>{}, k);
        }
        else { TSR(1); }
        const bool l0 = ls < 64;
        const rsrc_t rd = l0 ? rs : ro;
        const unsigned vd = opq((l0 ? O_XIN + XB + lo : lout - 64 * 65536) + tile * 32768), fs = l0 ? 2048 : 128;
        v4u x[4];
        f32x4 d[4][2], S, Q;
        auto unp = [&](auto F, auto I) { unp2<NV + 16 * b + 4 * F.v + I.v, 2 * (I.v & 1)>(d[F.v][I.v >> 1]); };
        auto fin = [&](auto F) {
            constexpr int f = F.v;
            fence(d[f][0], d[f][1]);
#pragma unroll
            for (int i = 0; i < 4; ++i) x[f][i] = pack2(d[f][i >> 1][2 * (i & 1)], d[f][i >> 1][2 * (i & 1) + 1]);
            asm volatile("" : "+v"(x[f]));
        };
        auto put = [&](auto F) { constexpr int f = F.v; st16s(x[f], rd, vd, f * fs); SB; mfv<f == 0>(S, one, x[f]); mfv<f == 0>(Q, x[f], x[f]); SB; };
        wvm<24>();
        sfor<0, 4>([&](auto I) { unp(IC<0>{}, I); });
        sfor<0, 4>([&](auto F) {
            constexpr int f = F.v;
            wvm<16 - 5 * f + (f ? f - 1 : 0)>();
            nop<1>();
            sfor<0, 10>([&](auto N) {
                mfB<80 * b + 20 * f + 4 * (N.v >> 1), false>(d[f][N.v & 1], sel[N.v & 1]);
                if constexpr (N.v < 4 && f < 3) unp(IC<(f + 1) & 3>{}, N);
                if constexpr (N.v == 5 && f > 0) fin(IC<(f + 3) & 3>{});
                SB;
            });
            if constexpr (f > 0) put(IC<(f + 3) & 3>{});
        });
        wvm<3>();
        TSR(2);
#ifdef TS
        tsr[4] = pre + 1;
#endif
        const bool p1 = ltv<VP>(nx) == 0;
        if (!p1) ldv1<VP + 1>(rs, vfl, sfn);
        nop<3>();
        fin(IC<3>{});
        nop<1>();
        put(IC<3>{});
        auto second = [&](auto NS) {
            wvm<NS.v>();
            pre = ltv<VP + 1>(nx) == 0;
            if (pre) { issue(IC<1 - b>{}, IC<0>{}, IC<24>{}, kn); if constexpr (NS.v == 5) wvm<25>(); }
            else {
                if constexpr (NS.v == 5) { ldv1<VP + 1>(rs, vfl, sfn); WV0; }
                late = ltv<VP + 1>(nx); far = ltv<VP + 1>(nx - 1);
            }
        };
        if (l0) {
            nop<7>();
            fence(S, Q);
            const float qd = selm(selm(Q[0], Q[1], 0xAAAAAAAAAAAAAAAAull), selm(Q[2], Q[3], 0xAAAAAAAAAAAAAAAAull), 0xCCCCCCCCCCCCCCCCull);
            *LDSP(f32x2, lw) = f32x2{S[0], qd};
            if (p1) issue(IC<1 - b>{}, IC<0>{}, IC<RPA>{}, kn);
            f32x2 xf[4][4];
#pragma unroll
            for (int f = 0; f < 4; ++f)
#pragma unroll
                for (int i = 0; i < 4; ++i) { xf[f][i] = f32x2{bflo(x[f][i]), bfhi(x[f][i])}; asm volatile("" : "+v"(xf[f][i])); }
            TSR(5);
            WL0; BAR;
            f32x4 t[4];
#pragma unroll
            for (int i = 0; i < 4; ++i) t[i] = __builtin_bit_cast(f32x4, ldsq((c & 7) * 64, i * 16));
            const f32x4 t4 = (t[0] + t[1]) + (t[2] + t[3]);
            const float mean = (t4[0] + t4[2]) * (1.0f / 1024.0f), var = fmaxf(rbf(fmaf(t4[1] + t4[3], 1.0f / 1024.0f, -mean * mean)), 0.f);
            const float inv = __builtin_amdgcn_rcpf(rbf(__builtin_amdgcn_sqrtf(rbf(var + 1e-5f)))), mi = -rbf(mean) * inv;
#pragma unroll
            for (int f = 0; f < 4; ++f) {
                v4u o;
#pragma unroll
                for (int i = 0; i < 4; ++i) xf[f][i] = __builtin_elementwise_fma(xf[f][i], f32x2{inv, inv}, f32x2{mi, mi});
#pragma unroll
                for (int i = 0; i < 4; ++i) o[i] = pack2(xf[f][i][0], xf[f][i][1]);
                st16(o, rs, vd + (O_XN - O_XIN) + f * 2048);
            }
            TSR(6);
            if (p1) { issue(IC<1 - b>{}, IC<RPA>{}, IC<24>{}, kn); TSR(7); wvm<25 - RPA>(); pre = true; } else second(IC<5>{});
            BAR;
            st4(0u, rs, lane0 < 2 && w == 0 ? na(nf ^ (FSIDE ? 0 : 8192), N_FX) + tile * 16 + (cu & 1) * 8 + lane0 * 4 : OOB, 0);
        }
        else if (k >= 4 * (NU - N2H)) {
            nop<7>(); fence(S);
            pre = false; late = far = 1;
            if (!last && __builtin_amdgcn_fcmpf(S[0], S[0], 8) != 0) { last = __builtin_amdgcn_sicmp((int)ld4(rs, vfl + 16, sf), u + 1, 40) == 0; spin_bail(tries); late = 0; return k; }
        }
        else if (p1) { issue(IC<1 - b>{}, IC<0>{}, IC<24>{}, kn); pre = true; }
        else second(IC<1>{});
        TSR(3); TSF(cu > 255 ? 64 + (k >> 5 & 7) : u, 0xFF);
        return kn;
    };
#pragma nounroll
    for (int k = cu;;) {
        k = task(IC<0>{}, k);
        if (k >= 4 * NU) break;
        k = task(IC<1>{}, k);
        if (k >= 4 * NU) break;
    }
}
#ifndef LSW
#define LSW 0
#endif
#ifndef FNG
#define FNG 0x34344776u
#endif
#ifndef FNS
#define FNS 0x00000112u
#endif
#ifndef DLY_A
#define DLY_A 250
#endif
#ifndef DLY_B
#define DLY_B 0
#endif
#ifndef DLY_1
#define DLY_1 1600
#endif
#ifndef DLY_G
#define DLY_G 1400
#endif
#ifndef DLY_D
#define DLY_D 1600
#endif
__global__ __launch_bounds__(256, 1) __attribute__((amdgpu_num_vgpr(NV))) void mega6(Args a) {
    asm volatile("" ::: "v255", "a255");
    const int w = __builtin_amdgcn_readfirstlane(threadIdx.x >> 6), b = blockIdx.x, xcd = b & 7, slot = b >> 3, l = (xcd >> 2) ^ LSW, idx = (slot & 7) * 4 + (xcd & 3);
    const int t0 = (int)__builtin_amdgcn_s_memrealtime();
    if (b < 256) { TSW(127, 0); TSC(125, 0); TSW(125, 1); }
    const int f = slot - 24, two = b < 256 ? 0 : 1 + ((b >> 8) & 2), k2 = b & 255;
    const int ia = l || !N2M || idx >= 26 ? -1 : idx < 20 ? 38 + (idx & 3) * 5 + (idx >> 2) : 12 + idx, ib = l || !N2M ? -1 : (idx & 3) * 8 + (idx >> 2);
    const int ng = (FNG >> 4 * xcd) & 15, ns = (FNS >> 4 * xcd) & 15, below = (1 << 4 * xcd) - 1;
    auto nib = [](unsigned x) { x = (x & 0x0F0F0F0F) + ((x >> 4) & 0x0F0F0F0F); return (int)((x * 0x01010101) >> 24); };
    int gu = f < 0 || (xcd >= 3 && f >= 4) ? -1 : f < ng ? nib(FNG & below) + f : -2, dn = (xcd - 3) * 4 + f - 4, cu = gu == -2 && f < ng + ns && !two ? nib(FNS & below) + f - ng : -1;
    if (two) { gu = k2 < 38 ? k2 : -1; dn = k2 - 38; }
    if (slot < 24 && l && (slot < 16 || idx >= LN_NOW)) pull_all(a, w, (slot >> 3) * 32 + idx - (slot < 16 ? 0 : LN_NOW), t0);
    if (slot < 8) { hold(t0, l ? DLY_1 : DLY_A); role_A(a, w, l, idx, (xcd & 3) * 2, ia); }
    else if (slot < 16) { hold(t0, l ? DLY_1 : DLY_B); role_B(a, w, l, idx, (xcd & 3) * 2, ib); }
    else if (gu >= 0) {
        const int q = gu * 4 + w;
        if (!two) use(ld4(mkrs(a.ws, WS_NEED), q < 2 * NVK + 2 ? prb(q >> 1, q & 1) + lane_id() * 32 : OOB, 0));
        hold(t0, two ? 0 : DLY_G); role_GU(a, w, gu, two);
    }
    else if (slot < 24 || cu >= 0) {
        unsigned nf;
        if (slot < 24) {
            nf = role_R(a, w, l, idx, t0) ^ (FSIDE * 8192); cu = 256 + idx;
            if (l) { if (idx + w == 0) vote_end(mkrs(a.ws, WS_NEED)); return; }
        } else {
            hold(t0, DLY_D); nf = side(mkrs(a.ws, WS_NEED), FSIDE);
        }
        role_RED(a, w, cu, nf);
    }
    else if (gu == -2) arm_last(a, (xcd >> 1 & 1) * 4 + w, 8, t0);
    else { if (xcd < 7 && !two) vote(mkrs(a.ws, WS_NEED), w, dn, l, t0); hold(t0, two ? 0 : DLY_D); role_DN(a, w, dn >> 2, dn & 3, two); }
}
extern "C" void kernel_launch(void* const* d_in, const int* in_sizes, int n_in, void* d_out, int out_size, void* d_ws, size_t ws_size, hipStream_t stream) {
    (void)in_sizes; (void)n_in; (void)out_size; (void)ws_size;
    (void)hipFuncSetAttribute((const void*)mega6, hipFuncAttributeMaxDynamicSharedMemorySize, 163840);
    d_ws = (void*)(((uintptr_t)d_ws + (2u << 20) - 1) & ~(uintptr_t)((2u << 20) - 1));
    Args a{(const u16*)d_in[0], (const u16*)d_in[1], (const u16*)d_in[2], (const u16*)d_in[3], (const u16*)d_in[4], (const u16*)d_in[5],
           (const u16*)d_in[8], (const u16*)d_in[9], (const u16*)d_in[10], (char*)d_ws, (u16*)d_out};
    mega6<<<256, 256, 163840, stream>>>(a);
}
```

```cpp
#include <hip/hip_runtime.h>
#include <stdint.h>

typedef unsigned short u16;
typedef unsigned long long u64;
typedef __attribute__((ext_vector_type(8))) __bf16 bf8;
typedef __attribute__((ext_vector_type(16))) __bf16 bf16v;
typedef __attribute__((ext_vector_type(4))) float f32x4;
typedef __attribute__((ext_vector_type(16))) float f32x16;
typedef __attribute__((ext_vector_type(2))) float f32x2;
typedef __attribute__((ext_vector_type(8))) unsigned v8u;
typedef __attribute__((ext_vector_type(4))) unsigned v4u;
typedef __attribute__((ext_vector_type(3))) unsigned v3u;
typedef __attribute__((ext_vector_type(2))) unsigned v2u;
typedef __amdgpu_buffer_rsrc_t rsrc_t;
typedef __attribute__((address_space(3))) char lds_char;
#define DEVI __device__ __forceinline__
#define LDSP(T, p) ((__attribute__((address_space(3))) T*)(p))

typedef __attribute__((ext_vector_type(2))) __bf16 bf16x2_t;
DEVI unsigned pack2(float lo, float hi) { f32x2 v = {lo, hi}; bf16x2_t b = __builtin_convertvector(v, bf16x2_t); return __builtin_bit_cast(unsigned, b); }
DEVI float bflo(unsigned u) { return __uint_as_float(u << 16); }
DEVI float bfhi(unsigned u) { return __uint_as_float(u & 0xffff0000u); }
DEVI float rbf(float x) { return bflo(pack2(x, x)); }
constexpr int NV = 112;
#define MR_(R, A_STR, V_STR, OUTS, INS, W) do { if constexpr ((R) < 256) asm volatile(A_STR : OUTS : INS, [lo] "n"(R), [hi] "n"((R) + (W) - 1)); \
                                                else asm volatile(V_STR : OUTS : INS, [lo] "n"((R) - 256 + NV), [hi] "n"((R) - 256 + NV + (W) - 1)); } while (0)
#define CM_ ,
template <int R> DEVI void mwr(unsigned x) { if constexpr (R < 256) asm volatile("v_accvgpr_write_b32 a[%1], %0" :: "v"(x), "n"(R)); else asm volatile("v_mov_b32 v[%1], %0" :: "v"(x), "n"(R - 256 + NV)); }
template <int R> DEVI void mwr4(v4u x) { mwr<R>(x[0]); mwr<R + 1>(x[1]); mwr<R + 2>(x[2]); mwr<R + 3>(x[3]); }
#define M16 "v_mfma_f32_16x16x32_bf16 "
#define M32 "v_mfma_f32_32x32x16_bf16 "
#define S16 "v_smfmac_f32_16x16x64_bf16 "
template <int R, bool Z> DEVI void mfA(f32x4& d, v4u b) {
    if constexpr (Z) MR_(R, M16 "%[d], a[%[lo]:%[hi]], %[b], 0", M16 "%[d], v[%[lo]:%[hi]], %[b], 0", [d] "=&v"(d), [b] "v"(b), 4);
    else MR_(R, M16 "%[d], a[%[lo]:%[hi]], %[b], %[d]", M16 "%[d], v[%[lo]:%[hi]], %[b], %[d]", [d] "+v"(d), [b] "v"(b), 4);
}
template <int R, bool Z> DEVI void mfB(f32x4& d, v4u a) {
    if constexpr (Z) MR_(R, M16 "%[d], %[a], a[%[lo]:%[hi]], 0", M16 "%[d], %[a], v[%[lo]:%[hi]], 0", [d] "=&v"(d), [a] "v"(a), 4);
    else MR_(R, M16 "%[d], %[a], a[%[lo]:%[hi]], %[d]", M16 "%[d], %[a], v[%[lo]:%[hi]], %[d]", [d] "+v"(d), [a] "v"(a), 4);
}
template <int R, bool Z> DEVI void mf32A(f32x16& d, v4u b) {
    if constexpr (Z) MR_(R, M32 "%[d], a[%[lo]:%[hi]], %[b], 0", M32 "%[d], v[%[lo]:%[hi]], %[b], 0", [d] "=&v"(d), [b] "v"(b), 4);
    else MR_(R, M32 "%[d], a[%[lo]:%[hi]], %[b], %[d]", M32 "%[d], v[%[lo]:%[hi]], %[b], %[d]", [d] "+v"(d), [b] "v"(b), 4);
}
template <bool Z> DEVI void mfv(f32x4& d, v4u a, v4u b) {
    if constexpr (Z) asm volatile(M16 "%0, %1, %2, 0" : "=&v"(d) : "v"(a), "v"(b)); else asm volatile(M16 "%0, %1, %2, %0" : "+v"(d) : "v"(a), "v"(b));
}
template <int R> DEVI void smf(f32x4& d, v4u a, int idx) { MR_(R, S16 "%[d], %[a], a[%[lo]:%[hi]], %[i]", S16 "%[d], %[a], v[%[lo]:%[hi]], %[i]", [d] "+v"(d), [a] "v"(a) CM_ [i] "v"(idx), 8); }
DEVI void smfv(f32x4& d, v4u a, v8u b, int idx) { asm volatile(S16 "%0, %1, %2, %3" : "+v"(d) : "v"(a), "v"(b), "v"(idx)); }
DEVI void zero4(f32x4& d) { d = f32x4{0.f, 0.f, 0.f, 0.f}; asm volatile("" : "+v"(d)); }
template <int N> DEVI void nop() { asm volatile("s_nop %0" :: "n"(N)); }
DEVI void fence(f32x4& d) { asm volatile("" : "+v"(d)); }
template <class... T> DEVI void fence(f32x4& d, T&... r) { fence(d); fence(r...); }
DEVI float selm(float a, float b, u64 m) { float r; asm("v_cndmask_b32 %0, %1, %2, %3" : "=v"(r) : "v"(a), "v"(b), "s"(m)); return r; }
DEVI int opqs(int x) { asm volatile("" : "+s"(x)); return x; }
DEVI v4u lo4(v8u v) { return __builtin_shufflevector(v, v, 0, 1, 2, 3); }
DEVI v4u hi4(v8u v) { return __builtin_shufflevector(v, v, 4, 5, 6, 7); }
DEVI v8u cat8(v4u a, v4u b) { return __builtin_shufflevector(a, b, 0, 1, 2, 3, 4, 5, 6, 7); }
constexpr int AUX_SC1 = 16;
DEVI v4u ld16(rsrc_t r, unsigned vo, unsigned so) { return __builtin_amdgcn_raw_buffer_load_b128(r, vo, so, AUX_SC1); }
DEVI v2u ld8(rsrc_t r, unsigned vo, unsigned so) { return __builtin_amdgcn_raw_buffer_load_b64(r, vo, so, AUX_SC1); }
DEVI unsigned ld4(rsrc_t r, unsigned vo, unsigned so) { return __builtin_amdgcn_raw_buffer_load_b32(r, vo, so, AUX_SC1); }
DEVI void st16(v4u v, rsrc_t r, unsigned vo) { __builtin_amdgcn_raw_buffer_store_b128(v, r, vo, 0, AUX_SC1); }
DEVI void st16s(v4u v, rsrc_t r, unsigned vo, unsigned so) { __builtin_amdgcn_raw_buffer_store_b128(v, r, vo, so, AUX_SC1); }
DEVI void st8(v2u v, rsrc_t r, unsigned vo, unsigned so) { __builtin_amdgcn_raw_buffer_store_b64(v, r, vo, so, AUX_SC1); }
DEVI void st4(unsigned v, rsrc_t r, unsigned vo, unsigned so) { __builtin_amdgcn_raw_buffer_store_b32(v, r, vo, so, AUX_SC1); }
#define CB asm volatile("" ::: "memory")
#define WL0 asm volatile("s_waitcnt lgkmcnt(0)" ::: "memory")
#define WV0 asm volatile("s_waitcnt vmcnt(0)" ::: "memory")
#define BAR do { __builtin_amdgcn_s_barrier(); CB; } while (0)
#define SB __builtin_amdgcn_sched_barrier(0)
DEVI float fadd(float a, float b) { float r; asm("v_add_f32 %0, %1, %2" : "=v"(r) : "v"(a), "v"(b)); return r; }
DEVI float vfma(float a, float b, float c) { float r; asm("v_fma_f32 %0, %1, %2, %3" : "=v"(r) : "v"(a), "v"(b), "v"(c)); return r; }
DEVI unsigned vxor(unsigned a, unsigned b) { unsigned r; asm volatile("v_xor_b32 %0, %1, %2" : "=v"(r) : "i"(b), "v"(a)); return r; }
DEVI int lane_id() { int r; asm volatile("v_mbcnt_lo_u32_b32 %0, -1, 0\n\tv_mbcnt_hi_u32_b32 %0, -1, %0" : "=v"(r)); __builtin_assume(r >= 0 && r < 64); return r; }
DEVI int opq(int x) { asm volatile("" : "+v"(x)); return x; }
DEVI float vmaxf(float a, float b) { float r; asm("v_max_f32 %0, %1, %2" : "=v"(r) : "v"(a), "v"(b)); return r; }
DEVI void swap32(float& a, float& b) { auto r = __builtin_amdgcn_permlane32_swap(__float_as_uint(a), __float_as_uint(b), false, false); a = __uint_as_float(r[0]); b = __uint_as_float(r[1]); }
DEVI void swap32u(unsigned& a, unsigned& b) { auto r = __builtin_amdgcn_permlane32_swap(a, b, false, false); a = r[0]; b = r[1]; }
DEVI void swap16u(unsigned& a, unsigned& b) { auto r = __builtin_amdgcn_permlane16_swap(a, b, false, false); a = r[0]; b = r[1]; }
DEVI float hadd(float a, float b) { swap32(a, b); return fadd(a, b); }
DEVI float red32(float x) { float y = x; swap32(x, y); return x + y; }
DEVI float red16(float x) { unsigned a = __float_as_uint(x), b = a; swap16u(a, b); return __uint_as_float(a) + __uint_as_float(b); }
DEVI float mx32(float x) { float y = x; swap32(x, y); return vmaxf(x, y); }
DEVI float mx16(float x) { unsigned a = __float_as_uint(x), b = a; swap16u(a, b); return vmaxf(__uint_as_float(a), __uint_as_float(b)); }
template <int N> struct IC { static constexpr int v = N; };
template <int I, int N, class F> DEVI void sfor(F&& f) { if constexpr (I < N) { f(IC<I>{}); sfor<I + 1, N>(f); } }
#ifndef NSEG
#define NSEG 64
#endif
#ifndef LAGD
#define LAGD 10
#endif
constexpr float SC_L2E = 0.08838834764831845f * 1.4426950408889634f;
constexpr unsigned SENT = 0xFFFFFFFFu;
#ifndef NUS_
#define NUS_ 8
#endif
#ifndef NRG_
#define NRG_ 8
#endif
#ifndef N2H
#define N2H 3
#endif
#ifndef NC
#define NC 8
#endif
constexpr int NREP = 8, NUS = NUS_, NRG = NRG_, NKF = 5, NGU = 38;
constexpr unsigned XB = 4u << 20;
constexpr unsigned O_XIN = 0;
constexpr unsigned O_XN  = O_XIN + 2 * XB;
constexpr unsigned O_XM  = O_XN + 2 * XB;
constexpr unsigned O_H   = O_XM + 2 * XB;
constexpr unsigned O_D   = O_H + 2u * 64 * 224 * 1024;
constexpr unsigned O_N = O_D + 2u * 64 * NKF * 65536;
constexpr unsigned DV(unsigned o) { return ((o & ~8191u) << 1) | (o & 8191u); }
DEVI unsigned na(unsigned nb, unsigned o) { return (DV(o) + nb) ^ ((o >> 7) & 8192u); }
constexpr unsigned N_PM  = 0;
constexpr unsigned N_PMM = N_PM + NRG * 16384;
constexpr unsigned N_PA  = (1u << 20) - 16384;
constexpr unsigned N_MEM = 1u << 20;
constexpr unsigned N_WM  = 2u << 20;
constexpr unsigned N_PTX = 6u << 20;
constexpr unsigned N_U   = 10u << 20;
static_assert(NRG == 8 && NUS == 8 && NREP == 8 && O_N % (4u << 20) == 0, "");
constexpr unsigned O_FLG_END = O_N + DV(N_MEM + NREP * 16384);
constexpr unsigned N_FX  = N_PA - 8192;
constexpr unsigned N_PG  = N_FX - 8192;
constexpr unsigned N_PD  = N_PG + 40 * 128;
constexpr unsigned PG2 = DV(N_PA + 8192 - N_PG);
constexpr unsigned O_PRB = O_N + (512u << 10), O_CAL = O_N + (768u << 10);
constexpr unsigned O_SX  = O_N + DV(N_PG) - 2u * 64 * 32 * 256;
constexpr unsigned O_FLG = O_SX;
static_assert(O_PRB >= O_N + DV(N_PMM + NRG * 8192) && O_FLG >= O_CAL + 8192 && (O_FLG_END - O_FLG) % 65536 == 0, "");
constexpr unsigned OOB = 0x80000000u;
constexpr unsigned WS_NEED = O_N + DV(N_U + NUS * (2u << 20));
constexpr unsigned O_PROF = 240u << 20;
static_assert(WS_NEED <= (200u << 20), "workspace too large");

#ifdef DRY
#define WSZ(x) 0u
#else
#define WSZ(x) (x)
#endif
struct Args { const u16 *x, *mem0, *Wq, *Wk, *Wv, *Wo, *Wg, *Wu, *Wd; char* ws; u16* out; };
#ifndef SPIN_LOG
#define SPIN_LOG 18
#endif
constexpr unsigned SPIN_CAP = 1u << SPIN_LOG;
DEVI void spin_bail(unsigned& spins) {
    asm volatile("s_add_u32 %0, %0, 1\n\ts_cmp_lt_u32 %0, %1\n\ts_cbranch_scc1 1f\n\ts_endpgm\n1:" : "+s"(spins) : "n"(SPIN_CAP) : "scc");
}
template <int K> DEVI void wrl(unsigned& v, u64& t) { asm volatile("" : "+s"(t)); asm volatile("v_writelane_b32 %0, %1, %2" : "+v"(v) : "s"((unsigned)t), "n"(K)); }
#ifdef TS
#ifndef TSCLK
#define TSCLK __builtin_amdgcn_s_memrealtime
#endif
#define TSV(u, i, v) __builtin_amdgcn_raw_buffer_store_b32((unsigned)(v), mkrs(a.ws, 256u << 20), lane_id() == 0 ? O_PROF + ((blockIdx.x * 4 + w) * 128 + (u)) * 64 + (i) * 4 : OOB, 0, 0)
#define TSW(u, i) TSV(u, i, TSCLK())
#define TSC(u, i) TSV(u, i, __builtin_amdgcn_s_memtime())
#define TSD u64 tsr[16] = {}
#define TSR(k) tsr[k] = rclk()
#define TSK(k) asm volatile("s_memtime %0" : "=s"(tsr[k]))
#define TSF(u, m) do { unsigned tv_ = 0; WL0; sfor<0, 16>([&](auto K) { if constexpr (((m) >> K.v) & 1) wrl<K.v>(tv_, tsr[K.v]); }); \
    const int tl_ = lane_id(); __builtin_amdgcn_raw_buffer_store_b32(tv_, mkrs(a.ws, 256u << 20), (tl_ < 16 && (((m) >> tl_) & 1)) ? O_PROF + ((blockIdx.x * 4 + w) * 128 + (u)) * 64 + tl_ * 4 : OOB, 0, 0); } while (0)
#if TS == 4
#undef TSV
#undef TSR
#undef TSK
#define TSV(u, i, v)
#define TSR(k)
#define TSK(k)
#define FSK(G, O, D) if constexpr ((G) >= (O) && ((G) - (O)) % (D) == 0 && ((G) - (O)) / (D) < FN) asm volatile("s_memtime %0" : "=s"(tsr[((G) - (O)) / (D)]))
#define FSF(u) TSF(u, (1 << FN) - 1)
#define FSD TSD
#endif
#else
#define TSW(u, i)
#define TSC(u, i)
#define TSV(u, i, v)
#define TSD
#define TSR(k)
#define TSK(k)
#define TSF(u, m)
#endif
#if TS != 4
#define FSK(G, O, D)
#define FSF(u)
#define FSD
#endif
DEVI unsigned rslot(int s) { return (unsigned)(s & (NRG - 1)); }
DEVI u64 pend(v4u d) { return __builtin_amdgcn_uicmp(max(d[0], d[2]), SENT, 32); }
DEVI u64 pend2(v2u d) { return __builtin_amdgcn_uicmp(d[0], SENT, 32); }
DEVI u64 pend1(unsigned d) { return __builtin_amdgcn_uicmp(d, SENT, 32); }
DEVI unsigned nosent(unsigned u) { return min(u, 0xFFFFFFFEu); }
#ifndef WPOL
#define WPOL ""
#endif
template <int OFF, bool SC1> DEVI void dma1(rsrc_t rs, unsigned vo, unsigned so) {
    if constexpr (SC1) asm volatile("buffer_load_dwordx4 %0, %1, %2 offen offset:%3 sc1 lds" :: "v"(vo), "s"(rs), "s"(so), "n"(OFF) : "memory");
    else asm volatile("buffer_load_dwordx4 %0, %1, %2 offen offset:%3 " WPOL " lds" :: "v"(vo), "s"(rs), "s"(so), "n"(OFF) : "memory");
}
DEVI void setm0(unsigned v) { asm volatile("s_mov_b32 m0, %0\n\ts_nop 0" :: "s"(v) : "memory"); }
DEVI void dma4(unsigned lds, rsrc_t rs, unsigned vo, unsigned so) { setm0(lds); dma1<0, true>(rs, vo, so); dma1<1024, true>(rs, vo, so); dma1<2048, true>(rs, vo, so); dma1<3072, true>(rs, vo, so); }
DEVI void dmaw(unsigned lds, rsrc_t rs, unsigned vo, unsigned so) { setm0(lds); asm volatile("buffer_load_dword %0, %1, %2 offen sc1 lds" :: "v"(vo), "s"(rs), "s"(so) : "memory"); }
template <int N> DEVI void wvm() { asm volatile("s_waitcnt vmcnt(%0)" :: "n"(N) : "memory"); }
template <int N> DEVI void wlg() { asm volatile("s_waitcnt lgkmcnt(%0)" :: "n"(N) : "memory"); }
DEVI void dma4p(u64 c0, u64 c1, u64 c2, u64 c3, rsrc_t rs, unsigned vo, unsigned so) {
    asm volatile(
        "s_cmp_eq_u64 %[c0], 0\n\ts_cbranch_scc1 1f\n\t"
        "buffer_load_dwordx4 %[vo], %[rs], %[so] offen sc1 lds\n"
        "1:\n\ts_cmp_eq_u64 %[c1], 0\n\ts_cbranch_scc1 2f\n\t"
        "buffer_load_dwordx4 %[vo], %[rs], %[so] offen offset:1024 sc1 lds\n"
        "2:\n\ts_cmp_eq_u64 %[c2], 0\n\ts_cbranch_scc1 3f\n\t"
        "buffer_load_dwordx4 %[vo], %[rs], %[so] offen offset:2048 sc1 lds\n"
        "3:\n\ts_cmp_eq_u64 %[c3], 0\n\ts_cbranch_scc1 4f\n\t"
        "buffer_load_dwordx4 %[vo], %[rs], %[so] offen offset:3072 sc1 lds\n"
        "4:"
        :: [c0] "s"(c0), [c1] "s"(c1), [c2] "s"(c2), [c3] "s"(c3), [vo] "v"(vo), [rs] "s"(rs), [so] "s"(so)
        : "memory", "scc");
}
#ifndef PSL
#define PSL 8
#endif
DEVI void wait_prog(rsrc_t rs, unsigned vo, unsigned so, int need) {
    unsigned spins = 0;
#pragma nounroll
    for (;;) {
        CB;
        const int v = (int)ld4(rs, vo, so);
        if (__builtin_amdgcn_sicmp(v, need, 40) == 0) return;
        spin_bail(spins);
        __builtin_amdgcn_s_sleep(PSL);
    }
}

typedef __attribute__((ext_vector_type(16))) int v16i;
typedef __attribute__((ext_vector_type(16))) unsigned v16u;
#ifdef TS
constexpr unsigned RSZ = 256u << 20;
#else
constexpr unsigned RSZ = WS_NEED;
#endif
DEVI u64 rclk() { u64 t; asm volatile("s_memrealtime %0" : "=s"(t)); return t; }
DEVI unsigned tsv(u64& t) { asm volatile("s_waitcnt lgkmcnt(0)" : "+s"(t)); return (unsigned)t; }
DEVI unsigned stamp(u64& t, int tag, int lead) { asm volatile("" : "+s"(t)); return (unsigned)tag << 24 | (((unsigned)t + lead) & 0xFFFFFFu); }
#ifndef SXTH
#define SXTH 20
#endif
DEVI int left(int t) { return (int)((unsigned)(t - (int)__builtin_amdgcn_s_memrealtime()) << 8); }
DEVI void hold(int t0, int d) { for (int n = 0; n < 4096 && (int)__builtin_amdgcn_s_memrealtime() - t0 < d; ++n) __builtin_amdgcn_s_sleep(4); }
DEVI int smin(int a, int b) { int r; asm("s_min_i32 %0, %1, %2" : "=s"(r) : "s"(a), "s"(b) : "scc"); return r; }
DEVI int smax(int a, int b) { int r; asm("s_max_i32 %0, %1, %2" : "=s"(r) : "s"(a), "s"(b) : "scc"); return r; }
DEVI void smin8x2(const v16i& f, int& m0, int& m1) {
    asm("s_min_i32 %0, %2, %3\n\ts_min_i32 %1, %10, %11\n\ts_min_i32 %0, %0, %4\n\ts_min_i32 %1, %1, %12\n\ts_min_i32 %0, %0, %5\n\ts_min_i32 %1, %1, %13\n\ts_min_i32 %0, %0, %6\n\ts_min_i32 %1, %1, %14\n\t"
        "s_min_i32 %0, %0, %7\n\ts_min_i32 %1, %1, %15\n\ts_min_i32 %0, %0, %8\n\ts_min_i32 %1, %1, %16\n\ts_min_i32 %0, %0, %9\n\ts_min_i32 %1, %1, %17"
        : "=&s"(m0), "=&s"(m1) : "s"(f[0]), "s"(f[1]), "s"(f[2]), "s"(f[3]), "s"(f[4]), "s"(f[5]), "s"(f[6]), "s"(f[7]),
          "s"(f[8]), "s"(f[9]), "s"(f[10]), "s"(f[11]), "s"(f[12]), "s"(f[13]), "s"(f[14]), "s"(f[15]) : "scc");
}
DEVI void smax8x2(const v16i& f, int& m0, int& m1) {
    asm("s_max_i32 %0, %2, %3\n\ts_max_i32 %1, %10, %11\n\ts_max_i32 %0, %0, %4\n\ts_max_i32 %1, %1, %12\n\ts_max_i32 %0, %0, %5\n\ts_max_i32 %1, %1, %13\n\ts_max_i32 %0, %0, %6\n\ts_max_i32 %1, %1, %14\n\t"
        "s_max_i32 %0, %0, %7\n\ts_max_i32 %1, %1, %15\n\ts_max_i32 %0, %0, %8\n\ts_max_i32 %1, %1, %16\n\ts_max_i32 %0, %0, %9\n\ts_max_i32 %1, %1, %17"
        : "=&s"(m0), "=&s"(m1) : "s"(f[0]), "s"(f[1]), "s"(f[2]), "s"(f[3]), "s"(f[4]), "s"(f[5]), "s"(f[6]), "s"(f[7]),
          "s"(f[8]), "s"(f[9]), "s"(f[10]), "s"(f[11]), "s"(f[12]), "s"(f[13]), "s"(f[14]), "s"(f[15]) : "scc");
}
#ifndef NVK
#define NVK 8
#endif
#ifndef DLY_V
#define DLY_V 500
#endif
#ifndef PSV
#define PSV 12
#endif
#ifndef DLY_P
#define DLY_P 150
#endif
DEVI unsigned prb(int k, int b) { return O_PRB + k * 16384 + b * 8192 + (k & 1) * 4096; }
DEVI void vote(rsrc_t rs, int w, int v, int far, int t0s) {
    const int lane = lane_id();
    constexpr unsigned N = (O_FLG_END - O_FLG) / 65536;
#pragma nounroll
    for (unsigned q = 0; q < N; ++q) st16(v4u{SENT, SENT, SENT, SENT}, rs, O_FLG + ((v * 4 + w) * N + q) * 1024 + lane * 16);
    const v2u e = ld8(rs, O_CAL + (lane & 31) * 256 + 128, 0);
    static_assert(NVK % 4 == 0 && NVK < 12, "");
    int best[2] = {1 << 30, 1 << 30};
    hold(t0s, DLY_P);
#pragma nounroll
    for (int k = 0; k <= NVK / 4; ++k)
#pragma unroll
        for (int b = 0; b < 2; ++b) {
            u64 t0, t1; int d;
            asm volatile("s_memtime %0\n\ts_waitcnt lgkmcnt(0)\n\ts_buffer_load_dword %2, %3, %4 glc\n\ts_waitcnt lgkmcnt(0)\n\ts_memtime %1\n\ts_waitcnt lgkmcnt(0)"
                         : "=&s"(t0), "=&s"(t1), "=&s"(d) : "s"(rs), "s"(prb(k ? 4 * k - w : 0, b) + v * 128) : "memory");
            if (k) best[b] = smin(best[b], (int)(t1 - t0));
        }
    *LDSP(int, (lane & 1) * 16 + w * 4) = best[lane & 1];
    WV0; WL0; BAR;
#pragma unroll
    for (int b = 0; b < 2; ++b) { const v4u t = *LDSP(const v4u, b * 16); best[b] = min(min(t[0], t[1]), min(t[2], t[3])); }
    st8(v2u{((e[0] + 2) & ~1u) | ((best[1] < best[0]) ^ far), e[1] + 1}, rs, lane < 32 && w == 0 ? O_CAL + lane * 256 + v * 8 : OOB, 0);
}
DEVI void vote_end(rsrc_t rs) {
    const unsigned o = O_CAL + (lane_id() & 31) * 256 + 128;
    const v2u e = ld8(rs, o, 0);
    st8(v2u{e[0] + 2, e[1] + 1}, rs, o, 0);
}
DEVI unsigned side(rsrc_t rs, int l) {
    unsigned spins = 0;
#pragma nounroll
    for (;;) {
        v16u f[2]; v2u e;
        const unsigned o = O_CAL + (blockIdx.x & 31) * 256;
        asm volatile("s_buffer_load_dwordx16 %0, %3, %4 glc\n\ts_buffer_load_dwordx16 %1, %3, %5 glc\n\ts_buffer_load_dwordx2 %2, %3, %6 glc\n\ts_waitcnt lgkmcnt(0)"
                     : "=&s"(f[0]), "=&s"(f[1]), "=&s"(e) : "s"(rs), "s"(o), "s"(o + 64), "s"(o + 128) : "memory");
        unsigned bad = 0, sum = 0;
#pragma unroll
        for (int i = 0; i < 16; ++i) { const unsigned x = f[i >> 3][2 * i & 15]; bad |= ((x ^ (e[0] + 2)) >> 1) | (f[i >> 3][(2 * i & 15) + 1] ^ (e[1] + 1)); sum += x & 1; }
#ifdef CSIDE
        sum = CSIDE * 16;
#endif
        if (bad == 0) return opqs(O_N + ((l ^ (sum > 8)) & 1) * 8192);
        spin_bail(spins);
        __builtin_amdgcn_s_sleep(PSV);
    }
}
#ifndef PSH
#define PSH 2
#endif
DEVI int wait_hint(rsrc_t rs, unsigned so, unsigned vo, int tag) {
#pragma nounroll
    for (int n = 0;; ++n) {
        v16i f;
        asm volatile("s_buffer_load_dwordx16 %0, %1, %2 glc\n\ts_waitcnt lgkmcnt(0)" : "=s"(f) : "s"(rs), "s"(so) : "memory");
        int m0, m1;
        smin8x2(f, m0, m1);
        if (smin(m0, m1) >= tag << 24) { smax8x2(f, m0, m1); return smax(m0, m1); }
        if (n == 40) { wait_prog(rs, vo, 0, tag << 24); n = 0; }
        __builtin_amdgcn_s_sleep(PSH);
    }
}
template <class WM> constexpr int widx(int j, int t) {
    int n = 0;
    for (int q = 0; q < j * WM::NP + t; ++q) n += WM::kind(q / WM::NP, q % WM::NP) == WM::kind(j, t);
    return n;
}
template <class WM> constexpr int lds_tile(int t) { for (int j = 0; j < WM::NT; ++j) if (WM::kind(j, t) == 1) return j; return -1; }
DEVI v4u ldsq(unsigned p, int off) { return *LDSP(const v4u, LDSP(char, p) + off); }
DEVI void stsq(unsigned p, int off, v4u v) { *LDSP(v4u, LDSP(char, p) + off) = v; }
DEVI v8u ldpair(unsigned wl, int slot) { return cat8(ldsq(wl, slot * 2048), ldsq(wl, slot * 2048 + 1024)); }
DEVI rsrc_t mkrs(const void* p, unsigned bytes) { return __builtin_amdgcn_make_buffer_rsrc((void*)p, 0, (int)bytes, 0x00020000); }
#ifndef NPL
#define NPL 72
#endif
#ifndef LNN
#define LNN 8
#endif
constexpr int LN_NOW = LNN, NP0 = NPL * 4;
template <int SL = 0> DEVI void pull(const void* p, int n, int q, int nq) {
    const rsrc_t r = mkrs(p, n * 1024);
    const unsigned vo = lane_id() * 16;
#pragma nounroll
    for (; q < n; q += nq) { asm volatile("buffer_load_dwordx4 a[0:3], %0, %1, %2 offen" :: "v"(vo), "s"(r), "s"(q * 1024) : "memory"); if (SL) __builtin_amdgcn_s_sleep(SL); }
}
#ifndef PQ
#define PQ 0
#endif
#ifndef DLY_F
#define DLY_F 1000
#endif
#ifndef PFS
#define PFS 5
#endif
DEVI void pull_all(const Args& a, int w, int q, int t0) {
    q = q * 4 + w;
    if (q < NP0) {
    pull(a.Wv, 8192, q, NP0); pull(a.Wo, 8192, q, NP0); if (!PQ) pull(a.Wq, 8192, q, NP0); pull(a.Wk, 8192, q, NP0);
    }
    if (q >= 256) return;
    hold(t0, DLY_F);
    pull<PFS>(q & 1 ? a.Wg : a.Wu, 7168, q >> 1, 128); pull<PFS>(a.Wd, 7168, q, 256);
}
template <class WM, int j, int ks, int O0, int O1> DEVI void put_frag(unsigned ta, unsigned wl, v8u* Wc) {
    constexpr int t = ks >> 1, f = ks & 1, k = WM::kind(j, t), ix = widx<WM>(j, t);
    if constexpr (k == 0) {
        constexpr int R = WM::RB + 8 * ix + 4 * f;
        if constexpr (R < 256) asm volatile("ds_read_b64_tr_b16 a[%1:%2], %0 offset:%5\n\tds_read_b64_tr_b16 a[%3:%4], %0 offset:%6" :: "v"(ta), "n"(R), "n"(R + 1), "n"(R + 2), "n"(R + 3), "n"(O0), "n"(O1) : "memory");
        else asm volatile("ds_read_b64_tr_b16 v[%1:%2], %0 offset:%5\n\tds_read_b64_tr_b16 v[%3:%4], %0 offset:%6" :: "v"(ta), "n"(R - 256 + NV), "n"(R - 255 + NV), "n"(R - 254 + NV), "n"(R - 253 + NV), "n"(O0), "n"(O1) : "memory");
    } else {
        v2u p, q;
        asm volatile("ds_read_b64_tr_b16 %0, %2 offset:%3\n\tds_read_b64_tr_b16 %1, %2 offset:%4\n\ts_waitcnt lgkmcnt(0)" : "=&v"(p), "=&v"(q) : "v"(ta), "n"(O0), "n"(O1) : "memory");
        const v4u F = v4u{p[0], p[1], q[0], q[1]};
        if constexpr (k == 1) stsq(wl, (WM::LB + ix) * 2048 + f * 1024, F);
        else if constexpr (f == 0) Wc[WM::CBI + ix] = cat8(F, F);
        else Wc[WM::CBI + ix] = cat8(lo4(Wc[WM::CBI + ix]), F);
    }
}
template <int NB, int DEPTH, int NI, bool SHARED = false, int NX = 0, class ISSUE, class PUT>
DEVI void load_blocks(ISSUE&& issue, PUT&& put) {
    sfor<0, DEPTH - 1>([&](auto B) { issue(B, IC<B.v % DEPTH>{}); });
    sfor<0, NB>([&](auto B) {
        constexpr int b = B.v, left = NB + NX - 1 - b < DEPTH - 2 ? NB + NX - 1 - b : DEPTH - 2;
        wvm<NI * left>();
        wlg<0>();
        if constexpr (SHARED) BAR;
        if constexpr (b + DEPTH - 1 < NB + NX) issue(IC<b + DEPTH - 1>{}, IC<(b + DEPTH - 1) % DEPTH>{});
        put(B, IC<b % DEPTH>{});
    });
    wlg<0>();
    if constexpr (SHARED) BAR;
}
DEVI void first_look(unsigned& fx, rsrc_t rs, unsigned nb) { asm volatile("buffer_load_dword %0, %1, %2, %3 offen sc1" : "=v"(fx) : "v"((lane_id() & 7) * 4), "s"(rs), "s"(na(nb, N_FX)) : "memory"); }
DEVI unsigned first_there(unsigned& fx) { asm volatile("" : "+v"(fx)); return pend1(fx) == 0 ? WS_NEED : 0u; }
DEVI void dma_blk(unsigned lds, rsrc_t rw, unsigned vo, unsigned so, unsigned rowb) { setm0(lds); dma1<0, false>(rw, vo, so); setm0(lds + 1024); dma1<0, false>(rw, vo, so + 16 * rowb); }
template <class WM, int NKS, int K0, int K1, int RTS, int LATE1, class FILL>
DEVI void gemm_pass(unsigned xa, unsigned wl, const v8u* Wc, v4u (&x)[2][3], f32x4 (&acc)[WM::NT][2], FILL&& fill) {
    auto rd = [&](auto KS, auto Q) {
        constexpr int ks = KS.v, q = Q.v, jl = lds_tile<WM>(ks >> 1), il = widx<WM>(jl < 0 ? 0 : jl, ks >> 1);
        if constexpr (q == 0 && jl >= 0) x[ks & 1][2] = ldsq(wl, (WM::LB + il) * 2048 + (ks & 1) * 1024);
        if constexpr (q == 1) x[ks & 1][1] = ldsq(xa, RTS + ks * 1024);
        if constexpr (q == 2) x[ks & 1][0] = ldsq(xa, ks * 1024);
    };
    sfor<K0 * 2 * WM::NT, K1 * 2 * WM::NT>([&](auto N) {
        constexpr int ks = N.v / (2 * WM::NT), n = N.v % (2 * WM::NT), j = n >> 1, rt = n & 1, t = ks >> 1, f = ks & 1, k = WM::kind(j, t), ix = widx<WM>(j, t);
        fill(IC<ks>{}, IC<n>{});
        SB;
        const v4u xv = x[ks & 1][rt];
        if constexpr (k == 0) mfA<WM::RB + 8 * ix + 4 * f, ks == 0>(acc[j][rt], xv);
        else if constexpr (k == 1) mfv<ks == 0>(acc[j][rt], x[ks & 1][2], xv);
        else mfv<ks == 0>(acc[j][rt], f ? hi4(Wc[WM::CBI + ix]) : lo4(Wc[WM::CBI + ix]), xv);
        if constexpr (ks + 1 < NKS) rd(IC<ks + 1>{}, IC<n - (ks == 1 ? LATE1 : 0)>{});
        SB;
    });
}
template <class WM> constexpr int next_lds(int n) { for (; n < 16 * WM::NT; ++n) if (WM::kind(2 * (n >> 5) + (n & 1), (n >> 1) & 15) == 1) return n; return -1; }
template <class WM> constexpr int lds_cnt(int n) { int c = 0; for (int q = next_lds<WM>(0); q >= 0 && q < n; q = next_lds<WM>(q + 1)) ++c; return c; }
template <class WM> constexpr int lds_nth(int k) { int q = next_lds<WM>(0); for (; k > 0 && q >= 0; --k) q = next_lds<WM>(q + 1); return q; }
template <class WM, int K, int NB> DEVI void pair_next(v8u (&pb)[NB], unsigned wl) {
    constexpr int n = lds_nth<WM>(K);
    if constexpr (n >= 0) { constexpr int ix = widx<WM>(2 * (n >> 5) + (n & 1), (n >> 1) & 15); pb[K % NB] = ldpair(wl, WM::LB + ix); }
}
template <class WM, int MO, int MR, int NB, class FILL>
DEVI void spass(unsigned ma, unsigned wl, const v8u* Wc, f32x4* e, v4u (&m)[MR], v8u (&pb)[NB], int sidx, FILL&& fill) {
    sfor<0, 16 * WM::NT>([&](auto N) {
        constexpr int n = N.v, t = (n >> 1) & 15, j = 2 * (n >> 5) + (n & 1), k = WM::kind(j, t), ix = widx<WM>(j, t), tn = (t + MR - 1) & 15, r = (n >> 1) % MR;
        fill(N);
        SB;
        if constexpr (k == 0) smf<WM::RB + 8 * ix>(e[j], m[r], sidx);
        else if constexpr (k == 1) { constexpr int q = lds_cnt<WM>(n); smfv(e[j], m[r], pb[q % NB], sidx); pair_next<WM, q + NB>(pb, wl); }
        else smfv(e[j], m[r], Wc[WM::CBI + ix], sidx);
        if constexpr ((n & 1) == 0 && n + 2 * MR - 2 < 16 * WM::NT) m[(r + MR - 1) % MR] = ldsq(ma, MO + (tn >> 3) * 32768 + (tn & 7) * 1024);
        SB;
    });
}
typedef __attribute__((ext_vector_type(4))) short s16x4;
DEVI v2u ldtr(unsigned p, int off) { return __builtin_bit_cast(v2u, __builtin_amdgcn_ds_read_tr16_b64_v4i16(LDSP(s16x4, LDSP(char, p) + off))); }
DEVI v4u ldtr2(unsigned p, int off) { const v2u a = ldtr(p, off), b = ldtr(p, off + 256); return v4u{a[0], a[1], b[0], b[1]}; }
DEVI void sts1(unsigned p, int off, unsigned v) { *LDSP(unsigned, LDSP(char, p) + off) = v; }
DEVI unsigned lds1(unsigned p, int off) { return *LDSP(const unsigned, LDSP(char, p) + off); }
DEVI void use(unsigned v) { asm volatile("" :: "v"(v) : "memory"); }
DEVI int mk_sidx(int lane) { return opq(0x4444 + (lane & 2) * 0x5555); }
DEVI int crow(int r, int hb) { return 4 * (r >> 1) + 2 * hb + (r & 1); }
DEVI v4u again(int id, unsigned nb, int w) { const u64 k = (u64)__builtin_amdgcn_kernarg_segment_ptr(); return v4u{(unsigned)k, (unsigned)(k >> 32), id < 0 ? ~0u : 256 + id + ((nb >> 4) & 512), (unsigned)w * 64}; }
#define A_LDS 0x28a0114002004410ull
#define A_VAR 0x0000000400020001ull
#define A_LOOP \
"s_brev_b32 s99, 1\ns_brev_b32 s98, 1\ns_brev_b32 s97, 1\ns_brev_b32 s96, 1\n" \
"s_branch 4f\n.p2align 6\n1:\ns_waitcnt lgkmcnt(0)\n" \
"v_mfma_f32_16x16x32_bf16 v[44:47], v[76:79], v[80:83], v[44:47]\nds_read_b128 v[36:39], v25 offset:3072\nds_read_b128 v[40:43], v24 offset:21504\nv_mfma_f32_16x16x32_bf16 v[48:51], v[76:79], v[32:35], v[48:51]\n" \
"ds_read_b128 v[84:87], v24 offset:54272\nv_bfe_u32 v31, v24, 6, 3\nv_mfma_f32_16x16x32_bf16 v[52:55], a[160:163], v[80:83], v[52:55]\ns_mov_b32 m0, s50\n" \
"s_add_u32 s99, s30, 2\nv_lshlrev_b32 v31, 2, v31\nv_mfma_f32_16x16x32_bf16 v[56:59], a[160:163], v[32:35], v[56:59]\ns_min_u32 s99, s99, 63\n" \
"s_lshl_b32 s99, s99, 5\ns_add_u32 s99, s99, s48\nv_mfma_f32_16x16x32_bf16 v[60:63], v[120:123], v[80:83], v[60:63]\ns_and_b32 s94, s30, 7\n" \
"s_lshl_b32 s94, s94, 18\ns_add_u32 s94, s94, s33\nv_mfma_f32_16x16x32_bf16 v[64:67], v[120:123], v[32:35], v[64:67]\ns_add_u32 s31, s31, 0x10000\n" \
"v_mfma_f32_16x16x32_bf16 v[68:71], v[224:227], v[80:83], v[68:71]\ns_waitcnt lgkmcnt(1)\nv_mfma_f32_16x16x32_bf16 v[72:75], v[224:227], v[32:35], v[72:75]\ns_waitcnt lgkmcnt(0)\n" \
"ds_read_b128 v[32:35], v24 offset:22528\nv_mfma_f32_16x16x32_bf16 v[44:47], v[36:39], v[40:43], v[44:47]\nds_read_b128 v[76:79], v24 offset:55296\nv_mfma_f32_16x16x32_bf16 v[48:51], v[36:39], v[84:87], v[48:51]\n" \
"v_mfma_f32_16x16x32_bf16 v[52:55], a[164:167], v[40:43], v[52:55]\nds_read_b128 v[36:39], v25 offset:18432\nv_mfma_f32_16x16x32_bf16 v[56:59], a[164:167], v[84:87], v[56:59]\nv_mfma_f32_16x16x32_bf16 v[60:63], v[124:127], v[40:43], v[60:63]\n" \
"v_mfma_f32_16x16x32_bf16 v[64:67], v[124:127], v[84:87], v[64:67]\nv_mfma_f32_16x16x32_bf16 v[68:71], v[228:231], v[40:43], v[68:71]\ns_waitcnt lgkmcnt(1)\nv_mfma_f32_16x16x32_bf16 v[72:75], v[228:231], v[84:87], v[72:75]\n" \
"ds_read_b128 v[40:43], v24 offset:23552\nv_mfma_f32_16x16x32_bf16 v[44:47], a[64:67], v[32:35], v[44:47]\nds_read_b128 v[80:83], v24 offset:56320\nv_mfma_f32_16x16x32_bf16 v[48:51], a[64:67], v[76:79], v[48:51]\n" \
"v_mfma_f32_16x16x32_bf16 v[52:55], a[168:171], v[32:35], v[52:55]\nds_read_b128 v[84:87], v25 offset:19456\nv_mfma_f32_16x16x32_bf16 v[56:59], a[168:171], v[76:79], v[56:59]\nv_mfma_f32_16x16x32_bf16 v[60:63], v[128:131], v[32:35], v[60:63]\n" \
"s_waitcnt lgkmcnt(3)\nv_mfma_f32_16x16x32_bf16 v[64:67], v[128:131], v[76:79], v[64:67]\nv_mfma_f32_16x16x32_bf16 v[68:71], v[36:39], v[32:35], v[68:71]\ns_waitcnt vmcnt(8) lgkmcnt(1)\n" \
"s_barrier\nv_mfma_f32_16x16x32_bf16 v[72:75], v[36:39], v[76:79], v[72:75]\nds_read_b128 v[32:35], v24 offset:24576\nv_mfma_f32_16x16x32_bf16 v[44:47], a[68:71], v[40:43], v[44:47]\n" \
"ds_read_b128 v[36:39], v24 offset:57344\nv_mfma_f32_16x16x32_bf16 v[48:51], a[68:71], v[80:83], v[48:51]\nds_read_b128 v[76:79], v25 offset:12288\nv_mfma_f32_16x16x32_bf16 v[52:55], a[172:175], v[40:43], v[52:55]\n" \
"buffer_load_dword v31, s[8:11], s99 offen offset:0 sc1 lds\nv_mfma_f32_16x16x32_bf16 v[56:59], a[172:175], v[80:83], v[56:59]\ns_add_u32 m0, s32, 0x4000\nv_mfma_f32_16x16x32_bf16 v[60:63], v[132:135], v[40:43], v[60:63]\n" \
"s_waitcnt lgkmcnt(3)\nv_mfma_f32_16x16x32_bf16 v[64:67], v[132:135], v[80:83], v[64:67]\nv_mfma_f32_16x16x32_bf16 v[68:71], v[84:87], v[40:43], v[68:71]\ns_waitcnt lgkmcnt(1)\n" \
"v_mfma_f32_16x16x32_bf16 v[72:75], v[84:87], v[80:83], v[72:75]\nds_read_b128 v[40:43], v24 offset:25600\nv_mfma_f32_16x16x32_bf16 v[44:47], a[72:75], v[32:35], v[44:47]\nds_read_b128 v[80:83], v24 offset:58368\n" \
"v_mfma_f32_16x16x32_bf16 v[48:51], a[72:75], v[36:39], v[48:51]\nds_read_b128 v[84:87], v25 offset:13312\nv_mfma_f32_16x16x32_bf16 v[52:55], a[176:179], v[32:35], v[52:55]\ns_waitcnt lgkmcnt(3)\n" \
"v_mfma_f32_16x16x32_bf16 v[56:59], a[176:179], v[36:39], v[56:59]\nv_mfma_f32_16x16x32_bf16 v[60:63], v[76:79], v[32:35], v[60:63]\nv_mfma_f32_16x16x32_bf16 v[64:67], v[76:79], v[36:39], v[64:67]\nv_mfma_f32_16x16x32_bf16 v[68:71], v[232:235], v[32:35], v[68:71]\n" \
"s_waitcnt lgkmcnt(1)\nv_mfma_f32_16x16x32_bf16 v[72:75], v[232:235], v[36:39], v[72:75]\nbuffer_load_dwordx4 v24, s[8:11], s94 offen offset:0 sc1 lds\nv_mfma_f32_16x16x32_bf16 v[44:47], a[76:79], v[40:43], v[44:47]\n" \
"ds_read_b128 v[32:35], v24 offset:26624\nv_mfma_f32_16x16x32_bf16 v[48:51], a[76:79], v[80:83], v[48:51]\nds_read_b128 v[36:39], v24 offset:59392\nv_mfma_f32_16x16x32_bf16 v[52:55], a[180:183], v[40:43], v[52:55]\n" \
"s_waitcnt lgkmcnt(2)\nbuffer_load_dwordx4 v24, s[8:11], s94 offen offset:1024 sc1 lds\nv_mfma_f32_16x16x32_bf16 v[56:59], a[180:183], v[80:83], v[56:59]\nbuffer_load_dwordx4 v24, s[8:11], s94 offen offset:2048 sc1 lds\n" \
"v_mfma_f32_16x16x32_bf16 v[60:63], v[84:87], v[40:43], v[60:63]\nbuffer_load_dwordx4 v24, s[8:11], s94 offen offset:3072 sc1 lds\nv_mfma_f32_16x16x32_bf16 v[64:67], v[84:87], v[80:83], v[64:67]\nds_read_b128 v[76:79], v25 offset:20480\n" \
"s_and_b32 s93, s30, 15\nv_mfma_f32_16x16x32_bf16 v[68:71], v[236:239], v[40:43], v[68:71]\ns_lshl_b32 s92, s93, 17\ns_add_u32 s92, s92, s35\n" \
"s_and_b32 s93, s93, 7\nv_mfma_f32_16x16x32_bf16 v[72:75], v[236:239], v[80:83], v[72:75]\ns_waitcnt lgkmcnt(1)\nds_read_b128 v[40:43], v24 offset:27648\n" \
"v_mfma_f32_16x16x32_bf16 v[44:47], a[80:83], v[32:35], v[44:47]\nds_read_b128 v[80:83], v24 offset:60416\ns_lshr_b32 s95, s30, s41\nv_mfma_f32_16x16x32_bf16 v[48:51], a[80:83], v[36:39], v[48:51]\n" \
"s_and_b32 s95, s95, 1\ns_lshl_b32 s93, s93, 14\ns_lshl1_add_u32 s99, s93, s34\nv_mfma_f32_16x16x32_bf16 v[52:55], a[184:187], v[32:35], v[52:55]\n" \
"ds_read_b128 v[84:87], v25 offset:21504\ns_or_b32 s98, s92, s36\nv_mfma_f32_16x16x32_bf16 v[56:59], a[184:187], v[36:39], v[56:59]\ns_or_b32 s97, s92, s37\n" \
"s_and_b32 s92, s92, s39\ns_andn2_b32 s93, s93, s39\nv_mfma_f32_16x16x32_bf16 v[60:63], v[136:139], v[32:35], v[60:63]\nds_read_b128 v[88:91], v25 offset:4096\n" \
"s_add_u32 s96, s92, s38\nv_mfma_f32_16x16x32_bf16 v[64:67], v[136:139], v[36:39], v[64:67]\ns_waitcnt lgkmcnt(4)\ns_add_u32 s96, s96, s93\n" \
"s_add_u32 s30, s30, 1\nv_mfma_f32_16x16x32_bf16 v[68:71], v[76:79], v[32:35], v[68:71]\ns_lshl_b32 s93, s30, 24\ns_cmp_le_u32 s30, 64\n" \
"s_cselect_b32 s51, s14, 0\nv_mfma_f32_16x16x32_bf16 v[72:75], v[76:79], v[36:39], v[72:75]\ns_waitcnt lgkmcnt(2)\nds_read_b128 v[32:35], v24 offset:28672\n" \
"v_mfma_f32_16x16x32_bf16 v[44:47], a[84:87], v[40:43], v[44:47]\nds_read_b128 v[36:39], v24 offset:61440\nv_mfma_f32_16x16x32_bf16 v[48:51], a[84:87], v[80:83], v[48:51]\nv_mfma_f32_16x16x32_bf16 v[52:55], a[188:191], v[40:43], v[52:55]\n" \
"v_mfma_f32_16x16x32_bf16 v[56:59], a[188:191], v[80:83], v[56:59]\nv_mfma_f32_16x16x32_bf16 v[60:63], v[140:143], v[40:43], v[60:63]\ns_waitcnt lgkmcnt(2)\nds_read_b128 v[76:79], v25 offset:5120\n" \
"v_mfma_f32_16x16x32_bf16 v[64:67], v[140:143], v[80:83], v[64:67]\nv_mfma_f32_16x16x32_bf16 v[68:71], v[84:87], v[40:43], v[68:71]\ns_waitcnt lgkmcnt(1)\nv_mfma_f32_16x16x32_bf16 v[72:75], v[84:87], v[80:83], v[72:75]\n" \
"ds_read_b128 v[40:43], v24 offset:29696\nv_mfma_f32_16x16x32_bf16 v[44:47], v[88:91], v[32:35], v[44:47]\nds_read_b128 v[80:83], v24 offset:62464\nv_mfma_f32_16x16x32_bf16 v[48:51], v[88:91], v[36:39], v[48:51]\n" \
"v_mfma_f32_16x16x32_bf16 v[52:55], a[192:195], v[32:35], v[52:55]\nv_mfma_f32_16x16x32_bf16 v[56:59], a[192:195], v[36:39], v[56:59]\nv_mfma_f32_16x16x32_bf16 v[60:63], v[144:147], v[32:35], v[60:63]\nv_mfma_f32_16x16x32_bf16 v[64:67], v[144:147], v[36:39], v[64:67]\n" \
"v_mfma_f32_16x16x32_bf16 v[68:71], v[240:243], v[32:35], v[68:71]\ns_waitcnt lgkmcnt(0)\nv_mfma_f32_16x16x32_bf16 v[72:75], v[240:243], v[36:39], v[72:75]\nds_read_b128 v[32:35], v24 offset:30720\n" \
"v_add_u32 v31, s32, v24\nv_mfma_f32_16x16x32_bf16 v[44:47], v[76:79], v[40:43], v[44:47]\nds_read_b128 v[36:39], v24 offset:63488\nv_mfma_f32_16x16x32_bf16 v[48:51], v[76:79], v[80:83], v[48:51]\n" \
"v_mfma_f32_16x16x32_bf16 v[52:55], a[196:199], v[40:43], v[52:55]\nv_mfma_f32_16x16x32_bf16 v[56:59], a[196:199], v[80:83], v[56:59]\nv_mfma_f32_16x16x32_bf16 v[60:63], v[148:151], v[40:43], v[60:63]\nv_mfma_f32_16x16x32_bf16 v[64:67], v[148:151], v[80:83], v[64:67]\n" \
"v_mfma_f32_16x16x32_bf16 v[68:71], v[244:247], v[40:43], v[68:71]\ns_waitcnt vmcnt(0) lgkmcnt(0)\nds_read_b128 v[40:43], v31 offset:16384\nds_read_b128 v[76:79], v31 offset:17408\n" \
"v_mfma_f32_16x16x32_bf16 v[72:75], v[244:247], v[80:83], v[72:75]\nds_read_b128 v[84:87], v31 offset:18432\nds_read_b128 v[80:83], v31 offset:19456\nv_mfma_f32_16x16x32_bf16 v[44:47], a[88:91], v[32:35], v[44:47]\n" \
"ds_read_b128 v[88:91], v24 offset:31744\nv_mfma_f32_16x16x32_bf16 v[48:51], a[88:91], v[36:39], v[48:51]\nds_read_b128 v[92:95], v24 offset:64512\nv_mfma_f32_16x16x32_bf16 v[52:55], a[200:203], v[32:35], v[52:55]\n" \
"v_mfma_f32_16x16x32_bf16 v[56:59], a[200:203], v[36:39], v[56:59]\ns_waitcnt lgkmcnt(3)\nv_max3_u32 v96, v40, v76, v84\nv_mov_b64 v[100:101], 0\n" \
"v_mfma_f32_16x16x32_bf16 v[60:63], v[152:155], v[32:35], v[60:63]\ns_waitcnt lgkmcnt(2)\nv_max_u32 v96, v96, v80\nv_cmp_eq_u32 vcc, -1, v96\n" \
"v_mfma_f32_16x16x32_bf16 v[64:67], v[152:155], v[36:39], v[64:67]\nds_read_b128 v[104:107], v25 offset:0\nv_mov_b64 v[102:103], 0\nv_mfma_f32_16x16x32_bf16 v[68:71], v[248:251], v[32:35], v[68:71]\n" \
"ds_read_b128 v[108:111], v25 offset:1024\nv_mov_b64 v[32:33], 0\nv_mfma_f32_16x16x32_bf16 v[72:75], v[248:251], v[36:39], v[72:75]\ns_waitcnt lgkmcnt(2)\n" \
"s_cbranch_vccnz 2f\n3:\nv_mfma_f32_16x16x32_bf16 v[44:47], a[92:95], v[88:91], v[44:47]\ns_barrier\n" \
"s_memrealtime s[56:57]\nv_mfma_f32_16x16x32_bf16 v[52:55], a[204:207], v[88:91], v[52:55]\nds_read_b128 v[36:39], v24 offset:20480\nv_mov_b64 v[34:35], 0\n" \
"v_mfma_f32_16x16x32_bf16 v[60:63], v[156:159], v[88:91], v[60:63]\nds_read_b128 v[40:43], v24 offset:21504\nv_add_u32 v31, s44, v24\nv_mfma_f32_16x16x32_bf16 v[68:71], v[252:255], v[88:91], v[68:71]\n" \
"ds_read_b128 v[76:79], v24 offset:16384\nv_cvt_pk_bf16_f32 v44, v44, v52\nv_mfma_f32_16x16x32_bf16 v[48:51], a[92:95], v[92:95], v[48:51]\nds_read_b128 v[80:83], v24 offset:22528\n" \
"v_cvt_pk_bf16_f32 v45, v45, v53\nv_mfma_f32_16x16x32_bf16 v[56:59], a[204:207], v[92:95], v[56:59]\nds_read_b128 v[84:87], v24 offset:23552\nv_cvt_pk_bf16_f32 v46, v46, v54\n" \
"v_mfma_f32_16x16x32_bf16 v[64:67], v[156:159], v[92:95], v[64:67]\nds_read_b128 v[88:91], v24 offset:17408\nv_cvt_pk_bf16_f32 v47, v47, v55\nv_mfma_f32_16x16x32_bf16 v[72:75], v[252:255], v[92:95], v[72:75]\n" \
"s_waitcnt lgkmcnt(5)\nds_write_b64 v31, v[44:45] offset:0\nv_smfmac_f32_16x16x64_bf16 v[100:103], v[36:39], v[104:111], v26\nds_read_b128 v[52:55], v24 offset:18432\n" \
"v_cvt_pk_bf16_f32 v60, v60, v68\nv_smfmac_f32_16x16x64_bf16 v[32:35], v[36:39], a[120:127], v26\ns_waitcnt lgkmcnt(5)\nds_write_b64 v31, v[46:47] offset:8\n" \
"v_smfmac_f32_16x16x64_bf16 v[100:103], v[40:43], a[24:31], v26\nds_read_b128 v[92:95], v24 offset:19456\nv_cvt_pk_bf16_f32 v61, v61, v69\nv_smfmac_f32_16x16x64_bf16 v[32:35], v[40:43], a[128:135], v26\n" \
"ds_write_b64 v31, v[60:61] offset:32768\nv_smfmac_f32_16x16x64_bf16 v[100:103], v[76:79], v[0:7], v26\nds_read_b128 v[44:47], v24 offset:49152\nv_cvt_pk_bf16_f32 v62, v62, v70\n" \
"v_smfmac_f32_16x16x64_bf16 v[32:35], v[76:79], a[96:103], v26\ns_waitcnt lgkmcnt(5)\ns_add_u32 s94, s56, 0x7e\nv_cvt_pk_bf16_f32 v63, v63, v71\n" \
"v_smfmac_f32_16x16x64_bf16 v[100:103], v[80:83], a[32:39], v26\nds_read_b128 v[68:71], v24 offset:50176\ns_and_b32 s94, s94, 0xffffff\nv_smfmac_f32_16x16x64_bf16 v[32:35], v[80:83], a[136:143], v26\n" \
"ds_read_b128 v[76:79], v25 offset:6144\ns_or_b32 s94, s94, s93\nv_smfmac_f32_16x16x64_bf16 v[100:103], v[84:87], a[40:47], v26\nds_read_b128 v[80:83], v25 offset:7168\n" \
"v_mov_b32 v60, s94\nv_smfmac_f32_16x16x64_bf16 v[32:35], v[84:87], a[144:151], v26\nds_read_b128 v[96:99], v24 offset:51200\nv_cvt_pk_bf16_f32 v48, v48, v56\n" \
"v_smfmac_f32_16x16x64_bf16 v[100:103], v[88:91], a[0:7], v26\nds_read_b128 v[104:107], v25 offset:2048\nv_cvt_pk_bf16_f32 v49, v49, v57\nv_smfmac_f32_16x16x64_bf16 v[32:35], v[88:91], v[8:15], v26\n" \
"s_waitcnt lgkmcnt(5)\nds_read_b128 v[108:111], v25 offset:3072\nv_smfmac_f32_16x16x64_bf16 v[100:103], v[52:55], a[8:15], v26\nds_read_b128 v[84:87], v24 offset:56320\n" \
"v_cvt_pk_bf16_f32 v50, v50, v58\nv_smfmac_f32_16x16x64_bf16 v[32:35], v[52:55], a[104:111], v26\nds_write_b64 v31, v[62:63] offset:32776\nv_smfmac_f32_16x16x64_bf16 v[100:103], v[92:95], a[16:23], v26\n" \
"buffer_store_dword v60, v29, s[8:11], s49 offen offset:0 sc1\nv_smfmac_f32_16x16x64_bf16 v[32:35], v[92:95], a[112:119], v26\nds_read_b128 v[52:55], v24 offset:52224\nv_cvt_pk_bf16_f32 v51, v51, v59\n" \
"v_smfmac_f32_16x16x64_bf16 v[100:103], v[44:47], a[48:55], v26\nds_read_b128 v[56:59], v24 offset:53248\nv_cvt_pk_bf16_f32 v64, v64, v72\nv_smfmac_f32_16x16x64_bf16 v[32:35], v[44:47], a[152:159], v26\n" \
"s_waitcnt lgkmcnt(4)\nds_write_b64 v31, v[48:49] offset:4096\nv_smfmac_f32_16x16x64_bf16 v[100:103], v[68:71], a[56:63], v26\nds_read_b128 v[60:63], v24 offset:54272\n" \
"v_cvt_pk_bf16_f32 v65, v65, v73\nv_smfmac_f32_16x16x64_bf16 v[32:35], v[68:71], v[76:83], v26\nds_read_b128 v[92:95], v24 offset:55296\nv_cvt_pk_bf16_f32 v66, v66, v74\n" \
"v_smfmac_f32_16x16x64_bf16 v[100:103], v[96:99], v[104:111], v26\nds_read_b128 v[68:71], v25 offset:4096\nv_cvt_pk_bf16_f32 v67, v67, v75\nv_smfmac_f32_16x16x64_bf16 v[32:35], v[96:99], a[160:167], v26\n" \
"s_waitcnt lgkmcnt(5)\nds_read_b128 v[72:75], v25 offset:5120\nv_smfmac_f32_16x16x64_bf16 v[100:103], v[84:87], a[88:95], v26\nds_write_b64 v31, v[50:51] offset:4104\n" \
"v_smfmac_f32_16x16x64_bf16 v[32:35], v[84:87], a[200:207], v26\nds_read_b128 v[76:79], v25 offset:14336\nv_mov_b64 v[48:49], 0\nv_smfmac_f32_16x16x64_bf16 v[100:103], v[52:55], a[64:71], v26\n" \
"ds_read_b128 v[80:83], v25 offset:15360\nv_mov_b64 v[50:51], 0\nv_smfmac_f32_16x16x64_bf16 v[32:35], v[52:55], a[168:175], v26\ns_waitcnt lgkmcnt(5)\n" \
"ds_write_b64 v31, v[64:65] offset:36864\nv_smfmac_f32_16x16x64_bf16 v[100:103], v[56:59], a[72:79], v26\nds_read_b128 v[96:99], v24 offset:51200\nv_mov_b64 v[64:65], 0\n" \
"v_smfmac_f32_16x16x64_bf16 v[32:35], v[56:59], a[176:183], v26\nds_write_b64 v31, v[66:67] offset:36872\nv_smfmac_f32_16x16x64_bf16 v[100:103], v[60:63], a[80:87], v26\nds_read_b128 v[56:59], v24 offset:18432\n" \
"v_mov_b64 v[66:67], 0\nv_smfmac_f32_16x16x64_bf16 v[32:35], v[60:63], a[184:191], v26\ns_waitcnt lgkmcnt(5)\nds_read_b32 v31, v28 offset:512\n" \
"v_smfmac_f32_16x16x64_bf16 v[100:103], v[92:95], v[68:75], v26\nv_add_u32 v60, s45, v24\nv_smfmac_f32_16x16x64_bf16 v[32:35], v[92:95], a[192:199], v26\nds_read_b128 v[68:71], v24 offset:54272\n" \
"v_cndmask_b32 v60, v60, v28, s[28:29]\nv_smfmac_f32_16x16x64_bf16 v[48:51], v[40:43], a[240:247], v26\ns_waitcnt lgkmcnt(4)\nds_read_b128 v[104:107], v25 offset:18432\n" \
"v_smfmac_f32_16x16x64_bf16 v[64:67], v[40:43], v[76:83], v26\nds_read_b128 v[108:111], v25 offset:19456\nv_add_f32 v40, v100, v102\nv_smfmac_f32_16x16x64_bf16 v[48:51], v[36:39], a[232:239], v26\n" \
"ds_read_b128 v[72:75], v25 offset:20480\nv_add_f32 v41, v32, v34\nv_smfmac_f32_16x16x64_bf16 v[64:67], v[36:39], v[192:199], v26\nds_read_b128 v[76:79], v25 offset:21504\n" \
"v_add_f32 v42, v101, v103\nv_smfmac_f32_16x16x64_bf16 v[48:51], v[96:99], v[120:127], v26\nds_read_b128 v[36:39], v24 offset:19456\nv_add_f32 v43, v33, v35\n" \
"v_smfmac_f32_16x16x64_bf16 v[64:67], v[96:99], v[224:231], v26\nds_read_b128 v[32:35], v24 offset:50176\nv_cvt_pk_bf16_f32 v62, v40, v41\nv_smfmac_f32_16x16x64_bf16 v[48:51], v[84:87], v[152:159], v26\n" \
"ds_read_b128 v[80:83], v25 offset:10240\nv_cvt_pk_bf16_f32 v63, v42, v43\nv_smfmac_f32_16x16x64_bf16 v[64:67], v[84:87], v[248:255], v26\nds_write2_b32 v27, v62, v63 offset0:0 offset1:4\n" \
"v_smfmac_f32_16x16x64_bf16 v[48:51], v[92:95], v[144:151], v26\ns_waitcnt lgkmcnt(5)\nds_read_b128 v[84:87], v25 offset:11264\nv_smfmac_f32_16x16x64_bf16 v[64:67], v[92:95], v[240:247], v26\n" \
"ds_read_b128 v[40:43], v24 offset:53248\nv_cmp_eq_u32 vcc, -1, v31\nv_smfmac_f32_16x16x64_bf16 v[48:51], v[56:59], v[16:23], v26\nv_smfmac_f32_16x16x64_bf16 v[64:67], v[56:59], v[176:183], v26\n" \
"ds_read_b128 v[92:95], v24 offset:23552\nv_smfmac_f32_16x16x64_bf16 v[48:51], v[52:55], v[128:135], v26\nds_read_b128 v[96:99], v25 offset:12288\nv_smfmac_f32_16x16x64_bf16 v[64:67], v[52:55], v[104:111], v26\n" \
"s_waitcnt lgkmcnt(4)\nds_read_b128 v[100:103], v25 offset:13312\nv_smfmac_f32_16x16x64_bf16 v[48:51], v[68:71], v[136:143], v26\ns_barrier\n" \
"v_smfmac_f32_16x16x64_bf16 v[64:67], v[68:71], v[72:79], v26\nds_read_b128 v[52:55], v25 offset:16384\nv_mov_b32 v63, s43\nv_smfmac_f32_16x16x64_bf16 v[48:51], v[36:39], a[224:231], v26\n" \
"ds_read_b128 v[56:59], v25 offset:17408\nv_cndmask_b32 v76, v63, 0, s[16:17]\nv_smfmac_f32_16x16x64_bf16 v[64:67], v[36:39], v[184:191], v26\ns_waitcnt lgkmcnt(4)\n" \
"ds_read_b128 v[36:39], v24 offset:22528\nv_smfmac_f32_16x16x64_bf16 v[48:51], v[44:47], v[80:87], v26\nds_read_b128 v[68:71], v25 offset:8192\nv_cndmask_b32 v77, v63, 0, s[18:19]\n" \
"v_smfmac_f32_16x16x64_bf16 v[64:67], v[44:47], v[208:215], v26\nds_read_b128 v[72:75], v25 offset:9216\nv_cndmask_b32 v78, v63, 0, s[20:21]\nv_smfmac_f32_16x16x64_bf16 v[48:51], v[32:35], v[112:119], v26\n" \
"ds_read_b128 v[44:47], v24 offset:16384\nv_cndmask_b32 v79, v63, 0, s[22:23]\nv_smfmac_f32_16x16x64_bf16 v[64:67], v[32:35], v[216:223], v26\nds_read_b128 v[80:83], v60 offset:0\n" \
"v_mov_b32 v62, 0xff61b1e6\nv_smfmac_f32_16x16x64_bf16 v[48:51], v[88:91], a[216:223], v26\nds_read_b128 v[32:35], v24 offset:57344\nv_cndmask_b32 v84, v62, v63, s[16:17]\n" \
"v_smfmac_f32_16x16x64_bf16 v[64:67], v[88:91], v[168:175], v26\ns_waitcnt lgkmcnt(6)\nds_read_b128 v[88:91], v24 offset:61440\nv_smfmac_f32_16x16x64_bf16 v[48:51], v[40:43], v[96:103], v26\n" \
"ds_read_b128 v[104:107], v60 offset:1024\nv_cndmask_b32 v85, v62, v63, s[18:19]\nv_smfmac_f32_16x16x64_bf16 v[64:67], v[40:43], v[232:239], v26\nds_read_b128 v[96:99], v24 offset:58368\n" \
"v_cndmask_b32 v86, v62, v63, s[20:21]\nv_smfmac_f32_16x16x64_bf16 v[48:51], v[92:95], a[248:255], v26\nds_read_b128 v[40:43], v24 offset:62464\nv_cndmask_b32 v87, v62, v63, s[22:23]\n" \
"v_smfmac_f32_16x16x64_bf16 v[64:67], v[92:95], v[52:59], v26\ns_waitcnt lgkmcnt(5)\nds_read_b128 v[56:59], v60 offset:2048\nv_smfmac_f32_16x16x64_bf16 v[48:51], v[36:39], v[68:75], v26\n" \
"ds_read_b128 v[52:55], v24 offset:59392\nv_smfmac_f32_16x16x64_bf16 v[64:67], v[36:39], v[200:207], v26\nds_read_b128 v[68:71], v24 offset:63488\nv_smfmac_f32_16x16x64_bf16 v[48:51], v[44:47], a[208:215], v26\n" \
"ds_read_b128 v[36:39], v60 offset:3072\nds_read_b128 v[60:63], v24 offset:60416\nv_smfmac_f32_16x16x64_bf16 v[64:67], v[44:47], v[160:167], v26\ns_waitcnt lgkmcnt(6)\n" \
"ds_read_b128 v[44:47], v24 offset:64512\nv_mfma_f32_16x16x32_bf16 v[76:79], v[32:35], v[80:83], v[76:79]\nds_read_b128 v[72:75], v24 offset:0\nv_add_f32 v32, v48, v50\n" \
"v_add_f32 v34, v49, v51\nv_mfma_f32_16x16x32_bf16 v[84:87], v[88:91], v[80:83], v[84:87]\ns_waitcnt lgkmcnt(6)\nv_add_f32 v33, v64, v66\n" \
"v_add_f32 v35, v65, v67\nv_cvt_pk_bf16_f32 v48, v32, v33\nv_cvt_pk_bf16_f32 v49, v34, v35\nv_mfma_f32_16x16x32_bf16 v[76:79], v[96:99], v[104:107], v[76:79]\n" \
"s_waitcnt lgkmcnt(4)\nds_write2_b32 v27, v48, v49 offset0:128 offset1:132\nv_mfma_f32_16x16x32_bf16 v[84:87], v[40:43], v[104:107], v[84:87]\nds_read_b128 v[32:35], v24 offset:32768\n" \
"v_mfma_f32_16x16x32_bf16 v[76:79], v[52:55], v[56:59], v[76:79]\ns_waitcnt lgkmcnt(4)\ns_cmp_eq_u64 vcc, 0\ns_cselect_b32 s14, s10, 0\n" \
"v_mfma_f32_16x16x32_bf16 v[84:87], v[68:71], v[56:59], v[84:87]\ns_cmp_eq_u32 s51, 0\ns_cbranch_scc1 4f\n5:\n" \
"v_mfma_f32_16x16x32_bf16 v[76:79], v[60:63], v[36:39], v[76:79]\ns_waitcnt lgkmcnt(1)\ns_barrier\nds_read_b128 v[40:43], v28 offset:512\n" \
"ds_read_b128 v[48:51], v28 offset:1536\nv_mfma_f32_16x16x32_bf16 v[84:87], v[44:47], v[36:39], v[84:87]\nds_read_b128 v[64:67], v28 offset:2560\nds_read_b128 v[88:91], v28 offset:3584\n" \
"v_mfma_f32_16x16x32_bf16 v[44:47], v[0:3], v[72:75], 0\nds_read_b128 v[92:95], v24 offset:1024\nv_max3_f32 v31, v76, v77, v78\nv_max3_f32 v31, v31, v79, s47\n" \
"v_mfma_f32_16x16x32_bf16 v[52:55], a[96:99], v[72:75], 0\nds_read_b128 v[96:99], v24 offset:33792\nv_max3_f32 v100, v84, v85, v86\nv_max3_f32 v101, v31, v100, v87\n" \
"v_max3_f32 v31, v31, v100, v87\nv_mfma_f32_16x16x32_bf16 v[60:63], a[208:211], v[72:75], 0\ns_waitcnt lgkmcnt(6)\nv_mul_f32 v100, s46, v31\n" \
"v_fma_f32 v76, v76, s46, -v100\nv_exp_f32 v76, v76\nv_mfma_f32_16x16x32_bf16 v[68:71], v[160:163], v[72:75], 0\ns_waitcnt lgkmcnt(4)\n" \
"v_fma_f32 v78, v78, s46, -v100\nv_exp_f32 v78, v78\nv_fma_f32 v77, v77, s46, -v100\nv_fma_f32 v79, v79, s46, -v100\n" \
"v_mfma_f32_16x16x32_bf16 v[108:111], v[40:43], v[80:83], 0\nv_exp_f32 v77, v77\nv_fma_f32 v84, v84, s46, -v100\nv_fma_f32 v86, v86, s46, -v100\n" \
"v_fma_f32 v85, v85, s46, -v100\nv_mfma_f32_16x16x32_bf16 v[108:111], v[48:51], v[104:107], v[108:111]\ns_waitcnt lgkmcnt(0)\nv_fma_f32 v87, v87, s46, -v100\n" \
"v_cndmask_b32 v40, v76, v78, s[24:25]\nv_mfma_f32_16x16x32_bf16 v[108:111], v[64:67], v[56:59], v[108:111]\nv_exp_f32 v79, v79\nv_mfma_f32_16x16x32_bf16 v[108:111], v[88:91], v[36:39], v[108:111]\n" \
"v_exp_f32 v84, v84\nv_mfma_f32_16x16x32_bf16 v[48:51], v[0:3], v[32:35], 0\nv_exp_f32 v86, v86\nv_exp_f32 v85, v85\n" \
"v_mfma_f32_16x16x32_bf16 v[56:59], a[96:99], v[32:35], 0\nv_exp_f32 v87, v87\nv_cndmask_b32 v41, v77, v79, s[24:25]\nv_cndmask_b32 v42, v84, v86, s[24:25]\n" \
"v_mfma_f32_16x16x32_bf16 v[64:67], a[208:211], v[32:35], 0\nv_cndmask_b32 v43, v85, v87, s[24:25]\nv_permlane32_swap_b32 v31, v101\nv_mfma_f32_16x16x32_bf16 v[72:75], v[160:163], v[32:35], 0\n" \
"v_max3_f32 v36, v108, v109, v110\nv_max3_f32 v31, v31, v101, v111\nv_max_f32 v37, v36, v31\nv_max_f32 v36, v36, v31\n" \
"v_mfma_f32_16x16x32_bf16 v[44:47], v[4:7], v[92:95], v[44:47]\nds_read_b128 v[32:35], v24 offset:2048\nv_permlane16_swap_b32 v36, v37\nv_mfma_f32_16x16x32_bf16 v[48:51], v[4:7], v[96:99], v[48:51]\n" \
"ds_read_b128 v[80:83], v24 offset:34816\nv_max_f32 v36, v36, v37\nv_mfma_f32_16x16x32_bf16 v[52:55], a[100:103], v[92:95], v[52:55]\nv_cndmask_b32 v38, v108, v110, s[26:27]\n" \
"v_fma_f32 v100, v36, -s46, v100\nv_mul_f32 v36, s46, v36\nv_cndmask_b32 v39, v109, v111, s[26:27]\nv_fma_f32 v38, v38, s46, -v36\n" \
"v_mfma_f32_16x16x32_bf16 v[56:59], a[100:103], v[96:99], v[56:59]\nv_add_f32 v88, v76, v78\nv_add_f32 v89, v77, v79\nv_fma_f32 v39, v39, s46, -v36\n" \
"v_add_f32 v90, v84, v86\nv_add_f32 v88, v88, v89\nv_mfma_f32_16x16x32_bf16 v[60:63], a[212:215], v[92:95], v[60:63]\nv_exp_f32 v100, v100\n" \
"v_exp_f32 v38, v38\nv_add_f32 v91, v85, v87\nv_mfma_f32_16x16x32_bf16 v[64:67], a[212:215], v[96:99], v[64:67]\nv_add_f32 v88, v88, v90\n" \
"v_exp_f32 v39, v39\nv_add_f32 v88, v88, v91\nv_fma_f32 v88, v88, v100, v38\nv_mfma_f32_16x16x32_bf16 v[68:71], v[164:167], v[92:95], v[68:71]\n" \
"s_waitcnt lgkmcnt(0)\nv_add_f32 v89, v88, v39\nv_add_f32 v88, v39, v88\nv_mfma_f32_16x16x32_bf16 v[72:75], v[164:167], v[96:99], v[72:75]\n" \
"ds_read_b128 v[92:95], v24 offset:3072\nv_permlane16_swap_b32 v88, v89\nv_add_f32 v90, v88, v89\nv_add_f32 v88, v89, v88\n" \
"v_mfma_f32_16x16x32_bf16 v[44:47], a[0:3], v[32:35], v[44:47]\nds_read_b128 v[96:99], v24 offset:35840\nv_permlane32_swap_b32 v88, v90\nv_mfma_f32_16x16x32_bf16 v[48:51], a[0:3], v[80:83], v[48:51]\n" \
"v_add_f32 v88, v88, v90\nv_mfma_f32_16x16x32_bf16 v[52:55], v[8:11], v[32:35], v[52:55]\nv_rcp_f32 v88, v88\nv_mfma_f32_16x16x32_bf16 v[56:59], v[8:11], v[80:83], v[56:59]\n" \
"v_mul_f32 v100, v100, v88\nv_mul_f32 v40, v40, v100\nv_mul_f32 v41, v41, v100\nv_mul_f32 v38, v38, v88\n" \
"v_mul_f32 v39, v39, v88\nv_mfma_f32_16x16x32_bf16 v[60:63], a[216:219], v[32:35], v[60:63]\nv_mul_f32 v42, v42, v100\nv_mul_f32 v43, v43, v100\n" \
"v_cvt_pk_bf16_f32 v36, v40, v41\nv_cvt_pk_bf16_f32 v31, v38, v39\nv_cvt_pk_bf16_f32 v37, v42, v43\nv_mfma_f32_16x16x32_bf16 v[64:67], a[216:219], v[80:83], v[64:67]\n" \
"v_and_or_b32 v36, v36, -2, s95\nv_and_or_b32 v31, v31, -2, s95\nv_mfma_f32_16x16x32_bf16 v[68:71], v[168:171], v[32:35], v[68:71]\ns_waitcnt lgkmcnt(0)\n" \
"buffer_store_dwordx2 v[36:37], v29, s[8:11], s99 offen offset:0 sc1\nbuffer_store_dword v31, v30, s[8:11], s96 offen offset:1536 sc1\nv_mfma_f32_16x16x32_bf16 v[72:75], v[168:171], v[80:83], v[72:75]\nds_read_b128 v[88:91], v24 offset:4096\n" \
"v_mul_f32 v76, v76, v100\nv_mfma_f32_16x16x32_bf16 v[44:47], a[4:7], v[92:95], v[44:47]\nds_read_b128 v[32:35], v24 offset:36864\nv_mul_f32 v77, v77, v100\n" \
"v_mfma_f32_16x16x32_bf16 v[48:51], a[4:7], v[96:99], v[48:51]\nv_mul_f32 v78, v78, v100\nv_mul_f32 v79, v79, v100\nv_mfma_f32_16x16x32_bf16 v[52:55], v[12:15], v[92:95], v[52:55]\n" \
"v_cvt_pk_bf16_f32 v80, v76, v77\nv_cvt_pk_bf16_f32 v81, v78, v79\nv_mfma_f32_16x16x32_bf16 v[56:59], v[12:15], v[96:99], v[56:59]\ns_add_u32 m0, s32, 0x4000\n" \
"v_and_or_b32 v80, v80, -2, s95\nv_mul_f32 v84, v84, v100\nv_mfma_f32_16x16x32_bf16 v[60:63], a[220:223], v[92:95], v[60:63]\nbuffer_store_dwordx2 v[80:81], v29, s[8:11], s97 offen offset:1024 sc1\n" \
"v_mul_f32 v85, v85, v100\nv_mfma_f32_16x16x32_bf16 v[64:67], a[220:223], v[96:99], v[64:67]\ns_add_u32 s99, s31, 0x4000\nv_mul_f32 v86, v86, v100\n" \
"v_mul_f32 v87, v87, v100\nv_mfma_f32_16x16x32_bf16 v[68:71], v[172:175], v[92:95], v[68:71]\ns_waitcnt lgkmcnt(0)\nv_cvt_pk_bf16_f32 v82, v84, v85\n" \
"v_cvt_pk_bf16_f32 v83, v86, v87\nv_mfma_f32_16x16x32_bf16 v[72:75], v[172:175], v[96:99], v[72:75]\nds_read_b128 v[36:39], v24 offset:5120\nv_mfma_f32_16x16x32_bf16 v[44:47], a[8:11], v[88:91], v[44:47]\n" \
"ds_read_b128 v[40:43], v24 offset:37888\nv_mfma_f32_16x16x32_bf16 v[48:51], a[8:11], v[32:35], v[48:51]\nbuffer_store_dwordx4 v[80:83], v24, s[8:11], s98 offen offset:0 sc1\nbuffer_load_dwordx4 v24, s[8:11], s99 offen offset:0 sc1 lds\n" \
"v_mfma_f32_16x16x32_bf16 v[52:55], a[104:107], v[88:91], v[52:55]\nv_mfma_f32_16x16x32_bf16 v[56:59], a[104:107], v[32:35], v[56:59]\nbuffer_load_dwordx4 v24, s[8:11], s99 offen offset:1024 sc1 lds\nv_mfma_f32_16x16x32_bf16 v[60:63], v[16:19], v[88:91], v[60:63]\n" \
"v_mfma_f32_16x16x32_bf16 v[64:67], v[16:19], v[32:35], v[64:67]\nv_mfma_f32_16x16x32_bf16 v[68:71], v[176:179], v[88:91], v[68:71]\ns_waitcnt lgkmcnt(0)\nv_mfma_f32_16x16x32_bf16 v[72:75], v[176:179], v[32:35], v[72:75]\n" \
"ds_read_b128 v[76:79], v24 offset:6144\nv_mfma_f32_16x16x32_bf16 v[44:47], a[12:15], v[36:39], v[44:47]\nds_read_b128 v[32:35], v24 offset:38912\nv_mfma_f32_16x16x32_bf16 v[48:51], a[12:15], v[40:43], v[48:51]\n" \
"buffer_load_dwordx4 v24, s[8:11], s99 offen offset:2048 sc1 lds\nv_mfma_f32_16x16x32_bf16 v[52:55], a[108:111], v[36:39], v[52:55]\nv_mfma_f32_16x16x32_bf16 v[56:59], a[108:111], v[40:43], v[56:59]\nbuffer_load_dwordx4 v24, s[8:11], s99 offen offset:3072 sc1 lds\n" \
"v_mfma_f32_16x16x32_bf16 v[60:63], v[20:23], v[36:39], v[60:63]\nv_mfma_f32_16x16x32_bf16 v[64:67], v[20:23], v[40:43], v[64:67]\nv_mfma_f32_16x16x32_bf16 v[68:71], v[180:183], v[36:39], v[68:71]\ns_waitcnt lgkmcnt(0)\n" \
"v_mfma_f32_16x16x32_bf16 v[72:75], v[180:183], v[40:43], v[72:75]\nds_read_b128 v[36:39], v24 offset:7168\nv_mfma_f32_16x16x32_bf16 v[44:47], a[16:19], v[76:79], v[44:47]\nds_read_b128 v[40:43], v24 offset:39936\n" \
"v_mfma_f32_16x16x32_bf16 v[48:51], a[16:19], v[32:35], v[48:51]\ns_add_u32 m0, s32, 0x6000\ns_add_u32 s99, s31, 0x6000\nv_mfma_f32_16x16x32_bf16 v[52:55], a[112:115], v[76:79], v[52:55]\n" \
"v_mfma_f32_16x16x32_bf16 v[56:59], a[112:115], v[32:35], v[56:59]\nv_mfma_f32_16x16x32_bf16 v[60:63], a[224:227], v[76:79], v[60:63]\nds_read_b128 v[80:83], v25 offset:0\nv_mfma_f32_16x16x32_bf16 v[64:67], a[224:227], v[32:35], v[64:67]\n" \
"buffer_load_dwordx4 v24, s[8:11], s99 offen offset:0 sc1 lds\nv_mfma_f32_16x16x32_bf16 v[68:71], v[184:187], v[76:79], v[68:71]\ns_waitcnt lgkmcnt(1)\nv_mfma_f32_16x16x32_bf16 v[72:75], v[184:187], v[32:35], v[72:75]\n" \
"ds_read_b128 v[76:79], v24 offset:8192\nv_mfma_f32_16x16x32_bf16 v[44:47], a[20:23], v[36:39], v[44:47]\nds_read_b128 v[32:35], v24 offset:40960\nv_mfma_f32_16x16x32_bf16 v[48:51], a[20:23], v[40:43], v[48:51]\n" \
"buffer_load_dwordx4 v24, s[8:11], s99 offen offset:1024 sc1 lds\nv_mfma_f32_16x16x32_bf16 v[52:55], a[116:119], v[36:39], v[52:55]\nv_mfma_f32_16x16x32_bf16 v[56:59], a[116:119], v[40:43], v[56:59]\nv_mfma_f32_16x16x32_bf16 v[60:63], a[228:231], v[36:39], v[60:63]\n" \
"ds_read_b128 v[84:87], v25 offset:1024\nv_mfma_f32_16x16x32_bf16 v[64:67], a[228:231], v[40:43], v[64:67]\nbuffer_load_dwordx4 v24, s[8:11], s99 offen offset:2048 sc1 lds\nv_mfma_f32_16x16x32_bf16 v[68:71], v[188:191], v[36:39], v[68:71]\n" \
"s_waitcnt lgkmcnt(1)\nv_mfma_f32_16x16x32_bf16 v[72:75], v[188:191], v[40:43], v[72:75]\nds_read_b128 v[36:39], v24 offset:9216\nv_mfma_f32_16x16x32_bf16 v[44:47], v[80:83], v[76:79], v[44:47]\n" \
"ds_read_b128 v[40:43], v24 offset:41984\nv_mfma_f32_16x16x32_bf16 v[48:51], v[80:83], v[32:35], v[48:51]\nbuffer_load_dwordx4 v24, s[8:11], s99 offen offset:3072 sc1 lds\nv_mfma_f32_16x16x32_bf16 v[52:55], a[120:123], v[76:79], v[52:55]\n" \
"v_mfma_f32_16x16x32_bf16 v[56:59], a[120:123], v[32:35], v[56:59]\nv_mfma_f32_16x16x32_bf16 v[60:63], a[232:235], v[76:79], v[60:63]\nv_mfma_f32_16x16x32_bf16 v[64:67], a[232:235], v[32:35], v[64:67]\nv_mfma_f32_16x16x32_bf16 v[68:71], v[192:195], v[76:79], v[68:71]\n" \
"s_waitcnt lgkmcnt(0)\nv_mfma_f32_16x16x32_bf16 v[72:75], v[192:195], v[32:35], v[72:75]\nds_read_b128 v[76:79], v24 offset:10240\nv_mfma_f32_16x16x32_bf16 v[44:47], v[84:87], v[36:39], v[44:47]\n" \
"ds_read_b128 v[32:35], v24 offset:43008\nv_mfma_f32_16x16x32_bf16 v[48:51], v[84:87], v[40:43], v[48:51]\nv_mfma_f32_16x16x32_bf16 v[52:55], a[124:127], v[36:39], v[52:55]\nds_read_b128 v[80:83], v25 offset:14336\n" \
"v_mfma_f32_16x16x32_bf16 v[56:59], a[124:127], v[40:43], v[56:59]\nv_mfma_f32_16x16x32_bf16 v[60:63], a[236:239], v[36:39], v[60:63]\nv_mfma_f32_16x16x32_bf16 v[64:67], a[236:239], v[40:43], v[64:67]\nv_mfma_f32_16x16x32_bf16 v[68:71], v[196:199], v[36:39], v[68:71]\n" \
"s_waitcnt lgkmcnt(1)\nv_mfma_f32_16x16x32_bf16 v[72:75], v[196:199], v[40:43], v[72:75]\nds_read_b128 v[36:39], v24 offset:11264\nv_mfma_f32_16x16x32_bf16 v[44:47], a[24:27], v[76:79], v[44:47]\n" \
"ds_read_b128 v[40:43], v24 offset:44032\nv_mfma_f32_16x16x32_bf16 v[48:51], a[24:27], v[32:35], v[48:51]\nv_mfma_f32_16x16x32_bf16 v[52:55], a[128:131], v[76:79], v[52:55]\nds_read_b128 v[84:87], v25 offset:15360\n" \
"v_mfma_f32_16x16x32_bf16 v[56:59], a[128:131], v[32:35], v[56:59]\nv_mfma_f32_16x16x32_bf16 v[60:63], a[240:243], v[76:79], v[60:63]\ns_waitcnt lgkmcnt(3)\nv_mfma_f32_16x16x32_bf16 v[64:67], a[240:243], v[32:35], v[64:67]\n" \
"v_mfma_f32_16x16x32_bf16 v[68:71], v[80:83], v[76:79], v[68:71]\ns_waitcnt lgkmcnt(1)\nv_mfma_f32_16x16x32_bf16 v[72:75], v[80:83], v[32:35], v[72:75]\nds_read_b128 v[76:79], v24 offset:12288\n" \
"v_mfma_f32_16x16x32_bf16 v[44:47], a[28:31], v[36:39], v[44:47]\nds_read_b128 v[32:35], v24 offset:45056\nv_mfma_f32_16x16x32_bf16 v[48:51], a[28:31], v[40:43], v[48:51]\nds_read_b128 v[80:83], v25 offset:8192\n" \
"v_mfma_f32_16x16x32_bf16 v[52:55], a[132:135], v[36:39], v[52:55]\nv_mfma_f32_16x16x32_bf16 v[56:59], a[132:135], v[40:43], v[56:59]\nv_mfma_f32_16x16x32_bf16 v[60:63], a[244:247], v[36:39], v[60:63]\ns_waitcnt lgkmcnt(3)\n" \
"v_mfma_f32_16x16x32_bf16 v[64:67], a[244:247], v[40:43], v[64:67]\nv_mfma_f32_16x16x32_bf16 v[68:71], v[84:87], v[36:39], v[68:71]\ns_waitcnt lgkmcnt(1)\nv_mfma_f32_16x16x32_bf16 v[72:75], v[84:87], v[40:43], v[72:75]\n" \
"ds_read_b128 v[36:39], v24 offset:13312\nv_mfma_f32_16x16x32_bf16 v[44:47], a[32:35], v[76:79], v[44:47]\nds_read_b128 v[40:43], v24 offset:46080\nv_mfma_f32_16x16x32_bf16 v[48:51], a[32:35], v[32:35], v[48:51]\n" \
"ds_read_b128 v[84:87], v25 offset:9216\nv_mfma_f32_16x16x32_bf16 v[52:55], a[136:139], v[76:79], v[52:55]\ns_waitcnt lgkmcnt(3)\nv_mfma_f32_16x16x32_bf16 v[56:59], a[136:139], v[32:35], v[56:59]\n" \
"v_mfma_f32_16x16x32_bf16 v[60:63], v[80:83], v[76:79], v[60:63]\nv_mfma_f32_16x16x32_bf16 v[64:67], v[80:83], v[32:35], v[64:67]\nv_mfma_f32_16x16x32_bf16 v[68:71], v[200:203], v[76:79], v[68:71]\ns_waitcnt lgkmcnt(1)\n" \
"v_mfma_f32_16x16x32_bf16 v[72:75], v[200:203], v[32:35], v[72:75]\nds_read_b128 v[76:79], v24 offset:14336\nv_mfma_f32_16x16x32_bf16 v[44:47], a[36:39], v[36:39], v[44:47]\nds_read_b128 v[32:35], v24 offset:47104\n" \
"v_mfma_f32_16x16x32_bf16 v[48:51], a[36:39], v[40:43], v[48:51]\nv_mfma_f32_16x16x32_bf16 v[52:55], a[140:143], v[36:39], v[52:55]\ns_waitcnt lgkmcnt(2)\nds_read_b128 v[80:83], v25 offset:16384\n" \
"v_mfma_f32_16x16x32_bf16 v[56:59], a[140:143], v[40:43], v[56:59]\nv_mfma_f32_16x16x32_bf16 v[60:63], v[84:87], v[36:39], v[60:63]\nv_mfma_f32_16x16x32_bf16 v[64:67], v[84:87], v[40:43], v[64:67]\nv_mfma_f32_16x16x32_bf16 v[68:71], v[204:207], v[36:39], v[68:71]\n" \
"s_waitcnt lgkmcnt(1)\nv_mfma_f32_16x16x32_bf16 v[72:75], v[204:207], v[40:43], v[72:75]\nds_read_b128 v[36:39], v24 offset:15360\nv_mfma_f32_16x16x32_bf16 v[44:47], a[40:43], v[76:79], v[44:47]\n" \
"ds_read_b128 v[40:43], v24 offset:48128\nv_mfma_f32_16x16x32_bf16 v[48:51], a[40:43], v[32:35], v[48:51]\nv_mfma_f32_16x16x32_bf16 v[52:55], a[144:147], v[76:79], v[52:55]\nds_read_b128 v[84:87], v25 offset:17408\n" \
"v_mfma_f32_16x16x32_bf16 v[56:59], a[144:147], v[32:35], v[56:59]\nv_mfma_f32_16x16x32_bf16 v[60:63], a[248:251], v[76:79], v[60:63]\ns_waitcnt lgkmcnt(3)\nv_mfma_f32_16x16x32_bf16 v[64:67], a[248:251], v[32:35], v[64:67]\n" \
"s_waitcnt vmcnt(4) lgkmcnt(1)\ns_barrier\nv_mfma_f32_16x16x32_bf16 v[68:71], v[80:83], v[76:79], v[68:71]\ns_add_u32 m0, s32, 0\n" \
"s_add_u32 s99, s31, 0x10000\nv_mfma_f32_16x16x32_bf16 v[72:75], v[80:83], v[32:35], v[72:75]\nds_read_b128 v[76:79], v24 offset:16384\nv_mfma_f32_16x16x32_bf16 v[44:47], a[44:47], v[36:39], v[44:47]\n" \
"ds_read_b128 v[32:35], v24 offset:49152\nv_mfma_f32_16x16x32_bf16 v[48:51], a[44:47], v[40:43], v[48:51]\nds_read_b128 v[80:83], v25 offset:10240\nv_mfma_f32_16x16x32_bf16 v[52:55], a[148:151], v[36:39], v[52:55]\n" \
"buffer_load_dwordx4 v24, s[12:15], s99 offen offset:0 sc1 lds\nv_mfma_f32_16x16x32_bf16 v[56:59], a[148:151], v[40:43], v[56:59]\nv_mfma_f32_16x16x32_bf16 v[60:63], a[252:255], v[36:39], v[60:63]\ns_waitcnt lgkmcnt(3)\n" \
"v_mfma_f32_16x16x32_bf16 v[64:67], a[252:255], v[40:43], v[64:67]\nbuffer_load_dwordx4 v24, s[12:15], s99 offen offset:1024 sc1 lds\nv_mfma_f32_16x16x32_bf16 v[68:71], v[84:87], v[36:39], v[68:71]\ns_waitcnt lgkmcnt(1)\n" \
"v_mfma_f32_16x16x32_bf16 v[72:75], v[84:87], v[40:43], v[72:75]\nds_read_b128 v[36:39], v24 offset:17408\nv_mfma_f32_16x16x32_bf16 v[44:47], a[48:51], v[76:79], v[44:47]\nds_read_b128 v[40:43], v24 offset:50176\n" \
"v_mfma_f32_16x16x32_bf16 v[48:51], a[48:51], v[32:35], v[48:51]\nds_read_b128 v[84:87], v25 offset:11264\nv_mfma_f32_16x16x32_bf16 v[52:55], a[152:155], v[76:79], v[52:55]\ns_waitcnt lgkmcnt(3)\n" \
"buffer_load_dwordx4 v24, s[12:15], s99 offen offset:2048 sc1 lds\nv_mfma_f32_16x16x32_bf16 v[56:59], a[152:155], v[32:35], v[56:59]\nv_mfma_f32_16x16x32_bf16 v[60:63], v[80:83], v[76:79], v[60:63]\nv_mfma_f32_16x16x32_bf16 v[64:67], v[80:83], v[32:35], v[64:67]\n" \
"buffer_load_dwordx4 v24, s[12:15], s99 offen offset:3072 sc1 lds\nv_mfma_f32_16x16x32_bf16 v[68:71], v[208:211], v[76:79], v[68:71]\ns_waitcnt lgkmcnt(1)\nds_read_b128 v[76:79], v25 offset:6144\n" \
"v_mfma_f32_16x16x32_bf16 v[72:75], v[208:211], v[32:35], v[72:75]\nds_read_b128 v[80:83], v24 offset:18432\nv_mfma_f32_16x16x32_bf16 v[44:47], a[52:55], v[36:39], v[44:47]\nds_read_b128 v[32:35], v24 offset:51200\n" \
"s_add_u32 m0, s32, 0x2000\nv_mfma_f32_16x16x32_bf16 v[48:51], a[52:55], v[40:43], v[48:51]\ns_add_u32 s99, s31, 0x12000\nv_mfma_f32_16x16x32_bf16 v[52:55], a[156:159], v[36:39], v[52:55]\n" \
"s_waitcnt lgkmcnt(3)\nv_mfma_f32_16x16x32_bf16 v[56:59], a[156:159], v[40:43], v[56:59]\nv_mfma_f32_16x16x32_bf16 v[60:63], v[84:87], v[36:39], v[60:63]\nbuffer_load_dwordx4 v24, s[12:15], s99 offen offset:0 sc1 lds\n" \
"v_mfma_f32_16x16x32_bf16 v[64:67], v[84:87], v[40:43], v[64:67]\nv_mfma_f32_16x16x32_bf16 v[68:71], v[212:215], v[36:39], v[68:71]\ns_waitcnt lgkmcnt(0)\nds_read_b128 v[36:39], v25 offset:7168\n" \
"v_mfma_f32_16x16x32_bf16 v[72:75], v[212:215], v[40:43], v[72:75]\nds_read_b128 v[84:87], v24 offset:19456\nv_mfma_f32_16x16x32_bf16 v[44:47], a[56:59], v[80:83], v[44:47]\nds_read_b128 v[40:43], v24 offset:52224\n" \
"v_mfma_f32_16x16x32_bf16 v[48:51], a[56:59], v[32:35], v[48:51]\nbuffer_load_dwordx4 v24, s[12:15], s99 offen offset:1024 sc1 lds\nv_mfma_f32_16x16x32_bf16 v[52:55], v[76:79], v[80:83], v[52:55]\nv_mfma_f32_16x16x32_bf16 v[56:59], v[76:79], v[32:35], v[56:59]\n" \
"v_mfma_f32_16x16x32_bf16 v[60:63], v[112:115], v[80:83], v[60:63]\nds_read_b128 v[76:79], v25 offset:2048\nv_mfma_f32_16x16x32_bf16 v[64:67], v[112:115], v[32:35], v[64:67]\nbuffer_load_dwordx4 v24, s[12:15], s99 offen offset:2048 sc1 lds\n" \
"v_mfma_f32_16x16x32_bf16 v[68:71], v[216:219], v[80:83], v[68:71]\ns_waitcnt lgkmcnt(1)\nv_mfma_f32_16x16x32_bf16 v[72:75], v[216:219], v[32:35], v[72:75]\nds_read_b128 v[80:83], v24 offset:20480\n" \
"v_mfma_f32_16x16x32_bf16 v[44:47], a[60:63], v[84:87], v[44:47]\nds_read_b128 v[32:35], v24 offset:53248\nv_mfma_f32_16x16x32_bf16 v[48:51], a[60:63], v[40:43], v[48:51]\nbuffer_load_dwordx4 v24, s[12:15], s99 offen offset:3072 sc1 lds\n" \
"v_mfma_f32_16x16x32_bf16 v[52:55], v[36:39], v[84:87], v[52:55]\nv_mfma_f32_16x16x32_bf16 v[56:59], v[36:39], v[40:43], v[56:59]\nv_mfma_f32_16x16x32_bf16 v[60:63], v[116:119], v[84:87], v[60:63]\nv_mfma_f32_16x16x32_bf16 v[64:67], v[116:119], v[40:43], v[64:67]\n" \
"v_mfma_f32_16x16x32_bf16 v[68:71], v[220:223], v[84:87], v[68:71]\ns_cmp_eq_u32 s30, 64\nv_mfma_f32_16x16x32_bf16 v[72:75], v[220:223], v[40:43], v[72:75]\ns_cbranch_scc0 1b\n" \
"s_waitcnt vmcnt(0)\ns_barrier\nv_mov_b32 v40, 0x41000000\nbuffer_store_dword v40, v29, s[8:11], s49 offen offset:0 sc1\n" \
"s_branch 10f\n2:\ns_add_u32 s52, s52, 1\ns_cmp_lt_u32 s52, 0x40000\n" \
"s_cbranch_scc0 9f\nv_cmp_eq_u32 vcc, -1, v40\ns_cbranch_vccz 6f\nbuffer_load_dwordx4 v24, s[8:11], s94 offen offset:0 sc1 lds\n" \
"6:\nv_cmp_eq_u32 vcc, -1, v76\ns_cbranch_vccz 6f\nbuffer_load_dwordx4 v24, s[8:11], s94 offen offset:1024 sc1 lds\n" \
"6:\nv_cmp_eq_u32 vcc, -1, v84\ns_cbranch_vccz 6f\nbuffer_load_dwordx4 v24, s[8:11], s94 offen offset:2048 sc1 lds\n" \
"6:\nv_cmp_eq_u32 vcc, -1, v80\ns_cbranch_vccz 6f\nbuffer_load_dwordx4 v24, s[8:11], s94 offen offset:3072 sc1 lds\n" \
"6:\ns_waitcnt vmcnt(0)\nds_read_b128 v[40:43], v31 offset:16384\nds_read_b128 v[76:79], v31 offset:17408\n" \
"ds_read_b128 v[84:87], v31 offset:18432\nds_read_b128 v[80:83], v31 offset:19456\ns_waitcnt lgkmcnt(0)\nv_max3_u32 v96, v40, v76, v84\n" \
"v_max_u32 v96, v96, v80\nv_cmp_eq_u32 vcc, -1, v96\ns_cbranch_vccnz 2b\ns_branch 3b\n" \
"4:\ns_cmp_eq_u32 s30, 64\ns_cbranch_scc1 5b\nv_bfe_u32 v40, v24, 6, 3\n" \
"v_lshlrev_b32 v40, 2, v40\ns_lshl_b32 s94, s30, 5\ns_add_u32 s94, s94, s48\ns_cmp_lg_u32 s58, 0\n" \
"s_cbranch_scc1 8f\n7:\nbuffer_load_dword v41, v40, s[8:11], s94 offen sc1\ns_waitcnt vmcnt(0)\n" \
"v_cmp_eq_u32 vcc, -1, v41\ns_cbranch_vccz 8f\ns_add_u32 s52, s52, 1\ns_cmp_lt_u32 s52, 0x40000\n" \
"s_cbranch_scc0 9f\ns_sleep 8\ns_branch 7b\n8:\n" \
"s_mov_b32 m0, s53\ns_add_u32 s94, s30, 1\ns_min_u32 s94, s94, 63\ns_lshl_b32 s94, s94, 5\n" \
"s_add_u32 s94, s94, s48\nbuffer_load_dword v40, s[8:11], s94 offen offset:0 sc1 lds\ns_cmp_lg_u32 s58, 0\ns_mov_b32 s58, 0\n" \
"s_cbranch_scc1 6f\ns_add_u32 m0, s32, 0\ns_add_u32 s94, s31, 0\nbuffer_load_dwordx4 v24, s[8:11], s94 offen offset:0 sc1 lds\n" \
"buffer_load_dwordx4 v24, s[8:11], s94 offen offset:1024 sc1 lds\nbuffer_load_dwordx4 v24, s[8:11], s94 offen offset:2048 sc1 lds\nbuffer_load_dwordx4 v24, s[8:11], s94 offen offset:3072 sc1 lds\ns_add_u32 m0, s32, 0x2000\n" \
"s_add_u32 s94, s31, 0x2000\nbuffer_load_dwordx4 v24, s[8:11], s94 offen offset:0 sc1 lds\nbuffer_load_dwordx4 v24, s[8:11], s94 offen offset:1024 sc1 lds\nbuffer_load_dwordx4 v24, s[8:11], s94 offen offset:2048 sc1 lds\n" \
"buffer_load_dwordx4 v24, s[8:11], s94 offen offset:3072 sc1 lds\n6:\nv_lshrrev_b32 v40, 2, v24\nv_add_u32 v40, 0x11000, v40\n" \
"s_waitcnt vmcnt(0) lgkmcnt(0)\ns_barrier\nds_read_b32 v41, v40 offset:0\nds_read_b128 v[72:75], v24 offset:0\n" \
"ds_read_b128 v[32:35], v24 offset:32768\ns_waitcnt lgkmcnt(0)\nv_cmp_eq_u32 vcc, -1, v41\ns_cmp_eq_u64 vcc, 0\n" \
"s_cselect_b32 s14, s10, 0\ns_branch 5b\n10:\ns_waitcnt vmcnt(0) lgkmcnt(0)\n" \
"s_cmp_lt_i32 s6, 0\ns_cbranch_scc1 9f\ns_mov_b64 s[0:1], s[4:5]\ns_mov_b32 s2, s6\n" \
"v_lshrrev_b32 v0, 4, v24\nv_add_u32 v0, s7, v0\ns_barrier\ns_getpc_b64 s[8:9]\n" \
"11:\ns_add_u32 s8, s8, _Z5mega64Args-11b\ns_addc_u32 s9, s9, -1\ns_setpc_b64 s[8:9]\n" \
"9:\ns_waitcnt vmcnt(0)\ns_endpgm\n"
#define B_LDS 0x44840c0202a00110ull
#define B_VAR 0x0000000000010001ull
#define B_LV 8192
#define B_LOOP \
"v_mov_b32 v33, v20\nv_cmp_eq_u32 vcc, -1, v33\nv_bfe_u32 v32, v16, 4, 3\nv_lshlrev_b32 v32, 2, v32\n" \
"s_nop 2\ns_cbranch_vccz 6f\n7:\ns_add_u32 s30, s30, 1\n" \
"s_cmp_lt_u32 s30, 0x40000\ns_cbranch_scc0 9f\ns_sleep 8\nbuffer_load_dword v33, v32, s[8:11], s26 offen sc1\n" \
"s_waitcnt vmcnt(0)\nv_cmp_eq_u32 vcc, -1, v33\ns_nop 4\ns_cbranch_vccnz 7b\n" \
"s_add_u32 m0, s22, 0\ns_add_u32 s43, s21, 0\nbuffer_load_dwordx4 v16, s[8:11], s43 offen offset:0 sc1 lds\nbuffer_load_dwordx4 v16, s[8:11], s43 offen offset:1024 sc1 lds\n" \
"buffer_load_dwordx4 v16, s[8:11], s43 offen offset:2048 sc1 lds\nbuffer_load_dwordx4 v16, s[8:11], s43 offen offset:3072 sc1 lds\ns_add_u32 m0, s22, 0x2000\ns_add_u32 s43, s21, 0x2000\n" \
"buffer_load_dwordx4 v16, s[8:11], s43 offen offset:0 sc1 lds\nbuffer_load_dwordx4 v16, s[8:11], s43 offen offset:1024 sc1 lds\nbuffer_load_dwordx4 v16, s[8:11], s43 offen offset:2048 sc1 lds\nbuffer_load_dwordx4 v16, s[8:11], s43 offen offset:3072 sc1 lds\n" \
"s_add_u32 m0, s22, 0x4000\ns_add_u32 s43, s21, 0x4000\nbuffer_load_dwordx4 v16, s[8:11], s43 offen offset:0 sc1 lds\nbuffer_load_dwordx4 v16, s[8:11], s43 offen offset:1024 sc1 lds\n" \
"buffer_load_dwordx4 v16, s[8:11], s43 offen offset:2048 sc1 lds\nbuffer_load_dwordx4 v16, s[8:11], s43 offen offset:3072 sc1 lds\n6:\ns_waitcnt vmcnt(0)\n" \
"s_barrier\nds_read_b128 v[36:39], v16 offset:0\nds_read_b128 v[40:43], v16 offset:32768\nds_read_b128 v[72:75], v16 offset:1024\n" \
"ds_read_b128 v[80:83], v16 offset:33792\nds_read_b128 v[84:87], v16 offset:2048\nds_read_b128 v[88:91], v16 offset:34816\nds_read_b128 v[92:95], v16 offset:3072\n" \
"ds_read_b128 v[96:99], v16 offset:35840\nds_read_b128 v[48:51], v16 offset:4096\nds_read_b128 v[56:59], v16 offset:36864\nds_read_b128 v[60:63], v16 offset:5120\n" \
"ds_read_b128 v[64:67], v16 offset:37888\nds_read_b128 v[32:35], v16 offset:6144\nds_read_b128 v[76:79], v16 offset:38912\nds_read_b128 v[68:71], v16 offset:7168\n" \
"ds_read_b128 v[28:31], v16 offset:39936\ns_mov_b32 s20, -1\ns_add_u32 s21, s21, 0x10000\ns_mov_b32 s14, s10\n" \
"s_waitcnt lgkmcnt(0)\nv_mfma_f32_16x16x32_bf16 v[24:27], v[0:3], v[36:39], 0\nv_mfma_f32_16x16x32_bf16 v[44:47], v[0:3], v[40:43], 0\nv_mfma_f32_16x16x32_bf16 v[52:55], v[8:11], v[36:39], 0\n" \
"v_mfma_f32_16x16x32_bf16 v[20:23], v[8:11], v[40:43], 0\nv_mfma_f32_16x16x32_bf16 v[24:27], v[4:7], v[72:75], v[24:27]\nv_mfma_f32_16x16x32_bf16 v[44:47], v[4:7], v[80:83], v[44:47]\nv_mfma_f32_16x16x32_bf16 v[52:55], v[12:15], v[72:75], v[52:55]\n" \
"v_mfma_f32_16x16x32_bf16 v[20:23], v[12:15], v[80:83], v[20:23]\nv_mfma_f32_16x16x32_bf16 v[24:27], a[0:3], v[84:87], v[24:27]\nv_mfma_f32_16x16x32_bf16 v[44:47], a[0:3], v[88:91], v[44:47]\nv_mfma_f32_16x16x32_bf16 v[52:55], a[104:107], v[84:87], v[52:55]\n" \
"v_mfma_f32_16x16x32_bf16 v[20:23], a[104:107], v[88:91], v[20:23]\nv_mfma_f32_16x16x32_bf16 v[24:27], a[4:7], v[92:95], v[24:27]\nv_mfma_f32_16x16x32_bf16 v[44:47], a[4:7], v[96:99], v[44:47]\nv_mfma_f32_16x16x32_bf16 v[52:55], a[108:111], v[92:95], v[52:55]\n" \
"v_mfma_f32_16x16x32_bf16 v[20:23], a[108:111], v[96:99], v[20:23]\ns_branch 1f\n.p2align 6\n1:\n" \
"s_waitcnt lgkmcnt(4)\nv_mfma_f32_16x16x32_bf16 v[24:27], a[8:11], v[48:51], v[24:27]\ns_add_u32 m0, s22, 0\ns_add_u32 s43, s20, 7\n" \
"s_and_b32 s43, s43, 7\nv_mfma_f32_16x16x32_bf16 v[44:47], a[8:11], v[56:59], v[44:47]\nds_read_b128 v[36:39], v17 offset:0\ns_lshl_b32 s43, s43, 18\n" \
"v_mfma_f32_16x16x32_bf16 v[52:55], a[112:115], v[48:51], v[52:55]\ns_add_u32 s43, s43, s23\nv_mfma_f32_16x16x32_bf16 v[20:23], a[112:115], v[56:59], v[20:23]\nv_mfma_f32_16x16x32_bf16 v[24:27], a[12:15], v[60:63], v[24:27]\n" \
"s_waitcnt vmcnt(7) lgkmcnt(1)\ns_barrier\nv_mfma_f32_16x16x32_bf16 v[44:47], a[12:15], v[64:67], v[44:47]\nds_read_b128 v[40:43], v16 offset:8192\n" \
"v_mfma_f32_16x16x32_bf16 v[52:55], a[116:119], v[60:63], v[52:55]\nds_read_b128 v[48:51], v16 offset:40960\nv_mfma_f32_16x16x32_bf16 v[20:23], a[116:119], v[64:67], v[20:23]\nds_read_b128 v[60:63], v16 offset:9216\n" \
"v_mfma_f32_16x16x32_bf16 v[24:27], a[16:19], v[32:35], v[24:27]\nds_read_b128 v[64:67], v17 offset:1024\nv_mfma_f32_16x16x32_bf16 v[44:47], a[16:19], v[76:79], v[44:47]\nds_read_b128 v[56:59], v16 offset:41984\n" \
"v_mfma_f32_16x16x32_bf16 v[52:55], a[120:123], v[32:35], v[52:55]\nv_mfma_f32_16x16x32_bf16 v[20:23], a[120:123], v[76:79], v[20:23]\nbuffer_load_dwordx4 v16, s[8:11], s43 offen offset:0 sc1 lds\nv_mfma_f32_16x16x32_bf16 v[24:27], a[20:23], v[68:71], v[24:27]\n" \
"ds_read_b128 v[32:35], v16 offset:10240\nv_mfma_f32_16x16x32_bf16 v[44:47], a[20:23], v[28:31], v[44:47]\nds_read_b128 v[72:75], v16 offset:43008\nv_mfma_f32_16x16x32_bf16 v[52:55], a[124:127], v[68:71], v[52:55]\n" \
"s_waitcnt lgkmcnt(3)\nbuffer_load_dwordx4 v16, s[8:11], s43 offen offset:1024 sc1 lds\nv_mfma_f32_16x16x32_bf16 v[20:23], a[124:127], v[28:31], v[20:23]\nds_read_b128 v[68:71], v17 offset:4096\n" \
"v_mfma_f32_16x16x32_bf16 v[24:27], v[36:39], v[40:43], v[24:27]\nds_read_b128 v[28:31], v16 offset:11264\nv_mfma_f32_16x16x32_bf16 v[44:47], v[36:39], v[48:51], v[44:47]\nbuffer_load_dwordx4 v16, s[8:11], s43 offen offset:2048 sc1 lds\n" \
"v_mfma_f32_16x16x32_bf16 v[52:55], a[128:131], v[40:43], v[52:55]\nds_read_b128 v[76:79], v16 offset:44032\nv_mfma_f32_16x16x32_bf16 v[20:23], a[128:131], v[48:51], v[20:23]\ns_waitcnt lgkmcnt(3)\n" \
"ds_read_b128 v[36:39], v17 offset:5120\nv_mfma_f32_16x16x32_bf16 v[24:27], v[64:67], v[60:63], v[24:27]\nbuffer_load_dwordx4 v16, s[8:11], s43 offen offset:3072 sc1 lds\nv_mfma_f32_16x16x32_bf16 v[44:47], v[64:67], v[56:59], v[44:47]\n" \
"ds_read_b128 v[48:51], v16 offset:12288\ns_add_u32 m0, s22, 0x6000\nv_mfma_f32_16x16x32_bf16 v[52:55], a[132:135], v[60:63], v[52:55]\nds_read_b128 v[64:67], v16 offset:45056\n" \
"s_add_u32 s42, s21, -0xa000\nv_mfma_f32_16x16x32_bf16 v[20:23], a[132:135], v[56:59], v[20:23]\nds_read_b128 v[60:63], v16 offset:13312\nv_mfma_f32_16x16x32_bf16 v[24:27], a[24:27], v[32:35], v[24:27]\n" \
"s_waitcnt lgkmcnt(4)\nds_read_b128 v[40:43], v16 offset:46080\nv_mfma_f32_16x16x32_bf16 v[44:47], a[24:27], v[72:75], v[44:47]\nbuffer_load_dwordx4 v16, s[12:15], s42 offen offset:0 sc1 lds\n" \
"v_mfma_f32_16x16x32_bf16 v[52:55], v[68:71], v[32:35], v[52:55]\nv_mfma_f32_16x16x32_bf16 v[20:23], v[68:71], v[72:75], v[20:23]\nds_read_b128 v[56:59], v16 offset:14336\nv_mfma_f32_16x16x32_bf16 v[24:27], a[28:31], v[28:31], v[24:27]\n" \
"s_waitcnt lgkmcnt(3)\nbuffer_load_dwordx4 v16, s[12:15], s42 offen offset:1024 sc1 lds\nv_mfma_f32_16x16x32_bf16 v[44:47], a[28:31], v[76:79], v[44:47]\nds_read_b128 v[32:35], v16 offset:47104\n" \
"v_mfma_f32_16x16x32_bf16 v[52:55], v[36:39], v[28:31], v[52:55]\nds_read_b128 v[68:71], v17 offset:6144\nv_mfma_f32_16x16x32_bf16 v[20:23], v[36:39], v[76:79], v[20:23]\nbuffer_load_dwordx4 v16, s[12:15], s42 offen offset:2048 sc1 lds\n" \
"v_mfma_f32_16x16x32_bf16 v[24:27], a[32:35], v[48:51], v[24:27]\nds_read_b128 v[72:75], v16 offset:15360\nv_lshlrev_b32 v37, 5, v16\nv_mfma_f32_16x16x32_bf16 v[44:47], a[32:35], v[64:67], v[44:47]\n" \
"ds_read_b128 v[28:31], v16 offset:48128\nv_and_b32 v36, 0x7000, v37\nv_mfma_f32_16x16x32_bf16 v[52:55], a[136:139], v[48:51], v[52:55]\ns_waitcnt lgkmcnt(4)\n" \
"buffer_load_dwordx4 v16, s[12:15], s42 offen offset:3072 sc1 lds\nv_mfma_f32_16x16x32_bf16 v[20:23], a[136:139], v[64:67], v[20:23]\nds_read_b128 v[48:51], v17 offset:7168\ns_add_u32 m0, s22, 0\n" \
"v_mfma_f32_16x16x32_bf16 v[24:27], a[36:39], v[60:63], v[24:27]\nds_read_b128 v[64:67], v16 offset:16384\nv_lshl_add_u32 v37, v36, 17, v37\nv_mfma_f32_16x16x32_bf16 v[44:47], a[36:39], v[40:43], v[44:47]\n" \
"ds_read_b128 v[76:79], v17 offset:2048\nv_mfma_f32_16x16x32_bf16 v[52:55], a[140:143], v[60:63], v[52:55]\nds_read_b128 v[80:83], v16 offset:49152\nv_mfma_f32_16x16x32_bf16 v[20:23], a[140:143], v[40:43], v[20:23]\n" \
"s_waitcnt lgkmcnt(4)\nds_read_b128 v[60:63], v16 offset:17408\nv_mfma_f32_16x16x32_bf16 v[24:27], a[40:43], v[56:59], v[24:27]\nds_read_b128 v[38:41], v17 offset:3072\n" \
"v_mfma_f32_16x16x32_bf16 v[44:47], a[40:43], v[32:35], v[44:47]\nds_read_b128 v[84:87], v16 offset:50176\nv_mfma_f32_16x16x32_bf16 v[52:55], v[68:71], v[56:59], v[52:55]\nv_mfma_f32_16x16x32_bf16 v[20:23], v[68:71], v[32:35], v[20:23]\n" \
"ds_read_b128 v[56:59], v16 offset:18432\nv_mfma_f32_16x16x32_bf16 v[24:27], a[44:47], v[72:75], v[24:27]\ns_waitcnt lgkmcnt(4)\nds_read_b128 v[32:35], v16 offset:51200\n" \
"v_mfma_f32_16x16x32_bf16 v[44:47], a[44:47], v[28:31], v[44:47]\nds_read_b128 v[68:71], v17 offset:8192\nv_add_u32 v36, s22, v16\nv_mfma_f32_16x16x32_bf16 v[52:55], v[48:51], v[72:75], v[52:55]\n" \
"v_mfma_f32_16x16x32_bf16 v[20:23], v[48:51], v[28:31], v[20:23]\nds_read_b128 v[72:75], v16 offset:19456\nv_mfma_f32_16x16x32_bf16 v[24:27], v[76:79], v[64:67], v[24:27]\nds_read_b128 v[28:31], v16 offset:52224\n" \
"v_mfma_f32_16x16x32_bf16 v[44:47], v[76:79], v[80:83], v[44:47]\nds_read_b128 v[48:51], v17 offset:9216\nv_mfma_f32_16x16x32_bf16 v[52:55], a[144:147], v[64:67], v[52:55]\ns_waitcnt lgkmcnt(5)\n" \
"ds_read_b128 v[92:95], v17 offset:9216\nv_mfma_f32_16x16x32_bf16 v[20:23], a[144:147], v[80:83], v[20:23]\nds_read_b128 v[64:67], v16 offset:20480\nv_mfma_f32_16x16x32_bf16 v[24:27], v[38:41], v[60:63], v[24:27]\n" \
"ds_read_b128 v[76:79], v16 offset:53248\nv_mfma_f32_16x16x32_bf16 v[44:47], v[38:41], v[84:87], v[44:47]\nds_read_b128 v[88:91], v17 offset:8192\nv_mfma_f32_16x16x32_bf16 v[52:55], a[148:151], v[60:63], v[52:55]\n" \
"s_waitcnt vmcnt(4)\nds_read_b128 v[38:41], v36 offset:0\nds_read_b128 v[60:63], v36 offset:1024\nds_read_b128 v[80:83], v36 offset:2048\n" \
"v_mfma_f32_16x16x32_bf16 v[20:23], a[148:151], v[84:87], v[20:23]\ns_waitcnt lgkmcnt(7)\nds_read_b128 v[96:99], v36 offset:3072\nv_mfma_f32_16x16x32_bf16 v[24:27], a[48:51], v[56:59], v[24:27]\n" \
"ds_read_b128 v[100:103], v17 offset:4096\nv_mfma_f32_16x16x32_bf16 v[44:47], a[48:51], v[32:35], v[44:47]\nds_read_b128 v[104:107], v17 offset:5120\nv_mov_b64 v[84:85], 0\n" \
"v_mfma_f32_16x16x32_bf16 v[52:55], v[68:71], v[56:59], v[52:55]\ns_waitcnt lgkmcnt(3)\nv_max3_u32 v42, v38, v60, v80\nv_mov_b64 v[86:87], 0\n" \
"v_mfma_f32_16x16x32_bf16 v[20:23], v[68:71], v[32:35], v[20:23]\ns_waitcnt lgkmcnt(2)\nv_max_u32 v42, v42, v96\nv_cmp_eq_u32 vcc, -1, v42\n" \
"v_mfma_f32_16x16x32_bf16 v[24:27], a[52:55], v[72:75], v[24:27]\nv_mov_b64 v[32:33], 0\nv_mov_b64 v[34:35], 0\nv_mfma_f32_16x16x32_bf16 v[44:47], a[52:55], v[28:31], v[44:47]\n" \
"s_cbranch_vccnz 2f\n3:\nv_mfma_f32_16x16x32_bf16 v[52:55], v[48:51], v[72:75], v[52:55]\ns_barrier\n" \
"ds_read_b128 v[38:41], v16 offset:2048\nv_mfma_f32_16x16x32_bf16 v[20:23], v[92:95], v[28:31], v[20:23]\ns_memrealtime s[40:41]\nds_read_b128 v[56:59], v16 offset:5120\n" \
"v_mfma_f32_16x16x32_bf16 v[24:27], a[56:59], v[64:67], v[24:27]\nds_read_b128 v[28:31], v16 offset:1024\ns_lshl_b32 s43, s20, 24\nv_mfma_f32_16x16x32_bf16 v[44:47], a[56:59], v[76:79], v[44:47]\n" \
"ds_read_b128 v[48:51], v16 offset:33792\nv_mfma_f32_16x16x32_bf16 v[52:55], a[152:155], v[64:67], v[52:55]\nv_mfma_f32_16x16x32_bf16 v[20:23], a[152:155], v[76:79], v[20:23]\ns_waitcnt lgkmcnt(3)\n" \
"ds_read_b128 v[60:63], v16 offset:3072\nv_smfmac_f32_16x16x64_bf16 v[84:87], v[38:41], a[8:15], v18\ns_waitcnt lgkmcnt(3)\nv_smfmac_f32_16x16x64_bf16 v[32:35], v[38:41], a[112:119], v18\n" \
"ds_read_b128 v[64:67], v16 offset:34816\nv_smfmac_f32_16x16x64_bf16 v[84:87], v[56:59], a[24:31], v18\ns_waitcnt lgkmcnt(3)\nds_read_b128 v[68:71], v17 offset:6144\n" \
"v_smfmac_f32_16x16x64_bf16 v[32:35], v[56:59], v[100:107], v18\nds_read_b128 v[38:41], v16 offset:36864\nv_smfmac_f32_16x16x64_bf16 v[84:87], v[28:31], a[0:7], v18\ns_waitcnt lgkmcnt(4)\n" \
"ds_read_b128 v[72:75], v17 offset:7168\nv_smfmac_f32_16x16x64_bf16 v[32:35], v[28:31], a[104:111], v18\nds_read_b128 v[56:59], v16 offset:6144\ns_add_u32 s42, s40, 0x67\n" \
"v_smfmac_f32_16x16x64_bf16 v[84:87], v[48:51], a[48:55], v18\ns_and_b32 s42, s42, 0xffffff\ns_or_b32 s42, s42, s43\nv_mov_b32 v28, s42\n" \
"v_smfmac_f32_16x16x64_bf16 v[32:35], v[48:51], v[88:95], v18\ns_waitcnt lgkmcnt(4)\nds_read_b128 v[76:79], v16 offset:7168\nv_smfmac_f32_16x16x64_bf16 v[84:87], v[60:63], a[16:23], v18\n" \
"buffer_store_dword v28, v37, s[8:11], s27 offen offset:0 sc1\nv_smfmac_f32_16x16x64_bf16 v[32:35], v[60:63], a[120:127], v18\nds_read_b128 v[48:51], v16 offset:35840\nv_smfmac_f32_16x16x64_bf16 v[84:87], v[64:67], a[56:63], v18\n" \
"s_waitcnt lgkmcnt(4)\nv_smfmac_f32_16x16x64_bf16 v[32:35], v[64:67], a[152:159], v18\nds_read_b128 v[28:31], v16 offset:0\ns_add_u32 s16, s20, 1\n" \
"v_smfmac_f32_16x16x64_bf16 v[84:87], v[38:41], a[72:79], v18\nds_read_b128 v[60:63], v17 offset:0\nv_mov_b64 v[36:37], 0\nv_smfmac_f32_16x16x64_bf16 v[32:35], v[38:41], a[168:175], v18\n" \
"s_waitcnt lgkmcnt(4)\nds_read_b128 v[40:43], v16 offset:39936\nv_smfmac_f32_16x16x64_bf16 v[84:87], v[56:59], a[32:39], v18\nds_read_b128 v[64:67], v17 offset:1024\n" \
"v_mov_b64 v[38:39], 0\nv_smfmac_f32_16x16x64_bf16 v[32:35], v[56:59], a[136:143], v18\ns_waitcnt lgkmcnt(4)\nds_read_b128 v[56:59], v16 offset:37888\n" \
"v_smfmac_f32_16x16x64_bf16 v[84:87], v[76:79], a[40:47], v18\nds_read_b128 v[88:91], v17 offset:2048\ns_and_b32 s16, s16, 7\nv_smfmac_f32_16x16x64_bf16 v[32:35], v[76:79], v[68:75], v18\n" \
"ds_read_b128 v[80:83], v16 offset:4096\ns_lshl_b32 s16, s16, 22\nv_smfmac_f32_16x16x64_bf16 v[84:87], v[48:51], a[64:71], v18\nds_read_b128 v[92:95], v17 offset:3072\n" \
"s_add_u32 s16, s16, s25\nv_smfmac_f32_16x16x64_bf16 v[32:35], v[48:51], a[160:167], v18\ns_waitcnt lgkmcnt(6)\nds_read_b128 v[48:51], v16 offset:38912\n" \
"v_smfmac_f32_16x16x64_bf16 v[84:87], v[28:31], v[0:7], v18\nds_read_b128 v[72:75], v16 offset:21504\ns_add_u32 s17, s16, 0x1000\nv_smfmac_f32_16x16x64_bf16 v[32:35], v[28:31], v[8:15], v18\n" \
"s_waitcnt lgkmcnt(6)\nds_read_b128 v[28:31], v16 offset:32768\nv_smfmac_f32_16x16x64_bf16 v[84:87], v[40:43], a[96:103], v18\nds_read_b128 v[68:71], v16 offset:54272\n" \
"s_add_u32 s18, s16, 0x4000\nv_smfmac_f32_16x16x64_bf16 v[32:35], v[40:43], a[192:199], v18\ns_waitcnt lgkmcnt(5)\nds_read_b128 v[40:43], v16 offset:22528\n" \
"v_smfmac_f32_16x16x64_bf16 v[84:87], v[56:59], a[80:87], v18\nds_read_b128 v[76:79], v16 offset:55296\nv_lshrrev_b32 v96, 16, v18\nv_smfmac_f32_16x16x64_bf16 v[32:35], v[56:59], a[176:183], v18\n" \
"ds_write_b128 v16, v[36:39] offset:45056\nv_smfmac_f32_16x16x64_bf16 v[84:87], v[80:83], v[60:67], v18\ns_add_u32 s19, s16, 0x5000\ns_and_b32 s43, s20, 2\n" \
"s_lshl_b32 s43, s43, 12\nv_smfmac_f32_16x16x64_bf16 v[32:35], v[80:83], a[128:135], v18\ns_waitcnt lgkmcnt(4)\nds_read_b128 v[56:59], v16 offset:23552\n" \
"v_smfmac_f32_16x16x64_bf16 v[84:87], v[48:51], a[88:95], v18\nds_read_b128 v[60:63], v16 offset:56320\ns_and_b32 s42, s20, 7\nv_smfmac_f32_16x16x64_bf16 v[32:35], v[48:51], a[184:191], v18\n" \
"ds_read_b128 v[98:101], v17 offset:10240\ns_lshl_b32 s42, s42, 20\nv_smfmac_f32_16x16x64_bf16 v[84:87], v[28:31], v[88:95], v18\nds_read_b128 v[102:105], v17 offset:11264\n" \
"s_add_u32 s42, s42, s24\nv_smfmac_f32_16x16x64_bf16 v[32:35], v[28:31], a[144:151], v18\ns_waitcnt lgkmcnt(4)\nv_mfma_f32_16x16x32_bf16 v[24:27], a[60:63], v[72:75], v[24:27]\n" \
"v_add_f32 v84, v84, v86\nv_add_f32 v85, v85, v87\nv_cvt_pk_bf16_f32 v84, v84, v85\nv_mfma_f32_16x16x32_bf16 v[44:47], a[60:63], v[68:71], v[44:47]\n" \
"v_add_f32 v32, v32, v34\nv_add_f32 v33, v33, v35\nv_cvt_pk_bf16_f32 v32, v32, v33\nv_mfma_f32_16x16x32_bf16 v[52:55], a[156:159], v[72:75], v[52:55]\n" \
"ds_write2_b32 v96, v84, v32 offset0:0 offset1:4\nv_mfma_f32_16x16x32_bf16 v[20:23], a[156:159], v[68:71], v[20:23]\ns_add_u32 s99, s20, 2\nv_mov_b64 v[28:29], 0\n" \
"v_mov_b64 v[30:31], 0\nv_mfma_f32_16x16x32_bf16 v[24:27], a[64:67], v[40:43], v[24:27]\ns_min_u32 s99, s99, 63\nv_bfe_u32 v32, v16, 4, 3\n" \
"v_lshlrev_b32 v32, 2, v32\nv_mfma_f32_16x16x32_bf16 v[44:47], a[64:67], v[76:79], v[44:47]\ns_waitcnt vmcnt(1) lgkmcnt(0)\ns_barrier\n" \
"ds_read_b64_tr_b16 v[48:49], v19 offset:0\nv_mfma_f32_16x16x32_bf16 v[52:55], a[160:163], v[40:43], v[52:55]\nds_read_b64_tr_b16 v[50:51], v19 offset:256\nds_read_b64_tr_b16 v[40:41], v19 offset:1024\n" \
"ds_read_b64_tr_b16 v[42:43], v19 offset:1280\nv_mfma_f32_16x16x32_bf16 v[20:23], a[160:163], v[76:79], v[20:23]\nds_read_b128 v[64:67], v16 offset:24576\nv_mov_b64 v[68:69], 0\n" \
"v_mfma_f32_16x16x32_bf16 v[24:27], a[68:71], v[56:59], v[24:27]\ns_lshl_b32 s99, s99, 5\ns_add_u32 s99, s99, s26\nv_mov_b64 v[70:71], 0\n" \
"v_mfma_f32_16x16x32_bf16 v[44:47], a[68:71], v[60:63], v[44:47]\ns_waitcnt lgkmcnt(3)\nbuffer_load_dword v33, v32, s[8:11], s99 offen sc1\nv_mfma_f32_16x16x32_bf16 v[52:55], a[164:167], v[56:59], v[52:55]\n" \
"ds_read_b128 v[72:75], v16 offset:25600\nv_mov_b64 v[76:77], 0\nv_smfmac_f32_16x16x64_bf16 v[36:39], v[48:51], a[200:207], v18\ns_waitcnt lgkmcnt(2)\n" \
"ds_read_b128 v[88:91], v17 offset:12288\nv_smfmac_f32_16x16x64_bf16 v[36:39], v[40:43], v[98:105], v18\nds_read_b128 v[56:59], v16 offset:45056\nv_mov_b64 v[78:79], 0\n" \
"v_smfmac_f32_16x16x64_bf16 v[28:31], v[48:51], a[224:231], v18\nds_read_b128 v[92:95], v17 offset:13312\nv_mfma_f32_16x16x32_bf16 v[20:23], a[164:167], v[60:63], v[20:23]\nds_read_b128 v[80:83], v17 offset:14336\n" \
"v_add_f32 v36, v36, v38\nv_smfmac_f32_16x16x64_bf16 v[28:31], v[40:43], a[232:239], v18\ns_waitcnt lgkmcnt(5)\nds_read_b128 v[60:63], v16 offset:45056\n" \
"v_mfma_f32_16x16x32_bf16 v[24:27], a[72:75], v[64:67], v[24:27]\nds_read_b128 v[84:87], v17 offset:15360\nv_add_f32 v37, v37, v39\nv_smfmac_f32_16x16x64_bf16 v[68:71], v[48:51], a[208:215], v18\n" \
"ds_read_b128 v[108:111], v16 offset:26624\nv_add_f32 v28, v28, v30\nv_smfmac_f32_16x16x64_bf16 v[68:71], v[40:43], a[216:223], v18\nds_read_b128 v[96:99], v16 offset:45056\n" \
"v_add_f32 v29, v29, v31\nv_smfmac_f32_16x16x64_bf16 v[76:79], v[48:51], a[240:247], v18\nds_read_b128 v[100:103], v17 offset:16384\nv_cvt_pk_bf16_f32 v34, v36, v37\n" \
"v_mfma_f32_16x16x32_bf16 v[52:55], a[168:171], v[64:67], v[52:55]\ns_waitcnt lgkmcnt(5)\nv_add_f32 v68, v68, v70\nv_add_f32 v69, v69, v71\n" \
"v_smfmac_f32_16x16x64_bf16 v[76:79], v[40:43], a[248:255], v18\nds_read_b128 v[64:67], v16 offset:45056\nv_cvt_pk_bf16_f32 v35, v28, v29\nv_smfmac_f32_16x16x64_bf16 v[56:59], v[48:51], v[112:119], v18\n" \
"ds_read_b128 v[104:107], v17 offset:17408\nv_cvt_pk_bf16_f32 v36, v68, v69\nv_mfma_f32_16x16x32_bf16 v[24:27], a[76:79], v[72:75], v[24:27]\ns_waitcnt lgkmcnt(4)\n" \
"v_permlane32_swap_b32 v34, v35\nv_smfmac_f32_16x16x64_bf16 v[56:59], v[40:43], v[120:127], v18\nds_read_b128 v[68:71], v16 offset:45056\nv_add_f32 v76, v76, v78\n" \
"v_smfmac_f32_16x16x64_bf16 v[60:63], v[48:51], v[128:135], v18\nds_read_b128 v[28:31], v16 offset:27648\nv_add_f32 v77, v77, v79\nv_cvt_pk_bf16_f32 v37, v76, v77\n" \
"v_smfmac_f32_16x16x64_bf16 v[60:63], v[40:43], v[136:143], v18\nds_read_b128 v[76:79], v17 offset:18432\nv_permlane32_swap_b32 v36, v37\nv_mfma_f32_16x16x32_bf16 v[52:55], a[172:175], v[72:75], v[52:55]\n" \
"s_waitcnt lgkmcnt(4)\nv_permlane16_swap_b32 v34, v36\nv_smfmac_f32_16x16x64_bf16 v[96:99], v[48:51], v[88:95], v18\nds_read_b128 v[72:75], v16 offset:45056\n" \
"v_permlane16_swap_b32 v35, v37\nv_and_or_b32 v34, v34, -2, s32\nv_smfmac_f32_16x16x64_bf16 v[96:99], v[40:43], v[80:87], v18\nds_read_b128 v[88:91], v16 offset:28672\n" \
"v_add_f32 v56, v56, v58\nv_mfma_f32_16x16x32_bf16 v[24:27], a[80:83], v[108:111], v[24:27]\nbuffer_store_dwordx4 v[34:37], v16, s[8:11], s29 offen offset:0 sc1\nv_add_f32 v57, v57, v59\n" \
"v_add_f32 v60, v60, v62\nv_add_f32 v61, v61, v63\nv_smfmac_f32_16x16x64_bf16 v[64:67], v[48:51], v[144:151], v18\nds_read_b128 v[34:37], v16 offset:45056\n" \
"v_add_f32 v96, v96, v98\nv_smfmac_f32_16x16x64_bf16 v[64:67], v[40:43], v[152:159], v18\nds_read_b128 v[80:83], v17 offset:19456\nv_add_f32 v97, v97, v99\n" \
"v_mfma_f32_16x16x32_bf16 v[52:55], a[176:179], v[108:111], v[52:55]\ns_waitcnt lgkmcnt(3)\nv_cvt_pk_bf16_f32 v58, v56, v57\nv_cvt_pk_bf16_f32 v59, v60, v61\n" \
"v_smfmac_f32_16x16x64_bf16 v[68:71], v[48:51], v[160:167], v18\nds_read_b128 v[92:95], v16 offset:45056\nv_add_f32 v64, v64, v66\nv_smfmac_f32_16x16x64_bf16 v[68:71], v[40:43], v[168:175], v18\n" \
"ds_read_b128 v[84:87], v16 offset:45056\nv_add_f32 v65, v65, v67\nv_mfma_f32_16x16x32_bf16 v[24:27], a[84:87], v[28:31], v[24:27]\nv_permlane32_swap_b32 v58, v59\n" \
"v_cvt_pk_bf16_f32 v60, v96, v97\nv_cvt_pk_bf16_f32 v61, v64, v65\nv_smfmac_f32_16x16x64_bf16 v[72:75], v[48:51], v[184:191], v18\nds_read_b128 v[62:65], v16 offset:29696\n" \
"v_permlane32_swap_b32 v60, v61\nv_smfmac_f32_16x16x64_bf16 v[72:75], v[40:43], v[192:199], v18\ns_waitcnt lgkmcnt(3)\nv_permlane16_swap_b32 v58, v60\n" \
"v_mfma_f32_16x16x32_bf16 v[52:55], a[180:183], v[28:31], v[52:55]\nv_permlane16_swap_b32 v59, v61\nv_and_or_b32 v58, v58, -2, s32\nv_smfmac_f32_16x16x64_bf16 v[34:37], v[48:51], v[100:107], v18\n" \
"ds_read_b128 v[28:31], v16 offset:45056\nv_add_f32 v68, v68, v70\nv_smfmac_f32_16x16x64_bf16 v[34:37], v[40:43], v[176:183], v18\ns_waitcnt lgkmcnt(2)\n" \
"buffer_store_dwordx4 v[58:61], v16, s[8:11], s29 offen offset:1024 sc1\nds_read_b128 v[96:99], v17 offset:20480\nv_add_f32 v69, v69, v71\nv_mfma_f32_16x16x32_bf16 v[24:27], a[88:91], v[88:91], v[24:27]\n" \
"ds_read_b128 v[100:103], v17 offset:21504\nv_add_f32 v72, v72, v74\nv_smfmac_f32_16x16x64_bf16 v[92:95], v[48:51], v[200:207], v18\nds_read_b128 v[56:59], v16 offset:45056\n" \
"v_add_f32 v73, v73, v75\nv_smfmac_f32_16x16x64_bf16 v[92:95], v[40:43], v[76:83], v18\nds_read_b128 v[104:107], v16 offset:30720\nv_add_f32 v34, v34, v36\n" \
"v_smfmac_f32_16x16x64_bf16 v[84:87], v[48:51], v[208:215], v18\nds_read_b128 v[74:77], v16 offset:45056\nv_add_f32 v35, v35, v37\nv_mfma_f32_16x16x32_bf16 v[52:55], a[184:187], v[88:91], v[52:55]\n" \
"s_waitcnt lgkmcnt(5)\nv_add_f32 v92, v92, v94\nv_add_f32 v93, v93, v95\nv_smfmac_f32_16x16x64_bf16 v[84:87], v[40:43], v[216:223], v18\n" \
"ds_read_b128 v[88:91], v17 offset:22528\nv_cvt_pk_bf16_f32 v36, v68, v69\nv_mfma_f32_16x16x32_bf16 v[24:27], a[92:95], v[62:65], v[24:27]\nv_cvt_pk_bf16_f32 v37, v72, v73\n" \
"v_cvt_pk_bf16_f32 v38, v34, v35\nv_cvt_pk_bf16_f32 v39, v92, v93\nv_smfmac_f32_16x16x64_bf16 v[28:31], v[48:51], v[232:239], v18\nds_read_b128 v[92:95], v17 offset:23552\n" \
"v_permlane32_swap_b32 v36, v37\nv_permlane32_swap_b32 v38, v39\nv_smfmac_f32_16x16x64_bf16 v[28:31], v[40:43], v[240:247], v18\nds_read_b128 v[66:69], v16 offset:31744\n" \
"v_permlane16_swap_b32 v36, v38\nv_mfma_f32_16x16x32_bf16 v[52:55], a[188:191], v[62:65], v[52:55]\ns_waitcnt lgkmcnt(3)\nv_permlane16_swap_b32 v37, v39\n" \
"v_and_or_b32 v36, v36, -2, s32\nv_smfmac_f32_16x16x64_bf16 v[56:59], v[48:51], v[96:103], v18\nds_read_b128 v[60:63], v16 offset:57344\nv_add_f32 v84, v84, v86\n" \
"v_smfmac_f32_16x16x64_bf16 v[56:59], v[40:43], v[224:231], v18\nds_read_b128 v[70:73], v16 offset:58368\nv_add_f32 v85, v85, v87\nv_mfma_f32_16x16x32_bf16 v[24:27], a[96:99], v[104:107], v[24:27]\n" \
"s_waitcnt lgkmcnt(3)\nbuffer_store_dwordx4 v[36:39], v16, s[8:11], s29 offen offset:2048 sc1\nv_add_f32 v28, v28, v30\nv_add_f32 v29, v29, v31\n" \
"v_cvt_pk_bf16_f32 v34, v84, v85\nv_smfmac_f32_16x16x64_bf16 v[74:77], v[48:51], v[88:95], v18\nds_read_b128 v[78:81], v16 offset:59392\nv_add_f32 v56, v56, v58\n" \
"v_add_f32 v57, v57, v59\nv_cvt_pk_bf16_f32 v35, v28, v29\nv_cvt_pk_bf16_f32 v36, v56, v57\nv_smfmac_f32_16x16x64_bf16 v[74:77], v[40:43], v[248:255], v18\n" \
"ds_read_b128 v[48:51], v16 offset:60416\nv_permlane32_swap_b32 v34, v35\nv_mfma_f32_16x16x32_bf16 v[52:55], a[192:195], v[104:107], v[52:55]\ns_waitcnt lgkmcnt(3)\n" \
"ds_read_b128 v[28:31], v16 offset:61440\nv_mfma_f32_16x16x32_bf16 v[24:27], a[100:103], v[66:69], v[24:27]\nv_add_f32 v74, v74, v76\nv_add_f32 v75, v75, v77\n" \
"v_cvt_pk_bf16_f32 v37, v74, v75\nv_mfma_f32_16x16x32_bf16 v[52:55], a[196:199], v[66:69], v[52:55]\nds_read_b128 v[38:41], v16 offset:62464\nv_permlane32_swap_b32 v36, v37\n" \
"v_mfma_f32_16x16x32_bf16 v[44:47], a[72:75], v[60:63], v[44:47]\ns_waitcnt lgkmcnt(3)\nv_permlane16_swap_b32 v34, v36\nv_permlane16_swap_b32 v35, v37\n" \
"v_and_or_b32 v34, v34, -2, s32\nv_mfma_f32_16x16x32_bf16 v[20:23], a[168:171], v[60:63], v[20:23]\nbuffer_store_dwordx4 v[34:37], v16, s[8:11], s29 offen offset:3072 sc1\ns_bfe_u32 s32, s20, 0x10003\n" \
"v_cvt_pk_bf16_f32 v24, v24, v25\nv_cvt_pk_bf16_f32 v25, v26, v27\nv_mfma_f32_16x16x32_bf16 v[44:47], a[76:79], v[70:73], v[44:47]\ns_xor_b32 s29, s42, s43\n" \
"v_cvt_pk_bf16_f32 v26, v52, v53\nv_cvt_pk_bf16_f32 v27, v54, v55\nv_mfma_f32_16x16x32_bf16 v[20:23], a[172:175], v[70:73], v[20:23]\nds_read_b128 v[34:37], v16 offset:63488\n" \
"v_mfma_f32_16x16x32_bf16 v[44:47], a[80:83], v[78:81], v[44:47]\ns_waitcnt vmcnt(4) lgkmcnt(1)\nv_cmp_eq_u32 s[42:43], -1, v33\nv_mfma_f32_16x16x32_bf16 v[20:23], a[176:179], v[78:81], v[20:23]\n" \
"ds_read_b128 v[52:55], v16 offset:64512\nv_mfma_f32_16x16x32_bf16 v[44:47], a[84:87], v[48:51], v[44:47]\ns_cmp_eq_u64 s[42:43], 0\ns_cselect_b32 s14, s10, 0\n" \
"s_cmp_lt_i32 s20, 62\ns_cselect_b32 s14, s14, 0\ns_cmp_eq_u32 s14, 0\ns_cbranch_scc1 4f\n" \
"5:\nv_add_u32 v32, s28, v16\nv_mfma_f32_16x16x32_bf16 v[20:23], a[180:183], v[48:51], v[20:23]\nds_write_b128 v32, v[24:27] offset:0\n" \
"s_add_u32 m0, s22, 0\nv_mfma_f32_16x16x32_bf16 v[44:47], a[88:91], v[28:31], v[44:47]\nbuffer_load_dwordx4 v16, s[12:15], s21 offen offset:0 sc1 lds\nv_mfma_f32_16x16x32_bf16 v[20:23], a[184:187], v[28:31], v[20:23]\n" \
"ds_read_b128 v[60:63], v17 offset:10240\ns_add_u32 s20, s20, 1\nv_mfma_f32_16x16x32_bf16 v[44:47], a[92:95], v[38:41], v[44:47]\ns_waitcnt lgkmcnt(1)\n" \
"s_barrier\nds_read_b128 v[48:51], v16 offset:8192\nv_mfma_f32_16x16x32_bf16 v[20:23], a[188:191], v[38:41], v[20:23]\nds_read_b128 v[68:71], v16 offset:9216\n" \
"v_mfma_f32_16x16x32_bf16 v[44:47], a[96:99], v[34:37], v[44:47]\nds_read_b128 v[24:27], v16 offset:10240\nv_add_u32 v38, s28, v16\nv_mfma_f32_16x16x32_bf16 v[20:23], a[192:195], v[34:37], v[20:23]\n" \
"buffer_load_dwordx4 v16, s[12:15], s21 offen offset:1024 sc1 lds\nds_read_b128 v[56:59], v16 offset:11264\nds_read_b128 v[32:35], v17 offset:11264\nv_mfma_f32_16x16x32_bf16 v[44:47], a[100:103], v[52:55], v[44:47]\n" \
"s_waitcnt lgkmcnt(4)\nv_mfma_f32_16x16x32_bf16 v[20:23], a[196:199], v[52:55], v[20:23]\ns_waitcnt lgkmcnt(3)\nv_mfma_f32_16x16x32_bf16 v[28:31], v[48:51], a[200:203], 0\n" \
"s_waitcnt lgkmcnt(2)\nv_cvt_pk_bf16_f32 v44, v44, v45\nv_cvt_pk_bf16_f32 v45, v46, v47\nv_mfma_f32_16x16x32_bf16 v[28:31], v[68:71], a[204:207], v[28:31]\n" \
"s_waitcnt lgkmcnt(0)\nv_cvt_pk_bf16_f32 v46, v20, v21\nv_cvt_pk_bf16_f32 v47, v22, v23\nv_mfma_f32_16x16x32_bf16 v[28:31], v[24:27], v[60:63], v[28:31]\n" \
"buffer_load_dwordx4 v16, s[12:15], s21 offen offset:2048 sc1 lds\nv_mfma_f32_16x16x32_bf16 v[28:31], v[56:59], v[32:35], v[28:31]\nds_write_b128 v38, v[44:47] offset:32768\nv_mfma_f32_16x16x32_bf16 v[40:43], v[48:51], a[208:211], 0\n" \
"v_mfma_f32_16x16x32_bf16 v[40:43], v[68:71], a[212:215], v[40:43]\nv_cvt_pk_bf16_f32 v28, v28, v29\nv_cvt_pk_bf16_f32 v29, v30, v31\nv_mfma_f32_16x16x32_bf16 v[40:43], v[24:27], a[216:219], v[40:43]\n" \
"s_add_u32 s43, s21, 0x4000\nds_read_b128 v[60:63], v17 offset:18432\nv_mfma_f32_16x16x32_bf16 v[40:43], v[56:59], a[220:223], v[40:43]\ns_waitcnt lgkmcnt(1)\n" \
"s_barrier\nv_mfma_f32_16x16x32_bf16 v[20:23], v[48:51], a[224:227], 0\nbuffer_load_dwordx4 v16, s[12:15], s21 offen offset:3072 sc1 lds\nv_mfma_f32_16x16x32_bf16 v[20:23], v[68:71], a[228:231], v[20:23]\n" \
"s_add_u32 m0, s22, 0x4000\nds_read_b128 v[64:67], v17 offset:19456\nv_mfma_f32_16x16x32_bf16 v[20:23], v[24:27], a[232:235], v[20:23]\nbuffer_load_dwordx4 v16, s[12:15], s43 offen offset:0 sc1 lds\n" \
"v_mfma_f32_16x16x32_bf16 v[20:23], v[56:59], a[236:239], v[20:23]\nds_read_b128 v[32:35], v16 offset:40960\nv_cvt_pk_bf16_f32 v40, v40, v41\nv_mfma_f32_16x16x32_bf16 v[44:47], v[48:51], v[144:147], 0\n" \
"ds_read_b128 v[36:39], v16 offset:41984\nv_cvt_pk_bf16_f32 v41, v42, v43\nv_mfma_f32_16x16x32_bf16 v[44:47], v[68:71], v[148:151], v[44:47]\nds_read_b128 v[52:55], v16 offset:43008\n" \
"v_cvt_pk_bf16_f32 v20, v20, v21\nv_mfma_f32_16x16x32_bf16 v[44:47], v[24:27], v[152:155], v[44:47]\nds_read_b128 v[72:75], v17 offset:10240\nv_cvt_pk_bf16_f32 v21, v22, v23\n" \
"v_mfma_f32_16x16x32_bf16 v[44:47], v[56:59], v[156:159], v[44:47]\nbuffer_load_dwordx4 v16, s[12:15], s43 offen offset:1024 sc1 lds\nv_mfma_f32_16x16x32_bf16 v[84:87], v[48:51], v[200:203], 0\ns_waitcnt lgkmcnt(4)\n" \
"ds_read_b128 v[76:79], v16 offset:44032\nv_mfma_f32_16x16x32_bf16 v[84:87], v[68:71], v[204:207], v[84:87]\nds_read_b128 v[80:83], v17 offset:11264\nv_cvt_pk_bf16_f32 v44, v44, v45\n" \
"v_mfma_f32_16x16x32_bf16 v[84:87], v[24:27], v[60:63], v[84:87]\ns_waitcnt lgkmcnt(4)\nv_cvt_pk_bf16_f32 v45, v46, v47\nv_mfma_f32_16x16x32_bf16 v[84:87], v[56:59], v[64:67], v[84:87]\n" \
"v_mfma_f32_16x16x32_bf16 v[60:63], v[32:35], a[200:203], 0\ns_waitcnt lgkmcnt(2)\nbuffer_load_dwordx4 v16, s[12:15], s43 offen offset:2048 sc1 lds\nv_mfma_f32_16x16x32_bf16 v[60:63], v[36:39], a[204:207], v[60:63]\n" \
"v_cvt_pk_bf16_f32 v84, v84, v85\nv_cvt_pk_bf16_f32 v85, v86, v87\nv_mfma_f32_16x16x32_bf16 v[60:63], v[52:55], v[72:75], v[60:63]\ns_waitcnt lgkmcnt(0)\n" \
"v_mfma_f32_16x16x32_bf16 v[60:63], v[76:79], v[80:83], v[60:63]\nv_mfma_f32_16x16x32_bf16 v[64:67], v[32:35], a[208:211], 0\nbuffer_load_dwordx4 v16, s[12:15], s43 offen offset:3072 sc1 lds\nv_mfma_f32_16x16x32_bf16 v[64:67], v[36:39], a[212:215], v[64:67]\n" \
"v_cvt_pk_bf16_f32 v30, v60, v61\nv_cvt_pk_bf16_f32 v31, v62, v63\nv_mfma_f32_16x16x32_bf16 v[64:67], v[52:55], a[216:219], v[64:67]\nds_read_b128 v[60:63], v17 offset:18432\n" \
"v_mfma_f32_16x16x32_bf16 v[64:67], v[76:79], a[220:223], v[64:67]\nds_read_b128 v[80:83], v17 offset:19456\nv_mfma_f32_16x16x32_bf16 v[72:75], v[32:35], a[224:227], 0\nv_mfma_f32_16x16x32_bf16 v[72:75], v[36:39], a[228:231], v[72:75]\n" \
"buffer_store_dwordx4 v[28:31], v16, s[8:11], s16 offen offset:0 sc1\nv_cvt_pk_bf16_f32 v42, v64, v65\nv_cvt_pk_bf16_f32 v43, v66, v67\nv_mfma_f32_16x16x32_bf16 v[72:75], v[52:55], a[232:235], v[72:75]\n" \
"v_mfma_f32_16x16x32_bf16 v[72:75], v[76:79], a[236:239], v[72:75]\nv_mfma_f32_16x16x32_bf16 v[28:31], v[32:35], v[144:147], 0\nv_mfma_f32_16x16x32_bf16 v[28:31], v[36:39], v[148:151], v[28:31]\nv_cvt_pk_bf16_f32 v22, v72, v73\n" \
"v_cvt_pk_bf16_f32 v23, v74, v75\nv_mfma_f32_16x16x32_bf16 v[28:31], v[52:55], v[152:155], v[28:31]\nv_mfma_f32_16x16x32_bf16 v[28:31], v[76:79], v[156:159], v[28:31]\nv_mfma_f32_16x16x32_bf16 v[64:67], v[32:35], v[200:203], 0\n" \
"s_waitcnt lgkmcnt(0)\nbuffer_store_dwordx4 v[40:43], v16, s[8:11], s16 offen offset:1024 sc1\nv_mfma_f32_16x16x32_bf16 v[64:67], v[36:39], v[204:207], v[64:67]\nv_cvt_pk_bf16_f32 v46, v28, v29\n" \
"v_cvt_pk_bf16_f32 v47, v30, v31\nv_mfma_f32_16x16x32_bf16 v[64:67], v[52:55], v[60:63], v[64:67]\nv_mfma_f32_16x16x32_bf16 v[64:67], v[76:79], v[80:83], v[64:67]\nv_mfma_f32_16x16x32_bf16 v[28:31], v[48:51], a[240:243], 0\n" \
"v_mfma_f32_16x16x32_bf16 v[40:43], v[32:35], a[240:243], 0\nv_cvt_pk_bf16_f32 v86, v64, v65\nv_cvt_pk_bf16_f32 v87, v66, v67\nv_mfma_f32_16x16x32_bf16 v[28:31], v[68:71], a[244:247], v[28:31]\n" \
"v_mfma_f32_16x16x32_bf16 v[40:43], v[36:39], a[244:247], v[40:43]\nv_mfma_f32_16x16x32_bf16 v[28:31], v[24:27], a[248:251], v[28:31]\nbuffer_store_dwordx4 v[20:23], v16, s[8:11], s16 offen offset:2048 sc1\nds_read_b128 v[60:63], v17 offset:12288\n" \
"v_mfma_f32_16x16x32_bf16 v[40:43], v[52:55], a[248:251], v[40:43]\nv_mfma_f32_16x16x32_bf16 v[28:31], v[56:59], a[252:255], v[28:31]\nds_read_b128 v[20:23], v17 offset:13312\nv_mfma_f32_16x16x32_bf16 v[40:43], v[76:79], a[252:255], v[40:43]\n" \
"v_mfma_f32_16x16x32_bf16 v[80:83], v[48:51], v[128:131], 0\nds_read_b128 v[64:67], v17 offset:14336\nv_cvt_pk_bf16_f32 v28, v28, v29\nv_mfma_f32_16x16x32_bf16 v[72:75], v[32:35], v[128:131], 0\n" \
"v_cvt_pk_bf16_f32 v29, v30, v31\nv_cvt_pk_bf16_f32 v30, v40, v41\nv_mfma_f32_16x16x32_bf16 v[80:83], v[68:71], v[132:135], v[80:83]\ns_waitcnt vmcnt(7)\n" \
"s_barrier\nv_mfma_f32_16x16x32_bf16 v[72:75], v[36:39], v[132:135], v[72:75]\nbuffer_store_dwordx4 v[44:47], v16, s[8:11], s17 offen offset:3072 sc1\nds_read_b128 v[88:91], v17 offset:15360\n" \
"v_cvt_pk_bf16_f32 v31, v42, v43\nv_mfma_f32_16x16x32_bf16 v[80:83], v[24:27], v[136:139], v[80:83]\nv_mfma_f32_16x16x32_bf16 v[72:75], v[52:55], v[136:139], v[72:75]\nv_mfma_f32_16x16x32_bf16 v[80:83], v[56:59], v[140:143], v[80:83]\n" \
"s_waitcnt lgkmcnt(1)\nv_mfma_f32_16x16x32_bf16 v[72:75], v[76:79], v[140:143], v[72:75]\nv_mfma_f32_16x16x32_bf16 v[44:47], v[48:51], v[60:63], 0\nv_cvt_pk_bf16_f32 v80, v80, v81\n" \
"v_cvt_pk_bf16_f32 v81, v82, v83\nv_mfma_f32_16x16x32_bf16 v[40:43], v[32:35], v[60:63], 0\nv_cvt_pk_bf16_f32 v82, v72, v73\nv_cvt_pk_bf16_f32 v83, v74, v75\n" \
"v_mfma_f32_16x16x32_bf16 v[44:47], v[68:71], v[20:23], v[44:47]\nv_mfma_f32_16x16x32_bf16 v[40:43], v[36:39], v[20:23], v[40:43]\nbuffer_store_dwordx4 v[84:87], v16, s[8:11], s18 offen offset:3072 sc1\ns_add_u32 m0, s22, 0x2000\n" \
"s_add_u32 s43, s21, 0x2000\ns_add_u32 s21, s21, 0x10000\nv_mfma_f32_16x16x32_bf16 v[44:47], v[24:27], v[64:67], v[44:47]\ns_waitcnt lgkmcnt(0)\n" \
"ds_read_b128 v[20:23], v17 offset:16384\nv_mfma_f32_16x16x32_bf16 v[40:43], v[52:55], v[64:67], v[40:43]\nv_mfma_f32_16x16x32_bf16 v[44:47], v[56:59], v[88:91], v[44:47]\nds_read_b128 v[60:63], v17 offset:17408\n" \
"v_mfma_f32_16x16x32_bf16 v[40:43], v[76:79], v[88:91], v[40:43]\nbuffer_load_dwordx4 v16, s[12:15], s43 offen offset:0 sc1 lds\nv_mfma_f32_16x16x32_bf16 v[64:67], v[48:51], v[112:115], 0\nv_cvt_pk_bf16_f32 v44, v44, v45\n" \
"v_cvt_pk_bf16_f32 v45, v46, v47\nv_mfma_f32_16x16x32_bf16 v[72:75], v[32:35], v[112:115], 0\nv_cvt_pk_bf16_f32 v46, v40, v41\nv_cvt_pk_bf16_f32 v47, v42, v43\n" \
"v_mfma_f32_16x16x32_bf16 v[64:67], v[68:71], v[116:119], v[64:67]\nbuffer_store_dwordx4 v[28:31], v16, s[8:11], s16 offen offset:3072 sc1\nv_mfma_f32_16x16x32_bf16 v[72:75], v[36:39], v[116:119], v[72:75]\nv_mfma_f32_16x16x32_bf16 v[64:67], v[24:27], v[120:123], v[64:67]\n" \
"v_mfma_f32_16x16x32_bf16 v[72:75], v[52:55], v[120:123], v[72:75]\nv_mfma_f32_16x16x32_bf16 v[64:67], v[56:59], v[124:127], v[64:67]\ns_waitcnt lgkmcnt(0)\nv_mfma_f32_16x16x32_bf16 v[72:75], v[76:79], v[124:127], v[72:75]\n" \
"v_mfma_f32_16x16x32_bf16 v[28:31], v[48:51], v[20:23], 0\nv_cvt_pk_bf16_f32 v64, v64, v65\nv_cvt_pk_bf16_f32 v65, v66, v67\nv_mfma_f32_16x16x32_bf16 v[40:43], v[32:35], v[20:23], 0\n" \
"buffer_store_dwordx4 v[80:83], v16, s[8:11], s17 offen offset:2048 sc1\nbuffer_load_dwordx4 v16, s[12:15], s43 offen offset:1024 sc1 lds\nv_cvt_pk_bf16_f32 v66, v72, v73\nv_mfma_f32_16x16x32_bf16 v[28:31], v[68:71], v[60:63], v[28:31]\n" \
"v_cvt_pk_bf16_f32 v67, v74, v75\nv_mfma_f32_16x16x32_bf16 v[40:43], v[36:39], v[60:63], v[40:43]\nv_mfma_f32_16x16x32_bf16 v[28:31], v[24:27], v[176:179], v[28:31]\nv_mfma_f32_16x16x32_bf16 v[40:43], v[52:55], v[176:179], v[40:43]\n" \
"v_mfma_f32_16x16x32_bf16 v[28:31], v[56:59], v[180:183], v[28:31]\nv_mfma_f32_16x16x32_bf16 v[40:43], v[76:79], v[180:183], v[40:43]\nv_mfma_f32_16x16x32_bf16 v[20:23], v[48:51], v[184:187], 0\nv_cvt_pk_bf16_f32 v28, v28, v29\n" \
"v_cvt_pk_bf16_f32 v29, v30, v31\nv_mfma_f32_16x16x32_bf16 v[60:63], v[32:35], v[184:187], 0\nbuffer_store_dwordx4 v[44:47], v16, s[8:11], s17 offen offset:1024 sc1\nv_cvt_pk_bf16_f32 v30, v40, v41\n" \
"v_cvt_pk_bf16_f32 v31, v42, v43\nv_mfma_f32_16x16x32_bf16 v[20:23], v[68:71], v[188:191], v[20:23]\nv_mfma_f32_16x16x32_bf16 v[60:63], v[36:39], v[188:191], v[60:63]\nbuffer_load_dwordx4 v16, s[12:15], s43 offen offset:2048 sc1 lds\n" \
"v_mfma_f32_16x16x32_bf16 v[20:23], v[24:27], v[192:195], v[20:23]\nv_mfma_f32_16x16x32_bf16 v[60:63], v[52:55], v[192:195], v[60:63]\nv_mfma_f32_16x16x32_bf16 v[20:23], v[56:59], v[196:199], v[20:23]\nv_mfma_f32_16x16x32_bf16 v[60:63], v[76:79], v[196:199], v[60:63]\n" \
"v_mfma_f32_16x16x32_bf16 v[40:43], v[48:51], v[160:163], 0\nbuffer_store_dwordx4 v[64:67], v16, s[8:11], s17 offen offset:0 sc1\nv_cvt_pk_bf16_f32 v20, v20, v21\nv_cvt_pk_bf16_f32 v21, v22, v23\n" \
"v_mfma_f32_16x16x32_bf16 v[44:47], v[32:35], v[160:163], 0\nv_cvt_pk_bf16_f32 v22, v60, v61\nv_cvt_pk_bf16_f32 v23, v62, v63\nv_mfma_f32_16x16x32_bf16 v[40:43], v[68:71], v[164:167], v[40:43]\n" \
"v_mfma_f32_16x16x32_bf16 v[44:47], v[36:39], v[164:167], v[44:47]\nv_mfma_f32_16x16x32_bf16 v[40:43], v[24:27], v[168:171], v[40:43]\nds_read_b128 v[60:63], v17 offset:20480\nv_mfma_f32_16x16x32_bf16 v[44:47], v[52:55], v[168:171], v[44:47]\n" \
"buffer_load_dwordx4 v16, s[12:15], s43 offen offset:3072 sc1 lds\nv_mfma_f32_16x16x32_bf16 v[40:43], v[56:59], v[172:175], v[40:43]\nds_read_b128 v[64:67], v17 offset:21504\nv_mfma_f32_16x16x32_bf16 v[44:47], v[76:79], v[172:175], v[44:47]\n" \
"v_mfma_f32_16x16x32_bf16 v[72:75], v[48:51], v[232:235], 0\nbuffer_store_dwordx4 v[28:31], v16, s[8:11], s18 offen offset:1024 sc1\nv_cvt_pk_bf16_f32 v40, v40, v41\nv_cvt_pk_bf16_f32 v41, v42, v43\n" \
"v_mfma_f32_16x16x32_bf16 v[28:31], v[32:35], v[232:235], 0\nv_cvt_pk_bf16_f32 v42, v44, v45\nv_cvt_pk_bf16_f32 v43, v46, v47\nv_mfma_f32_16x16x32_bf16 v[72:75], v[68:71], v[236:239], v[72:75]\n" \
"v_mfma_f32_16x16x32_bf16 v[28:31], v[36:39], v[236:239], v[28:31]\nv_mfma_f32_16x16x32_bf16 v[72:75], v[24:27], v[240:243], v[72:75]\nv_mfma_f32_16x16x32_bf16 v[28:31], v[52:55], v[240:243], v[28:31]\nv_mfma_f32_16x16x32_bf16 v[72:75], v[56:59], v[244:247], v[72:75]\n" \
"s_waitcnt lgkmcnt(0)\nv_mfma_f32_16x16x32_bf16 v[28:31], v[76:79], v[244:247], v[28:31]\nbuffer_store_dwordx4 v[20:23], v16, s[8:11], s18 offen offset:2048 sc1\nv_mfma_f32_16x16x32_bf16 v[44:47], v[48:51], v[60:63], 0\n" \
"v_cvt_pk_bf16_f32 v72, v72, v73\nv_cvt_pk_bf16_f32 v73, v74, v75\nv_mfma_f32_16x16x32_bf16 v[20:23], v[32:35], v[60:63], 0\nv_cvt_pk_bf16_f32 v74, v28, v29\n" \
"v_cvt_pk_bf16_f32 v75, v30, v31\nv_mfma_f32_16x16x32_bf16 v[44:47], v[68:71], v[64:67], v[44:47]\nv_mfma_f32_16x16x32_bf16 v[20:23], v[36:39], v[64:67], v[20:23]\nv_mfma_f32_16x16x32_bf16 v[44:47], v[24:27], v[224:227], v[44:47]\n" \
"ds_read_b128 v[60:63], v17 offset:22528\nv_mfma_f32_16x16x32_bf16 v[20:23], v[52:55], v[224:227], v[20:23]\nv_mfma_f32_16x16x32_bf16 v[44:47], v[56:59], v[228:231], v[44:47]\nbuffer_store_dwordx4 v[40:43], v16, s[8:11], s18 offen offset:0 sc1\n" \
"ds_read_b128 v[28:31], v17 offset:23552\nv_mfma_f32_16x16x32_bf16 v[20:23], v[76:79], v[228:231], v[20:23]\nv_mfma_f32_16x16x32_bf16 v[64:67], v[48:51], v[208:211], 0\nv_cvt_pk_bf16_f32 v44, v44, v45\n" \
"v_cvt_pk_bf16_f32 v45, v46, v47\nv_mfma_f32_16x16x32_bf16 v[40:43], v[32:35], v[208:211], 0\nv_cvt_pk_bf16_f32 v46, v20, v21\nv_cvt_pk_bf16_f32 v47, v22, v23\n" \
"v_mfma_f32_16x16x32_bf16 v[64:67], v[68:71], v[212:215], v[64:67]\nv_mfma_f32_16x16x32_bf16 v[40:43], v[36:39], v[212:215], v[40:43]\nds_read_b128 v[20:23], v16 offset:0\nv_mfma_f32_16x16x32_bf16 v[64:67], v[24:27], v[216:219], v[64:67]\n" \
"ds_read_b128 v[80:83], v16 offset:32768\nv_mfma_f32_16x16x32_bf16 v[40:43], v[52:55], v[216:219], v[40:43]\nv_mfma_f32_16x16x32_bf16 v[64:67], v[56:59], v[220:223], v[64:67]\ns_waitcnt lgkmcnt(2)\n" \
"buffer_store_dwordx4 v[72:75], v16, s[8:11], s19 offen offset:2048 sc1\nv_mfma_f32_16x16x32_bf16 v[40:43], v[76:79], v[220:223], v[40:43]\nds_read_b128 v[84:87], v16 offset:1024\nv_mfma_f32_16x16x32_bf16 v[72:75], v[48:51], v[60:63], 0\n" \
"ds_read_b128 v[88:91], v16 offset:33792\nv_cvt_pk_bf16_f32 v64, v64, v65\nv_mfma_f32_16x16x32_bf16 v[48:51], v[32:35], v[60:63], 0\nv_cvt_pk_bf16_f32 v65, v66, v67\n" \
"v_cvt_pk_bf16_f32 v66, v40, v41\nv_mfma_f32_16x16x32_bf16 v[72:75], v[68:71], v[28:31], v[72:75]\nv_cvt_pk_bf16_f32 v67, v42, v43\nv_mfma_f32_16x16x32_bf16 v[48:51], v[36:39], v[28:31], v[48:51]\n" \
"ds_read_b128 v[32:35], v16 offset:2048\nv_mfma_f32_16x16x32_bf16 v[72:75], v[24:27], v[248:251], v[72:75]\nds_read_b128 v[28:31], v16 offset:34816\nv_mfma_f32_16x16x32_bf16 v[48:51], v[52:55], v[248:251], v[48:51]\n" \
"buffer_store_dwordx4 v[44:47], v16, s[8:11], s19 offen offset:1024 sc1\nv_mfma_f32_16x16x32_bf16 v[72:75], v[56:59], v[252:255], v[72:75]\ns_waitcnt lgkmcnt(2)\nv_mfma_f32_16x16x32_bf16 v[48:51], v[76:79], v[252:255], v[48:51]\n" \
"ds_read_b128 v[36:39], v16 offset:3072\nv_mfma_f32_16x16x32_bf16 v[24:27], v[0:3], v[20:23], 0\nds_read_b128 v[40:43], v16 offset:35840\nv_cvt_pk_bf16_f32 v72, v72, v73\n" \
"v_mfma_f32_16x16x32_bf16 v[44:47], v[0:3], v[80:83], 0\nv_cvt_pk_bf16_f32 v73, v74, v75\nv_cvt_pk_bf16_f32 v74, v48, v49\nv_mfma_f32_16x16x32_bf16 v[52:55], v[8:11], v[20:23], 0\n" \
"v_cvt_pk_bf16_f32 v75, v50, v51\nv_mfma_f32_16x16x32_bf16 v[20:23], v[8:11], v[80:83], 0\nds_read_b128 v[48:51], v16 offset:4096\nv_mfma_f32_16x16x32_bf16 v[24:27], v[4:7], v[84:87], v[24:27]\n" \
"ds_read_b128 v[56:59], v16 offset:36864\nv_mfma_f32_16x16x32_bf16 v[44:47], v[4:7], v[88:91], v[44:47]\nbuffer_store_dwordx4 v[64:67], v16, s[8:11], s19 offen offset:0 sc1\nv_mfma_f32_16x16x32_bf16 v[52:55], v[12:15], v[84:87], v[52:55]\n" \
"s_waitcnt lgkmcnt(2)\nv_mfma_f32_16x16x32_bf16 v[20:23], v[12:15], v[88:91], v[20:23]\nds_read_b128 v[60:63], v16 offset:5120\nv_mfma_f32_16x16x32_bf16 v[24:27], a[0:3], v[32:35], v[24:27]\n" \
"ds_read_b128 v[64:67], v16 offset:37888\nv_mfma_f32_16x16x32_bf16 v[44:47], a[0:3], v[28:31], v[44:47]\nv_mfma_f32_16x16x32_bf16 v[52:55], a[104:107], v[32:35], v[52:55]\nv_mfma_f32_16x16x32_bf16 v[20:23], a[104:107], v[28:31], v[20:23]\n" \
"ds_read_b128 v[32:35], v16 offset:6144\nv_mfma_f32_16x16x32_bf16 v[24:27], a[4:7], v[36:39], v[24:27]\nbuffer_store_dwordx4 v[72:75], v16, s[8:11], s19 offen offset:3072 sc1\nds_read_b128 v[76:79], v16 offset:38912\n" \
"v_mfma_f32_16x16x32_bf16 v[44:47], a[4:7], v[40:43], v[44:47]\nds_read_b128 v[68:71], v16 offset:7168\nv_mfma_f32_16x16x32_bf16 v[52:55], a[108:111], v[36:39], v[52:55]\nds_read_b128 v[28:31], v16 offset:39936\n" \
"v_mfma_f32_16x16x32_bf16 v[20:23], a[108:111], v[40:43], v[20:23]\ns_branch 1b\n2:\ns_cmp_lt_i32 s20, 1\n" \
"s_cbranch_scc1 3b\ns_add_u32 s30, s30, 1\ns_cmp_lt_u32 s30, 0x40000\ns_cbranch_scc0 9f\n" \
"v_cmp_eq_u32 vcc, -1, v38\ns_cbranch_vccz 6f\nbuffer_load_dwordx4 v16, s[8:11], s43 offen offset:0 sc1 lds\n6:\n" \
"v_cmp_eq_u32 vcc, -1, v60\ns_cbranch_vccz 6f\nbuffer_load_dwordx4 v16, s[8:11], s43 offen offset:1024 sc1 lds\n6:\n" \
"v_cmp_eq_u32 vcc, -1, v80\ns_cbranch_vccz 6f\nbuffer_load_dwordx4 v16, s[8:11], s43 offen offset:2048 sc1 lds\n6:\n" \
"v_cmp_eq_u32 vcc, -1, v96\ns_cbranch_vccz 6f\nbuffer_load_dwordx4 v16, s[8:11], s43 offen offset:3072 sc1 lds\n6:\n" \
"s_waitcnt vmcnt(0)\nds_read_b128 v[38:41], v36 offset:0\nds_read_b128 v[60:63], v36 offset:1024\nds_read_b128 v[80:83], v36 offset:2048\n" \
"ds_read_b128 v[96:99], v36 offset:3072\ns_waitcnt lgkmcnt(0)\nv_max3_u32 v42, v38, v60, v80\nv_max_u32 v42, v42, v96\n" \
"v_cmp_eq_u32 vcc, -1, v42\ns_cbranch_vccnz 2b\ns_branch 3b\n4:\n" \
"s_cmp_ge_i32 s20, 64\ns_cbranch_scc1 10f\ns_cmp_ge_i32 s20, 62\ns_cbranch_scc1 5b\n" \
"v_bfe_u32 v32, v16, 4, 3\nv_lshlrev_b32 v32, 2, v32\ns_lshl_b32 s43, s20, 5\ns_add_u32 s43, s43, 64\n" \
"s_add_u32 s43, s43, s26\n7:\nv_cmp_eq_u32 vcc, -1, v33\ns_cbranch_vccz 8f\n" \
"s_add_u32 s30, s30, 1\ns_cmp_lt_u32 s30, 0x40000\ns_cbranch_scc0 9f\ns_sleep 8\n" \
"buffer_load_dword v33, v32, s[8:11], s43 offen sc1\ns_waitcnt vmcnt(0)\ns_branch 7b\n8:\n" \
"s_mov_b32 s14, s10\ns_branch 5b\n10:\ns_waitcnt vmcnt(0) lgkmcnt(0)\n" \
"s_cmp_lt_i32 s6, 0\ns_cbranch_scc1 9f\ns_mov_b64 s[0:1], s[4:5]\ns_mov_b32 s2, s6\n" \
"v_lshrrev_b32 v0, 4, v16\nv_add_u32 v0, s7, v0\ns_barrier\ns_getpc_b64 s[8:9]\n" \
"11:\ns_add_u32 s8, s8, _Z5mega64Args-11b\ns_addc_u32 s9, s9, -1\ns_setpc_b64 s[8:9]\n" \
"9:\ns_waitcnt vmcnt(0)\ns_endpgm\n"
#define D_LOOP \
"s_branch 4f\n.p2align 6\n1:\ns_waitcnt lgkmcnt(0)\n" \
"v_mfma_f32_16x16x32_bf16 v[44:47], a[8:11], v[8:11], v[44:47]\nds_read_b128 v[16:19], v4 offset:3072\ns_mov_b32 m0, s24\nv_mfma_f32_16x16x32_bf16 v[64:67], a[8:11], v[12:15], v[64:67]\n" \
"ds_read_b128 v[32:35], v4 offset:35840\nbuffer_load_dword v1, s[16:19], 0 offen offset:0 sc1 lds\nv_mfma_f32_16x16x32_bf16 v[48:51], a[104:107], v[8:11], v[48:51]\ns_add_u32 m0, s23, 0\n" \
"s_and_b32 s98, s20, 1\ns_lshl_b32 s98, s98, 3\nv_mfma_f32_16x16x32_bf16 v[20:23], a[104:107], v[12:15], v[20:23]\nv_mfma_f32_16x16x32_bf16 v[56:59], a[200:203], v[8:11], v[56:59]\n" \
"v_mfma_f32_16x16x32_bf16 v[24:27], a[200:203], v[12:15], v[24:27]\nv_mfma_f32_16x16x32_bf16 v[60:63], v[152:155], v[8:11], v[60:63]\ns_waitcnt lgkmcnt(1)\nv_mfma_f32_16x16x32_bf16 v[28:31], v[152:155], v[12:15], v[28:31]\n" \
"s_waitcnt lgkmcnt(0)\nds_read_b128 v[8:11], v4 offset:4096\nv_mfma_f32_16x16x32_bf16 v[44:47], a[12:15], v[16:19], v[44:47]\nds_read_b128 v[12:15], v4 offset:36864\n" \
"v_mfma_f32_16x16x32_bf16 v[64:67], a[12:15], v[32:35], v[64:67]\nv_mfma_f32_16x16x32_bf16 v[48:51], a[108:111], v[16:19], v[48:51]\nv_mfma_f32_16x16x32_bf16 v[20:23], a[108:111], v[32:35], v[20:23]\nv_mfma_f32_16x16x32_bf16 v[56:59], a[204:207], v[16:19], v[56:59]\n" \
"v_mfma_f32_16x16x32_bf16 v[24:27], a[204:207], v[32:35], v[24:27]\nv_mfma_f32_16x16x32_bf16 v[60:63], v[156:159], v[16:19], v[60:63]\ns_waitcnt lgkmcnt(0)\nv_mfma_f32_16x16x32_bf16 v[28:31], v[156:159], v[32:35], v[28:31]\n" \
"ds_read_b128 v[16:19], v4 offset:5120\nv_mfma_f32_16x16x32_bf16 v[44:47], a[16:19], v[8:11], v[44:47]\nds_read_b128 v[32:35], v4 offset:37888\nv_mfma_f32_16x16x32_bf16 v[64:67], a[16:19], v[12:15], v[64:67]\n" \
"v_mfma_f32_16x16x32_bf16 v[48:51], a[112:115], v[8:11], v[48:51]\nv_mfma_f32_16x16x32_bf16 v[20:23], a[112:115], v[12:15], v[20:23]\nv_mfma_f32_16x16x32_bf16 v[56:59], a[208:211], v[8:11], v[56:59]\nv_mfma_f32_16x16x32_bf16 v[24:27], a[208:211], v[12:15], v[24:27]\n" \
"v_mfma_f32_16x16x32_bf16 v[60:63], v[160:163], v[8:11], v[60:63]\ns_waitcnt lgkmcnt(0)\nv_mfma_f32_16x16x32_bf16 v[28:31], v[160:163], v[12:15], v[28:31]\nds_read_b128 v[8:11], v4 offset:6144\n" \
"s_add_u32 s99, s20, 1\nv_mfma_f32_16x16x32_bf16 v[44:47], a[20:23], v[16:19], v[44:47]\nds_read_b128 v[12:15], v4 offset:38912\ns_lshr_b32 s97, s99, 1\n" \
"v_mfma_f32_16x16x32_bf16 v[64:67], a[20:23], v[32:35], v[64:67]\ns_and_b32 s99, s99, 1\ns_lshl_b32 s99, s99, 3\nv_readlane_b32 s97, v3, s97\n" \
"v_mfma_f32_16x16x32_bf16 v[48:51], a[116:119], v[16:19], v[48:51]\nv_mfma_f32_16x16x32_bf16 v[20:23], a[116:119], v[32:35], v[20:23]\ns_lshr_b32 s97, s97, s99\ns_and_b32 s97, s97, 0x7f\n" \
"s_mul_i32 s97, s97, 0x38000\nv_mfma_f32_16x16x32_bf16 v[56:59], a[212:215], v[16:19], v[56:59]\ns_add_u32 s97, s97, s21\nv_mfma_f32_16x16x32_bf16 v[24:27], a[212:215], v[32:35], v[24:27]\n" \
"v_mfma_f32_16x16x32_bf16 v[60:63], v[164:167], v[16:19], v[60:63]\ns_waitcnt lgkmcnt(0)\nv_mfma_f32_16x16x32_bf16 v[28:31], v[164:167], v[32:35], v[28:31]\nds_read_b128 v[16:19], v4 offset:7168\n" \
"v_mfma_f32_16x16x32_bf16 v[44:47], a[24:27], v[8:11], v[44:47]\nds_read_b128 v[32:35], v4 offset:39936\nv_mfma_f32_16x16x32_bf16 v[64:67], a[24:27], v[12:15], v[64:67]\nv_mfma_f32_16x16x32_bf16 v[48:51], a[120:123], v[8:11], v[48:51]\n" \
"v_mfma_f32_16x16x32_bf16 v[20:23], a[120:123], v[12:15], v[20:23]\nv_mfma_f32_16x16x32_bf16 v[56:59], a[216:219], v[8:11], v[56:59]\nv_mfma_f32_16x16x32_bf16 v[24:27], a[216:219], v[12:15], v[24:27]\nv_mfma_f32_16x16x32_bf16 v[60:63], v[168:171], v[8:11], v[60:63]\n" \
"s_waitcnt lgkmcnt(0)\nv_mfma_f32_16x16x32_bf16 v[28:31], v[168:171], v[12:15], v[28:31]\nds_read_b128 v[8:11], v4 offset:8192\nv_mov_b32 v111, s20\n" \
"v_mfma_f32_16x16x32_bf16 v[44:47], a[28:31], v[16:19], v[44:47]\nds_read_b128 v[12:15], v4 offset:40960\ns_add_u32 s99, s20, 2\nv_mfma_f32_16x16x32_bf16 v[64:67], a[28:31], v[32:35], v[64:67]\n" \
"s_cmp_le_u32 s99, s28\ns_cselect_b32 s99, s99, 0x1000\nv_mfma_f32_16x16x32_bf16 v[48:51], a[124:127], v[16:19], v[48:51]\nv_mfma_f32_16x16x32_bf16 v[20:23], a[124:127], v[32:35], v[20:23]\n" \
"v_mfma_f32_16x16x32_bf16 v[56:59], a[220:223], v[16:19], v[56:59]\ns_waitcnt vmcnt(0)\ns_barrier\nv_mfma_f32_16x16x32_bf16 v[24:27], a[220:223], v[32:35], v[24:27]\n" \
"buffer_store_dword v111, v2, s[8:11], s25 offen offset:0 sc1\nv_mfma_f32_16x16x32_bf16 v[60:63], v[172:175], v[16:19], v[60:63]\nds_read_b32 v110, v5 offset:0\ns_mov_b32 s25, 0\n" \
"v_mfma_f32_16x16x32_bf16 v[28:31], v[172:175], v[32:35], v[28:31]\ns_waitcnt lgkmcnt(1)\nds_read_b128 v[16:19], v4 offset:9216\nv_mfma_f32_16x16x32_bf16 v[44:47], a[32:35], v[8:11], v[44:47]\n" \
"ds_read_b128 v[32:35], v4 offset:41984\nv_mfma_f32_16x16x32_bf16 v[64:67], a[32:35], v[12:15], v[64:67]\nv_mfma_f32_16x16x32_bf16 v[48:51], a[128:131], v[8:11], v[48:51]\ns_waitcnt lgkmcnt(2)\n" \
"v_cmp_gt_i32 s[94:95], s99, v110\nv_mfma_f32_16x16x32_bf16 v[20:23], a[128:131], v[12:15], v[20:23]\nv_mfma_f32_16x16x32_bf16 v[56:59], a[224:227], v[8:11], v[56:59]\ns_cmp_eq_u64 s[94:95], 0\n" \
"s_cselect_b32 s14, s10, 0\nv_mfma_f32_16x16x32_bf16 v[24:27], a[224:227], v[12:15], v[24:27]\nv_mfma_f32_16x16x32_bf16 v[60:63], v[176:179], v[8:11], v[60:63]\ns_waitcnt lgkmcnt(0)\n" \
"v_mfma_f32_16x16x32_bf16 v[28:31], v[176:179], v[12:15], v[28:31]\nds_read_b128 v[8:11], v4 offset:10240\nv_mfma_f32_16x16x32_bf16 v[44:47], a[36:39], v[16:19], v[44:47]\nds_read_b128 v[12:15], v4 offset:43008\n" \
"v_mfma_f32_16x16x32_bf16 v[64:67], a[36:39], v[32:35], v[64:67]\nv_mfma_f32_16x16x32_bf16 v[48:51], a[132:135], v[16:19], v[48:51]\nv_mfma_f32_16x16x32_bf16 v[20:23], a[132:135], v[32:35], v[20:23]\nbuffer_load_dwordx4 v0, s[12:15], s97 offen offset:0 sc1 lds\n" \
"v_mfma_f32_16x16x32_bf16 v[56:59], a[228:231], v[16:19], v[56:59]\nv_mfma_f32_16x16x32_bf16 v[24:27], a[228:231], v[32:35], v[24:27]\nv_mfma_f32_16x16x32_bf16 v[60:63], v[180:183], v[16:19], v[60:63]\ns_waitcnt lgkmcnt(0)\n" \
"v_mfma_f32_16x16x32_bf16 v[28:31], v[180:183], v[32:35], v[28:31]\nbuffer_load_dwordx4 v0, s[12:15], s97 offen offset:1024 sc1 lds\nv_mfma_f32_16x16x32_bf16 v[44:47], a[40:43], v[8:11], v[44:47]\nds_read_b128 v[16:19], v4 offset:11264\n" \
"v_mfma_f32_16x16x32_bf16 v[64:67], a[40:43], v[12:15], v[64:67]\nds_read_b128 v[32:35], v4 offset:44032\nv_mfma_f32_16x16x32_bf16 v[48:51], a[136:139], v[8:11], v[48:51]\nv_mfma_f32_16x16x32_bf16 v[20:23], a[136:139], v[12:15], v[20:23]\n" \
"buffer_load_dwordx4 v0, s[12:15], s97 offen offset:2048 sc1 lds\nv_mfma_f32_16x16x32_bf16 v[56:59], a[232:235], v[8:11], v[56:59]\nv_mfma_f32_16x16x32_bf16 v[24:27], a[232:235], v[12:15], v[24:27]\nv_mfma_f32_16x16x32_bf16 v[60:63], v[184:187], v[8:11], v[60:63]\n" \
"s_waitcnt lgkmcnt(1)\nv_mfma_f32_16x16x32_bf16 v[28:31], v[184:187], v[12:15], v[28:31]\ns_waitcnt lgkmcnt(0)\nbuffer_load_dwordx4 v0, s[12:15], s97 offen offset:3072 sc1 lds\n" \
"v_mfma_f32_16x16x32_bf16 v[44:47], a[44:47], v[16:19], v[44:47]\nds_read_b128 v[8:11], v4 offset:12288\ns_add_u32 m0, s23, 0x1000\nv_mfma_f32_16x16x32_bf16 v[64:67], a[44:47], v[32:35], v[64:67]\n" \
"ds_read_b128 v[12:15], v4 offset:45056\ns_add_u32 s97, s97, 0x1000\nv_mfma_f32_16x16x32_bf16 v[48:51], a[140:143], v[16:19], v[48:51]\nv_mfma_f32_16x16x32_bf16 v[20:23], a[140:143], v[32:35], v[20:23]\n" \
"buffer_load_dwordx4 v0, s[12:15], s97 offen offset:0 sc1 lds\nv_mfma_f32_16x16x32_bf16 v[56:59], a[236:239], v[16:19], v[56:59]\nv_mfma_f32_16x16x32_bf16 v[24:27], a[236:239], v[32:35], v[24:27]\nv_mfma_f32_16x16x32_bf16 v[60:63], v[188:191], v[16:19], v[60:63]\n" \
"s_waitcnt lgkmcnt(1)\nv_mfma_f32_16x16x32_bf16 v[28:31], v[188:191], v[32:35], v[28:31]\ns_waitcnt lgkmcnt(0)\nbuffer_load_dwordx4 v0, s[12:15], s97 offen offset:1024 sc1 lds\n" \
"v_mfma_f32_16x16x32_bf16 v[44:47], a[48:51], v[8:11], v[44:47]\nds_read_b128 v[16:19], v4 offset:13312\nv_mfma_f32_16x16x32_bf16 v[64:67], a[48:51], v[12:15], v[64:67]\nds_read_b128 v[32:35], v4 offset:46080\n" \
"v_mfma_f32_16x16x32_bf16 v[48:51], a[144:147], v[8:11], v[48:51]\nv_mfma_f32_16x16x32_bf16 v[20:23], a[144:147], v[12:15], v[20:23]\nbuffer_load_dwordx4 v0, s[12:15], s97 offen offset:2048 sc1 lds\nv_mfma_f32_16x16x32_bf16 v[56:59], a[240:243], v[8:11], v[56:59]\n" \
"v_mfma_f32_16x16x32_bf16 v[24:27], a[240:243], v[12:15], v[24:27]\nv_mfma_f32_16x16x32_bf16 v[60:63], v[192:195], v[8:11], v[60:63]\ns_waitcnt lgkmcnt(1)\nv_mfma_f32_16x16x32_bf16 v[28:31], v[192:195], v[12:15], v[28:31]\n" \
"s_waitcnt lgkmcnt(0)\nbuffer_load_dwordx4 v0, s[12:15], s97 offen offset:3072 sc1 lds\nv_mfma_f32_16x16x32_bf16 v[44:47], a[52:55], v[16:19], v[44:47]\nds_read_b128 v[8:11], v4 offset:14336\n" \
"s_add_u32 m0, s23, 0x2000\nv_mfma_f32_16x16x32_bf16 v[64:67], a[52:55], v[32:35], v[64:67]\nds_read_b128 v[12:15], v4 offset:47104\ns_add_u32 s97, s97, 0x1000\n" \
"v_mfma_f32_16x16x32_bf16 v[48:51], a[148:151], v[16:19], v[48:51]\ns_xor_b32 s23, s23, 0x10000\nv_mfma_f32_16x16x32_bf16 v[20:23], a[148:151], v[32:35], v[20:23]\nbuffer_load_dwordx4 v0, s[12:15], s97 offen offset:0 sc1 lds\n" \
"v_mfma_f32_16x16x32_bf16 v[56:59], a[244:247], v[16:19], v[56:59]\nv_mfma_f32_16x16x32_bf16 v[24:27], a[244:247], v[32:35], v[24:27]\nv_mfma_f32_16x16x32_bf16 v[60:63], v[196:199], v[16:19], v[60:63]\ns_waitcnt lgkmcnt(1)\n" \
"v_mfma_f32_16x16x32_bf16 v[28:31], v[196:199], v[32:35], v[28:31]\ns_waitcnt lgkmcnt(0)\nbuffer_load_dwordx4 v0, s[12:15], s97 offen offset:1024 sc1 lds\nv_mfma_f32_16x16x32_bf16 v[44:47], a[56:59], v[8:11], v[44:47]\n" \
"ds_read_b128 v[16:19], v4 offset:15360\nv_mfma_f32_16x16x32_bf16 v[64:67], a[56:59], v[12:15], v[64:67]\nds_read_b128 v[32:35], v4 offset:48128\nv_mfma_f32_16x16x32_bf16 v[48:51], a[152:155], v[8:11], v[48:51]\n" \
"v_mfma_f32_16x16x32_bf16 v[20:23], a[152:155], v[12:15], v[20:23]\nbuffer_load_dwordx4 v0, s[12:15], s97 offen offset:2048 sc1 lds\nv_mfma_f32_16x16x32_bf16 v[56:59], a[248:251], v[8:11], v[56:59]\nv_mfma_f32_16x16x32_bf16 v[24:27], a[248:251], v[12:15], v[24:27]\n" \
"v_mfma_f32_16x16x32_bf16 v[60:63], v[200:203], v[8:11], v[60:63]\ns_waitcnt lgkmcnt(1)\nv_mfma_f32_16x16x32_bf16 v[28:31], v[200:203], v[12:15], v[28:31]\ns_waitcnt lgkmcnt(0)\n" \
"buffer_load_dwordx4 v0, s[12:15], s97 offen offset:3072 sc1 lds\nv_mfma_f32_16x16x32_bf16 v[44:47], a[60:63], v[16:19], v[44:47]\nds_read_b128 v[8:11], v4 offset:16384\nv_mfma_f32_16x16x32_bf16 v[64:67], a[60:63], v[32:35], v[64:67]\n" \
"ds_read_b128 v[12:15], v4 offset:49152\nv_mfma_f32_16x16x32_bf16 v[48:51], a[156:159], v[16:19], v[48:51]\nv_mfma_f32_16x16x32_bf16 v[20:23], a[156:159], v[32:35], v[20:23]\nv_mfma_f32_16x16x32_bf16 v[56:59], a[252:255], v[16:19], v[56:59]\n" \
"v_mfma_f32_16x16x32_bf16 v[24:27], a[252:255], v[32:35], v[24:27]\nv_mfma_f32_16x16x32_bf16 v[60:63], v[204:207], v[16:19], v[60:63]\ns_waitcnt lgkmcnt(1)\nv_mfma_f32_16x16x32_bf16 v[28:31], v[204:207], v[32:35], v[28:31]\n" \
"s_waitcnt lgkmcnt(0)\nds_read_b128 v[16:19], v4 offset:17408\nv_mfma_f32_16x16x32_bf16 v[44:47], a[64:67], v[8:11], v[44:47]\nds_read_b128 v[32:35], v4 offset:50176\n" \
"v_mfma_f32_16x16x32_bf16 v[64:67], a[64:67], v[12:15], v[64:67]\nv_mfma_f32_16x16x32_bf16 v[48:51], a[160:163], v[8:11], v[48:51]\nv_mfma_f32_16x16x32_bf16 v[20:23], a[160:163], v[12:15], v[20:23]\nv_mfma_f32_16x16x32_bf16 v[56:59], v[112:115], v[8:11], v[56:59]\n" \
"v_mfma_f32_16x16x32_bf16 v[24:27], v[112:115], v[12:15], v[24:27]\nv_mfma_f32_16x16x32_bf16 v[60:63], v[208:211], v[8:11], v[60:63]\ns_waitcnt lgkmcnt(0)\nv_mfma_f32_16x16x32_bf16 v[28:31], v[208:211], v[12:15], v[28:31]\n" \
"ds_read_b128 v[8:11], v4 offset:18432\nv_mfma_f32_16x16x32_bf16 v[44:47], a[68:71], v[16:19], v[44:47]\nds_read_b128 v[12:15], v4 offset:51200\nv_mfma_f32_16x16x32_bf16 v[64:67], a[68:71], v[32:35], v[64:67]\n" \
"v_mfma_f32_16x16x32_bf16 v[48:51], a[164:167], v[16:19], v[48:51]\nv_mfma_f32_16x16x32_bf16 v[20:23], a[164:167], v[32:35], v[20:23]\ns_lshr_b32 s99, s20, 1\ns_add_u32 s20, s20, 1\n" \
"v_readlane_b32 s99, v3, s99\nv_mfma_f32_16x16x32_bf16 v[56:59], v[116:119], v[16:19], v[56:59]\nv_mfma_f32_16x16x32_bf16 v[24:27], v[116:119], v[32:35], v[24:27]\ns_lshr_b32 s99, s99, s98\n" \
"s_and_b32 s99, s99, 0x7f\ns_mul_i32 s99, s99, 0x50000\nv_mfma_f32_16x16x32_bf16 v[60:63], v[212:215], v[16:19], v[60:63]\ns_waitcnt lgkmcnt(0)\n" \
"s_add_u32 s99, s99, s22\ns_add_u32 s98, s99, 0x8000\nv_mfma_f32_16x16x32_bf16 v[28:31], v[212:215], v[32:35], v[28:31]\nds_read_b128 v[16:19], v4 offset:19456\n" \
"v_mfma_f32_16x16x32_bf16 v[44:47], a[72:75], v[8:11], v[44:47]\nds_read_b128 v[32:35], v4 offset:52224\nv_mfma_f32_16x16x32_bf16 v[64:67], a[72:75], v[12:15], v[64:67]\nv_mfma_f32_16x16x32_bf16 v[48:51], a[168:171], v[8:11], v[48:51]\n" \
"v_mfma_f32_16x16x32_bf16 v[20:23], a[168:171], v[12:15], v[20:23]\nv_mfma_f32_16x16x32_bf16 v[56:59], v[120:123], v[8:11], v[56:59]\nv_mfma_f32_16x16x32_bf16 v[24:27], v[120:123], v[12:15], v[24:27]\nv_mfma_f32_16x16x32_bf16 v[60:63], v[216:219], v[8:11], v[60:63]\n" \
"s_waitcnt lgkmcnt(0)\nv_mfma_f32_16x16x32_bf16 v[28:31], v[216:219], v[12:15], v[28:31]\nds_read_b128 v[8:11], v4 offset:20480\nv_mfma_f32_16x16x32_bf16 v[44:47], a[76:79], v[16:19], v[44:47]\n" \
"ds_read_b128 v[12:15], v4 offset:53248\nv_mfma_f32_16x16x32_bf16 v[64:67], a[76:79], v[32:35], v[64:67]\nv_mfma_f32_16x16x32_bf16 v[48:51], a[172:175], v[16:19], v[48:51]\nv_mfma_f32_16x16x32_bf16 v[20:23], a[172:175], v[32:35], v[20:23]\n" \
"v_mfma_f32_16x16x32_bf16 v[56:59], v[124:127], v[16:19], v[56:59]\nv_mfma_f32_16x16x32_bf16 v[24:27], v[124:127], v[32:35], v[24:27]\nv_mfma_f32_16x16x32_bf16 v[60:63], v[220:223], v[16:19], v[60:63]\ns_waitcnt lgkmcnt(0)\n" \
"v_mfma_f32_16x16x32_bf16 v[28:31], v[220:223], v[32:35], v[28:31]\nds_read_b128 v[36:39], v4 offset:21504\nv_mfma_f32_16x16x32_bf16 v[44:47], a[80:83], v[8:11], v[44:47]\nds_read_b128 v[32:35], v4 offset:54272\n" \
"v_mfma_f32_16x16x32_bf16 v[64:67], a[80:83], v[12:15], v[64:67]\nv_mfma_f32_16x16x32_bf16 v[48:51], a[176:179], v[8:11], v[48:51]\nv_mfma_f32_16x16x32_bf16 v[20:23], a[176:179], v[12:15], v[20:23]\nv_mfma_f32_16x16x32_bf16 v[56:59], v[128:131], v[8:11], v[56:59]\n" \
"v_mfma_f32_16x16x32_bf16 v[24:27], v[128:131], v[12:15], v[24:27]\nv_mfma_f32_16x16x32_bf16 v[60:63], v[224:227], v[8:11], v[60:63]\ns_waitcnt lgkmcnt(0)\nv_mfma_f32_16x16x32_bf16 v[28:31], v[224:227], v[12:15], v[28:31]\n" \
"ds_read_b128 v[40:43], v4 offset:22528\nv_mfma_f32_16x16x32_bf16 v[44:47], a[84:87], v[36:39], v[44:47]\nds_read_b128 v[16:19], v4 offset:55296\nv_xor_b32 v4, 0x10000, v4\n" \
"v_mfma_f32_16x16x32_bf16 v[64:67], a[84:87], v[32:35], v[64:67]\nv_mfma_f32_16x16x32_bf16 v[48:51], a[180:183], v[36:39], v[48:51]\nv_mfma_f32_16x16x32_bf16 v[20:23], a[180:183], v[32:35], v[20:23]\nv_mfma_f32_16x16x32_bf16 v[56:59], v[132:135], v[36:39], v[56:59]\n" \
"v_mfma_f32_16x16x32_bf16 v[24:27], v[132:135], v[32:35], v[24:27]\nv_mfma_f32_16x16x32_bf16 v[60:63], v[228:231], v[36:39], v[60:63]\ns_waitcnt lgkmcnt(0)\nv_mfma_f32_16x16x32_bf16 v[28:31], v[228:231], v[32:35], v[28:31]\n" \
"s_waitcnt vmcnt(0)\ns_barrier\nv_mfma_f32_16x16x32_bf16 v[44:47], a[88:91], v[40:43], v[44:47]\nds_read_b128 v[8:11], v4 offset:0\n" \
"v_mfma_f32_16x16x32_bf16 v[48:51], a[184:187], v[40:43], v[48:51]\nds_read_b128 v[12:15], v4 offset:32768\nv_mfma_f32_16x16x32_bf16 v[56:59], v[136:139], v[40:43], v[56:59]\nv_cvt_pk_bf16_f32 v36, v44, v45\n" \
"v_cvt_pk_bf16_f32 v37, v46, v47\nv_mfma_f32_16x16x32_bf16 v[60:63], v[232:235], v[40:43], v[60:63]\nv_cvt_pk_bf16_f32 v38, v48, v49\nv_cvt_pk_bf16_f32 v39, v50, v51\n" \
"v_mfma_f32_16x16x32_bf16 v[64:67], a[88:91], v[16:19], v[64:67]\nv_cvt_pk_bf16_f32 v32, v56, v57\nv_cvt_pk_bf16_f32 v33, v58, v59\nv_mfma_f32_16x16x32_bf16 v[20:23], a[184:187], v[16:19], v[20:23]\n" \
"buffer_store_dwordx4 v[36:39], v6, s[8:11], s99 offen offset:0 sc1\nv_cvt_pk_bf16_f32 v34, v60, v61\nv_cvt_pk_bf16_f32 v35, v62, v63\nv_mfma_f32_16x16x32_bf16 v[24:27], v[136:139], v[16:19], v[24:27]\n" \
"s_waitcnt lgkmcnt(1)\nv_cvt_pk_bf16_f32 v36, v64, v65\nv_cvt_pk_bf16_f32 v37, v66, v67\nv_mfma_f32_16x16x32_bf16 v[28:31], v[232:235], v[16:19], v[28:31]\n" \
"ds_read_b128 v[40:43], v4 offset:1024\nv_cvt_pk_bf16_f32 v38, v20, v21\nv_mfma_f32_16x16x32_bf16 v[44:47], a[0:3], v[8:11], 0\nds_read_b128 v[16:19], v4 offset:33792\n" \
"v_cvt_pk_bf16_f32 v39, v22, v23\nv_mfma_f32_16x16x32_bf16 v[48:51], a[96:99], v[8:11], 0\nbuffer_store_dwordx4 v[32:35], v6, s[8:11], s99 offen offset:512 sc1\nv_cvt_pk_bf16_f32 v52, v24, v25\n" \
"v_cvt_pk_bf16_f32 v53, v26, v27\nv_cvt_pk_bf16_f32 v54, v28, v29\nv_mfma_f32_16x16x32_bf16 v[56:59], a[192:195], v[8:11], 0\ns_waitcnt lgkmcnt(2)\n" \
"v_cvt_pk_bf16_f32 v55, v30, v31\nv_mfma_f32_16x16x32_bf16 v[60:63], v[144:147], v[8:11], 0\nv_mfma_f32_16x16x32_bf16 v[64:67], a[0:3], v[12:15], 0\nv_mfma_f32_16x16x32_bf16 v[20:23], a[96:99], v[12:15], 0\n" \
"buffer_store_dwordx4 v[36:39], v6, s[8:11], s98 offen offset:0 sc1\nv_mfma_f32_16x16x32_bf16 v[24:27], a[192:195], v[12:15], 0\ns_waitcnt lgkmcnt(0)\nv_mfma_f32_16x16x32_bf16 v[28:31], v[144:147], v[12:15], 0\n" \
"ds_read_b128 v[8:11], v4 offset:2048\nv_mfma_f32_16x16x32_bf16 v[44:47], a[4:7], v[40:43], v[44:47]\nds_read_b128 v[12:15], v4 offset:34816\nv_mfma_f32_16x16x32_bf16 v[64:67], a[4:7], v[16:19], v[64:67]\n" \
"buffer_store_dwordx4 v[52:55], v6, s[8:11], s98 offen offset:512 sc1\nv_mfma_f32_16x16x32_bf16 v[48:51], a[100:103], v[40:43], v[48:51]\nv_mfma_f32_16x16x32_bf16 v[20:23], a[100:103], v[16:19], v[20:23]\nv_mfma_f32_16x16x32_bf16 v[56:59], a[196:199], v[40:43], v[56:59]\n" \
"v_mfma_f32_16x16x32_bf16 v[24:27], a[196:199], v[16:19], v[24:27]\nv_mfma_f32_16x16x32_bf16 v[60:63], v[148:151], v[40:43], v[60:63]\ns_cmp_eq_u32 s14, 0\nv_mfma_f32_16x16x32_bf16 v[28:31], v[148:151], v[16:19], v[28:31]\n" \
"s_cbranch_scc0 1b\n4:\nbuffer_load_dword v68, v1, s[8:11], 0 offen sc1\nv_mov_b32 v69, s20\n" \
"s_add_u32 s40, s20, 1\ns_waitcnt vmcnt(0)\nv_cmp_gt_i32 vcc, s40, v68\ns_cbranch_vccz 8f\n" \
"buffer_store_dword v69, v2, s[8:11], 0 offen sc1\ns_mov_b32 s25, 0x40000000\n7:\ns_add_u32 s26, s26, 1\n" \
"s_cmp_lt_u32 s26, 0x40000\ns_cbranch_scc0 9f\ns_sleep 8\nbuffer_load_dword v68, v1, s[8:11], 0 offen sc1\n" \
"s_waitcnt vmcnt(0)\nv_cmp_gt_i32 vcc, s40, v68\ns_cbranch_vccnz 7b\n8:\n" \
"s_cmp_ge_u32 s20, s28\ns_cbranch_scc1 10f\ns_lshr_b32 s40, s20, 1\nv_readlane_b32 s40, v3, s40\n" \
"s_and_b32 s41, s20, 1\ns_lshl_b32 s41, s41, 3\ns_lshr_b32 s40, s40, s41\ns_and_b32 s40, s40, 0x7f\n" \
"s_mul_i32 s40, s40, 0x38000\ns_add_u32 s40, s40, s21\ns_xor_b32 s41, s23, 0x10000\ns_add_u32 m0, s41, 0\n" \
"s_add_u32 s40, s40, 0\nbuffer_load_dwordx4 v0, s[8:11], s40 offen offset:0 sc1 lds\nbuffer_load_dwordx4 v0, s[8:11], s40 offen offset:1024 sc1 lds\nbuffer_load_dwordx4 v0, s[8:11], s40 offen offset:2048 sc1 lds\n" \
"buffer_load_dwordx4 v0, s[8:11], s40 offen offset:3072 sc1 lds\ns_add_u32 m0, s41, 0x1000\ns_add_u32 s40, s40, 0x1000\nbuffer_load_dwordx4 v0, s[8:11], s40 offen offset:0 sc1 lds\n" \
"buffer_load_dwordx4 v0, s[8:11], s40 offen offset:1024 sc1 lds\nbuffer_load_dwordx4 v0, s[8:11], s40 offen offset:2048 sc1 lds\nbuffer_load_dwordx4 v0, s[8:11], s40 offen offset:3072 sc1 lds\ns_add_u32 m0, s41, 0x2000\n" \
"s_add_u32 s40, s40, 0x1000\nbuffer_load_dwordx4 v0, s[8:11], s40 offen offset:0 sc1 lds\nbuffer_load_dwordx4 v0, s[8:11], s40 offen offset:1024 sc1 lds\nbuffer_load_dwordx4 v0, s[8:11], s40 offen offset:2048 sc1 lds\n" \
"buffer_load_dwordx4 v0, s[8:11], s40 offen offset:3072 sc1 lds\nbuffer_store_dword v69, v2, s[8:11], s25 offen sc1\ns_mov_b32 s25, 0x40000000\ns_waitcnt vmcnt(1) lgkmcnt(0)\n" \
"s_barrier\nds_read_b128 v[68:71], v4 offset:0\nds_read_b128 v[72:75], v4 offset:32768\nds_read_b128 v[76:79], v4 offset:1024\n" \
"ds_read_b128 v[80:83], v4 offset:33792\nds_read_b128 v[8:11], v4 offset:2048\nds_read_b128 v[12:15], v4 offset:34816\ns_waitcnt lgkmcnt(0)\n" \
"v_mfma_f32_16x16x32_bf16 v[44:47], a[0:3], v[68:71], 0\nv_mfma_f32_16x16x32_bf16 v[48:51], a[96:99], v[68:71], 0\nv_mfma_f32_16x16x32_bf16 v[56:59], a[192:195], v[68:71], 0\nv_mfma_f32_16x16x32_bf16 v[60:63], v[144:147], v[68:71], 0\n" \
"v_mfma_f32_16x16x32_bf16 v[64:67], a[0:3], v[72:75], 0\nv_mfma_f32_16x16x32_bf16 v[20:23], a[96:99], v[72:75], 0\nv_mfma_f32_16x16x32_bf16 v[24:27], a[192:195], v[72:75], 0\nv_mfma_f32_16x16x32_bf16 v[28:31], v[144:147], v[72:75], 0\n" \
"v_mfma_f32_16x16x32_bf16 v[44:47], a[4:7], v[76:79], v[44:47]\nv_mfma_f32_16x16x32_bf16 v[48:51], a[100:103], v[76:79], v[48:51]\nv_mfma_f32_16x16x32_bf16 v[56:59], a[196:199], v[76:79], v[56:59]\nv_mfma_f32_16x16x32_bf16 v[60:63], v[148:151], v[76:79], v[60:63]\n" \
"v_mfma_f32_16x16x32_bf16 v[64:67], a[4:7], v[80:83], v[64:67]\nv_mfma_f32_16x16x32_bf16 v[20:23], a[100:103], v[80:83], v[20:23]\nv_mfma_f32_16x16x32_bf16 v[24:27], a[196:199], v[80:83], v[24:27]\nv_mfma_f32_16x16x32_bf16 v[28:31], v[148:151], v[80:83], v[28:31]\n" \
"s_branch 1b\n10:\ns_lshr_b32 s42, s20, 1\nv_readlane_b32 s42, v3, s42\n" \
"s_and_b32 s41, s20, 1\ns_lshl_b32 s41, s41, 3\ns_lshr_b32 s42, s42, s41\ns_and_b32 s42, s42, 0x7f\n" \
"s_mul_i32 s43, s42, 0x50000\ns_add_u32 s43, s43, s30\ns_mul_i32 s42, s42, 0x38000\ns_add_u32 s42, s42, s29\n" \
"s_and_b32 s41, s23, 0x10000\ns_xor_b32 s41, s41, 0x10000\ns_sub_u32 s40, s24, 0x27000\ns_mul_i32 s40, s40, 24\n" \
"s_add_u32 s41, s41, s40\nbuffer_store_dword v69, v2, s[8:11], s25 offen sc1\ns_mov_b32 s25, 0x40000000\ns_add_u32 s20, s20, 1\n" \
"v_add_u32 v32, s41, v0\ns_add_u32 s40, s42, 0x1000\ns_cmp_eq_u32 s24, 0x27300\ns_cselect_b32 s44, 0x400, 0\n" \
"s_sub_u32 s44, s40, s44\n11:\ns_add_u32 m0, s41, 0\ns_nop 0\n" \
"buffer_load_dwordx4 v0, s[8:11], s42 offen offset:0 sc1 lds\nbuffer_load_dwordx4 v0, s[8:11], s42 offen offset:1024 sc1 lds\nbuffer_load_dwordx4 v0, s[8:11], s42 offen offset:2048 sc1 lds\nbuffer_load_dwordx4 v0, s[8:11], s42 offen offset:3072 sc1 lds\n" \
"s_add_u32 m0, s41, 0x1000\ns_nop 0\nbuffer_load_dwordx4 v0, s[8:11], s40 offen offset:0 sc1 lds\nbuffer_load_dwordx4 v0, s[8:11], s44 offen offset:1024 sc1 lds\n" \
"s_waitcnt vmcnt(0)\nds_read_b128 v[8:11], v32 offset:0\nds_read_b128 v[12:15], v32 offset:1024\nds_read_b128 v[16:19], v32 offset:2048\n" \
"ds_read_b128 v[20:23], v32 offset:3072\nds_read_b128 v[24:27], v32 offset:4096\nds_read_b128 v[28:31], v32 offset:5120\ns_waitcnt lgkmcnt(0)\n" \
"v_max3_u32 v33, v8, v10, v12\nv_max3_u32 v33, v33, v14, v16\nv_max3_u32 v33, v33, v18, v20\nv_max3_u32 v33, v33, v22, v24\n" \
"v_max3_u32 v33, v33, v26, v28\nv_max_u32 v33, v33, v30\nv_cmp_eq_u32 vcc, -1, v33\ns_nop 4\n" \
"s_cbranch_vccz 16f\ns_add_u32 s26, s26, 1\ns_cmp_lt_u32 s26, 0x40000\ns_cbranch_scc0 9f\n" \
"s_branch 11b\n16:\ns_barrier\nv_mov_b32 v69, s20\n" \
"ds_read_b128 v[8:11], v4 offset:0\nds_read_b128 v[12:15], v4 offset:1024\nds_read_b128 v[16:19], v4 offset:2048\nds_read_b128 v[20:23], v4 offset:3072\n" \
"ds_read_b128 v[24:27], v4 offset:4096\ns_waitcnt lgkmcnt(4)\nv_mfma_f32_16x16x32_bf16 v[28:31], a[0:3], v[8:11], 0\nv_mfma_f32_16x16x32_bf16 v[32:35], a[96:99], v[8:11], 0\n" \
"v_mfma_f32_16x16x32_bf16 v[36:39], a[192:195], v[8:11], 0\nv_mfma_f32_16x16x32_bf16 v[40:43], v[144:147], v[8:11], 0\nds_read_b128 v[8:11], v4 offset:5120\ns_waitcnt lgkmcnt(4)\n" \
"v_mfma_f32_16x16x32_bf16 v[28:31], a[4:7], v[12:15], v[28:31]\nv_mfma_f32_16x16x32_bf16 v[32:35], a[100:103], v[12:15], v[32:35]\nv_mfma_f32_16x16x32_bf16 v[36:39], a[196:199], v[12:15], v[36:39]\nv_mfma_f32_16x16x32_bf16 v[40:43], v[148:151], v[12:15], v[40:43]\n" \
"ds_read_b128 v[12:15], v4 offset:6144\ns_waitcnt lgkmcnt(4)\nv_mfma_f32_16x16x32_bf16 v[28:31], a[8:11], v[16:19], v[28:31]\nv_mfma_f32_16x16x32_bf16 v[32:35], a[104:107], v[16:19], v[32:35]\n" \
"v_mfma_f32_16x16x32_bf16 v[36:39], a[200:203], v[16:19], v[36:39]\nv_mfma_f32_16x16x32_bf16 v[40:43], v[152:155], v[16:19], v[40:43]\nds_read_b128 v[16:19], v4 offset:7168\ns_waitcnt lgkmcnt(4)\n" \
"v_mfma_f32_16x16x32_bf16 v[28:31], a[12:15], v[20:23], v[28:31]\nv_mfma_f32_16x16x32_bf16 v[32:35], a[108:111], v[20:23], v[32:35]\nv_mfma_f32_16x16x32_bf16 v[36:39], a[204:207], v[20:23], v[36:39]\nv_mfma_f32_16x16x32_bf16 v[40:43], v[156:159], v[20:23], v[40:43]\n" \
"ds_read_b128 v[20:23], v4 offset:8192\ns_waitcnt lgkmcnt(4)\nv_mfma_f32_16x16x32_bf16 v[28:31], a[16:19], v[24:27], v[28:31]\nv_mfma_f32_16x16x32_bf16 v[32:35], a[112:115], v[24:27], v[32:35]\n" \
"v_mfma_f32_16x16x32_bf16 v[36:39], a[208:211], v[24:27], v[36:39]\nv_mfma_f32_16x16x32_bf16 v[40:43], v[160:163], v[24:27], v[40:43]\nds_read_b128 v[24:27], v4 offset:9216\ns_waitcnt lgkmcnt(4)\n" \
"v_mfma_f32_16x16x32_bf16 v[28:31], a[20:23], v[8:11], v[28:31]\nv_mfma_f32_16x16x32_bf16 v[32:35], a[116:119], v[8:11], v[32:35]\nv_mfma_f32_16x16x32_bf16 v[36:39], a[212:215], v[8:11], v[36:39]\nv_mfma_f32_16x16x32_bf16 v[40:43], v[164:167], v[8:11], v[40:43]\n" \
"ds_read_b128 v[8:11], v4 offset:10240\ns_waitcnt lgkmcnt(4)\nv_mfma_f32_16x16x32_bf16 v[28:31], a[24:27], v[12:15], v[28:31]\nv_mfma_f32_16x16x32_bf16 v[32:35], a[120:123], v[12:15], v[32:35]\n" \
"v_mfma_f32_16x16x32_bf16 v[36:39], a[216:219], v[12:15], v[36:39]\nv_mfma_f32_16x16x32_bf16 v[40:43], v[168:171], v[12:15], v[40:43]\nds_read_b128 v[12:15], v4 offset:11264\ns_waitcnt lgkmcnt(4)\n" \
"v_mfma_f32_16x16x32_bf16 v[28:31], a[28:31], v[16:19], v[28:31]\nv_mfma_f32_16x16x32_bf16 v[32:35], a[124:127], v[16:19], v[32:35]\nv_mfma_f32_16x16x32_bf16 v[36:39], a[220:223], v[16:19], v[36:39]\nv_mfma_f32_16x16x32_bf16 v[40:43], v[172:175], v[16:19], v[40:43]\n" \
"ds_read_b128 v[16:19], v4 offset:12288\ns_waitcnt lgkmcnt(4)\nv_mfma_f32_16x16x32_bf16 v[28:31], a[32:35], v[20:23], v[28:31]\nv_mfma_f32_16x16x32_bf16 v[32:35], a[128:131], v[20:23], v[32:35]\n" \
"v_mfma_f32_16x16x32_bf16 v[36:39], a[224:227], v[20:23], v[36:39]\nv_mfma_f32_16x16x32_bf16 v[40:43], v[176:179], v[20:23], v[40:43]\nds_read_b128 v[20:23], v4 offset:13312\ns_waitcnt lgkmcnt(4)\n" \
"v_mfma_f32_16x16x32_bf16 v[28:31], a[36:39], v[24:27], v[28:31]\nv_mfma_f32_16x16x32_bf16 v[32:35], a[132:135], v[24:27], v[32:35]\nv_mfma_f32_16x16x32_bf16 v[36:39], a[228:231], v[24:27], v[36:39]\nv_mfma_f32_16x16x32_bf16 v[40:43], v[180:183], v[24:27], v[40:43]\n" \
"ds_read_b128 v[24:27], v4 offset:14336\ns_waitcnt lgkmcnt(4)\nv_mfma_f32_16x16x32_bf16 v[28:31], a[40:43], v[8:11], v[28:31]\nv_mfma_f32_16x16x32_bf16 v[32:35], a[136:139], v[8:11], v[32:35]\n" \
"v_mfma_f32_16x16x32_bf16 v[36:39], a[232:235], v[8:11], v[36:39]\nv_mfma_f32_16x16x32_bf16 v[40:43], v[184:187], v[8:11], v[40:43]\nds_read_b128 v[8:11], v4 offset:15360\ns_waitcnt lgkmcnt(4)\n" \
"v_mfma_f32_16x16x32_bf16 v[28:31], a[44:47], v[12:15], v[28:31]\nv_mfma_f32_16x16x32_bf16 v[32:35], a[140:143], v[12:15], v[32:35]\nv_mfma_f32_16x16x32_bf16 v[36:39], a[236:239], v[12:15], v[36:39]\nv_mfma_f32_16x16x32_bf16 v[40:43], v[188:191], v[12:15], v[40:43]\n" \
"ds_read_b128 v[12:15], v4 offset:16384\ns_waitcnt lgkmcnt(4)\nv_mfma_f32_16x16x32_bf16 v[28:31], a[48:51], v[16:19], v[28:31]\nv_mfma_f32_16x16x32_bf16 v[32:35], a[144:147], v[16:19], v[32:35]\n" \
"v_mfma_f32_16x16x32_bf16 v[36:39], a[240:243], v[16:19], v[36:39]\nv_mfma_f32_16x16x32_bf16 v[40:43], v[192:195], v[16:19], v[40:43]\nds_read_b128 v[16:19], v4 offset:17408\ns_waitcnt lgkmcnt(4)\n" \
"v_mfma_f32_16x16x32_bf16 v[28:31], a[52:55], v[20:23], v[28:31]\nv_mfma_f32_16x16x32_bf16 v[32:35], a[148:151], v[20:23], v[32:35]\nv_mfma_f32_16x16x32_bf16 v[36:39], a[244:247], v[20:23], v[36:39]\nv_mfma_f32_16x16x32_bf16 v[40:43], v[196:199], v[20:23], v[40:43]\n" \
"ds_read_b128 v[20:23], v4 offset:18432\ns_waitcnt lgkmcnt(4)\nv_mfma_f32_16x16x32_bf16 v[28:31], a[56:59], v[24:27], v[28:31]\nv_mfma_f32_16x16x32_bf16 v[32:35], a[152:155], v[24:27], v[32:35]\n" \
"v_mfma_f32_16x16x32_bf16 v[36:39], a[248:251], v[24:27], v[36:39]\nv_mfma_f32_16x16x32_bf16 v[40:43], v[200:203], v[24:27], v[40:43]\nds_read_b128 v[24:27], v4 offset:19456\ns_waitcnt lgkmcnt(4)\n" \
"v_mfma_f32_16x16x32_bf16 v[28:31], a[60:63], v[8:11], v[28:31]\nv_mfma_f32_16x16x32_bf16 v[32:35], a[156:159], v[8:11], v[32:35]\nv_mfma_f32_16x16x32_bf16 v[36:39], a[252:255], v[8:11], v[36:39]\nv_mfma_f32_16x16x32_bf16 v[40:43], v[204:207], v[8:11], v[40:43]\n" \
"ds_read_b128 v[8:11], v4 offset:20480\ns_waitcnt lgkmcnt(4)\nv_mfma_f32_16x16x32_bf16 v[28:31], a[64:67], v[12:15], v[28:31]\nv_mfma_f32_16x16x32_bf16 v[32:35], a[160:163], v[12:15], v[32:35]\n" \
"v_mfma_f32_16x16x32_bf16 v[36:39], v[112:115], v[12:15], v[36:39]\nv_mfma_f32_16x16x32_bf16 v[40:43], v[208:211], v[12:15], v[40:43]\nds_read_b128 v[12:15], v4 offset:21504\ns_waitcnt lgkmcnt(4)\n" \
"v_mfma_f32_16x16x32_bf16 v[28:31], a[68:71], v[16:19], v[28:31]\nv_mfma_f32_16x16x32_bf16 v[32:35], a[164:167], v[16:19], v[32:35]\nv_mfma_f32_16x16x32_bf16 v[36:39], v[116:119], v[16:19], v[36:39]\nv_mfma_f32_16x16x32_bf16 v[40:43], v[212:215], v[16:19], v[40:43]\n" \
"ds_read_b128 v[16:19], v4 offset:22528\ns_waitcnt lgkmcnt(4)\nv_mfma_f32_16x16x32_bf16 v[28:31], a[72:75], v[20:23], v[28:31]\nv_mfma_f32_16x16x32_bf16 v[32:35], a[168:171], v[20:23], v[32:35]\n" \
"v_mfma_f32_16x16x32_bf16 v[36:39], v[120:123], v[20:23], v[36:39]\nv_mfma_f32_16x16x32_bf16 v[40:43], v[216:219], v[20:23], v[40:43]\ns_waitcnt lgkmcnt(3)\nv_mfma_f32_16x16x32_bf16 v[28:31], a[76:79], v[24:27], v[28:31]\n" \
"v_mfma_f32_16x16x32_bf16 v[32:35], a[172:175], v[24:27], v[32:35]\nv_mfma_f32_16x16x32_bf16 v[36:39], v[124:127], v[24:27], v[36:39]\nv_mfma_f32_16x16x32_bf16 v[40:43], v[220:223], v[24:27], v[40:43]\ns_waitcnt lgkmcnt(2)\n" \
"v_mfma_f32_16x16x32_bf16 v[28:31], a[80:83], v[8:11], v[28:31]\nv_mfma_f32_16x16x32_bf16 v[32:35], a[176:179], v[8:11], v[32:35]\nv_mfma_f32_16x16x32_bf16 v[36:39], v[128:131], v[8:11], v[36:39]\nv_mfma_f32_16x16x32_bf16 v[40:43], v[224:227], v[8:11], v[40:43]\n" \
"s_waitcnt lgkmcnt(1)\nv_mfma_f32_16x16x32_bf16 v[28:31], a[84:87], v[12:15], v[28:31]\nv_mfma_f32_16x16x32_bf16 v[32:35], a[180:183], v[12:15], v[32:35]\nv_mfma_f32_16x16x32_bf16 v[36:39], v[132:135], v[12:15], v[36:39]\n" \
"v_mfma_f32_16x16x32_bf16 v[40:43], v[228:231], v[12:15], v[40:43]\ns_waitcnt lgkmcnt(0)\nv_mfma_f32_16x16x32_bf16 v[28:31], a[88:91], v[16:19], v[28:31]\nv_mfma_f32_16x16x32_bf16 v[32:35], a[184:187], v[16:19], v[32:35]\n" \
"v_mfma_f32_16x16x32_bf16 v[36:39], v[136:139], v[16:19], v[36:39]\nv_mfma_f32_16x16x32_bf16 v[40:43], v[232:235], v[16:19], v[40:43]\nbuffer_store_dword v69, v2, s[8:11], 0 offen sc1\ns_nop 7\n" \
"v_cvt_pk_bf16_f32 v8, v28, v29\nv_cvt_pk_bf16_f32 v9, v30, v31\nv_cvt_pk_bf16_f32 v10, v32, v33\nv_cvt_pk_bf16_f32 v11, v34, v35\n" \
"buffer_store_dwordx4 v[8:11], v6, s[8:11], s43 offen offset:0 sc1\nv_cvt_pk_bf16_f32 v12, v36, v37\nv_cvt_pk_bf16_f32 v13, v38, v39\nv_cvt_pk_bf16_f32 v14, v40, v41\n" \
"v_cvt_pk_bf16_f32 v15, v42, v43\nbuffer_store_dwordx4 v[12:15], v6, s[8:11], s43 offen offset:512 sc1\ns_waitcnt vmcnt(0)\nbuffer_store_dword v69, v2, s[8:11], 0 offen offset:16 sc1\n" \
"s_cmp_ge_u32 s20, s31\ns_cbranch_scc1 9f\ns_add_u32 s40, s20, 1\ns_barrier\n" \
"s_branch 7b\n9:\ns_waitcnt vmcnt(0)\ns_endpgm\n"
#define G_LOOP \
"s_branch 4f\n.p2align 6\n1:\ns_waitcnt lgkmcnt(2)\n" \
"v_mfma_f32_16x16x32_bf16 v[8:11], a[104:107], v[36:39], v[8:11]\ns_lshr_b32 s99, s20, 1\ns_and_b32 s98, s20, 1\nv_mov_b32 v110, s20\n" \
"v_mfma_f32_16x16x32_bf16 v[12:15], a[104:107], v[40:43], v[12:15]\ns_add_u32 s20, s20, 1\ns_add_u32 s97, s20, 1\ns_lshr_b32 s96, s97, 1\n" \
"v_mfma_f32_16x16x32_bf16 v[16:19], a[232:235], v[36:39], v[16:19]\nds_read_b128 v[52:55], v7 offset:28672\nv_readlane_b32 s96, v6, s96\nv_mfma_f32_16x16x32_bf16 v[20:23], a[232:235], v[40:43], v[20:23]\n" \
"ds_read_b128 v[56:59], v7 offset:61440\nv_xor_b32 v4, 0x400, v4\nv_mfma_f32_16x16x32_bf16 v[24:27], v[216:219], v[36:39], v[24:27]\ns_and_b32 s97, s97, 1\n" \
"s_lshl_b32 s97, s97, 3\ns_lshr_b32 s96, s96, s97\nv_mfma_f32_16x16x32_bf16 v[28:31], v[216:219], v[40:43], v[28:31]\ns_waitcnt lgkmcnt(2)\n" \
"s_and_b32 s96, s96, 0x7f\ns_lshl_b32 s97, s96, 13\nv_mfma_f32_16x16x32_bf16 v[8:11], a[108:111], v[44:47], v[8:11]\ns_lshl_b32 s98, s98, 3\n" \
"v_mov_b32 v32, v111\nv_mov_b32 v33, v111\nv_mfma_f32_16x16x32_bf16 v[12:15], a[108:111], v[48:51], v[12:15]\ns_lshl_b32 s96, s96, 16\n" \
"v_mul_f32 v252, 0xc0135761, v34\nv_mul_f32 v34, v34, v34\nv_mfma_f32_16x16x32_bf16 v[16:19], a[236:239], v[44:47], v[16:19]\nds_read_b128 v[36:39], v7 offset:29696\n" \
"v_readlane_b32 s99, v6, s99\nv_mfma_f32_16x16x32_bf16 v[20:23], a[236:239], v[48:51], v[20:23]\nds_read_b128 v[40:43], v7 offset:62464\nv_mul_f32 v34, v34, v252\n" \
"v_mfma_f32_16x16x32_bf16 v[24:27], v[220:223], v[44:47], v[24:27]\nds_read_b128 v[60:63], v7 offset:30720\nv_mul_f32 v254, v35, v35\nv_mfma_f32_16x16x32_bf16 v[28:31], v[220:223], v[48:51], v[28:31]\n" \
"s_waitcnt lgkmcnt(3)\nv_permlane16_swap_b32 v32, v33\nv_mfma_f32_16x16x32_bf16 v[8:11], a[112:115], v[52:55], v[8:11]\nds_read_b128 v[44:47], v7 offset:63488\n" \
"v_mul_f32 v253, 0x3d372713, v34\nv_mfma_f32_16x16x32_bf16 v[12:15], a[112:115], v[56:59], v[12:15]\ns_lshr_b32 s99, s99, s98\ns_and_b32 s99, s99, 0x7f\n" \
"s_mul_i32 s99, s99, 0x38000\nv_mfma_f32_16x16x32_bf16 v[16:19], a[240:243], v[52:55], v[16:19]\nds_read_b128 v[48:51], v7 offset:31744\ns_or_b32 s16, s99, s18\n" \
"v_mfma_f32_16x16x32_bf16 v[20:23], a[240:243], v[56:59], v[20:23]\ns_or_b32 s17, s99, s19\ns_add_u32 s96, s96, s21\nv_mfma_f32_16x16x32_bf16 v[24:27], v[224:227], v[52:55], v[24:27]\n" \
"ds_read_b128 v[64:67], v7 offset:64512\nv_xor_b32 v7, 0x10000, v7\nv_mfma_f32_16x16x32_bf16 v[28:31], v[224:227], v[56:59], v[28:31]\ns_waitcnt lgkmcnt(3)\n" \
"v_mfma_f32_16x16x32_bf16 v[8:11], a[116:119], v[36:39], v[8:11]\nv_mfma_f32_16x16x32_bf16 v[12:15], a[116:119], v[40:43], v[12:15]\nv_mfma_f32_16x16x32_bf16 v[16:19], a[244:247], v[36:39], v[16:19]\ns_waitcnt vmcnt(3) lgkmcnt(0)\n" \
"s_barrier\nv_mfma_f32_16x16x32_bf16 v[20:23], a[244:247], v[40:43], v[20:23]\nds_read_b128 v[52:55], v4 offset:0\nv_mfma_f32_16x16x32_bf16 v[24:27], v[228:231], v[36:39], v[24:27]\n" \
"ds_read_b128 v[56:59], v7 offset:0\nv_mfma_f32_16x16x32_bf16 v[28:31], v[228:231], v[40:43], v[28:31]\nds_read_b128 v[36:39], v7 offset:32768\nv_mfma_f32_16x16x32_bf16 v[8:11], a[120:123], v[60:63], v[8:11]\n" \
"ds_read_b128 v[40:43], v7 offset:1024\nv_mfma_f32_16x16x32_bf16 v[12:15], a[120:123], v[44:47], v[12:15]\ns_waitcnt lgkmcnt(3)\nv_add_f32 v52, v52, v53\n" \
"v_mfma_f32_16x16x32_bf16 v[8:11], a[124:127], v[48:51], v[8:11]\nbuffer_load_dwordx4 v[68:71], v1, s[8:11], s97 offen offset:1024 sc1\nv_mfma_f32_16x16x32_bf16 v[12:15], a[124:127], v[64:67], v[12:15]\nbuffer_load_dwordx4 v[72:75], v1, s[8:11], s97 offen offset:0 sc1\n" \
"v_mfma_f32_16x16x32_bf16 v[16:19], a[248:251], v[60:63], v[16:19]\nds_read_b128 v[76:79], v7 offset:33792\nv_add_f32 v54, v54, v55\nv_mfma_f32_16x16x32_bf16 v[20:23], a[248:251], v[44:47], v[20:23]\n" \
"v_cmp_u_f32 s[98:99], v52, v54\nv_fma_f32 v52, v32, v240, v8\nv_mfma_f32_16x16x32_bf16 v[16:19], a[252:255], v[48:51], v[16:19]\ns_waitcnt vmcnt(2)\n" \
"buffer_store_dword v110, v3, s[8:11], s23 offen offset:0 sc1\nv_mfma_f32_16x16x32_bf16 v[20:23], a[252:255], v[64:67], v[20:23]\ns_mov_b32 s23, 0\nv_fma_f32 v53, v32, v241, v9\n" \
"v_fma_f32 v54, v32, v242, v10\nv_mfma_f32_16x16x32_bf16 v[24:27], v[232:235], v[60:63], v[24:27]\ns_cmp_lt_u32 s20, s26\ns_cselect_b32 s98, s98, -1\n" \
"v_fma_f32 v55, v32, v243, v11\nv_mfma_f32_16x16x32_bf16 v[28:31], v[232:235], v[44:47], v[28:31]\nv_fma_f32 v60, v33, v240, v12\nv_fma_f32 v61, v33, v241, v13\n" \
"v_mfma_f32_16x16x32_bf16 v[24:27], v[236:239], v[48:51], v[24:27]\ns_waitcnt lgkmcnt(0)\nv_fma_f32 v62, v33, v242, v14\nv_fma_f32 v63, v33, v243, v15\n" \
"v_mfma_f32_16x16x32_bf16 v[28:31], v[236:239], v[64:67], v[28:31]\ns_cmp_lg_u64 s[98:99], 0\ns_cbranch_scc1 4f\n5:\n" \
"v_mfma_f32_16x16x32_bf16 v[8:11], a[0:3], v[56:59], 0\ns_xor_b32 m0, s22, 0\nv_fma_f32 v44, v32, v244, v16\nv_fma_f32 v45, v32, v245, v17\n" \
"v_mfma_f32_16x16x32_bf16 v[12:15], a[0:3], v[36:39], 0\nv_fma_f32 v46, v32, v246, v18\nv_fma_f32 v47, v32, v247, v19\nv_mfma_f32_16x16x32_bf16 v[8:11], a[4:7], v[40:43], v[8:11]\n" \
"v_fma_f32 v48, v33, v244, v20\nv_fma_f32 v49, v33, v245, v21\nv_mfma_f32_16x16x32_bf16 v[12:15], a[4:7], v[76:79], v[12:15]\nv_fma_f32 v50, v33, v246, v22\n" \
"v_fma_f32 v51, v33, v247, v23\nv_mfma_f32_16x16x32_bf16 v[16:19], a[128:131], v[56:59], 0\nds_read_b128 v[64:67], v7 offset:2048\nv_fma_f32 v80, v32, v248, v24\n" \
"v_mfma_f32_16x16x32_bf16 v[20:23], a[128:131], v[36:39], 0\nds_read_b128 v[84:87], v7 offset:34816\nv_fma_f32 v81, v32, v249, v25\nv_mfma_f32_16x16x32_bf16 v[16:19], a[132:135], v[40:43], v[16:19]\n" \
"v_fma_f32 v82, v32, v250, v26\nv_fma_f32 v83, v32, v251, v27\nv_mfma_f32_16x16x32_bf16 v[20:23], a[132:135], v[76:79], v[20:23]\nv_fma_f32 v88, v33, v248, v28\n" \
"v_fma_f32 v89, v33, v249, v29\nv_fma_f32 v90, v33, v250, v30\nv_fma_f32 v91, v33, v251, v31\nv_mfma_f32_16x16x32_bf16 v[24:27], v[112:115], v[56:59], 0\n" \
"ds_read_b128 v[92:95], v7 offset:3072\nv_xor_b32 v5, 0x400, v5\nv_mfma_f32_16x16x32_bf16 v[28:31], v[112:115], v[36:39], 0\nds_read_b128 v[56:59], v7 offset:35840\n" \
"v_mfma_f32_16x16x32_bf16 v[24:27], v[116:119], v[40:43], v[24:27]\ns_waitcnt lgkmcnt(3)\nv_permlane16_swap_b32 v52, v60\nv_mfma_f32_16x16x32_bf16 v[28:31], v[116:119], v[76:79], v[28:31]\n" \
"s_waitcnt lgkmcnt(2)\nv_permlane16_swap_b32 v53, v61\nv_mfma_f32_16x16x32_bf16 v[8:11], a[8:11], v[64:67], v[8:11]\nv_permlane16_swap_b32 v54, v62\n" \
"v_mfma_f32_16x16x32_bf16 v[12:15], a[8:11], v[84:87], v[12:15]\nv_permlane16_swap_b32 v55, v63\nv_mfma_f32_16x16x32_bf16 v[16:19], a[136:139], v[64:67], v[16:19]\nds_read_b128 v[36:39], v7 offset:4096\n" \
"v_mul_f32 v40, v52, v52\nv_mfma_f32_16x16x32_bf16 v[20:23], a[136:139], v[84:87], v[20:23]\nds_read_b128 v[76:79], v7 offset:36864\nv_mul_f32 v41, v53, v53\n" \
"v_mfma_f32_16x16x32_bf16 v[24:27], v[120:123], v[64:67], v[24:27]\ns_waitcnt lgkmcnt(2)\nv_mul_f32 v42, v54, v54\nv_mul_f32 v43, v55, v55\n" \
"v_mfma_f32_16x16x32_bf16 v[28:31], v[120:123], v[84:87], v[28:31]\nv_fma_f32 v40, v40, v253, v252\nv_fma_f32 v41, v41, v253, v252\nv_mfma_f32_16x16x32_bf16 v[8:11], a[12:15], v[92:95], v[8:11]\n" \
"v_fma_f32 v42, v42, v253, v252\nv_fma_f32 v43, v43, v253, v252\nv_mfma_f32_16x16x32_bf16 v[12:15], a[12:15], v[56:59], v[12:15]\nv_mul_f32 v40, v40, v52\n" \
"v_mul_f32 v41, v41, v53\nv_mfma_f32_16x16x32_bf16 v[16:19], a[140:143], v[92:95], v[16:19]\nds_read_b128 v[64:67], v7 offset:5120\nv_mul_f32 v42, v42, v54\n" \
"v_mfma_f32_16x16x32_bf16 v[20:23], a[140:143], v[56:59], v[20:23]\nds_read_b128 v[84:87], v7 offset:37888\nv_mul_f32 v43, v43, v55\nv_mfma_f32_16x16x32_bf16 v[24:27], v[124:127], v[92:95], v[24:27]\n" \
"s_waitcnt lgkmcnt(2)\nv_exp_f32 v40, v40\nv_mfma_f32_16x16x32_bf16 v[28:31], v[124:127], v[56:59], v[28:31]\nv_exp_f32 v41, v41\n" \
"v_mfma_f32_16x16x32_bf16 v[8:11], a[16:19], v[36:39], v[8:11]\nv_exp_f32 v42, v42\nv_mfma_f32_16x16x32_bf16 v[12:15], a[16:19], v[76:79], v[12:15]\nv_exp_f32 v43, v43\n" \
"v_mfma_f32_16x16x32_bf16 v[16:19], a[144:147], v[36:39], v[16:19]\nds_read_b128 v[56:59], v7 offset:6144\nv_fma_f32 v40, v40, v254, v254\nv_mfma_f32_16x16x32_bf16 v[20:23], a[144:147], v[76:79], v[20:23]\n" \
"ds_read_b128 v[92:95], v7 offset:38912\nv_fma_f32 v41, v41, v254, v254\nv_mfma_f32_16x16x32_bf16 v[24:27], v[128:131], v[36:39], v[24:27]\ns_waitcnt lgkmcnt(2)\n" \
"v_rcp_f32 v40, v40\nv_mfma_f32_16x16x32_bf16 v[28:31], v[128:131], v[76:79], v[28:31]\nv_rcp_f32 v41, v41\nv_mfma_f32_16x16x32_bf16 v[8:11], a[20:23], v[64:67], v[8:11]\n" \
"v_fma_f32 v42, v42, v254, v254\nv_fma_f32 v43, v43, v254, v254\nv_mfma_f32_16x16x32_bf16 v[12:15], a[20:23], v[84:87], v[12:15]\nv_rcp_f32 v42, v42\n" \
"v_mfma_f32_16x16x32_bf16 v[16:19], a[148:151], v[64:67], v[16:19]\nds_read_b128 v[36:39], v7 offset:7168\nv_mul_f32 v60, v52, v60\nv_mfma_f32_16x16x32_bf16 v[20:23], a[148:151], v[84:87], v[20:23]\n" \
"s_waitcnt vmcnt(1)\nv_max3_u32 v35, v72, v74, v68\nv_mul_f32 v61, v53, v61\nv_mfma_f32_16x16x32_bf16 v[24:27], v[132:135], v[64:67], v[24:27]\n" \
"ds_read_b128 v[76:79], v7 offset:39936\nv_max_u32 v35, v35, v70\nv_mfma_f32_16x16x32_bf16 v[28:31], v[132:135], v[84:87], v[28:31]\ns_waitcnt lgkmcnt(2)\n" \
"v_cmp_eq_u32 s[98:99], -1, v35\nv_mul_f32 v62, v54, v62\nv_mfma_f32_16x16x32_bf16 v[8:11], a[24:27], v[56:59], v[8:11]\nv_rcp_f32 v43, v43\n" \
"v_mfma_f32_16x16x32_bf16 v[12:15], a[24:27], v[92:95], v[12:15]\ns_cmp_eq_u64 s[98:99], 0\ns_cselect_b32 s14, s10, 0\nv_mul_f32 v63, v55, v63\n" \
"v_mfma_f32_16x16x32_bf16 v[16:19], a[152:155], v[56:59], v[16:19]\nbuffer_load_dwordx4 v0, s[12:15], s96 offen offset:0 sc1 lds\nv_mfma_f32_16x16x32_bf16 v[20:23], a[152:155], v[92:95], v[20:23]\nds_read_b128 v[52:55], v7 offset:8192\n" \
"v_mul_f32 v60, v60, v40\nv_mfma_f32_16x16x32_bf16 v[24:27], v[136:139], v[56:59], v[24:27]\nds_read_b128 v[64:67], v7 offset:40960\nv_mul_f32 v61, v61, v41\n" \
"v_mfma_f32_16x16x32_bf16 v[28:31], v[136:139], v[92:95], v[28:31]\ns_waitcnt lgkmcnt(2)\nv_mul_f32 v62, v62, v42\nv_mul_f32 v63, v63, v43\n" \
"v_mfma_f32_16x16x32_bf16 v[8:11], a[28:31], v[36:39], v[8:11]\nv_cvt_pk_bf16_f32 v60, v60, v61\nv_cvt_pk_bf16_f32 v61, v62, v63\nv_mfma_f32_16x16x32_bf16 v[12:15], a[28:31], v[76:79], v[12:15]\n" \
"buffer_load_dwordx4 v0, s[12:15], s96 offen offset:1024 sc1 lds\nv_mfma_f32_16x16x32_bf16 v[16:19], a[156:159], v[36:39], v[16:19]\nds_read_b128 v[40:43], v7 offset:9216\nv_mfma_f32_16x16x32_bf16 v[20:23], a[156:159], v[76:79], v[20:23]\n" \
"ds_read_b128 v[56:59], v7 offset:41984\nv_mfma_f32_16x16x32_bf16 v[24:27], v[140:143], v[36:39], v[24:27]\ns_waitcnt lgkmcnt(2)\nv_permlane16_swap_b32 v44, v48\n" \
"v_mfma_f32_16x16x32_bf16 v[28:31], v[140:143], v[76:79], v[28:31]\nbuffer_load_dwordx4 v0, s[12:15], s96 offen offset:2048 sc1 lds\nv_mfma_f32_16x16x32_bf16 v[8:11], a[32:35], v[52:55], v[8:11]\nv_permlane16_swap_b32 v45, v49\n" \
"v_mfma_f32_16x16x32_bf16 v[12:15], a[32:35], v[64:67], v[12:15]\nv_permlane16_swap_b32 v46, v50\nv_mfma_f32_16x16x32_bf16 v[16:19], a[160:163], v[52:55], v[16:19]\nds_read_b128 v[36:39], v7 offset:10240\n" \
"v_mul_f32 v76, v44, v44\nv_mfma_f32_16x16x32_bf16 v[20:23], a[160:163], v[64:67], v[20:23]\nds_read_b128 v[84:87], v7 offset:43008\nv_mul_f32 v77, v45, v45\n" \
"v_mfma_f32_16x16x32_bf16 v[24:27], v[144:147], v[52:55], v[24:27]\ns_waitcnt lgkmcnt(2)\nbuffer_load_dwordx4 v0, s[12:15], s96 offen offset:3072 sc1 lds\nv_mfma_f32_16x16x32_bf16 v[28:31], v[144:147], v[64:67], v[28:31]\n" \
"s_xor_b32 m0, s22, 0x1000\ns_xor_b32 s96, s96, 0x1000\nv_mul_f32 v78, v46, v46\nv_mfma_f32_16x16x32_bf16 v[8:11], a[36:39], v[40:43], v[8:11]\n" \
"v_permlane16_swap_b32 v47, v51\nv_mfma_f32_16x16x32_bf16 v[12:15], a[36:39], v[56:59], v[12:15]\nv_mul_f32 v79, v47, v47\nv_fma_f32 v76, v76, v253, v252\n" \
"v_mfma_f32_16x16x32_bf16 v[16:19], a[164:167], v[40:43], v[16:19]\nds_read_b128 v[52:55], v7 offset:11264\nv_fma_f32 v77, v77, v253, v252\nv_mfma_f32_16x16x32_bf16 v[20:23], a[164:167], v[56:59], v[20:23]\n" \
"buffer_load_dwordx4 v0, s[12:15], s96 offen offset:0 sc1 lds\nv_mfma_f32_16x16x32_bf16 v[24:27], v[148:151], v[40:43], v[24:27]\nds_read_b128 v[64:67], v7 offset:44032\nv_fma_f32 v78, v78, v253, v252\n" \
"v_mfma_f32_16x16x32_bf16 v[28:31], v[148:151], v[56:59], v[28:31]\ns_waitcnt lgkmcnt(2)\nv_fma_f32 v79, v79, v253, v252\nv_mul_f32 v76, v76, v44\n" \
"v_mfma_f32_16x16x32_bf16 v[8:11], a[40:43], v[36:39], v[8:11]\nv_exp_f32 v76, v76\nv_mfma_f32_16x16x32_bf16 v[12:15], a[40:43], v[84:87], v[12:15]\nv_mul_f32 v77, v77, v45\n" \
"v_mul_f32 v78, v78, v46\nv_mfma_f32_16x16x32_bf16 v[16:19], a[168:171], v[36:39], v[16:19]\nbuffer_load_dwordx4 v0, s[12:15], s96 offen offset:1024 sc1 lds\nv_mfma_f32_16x16x32_bf16 v[20:23], a[168:171], v[84:87], v[20:23]\n" \
"ds_read_b128 v[40:43], v7 offset:12288\nv_mul_f32 v79, v79, v47\nv_mfma_f32_16x16x32_bf16 v[24:27], v[152:155], v[36:39], v[24:27]\nds_read_b128 v[56:59], v7 offset:45056\n" \
"v_fma_f32 v76, v76, v254, v254\nv_mfma_f32_16x16x32_bf16 v[28:31], v[152:155], v[84:87], v[28:31]\ns_waitcnt lgkmcnt(2)\nv_exp_f32 v77, v77\n" \
"v_mfma_f32_16x16x32_bf16 v[8:11], a[44:47], v[52:55], v[8:11]\nv_exp_f32 v78, v78\nv_mfma_f32_16x16x32_bf16 v[12:15], a[44:47], v[64:67], v[12:15]\nbuffer_load_dwordx4 v0, s[12:15], s96 offen offset:2048 sc1 lds\n" \
"v_mfma_f32_16x16x32_bf16 v[16:19], a[172:175], v[52:55], v[16:19]\nds_read_b128 v[36:39], v7 offset:13312\nv_fma_f32 v77, v77, v254, v254\nv_mfma_f32_16x16x32_bf16 v[20:23], a[172:175], v[64:67], v[20:23]\n" \
"ds_read_b128 v[84:87], v7 offset:46080\nv_fma_f32 v78, v78, v254, v254\nv_mfma_f32_16x16x32_bf16 v[24:27], v[156:159], v[52:55], v[24:27]\ns_waitcnt lgkmcnt(2)\n" \
"v_exp_f32 v79, v79\nv_mfma_f32_16x16x32_bf16 v[28:31], v[156:159], v[64:67], v[28:31]\nv_rcp_f32 v76, v76\nv_mfma_f32_16x16x32_bf16 v[8:11], a[48:51], v[40:43], v[8:11]\n" \
"buffer_load_dwordx4 v0, s[12:15], s96 offen offset:3072 sc1 lds\nv_mfma_f32_16x16x32_bf16 v[12:15], a[48:51], v[56:59], v[12:15]\ns_xor_b32 m0, s22, 0x8000\ns_xor_b32 s96, s96, 0x9000\n" \
"v_fma_f32 v79, v79, v254, v254\nv_mfma_f32_16x16x32_bf16 v[16:19], a[176:179], v[40:43], v[16:19]\nds_read_b128 v[52:55], v7 offset:14336\nv_mul_f32 v48, v44, v48\n" \
"v_mfma_f32_16x16x32_bf16 v[20:23], a[176:179], v[56:59], v[20:23]\nds_read_b128 v[64:67], v7 offset:47104\nv_mul_f32 v49, v45, v49\nv_mfma_f32_16x16x32_bf16 v[24:27], v[160:163], v[40:43], v[24:27]\n" \
"s_waitcnt lgkmcnt(2)\nv_rcp_f32 v77, v77\nv_mfma_f32_16x16x32_bf16 v[28:31], v[160:163], v[56:59], v[28:31]\nbuffer_load_dwordx4 v0, s[12:15], s96 offen offset:0 sc1 lds\n" \
"v_mfma_f32_16x16x32_bf16 v[8:11], a[52:55], v[36:39], v[8:11]\nv_rcp_f32 v78, v78\nv_mfma_f32_16x16x32_bf16 v[12:15], a[52:55], v[84:87], v[12:15]\nv_rcp_f32 v79, v79\n" \
"v_mfma_f32_16x16x32_bf16 v[16:19], a[180:183], v[36:39], v[16:19]\nds_read_b128 v[40:43], v7 offset:15360\nv_mul_f32 v50, v46, v50\nv_mfma_f32_16x16x32_bf16 v[20:23], a[180:183], v[84:87], v[20:23]\n" \
"ds_read_b128 v[56:59], v7 offset:48128\nv_mul_f32 v51, v47, v51\nv_mfma_f32_16x16x32_bf16 v[24:27], v[164:167], v[36:39], v[24:27]\ns_waitcnt lgkmcnt(2)\n" \
"buffer_load_dwordx4 v0, s[12:15], s96 offen offset:1024 sc1 lds\nv_mfma_f32_16x16x32_bf16 v[28:31], v[164:167], v[84:87], v[28:31]\nv_mul_f32 v48, v48, v76\nv_mul_f32 v49, v49, v77\n" \
"v_mfma_f32_16x16x32_bf16 v[8:11], a[56:59], v[52:55], v[8:11]\nv_mul_f32 v50, v50, v78\nv_mul_f32 v51, v51, v79\nv_mfma_f32_16x16x32_bf16 v[12:15], a[56:59], v[64:67], v[12:15]\n" \
"v_cvt_pk_bf16_f32 v48, v48, v49\nv_cvt_pk_bf16_f32 v49, v50, v51\nv_mfma_f32_16x16x32_bf16 v[16:19], a[184:187], v[52:55], v[16:19]\nds_read_b128 v[36:39], v7 offset:16384\n" \
"v_mfma_f32_16x16x32_bf16 v[20:23], a[184:187], v[64:67], v[20:23]\nbuffer_load_dwordx4 v0, s[12:15], s96 offen offset:2048 sc1 lds\nv_mfma_f32_16x16x32_bf16 v[24:27], v[168:171], v[52:55], v[24:27]\ns_waitcnt lgkmcnt(1)\n" \
"ds_read_b128 v[44:47], v7 offset:49152\nv_mfma_f32_16x16x32_bf16 v[28:31], v[168:171], v[64:67], v[28:31]\nv_permlane16_swap_b32 v80, v88\nv_mfma_f32_16x16x32_bf16 v[8:11], a[60:63], v[40:43], v[8:11]\n" \
"v_permlane16_swap_b32 v81, v89\nv_mfma_f32_16x16x32_bf16 v[12:15], a[60:63], v[56:59], v[12:15]\nv_permlane16_swap_b32 v82, v90\nv_mfma_f32_16x16x32_bf16 v[16:19], a[188:191], v[40:43], v[16:19]\n" \
"buffer_load_dwordx4 v0, s[12:15], s96 offen offset:3072 sc1 lds\nv_mfma_f32_16x16x32_bf16 v[20:23], a[188:191], v[56:59], v[20:23]\nds_read_b128 v[52:55], v7 offset:17408\ns_xor_b32 m0, s22, 0x9000\n" \
"v_mfma_f32_16x16x32_bf16 v[24:27], v[172:175], v[40:43], v[24:27]\nds_read_b128 v[64:67], v7 offset:50176\ns_xor_b32 s96, s96, 0x1000\nv_mfma_f32_16x16x32_bf16 v[28:31], v[172:175], v[56:59], v[28:31]\n" \
"s_waitcnt lgkmcnt(2)\nv_permlane16_swap_b32 v83, v91\nv_mfma_f32_16x16x32_bf16 v[8:11], a[64:67], v[36:39], v[8:11]\ns_xor_b32 s22, s22, 0x10000\n" \
"v_mul_f32 v40, v80, v80\nv_mul_f32 v41, v81, v81\nv_mfma_f32_16x16x32_bf16 v[12:15], a[64:67], v[44:47], v[12:15]\nbuffer_load_dwordx4 v0, s[12:15], s96 offen offset:0 sc1 lds\n" \
"v_mfma_f32_16x16x32_bf16 v[16:19], a[192:195], v[36:39], v[16:19]\nds_read_b128 v[56:59], v7 offset:18432\nv_mul_f32 v42, v82, v82\nv_mfma_f32_16x16x32_bf16 v[20:23], a[192:195], v[44:47], v[20:23]\n" \
"ds_read_b128 v[76:79], v7 offset:51200\nv_mul_f32 v43, v83, v83\nv_mfma_f32_16x16x32_bf16 v[24:27], v[176:179], v[36:39], v[24:27]\ns_waitcnt lgkmcnt(2)\n" \
"v_fma_f32 v40, v40, v253, v252\nv_fma_f32 v41, v41, v253, v252\nv_mfma_f32_16x16x32_bf16 v[28:31], v[176:179], v[44:47], v[28:31]\nv_fma_f32 v42, v42, v253, v252\n" \
"v_fma_f32 v43, v43, v253, v252\nv_mfma_f32_16x16x32_bf16 v[8:11], a[68:71], v[52:55], v[8:11]\nbuffer_load_dwordx4 v0, s[12:15], s96 offen offset:1024 sc1 lds\nv_mfma_f32_16x16x32_bf16 v[12:15], a[68:71], v[64:67], v[12:15]\n" \
"v_mul_f32 v40, v40, v80\nv_mul_f32 v41, v41, v81\nv_mfma_f32_16x16x32_bf16 v[16:19], a[196:199], v[52:55], v[16:19]\nds_read_b128 v[36:39], v7 offset:19456\n" \
"v_mul_f32 v42, v42, v82\nv_mfma_f32_16x16x32_bf16 v[20:23], a[196:199], v[64:67], v[20:23]\nds_read_b128 v[44:47], v7 offset:52224\nv_mul_f32 v43, v43, v83\n" \
"v_mfma_f32_16x16x32_bf16 v[24:27], v[180:183], v[52:55], v[24:27]\ns_waitcnt lgkmcnt(2)\nv_exp_f32 v40, v40\nv_mfma_f32_16x16x32_bf16 v[28:31], v[180:183], v[64:67], v[28:31]\n" \
"buffer_load_dwordx4 v0, s[12:15], s96 offen offset:2048 sc1 lds\nv_mfma_f32_16x16x32_bf16 v[8:11], a[72:75], v[56:59], v[8:11]\nv_exp_f32 v41, v41\nv_mfma_f32_16x16x32_bf16 v[12:15], a[72:75], v[76:79], v[12:15]\n" \
"v_exp_f32 v42, v42\nv_mfma_f32_16x16x32_bf16 v[16:19], a[200:203], v[56:59], v[16:19]\nds_read_b128 v[52:55], v7 offset:20480\nv_fma_f32 v40, v40, v254, v254\n" \
"v_mfma_f32_16x16x32_bf16 v[20:23], a[200:203], v[76:79], v[20:23]\nds_read_b128 v[64:67], v7 offset:53248\nv_fma_f32 v41, v41, v254, v254\nv_mfma_f32_16x16x32_bf16 v[24:27], v[184:187], v[56:59], v[24:27]\n" \
"s_waitcnt lgkmcnt(2)\nbuffer_load_dwordx4 v0, s[12:15], s96 offen offset:3072 sc1 lds\nv_mfma_f32_16x16x32_bf16 v[28:31], v[184:187], v[76:79], v[28:31]\nv_exp_f32 v43, v43\n" \
"v_mfma_f32_16x16x32_bf16 v[8:11], a[76:79], v[36:39], v[8:11]\nv_rcp_f32 v40, v40\nv_mfma_f32_16x16x32_bf16 v[12:15], a[76:79], v[44:47], v[12:15]\nv_rcp_f32 v41, v41\n" \
"v_mfma_f32_16x16x32_bf16 v[16:19], a[204:207], v[36:39], v[16:19]\nds_read_b128 v[56:59], v7 offset:21504\nv_fma_f32 v42, v42, v254, v254\nv_mfma_f32_16x16x32_bf16 v[20:23], a[204:207], v[44:47], v[20:23]\n" \
"ds_read_b128 v[76:79], v7 offset:54272\nv_fma_f32 v43, v43, v254, v254\nv_mfma_f32_16x16x32_bf16 v[24:27], v[188:191], v[36:39], v[24:27]\ns_waitcnt lgkmcnt(2)\n" \
"v_rcp_f32 v42, v42\nv_mfma_f32_16x16x32_bf16 v[28:31], v[188:191], v[44:47], v[28:31]\nv_rcp_f32 v43, v43\nv_mfma_f32_16x16x32_bf16 v[8:11], a[80:83], v[52:55], v[8:11]\n" \
"buffer_store_dwordx2 v[60:61], v2, s[8:11], s16 offen offset:0 sc1\nv_mul_f32 v88, v80, v88\nv_mfma_f32_16x16x32_bf16 v[12:15], a[80:83], v[64:67], v[12:15]\nv_mul_f32 v89, v81, v89\n" \
"v_mul_f32 v90, v82, v90\nv_mfma_f32_16x16x32_bf16 v[16:19], a[208:211], v[52:55], v[16:19]\nbuffer_store_dwordx2 v[48:49], v2, s[8:11], s17 offen offset:256 sc1\nv_mul_f32 v91, v83, v91\n" \
"v_mfma_f32_16x16x32_bf16 v[20:23], a[208:211], v[64:67], v[20:23]\nv_mul_f32 v88, v88, v40\nv_mul_f32 v89, v89, v41\nv_mfma_f32_16x16x32_bf16 v[24:27], v[192:195], v[52:55], v[24:27]\n" \
"ds_read_b128 v[36:39], v7 offset:22528\nv_mul_f32 v90, v90, v42\nv_mul_f32 v91, v91, v43\nv_mfma_f32_16x16x32_bf16 v[28:31], v[192:195], v[64:67], v[28:31]\n" \
"s_waitcnt lgkmcnt(1)\nds_read_b128 v[40:43], v7 offset:55296\nv_cvt_pk_bf16_f32 v88, v88, v89\nv_mfma_f32_16x16x32_bf16 v[8:11], a[84:87], v[56:59], v[8:11]\n" \
"ds_read_b128 v[44:47], v4 offset:0\nv_cvt_pk_bf16_f32 v89, v90, v91\nv_mfma_f32_16x16x32_bf16 v[12:15], a[84:87], v[76:79], v[12:15]\nbuffer_store_dwordx2 v[88:89], v2, s[8:11], s17 offen offset:512 sc1\n" \
"v_add_f32 v72, v72, v68\nv_mfma_f32_16x16x32_bf16 v[16:19], a[212:215], v[56:59], v[16:19]\nds_read_b128 v[48:51], v7 offset:23552\nv_add_f32 v73, v73, v69\n" \
"v_mfma_f32_16x16x32_bf16 v[20:23], a[212:215], v[76:79], v[20:23]\nds_read_b128 v[52:55], v7 offset:56320\nv_add_f32 v74, v74, v70\nv_mfma_f32_16x16x32_bf16 v[24:27], v[196:199], v[56:59], v[24:27]\n" \
"ds_read_b128 v[60:63], v4 offset:512\nv_add_f32 v75, v75, v71\nv_mfma_f32_16x16x32_bf16 v[28:31], v[196:199], v[76:79], v[28:31]\ns_waitcnt lgkmcnt(4)\n" \
"v_permlane16_swap_b32 v72, v74\nv_mfma_f32_16x16x32_bf16 v[8:11], a[88:91], v[36:39], v[8:11]\nv_permlane16_swap_b32 v73, v75\nv_mfma_f32_16x16x32_bf16 v[12:15], a[88:91], v[40:43], v[12:15]\n" \
"s_waitcnt lgkmcnt(3)\nv_add_f32 v44, v44, v45\nv_add_f32 v46, v46, v47\nv_mfma_f32_16x16x32_bf16 v[16:19], a[216:219], v[36:39], v[16:19]\n" \
"ds_read_b128 v[56:59], v7 offset:24576\nv_add_f32 v44, v44, v46\nv_mfma_f32_16x16x32_bf16 v[20:23], a[216:219], v[40:43], v[20:23]\nds_read_b128 v[64:67], v7 offset:57344\n" \
"v_mul_f32 v44, 0xba800000, v44\nv_mfma_f32_16x16x32_bf16 v[24:27], v[200:203], v[36:39], v[24:27]\ns_waitcnt lgkmcnt(2)\nv_add_f32 v72, v72, v74\n" \
"v_add_f32 v73, v73, v75\nv_mfma_f32_16x16x32_bf16 v[28:31], v[200:203], v[40:43], v[28:31]\nv_add_f32 v60, v60, v61\nv_add_f32 v62, v62, v63\n" \
"v_mfma_f32_16x16x32_bf16 v[8:11], a[92:95], v[48:51], v[8:11]\nv_add_f32 v60, v60, v62\nv_mul_f32 v61, v44, -v44\nv_mfma_f32_16x16x32_bf16 v[12:15], a[92:95], v[52:55], v[12:15]\n" \
"v_fmamk_f32 v35, v60, 0x3a800000, v61\nv_cvt_pk_bf16_f32 v35, v35, v35\nv_mfma_f32_16x16x32_bf16 v[16:19], a[220:223], v[48:51], v[16:19]\nds_read_b128 v[60:63], v7 offset:25600\n" \
"v_lshlrev_b32 v35, 16, v35\nv_mfma_f32_16x16x32_bf16 v[20:23], a[220:223], v[52:55], v[20:23]\nds_read_b128 v[68:71], v7 offset:58368\nv_max_f32 v35, 0, v35\n" \
"v_mfma_f32_16x16x32_bf16 v[24:27], v[204:207], v[48:51], v[24:27]\ns_waitcnt lgkmcnt(2)\nv_add_f32 v35, 0x3727c5ac, v35\nv_cvt_pk_bf16_f32 v111, v35, v44\n" \
"v_mfma_f32_16x16x32_bf16 v[28:31], v[204:207], v[52:55], v[28:31]\nv_permlane32_swap_b32 v72, v73\nv_mfma_f32_16x16x32_bf16 v[8:11], a[96:99], v[56:59], v[8:11]\nv_lshlrev_b32 v35, 16, v111\n" \
"v_add_f32 v72, v72, v73\nv_mfma_f32_16x16x32_bf16 v[12:15], a[96:99], v[64:67], v[12:15]\nv_sqrt_f32 v35, v35\nv_mfma_f32_16x16x32_bf16 v[16:19], a[224:227], v[56:59], v[16:19]\n" \
"ds_read_b128 v[36:39], v7 offset:26624\nv_cvt_pk_bf16_f32 v35, v35, v35\nv_mfma_f32_16x16x32_bf16 v[20:23], a[224:227], v[64:67], v[20:23]\nds_read_b128 v[40:43], v7 offset:59392\n" \
"v_lshlrev_b32 v35, 16, v35\nv_mfma_f32_16x16x32_bf16 v[24:27], v[208:211], v[56:59], v[24:27]\ns_waitcnt lgkmcnt(2)\nv_rcp_f32 v34, v35\n" \
"v_mfma_f32_16x16x32_bf16 v[28:31], v[208:211], v[64:67], v[28:31]\nds_write_b32 v5, v72 offset:0\nv_mfma_f32_16x16x32_bf16 v[8:11], a[100:103], v[60:63], v[8:11]\nv_and_b32 v111, 0xffff0000, v111\n" \
"v_mfma_f32_16x16x32_bf16 v[12:15], a[100:103], v[68:71], v[12:15]\nv_mfma_f32_16x16x32_bf16 v[16:19], a[228:231], v[60:63], v[16:19]\nds_read_b128 v[44:47], v7 offset:27648\nv_mfma_f32_16x16x32_bf16 v[20:23], a[228:231], v[68:71], v[20:23]\n" \
"ds_read_b128 v[48:51], v7 offset:60416\nv_mfma_f32_16x16x32_bf16 v[24:27], v[212:215], v[60:63], v[24:27]\nv_mfma_f32_16x16x32_bf16 v[28:31], v[212:215], v[68:71], v[28:31]\ns_branch 1b\n" \
"4:\ns_barrier\ns_lshr_b32 s94, s20, 1\nv_readlane_b32 s94, v6, s94\n" \
"s_and_b32 s95, s20, 1\ns_lshl_b32 s95, s95, 3\ns_lshr_b32 s94, s94, s95\ns_and_b32 s94, s94, 0x7f\n" \
"s_lshl_b32 s95, s94, 13\nbuffer_load_dwordx4 v[92:95], v1, s[8:11], s95 offen offset:0 sc1\nbuffer_load_dwordx4 v[96:99], v1, s[8:11], s95 offen offset:1024 sc1\nv_fma_f32 v80, v32, v240, v8\n" \
"v_fma_f32 v81, v32, v241, v9\nv_fma_f32 v82, v32, v242, v10\nv_fma_f32 v83, v32, v243, v11\nv_fma_f32 v84, v33, v240, v12\n" \
"v_fma_f32 v85, v33, v241, v13\nv_fma_f32 v86, v33, v242, v14\nv_fma_f32 v87, v33, v243, v15\nv_permlane16_swap_b32 v80, v84\n" \
"v_permlane16_swap_b32 v81, v85\nv_permlane16_swap_b32 v82, v86\nv_permlane16_swap_b32 v83, v87\nv_mul_f32 v88, v80, v80\n" \
"v_mul_f32 v89, v81, v81\nv_mul_f32 v90, v82, v82\nv_mul_f32 v91, v83, v83\nv_fma_f32 v88, v88, v253, v252\n" \
"v_fma_f32 v89, v89, v253, v252\nv_fma_f32 v90, v90, v253, v252\nv_fma_f32 v91, v91, v253, v252\nv_mul_f32 v88, v88, v80\n" \
"v_mul_f32 v89, v89, v81\nv_mul_f32 v90, v90, v82\nv_mul_f32 v91, v91, v83\nv_mul_f32 v84, v80, v84\n" \
"v_mul_f32 v85, v81, v85\nv_mul_f32 v86, v82, v86\nv_mul_f32 v87, v83, v87\nv_exp_f32 v88, v88\n" \
"v_exp_f32 v89, v89\nv_exp_f32 v90, v90\nv_exp_f32 v91, v91\nv_fma_f32 v88, v88, v254, v254\n" \
"v_fma_f32 v89, v89, v254, v254\nv_fma_f32 v90, v90, v254, v254\nv_fma_f32 v91, v91, v254, v254\nv_rcp_f32 v88, v88\n" \
"v_rcp_f32 v89, v89\nv_rcp_f32 v90, v90\nv_rcp_f32 v91, v91\nv_mul_f32 v84, v84, v88\n" \
"v_mul_f32 v85, v85, v89\nv_mul_f32 v86, v86, v90\nv_mul_f32 v87, v87, v91\nv_cvt_pk_bf16_f32 v84, v84, v85\n" \
"v_cvt_pk_bf16_f32 v85, v86, v87\nbuffer_store_dwordx2 v[84:85], v2, s[8:11], s16 offen offset:0 sc1\nv_fma_f32 v80, v32, v244, v16\nv_fma_f32 v81, v32, v245, v17\n" \
"v_fma_f32 v82, v32, v246, v18\nv_fma_f32 v83, v32, v247, v19\nv_fma_f32 v84, v33, v244, v20\nv_fma_f32 v85, v33, v245, v21\n" \
"v_fma_f32 v86, v33, v246, v22\nv_fma_f32 v87, v33, v247, v23\nv_permlane16_swap_b32 v80, v84\nv_permlane16_swap_b32 v81, v85\n" \
"v_permlane16_swap_b32 v82, v86\nv_permlane16_swap_b32 v83, v87\nv_mul_f32 v88, v80, v80\nv_mul_f32 v89, v81, v81\n" \
"v_mul_f32 v90, v82, v82\nv_mul_f32 v91, v83, v83\nv_fma_f32 v88, v88, v253, v252\nv_fma_f32 v89, v89, v253, v252\n" \
"v_fma_f32 v90, v90, v253, v252\nv_fma_f32 v91, v91, v253, v252\nv_mul_f32 v88, v88, v80\nv_mul_f32 v89, v89, v81\n" \
"v_mul_f32 v90, v90, v82\nv_mul_f32 v91, v91, v83\nv_mul_f32 v84, v80, v84\nv_mul_f32 v85, v81, v85\n" \
"v_mul_f32 v86, v82, v86\nv_mul_f32 v87, v83, v87\nv_exp_f32 v88, v88\nv_exp_f32 v89, v89\n" \
"v_exp_f32 v90, v90\nv_exp_f32 v91, v91\nv_fma_f32 v88, v88, v254, v254\nv_fma_f32 v89, v89, v254, v254\n" \
"v_fma_f32 v90, v90, v254, v254\nv_fma_f32 v91, v91, v254, v254\nv_rcp_f32 v88, v88\nv_rcp_f32 v89, v89\n" \
"v_rcp_f32 v90, v90\nv_rcp_f32 v91, v91\nv_mul_f32 v84, v84, v88\nv_mul_f32 v85, v85, v89\n" \
"v_mul_f32 v86, v86, v90\nv_mul_f32 v87, v87, v91\nv_cvt_pk_bf16_f32 v84, v84, v85\nv_cvt_pk_bf16_f32 v85, v86, v87\n" \
"buffer_store_dwordx2 v[84:85], v2, s[8:11], s17 offen offset:256 sc1\nv_fma_f32 v80, v32, v248, v24\nv_fma_f32 v81, v32, v249, v25\nv_fma_f32 v82, v32, v250, v26\n" \
"v_fma_f32 v83, v32, v251, v27\nv_fma_f32 v84, v33, v248, v28\nv_fma_f32 v85, v33, v249, v29\nv_fma_f32 v86, v33, v250, v30\n" \
"v_fma_f32 v87, v33, v251, v31\nv_permlane16_swap_b32 v80, v84\nv_permlane16_swap_b32 v81, v85\nv_permlane16_swap_b32 v82, v86\n" \
"v_permlane16_swap_b32 v83, v87\nv_mul_f32 v88, v80, v80\nv_mul_f32 v89, v81, v81\nv_mul_f32 v90, v82, v82\n" \
"v_mul_f32 v91, v83, v83\nv_fma_f32 v88, v88, v253, v252\nv_fma_f32 v89, v89, v253, v252\nv_fma_f32 v90, v90, v253, v252\n" \
"v_fma_f32 v91, v91, v253, v252\nv_mul_f32 v88, v88, v80\nv_mul_f32 v89, v89, v81\nv_mul_f32 v90, v90, v82\n" \
"v_mul_f32 v91, v91, v83\nv_mul_f32 v84, v80, v84\nv_mul_f32 v85, v81, v85\nv_mul_f32 v86, v82, v86\n" \
"v_mul_f32 v87, v83, v87\nv_exp_f32 v88, v88\nv_exp_f32 v89, v89\nv_exp_f32 v90, v90\n" \
"v_exp_f32 v91, v91\nv_fma_f32 v88, v88, v254, v254\nv_fma_f32 v89, v89, v254, v254\nv_fma_f32 v90, v90, v254, v254\n" \
"v_fma_f32 v91, v91, v254, v254\nv_rcp_f32 v88, v88\nv_rcp_f32 v89, v89\nv_rcp_f32 v90, v90\n" \
"v_rcp_f32 v91, v91\nv_mul_f32 v84, v84, v88\nv_mul_f32 v85, v85, v89\nv_mul_f32 v86, v86, v90\n" \
"v_mul_f32 v87, v87, v91\nv_cvt_pk_bf16_f32 v84, v84, v85\nv_cvt_pk_bf16_f32 v85, v86, v87\nbuffer_store_dwordx2 v[84:85], v2, s[8:11], s17 offen offset:512 sc1\n" \
"s_mov_b32 s16, 0x80000000\ns_mov_b32 s17, 0x80000000\nv_mov_b32 v80, s20\ns_waitcnt vmcnt(3)\n" \
"v_max3_u32 v81, v92, v94, v96\nv_max_u32 v81, v81, v98\nv_cmp_eq_u32 vcc, -1, v81\ns_cbranch_vccz 8f\n" \
"6:\ns_waitcnt vmcnt(0)\nbuffer_store_dword v80, v3, s[8:11], 0 offen offset:0 sc1\ns_mov_b32 s23, 0x40000000\n" \
"7:\ns_add_u32 s24, s24, 1\ns_cmp_lt_u32 s24, 0x40000\ns_cbranch_scc0 9f\n" \
"buffer_load_dwordx4 v[92:95], v1, s[8:11], s95 offen offset:0 sc1\nbuffer_load_dwordx4 v[96:99], v1, s[8:11], s95 offen offset:1024 sc1\ns_waitcnt vmcnt(0)\nv_max3_u32 v81, v92, v94, v96\n" \
"v_max_u32 v81, v81, v98\nv_cmp_eq_u32 vcc, -1, v81\ns_cbranch_vccz 8f\ns_sleep 8\n" \
"s_branch 7b\n8:\ns_cmp_ge_u32 s20, s26\ns_cbranch_scc1 10f\n" \
"s_lshl_b32 s94, s94, 16\ns_add_u32 s94, s94, s21\ns_xor_b32 s95, s22, 0x10000\ns_xor_b32 m0, s95, 0\n" \
"s_xor_b32 s94, s94, 0\nbuffer_load_dwordx4 v0, s[8:11], s94 offen offset:0 sc1 lds\nbuffer_load_dwordx4 v0, s[8:11], s94 offen offset:1024 sc1 lds\nbuffer_load_dwordx4 v0, s[8:11], s94 offen offset:2048 sc1 lds\n" \
"buffer_load_dwordx4 v0, s[8:11], s94 offen offset:3072 sc1 lds\ns_xor_b32 m0, s95, 0x1000\ns_xor_b32 s94, s94, 0x1000\nbuffer_load_dwordx4 v0, s[8:11], s94 offen offset:0 sc1 lds\n" \
"buffer_load_dwordx4 v0, s[8:11], s94 offen offset:1024 sc1 lds\nbuffer_load_dwordx4 v0, s[8:11], s94 offen offset:2048 sc1 lds\nbuffer_load_dwordx4 v0, s[8:11], s94 offen offset:3072 sc1 lds\ns_xor_b32 m0, s95, 0x8000\n" \
"s_xor_b32 s94, s94, 0x9000\nbuffer_load_dwordx4 v0, s[8:11], s94 offen offset:0 sc1 lds\nbuffer_load_dwordx4 v0, s[8:11], s94 offen offset:1024 sc1 lds\nbuffer_load_dwordx4 v0, s[8:11], s94 offen offset:2048 sc1 lds\n" \
"buffer_load_dwordx4 v0, s[8:11], s94 offen offset:3072 sc1 lds\ns_xor_b32 m0, s95, 0x9000\ns_xor_b32 s94, s94, 0x1000\nbuffer_load_dwordx4 v0, s[8:11], s94 offen offset:0 sc1 lds\n" \
"buffer_load_dwordx4 v0, s[8:11], s94 offen offset:1024 sc1 lds\nbuffer_load_dwordx4 v0, s[8:11], s94 offen offset:2048 sc1 lds\nbuffer_load_dwordx4 v0, s[8:11], s94 offen offset:3072 sc1 lds\nv_add_f32 v92, v92, v96\n" \
"v_add_f32 v93, v93, v97\nv_add_f32 v94, v94, v98\nv_add_f32 v95, v95, v99\ns_nop 1\n" \
"v_permlane16_swap_b32 v92, v94\nv_permlane16_swap_b32 v93, v95\nv_add_f32 v92, v92, v94\nv_add_f32 v93, v93, v95\n" \
"s_nop 1\nv_permlane32_swap_b32 v92, v93\nv_add_f32 v92, v92, v93\nds_write_b32 v5, v92\n" \
"s_add_u32 s97, s20, 1\ns_lshr_b32 s96, s97, 1\nv_readlane_b32 s96, v6, s96\ns_and_b32 s97, s97, 1\n" \
"s_lshl_b32 s97, s97, 3\ns_lshr_b32 s96, s96, s97\ns_and_b32 s96, s96, 0x7f\ns_lshl_b32 s97, s96, 13\n" \
"s_lshl_b32 s96, s96, 16\ns_add_u32 s96, s96, s21\nbuffer_load_dwordx4 v[72:75], v1, s[8:11], s97 offen offset:0 sc1\nbuffer_load_dwordx4 v[68:71], v1, s[8:11], s97 offen offset:1024 sc1\n" \
"s_waitcnt vmcnt(18)\nbuffer_store_dword v80, v3, s[8:11], s23 offen offset:0 sc1\ns_mov_b32 s23, 0x40000000\ns_waitcnt vmcnt(3) lgkmcnt(0)\n" \
"s_barrier\nds_read_b128 v[56:59], v7 offset:0\nds_read_b128 v[36:39], v7 offset:32768\nds_read_b128 v[40:43], v7 offset:1024\n" \
"ds_read_b128 v[76:79], v7 offset:33792\ns_waitcnt lgkmcnt(0)\ns_branch 5b\n10:\n" \
"s_mul_i32 s16, s94, 0x38000\ns_lshl_b32 s94, s94, 16\ns_and_b32 s95, s21, 0xffff6fff\ns_add_u32 s94, s94, s95\n" \
"s_lshl_b32 s95, s27, 15\ns_add_u32 s94, s94, s95\ns_xor_b32 s95, s22, 0x10000\ns_and_b32 s95, s95, 0xffff6fff\n" \
"s_add_u32 m0, s95, 0\ns_add_u32 s94, s94, 0\nbuffer_load_dwordx4 v0, s[8:11], s94 offen offset:0 sc1 lds\nbuffer_load_dwordx4 v0, s[8:11], s94 offen offset:1024 sc1 lds\n" \
"buffer_load_dwordx4 v0, s[8:11], s94 offen offset:2048 sc1 lds\nbuffer_load_dwordx4 v0, s[8:11], s94 offen offset:3072 sc1 lds\ns_add_u32 m0, s95, 0x1000\ns_add_u32 s94, s94, 0x1000\n" \
"buffer_load_dwordx4 v0, s[8:11], s94 offen offset:0 sc1 lds\nbuffer_load_dwordx4 v0, s[8:11], s94 offen offset:1024 sc1 lds\nbuffer_load_dwordx4 v0, s[8:11], s94 offen offset:2048 sc1 lds\nbuffer_load_dwordx4 v0, s[8:11], s94 offen offset:3072 sc1 lds\n" \
"v_add_f32 v92, v92, v96\nv_add_f32 v93, v93, v97\nv_add_f32 v94, v94, v98\nv_add_f32 v95, v95, v99\n" \
"s_nop 1\nv_permlane16_swap_b32 v92, v94\nv_permlane16_swap_b32 v93, v95\nv_add_f32 v92, v92, v94\n" \
"v_add_f32 v93, v93, v95\ns_nop 1\nv_permlane32_swap_b32 v92, v93\nv_add_f32 v92, v92, v93\n" \
"ds_write_b32 v5, v92\ns_waitcnt vmcnt(8)\nbuffer_store_dword v80, v3, s[8:11], s23 offen offset:0 sc1\ns_mov_b32 s23, 0x40000000\n" \
"s_lshl_b32 s94, s27, 7\nv_and_b32 v4, 0xffffff7f, v4\nv_or_b32 v4, s94, v4\ns_mul_i32 s94, s27, 0x1c000\n" \
"s_add_u32 s94, s94, s16\ns_or_b32 s16, s94, s18\ns_or_b32 s17, s94, s19\nv_lshlrev_b32 v81, 23, v0\n" \
"v_and_b32 v81, 0x80000000, v81\nv_or_b32 v2, v2, v81\ns_add_u32 s20, s20, 1\nv_mov_b32 v80, s20\n" \
"s_waitcnt vmcnt(1) lgkmcnt(0)\ns_barrier\nds_read_b128 v[60:63], v4 offset:0\nds_read_b128 v[64:67], v4 offset:512\n" \
"ds_read_b128 v[36:39], v7 offset:0\nds_read_b128 v[40:43], v7 offset:1024\nds_read_b128 v[44:47], v7 offset:2048\nds_read_b128 v[48:51], v7 offset:3072\n" \
"ds_read_b128 v[52:55], v7 offset:4096\nds_read_b128 v[56:59], v7 offset:5120\ns_waitcnt lgkmcnt(5)\nv_mfma_f32_16x16x32_bf16 v[8:11], a[0:3], v[36:39], 0\n" \
"v_add_f32 v60, v60, v61\nv_mfma_f32_16x16x32_bf16 v[16:19], a[128:131], v[36:39], 0\nv_add_f32 v62, v62, v63\nv_mfma_f32_16x16x32_bf16 v[24:27], v[112:115], v[36:39], 0\n" \
"v_add_f32 v60, v60, v62\nds_read_b128 v[36:39], v7 offset:6144\ns_waitcnt lgkmcnt(5)\nv_mfma_f32_16x16x32_bf16 v[8:11], a[4:7], v[40:43], v[8:11]\n" \
"v_add_f32 v64, v64, v65\nv_mfma_f32_16x16x32_bf16 v[16:19], a[132:135], v[40:43], v[16:19]\nv_add_f32 v66, v66, v67\nv_mfma_f32_16x16x32_bf16 v[24:27], v[116:119], v[40:43], v[24:27]\n" \
"v_add_f32 v64, v64, v66\nds_read_b128 v[40:43], v7 offset:7168\ns_waitcnt lgkmcnt(5)\nv_mfma_f32_16x16x32_bf16 v[8:11], a[8:11], v[44:47], v[8:11]\n" \
"v_mul_f32 v60, 0xba800000, v60\nv_mfma_f32_16x16x32_bf16 v[16:19], a[136:139], v[44:47], v[16:19]\nv_mul_f32 v65, v60, -v60\nv_mfma_f32_16x16x32_bf16 v[24:27], v[120:123], v[44:47], v[24:27]\n" \
"v_fmamk_f32 v68, v64, 0x3a800000, v65\nds_read_b128 v[44:47], v7 offset:8192\ns_waitcnt lgkmcnt(5)\nv_mfma_f32_16x16x32_bf16 v[8:11], a[12:15], v[48:51], v[8:11]\n" \
"v_cvt_pk_bf16_f32 v68, v68, v68\nv_mfma_f32_16x16x32_bf16 v[16:19], a[140:143], v[48:51], v[16:19]\nv_lshlrev_b32 v68, 16, v68\nv_mfma_f32_16x16x32_bf16 v[24:27], v[124:127], v[48:51], v[24:27]\n" \
"v_max_f32 v68, 0, v68\nds_read_b128 v[48:51], v7 offset:9216\ns_waitcnt lgkmcnt(5)\nv_mfma_f32_16x16x32_bf16 v[8:11], a[16:19], v[52:55], v[8:11]\n" \
"v_add_f32 v68, 0x3727c5ac, v68\nv_mfma_f32_16x16x32_bf16 v[16:19], a[144:147], v[52:55], v[16:19]\nv_cvt_pk_bf16_f32 v70, v68, v60\nv_mfma_f32_16x16x32_bf16 v[24:27], v[128:131], v[52:55], v[24:27]\n" \
"v_lshlrev_b32 v68, 16, v70\nds_read_b128 v[52:55], v7 offset:10240\ns_waitcnt lgkmcnt(5)\nv_mfma_f32_16x16x32_bf16 v[8:11], a[20:23], v[56:59], v[8:11]\n" \
"v_sqrt_f32 v68, v68\nv_mfma_f32_16x16x32_bf16 v[16:19], a[148:151], v[56:59], v[16:19]\nv_cvt_pk_bf16_f32 v68, v68, v68\nv_mfma_f32_16x16x32_bf16 v[24:27], v[132:135], v[56:59], v[24:27]\n" \
"v_lshlrev_b32 v68, 16, v68\nds_read_b128 v[56:59], v7 offset:11264\ns_waitcnt lgkmcnt(5)\nv_mfma_f32_16x16x32_bf16 v[8:11], a[24:27], v[36:39], v[8:11]\n" \
"v_and_b32 v70, 0xffff0000, v70\nv_mfma_f32_16x16x32_bf16 v[16:19], a[152:155], v[36:39], v[16:19]\nv_rcp_f32 v69, v68\nv_mfma_f32_16x16x32_bf16 v[24:27], v[136:139], v[36:39], v[24:27]\n" \
"v_mul_f32 v254, v68, v68\nds_read_b128 v[36:39], v7 offset:12288\ns_waitcnt lgkmcnt(5)\nv_mfma_f32_16x16x32_bf16 v[8:11], a[28:31], v[40:43], v[8:11]\n" \
"v_mul_f32 v252, 0xc0135761, v69\nv_mfma_f32_16x16x32_bf16 v[16:19], a[156:159], v[40:43], v[16:19]\nv_mul_f32 v69, v69, v69\nv_mfma_f32_16x16x32_bf16 v[24:27], v[140:143], v[40:43], v[24:27]\n" \
"v_mul_f32 v69, v69, v252\nds_read_b128 v[40:43], v7 offset:13312\ns_waitcnt lgkmcnt(5)\nv_mfma_f32_16x16x32_bf16 v[8:11], a[32:35], v[44:47], v[8:11]\n" \
"v_mul_f32 v253, 0x3d372713, v69\nv_mfma_f32_16x16x32_bf16 v[16:19], a[160:163], v[44:47], v[16:19]\nv_mov_b32 v32, v70\nv_mfma_f32_16x16x32_bf16 v[24:27], v[144:147], v[44:47], v[24:27]\n" \
"v_mov_b32 v33, v70\nds_read_b128 v[44:47], v7 offset:14336\ns_waitcnt lgkmcnt(5)\nv_mfma_f32_16x16x32_bf16 v[8:11], a[36:39], v[48:51], v[8:11]\n" \
"v_permlane16_swap_b32 v32, v33\nv_mfma_f32_16x16x32_bf16 v[16:19], a[164:167], v[48:51], v[16:19]\nv_mfma_f32_16x16x32_bf16 v[24:27], v[148:151], v[48:51], v[24:27]\nds_read_b128 v[48:51], v7 offset:15360\n" \
"s_waitcnt lgkmcnt(5)\nv_mfma_f32_16x16x32_bf16 v[8:11], a[40:43], v[52:55], v[8:11]\nv_mfma_f32_16x16x32_bf16 v[16:19], a[168:171], v[52:55], v[16:19]\nv_mfma_f32_16x16x32_bf16 v[24:27], v[152:155], v[52:55], v[24:27]\n" \
"ds_read_b128 v[52:55], v7 offset:16384\ns_waitcnt lgkmcnt(5)\nv_mfma_f32_16x16x32_bf16 v[8:11], a[44:47], v[56:59], v[8:11]\nv_mfma_f32_16x16x32_bf16 v[16:19], a[172:175], v[56:59], v[16:19]\n" \
"v_mfma_f32_16x16x32_bf16 v[24:27], v[156:159], v[56:59], v[24:27]\nds_read_b128 v[56:59], v7 offset:17408\ns_waitcnt lgkmcnt(5)\nv_mfma_f32_16x16x32_bf16 v[8:11], a[48:51], v[36:39], v[8:11]\n" \
"v_mfma_f32_16x16x32_bf16 v[16:19], a[176:179], v[36:39], v[16:19]\nv_mfma_f32_16x16x32_bf16 v[24:27], v[160:163], v[36:39], v[24:27]\nds_read_b128 v[36:39], v7 offset:18432\ns_waitcnt lgkmcnt(5)\n" \
"v_mfma_f32_16x16x32_bf16 v[8:11], a[52:55], v[40:43], v[8:11]\nv_mfma_f32_16x16x32_bf16 v[16:19], a[180:183], v[40:43], v[16:19]\nv_mfma_f32_16x16x32_bf16 v[24:27], v[164:167], v[40:43], v[24:27]\nds_read_b128 v[40:43], v7 offset:19456\n" \
"s_waitcnt lgkmcnt(5)\nv_mfma_f32_16x16x32_bf16 v[8:11], a[56:59], v[44:47], v[8:11]\nv_mfma_f32_16x16x32_bf16 v[16:19], a[184:187], v[44:47], v[16:19]\nv_mfma_f32_16x16x32_bf16 v[24:27], v[168:171], v[44:47], v[24:27]\n" \
"ds_read_b128 v[44:47], v7 offset:20480\ns_waitcnt lgkmcnt(5)\nv_mfma_f32_16x16x32_bf16 v[8:11], a[60:63], v[48:51], v[8:11]\nv_mfma_f32_16x16x32_bf16 v[16:19], a[188:191], v[48:51], v[16:19]\n" \
"v_mfma_f32_16x16x32_bf16 v[24:27], v[172:175], v[48:51], v[24:27]\nds_read_b128 v[48:51], v7 offset:21504\ns_waitcnt lgkmcnt(5)\nv_mfma_f32_16x16x32_bf16 v[8:11], a[64:67], v[52:55], v[8:11]\n" \
"v_mfma_f32_16x16x32_bf16 v[16:19], a[192:195], v[52:55], v[16:19]\nv_mfma_f32_16x16x32_bf16 v[24:27], v[176:179], v[52:55], v[24:27]\nds_read_b128 v[52:55], v7 offset:22528\ns_waitcnt lgkmcnt(5)\n" \
"v_mfma_f32_16x16x32_bf16 v[8:11], a[68:71], v[56:59], v[8:11]\nv_mfma_f32_16x16x32_bf16 v[16:19], a[196:199], v[56:59], v[16:19]\nv_mfma_f32_16x16x32_bf16 v[24:27], v[180:183], v[56:59], v[24:27]\nds_read_b128 v[56:59], v7 offset:23552\n" \
"s_waitcnt lgkmcnt(5)\nv_mfma_f32_16x16x32_bf16 v[8:11], a[72:75], v[36:39], v[8:11]\nv_mfma_f32_16x16x32_bf16 v[16:19], a[200:203], v[36:39], v[16:19]\nv_mfma_f32_16x16x32_bf16 v[24:27], v[184:187], v[36:39], v[24:27]\n" \
"ds_read_b128 v[36:39], v7 offset:24576\ns_waitcnt lgkmcnt(5)\nv_mfma_f32_16x16x32_bf16 v[8:11], a[76:79], v[40:43], v[8:11]\nv_mfma_f32_16x16x32_bf16 v[16:19], a[204:207], v[40:43], v[16:19]\n" \
"v_mfma_f32_16x16x32_bf16 v[24:27], v[188:191], v[40:43], v[24:27]\nds_read_b128 v[40:43], v7 offset:25600\ns_waitcnt lgkmcnt(5)\nv_mfma_f32_16x16x32_bf16 v[8:11], a[80:83], v[44:47], v[8:11]\n" \
"v_mfma_f32_16x16x32_bf16 v[16:19], a[208:211], v[44:47], v[16:19]\nv_mfma_f32_16x16x32_bf16 v[24:27], v[192:195], v[44:47], v[24:27]\nds_read_b128 v[44:47], v7 offset:26624\ns_waitcnt lgkmcnt(5)\n" \
"v_mfma_f32_16x16x32_bf16 v[8:11], a[84:87], v[48:51], v[8:11]\nv_mfma_f32_16x16x32_bf16 v[16:19], a[212:215], v[48:51], v[16:19]\nv_mfma_f32_16x16x32_bf16 v[24:27], v[196:199], v[48:51], v[24:27]\nds_read_b128 v[48:51], v7 offset:27648\n" \
"s_waitcnt lgkmcnt(5)\nv_mfma_f32_16x16x32_bf16 v[8:11], a[88:91], v[52:55], v[8:11]\nv_mfma_f32_16x16x32_bf16 v[16:19], a[216:219], v[52:55], v[16:19]\nv_mfma_f32_16x16x32_bf16 v[24:27], v[200:203], v[52:55], v[24:27]\n" \
"ds_read_b128 v[52:55], v7 offset:28672\ns_waitcnt lgkmcnt(5)\nv_mfma_f32_16x16x32_bf16 v[8:11], a[92:95], v[56:59], v[8:11]\nv_mfma_f32_16x16x32_bf16 v[16:19], a[220:223], v[56:59], v[16:19]\n" \
"v_mfma_f32_16x16x32_bf16 v[24:27], v[204:207], v[56:59], v[24:27]\nds_read_b128 v[56:59], v7 offset:29696\ns_waitcnt lgkmcnt(5)\nv_mfma_f32_16x16x32_bf16 v[8:11], a[96:99], v[36:39], v[8:11]\n" \
"v_mfma_f32_16x16x32_bf16 v[16:19], a[224:227], v[36:39], v[16:19]\nv_mfma_f32_16x16x32_bf16 v[24:27], v[208:211], v[36:39], v[24:27]\nds_read_b128 v[36:39], v7 offset:30720\ns_waitcnt lgkmcnt(5)\n" \
"v_mfma_f32_16x16x32_bf16 v[8:11], a[100:103], v[40:43], v[8:11]\nv_mfma_f32_16x16x32_bf16 v[16:19], a[228:231], v[40:43], v[16:19]\nv_mfma_f32_16x16x32_bf16 v[24:27], v[212:215], v[40:43], v[24:27]\nds_read_b128 v[40:43], v7 offset:31744\n" \
"s_waitcnt lgkmcnt(5)\nv_mfma_f32_16x16x32_bf16 v[8:11], a[104:107], v[44:47], v[8:11]\nv_mfma_f32_16x16x32_bf16 v[16:19], a[232:235], v[44:47], v[16:19]\nv_mfma_f32_16x16x32_bf16 v[24:27], v[216:219], v[44:47], v[24:27]\n" \
"s_waitcnt lgkmcnt(4)\nv_mfma_f32_16x16x32_bf16 v[8:11], a[108:111], v[48:51], v[8:11]\nv_mfma_f32_16x16x32_bf16 v[16:19], a[236:239], v[48:51], v[16:19]\nv_mfma_f32_16x16x32_bf16 v[24:27], v[220:223], v[48:51], v[24:27]\n" \
"s_waitcnt lgkmcnt(3)\nv_mfma_f32_16x16x32_bf16 v[8:11], a[112:115], v[52:55], v[8:11]\nv_mfma_f32_16x16x32_bf16 v[16:19], a[240:243], v[52:55], v[16:19]\nv_mfma_f32_16x16x32_bf16 v[24:27], v[224:227], v[52:55], v[24:27]\n" \
"s_waitcnt lgkmcnt(2)\nv_mfma_f32_16x16x32_bf16 v[8:11], a[116:119], v[56:59], v[8:11]\nv_mfma_f32_16x16x32_bf16 v[16:19], a[244:247], v[56:59], v[16:19]\nv_mfma_f32_16x16x32_bf16 v[24:27], v[228:231], v[56:59], v[24:27]\n" \
"s_waitcnt lgkmcnt(1)\nv_mfma_f32_16x16x32_bf16 v[8:11], a[120:123], v[36:39], v[8:11]\nv_mfma_f32_16x16x32_bf16 v[16:19], a[248:251], v[36:39], v[16:19]\nv_mfma_f32_16x16x32_bf16 v[24:27], v[232:235], v[36:39], v[24:27]\n" \
"s_waitcnt lgkmcnt(0)\nv_mfma_f32_16x16x32_bf16 v[8:11], a[124:127], v[40:43], v[8:11]\nv_mfma_f32_16x16x32_bf16 v[16:19], a[252:255], v[40:43], v[16:19]\nv_mfma_f32_16x16x32_bf16 v[24:27], v[236:239], v[40:43], v[24:27]\n" \
"buffer_store_dword v80, v3, s[8:11], 0 offen offset:0 sc1\ns_nop 7\nv_fma_f32 v36, v32, v240, v8\nv_fma_f32 v48, v32, v244, v16\n" \
"v_fma_f32 v60, v32, v248, v24\nv_fma_f32 v37, v32, v241, v9\nv_fma_f32 v49, v32, v245, v17\nv_fma_f32 v61, v32, v249, v25\n" \
"v_fma_f32 v38, v32, v242, v10\nv_fma_f32 v50, v32, v246, v18\nv_fma_f32 v62, v32, v250, v26\nv_fma_f32 v39, v32, v243, v11\n" \
"v_fma_f32 v51, v32, v247, v19\nv_fma_f32 v63, v32, v251, v27\nv_fma_f32 v40, v33, v240, v8\nv_fma_f32 v52, v33, v244, v16\n" \
"v_fma_f32 v64, v33, v248, v24\nv_fma_f32 v41, v33, v241, v9\nv_fma_f32 v53, v33, v245, v17\nv_fma_f32 v65, v33, v249, v25\n" \
"v_fma_f32 v42, v33, v242, v10\nv_fma_f32 v54, v33, v246, v18\nv_fma_f32 v66, v33, v250, v26\nv_fma_f32 v43, v33, v243, v11\n" \
"v_fma_f32 v55, v33, v247, v19\nv_fma_f32 v67, v33, v251, v27\nv_permlane16_swap_b32 v36, v40\nv_permlane16_swap_b32 v48, v52\n" \
"v_permlane16_swap_b32 v60, v64\nv_permlane16_swap_b32 v37, v41\nv_permlane16_swap_b32 v49, v53\nv_permlane16_swap_b32 v61, v65\n" \
"v_permlane16_swap_b32 v38, v42\nv_permlane16_swap_b32 v50, v54\nv_permlane16_swap_b32 v62, v66\nv_permlane16_swap_b32 v39, v43\n" \
"v_permlane16_swap_b32 v51, v55\nv_permlane16_swap_b32 v63, v67\nv_mul_f32 v44, v36, v36\nv_mul_f32 v56, v48, v48\n" \
"v_mul_f32 v68, v60, v60\nv_mul_f32 v45, v37, v37\nv_mul_f32 v57, v49, v49\nv_mul_f32 v69, v61, v61\n" \
"v_mul_f32 v46, v38, v38\nv_mul_f32 v58, v50, v50\nv_mul_f32 v70, v62, v62\nv_mul_f32 v47, v39, v39\n" \
"v_mul_f32 v59, v51, v51\nv_mul_f32 v71, v63, v63\nv_fma_f32 v44, v44, v253, v252\nv_fma_f32 v56, v56, v253, v252\n" \
"v_fma_f32 v68, v68, v253, v252\nv_fma_f32 v45, v45, v253, v252\nv_fma_f32 v57, v57, v253, v252\nv_fma_f32 v69, v69, v253, v252\n" \
"v_fma_f32 v46, v46, v253, v252\nv_fma_f32 v58, v58, v253, v252\nv_fma_f32 v70, v70, v253, v252\nv_fma_f32 v47, v47, v253, v252\n" \
"v_fma_f32 v59, v59, v253, v252\nv_fma_f32 v71, v71, v253, v252\nv_mul_f32 v44, v44, v36\nv_mul_f32 v56, v56, v48\n" \
"v_mul_f32 v68, v68, v60\nv_mul_f32 v45, v45, v37\nv_mul_f32 v57, v57, v49\nv_mul_f32 v69, v69, v61\n" \
"v_mul_f32 v46, v46, v38\nv_mul_f32 v58, v58, v50\nv_mul_f32 v70, v70, v62\nv_mul_f32 v47, v47, v39\n" \
"v_mul_f32 v59, v59, v51\nv_mul_f32 v71, v71, v63\nv_mul_f32 v40, v36, v40\nv_mul_f32 v52, v48, v52\n" \
"v_mul_f32 v64, v60, v64\nv_mul_f32 v41, v37, v41\nv_mul_f32 v53, v49, v53\nv_mul_f32 v65, v61, v65\n" \
"v_mul_f32 v42, v38, v42\nv_mul_f32 v54, v50, v54\nv_mul_f32 v66, v62, v66\nv_mul_f32 v43, v39, v43\n" \
"v_mul_f32 v55, v51, v55\nv_mul_f32 v67, v63, v67\nv_exp_f32 v44, v44\nv_exp_f32 v56, v56\n" \
"v_exp_f32 v68, v68\nv_exp_f32 v45, v45\nv_exp_f32 v57, v57\nv_exp_f32 v69, v69\n" \
"v_exp_f32 v46, v46\nv_exp_f32 v58, v58\nv_exp_f32 v70, v70\nv_exp_f32 v47, v47\n" \
"v_exp_f32 v59, v59\nv_exp_f32 v71, v71\nv_fma_f32 v44, v44, v254, v254\nv_fma_f32 v56, v56, v254, v254\n" \
"v_fma_f32 v68, v68, v254, v254\nv_fma_f32 v45, v45, v254, v254\nv_fma_f32 v57, v57, v254, v254\nv_fma_f32 v69, v69, v254, v254\n" \
"v_fma_f32 v46, v46, v254, v254\nv_fma_f32 v58, v58, v254, v254\nv_fma_f32 v70, v70, v254, v254\nv_fma_f32 v47, v47, v254, v254\n" \
"v_fma_f32 v59, v59, v254, v254\nv_fma_f32 v71, v71, v254, v254\nv_rcp_f32 v44, v44\nv_rcp_f32 v56, v56\n" \
"v_rcp_f32 v68, v68\nv_rcp_f32 v45, v45\nv_rcp_f32 v57, v57\nv_rcp_f32 v69, v69\n" \
"v_rcp_f32 v46, v46\nv_rcp_f32 v58, v58\nv_rcp_f32 v70, v70\nv_rcp_f32 v47, v47\n" \
"v_rcp_f32 v59, v59\nv_rcp_f32 v71, v71\nv_mul_f32 v40, v40, v44\nv_mul_f32 v52, v52, v56\n" \
"v_mul_f32 v64, v64, v68\nv_mul_f32 v41, v41, v45\nv_mul_f32 v53, v53, v57\nv_mul_f32 v65, v65, v69\n" \
"v_mul_f32 v42, v42, v46\nv_mul_f32 v54, v54, v58\nv_mul_f32 v66, v66, v70\nv_mul_f32 v43, v43, v47\n" \
"v_mul_f32 v55, v55, v59\nv_mul_f32 v67, v67, v71\nv_cvt_pk_bf16_f32 v40, v40, v41\nv_cvt_pk_bf16_f32 v52, v52, v53\n" \
"v_cvt_pk_bf16_f32 v64, v64, v65\nv_cvt_pk_bf16_f32 v41, v42, v43\nv_cvt_pk_bf16_f32 v53, v54, v55\nv_cvt_pk_bf16_f32 v65, v66, v67\n" \
"v_min_u32 v40, -2, v40\nv_min_u32 v52, -2, v52\nv_min_u32 v64, -2, v64\nbuffer_store_dwordx2 v[40:41], v2, s[8:11], s16 offen offset:0 sc1\n" \
"buffer_store_dwordx2 v[52:53], v2, s[8:11], s17 offen offset:256 sc1\nbuffer_store_dwordx2 v[64:65], v2, s[8:11], s17 offen offset:512 sc1\ns_cmp_ge_u32 s20, s28\ns_cbranch_scc1 9f\n" \
"s_lshr_b32 s94, s20, 1\nv_readlane_b32 s94, v6, s94\ns_and_b32 s95, s20, 1\ns_lshl_b32 s95, s95, 3\n" \
"s_lshr_b32 s94, s94, s95\ns_and_b32 s94, s94, 0x7f\ns_lshl_b32 s95, s94, 13\ns_barrier\n" \
"s_branch 7b\n9:\ns_waitcnt vmcnt(0)\ns_endpgm\n"
struct WMA { static constexpr int NT = 4, NP = 16, RB = 0, LB = 0, CBI = 0;
             static constexpr int kind(int j, int t) { return (A_LDS >> (16 * j + t) & 1) ? 1 : (A_VAR >> (16 * j + t) & 1) ? 2 : 0; } };
namespace la { constexpr int L_Q = 24576, L_K = 57344, L_QM = 65536, L_KM = 66048, L_SP = 69632, L_W = 73728; }
#ifndef LEAD_B
#define LEAD_B 132
#endif
DEVI float dror8(float x) { return __uint_as_float(__builtin_amdgcn_update_dpp(0u, __float_as_uint(x), 0x128, 0xF, 0xF, true)); }
DEVI float scm(float a, float m) { float r; asm("v_fma_f32 %0, %1, %2, -%3" : "=v"(r) : "v"(a), "s"(SC_L2E), "v"(m)); return r; }
DEVI float ex2(float x) { return __builtin_amdgcn_exp2f(x); }
DEVI float max3(float a, float b, float c) { float r; asm("v_max3_f32 %0, %1, %2, %3" : "=v"(r) : "v"(a), "v"(b), "v"(c)); return r; }
constexpr u64 tri(int i) { u64 m = 0; for (int l = 0; l < 64; ++l) if (4 * (l >> 4) + i <= (l & 15)) m |= 1ull << l; return m; }
DEVI unsigned vbfe(unsigned a, int o, int n) { unsigned r; asm volatile("v_bfe_u32 %0, %1, %2, %3" : "=v"(r) : "v"(a), "i"(o), "i"(n)); return r; }
DEVI unsigned vadds(unsigned a, unsigned b) { unsigned r; asm volatile("v_add_u32 %0, %1, %2" : "=v"(r) : "s"(b), "v"(a)); return r; }
DEVI float vexp(float a) { float r; asm volatile("v_exp_f32 %0, %1" : "=v"(r) : "v"(a)); return r; }
DEVI unsigned vors(unsigned a, unsigned b) { unsigned r; asm volatile("v_or_b32 %0, %1, %2" : "=v"(r) : "s"(b), "v"(a)); return r; }
DEVI float faddv(float a, float b) { float r; asm volatile("v_add_f32 %0, %1, %2" : "=v"(r) : "v"(a), "v"(b)); return r; }
DEVI unsigned packv(float a, float b) { unsigned r; asm volatile("v_cvt_pk_bf16_f32 %0, %1, %2" : "=v"(r) : "v"(a), "v"(b)); return r; }
DEVI void setm0q(unsigned v) { asm volatile("s_mov_b32 m0, %0" :: "s"(v) : "memory"); }
template <int OFF> DEVI void st16o(v4u v, rsrc_t r, unsigned vo, unsigned so) {
    asm volatile("buffer_store_dwordx4 %0, %1, %2, %3 offen offset:%4 sc1" :: "v"(v), "v"(vo), "s"(r), "s"(so), "n"(OFF) : "memory"); }
DEVI unsigned vlsa(unsigned a, int sh, unsigned c) { unsigned r; asm volatile("v_lshl_add_u32 %0, %1, %2, %3" : "=v"(r) : "v"(a), "i"(sh), "v"(c)); return r; }
DEVI unsigned vlss(unsigned a, int sh, unsigned c) { unsigned r; asm volatile("v_lshl_add_u32 %0, %1, %2, %3" : "=v"(r) : "v"(a), "i"(sh), "s"(c)); return r; }
template <int OFF> DEVI void sts4h(unsigned p, unsigned u0, unsigned u1) {
    asm volatile("ds_write_b16 %0, %1 offset:%3\n\tds_write_b16_d16_hi %0, %1 offset:%3+16\n\tds_write_b16 %0, %2 offset:%3+8\n\tds_write_b16_d16_hi %0, %2 offset:%3+24" :: "v"(p), "v"(u0), "v"(u1), "n"(OFF) : "memory");
}
DEVI void zero4v(f32x4& d) { f32x2 a, b; asm volatile("v_mov_b64 %0, 0\n\tv_mov_b64 %1, 0" : "=v"(a), "=v"(b)); d = __builtin_shufflevector(a, b, 0, 1, 2, 3); }
DEVI float sel0(float a, u64 m) { float r; asm("v_cndmask_b32 %0, %1, 0, %2" : "=v"(r) : "v"(a), "s"(m)); return r; }
DEVI float vmul(float a, float b) { float r; asm("v_mul_f32 %0, %1, %2" : "=v"(r) : "v"(a), "v"(b)); return r; }
DEVI void swap16(float& a, float& b) { unsigned x = __float_as_uint(a), y = __float_as_uint(b); swap16u(x, y); a = __uint_as_float(x); b = __uint_as_float(y); }
#ifndef PACE
#define PACE 7
#endif
DEVI void pace(int l) { asm volatile("s_cmp_eq_u32 %0, 0\n\ts_cbranch_scc1 1f\n\ts_sleep %1\n1:" :: "s"(l), "n"(PACE) : "scc"); }
DEVI void role_A(const Args& a, int w, int l, int h, int rep, int id2) {
    asm volatile("; MARK role_A"); __builtin_amdgcn_s_waitcnt(0);
    using namespace la;
    const int lane0 = lane_id();
    const rsrc_t rs = mkrs(a.ws, WS_NEED);
    const unsigned lane16 = lane0 * 16, wl = L_W + w * (11 * 2048) + lane16;
    v8u Wc[3];
    unsigned nb, fx, ok;
    const unsigned own = (w >> 1) * 32768 + (w & 1) * 4096;
    {
        const int lane = lane0;
        const unsigned ta = (lane >> 4) * 2048 + w * 256 + (lane & 15) * 16;
        const rsrc_t r1 = mkrs(a.Wq, WSZ(8u << 20)), r2 = mkrs(a.Wk, WSZ(8u << 20));
        const unsigned v1 = (8 * w + ((lane >> 2) & 3)) * 8192 + h * 256 + (lane >> 4) * 64 + (lane & 3) * 16;
        const unsigned lw = opqs(w * 2048);
        load_blocks<32, 4, 4, true, 2>([&](auto B, auto S) {
            if constexpr (B.v == 32) ok = first_there(fx);
            if constexpr (B.v >= 32) dma4(opqs(own + S.v * 8192), mkrs(a.ws, ok), lane16, opqs(O_XN + l * XB + own + S.v * 8192));
            else { pace(l); sfor<0, 2>([&](auto I) { dma_blk(lw + S.v * 8192 + I.v * 32768, B.v < 16 ? r1 : r2, v1, opqs((2 * (B.v & 15) + I.v) * 32 * 8192), 2048); }); } },
                                       [&](auto B, auto S) { sfor<0, 4>([&](auto T) { constexpr int o = S.v * 8192 + (T.v >> 1) * 32768 + (T.v & 1) * 8;
                                                                                      put_frag<WMA, (B.v >> 4) * 2 + (T.v & 1), 2 * (B.v & 15) + (T.v >> 1), o, o + 1024>(ta, wl, Wc); });
                                                             if constexpr (B.v == 24) { nb = side(rs, l); first_look(fx, rs, nb); } });
        TSW(127, 2);
    }
    const unsigned spm = na(nb, N_PM + h * 512), spmm = na(nb, N_PMM + h * 128), smem = na(nb, N_MEM + rep * 16384 + w * 4096);
    const int lane = lane_id(), c = lane & 15, g = lane >> 4;
    const unsigned wlo = (unsigned)(uintptr_t)a.ws, whi = (unsigned)((uintptr_t)a.ws >> 32), hi = -(w >> 1);
    const v8u pv{lane16, wl, (lane & 2) ? 0xEEEEu : 0x4444u, (unsigned)(L_QM + w * 1024 + (c >> 2) * 128 + g * 32 + (c & 3) * 4), (unsigned)(L_QM + g * 128 + (c & 7) * 16),
                 w == 2 ? ((g >> 1) * 16 + crow(c & 7, c >> 3)) * 16 + (g & 1) * 8 : w < 2 ? lane * 8 : lane < NC ? DV(lane * 512) : 1u << 30,
                 w < 2 ? c * 16 + g * 4 : c < 8 ? crow(c, g & 1) * 8 + (g >> 1) * 4 : 1u << 30, 0u};
    const v16u s0{wlo, whi, RSZ, 0x20000u, wlo, whi, 0u, 0x20000u, (unsigned)tri(0) | hi, (unsigned)(tri(0) >> 32) | hi, (unsigned)tri(1) | hi, (unsigned)(tri(1) >> 32) | hi,
                  (unsigned)tri(2) | hi, (unsigned)(tri(2) >> 32) | hi, (unsigned)tri(3) | hi, (unsigned)(tri(3) >> 32) | hi};
    const v16u s1{0xFF00FF00u, 0xFF00FF00u, 0u, ~0u, hi, hi, 0u, O_XN + l * XB + own, own, smem, w == 2 ? spm : OOB, nb + DV(N_PTX + h * 2048), w == 1 ? 0u : OOB, w == 0 ? 0u : OOB,
                  w < 2 ? w * 256 : w == 2 ? spmm - 1536 : OOB, ~hi};
    const v16u s2{w < 2 ? w * 1024 : w == 2 ? spm : spmm, w < 2 ? 4u : 3u, w < 2 ? 0u : w == 2 ? 0x200u : 0x380u, w ? 0u : __float_as_uint(-3e38f), (unsigned)(L_Q + w * 1024), (unsigned)(L_Q + (w & 1) * 4096),
                  __float_as_uint(SC_L2E), __float_as_uint(-1e30f), na(nb, N_FX), w == 3 ? na(nb, N_PA + ((h >> 3) * 32 + 8 + (h & 7)) * 4) : OOB, (unsigned)(L_KM + (w >> 1) * 1024 + (w & 1) * 256),
                  0u, 0u, (unsigned)(L_SP + w * 256), O_PROF + (blockIdx.x * 4 + w) * 8192, na(nb, N_WM + h * 16384 + w * 4096)};
    asm volatile(A_LOOP :: "{v[0:7]}"(Wc[0]), "{v[8:15]}"(Wc[1]), "{v[16:23]}"(Wc[2]), "{v[24:31]}"(pv), "{s[8:23]}"(s0), "{s[24:39]}"(s1), "{s[40:55]}"(s2),
                 "{s58}"(ok), "{s[4:7]}"(again(id2, nb, w)) : "memory", "s99", "vcc");
}
constexpr int bkind(int n) { return (B_LDS >> n & 1) ? 1 : (B_VAR >> n & 1) ? 2 : 0; }
struct WMV { static constexpr int NT = 2, NP = 16, RB = 0, LB = 0, CBI = 0; static constexpr int kind(int j, int t) { return bkind(16 * j + t); } };
struct WMO { static constexpr int NT = 16, NP = 2, LB = __builtin_popcount((unsigned)B_LDS), CBI = __builtin_popcount((unsigned)B_VAR), RB = 8 * (32 - LB - CBI);
             static constexpr int kind(int j, int t) { return bkind(32 + 2 * j + t); } };
#ifndef B_LV
#define B_LV 24576
#endif
namespace lb { constexpr int L_V = B_LV, L_VM = B_LV + 4096, L_W = 65536; }
DEVI void role_B(const Args& a, int w, int l, int h, int rep, int id2) {
    asm volatile("; MARK role_B"); __builtin_amdgcn_s_waitcnt(0);
    using namespace lb;
    const int lane0 = lane_id();
    const rsrc_t rs = mkrs(a.ws, WS_NEED);
    const unsigned lane16 = lane0 * 16, wl = L_W + w * (12 * 2048) + lane16;
    v8u Wc[2];
    unsigned nb, fx, ok;
    const unsigned own = (w >> 1) * 32768 + (w & 1) * 4096;
    {
        const int lane = lane0;
        const unsigned ta = (lane >> 4) * 2048 + w * 256 + (lane & 15) * 16, tb = (lane >> 5) * 32768 + w * 2048 + (lane & 16) * 64 + (lane & 15) * 16;
        const rsrc_t r1 = mkrs(a.Wv, WSZ(8u << 20)), r2 = mkrs(a.Wo, WSZ(8u << 20));
        const unsigned v1 = (8 * w + ((lane >> 2) & 3)) * 8192 + h * 256 + (lane >> 4) * 64 + (lane & 3) * 16, v2 = (h * 128 + ((lane >> 2) & 7)) * 2048 + w * 512 + (lane >> 5) * 64 + (lane & 3) * 16;
        const unsigned lw = opqs(w * 2048);
        load_blocks<32, 4, 4, true, 3>([&](auto B, auto S) {
            constexpr int b = B.v & 15;
            if constexpr (B.v == 32) ok = first_there(fx);
            if constexpr (B.v < 32) pace(l);
            if constexpr (B.v >= 32) dma4(opqs(own + S.v * 8192), mkrs(a.ws, ok), lane16, opqs(O_XN + l * XB + own + S.v * 8192));
            else sfor<0, 2>([&](auto I) { if constexpr (B.v < 16) dma_blk(lw + S.v * 8192 + I.v * 32768, r1, v1, opqs((2 * b + I.v) * 32 * 8192), 2048);
                                     else dma_blk(lw + S.v * 8192 + I.v * 32768, r2, v2, opqs(((b & 3) * 32 + I.v * 16) * 2048 + (b >> 2) * 128), 1024); });
        }, [&](auto B, auto S) {
            constexpr int b = B.v & 15;
            sfor<0, 4>([&](auto T) { constexpr int o = S.v * 8192 + (T.v >> 1) * (B.v < 16 ? 32768 : 512) + (T.v & 1) * 8;
                                     if constexpr (B.v < 16) put_frag<WMV, T.v & 1, 2 * b + (T.v >> 1), o, o + 1024>(ta, wl, Wc); else put_frag<WMO, (b & 12) + T.v, b & 3, o, o + 256>(tb, wl, Wc); });
            if constexpr (B.v == 24) { nb = side(rs, l); first_look(fx, rs, nb); }
        });
        TSW(127, 2);
    }
    const int lane = lane_id(), c = lane & 15, g = lane >> 4;
    const unsigned wlo = (unsigned)(uintptr_t)a.ws, whi = (unsigned)((uintptr_t)a.ws >> 32);
    const unsigned prow = L_VM + w * 512 + ((c >> 2) & 1) * 256 + g * 64 + (c >> 3) * 32 + (c & 1) * 8 + ((c >> 1) & 1) * 4;
    const v4u pv{lane16, wl, ((lane & 2) ? 0xEEEEu : 0x4444u) | prow << 16, (unsigned)(L_VM + (g >> 1) * 512 + (lane & 3) * 64 + (g & 1) * 32 + ((lane >> 2) & 3) * 8)};
    const v16u s0{wlo, whi, RSZ, 0x20000u, wlo, whi, 0u, 0x20000u, OOB, OOB, OOB, OOB, ~1u, O_XN + l * XB + own, own, na(nb, N_MEM + rep * 16384 + w * 4096)};
    const v8u s1{na(nb, N_WM + h * 16384 + w * 4096), na(nb, N_U + h * 65536 + w * 16384), na(nb, N_FX), w == 3 ? na(nb, N_PA + ((h >> 3) * 32 + (h & 7)) * 4) : OOB,
                 (unsigned)(L_V + w * 1024), OOB, 0u, O_PROF + (blockIdx.x * 4 + w) * 8192 + 64};
    asm volatile(B_LOOP :: "{v[0:7]}"(Wc[0]), "{v[8:15]}"(Wc[1]), "{v[16:19]}"(pv), "{v20}"(fx), "{s[8:23]}"(s0), "{s[24:31]}"(s1), "{s32}"(1u), "{s[4:7]}"(again(id2, nb, w)) : "memory", "s99", "vcc");
}
template <class NB> DEVI void put_tile(v4u (&u)[8], rsrc_t rs, int tile, int w, int lane, NB&& nbf) {
    const unsigned vo = tile * 32768 + w * 8192 + lane * 16, red = (lane & 15) * 16;
    float s = 0.f;
#pragma unroll
    for (int f = 0; f < 8; ++f) {
#pragma unroll
        for (int i = 0; i < 4; ++i) s += bflo(u[f][i]) + bfhi(u[f][i]);
    }
    s = red32(red16(s));
    BAR;
    if (lane < 16) *LDSP(float, red + w * 4) = s;
    WL0; BAR;
    const f32x4 sa = __builtin_bit_cast(f32x4, ldsq(red, 0));
    const float mean = ((sa[0] + sa[1]) + (sa[2] + sa[3])) * (1.0f / 1024.0f);
    float q = 0.f;
#pragma unroll
    for (int f = 0; f < 8; ++f) {
        asm volatile("" : "+v"(u[f]));
#pragma unroll
        for (int i = 0; i < 4; ++i) { const float d0 = bflo(u[f][i]) - mean, d1 = bfhi(u[f][i]) - mean; q = fmaf(d0, d0, fmaf(d1, d1, q)); }
    }
    q = red32(red16(q));
    if (lane < 16) *LDSP(float, red + 256 + w * 4) = q;
    WL0; BAR;
    const f32x4 qa = __builtin_bit_cast(f32x4, ldsq(red, 256));
    const float mb = rbf(mean), inv = 1.0f / rbf(sqrtf(rbf(rbf(((qa[0] + qa[1]) + (qa[2] + qa[3])) * (1.0f / 1024.0f)) + 1e-5f)));
#pragma unroll
    for (int f = 0; f < 8; ++f) {
        v4u o;
        asm volatile("" : "+v"(u[f]));
#pragma unroll
        for (int i = 0; i < 4; ++i) { const unsigned d = pack2(bflo(u[f][i]) - mb, bfhi(u[f][i]) - mb); o[i] = pack2(bflo(d) * inv, bfhi(d) * inv); }
        st16(o, rs, O_XN + vo + f * 1024);
    }
    const unsigned nb = nbf();
    WV0;
    if (lane == 0) st4(0u, rs, na(nb, N_FX) + tile * 16 + w * 4, 0);
}
#ifndef SXL
#define SXL 1
#endif
#ifndef HLD
#define HLD 158
#endif
#ifndef SXH
#define SXH 30
#endif
#ifndef UIN
#define UIN 8
#endif
namespace lr { constexpr int PT1 = NV + 48, PT0 = NV + 80, PTM = NV + 96, VPM = NV, VMM = NV + 16, VW = NV + 28, VX = NV + 44, L_M = 1024, MD = 288, MW = 1536, L_T = 8192, VE = NV + 112; }
template <int R, int N, int OFF> DEVI void lda(rsrc_t rs, unsigned vo, unsigned so) {
    if constexpr (N == 4) asm volatile("buffer_load_dwordx4 a[%3:%4], %0, %1, %2 offen offset:%5 sc1" :: "v"(vo), "s"(rs), "s"(so), "n"(R), "n"(R + 3), "n"(OFF) : "memory");
    else asm volatile("buffer_load_dwordx2 a[%3:%4], %0, %1, %2 offen offset:%5 sc1" :: "v"(vo), "s"(rs), "s"(so), "n"(R), "n"(R + 1), "n"(OFF) : "memory");
}
template <int R, int N, int OFF> DEVI void ldv(rsrc_t rs, unsigned vo, unsigned so) {
    if constexpr (N == 4) asm volatile("buffer_load_dwordx4 v[%3:%4], %0, %1, %2 offen offset:%5 sc1" :: "v"(vo), "s"(rs), "s"(so), "n"(R), "n"(R + 3), "n"(OFF) : "memory");
    else asm volatile("buffer_load_dwordx2 v[%3:%4], %0, %1, %2 offen offset:%5 sc1" :: "v"(vo), "s"(rs), "s"(so), "n"(R), "n"(R + 1), "n"(OFF) : "memory");
}
template <int R> DEVI unsigned rdv() { unsigned t; asm volatile("v_mov_b32 %0, v[%1]" : "=v"(t) : "n"(R)); return t; }
template <int RA, int RB, bool Z, bool AV> DEVI void mfh(f32x4& d) {
    if constexpr (AV) { if constexpr (Z) asm volatile(M16 "%0, v[%1:%2], v[%3:%4], 0" : "=v"(d) : "n"(RA), "n"(RA + 3), "n"(RB), "n"(RB + 3));
                        else asm volatile(M16 "%0, v[%1:%2], v[%3:%4], %0" : "+v"(d) : "n"(RA), "n"(RA + 3), "n"(RB), "n"(RB + 3)); }
    else { if constexpr (Z) asm volatile(M16 "%0, a[%1:%2], v[%3:%4], 0" : "=v"(d) : "n"(RA), "n"(RA + 3), "n"(RB), "n"(RB + 3));
           else asm volatile(M16 "%0, a[%1:%2], v[%3:%4], %0" : "+v"(d) : "n"(RA), "n"(RA + 3), "n"(RB), "n"(RB + 3)); }
}
template <int RA, int RB, bool Z> DEVI void mfh16(f32x4& d) {
    if constexpr (Z) asm volatile("v_mfma_f32_16x16x16_bf16 %0, a[%1:%2], v[%3:%4], 0" : "=v"(d) : "n"(RA), "n"(RA + 1), "n"(RB), "n"(RB + 1));
    else asm volatile("v_mfma_f32_16x16x16_bf16 %0, a[%1:%2], v[%3:%4], %0" : "+v"(d) : "n"(RA), "n"(RA + 1), "n"(RB), "n"(RB + 1));
}
template <int R> DEVI u64 ltv(int need) { u64 m; asm volatile("v_cmp_lt_i32 %0, v[%2], %1" : "=s"(m) : "v"(need), "n"(R)); return m; }
template <int R> DEVI void ldv4(rsrc_t rs, unsigned vo, unsigned so) { asm volatile("buffer_load_dwordx4 v[%3:%4], %0, %1, %2 offen sc1" :: "v"(vo), "s"(rs), "s"(so), "n"(R), "n"(R + 3) : "memory"); }
#define SM_(E, A, B) "v_smfmac_f32_16x16x64_bf16 v[%[e]+" #E ":%[e]+" #E "+3], " A ", " B ", %[si]\n\t"
#define TG_(M, R) "v_bitop3_b32 v[%[e]+" #M "], v[%[e]+" #M "], v[" R "], %[tg] bitop3:0xf6\n\t"
#define PP_(P, N) "s_waitcnt vmcnt(" #N ")\n\t" SM_(0, "v[%[p]+4*" #P ":%[p]+4*" #P "+3]", "a[%[u]+16*" #P ":%[u]+16*" #P "+7]") TG_(8, "%[p]+4*" #P)
#define PQ_(P) SM_(4, "v[%[p]+4*" #P ":%[p]+4*" #P "+3]", "a[%[u]+16*" #P "+8:%[u]+16*" #P "+15]") TG_(9, "%[p]+4*" #P "+2")
#define ZE_ "v_mov_b64 v[%[e]:%[e]+1], 0\n\tv_mov_b64 v[%[e]+2:%[e]+3], 0\n\tv_mov_b64 v[%[e]+4:%[e]+5], 0\n\tv_mov_b64 v[%[e]+6:%[e]+7], 0\n\tv_mov_b64 v[%[e]+8:%[e]+9], 0\n\t"
template <int U0> DEVI unsigned chain(rsrc_t rs, int tgo, unsigned sp, unsigned sm, unsigned sw0, unsigned sw1, unsigned tg, unsigned spt, unsigned lane16, unsigned lane8, unsigned vmm, unsigned vw, int sidx,
                                      unsigned lred, unsigned lrr, unsigned vpub, v2u sxv, unsigned sxo) {
    using namespace lr;
    unsigned n, d;
    asm volatile(ZE_ "s_mov_b32 %[n], 0\n\ts_memrealtime s[98:99]\n\ts_add_u32 %[d], %[pt], 0x800\n\ts_waitcnt lgkmcnt(0)\n\ts_sub_i32 s99, %[t], s98\n\ts_lshl_b32 s99, s99, 8\n\ts_sub_u32 s99, s99, 1\n\t"
        "s_cmp_lt_u32 s99, 0x10000\n\ts_cbranch_scc1 2f\n\ts_branch 3f\n"
        "1:\n\ts_add_u32 %[n], %[n], 1\n\ts_cmp_lt_u32 %[n], %[cap]\n\ts_cbranch_scc1 4f\n\ts_endpgm\n4:\n\t" ZE_ "v_bfrev_b32 %[so], 1\n\ts_branch 3f\n"
        "2:\n\ts_memrealtime s[98:99]\n\ts_waitcnt lgkmcnt(0)\n\ts_sub_i32 s99, %[t], s98\n\ts_lshl_b32 s99, s99, 8\n\ts_cmp_gt_i32 s99, 0\n\ts_cbranch_scc1 2b\n"
        "3:\n\t"
        "buffer_load_dwordx4 v[%[p]:%[p]+3], %[l16], %[rs], %[sp] offen sc1\n\t"
        "buffer_load_dwordx4 v[%[p]+4:%[p]+7], %[l16], %[rs], %[sp] offen offset:1024 sc1\n\t"
        "buffer_load_dwordx4 v[%[p]+8:%[p]+11], %[l16], %[rs], %[sp] offen offset:2048 sc1\n\t"
        "buffer_load_dwordx4 v[%[p]+12:%[p]+15], %[l16], %[rs], %[sp] offen offset:3072 sc1\n\t"
        "buffer_load_dwordx2 v[%[p]+16:%[p]+17], %[vm], %[rs], %[sm] offen sc1\n\t"
        "buffer_load_dwordx2 v[%[p]+18:%[p]+19], %[vm], %[rs], %[sm] offen offset:128 sc1\n\t"
        "buffer_load_dwordx4 v[%[w]:%[w]+3], %[vw], %[rs], %[sw0] offen sc1\n\t"
        "buffer_load_dwordx4 v[%[w]+4:%[w]+7], %[vw], %[rs], %[sw1] offen sc1\n\t"
        "buffer_load_dwordx4 v[%[w]+8:%[w]+11], %[vw], %[rs], %[sw0] offen offset:256 sc1\n\t"
        "buffer_load_dwordx4 v[%[w]+12:%[w]+15], %[vw], %[rs], %[sw1] offen offset:256 sc1\n\t"
        PP_(0, 9) "buffer_store_dwordx2 %[sx], %[so], %[rs], 0 offen sc1\n\t" PQ_(0) PP_(1, 9) PQ_(1) PP_(2, 8) PQ_(2) PP_(3, 7) PQ_(3)
        "s_waitcnt vmcnt(5)\n\t" TG_(8, "%[p]+16") TG_(9, "%[p]+17") TG_(8, "%[p]+18") TG_(9, "%[p]+19")
        "s_waitcnt vmcnt(3)\n\t" SM_(0, "v[%[p]+16:%[p]+19]", "v[%[w]:%[w]+7]") TG_(8, "%[w]") TG_(9, "%[w]+4")
        "s_waitcnt vmcnt(1)\n\t" SM_(4, "v[%[p]+16:%[p]+19]", "v[%[w]+8:%[w]+15]") TG_(8, "%[w]+8") TG_(9, "%[w]+12")
        "v_bitop3_b32 v[%[e]+8], v[%[e]+8], v[%[e]+9], 1 bitop3:0xa8\n\tv_cmp_ne_u32 vcc, 0, v[%[e]+8]\n\ts_nop 0\n\t"
        "v_add_f32 v[%[e]], v[%[e]], v[%[e]+2]\n\tv_add_f32 v[%[e]+1], v[%[e]+1], v[%[e]+3]\n\tds_write2_b32 %[lw], v[%[e]], v[%[e]+1] offset1:2\n\ts_nop 0\n\t"
        "v_add_f32 v[%[e]+4], v[%[e]+4], v[%[e]+6]\n\tv_add_f32 v[%[e]+5], v[%[e]+5], v[%[e]+7]\n\tds_write2_b32 %[lw], v[%[e]+4], v[%[e]+5] offset0:%[md] offset1:%[md]+2\n\t"
        "s_cbranch_vccnz 1b\n\t"
        "buffer_load_dwordx4 v[%[t1]:%[t1]+3], %[l16], %[rs], %[pt] offen sc1\n\tbuffer_load_dwordx2 v[%[t0]:%[t0]+1], %[l8], %[rs], %[pt] offen offset:1024 sc1\n\t"
        "buffer_load_dwordx4 v[%[t1]+4:%[t1]+7], %[l16], %[rs], %[d] offen sc1\n\tbuffer_load_dwordx2 v[%[t0]+2:%[t0]+3], %[l8], %[rs], %[d] offen offset:1024 sc1\n\t"
        "s_waitcnt lgkmcnt(0)\n\ts_barrier\n\t"
        "ds_read2st64_b64 v[%[e]:%[e]+3], %[lr] offset1:3\n\tds_read2st64_b64 v[%[e]+4:%[e]+7], %[lr] offset0:6 offset1:9\n\ts_waitcnt lgkmcnt(1)\n\t"
        "v_add_f32 v[%[e]], v[%[e]], v[%[e]+2]\n\tv_add_f32 v[%[e]+1], v[%[e]+1], v[%[e]+3]\n\ts_waitcnt lgkmcnt(0)\n\t"
        "v_add_f32 v[%[e]+4], v[%[e]+4], v[%[e]+6]\n\tv_add_f32 v[%[e]+5], v[%[e]+5], v[%[e]+7]\n\tv_add_f32 v[%[e]], v[%[e]], v[%[e]+4]\n\tv_add_f32 v[%[e]+1], v[%[e]+1], v[%[e]+5]\n\t"
        "v_cvt_pk_bf16_f32 v[%[e]+2], v[%[e]], v[%[e]+1]\n\tv_min_u32 v[%[e]], -2, v[%[e]+2]\n\tv_min_u32 v[%[e]+1], -2, v[%[e]+2]\n\ts_nop 1\n\t"
        "v_permlane16_swap_b32 v[%[e]], v[%[e]+1]\n\tv_mov_b64 v[%[e]+2:%[e]+3], v[%[e]:%[e]+1]\n\ts_nop 1\n\t"
        "v_permlane32_swap_b32 v[%[e]], v[%[e]+2]\n\tv_permlane32_swap_b32 v[%[e]+1], v[%[e]+3]\n\t"
        "buffer_store_dwordx4 v[%[e]:%[e]+3], %[vp], %[rs], 0 offen sc1"
        : [n] "=&s"(n), [d] "=&s"(d), [so] "+v"(sxo)
        : [rs] "s"(rs), [t] "s"(tgo), [sp] "s"(sp), [sm] "s"(sm), [sw0] "s"(sw0), [sw1] "s"(sw1), [tg] "s"(tg), [pt] "s"(spt), [l16] "v"(lane16), [l8] "v"(lane8), [vm] "v"(vmm), [vw] "v"(vw), [si] "v"(sidx),
          [lw] "v"(lred), [lr] "v"(lrr), [vp] "v"(vpub), [sx] "v"(sxv), [e] "n"(VE), [p] "n"(VPM), [w] "n"(VW), [u] "n"(U0), [t1] "n"(PT1), [t0] "n"(PT0), [md] "n"(MD / 4), [cap] "n"(SPIN_CAP)
        : "memory", "scc", "vcc", "s98", "s99");
    return n;
}
template <int R> DEVI void tagd(unsigned& m, unsigned t) { asm volatile("v_bitop3_b32 %0, %0, v[%2], %1 bitop3:0xf6" : "+v"(m) : "s"(t), "n"(R)); }
#ifndef DLY_R
#define DLY_R 1200
#endif
#ifndef DLY_X
#define DLY_X 1300
#endif
constexpr unsigned O_ARM = O_N + DV(N_PA) + 96;
DEVI void arm_last(const Args& a, int q, int n, int t0) {
    const rsrc_t rs = mkrs(a.ws, WS_NEED);
    hold(t0, DLY_V); side(rs, 0); hold(t0, 4000); hold(t0, 7000);
#pragma nounroll
    for (int k = q; k < N2H * (64 + 224 + 64 * NKF); k += n) {
        st16(v4u{SENT, SENT, SENT, SENT}, rs, (k < N2H * 64 ? O_XM + 2 * XB - N2H * 65536 : k < N2H * 288 ? O_D - N2H * 288 * 1024 : O_N - N2H * (288 + 64 * NKF) * 1024) + k * 1024 + lane_id() * 16);
        __builtin_amdgcn_s_sleep(8);
    }
    WV0;
    st4(0u, rs, lane_id() == 0 ? O_ARM + q * 4 : OOB, 0);
}
DEVI unsigned role_R(const Args& a, int w, int l, int T2, int t0) {
    asm volatile("; MARK role_R"); __builtin_amdgcn_s_waitcnt(0);
    using namespace lr;
    const int lane = lane_id(), c = lane & 15, g = lane >> 4;
    const rsrc_t rs = mkrs(a.ws, WS_NEED);
    const unsigned lane16 = lane * 16;
#pragma nounroll
    for (int k = 0; k < 4 * l; ++k) {
        const int tile = k * 32 + T2;
        if (tile >= LN_NOW) hold(t0, DLY_X);
        v4u u[8];
#pragma unroll
        for (int f = 0; f < 8; ++f) u[f] = *(const v4u*)(a.x + (size_t)(tile * 16 + c) * 1024 + (w * 8 + f) * 32 + g * 8);
        if (tile < 2 * NSEG) put_tile(u, rs, tile, w, lane, [&]() { hold(t0, DLY_V); return side(rs, 0); });
        TSW(127, 1 + k);
    }
    auto arm = [&](unsigned n2, int s) {
        if (s < 8)
#pragma unroll
            for (int k = 0; k < 4; ++k) st16(v4u{SENT, SENT, SENT, SENT}, rs, na(n2, N_WM + s * 524288 + T2 * 16384 + w * 4096) + k * 1024 + lane16);
        st16(v4u{SENT, SENT, SENT, SENT}, rs, w < 2 ? na(n2, N_PTX + s * 65536 + T2 * 2048) + w * 1024 + lane16 : lane >= (w == 2 ? 32 : 8) || s >= 8 ? OOB :
                                              na(n2, w == 2 ? N_PM + s * 16384 + T2 * 512 : N_PMM + s * 8192 + T2 * 128) + lane16);
    };
    hold(t0, DLY_R);
    const unsigned nb = side(rs, l), vmem = c < 8 ? na(nb, N_MEM + T2 * 512) + g * 65536 + w * 128 + c * 16 : OOB;
    auto put_mem = [&](v4u v, int s) { st16(v, rs, vmem + rslot(s) * (NREP * 32768)); };
    {
        const unsigned* m0 = (const unsigned*)(a.mem0 + ((w & 1) * 4 + ((lane >> 2) & 1) * 2 + (lane & 1)) * 1024 + T2 * 32 + (w >> 1) * 16 + ((lane >> 1) & 1) * 2);
        const v4u mv{nosent(m0[0]), m0[2], m0[4], m0[6]};
        put_mem(v4u{SENT, SENT, SENT, SENT}, 1); put_mem(v4u{SENT, SENT, SENT, SENT}, 2);
#pragma nounroll
        for (int k = 0; k < 16; ++k) arm(nb, k);
        WV0; BAR;
        put_mem(mv, 0);
    }
    const int sidx = opq((lane & 2) ? 0xEEEE : 0x4444);
    const unsigned vmm = (8 * w + 2 * g) * 128 + c * 8, vw = (8 * w + g) * 32768 + DV(T2 * 512) + c * 16, vptm = g * 2048 + 1536 + c * 16;
    const unsigned sfl = na(nb, N_PA + (unsigned)((T2 & (NC - 1)) * 4 + w) * 128), su0 = na(nb, N_U + 8 * w * 65536 + T2 * 2048), vfl = sfl + c * 4;
    const unsigned lred = L_M + w * MW + ((c >> 2) & 1) * 2 * MD + (c >> 3) * 128 + g * 32 + ((c >> 1) & 1) * 16 + (c & 1) * 4, lrr = L_M + g * MD + w * 64 + (c & 7) * 8;
    auto ld_u = [&](auto B, int s) {
        const unsigned so = su0 + rslot(s) * (4u << 20);
        sfor<0, 16>([&](auto I) { constexpr int j = I.v >> 1, e = I.v & 1; lda<64 * B.v + 16 * (j >> 1) + 8 * e + 4 * (j & 1), 4, e * 1024>(rs, lane16, so + j * 131072); });
    };
    wait_prog(rs, sfl + (lane & 7) * 4, 0, 0);
    ld_u(IC<0>{}, 0);
    const rsrc_t rxi = l ? rs : mkrs(a.x, XB);
    const unsigned vxi = l ? lane16 : c * 2048 + g * 16, sxi = (w & 1) * 32768 + (l ? O_XIN + XB + T2 * 1024 : T2 * 64);
    v2u sxv{0u, 0u};
    unsigned sxo = OOB;
    int tpub = t0;
    auto step = [&](auto B, int s) {
        constexpr int b = B.v;
        TSD;
        TSR(0); TSK(15);
        if (s == 8) { TSC(126, 0); TSW(126, 1); }
        if (s == 56) { TSC(126, 2); TSW(126, 3); }
        const unsigned spt = na(nb, N_PTX + (unsigned)((s & 15) * 32 + 8 * w) * 2048);
        auto ptx = [&](auto N) {
            constexpr int n = N.v & 31, j = n >> 1;
            if constexpr (n == 20) ldv4<VX>(rxi, vxi, sxi + s * 65536);
            else if constexpr (n >= 16) ldv<PTM + 4 * (n - 16), 4, (n & 1) * 256>(rs, vptm, spt + (j - 8) * 16384);
            else if constexpr (n & 1) ldv<PT0 + 2 * j, 2, 1024>(rs, lane * 8, spt + DV(j * 2048));
            else ldv<PT1 + 4 * j, 4, 0>(rs, lane16, spt + DV(j * 2048));
        };
        {
            const unsigned sw = opqs(na(nb, N_WM + rslot(s) * 524288)), sw1 = opqs(sw + 131072), sp = opqs(na(nb, N_PM + w * 4096) + rslot(s) * 32768), sm = opqs(na(nb, N_PMM) + rslot(s) * 16384);
            const unsigned vpub = opq(s + 1 < NSEG ? vmem + rslot(s + 1) * (NREP * 32768) : OOB), tg = opqs((s >> 3) & 1), spq = opqs(spt);
            SB;
            if ((int)__builtin_amdgcn_s_memrealtime() - tpub < HLD) { wvm<16 - UIN>(); st8(sxv, rs, sxo, 0); sxo = OOB; hold(tpub, HLD); }
            const int mx = wait_hint(rs, sfl, vfl, s + 1);
            if (left(mx) > SXH * 256) { wvm<16 - UIN>(); st8(sxv, rs, sxo, 0); sxo = OOB; }
            TSR(5);
            const unsigned spins = chain<64 * b>(rs, mx, sp, sm, sw, sw1, tg, spq, lane16, lane * 8, vmm, vw, sidx, lred, lrr, vpub, sxv, sxo);
#if defined(TS) && TS != 4
            tsr[8] = __builtin_amdgcn_readfirstlane(spins) + 1;
#endif
            tpub = (int)__builtin_amdgcn_s_memrealtime();
            TSR(2);
        }
        sfor<4, 21>(ptx);
        BAR;
        f32x4 t[2][2], u[2];
        const unsigned su = s + 1 < NSEG ? su0 + rslot(s + 1) * (4u << 20) : OOB;
        st16(v4u{SENT, SENT, SENT, SENT}, rs, s + 3 < NSEG ? vmem + rslot(s + 3) * (NREP * 32768) : OOB);
#pragma nounroll
        for (int n = 0;; ++n) {
            if (n) { sfor<32, 52>(ptx); WV0; }
            auto lu = [&](auto N) { constexpr int j = N.v >> 1, e = N.v & 1; lda<16 * (j >> 1) + 8 * e + 4 * (j & 1) + 64 * (1 - b), 4, e * 1024>(rs, lane16, su + j * 131072); };
            TSR(6);
            unsigned mt = opq(0);
            const unsigned tp = (s >> 4) & 1;
            sfor<0, 16>([&](auto N) {
                constexpr int j = N.v >> 1, e = N.v & 1, R = 64 * b + 16 * (j >> 1) + 8 * e + 4 * (j & 1), nu = N.v * UIN / 16;
                if constexpr (e == 0) wvm<21 - 2 * j + (j < 2) + nu - 1>();
                if constexpr ((N.v + 1) * UIN / 16 > nu) lu(IC<nu>{});
                mfh<R, PT1 + 4 * j, j == 0, false>(t[1][e]); tagd<e ? PT0 + 2 * j : PT1 + 4 * j>(mt, tp); mfh16<R, PT0 + 2 * j, j == 0>(u[e]);
            });
            wvm<1 + UIN>();
            sfor<0, 8>([&](auto N) { constexpr int q = N.v >> 2, rt = (N.v >> 1) & 1, e = N.v & 1; mfh<VW + 8 * e + 4 * q, PTM + 4 * (2 * q + rt), N.v < 2, true>(t[rt][e]);
                                     tagd<PTM + 2 * N.v>(mt, tp); tagd<PTM + 2 * N.v + 1>(mt, tp); });
            nop<7>(); fence(t[0][0], t[0][1], t[1][0], t[1][1], u[0], u[1]);
#pragma unroll
            for (int e = 0; e < 2; ++e)
#pragma unroll
                for (int i = 0; i < 4; ++i) t[0][e][i] = fadd(t[0][e][i], u[e][i]);
            if (__builtin_amdgcn_uicmp(mt & 1, 0u, 33) == 0 || n == 2) break;
            TSV(s, 7, 1);
            if (n == 1) wait_prog(rs, sfl + 32 + (lane & 7) * 4, 0, (s + 2) << 24);
        }
        TSR(3);
#pragma unroll
        for (int e = 0; e < 2; ++e) { stsq(lane16 + w * 4096, L_T + e * 1024, __builtin_bit_cast(v4u, t[0][e])); stsq(lane16 + w * 4096, L_T + 2048 + e * 1024, __builtin_bit_cast(v4u, t[1][e])); }
        WL0; BAR;
        if (w < 2) {
            float o[8], s1 = 0.f, s2 = 0.f;
#pragma unroll
            for (int e = 0; e < 2; ++e) {
                f32x4 q[4];
#pragma unroll
                for (int k = 0; k < 4; ++k) q[k] = __builtin_bit_cast(f32x4, ldsq(lane16 + w * 2048, L_T + k * 4096 + e * 1024));
#pragma unroll
                for (int i = 0; i < 4; ++i) o[4 * e + i] = fadd(fadd(q[0][i], q[1][i]), fadd(q[2][i], q[3][i]));
            }
            v4u x1;
            sfor<0, 4>([&](auto I) {
                constexpr int i = I.v;
                const unsigned x = rdv<VX + i>(), at = pack2(o[2 * i], o[2 * i + 1]);
                x1[i] = pack2(bflo(x) + bflo(at), bfhi(x) + bfhi(at));
                const float lo = bflo(x1[i]), hi = bfhi(x1[i]);
                s1 += lo + hi; s2 = fmaf(lo, lo, fmaf(hi, hi, s2));
            });
            st16(x1, rs, O_XM + l * XB + s * 65536 + w * 32768 + T2 * 1024 + lane16);
            sxv = v2u{nosent(__float_as_uint(red32(red16(s1)))), __float_as_uint(red32(red16(s2)))};
            sxo = lane < 16 ? O_SX + (unsigned)((l * 64 + s) * 32 + T2) * 256 + (SXL ? w * 128 + c * 8 : c * 16 + w * 8) : OOB;
        }
        TSR(4);
        {
            sfor<UIN, 16>([&](auto N) { constexpr int j = N.v >> 1, e = N.v & 1; lda<16 * (j >> 1) + 8 * e + 4 * (j & 1) + 64 * (1 - b), 4, e * 1024>(rs, lane16, su + j * 131072); });
        }
#if TS != 4
        TSF(s, 0x817D);
#endif
    };
#pragma nounroll
    for (int s = 0; s < NSEG; s += 2) {
        if (s == 48 && l && T2 == 0) wait_prog(rs, O_ARM + (lane & 7) * 4, 0, 0);
        step(IC<0>{}, s); step(IC<1>{}, s + 1);
    }
    wvm<16 - UIN>(); st8(sxv, rs, sxo, 0);
    WV0; BAR;
    return nb;
}
#ifndef FSIDE
#define FSIDE 1
#endif
#ifndef PDX
#define PDX 0
#endif
constexpr int NU = 2 * NSEG;
#ifndef PSF
#define PSF 24
#endif
#ifndef PSR
#define PSR 6
#endif
#ifndef R2D
#define R2D 400
#endif
#ifndef R2S
#define R2S 10
#endif
#ifndef R2P
#define R2P 120
#endif
#ifndef R2E
#define R2E 40
#endif
#ifndef W2P
#define W2P 0
#endif
#ifndef NRD
#define NRD 4
#endif
#ifndef RPA
#define RPA 8
#endif
#ifndef GROT
#define GROT 1
#endif
#ifndef LAGK
#define LAGK 4
#endif
#ifndef LAGP
#define LAGP 0
#endif
#ifndef N2F
#define N2F 45
#endif
#ifndef N2M
#define N2M 8
#endif
DEVI unsigned unit_ls(unsigned u, int two) {
    static_assert(NSEG == 64 && LAGD >= 6 && N2F + 2 * N2M <= 65 - N2H, "");
    static_assert(LAGP % 2 == 0 && LAGP < 2 * LAGK + 1, "");
    const unsigned d = u - LAGD + LAGP, p = d / (2 * LAGK + 1), r = d % (2 * LAGK + 1), n0 = LAGD - LAGP / 2 + p * (LAGK + 1) + (r >> 1);
    const unsigned n = u < LAGD || n0 >= 64 ? u : ((r & 1) || r == 2 * LAGK) ? n0 : 64 + u - n0;
    return (two ? (u < N2M ? 64 + N2F + 2 * u : NU - N2H - N2M + u) : n < 64 + N2F ? n : n + min(n - (63 + N2F), (unsigned)N2M)) & 127;
}
DEVI unsigned unit_ent(unsigned u, int two) { const unsigned n = unit_ls(u, two); return n | (u > LAGD && n < 64) << 7; }
DEVI unsigned unit_tab(int lane, int two) { return opq(unit_ent(2 * lane, two) | unit_ent(2 * lane + 1, two) << 8); }
DEVI unsigned unit_get(unsigned tab, int u) { return ((unsigned)__builtin_amdgcn_readlane(tab, (u >> 1) & 63) >> ((u & 1) * 8)) & 255; }
struct WMG { static constexpr int NT = 3, NP = 16, RB = 0, LB = 0, CBI = 0; static constexpr int kind(int, int) { return 0; } };
struct WMD { static constexpr int NT = 4, NP = 12, RB = 0, LB = 0, CBI = 0; static constexpr int kind(int, int) { return 0; } };
namespace lg { constexpr int L_ST = 155648, L_OK = 159744; }
DEVI void role_GU(const Args& a, int w, int j, int two) {
    if (!two) { TSC(125, 2); TSW(125, 3); }
    asm volatile("; MARK role_GU"); __builtin_amdgcn_s_waitcnt(0);
    using namespace lg;
    const int lane = lane_id(), c = lane & 15, g = lane >> 4;
    const rsrc_t rs = mkrs(a.ws, WS_NEED);
    const unsigned lane16 = lane * 16, hc = 96 * j + 24 * w;
    {
        const unsigned ta = g * 4096 + (lane & 1) * 2048 + w * 256 + ((lane >> 2) & 3) * 64 + ((lane >> 1) & 1) * 8, tb = ta + 65536;
        const rsrc_t r1 = mkrs(a.Wg, WSZ(7168u * 1024)), r2 = mkrs(a.Wu, WSZ(7168u * 1024));
        const unsigned cb = 192 * j + g * 48 + (lane & 3) * 16, v1 = (lane & 3) == 3 || cb >= 7168 ? OOB : (8 * w + ((lane >> 2) & 3)) * 7168 + cb, lw = opqs(w * 4096), z = opqs(0);
        load_blocks<32, 8, 4, true>([&](auto B, auto S) { if (W2P && two) __builtin_amdgcn_s_sleep(W2P); dma_blk(lw + S.v * 16384, r1, v1, z + B.v * 32 * 7168, 1792); dma_blk(lw + S.v * 16384 + 2048, r2, v1, z + B.v * 32 * 7168, 1792); },
                                    [&](auto B, auto S) { sfor<0, 3>([&](auto T) { constexpr int o = (S.v & 3) * 16384 + T.v * 16; put_frag<WMG, T.v, B.v, o, o + 1024>(S.v < 4 ? ta : tb, 0, nullptr); }); });
        if (!two) TSW(120 - 65536, 0);
        v4u one{0x3F803F80u, 0x3F803F80u, 0x3F803F80u, 0x3F803F80u};
        asm volatile("s_nop 1" : "+v"(one));
        f32x4 cs[3];
        sfor<0, 96>([&](auto N) { constexpr int t = N.v % 3, ks = N.v / 3; mfA<8 * widx<WMG>(t, ks >> 1) + 4 * (ks & 1), ks == 0>(cs[t], one); });
        nop<7>(); fence(cs[0], cs[1], cs[2]);
        sfor<0, 3>([&](auto T) { mwr4<384 + 4 * T.v>(__builtin_bit_cast(v4u, cs[T.v])); });
        BAR;
    }
    const unsigned nf = two ? O_N + PG2 + (((two >> 1) ^ FSIDE) & 1) * 8192 : side(rs, FSIDE);
    const unsigned wlo = (unsigned)(uintptr_t)a.ws, whi = (unsigned)((uintptr_t)a.ws >> 32);
    const unsigned xr = w * 8192 + (GROT ? (j & 1) * 4096 + (j & 2) * 16384 : 0);
    const v8u pv{lane16, O_SX + (8 * w + g) * 256 + c * 16, O_H + (g & 1) * 114688 + hc * 32 + c * 16 + (g >> 1) * 8, lane == 0 ? na(nf, N_PG) + j * 128 + w * 4 : OOB,
                 (unsigned)(L_ST + ((lane & 1) * 16 + ((lane & 31) >> 1)) * 16), L_ST + lane16 + w * 4, unit_tab(lane, two), lane16};
    const v16u s0{wlo, whi, RSZ, 0x20000u, wlo, whi, 0u, 0x20000u, OOB, OOB, hc < 3584 ? 0u : OOB, hc + 8 < 3584 ? 0u : OOB, 0u, O_XM + xr, 65536 + xr, 0u};
    const v4u s1{0u, O_PROF + ((blockIdx.x & 255) * 4 + w) * 8192, (unsigned)(two ? N2M : NU - N2M - N2H), (unsigned)(two & 1)};
    asm volatile(G_LOOP :: "{v[0:7]}"(pv), "{s[8:23]}"(s0), "{s[24:27]}"(s1), "{s28}"(two ? N2M + N2H : NU - N2M) : "memory", "s99", "vcc");
}
DEVI void role_DN(const Args& a, int w, int p, int cg, int two) {
    asm volatile("; MARK role_DN"); __builtin_amdgcn_s_waitcnt(0);
    using namespace lg;
    const int lane = lane_id();
    const rsrc_t rs = mkrs(a.ws, WS_NEED);
    const unsigned lane16 = lane * 16;
    const int F0 = p < 3 ? 23 * p : (p == 3 ? 68 : 89), zb = p < 2 ? -1 : (p == 4 ? 0 : 22);
    {
        const unsigned ta = w * 18432 + (lane >> 4) * 512 + ((lane >> 2) & 3) * 64 + (lane & 3) * 16;
        const rsrc_t r1 = mkrs(a.Wd, WSZ(3584u * 2048));
        const unsigned v1 = (F0 * 32 + (lane >> 2)) * 2048 + cg * 512 + w * 128 + (lane & 3) * 16;
        load_blocks<46, 9, 2>([&](auto B, auto S) { dma_blk(w * 18432 + S.v * 2048, r1, (B.v >> 1) == zb ? OOB : v1, (B.v >> 1) * 65536 + (B.v & 1) * 64, 2048); },
                              [&](auto B, auto S) { constexpr int ks = B.v >> 1, t = 2 * (B.v & 1), o = S.v * 2048;
                                                    put_frag<WMD, t, ks, o, o + 256>(ta, 0, nullptr); put_frag<WMD, t + 1, ks, o + 8, o + 264>(ta, 0, nullptr); });
        if (!two) TSW(120 - 65536, 0);
        BAR;
    }
    const int g0 = F0 / 3 * 4, gn = ((F0 + 22) / 3 + 1) * 4 - g0;
    const unsigned nf = two ? O_N + PG2 + (((two >> 1) ^ FSIDE) & 1) * 8192 : side(rs, FSIDE);
    const unsigned wlo = (unsigned)(uintptr_t)a.ws, whi = (unsigned)((uintptr_t)a.ws >> 32);
    const v8u pv{lane16, na(nf, N_PG) + g0 * 32 + (lane < gn ? (lane >> 2) * 128 + (lane & 3) * 4 : 0), lane == 0 ? na(nf ^ PDX, N_PD) + (cg * NKF + p) * 128 + w * 4 : OOB,
                 unit_tab(lane, two), lane16, (unsigned)(L_OK + lane * 4), (unsigned)((lane & 8) * 2048 + (lane >> 4) * 128 + (lane & 7) * 16), 0u};
    const v16u s0{wlo, whi, RSZ, 0x20000u, wlo, whi, 0u, 0x20000u, wlo, whi, w == 0 ? RSZ : 0u, 0x20000u, 0u, O_H + (w >> 1) * 114688 + (F0 + (w & 1) * 12) * 1024,
                  O_D + p * 65536 + (cg * 8 + w * 2) * 512, (unsigned)(65536 + (w >> 1) * 32768 + (w & 1) * 12288)};
    const v4u s1{(unsigned)(L_OK + w * 256), 0u, 0u, O_PROF + ((blockIdx.x & 255) * 4 + w) * 8192};
    asm volatile(D_LOOP :: "{v[0:7]}"(pv), "{s[8:23]}"(s0), "{s[24:27]}"(s1),
                 "{s[28:31]}"(v4u{(unsigned)(two ? N2M : NU - N2M - N2H), O_H + (two & 1) * 114688 + (F0 + 6 * w) * 1024, O_D + p * 65536 + (two & 1) * 32768 + (cg * 8 + w * 2) * 512, (unsigned)(two ? N2M + N2H : NU - N2M)}) : "memory", "s99", "vcc");
}
template <int R> DEVI void ldv1(rsrc_t rs, unsigned vo, unsigned so) { asm volatile("buffer_load_dword v[%3], %0, %1, %2 offen sc1" :: "v"(vo), "s"(rs), "s"(so), "n"(R) : "memory"); }
template <int R, int I> DEVI void unp2(f32x4& d) { float x, y; asm volatile("v_lshlrev_b32 %0, 16, v[%2]\n\tv_and_b32 %1, 0xffff0000, v[%2]" : "=v"(x), "=v"(y) : "n"(R)); d[I] = x; d[I + 1] = y; }
DEVI void role_RED(const Args& a, int w, int cu, unsigned nf) {
    asm volatile("; MARK role_RED"); __builtin_amdgcn_s_waitcnt(0);
    const int lane0 = lane_id(), c = lane0 & 15, g = lane0 >> 4;
    const rsrc_t rs = mkrs(a.ws, WS_NEED);
    v4u sel[2];
#pragma unroll
    for (int e = 0; e < 2; ++e)
#pragma unroll
        for (int i = 0; i < 4; ++i) sel[e][i] = (g == (c >> 2) && i == 2 * e + ((c >> 1) & 1)) ? ((c & 1) ? 0x3F800000u : 0x3F80u) : 0u;
    const unsigned vfl = na(nf ^ PDX, N_PD) + w * (NKF * 128) + (lane0 < 20 ? (lane0 >> 2) * 128 + (lane0 & 3) * 4 : 0);
    constexpr int VP = NV + 32;
    const unsigned lo = (c & 8) * 128 + g * 256 + (c & 7) * 16 + (cu & 1) * 128 + w * 8192, ld = (c & 8) * 64 + g * 128 + (c & 7) * 16 + (cu & 1) * 16384 + w * 4096;
    const unsigned lout = ((cu & 1) * 8 + (c & 7)) * 2048 + w * 512 + (c & 8) * 8 + g * 16, lw = g == (c >> 2) ? (c & 7) * 64 + w * 16 + (c & 8) : 1024 + lane0 * 8;
    const rsrc_t ro = mkrs(a.out, 4u << 20);
    v4u one{0x3F803F80u, 0x3F803F80u, 0x3F803F80u, 0x3F803F80u};
    asm volatile("" : "+v"(one));
    #pragma unroll
    for (int i = 0; i < 2; ++i) if (2 * lane0 + i < NU - N2M) *LDSP(unsigned char, 2048 + unit_ls(2 * lane0 + i, 0)) = 2 * lane0 + i;
    if (lane0 < N2M) *LDSP(unsigned char, 2048 + unit_ls(lane0, 1)) = 128 + lane0;
    WL0; BAR;
    const unsigned tab = opq(*LDSP(const unsigned short, 2048 + 2 * lane0));
    auto place = [&](int k) { return k >= 4 * (NU - N2H) && (k & 2) ? N2M + N2H + (k >> 2) : unit_get(tab, k >> 2); };
    auto next = [&](int k) { return k >= 256 ? k + 32 : k + NRD < 256 ? k + NRD : 4 * NU; };
    auto issue = [&](auto B, auto N0, auto N1, int k) {
        const unsigned ls = k >> 2, base = ((k >> 1) & 1) * 32768;
        sfor<N0.v, N1.v>([&](auto N) { constexpr int f = (N.v - 4) / 5, p = (N.v - 4) % 5;
            if constexpr (N.v < 4) ldv4<NV + 16 * B.v + 4 * N.v>(rs, lo, O_XM + ls * 65536 + base + N.v * 2048);
            else lda<80 * B.v + 20 * f + 4 * p, 4, f * 1024>(rs, ld, O_D + (ls * NKF + p) * 65536 + base); });
        if constexpr (N1.v == 24) { const int k2 = next(k); ldv1<VP>(rs, vfl, k2 < 4 * NU ? (place(k2) >> 7) * PG2 : 0u); }
    };
    u64 late = 1, far = 1;
    bool pre = false, last = false;
    unsigned tries = 0;
    auto task = [&](auto B, int k) {
        constexpr int b = B.v;
        const int ut = place(k), u = ut & 127, rt = (k >> 1) & 1, kn = next(k), un = kn < 4 * NU ? place(kn) : 127, nx = (un & 127) + 1;
        const unsigned sf = (ut >> 7) * PG2, sfn = (un >> 7) * PG2;
        const unsigned ls = k >> 2, tile = ls * 2 + rt;
        TSD; TSR(0);
        if (k >= 256 && k < 4 * (64 + R2E)) hold((int)__builtin_amdgcn_s_memrealtime(), k < 288 ? R2D + (k & 31) * R2S : R2P);
        if (!pre) {
            if (late) {
                unsigned spins = 0;
#pragma nounroll
                for (;;) {
                    if (far) __builtin_amdgcn_s_sleep(PSF); else __builtin_amdgcn_s_sleep(PSR);
                    CB;
                    const int v = (int)ld4(rs, vfl, sf);
                    if (__builtin_amdgcn_sicmp(v, u + 1, 40) == 0) break;
                    far = __builtin_amdgcn_sicmp(v, u, 40);
                    spin_bail(spins);
                }
            }
            TSR(1);
            issue(B, IC<0>{}, IC<24>{}, k);
        }
        else { TSR(1); }
        const bool l0 = ls < 64;
        const rsrc_t rd = l0 ? rs : ro;
        const unsigned vd = opq((l0 ? O_XIN + XB + lo : lout - 64 * 65536) + tile * 32768), fs = l0 ? 2048 : 128;
        v4u x[4];
        f32x4 d[4][2], S, Q;
        auto unp = [&](auto F, auto I) { unp2<NV + 16 * b + 4 * F.v + I.v, 2 * (I.v & 1)>(d[F.v][I.v >> 1]); };
        auto fin = [&](auto F) {
            constexpr int f = F.v;
            fence(d[f][0], d[f][1]);
#pragma unroll
            for (int i = 0; i < 4; ++i) x[f][i] = pack2(d[f][i >> 1][2 * (i & 1)], d[f][i >> 1][2 * (i & 1) + 1]);
            asm volatile("" : "+v"(x[f]));
        };
        auto put = [&](auto F) { constexpr int f = F.v; st16s(x[f], rd, vd, f * fs); SB; mfv<f == 0>(S, one, x[f]); mfv<f == 0>(Q, x[f], x[f]); SB; };
        wvm<24>();
        sfor<0, 4>([&](auto I) { unp(IC<0>{}, I); });
        sfor<0, 4>([&](auto F) {
            constexpr int f = F.v;
            wvm<16 - 5 * f + (f ? f - 1 : 0)>();
            nop<1>();
            sfor<0, 10>([&](auto N) {
                mfB<80 * b + 20 * f + 4 * (N.v >> 1), false>(d[f][N.v & 1], sel[N.v & 1]);
                if constexpr (N.v < 4 && f < 3) unp(IC<(f + 1) & 3>{}, N);
                if constexpr (N.v == 5 && f > 0) fin(IC<(f + 3) & 3>{});
                SB;
            });
            if constexpr (f > 0) put(IC<(f + 3) & 3>{});
        });
        wvm<3>();
        TSR(2);
#ifdef TS
        tsr[4] = pre + 1;
#endif
        const bool p1 = ltv<VP>(nx) == 0;
        if (!p1) ldv1<VP + 1>(rs, vfl, sfn);
        nop<3>();
        fin(IC<3>{});
        nop<1>();
        put(IC<3>{});
        auto second = [&](auto NS) {
            wvm<NS.v>();
            pre = ltv<VP + 1>(nx) == 0;
            if (pre) { issue(IC<1 - b>{}, IC<0>{}, IC<24>{}, kn); if constexpr (NS.v == 5) wvm<25>(); }
            else {
                if constexpr (NS.v == 5) { ldv1<VP + 1>(rs, vfl, sfn); WV0; }
                late = ltv<VP + 1>(nx); far = ltv<VP + 1>(nx - 1);
            }
        };
        if (l0) {
            nop<7>();
            fence(S, Q);
            const float qd = selm(selm(Q[0], Q[1], 0xAAAAAAAAAAAAAAAAull), selm(Q[2], Q[3], 0xAAAAAAAAAAAAAAAAull), 0xCCCCCCCCCCCCCCCCull);
            *LDSP(f32x2, lw) = f32x2{S[0], qd};
            if (p1) issue(IC<1 - b>{}, IC<0>{}, IC<RPA>{}, kn);
            f32x2 xf[4][4];
#pragma unroll
            for (int f = 0; f < 4; ++f)
#pragma unroll
                for (int i = 0; i < 4; ++i) { xf[f][i] = f32x2{bflo(x[f][i]), bfhi(x[f][i])}; asm volatile("" : "+v"(xf[f][i])); }
            TSR(5);
            WL0; BAR;
            f32x4 t[4];
#pragma unroll
            for (int i = 0; i < 4; ++i) t[i] = __builtin_bit_cast(f32x4, ldsq((c & 7) * 64, i * 16));
            const f32x4 t4 = (t[0] + t[1]) + (t[2] + t[3]);
            const float mean = (t4[0] + t4[2]) * (1.0f / 1024.0f), var = fmaxf(rbf(fmaf(t4[1] + t4[3], 1.0f / 1024.0f, -mean * mean)), 0.f);
            const float inv = __builtin_amdgcn_rcpf(rbf(__builtin_amdgcn_sqrtf(rbf(var + 1e-5f)))), mi = -rbf(mean) * inv;
#pragma unroll
            for (int f = 0; f < 4; ++f) {
                v4u o;
#pragma unroll
                for (int i = 0; i < 4; ++i) xf[f][i] = __builtin_elementwise_fma(xf[f][i], f32x2{inv, inv}, f32x2{mi, mi});
#pragma unroll
                for (int i = 0; i < 4; ++i) o[i] = pack2(xf[f][i][0], xf[f][i][1]);
                st16(o, rs, vd + (O_XN - O_XIN) + f * 2048);
            }
            TSR(6);
            if (p1) { issue(IC<1 - b>{}, IC<RPA>{}, IC<24>{}, kn); TSR(7); wvm<25 - RPA>(); pre = true; } else second(IC<5>{});
            BAR;
            st4(0u, rs, lane0 < 2 && w == 0 ? na(nf ^ (FSIDE ? 0 : 8192), N_FX) + tile * 16 + (cu & 1) * 8 + lane0 * 4 : OOB, 0);
        }
        else if (k >= 4 * (NU - N2H)) {
            nop<7>(); fence(S);
            pre = false; late = far = 1;
            if (!last && __builtin_amdgcn_fcmpf(S[0], S[0], 8) != 0) { last = __builtin_amdgcn_sicmp((int)ld4(rs, vfl + 16, sf), u + 1, 40) == 0; spin_bail(tries); late = 0; return k; }
        }
        else if (p1) { issue(IC<1 - b>{}, IC<0>{}, IC<24>{}, kn); pre = true; }
        else second(IC<1>{});
        TSR(3); TSF(cu > 255 ? 64 + (k >> 5 & 7) : u, 0xFF);
        return kn;
    };
#pragma nounroll
    for (int k = cu;;) {
        k = task(IC<0>{}, k);
        if (k >= 4 * NU) break;
        k = task(IC<1>{}, k);
        if (k >= 4 * NU) break;
    }
}
#ifndef LSW
#define LSW 0
#endif
#ifndef FNG
#define FNG 0x34344776u
#endif
#ifndef FNS
#define FNS 0x00000112u
#endif
#ifndef DLY_A
#define DLY_A 250
#endif
#ifndef DLY_B
#define DLY_B 0
#endif
#ifndef DLY_1
#define DLY_1 1600
#endif
#ifndef DLY_G
#define DLY_G 1400
#endif
#ifndef DLY_D
#define DLY_D 1600
#endif
__global__ __launch_bounds__(256, 1) __attribute__((amdgpu_num_vgpr(NV))) void mega6(Args a) {
    asm volatile("" ::: "v255", "a255");
    const int w = __builtin_amdgcn_readfirstlane(threadIdx.x >> 6), b = blockIdx.x, xcd = b & 7, slot = b >> 3, l = (xcd >> 2) ^ LSW, idx = (slot & 7) * 4 + (xcd & 3);
    const int t0 = (int)__builtin_amdgcn_s_memrealtime();
    if (b < 256) { TSW(127, 0); TSC(125, 0); TSW(125, 1); }
    const int f = slot - 24, two = b < 256 ? 0 : 1 + ((b >> 8) & 2), k2 = b & 255;
    const int ia = l || !N2M || idx >= 26 ? -1 : idx < 20 ? 38 + (idx & 3) * 5 + (idx >> 2) : 12 + idx, ib = l || !N2M ? -1 : (idx & 3) * 8 + (idx >> 2);
    const int ng = (FNG >> 4 * xcd) & 15, ns = (FNS >> 4 * xcd) & 15, below = (1 << 4 * xcd) - 1;
    auto nib = [](unsigned x) { x = (x & 0x0F0F0F0F) + ((x >> 4) & 0x0F0F0F0F); return (int)((x * 0x01010101) >> 24); };
    int gu = f < 0 || (xcd >= 3 && f >= 4) ? -1 : f < ng ? nib(FNG & below) + f : -2, dn = (xcd - 3) * 4 + f - 4, cu = gu == -2 && f < ng + ns && !two ? nib(FNS & below) + f - ng : -1;
    if (two) { gu = k2 < 38 ? k2 : -1; dn = k2 - 38; }
    if (slot < 24 && l && (slot < 16 || idx >= LN_NOW)) pull_all(a, w, (slot >> 3) * 32 + idx - (slot < 16 ? 0 : LN_NOW), t0);
    if (slot < 8) { hold(t0, l ? DLY_1 : DLY_A); role_A(a, w, l, idx, (xcd & 3) * 2, ia); }
    else if (slot < 16) { hold(t0, l ? DLY_1 : DLY_B); role_B(a, w, l, idx, (xcd & 3) * 2, ib); }
    else if (gu >= 0) {
        const int q = gu * 4 + w;
        if (!two) use(ld4(mkrs(a.ws, WS_NEED), q < 2 * NVK + 2 ? prb(q >> 1, q & 1) + lane_id() * 32 : OOB, 0));
        hold(t0, two ? 0 : DLY_G); role_GU(a, w, gu, two);
    }
    else if (slot < 24 || cu >= 0) {
        unsigned nf;
        if (slot < 24) {
            nf = role_R(a, w, l, idx, t0) ^ (FSIDE * 8192); cu = 256 + idx;
            if (l) { if (idx + w == 0) vote_end(mkrs(a.ws, WS_NEED)); return; }
        } else {
            hold(t0, DLY_D); nf = side(mkrs(a.ws, WS_NEED), FSIDE);
        }
        role_RED(a, w, cu, nf);
    }
    else if (gu == -2) arm_last(a, (xcd >> 1 & 1) * 4 + w, 8, t0);
    else { if (xcd < 7 && !two) vote(mkrs(a.ws, WS_NEED), w, dn, l, t0); hold(t0, two ? 0 : DLY_D); role_DN(a, w, dn >> 2, dn & 3, two); }
}
extern "C" void kernel_launch(void* const* d_in, const int* in_sizes, int n_in, void* d_out, int out_size, void* d_ws, size_t ws_size, hipStream_t stream) {
    (void)in_sizes; (void)n_in; (void)out_size; (void)ws_size;
    (void)hipFuncSetAttribute((const void*)mega6, hipFuncAttributeMaxDynamicSharedMemorySize, 163840);
    d_ws = (void*)(((uintptr_t)d_ws + (2u << 20) - 1) & ~(uintptr_t)((2u << 20) - 1));
    Args a{(const u16*)d_in[0], (const u16*)d_in[1], (const u16*)d_in[2], (const u16*)d_in[3], (const u16*)d_in[4], (const u16*)d_in[5],
           (const u16*)d_in[8], (const u16*)d_in[9], (const u16*)d_in[10], (char*)d_ws, (u16*)d_out};
    mega6<<<256, 256, 163840, stream>>>(a);
}
```
